```python
import torch

def _softmax(x, axis=-1):
    return torch.softmax(x, dim=axis)

def _gelu(x):
    return torch.nn.functional.gelu(x, approximate='tanh')

def _silu(x):
    return torch.nn.functional.silu(x)

def _sigmoid(x):
    return torch.sigmoid(x)

def _layernorm(x, axis=-1, eps=1e-05):
    m = x.mean(dim=axis, keepdim=True)
    v = x.var(dim=axis, keepdim=True, unbiased=False)
    return (x - m) / torch.sqrt(v + eps)

def _scatter_add(tgt, idx, src, axis=0):
    return tgt.clone().index_add_(axis, idx, src)

def _segment_sum(src, seg, n):
    out = torch.zeros((n,) + src.shape[1:], dtype=src.dtype)
    return out.index_add_(0, seg, src)

def _pad(x, pw):
    flat = tuple((v for pair in reversed(pw) for v in pair))
    return torch.nn.functional.pad(x, flat)
kernel_name = 'interleave_layers__memory_aug_recurrent_mem_tr__ssm_conv_h3__prompt'

def setup_inputs():

    _w = lambda a: (lambda t: t.to(torch.bfloat16) if t.is_floating_point() else t)(torch.as_tensor(a))

    def _A_setup():
        B, S, Dm = (1, 2048, 640)
        H, Dh = (32, 128)
        M, seg = (8, 32)
        seg = min(seg, S)
        S = S // seg * seg
        return {'x': _w((lambda *s: torch.randn(*s, dtype=torch.bfloat16) * 0.05)(B, S, Dm)), 'mem0': _w((lambda *s: torch.randn(*s, dtype=torch.bfloat16) * 0.4)(B, M, Dm)), 'Wq': _w((lambda *s: torch.randn(*s, dtype=torch.bfloat16) * 1.6)(Dm, H, Dh) * Dm ** (-0.5)), 'Wk': _w((lambda *s: torch.randn(*s, dtype=torch.bfloat16) * 1.6)(Dm, H, Dh) * Dm ** (-0.5)), 'Wv': _w((lambda *s: torch.randn(*s, dtype=torch.bfloat16) * 0.4)(Dm, H, Dh) * Dm ** (-0.5)), 'Wo': _w((lambda *s: torch.randn(*s, dtype=torch.bfloat16) * 0.4)(H, Dh, Dm) * (H * Dh) ** (-0.5)), 'seg_len': seg}
    _A_in = _A_setup()

    def _B_setup():
        B, S, D = (1, 2048, 640)
        ks = 4
        return {'q': _w((lambda *s: torch.randn(*s, dtype=torch.bfloat16) * 0.05)(B, S, D)), 'k': _w((lambda *s: torch.randn(*s, dtype=torch.bfloat16) * 0.4)(B, S, D)), 'v': _w((lambda *s: torch.randn(*s, dtype=torch.bfloat16) * 0.4)(B, S, D)), 'shift_k': _w((lambda *s: torch.randn(*s, dtype=torch.bfloat16) * 0.4)(D, ks) * 0.5), 'ssm_k': _w((lambda *s: torch.randn(*s, dtype=torch.bfloat16) * 0.8)(D, S) * (1.0 / S ** 0.5)), 'ssm_D': _w((lambda *s: torch.randn(*s, dtype=torch.bfloat16) * 0.4)(D))}
    _B_in = _B_setup()
    return {'A_x': _A_in['x'], 'A_mem0': _A_in['mem0'], 'A_Wq': _A_in['Wq'], 'A_Wk': _A_in['Wk'], 'A_Wv': _A_in['Wv'], 'A_Wo': _A_in['Wo'], 'A_seg_len': _A_in['seg_len'], 'B_q': _B_in['q'], 'B_k': _B_in['k'], 'B_v': _B_in['v'], 'B_shift_k': _B_in['shift_k'], 'B_ssm_k': _B_in['ssm_k'], 'B_ssm_D': _B_in['ssm_D']}

def reference(A_x, A_mem0, A_Wq, A_Wk, A_Wv, A_Wo, A_seg_len, B_q, B_k, B_v, B_shift_k, B_ssm_k, B_ssm_D):

    def _A_ref(x, mem0, Wq, Wk, Wv, Wo, seg_len):
        B, S, Dm = x.shape
        H, Dh = (Wq.shape[1], Wq.shape[2])
        M = mem0.shape[1]
        scale = Dh ** (-0.5)
        n_seg = S // seg_len
        L = M + seg_len
        idx = torch.arange(L)
        mask = (idx[:, None] < M) | (idx[:, None] >= idx[None, :])
        mem = mem0
        outs = []
        for s in range(n_seg):
            xs = x[:, s * seg_len:(s + 1) * seg_len, :].clone()
            h = torch.concatenate([mem, xs], axis=1)
            q = torch.einsum('btd,dhe->bhte', h, Wq)
            k = torch.einsum('btd,dhe->bhte', h, Wk)
            v = torch.einsum('btd,dhe->bhte', h, Wv)
            scores = torch.einsum('bhqe,bhke->bhqk', q, k) * scale
            scores = torch.where(mask[None, None], scores, -1000000000.0)
            p = _softmax(scores, axis=-1)
            o = torch.einsum('bhqk,bhke->bhqe', p, v)
            o = torch.einsum('bhte,hed->btd', o, Wo)
            mem = o[:, :M, :].clone()
            outs.append(o[:, M:, :])
        return torch.concatenate(outs, axis=1)

    def _B_ref(q, k, v, shift_k, ssm_k, ssm_D):
        B, S, D = q.shape
        ks = shift_k.shape[-1]
        k_pad = _pad(k, ((0, 0), (ks - 1, 0), (0, 0)))
        k_shift = torch.zeros_like(k)
        for t in range(ks):
            k_shift = k_shift + k_pad[:, t:t + S, :] * shift_k[:, ks - 1 - t][None, None, :]
        kv = k_shift * v
        L = 2 * S
        kv_pad = _pad(kv, ((0, 0), (0, S), (0, 0)))
        kf_pad = _pad(ssm_k, ((0, 0), (0, S)))
        kv_f = (lambda x, axes=(-2, -1): torch.fft.rfft2(x.float(), dim=axes))(kv_pad[:, None, :, :], axes=(1, 2))
        kf_f = (lambda x, axes=(-2, -1): torch.fft.rfft2(x.float(), dim=axes))(kf_pad[:, None, :], axes=(1, 2))
        kf_f = (lambda x, *d: x.permute(*d))(kf_f, 1, 2, 0)
        y_f = kv_f * kf_f[None, :, :, :]
        y = (lambda x, s, axes=(-2, -1): torch.fft.irfft2(x, s=s, dim=axes).to(torch.bfloat16))(y_f, (1, L), axes=(1, 2))[:, 0, :S, :]
        y = y + kv * ssm_D[None, None, :]
        return q * y
    x = A_x
    x = x + _A_ref(_layernorm(x, axis=-1), A_mem0, A_Wq, A_Wk, A_Wv, A_Wo, A_seg_len)
    x = x + _B_ref(_layernorm(x, axis=-1), B_k, B_v, B_shift_k, B_ssm_k, B_ssm_D)
    x = x + _A_ref(_layernorm(x, axis=-1), A_mem0, A_Wq, A_Wk, A_Wv, A_Wo, A_seg_len)
    x = x + _B_ref(_layernorm(x, axis=-1), B_k, B_v, B_shift_k, B_ssm_k, B_ssm_D)
    return x
```

```cpp
#include <hip/hip_runtime.h>
#include <hip/hip_bf16.h>

typedef unsigned short u16;
typedef unsigned int u32;
typedef u32 v2u __attribute__((ext_vector_type(2)));
typedef u32 v4u __attribute__((ext_vector_type(4)));
typedef u32 v8u __attribute__((ext_vector_type(8)));
typedef float f32x2 __attribute__((ext_vector_type(2)));
typedef float f32x4 __attribute__((ext_vector_type(4)));
typedef float f32x16 __attribute__((ext_vector_type(16)));
typedef __bf16 bf16x2 __attribute__((ext_vector_type(2)));
typedef __bf16 bf16x8 __attribute__((ext_vector_type(8)));
typedef short s16x4 __attribute__((ext_vector_type(4)));
#define LAS __attribute__((address_space(3)))
#define GAS __attribute__((address_space(1)))
typedef LAS unsigned char* lptr;
#define DEV __device__ __forceinline__

constexpr int SEQ = 2048, DM = 640, NH = 32, DH = 128, NSEG = 64, NQ = NH * DH;

constexpr u32 MB = 1u << 20;
constexpr u32 W_ORING = 0;
constexpr u32 W_MRING = MB;
constexpr u32 W_PROBE = 2 * MB - 65536;
constexpr u32 W_CTRL = 4 * MB;
constexpr u32 CTRL_BYTES = 128 * 1024;
constexpr u32 ACT = SEQ * DM * 2;
constexpr u32 W_H1 = 5 * MB, W_Y = W_H1 + ACT, W_H2 = W_Y + ACT, W_X2 = W_H2 + ACT;
constexpr u32 W_KQ = 16 * MB;
constexpr u32 W_VO = W_KQ + 64 * MB;
constexpr u32 W_P = W_VO + 64 * MB;
constexpr u32 P_ITEM = 32 * 4 * DM * 4;
constexpr u32 W_KVT = W_P + 2 * NSEG * P_ITEM;
constexpr u32 W_SP = W_KVT + ACT;
constexpr u32 W_END = W_KVT + 3 * MB;
static_assert((size_t)W_END + 2 * MB <= 256ull * MB, "workspace");
constexpr u32 F_ERR = 0, F_QKV = 16, F_O = F_QKV + 192, F_P = F_O + 64, F_H2 = F_P + 64, F_CLR = F_H2 + 32,
              F_POL = F_CLR, F_EPOCH = F_POL + 32, F_SLOT = F_EPOCH + 1, F_PRE = F_SLOT + 256, F_N = F_PRE + 192;
constexpr int NCV = 44, NPRE = 40;
static_assert(F_N * 128 <= CTRL_BYTES, "ctrl");
DEV u32 foff(u32 idx) { return W_CTRL + idx * 128u; }

constexpr u32 OOB = 0x80000000u;
constexpr u32 PSENT = 0x7F817F81u;
constexpr int SPIN_CAP = 40000;
constexpr int LDS_BYTES = 160 * 1024;
#define ST_HEAD 0
#define ST_WO 0

#define LST(idx, v) do {} while (0)
#define LS2(cond, idx) do {} while (0)
struct Params {
    const u16 *x, *mem0, *Wq, *Wk, *Wv, *Wo, *bk, *bv, *shift, *ssmk, *ssmD;
    u16* out;
    unsigned char* ws;
};

typedef __amdgpu_buffer_rsrc_t rsrc_t;
DEV rsrc_t mk_rsrc(const void* p, u32 bytes) { return __builtin_amdgcn_make_buffer_rsrc((void*)p, 0, bytes, 0x00020000); }
DEV v4u ld16s(rsrc_t r, u32 off, u32 so) { return __builtin_amdgcn_raw_buffer_load_b128(r, off, so, 16); }
DEV v2u ld8s(rsrc_t r, u32 off, u32 so) { return __builtin_amdgcn_raw_buffer_load_b64(r, off, so, 16); }
DEV v4u ldin16(rsrc_t r, u32 off, u32 so) { return __builtin_amdgcn_raw_buffer_load_b128(r, off, so, 2); }
DEV v2u ldin8(rsrc_t r, u32 off, u32 so) { return __builtin_amdgcn_raw_buffer_load_b64(r, off, so, 2); }
DEV u32 ld4(rsrc_t r, u32 off) { return __builtin_amdgcn_raw_buffer_load_b32(r, off, 0, 16); }
DEV void st16(rsrc_t r, u32 off, v4u v) { __builtin_amdgcn_raw_buffer_store_b128(v, r, off, 0, 16); }
DEV void st8s(rsrc_t r, u32 off, u32 so, v2u v) { __builtin_amdgcn_raw_buffer_store_b64(v, r, off, so, 16); }
DEV void st4s(rsrc_t r, u32 off, u32 so, u32 v) { __builtin_amdgcn_raw_buffer_store_b32(v, r, off, so, 16); }
#define DRAIN() asm volatile("s_waitcnt vmcnt(0)" ::: "memory")
#define VMW(n) asm volatile("s_waitcnt vmcnt(%0)" :: "i"(n) : "memory")
#define BAR() asm volatile("s_waitcnt lgkmcnt(0)\n\ts_barrier" ::: "memory")
#define SCHED() __builtin_amdgcn_sched_barrier(0)
#define CFENCE() asm volatile("" ::: "memory")
#define NOPS(n) asm volatile("s_nop %0" :: "i"(n))
#define PIN(x) asm volatile("" : "+v"(x))
#define PINS(x) asm volatile("" : "+s"(x))

template <int V> struct IC { static constexpr int value = V; };
template <int I, int N, class F> DEV void static_for(F&& f) { if constexpr (I < N) { f(IC<I>{}); static_for<I + 1, N>(f); } }
#define SFOR(var, n) static_for<0, n>([&](auto ic_) __attribute__((always_inline)) { constexpr int var = decltype(ic_)::value;
#define SEND });

DEV u32 pk(float a, float b) { bf16x2 v = {(__bf16)a, (__bf16)b}; return __builtin_bit_cast(u32, v); }
DEV float lo_f(u32 u) { return __builtin_bit_cast(float, u << 16); }
DEV float hi_f(u32 u) { return __builtin_bit_cast(float, u & 0xffff0000u); }
DEV f32x2 up2(u32 u) { return (f32x2){lo_f(u), hi_f(u)}; }
DEV u32 pk2(f32x2 v) { return pk(v.x, v.y); }
DEV float rbf(float a) { return (float)(__bf16)a; }
DEV v4u lds_r16(lptr p) { return *(LAS v4u*)p; }
DEV void lds_w16(lptr p, v4u v) { *(LAS v4u*)p = v; }
DEV v2u lds_tr(lptr p) { return __builtin_bit_cast(v2u, __builtin_amdgcn_ds_read_tr16_b64_v4i16((LAS s16x4*)p)); }
DEV v4u tr2(lptr p0, lptr p1) { const v2u a = lds_tr(p0), b = lds_tr(p1); return (v4u){a.x, a.y, b.x, b.y}; }
DEV v8u cat8(v4u a, v4u b) { return __builtin_shufflevector(a, b, 0, 1, 2, 3, 4, 5, 6, 7); }
DEV f32x4 mfma16(v4u a, v4u b, f32x4 c) { return __builtin_amdgcn_mfma_f32_16x16x32_bf16(__builtin_bit_cast(bf16x8, a), __builtin_bit_cast(bf16x8, b), c, 0, 0, 0); }
DEV f32x16 mfma32(v4u a, v4u b, f32x16 c) { return __builtin_amdgcn_mfma_f32_32x32x16_bf16(__builtin_bit_cast(bf16x8, a), __builtin_bit_cast(bf16x8, b), c, 0, 0, 0); }

DEV float f_(u32 u) { return __builtin_bit_cast(float, u); }
DEV u32 u_(float f) { return __builtin_bit_cast(u32, f); }
template <int CTRL> DEV float dpp(float v) { return f_((u32)__builtin_amdgcn_update_dpp(0, (int)u_(v), CTRL, 0xf, 0xf, false)); }
DEV float max16(float v) { auto r = __builtin_amdgcn_permlane16_swap(u_(v), u_(v), false, false); return fmaxf(f_(r[0]), f_(r[1])); }
DEV float max32(float v) { auto r = __builtin_amdgcn_permlane32_swap(u_(v), u_(v), false, false); return fmaxf(f_(r[0]), f_(r[1])); }
DEV float add16(float v) { auto r = __builtin_amdgcn_permlane16_swap(u_(v), u_(v), false, false); return f_(r[0]) + f_(r[1]); }
DEV float add32(float v) { auto r = __builtin_amdgcn_permlane32_swap(u_(v), u_(v), false, false); return f_(r[0]) + f_(r[1]); }
DEV float wave_sum(float v) {
    v += dpp<0xB1>(v);
    v += dpp<0x4E>(v);
    v += dpp<0x124>(v);
    v += dpp<0x128>(v);
    return add32(add16(v));
}
DEV bool all_(bool c) { return __builtin_amdgcn_ballot_w64(!c) == 0ull; }
DEV int uni(int v) { return __builtin_amdgcn_readfirstlane(v); }
DEV u32 uu(u32 v) { return (u32)__builtin_amdgcn_readfirstlane((int)v); }
DEV u32 rdl(u32 v, int l) { return (u32)__builtin_amdgcn_readlane((int)v, l); }
DEV u32 rt32() { return (u32)__builtin_amdgcn_s_memrealtime(); }

DEV void wait_flags(rsrc_t rs, u32 off, u32 want, bool& dead) {
    if (dead) return;
    for (int n = 0;; ++n) {
        CFENCE();
        const u32 v = ld4(rs, off);
        if (all_(off >= OOB || v >= want)) break;
        __builtin_amdgcn_s_sleep(2);
        if ((n & 63) == 63) { if (uni((int)ld4(rs, foff(F_ERR))) != 0) { dead = true; break; } }
        if (n > SPIN_CAP) { st4s(rs, foff(F_ERR), 0, 1u); dead = true; break; }
    }
    CFENCE();
}

DEV void pre_post(rsrc_t rs, int idx, u32 stage, v2u ep) { st8s(rs, threadIdx.x == 0 ? foff(F_PRE + idx) : OOB, 0, (v2u){ep.x + stage, ep.y}); }
DEV void pre_poll(rsrc_t rs, int first, int n, u32 stage, v2u ep) {
    const int lane = threadIdx.x & 63;
    for (int it = 0; it < SPIN_CAP; ++it) {
        CFENCE();
        const v2u a = ld8s(rs, foff(F_PRE + first + min(lane, n - 1)), 0), b = ld8s(rs, foff(F_PRE + first + min(lane + 64, n - 1)), 0);
        if (all_(a.y == ep.y && b.y == ep.y && max(a.x - ep.x - stage, b.x - ep.x - stage) < 8u - stage)) break;
        __builtin_amdgcn_s_sleep(2);
    }
    CFENCE();
}
DEV void pre_wait(rsrc_t rs, int first, int n, u32 stage, v2u ep) {
    if (uni(threadIdx.x >> 6) == 0) pre_poll(rs, first, n, stage, ep);
    BAR();
}
DEV u32 get_pol(rsrc_t rs) {
    const int lane = threadIdx.x & 63;
    const u32 raw = ld4(rs, lane < 16 ? foff(F_POL + lane) : OOB);
    return wave_sum((float)(int)raw) >= 0.f ? 1u : 0u;
}
#define NPROBE 16
DEV void pol_probe(rsrc_t rs, int id) {
    const int lane = threadIdx.x & 63;
    const u32 off0 = W_PROBE + (u32)(id >> 2) * 16384u + (u32)(id & 3) * 128u;
    st4s(rs, lane < 32 ? off0 + (u32)lane * 512 : OOB, 0, 0u); DRAIN();
    int d[2] = {1 << 16, 1 << 16};
#pragma unroll 1
    for (int q = 0; q < NPROBE; ++q) {
        const u32 half = (u32)(q ^ (q >> 1) ^ id) & 1u;
        const unsigned long long t0 = __builtin_amdgcn_s_memtime();
        const u32 v = ld4(rs, lane == 0 ? off0 + half * 8192 + (u32)(q >> 1) * 512 : OOB);
        asm volatile("s_waitcnt vmcnt(0)" :: "v"(v) : "memory");
        const int dt = (int)(__builtin_amdgcn_s_memtime() - t0);
        if (half) d[1] = min(d[1], dt); else d[0] = min(d[0], dt);
    }
    const u32 xcc = (u32)__builtin_amdgcn_s_getreg(20 | (3 << 11)) & 0xfu;
    st4s(rs, lane == 0 ? foff(F_POL + id) : OOB, 0, (u32)min(max((d[0] - d[1]) * ((xcc & 4u) ? -1 : 1), -250), 250));
}
DEV f32x2 ln_stats(const f32x2 (&x)[5]) {
    const f32x2 s2 = (x[0] + x[1]) + (x[2] + x[3]) + x[4];
    const float mean = wave_sum(s2.x + s2.y) * (1.0f / DM);
    f32x2 q = {0.f, 0.f};
#pragma unroll
    for (int i = 0; i < 5; ++i) { const f32x2 d = x[i] - mean; q += d * d; }
    const float var = wave_sum(q.x + q.y) * (1.0f / DM);
    const float sd = rbf(__builtin_amdgcn_sqrtf(rbf(rbf(var) + 1e-5f)));
    return (f32x2){rbf(mean), __builtin_amdgcn_rcpf(sd)};
}
DEV void store_h(rsrc_t rs, u32 base, int row, const u32 (&hp)[5]) {
    const u32 lane = threadIdx.x & 63;
    const u32 sw = (u32)(row & 15) * 16u, o8 = (lane >> 5) * 8192u + (((lane & 31u) * 8u) ^ sw), hb = base + (u32)(row >> 5) * (32 * DM * 2) + (u32)(row & 31) * 256u;
    st8s(rs, o8, hb, (v2u){hp[0], hp[1]});
    st8s(rs, o8, hb + 16384, (v2u){hp[2], hp[3]});
    st4s(rs, (lane * 4) ^ sw, hb + 32768, hp[4]);
}

#define DMA16(ldsb, vo, so) asm volatile("s_mov_b32 m0, %0\n\ts_nop 0\n\tbuffer_load_dwordx4 %1, %2, %3 offen sc1 lds" :: "s"(ldsb), "v"(vo), "s"(srd), "s"(so) : "memory")
#define DMA16I(ldsb, lo, vo, so, go) do { u32 so_; asm volatile("s_add_u32 m0, %1, %5\n\ts_add_u32 %0, %4, %6\n\tbuffer_load_dwordx4 %2, %3, %0 offen sc1 lds" \
                         : "=&s"(so_) : "s"(ldsb), "v"(vo), "s"(srd), "s"(so), "i"(lo), "i"(go) : "memory", "scc"); } while (0)
#define DMA32(ldsb, vo, so) asm volatile("s_mov_b32 m0, %0\n\ts_nop 0\n\tbuffer_load_dwordx4 %1, %2, %3 offen sc1 lds\n\tbuffer_load_dwordx4 %1, %2, %3 offen offset:1024 sc1 lds" \
                         :: "s"(ldsb), "v"(vo), "s"(srd), "s"(so) : "memory")
#define DMAIN(sr, ldsb, vo, so) asm volatile("s_mov_b32 m0, %0\n\ts_nop 0\n\tbuffer_load_dwordx4 %1, %2, %3 offen lds" :: "s"(ldsb), "v"(vo), "s"(sr), "s"(so) : "memory")
DEV v4u mk_srd(const void* q, u32 bytes) { return (v4u){uu((u32)(size_t)q), uu((u32)((size_t)q >> 32) & 0xffffu), bytes, 0x00020000u}; }
#define DMANT(sr, ldsb, vo, so) asm volatile("s_mov_b32 m0, %0\n\ts_nop 0\n\tbuffer_load_dwordx4 %1, %2, %3 offen nt lds" :: "s"(ldsb), "v"(vo), "s"(sr), "s"(so) : "memory")
#define MK_SRD() const v4u srd = mk_srd(p.ws, W_END)

constexpr int CV_W = 40960;
template <bool B> struct CvL {
    static constexpr int NF = B ? 5 : 3, NK = B ? 4 : 2, KP = 544 + NK * 256, CH = NF * 1024 + 4 * KP;
};
constexpr int CV_Y = 4 * CvL<false>::CH, CV_YP = 272;
static_assert(CV_Y + 32 * CV_YP <= CV_W && 2 * CvL<true>::CH <= CV_Y, "conv lds");
DEV int cv_chan(int u, int i) { return 16 * (u % 40) + 4 * i + u / 40; }
template <bool B> DEV void cv_filt(const Params& p, u32 dst, int d) {
    const u32 lane = threadIdx.x & 63;
    const v4u sf = mk_srd(p.ssmk + d * SEQ, SEQ * 2);
    DMANT(sf, dst, lane < 4u ? OOB : lane * 16u - 64u, 0);
#pragma unroll
    for (int i = 1; i < CvL<B>::NF; ++i) DMANT(sf, uu(dst + i * 1024), i == CvL<B>::NF - 1 && lane >= 4u ? OOB : lane * 16u, i * 1024 - 64);
}
template <bool B> DEV void cv_kvld(rsrc_t rs, int d, v4u* k) {
#pragma unroll
    for (int i = 0; i < CvL<B>::NK; ++i) k[i] = ld16s(rs, (threadIdx.x & 63) * 16u, uu(W_KVT + (u32)d * (SEQ * 2) + i * 1024));
}
template <bool B> DEV void cv_kvput(lptr F, const v4u* k) {
    const int lane = threadIdx.x & 63;
#pragma unroll
    for (int i = 0; i < CvL<B>::NK; ++i) lds_w16(F + CvL<B>::NF * 1024 + (lane & 3) * CvL<B>::KP + 512 + i * 256 + (lane >> 2) * 16, k[i]);
}
template <bool B> DEV void cv_zero(lptr F) {
    const int lane = threadIdx.x & 63;
#pragma unroll
    for (int i = 0; i < 4; ++i) lds_w16(F + (lane >> 5) * CvL<B>::CH + CvL<B>::NF * 1024 + i * CvL<B>::KP + (lane & 31) * 16, (v4u){0u, 0u, 0u, 0u});
}
#define M32V(d, a, b) asm volatile("v_mfma_f32_32x32x16_bf16 %0, %1, %2, %0" : "+v"(d) : "v"(a), "v"(b))
template <bool B> DEV void cv_run(const Params& p, rsrc_t rs, lptr F, int d, u32 D, f32x4 sp, float (&y)[16]) {
    constexpr int KP = CvL<B>::KP, FO = B ? 2048 : 0;
    const int lane = threadIdx.x & 63, n = lane & 31, hh = lane >> 5, c = lane & 3;
    lptr fa = F + 64 + (32 * hh + 16 * (c & 1) + 8 * ((lane >> 4) & 1) + 4 * (c >> 1) - (lane & 12)) * 2;
    lptr kl = F + CvL<B>::NF * 1024 + (32 + n - hh) * 16;
    v4u dg;
#pragma unroll
    for (int q = 0; q < 4; ++q) { const int k = (lane & 35) ? -1 : (n & 4) + (n >> 3); dg[q] = k == 2 * q ? D : k == 2 * q + 1 ? D << 16 : 0u; }
    v4u l[4], h[4]; v2u fc[4], fh[4];
    f32x16 acc[4];
#pragma unroll
    for (int j = 0; j < 4; ++j) { l[j] = lds_r16(kl + j * KP); if constexpr (B) h[j] = lds_r16(kl + j * KP + 512); }
#pragma unroll
    for (int j = 0; j < 2; ++j) { fc[j] = lds_tr(fa + FO - 32 * j); if constexpr (B) fh[j] = lds_tr(fa - 32 * j); }
    asm volatile("s_nop 1" : "+v"(dg));
#pragma unroll
    for (int j = 0; j < 4; ++j) asm volatile("v_mfma_f32_32x32x16_bf16 %0, %1, %2, 0" : "=&v"(acc[j]) : "v"(dg), "v"(B ? h[j] : l[j]));
    constexpr int U = B ? 2 : 4;
    kl -= 32 * U; fa -= 32;
#pragma unroll 1
    for (int m = 0; m < 32; m += 2 * U) {
#pragma unroll
        for (int z = 0; z < U; ++z) {
            const int c = 2 * (z & 1);
            const v4u f = __builtin_shufflevector(fc[c], fc[c + 1], 0, 1, 2, 3), g = __builtin_shufflevector(fh[c], fh[c + 1], 0, 1, 2, 3);
#pragma unroll
            for (int j = 0; j < 4; ++j) {
                M32V(acc[j], f, l[j]); l[j] = lds_r16(kl + j * KP + 32 * (U - z - 1));
                if (j < 2) fc[(c ^ 2) + j] = lds_tr(fa + FO + (z + 1) * 128 + 32 - 32 * j);
                SCHED();
            }
            if constexpr (B) {
#pragma unroll
                for (int j = 0; j < 4; ++j) {
                    M32V(acc[j], g, h[j]); h[j] = lds_r16(kl + j * KP + 512 + 32 * (U - z - 1));
                    if (j < 2) fh[(c ^ 2) + j] = lds_tr(fa + (z + 1) * 128 + 32 - 32 * j);
                    SCHED();
                }
            }
        }
        kl -= 32 * U; fa += 128 * U;
    }
    asm volatile("s_nop 11" : "+v"(acc[0]), "+v"(acc[1]), "+v"(acc[2]), "+v"(acc[3]));
    const float o[3] = {acc[1][15] + acc[2][14] + acc[3][13], acc[2][15] + acc[3][14], acc[3][15]};
    if constexpr (!B) st16(rs, lane == 63 ? W_SP + (u32)d * 16u : OOB, (v4u){u_(o[0]), u_(o[1]), u_(o[2]), 0u});
#pragma unroll
    for (int i = 0; i < 16; ++i) {
        float v = acc[0][i];
#pragma unroll
        for (int j = 1; j < 4; ++j) if (j <= i) v += acc[j][i - j];
        if (i < 3) { const float e = f_((u32)__builtin_amdgcn_ds_bpermute((hh ? lane - 32 : lane + 31) * 4, (int)u_(o[i]))); v += lane == 0 ? sp[i] : e; }
        y[i] = v;
    }
}
DEV void cv_put(lptr F, int pr, const float (&y0)[16], const float (&y1)[16]) {
    const int lane = threadIdx.x & 63;
#pragma unroll
    for (int i = 0; i < 16; ++i) *(LAS u32*)(F + CV_Y + (lane & 31) * CV_YP + (lane >> 5) * 128 + i * 8 + pr * 4) = pk(y0[i], y1[i]);
}
DEV void cv_out(rsrc_t rs, lptr F, int u, int blk0) {
    const int lane = threadIdx.x & 63;
    lptr q = F + CV_Y + ((lane >> 2) & 3) * CV_YP + ((lane >> 4) * 4 + (lane & 3)) * 8;
    const u32 yo = W_Y + (u32)(((lane >> 2) * 8 + blk0) * DM + 4 * u + (lane & 3)) * 16u;
#pragma unroll
    for (int i = 0; i < 8; ++i) { lptr a = q + (i >> 1) * (8 * CV_YP) + (i & 1) * 128; st16(rs, yo + (u32)((i & 1) * 128 + (i >> 1)) * (DM * 16), tr2(a, a + 4 * CV_YP)); }
}
struct KvIn { v2u k[19], v[16]; v4u s[2]; };
DEV void kvt_load(const Params& p, int tile, KvIn& in) {
    const int lane = threadIdx.x & 63;
    const rsrc_t rk = mk_rsrc(p.bk, ACT), rv = mk_rsrc(p.bv, ACT), rh = mk_rsrc(p.shift, DM * 8);
    const int row = (tile / 10) * 64 + (lane >> 4) * 16, col = (tile % 10) * 64 + (lane & 15) * 4;
    const u32 vo = (u32)(row * DM + col) * 2u, vp = row > 0 ? vo - 3 * (DM * 2) : OOB;
#pragma unroll
    for (int i = 0; i < 2; ++i) in.s[i] = ldin16(rh, (u32)col * 8u + i * 16, 0);
#pragma unroll
    for (int i = 0; i < 19; ++i) { in.k[i] = i < 3 ? ldin8(rk, vp, i * (DM * 2)) : ldin8(rk, vo, (i - 3) * (DM * 2)); if (i >= 3) in.v[i - 3] = ldin8(rv, vo, (i - 3) * (DM * 2)); }
}
DEV void kvt_make(rsrc_t rs, lptr xt, int tile, const KvIn& in) {
    const int lane = threadIdx.x & 63;
#pragma unroll
    for (int c = 0; c < 2; ++c) {
        const v4u sv = in.s[c];
        const f32x2 sh[4] = {{lo_f(sv.x), lo_f(sv.z)}, {hi_f(sv.x), hi_f(sv.z)}, {lo_f(sv.y), lo_f(sv.w)}, {hi_f(sv.y), hi_f(sv.w)}};
        f32x2 kf[19];
#pragma unroll
        for (int i = 0; i < 19; ++i) kf[i] = up2(in.k[i][c]);
        u32 kv[16];
#pragma unroll
        for (int i = 0; i < 16; ++i) kv[i] = pk2(((kf[i] * sh[3] + kf[i + 1] * sh[2]) + (kf[i + 2] * sh[1] + kf[i + 3] * sh[0])) * up2(in.v[i][c]));
#pragma unroll
        for (int q = 0; q < 8; ++q) {
            const u32 sel = (q & 1) ? 0x07060302u : 0x05040100u; const u32* z = kv + (q >> 1);
            *(LAS v2u*)(xt + ((2 * c + (q & 1)) * 16 + (lane & 15)) * 144 + (lane >> 5) * 64 + (q >> 1) * 16 + ((lane >> 4) & 1) * 8) =
                (v2u){__builtin_amdgcn_perm(z[4], z[0], sel), __builtin_amdgcn_perm(z[12], z[8], sel)};
        }
    }
    const u32 o = W_KVT + (u32)(((tile % 10) * 64 + 4 * (lane >> 3)) * SEQ + (tile / 10) * 64 + (lane & 7) * 8) * 2u;
#pragma unroll
    for (int i = 0; i < 8; ++i) st16(rs, o + (u32)(32 * (i & 1) + (i >> 1)) * (SEQ * 2), lds_r16(xt + (8 * i + (lane >> 3)) * 144 + (lane & 7) * 16));
}
struct RowX { v2u a, b; u32 c; };
DEV void ln_row(rsrc_t rs, int row, const RowX& r) {
    const f32x2 x[5] = {up2(r.a.x), up2(r.a.y), up2(r.b.x), up2(r.b.y), up2(r.c)};
    const f32x2 st = ln_stats(x);
    u32 hp[5];
#pragma unroll
    for (int q = 0; q < 5; ++q) hp[q] = pk2(up2(pk2(x[q] - st.x)) * st.y);
    store_h(rs, W_H1, row, hp);
}
DEV void pre_rings(const Params& p, rsrc_t rs, int j) {
    const u32 gi = (u32)(j * 256 + threadIdx.x), gt = gi * 16u, R = NPRE * 4096, n = ((gt >> 14) << 9) + ((gt & 8191u) >> 4);
    v4u m = {PSENT, PSENT, PSENT, PSENT};
    if (n < 640u) {
        m = *(const v4u*)(p.mem0 + (2 * ((n >> 2) & 3) + (n & 1)) * DM + (n >> 4) * 16 + (n & 2) * 4);
#pragma unroll
        for (int i = 0; i < 4; ++i) m[i] = m[i] == PSENT ? 0x7FC17FC1u : m[i];
    }
    st4s(rs, gi - 36u * 256u < F_CLR ? foff(gi - 36u * 256u) : OOB, 0, 0u);
#pragma unroll 1
    for (u32 o = gt; o < MB; o += R) st16(rs, W_ORING + o, (v4u){PSENT, PSENT, PSENT, PSENT});
    const u32 mo = gt < 4 * 32768 + 16384 ? W_MRING + gt : OOB;
    st16(rs, mo, m);
    st16(rs, mo + MB / 4, m);
    static_assert(NPRE * 4096 >= 4 * 32768 + 16384, "memory ring");
}
constexpr int LN0 = NPRE * 4;
DEV void conv_stage(const Params& p, rsrc_t rs, lptr lds, int j, int g0, int ge, v2u ep) {
    const int tid = threadIdx.x, lane = tid & 63, w = uni(tid >> 6), u = j * 4 + w;
    lptr F = lds + w * CV_W;
    const u32 fb = uu(w * CV_W);
    const rsrc_t rd = mk_rsrc(p.ssmD, DM * 2);
    float y0[16], y1[16];
    if (j < NPRE) {
        constexpr int CH = CvL<false>::CH;
        const rsrc_t rx = mk_rsrc(p.x, ACT);
        KvIn in;
        RowX r;
        r.a = ldin8(rx, lane * 8u, uu(u * (DM * 2))); r.b = ldin8(rx, lane * 8u + 512u, uu(u * (DM * 2)));
        r.c = __builtin_amdgcn_raw_buffer_load_b32(rx, lane * 4u + 1024u, uu(u * (DM * 2)), 2);
        kvt_load(p, u, in);
        pre_rings(p, rs, j);
        ln_row(rs, u, r);
        DRAIN(); BAR();
        pre_post(rs, j, 1u, ep);
        if (j == 20) LST(260, rt32());
        const u32 D = __builtin_amdgcn_raw_buffer_load_b16(rd, (u32)cv_chan(u, lane & 3) * 2u, 0, 0);
#pragma unroll 1
        for (int i = 0; i < 4; ++i) cv_filt<false>(p, fb + i * CH, cv_chan(u, i));
        kvt_make(rs, F + CV_Y, u, in);
        cv_zero<false>(F); cv_zero<false>(F + 2 * CH);
        DRAIN(); BAR();
        pre_post(rs, j, 2u, ep);
        if (j == 20) LST(262, rt32());
        pre_wait(rs, 0, NPRE, 2u, ep);
        if (j == 20) LST(281, rt32());
        {
            v4u k[8];
#pragma unroll
            for (int i = 0; i < 4; ++i) cv_kvld<false>(rs, cv_chan(u, i), k + 2 * i);
#pragma unroll
            for (int i = 0; i < 4; ++i) cv_kvput<false>(F + i * CH, k + 2 * i);
        }
        CFENCE();
        kvt_load(p, u + LN0, in);
        if (j == 20) LST(286, rt32());
#pragma unroll 1
        for (int i = 0; i < 4; i += 2) {
            cv_run<false>(p, rs, F + i * CH, cv_chan(u, i), rdl(D, i), f32x4{}, y0);
            cv_run<false>(p, rs, F + (i + 1) * CH, cv_chan(u, i + 1), rdl(D, i + 1), f32x4{}, y1);
            cv_put(F, i >> 1, y0, y1);
        }
        if (j == 20) LST(287, rt32());
        cv_out(rs, F, u, 0);
        DRAIN(); BAR();
        pre_post(rs, j, 3u, ep);
        if (j == 20) LST(263, rt32());
        kvt_make(rs, F + CV_Y, u + LN0, in);
    } else pol_probe(rs, u - NPRE * 4);
    DRAIN(); BAR();
    if (g0 < ge) {
        constexpr int CH = CvL<true>::CH;
        pre_post(rs, j, 4u, ep);
        cv_zero<true>(F);
        pre_wait(rs, 0, NPRE, 4u, ep);
        v4u k[4];
        auto fetch = [&](u32 dst, int d) { cv_filt<true>(p, dst, d); cv_kvld<true>(rs, d, k); };
        fetch(fb, cv_chan(g0 * 4 + w, 0));
#pragma unroll 1
        for (int g = g0; g < ge; g += 8) {
            const int un = g * 4 + w;
            const u32 ch = (u32)cv_chan(un, lane & 3), D = __builtin_amdgcn_raw_buffer_load_b16(rd, ch * 2u, 0, 0);
            const v4u sp = ld16s(rs, W_SP + ch * 16u, 0);
            if (g == 16) { LST(282, rt32()); LST(288, __builtin_amdgcn_s_memtime()); }
#pragma unroll 1
            for (int i = 0; i < 4; i += 2) {
                cv_kvput<true>(F, k);
                fetch(fb + CH, cv_chan(un, i + 1));
                cv_run<true>(p, rs, F, cv_chan(un, i), rdl(D, i), (f32x4){f_(rdl(sp.x, i)), f_(rdl(sp.y, i)), f_(rdl(sp.z, i)), 0.f}, y0);
                cv_kvput<true>(F + CH, k);
                fetch(fb, i == 0 ? cv_chan(un, 2) : cv_chan(min(un + 32, 159), 0));
                cv_run<true>(p, rs, F + CH, cv_chan(un, i + 1), rdl(D, i + 1), (f32x4){f_(rdl(sp.x, i + 1)), f_(rdl(sp.y, i + 1)), f_(rdl(sp.z, i + 1)), 0.f}, y1);
                cv_put(F, i >> 1, y0, y1);
            }
            cv_out(rs, F, un, 4);
            if (g == 16) { LST(283, rt32()); LST(289, __builtin_amdgcn_s_memtime()); }
        }
        DRAIN(); BAR();
    }
    pre_post(rs, j, 5u, ep);
    if (j == 20) LST(284, rt32());
    if (j == 3) LST(285, rt32());
}

#define M32A(first, d, a, b) do { if (first) asm volatile("v_mfma_f32_32x32x16_bf16 %0, %1, %2, 0" : "=&v"(d) : "a"(a), "v"(b)); \
                                  else asm volatile("v_mfma_f32_32x32x16_bf16 %0, %1, %2, %0" : "+v"(d) : "a"(a), "v"(b)); } while (0)
#define M32B(first, d, a, b) do { if (first) asm volatile("v_mfma_f32_32x32x16_bf16 %0, %1, %2, 0" : "=&v"(d) : "v"(a), "a"(b)); \
                                  else asm volatile("v_mfma_f32_32x32x16_bf16 %0, %1, %2, %0" : "+v"(d) : "v"(a), "a"(b)); } while (0)
#define VCVT(d, a, b) asm volatile("v_cvt_pk_bf16_f32 %0, %1, %2" : "=v"(d) : "v"(a), "v"(b))
#define LD4A(d, vo, so) asm volatile("buffer_load_dword %0, %1, %2, %3 offen sc1" : "=v"(d) : "v"(vo), "s"(srd), "s"(so) : "memory")
DEV u32 wst_src(int lane, int ld) { return (u32)((lane >> 4) * ld + 8 * ((lane & 15) ^ ((lane >> 4) << 2))) * 2u; }
DEV u32 wst_rd(int lane, int w) { const int q4 = (lane >> 2) & 3; return (u32)(q4 * 256 + ((w ^ q4) * 64) + ((lane >> 4) & 1) * 32 + (lane & 3) * 8); }

constexpr int QK_A = 65536;
constexpr int QK_SEL = 40960;
#define QK_W0 8
DEV void role_qkv(const Params& p, lptr lds, int c, v2u ep) {
    const int tid = threadIdx.x, lane = tid & 63, w = uni(tid >> 6), hh = lane >> 5, r32 = lane & 31;
    const int m = c >> 5, h = c & 31;
    const rsrc_t rs = mk_rsrc(p.ws, W_END);
    v4u wt[40];
    {
        const v4u sw = mk_srd(m == 0 ? p.Wq : m == 1 ? p.Wk : p.Wv, DM * NQ * 2), sx = mk_srd(p.x, ACT);
        const u32 vo = wst_src(lane, NQ) + (u32)h * (DH * 2), x8 = (u32)lane * 8u, x4 = (u32)lane * 4u;
        RowX xr[5];
        auto wfill = [&](int i0, int n) {
#pragma unroll 1
            for (int i = i0; i < i0 + n; ++i) DMAIN(sw, uu((4 * i + w) * 1024), vo, uu((16 * i + 4 * w) * (NQ * 2)));
        };
        wfill(0, QK_W0);
#pragma unroll
        for (int i = 0; i < 5; ++i)
            asm volatile("buffer_load_dwordx2 %0, %3, %5, %6 offen nt\n\tbuffer_load_dwordx2 %1, %3, %5, %6 offen offset:512 nt\n\tbuffer_load_dword %2, %4, %5, %6 offen offset:1024 nt"
                         : "=&v"(xr[i].a), "=&v"(xr[i].b), "=&v"(xr[i].c) : "v"(x8), "v"(x4), "s"(sx), "s"(uu((LN0 + 4 * c + w + 384 * i) * (DM * 2))) : "memory");
        wfill(QK_W0, 40 - QK_W0);
        if (c == 3) LST(275, rt32());
        VMW(40 - QK_W0);
        if (c == 3) LST(276, rt32());
#pragma unroll
        for (int i = 0; i < 5; ++i) {
            asm volatile("" : "+v"(xr[i].a), "+v"(xr[i].b), "+v"(xr[i].c));
            const int row = LN0 + 4 * c + w + 384 * i;
            if (i < 4 || row < SEQ) ln_row(rs, row, xr[i]);
        }
        if (c == 3) LST(264, rt32());
        VMW(12); BAR();
        lptr tb = lds + hh * 2048 + wst_rd(lane, w);
        SFOR(k, 40)
            lptr q = tb + (k >> 3) * 32768 + (k & 7) * 4096; wt[k] = tr2(q, q + 1024); asm volatile("" : "+a"(wt[k]));
        SEND
        DRAIN(); BAR();
        pre_post(rs, 96 + c, 1u, ep);
    }
    if (c == 3) LST(268, rt32());
    bool dead = false;
    int n0 = 0, n1 = 0, lim0 = 2;
    const u32 t16 = (u32)tid * 16u;
    u32 ar[8];
#pragma unroll
    for (int j = 0; j < 8; ++j) ar[j] = (u32)(r32 * 256 + ((2 * j + hh) ^ (r32 & 15)) * 16);
    MK_SRD();
#define XRD(ks) lds_r16(lds + ar[(ks) & 7] + ((ks) >> 3) * 8192)
    const u32 h2flag = (lane < 32 && w == 0) ? foff(F_H2 + lane) : OOB;
    const u32 ol = (m == 2 ? (u32)(2 * w + (r32 >> 4)) * 1024u : (u32)((r32 >> 4) * 4 + w) * 1024u) + (u32)(hh * 16 + (r32 & 15)) * 16u
                 + (m == 2 ? W_VO : m == 1 ? W_KQ : W_KQ + 8192u) + (u32)h * 16384u;
    const u32 fo = tid == 0 ? foff(F_QKV + c) : OOB;
    lptr selw = lds + QK_SEL + (w == 0 ? 0 : 64), selr = lds + QK_SEL;
#define QFLIP() asm volatile("v_xor_b32 %0, 16, %0\n\tv_xor_b32 %1, 16, %1" : "+v"(selw), "+v"(selr))
    auto late = [&](u32 fv) { const u32 need = n1 >= NSEG ? 0xFFFFFFFFu : dead ? 0u : (u32)n1 + 1u; return (u32)__builtin_amdgcn_ballot_w64(fv < need); };
    auto take = [&](u32 lt) -> int {
        const int item = lt == 0u ? (64 | n1) : n0 < lim0 ? n0 : -1;
        n1 += lt == 0u; n0 += item == n0;
        return item;
    };
    static_assert(W_H2 - W_H1 == 2 * 64 * (32 * DM * 2), "h");
    auto src = [&](int item) { return W_H1 + (u32)(item + (item & 64)) * (32 * DM * 2); };
    v4u x[8];
    pre_wait(rs, 0, NPRE, 1u, ep);
    if (c == 3) LST(269, rt32());
    u32 fill = uu(QK_A + w * 1024);
    auto run = [&](auto isv) __attribute__((always_inline)) {
      constexpr bool V = decltype(isv)::value;
      for (;;) {
        int cur;
        u32 lt;
        {
            if (lim0 < NSEG && n0 >= lim0) return;
            if (n0 >= NSEG && n1 >= NSEG) return;
            if (w == 0 && n0 >= NSEG) wait_flags(rs, h2flag, (u32)n1 + 1, dead);
            QFLIP(); CFENCE(); *(LAS u32*)selw = late(ld4(rs, h2flag));
            BAR();
            cur = take(uu(*(LAS u32*)selr));
            const u32 so = uu(src(cur)), dst = fill ^ QK_A;
            SFOR(i, 10) DMA16I(dst, i * 4096, t16, so, i * 4096); SEND
            QFLIP(); CFENCE(); *(LAS u32*)selw = late(ld4(rs, h2flag));
            DRAIN(); BAR();
            lt = *(LAS u32*)selr;
#pragma unroll
            for (int ks = 0; ks < 5; ++ks) x[ks] = XRD(ks);
        }
        f32x16 a0, a1 = {};
        u32 oa = OOB, pfo = OOB, pfv = 0, qfo = OOB, qfv = 0;
        v4u pp, pq;
#define QCV(d, i, j) VCVT(d, a1[i], a1[j])
#define QPK(a_, q_) do { if constexpr (V) QCV(pq[q_], 4 * (a_) + 8 * ((q_) >> 1) + 2 * ((q_) & 1), 4 * (a_) + 8 * ((q_) >> 1) + 2 * ((q_) & 1) + 1); else QCV(pq[q_], 4 * (a_) + (q_), 4 * (a_) + (q_) + 8); } while (0)
        do {
            u32 nsrc, nvo, fv, hb; int nxt;
#define QT(k) do {} while (0)
            QT(0);
            SCHED();
            SFOR(ks, 40)
                if constexpr (ks < 20) { if constexpr (V) M32B(ks == 0, a0, x[ks & 7], wt[ks]); else M32A(ks == 0, a0, wt[ks], x[ks & 7]); }
                else if constexpr (ks == 20) {
                    if constexpr (V) asm volatile("v_mfma_f32_32x32x16_bf16 %0, %1, %2, %3" : "=&v"(a1) : "v"(x[ks & 7]), "a"(wt[ks]), "v"(a0));
                    else asm volatile("v_mfma_f32_32x32x16_bf16 %0, %1, %2, %3" : "=&v"(a1) : "a"(wt[ks]), "v"(x[ks & 7]), "v"(a0));
                } else { if constexpr (V) M32B(false, a1, x[ks & 7], wt[ks]); else M32A(false, a1, wt[ks], x[ks & 7]); }
                x[(ks + 5) & 7] = XRD(ks < 35 ? ks + 5 : ks - 35);
                if constexpr (ks == 36) lt = *(LAS u32*)selr;
                SCHED();
                if constexpr (ks == 0) { lt = uu(lt); PINS(lt); }
                if constexpr (ks == 1) { nxt = take(lt); PINS(nxt); PINS(n0); PINS(n1); }
                if constexpr (ks == 2) { nsrc = src(nxt < 0 ? 0 : nxt); nvo = t16 | (nxt < 0 ? OOB : 0u); PINS(nsrc); PIN(nvo); }
                if constexpr (ks == 4) { QPK(0, 0); QPK(0, 1); QPK(0, 2); QPK(0, 3); pp = pq; }
                if constexpr (ks == 7) { QPK(1, 0); QPK(1, 1); QPK(1, 2); QPK(1, 3); }
                if constexpr (ks >= 3 && ks < 33 && ks % 3 == 0) DMA16I(fill, (ks / 3 - 1) * 4096, nvo, nsrc, (ks / 3 - 1) * 4096);
                if constexpr (ks == 10) { hb = (u32)cur * (NH * 16384u); PINS(hb); }
                if constexpr (ks == 16) LD4A(fv, h2flag, 0);
                if constexpr (ks == 18) { QT(1); asm volatile("s_waitcnt vmcnt(7)\n\ts_barrier" ::: "memory"); QT(2); }
                if constexpr (ks == 19) { st4s(rs, pfo, 0, pfv); pfo = qfo; pfv = qfv; }
                if constexpr (ks == 20) st16(rs, oa, pp);
                if constexpr (ks == 22) st16(rs, oa + 512, pq);
                if constexpr (ks == 25) { asm volatile("" :: "v"(a0)); QFLIP(); }
                if constexpr (ks == 26) { oa = ol + hb; PIN(oa); }
                if constexpr (ks == 37) { qfo = fo + (u32)(cur >> 6) * (96 * 128); qfv = (u32)(cur & 63) + 1; asm volatile("" : "+v"(qfo), "+v"(qfv)); }
                if constexpr (ks >= 27 && ks < 35) asm volatile("v_xor_b32 %0, 0x10000, %0" : "+v"(ar[(ks + 5) & 7]));
                if constexpr (ks == 32) { QT(3); asm volatile("s_waitcnt vmcnt(7)" : "+v"(fv) :: "memory"); QT(4); lt = late(fv); PINS(lt); }
                if constexpr (ks == 33) { PIN(lt); *(LAS u32*)selw = lt; }
                if constexpr (ks == 34) { QT(5); BAR(); QT(6); }
                if constexpr (ks == 39) { fill ^= QK_A; cur = nxt; PINS(fill); }
                SCHED();
            SEND
        } while (cur >= 0);
        asm volatile("s_nop 9" : "+v"(a1));
        QPK(0, 0); QPK(0, 1); QPK(0, 2); QPK(0, 3);
        st16(rs, oa, pq);
        SCHED(); NOPS(1);
        QPK(1, 0); QPK(1, 1); QPK(1, 2); QPK(1, 3);
        st16(rs, oa + 512, pq);
        DRAIN(); BAR();
        st4s(rs, pfo == qfo ? OOB : pfo, 0, pfv);
        st4s(rs, qfo, 0, qfv);
      }
    };
#pragma unroll 1
    for (int ph = 0; ph < 2; ++ph) {
        if (m == 2) run(IC<1>{}); else run(IC<0>{});
        if (c == 3 && ph == 0) LST(270, rt32());
        if (ph == 0) pre_wait(rs, 96, 96, 1u, ep);
        lim0 = NSEG;
    }
}

constexpr int OU_SEL = 131072;
DEV void role_out(const Params& p, lptr lds, int L, int kq, int ng, v2u ep) {
    const int tid = threadIdx.x, lane = tid & 63, w = uni(tid >> 6), hh = lane >> 5, r32 = lane & 31;
    const rsrc_t rs = mk_rsrc(p.ws, W_END);
    v4u wo[64];
    bool dead = false;
    {
        const v4u sw = mk_srd(p.Wo, DM * NQ * 2);
        const u32 vo = wst_src(lane, DM) + (u32)ng * 256u;
        auto fill = [&](int b) {
#pragma unroll 1
            for (int i = 0; i < 8; ++i) DMAIN(sw, uu((b % 5) * 32768 + (4 * i + w) * 1024), vo, uu((kq * 1024 + b * 128 + 16 * i + 4 * w) * (DM * 2)));
        };
#pragma unroll 1
        for (int b = 0; b < 5; ++b) fill(b);
        lptr tb = lds + hh * 1024 + wst_rd(lane, w);
        SFOR(b, 8)
            VMW(b < 4 ? 32 : 56 - 8 * b); BAR();
            lptr q = tb + (b % 5) * 32768;
#pragma unroll
            for (int k = 0; k < 8; ++k) { lptr a = q + (k >> 1) * 8192 + (k & 1) * 2048; wo[8 * b + k] = tr2(a, a + 4096); if (b < 7) asm volatile("" : "+a"(wo[8 * b + k])); else PIN(wo[8 * b + k]); }
            if constexpr (b < 3) { BAR(); fill(b + 5); }
        SEND
    }
    pre_wait(rs, 0, NPRE, 1u, ep);
    if (kq == 0 && ng == 0) LST(272 + L, rt32());
    MK_SRD();
    const u32 ofl = (lane < 8 && w == 0) ? foff(F_O + L * 32 + kq * 8 + lane) : OOB;
    const u32 l16 = (u32)lane * 16u;
    const u32 fo = (tid == 0) ? foff(F_P + L * 32 + kq * 5 + ng) : OOB;
    auto osrc = [&](int s) { return uu(W_VO + 8192u + (u32)((L * NSEG + s) * NH + kq * 8) * 16384u + w * 1024); };
#define ODMA(i, dst, vo, so) DMA16I(dst, (i) * 4096, vo, so, ((i) >> 1) * 16384 + ((i) & 1) * 4096)
    const u32 pvo = W_P + (u32)(L * NSEG) * P_ITEM + (u32)(4 * hh * 4 + kq) * (DM * 4) + (u32)(ng * 128 + w * 32 + r32) * 4u;
    lptr selw = lds + OU_SEL + (w == 0 ? 0 : 64);
    bool staged = false;
    u32 pfo = OOB;
    u32 buf = 0;
    BAR();
    for (int s = 0; s < NSEG; ++s) {
        const u32 nsrc = osrc(s + 1), ndst = uu((buf ^ 65536u) + w * 1024);
        lptr A = lds + buf + l16;
        if (!staged) {
            if (w == 0) wait_flags(rs, ofl, (u32)s + 1, dead);
            BAR();
            const u32 dst = uu(buf + w * 1024), src = osrc(s);
            SFOR(i, 16) ODMA(i, dst, l16, src); SEND
            VMW(10);
        }
        asm volatile("s_waitcnt vmcnt(26)\n\ts_barrier" ::: "memory");
        LS2(L == 0 && w == 0, 96 + kq * 5 + ng);
        v4u o[64];
#pragma unroll
        for (int k = 0; k < 3; ++k) o[k] = lds_r16(A + k * 1024);
        u32 nvo, fv; int rdy;
        f32x16 acc;
        SCHED();
        SFOR(k, 64)
            if constexpr (k < 56) M32B(k == 0, acc, o[k], wo[k]); else asm volatile("v_mfma_f32_32x32x16_bf16 %0, %1, %2, %0" : "+v"(acc) : "v"(o[k]), "v"(wo[k]));
            if constexpr (k == 21) rdy = *(LAS int*)(lds + OU_SEL);
            if constexpr (k < 61) o[k + 3] = lds_r16(A + (k + 3) * 1024);
            SCHED();
            if constexpr (k == 0) LD4A(fv, ofl, 0);
            if constexpr (k == 19) {
                asm volatile("s_waitcnt vmcnt(0)" : "+v"(fv) :: "memory");
                *(LAS int*)selw = (s + 1 < NSEG && ((u32)__builtin_amdgcn_ballot_w64(fv < (u32)s + 2) & 255u) == 0u) ? 1 : 0;
            }
            if constexpr (k == 20) { BAR(); st4s(rs, pfo, 0, (u32)s); }
            if constexpr (k == 24) { rdy = uni(rdy); nvo = l16 | (rdy ? 0u : OOB); PIN(nvo); }
            if constexpr (k >= 26 && k < 58 && (k & 1) == 0) ODMA((k - 26) >> 1, ndst, nvo, nsrc);
            SCHED();
        SEND
        staged = rdy != 0;
        u32 pa = pvo + (u32)s * P_ITEM;
        asm volatile("s_nop 9" : "+v"(acc), "+v"(pa));
#pragma unroll
        for (int i = 0; i < 16; ++i) st4s(rs, pa, (u32)((8 * (i >> 2) + (i & 3)) * (4 * DM * 4)), u_(acc[i]));
        SCHED();
        if (staged) pfo = fo;
        else { DRAIN(); BAR(); st4s(rs, fo, 0, (u32)s + 1); pfo = OOB; }
        buf ^= 65536u;
        LS2(L == 0 && w == 0, 160 + kq * 5 + ng);
    }
}

constexpr int RW_P = 20480, RW_X = RW_P + 10240;
DEV void role_row(const Params& p, lptr lds, int id, v2u ep) {
    const int lane = threadIdx.x & 63, w = uni(threadIdx.x >> 6);
    const int ri = id & 31, L = id >> 5;
    const rsrc_t rs = mk_rsrc(p.ws, W_END);
    bool dead = false, have = false;
    const u32 fp = lane < 20 ? foff(F_P + L * 32 + lane) : OOB, l16 = (u32)lane * 16u, lx = lane < 16 ? l16 + 1024u : OOB, fb = uu(w * CV_W);
    lptr F = lds + w * CV_W;
    MK_SRD();
    const v4u sx = mk_srd(p.x, ACT);
    auto yfill = [&](int b) {
#pragma unroll 1
        for (int i = 0; i < 5; ++i) DMA32(uu(fb + (b & 1) * 10240 + i * 2048), l16, uu(W_Y + (u32)(ri * 8 + b) * 10240u + i * 2048));
    };
    auto xfill = [&](int s) {
        const u32 d = uu(fb + RW_X + (s & 1) * 2048), o = uu((u32)(s * 32 + ri) * (DM * 2));
        if (L == 0) { DMANT(sx, d, l16, o); DMANT(sx, d + 1024, lx, o); } else { DMA16(d, l16, o + W_X2); DMA16(d + 1024, lx, o + W_X2); }
    };
    auto pfill = [&](int s) {
        const u32 o = uu(W_P + (u32)(L * NSEG + s) * P_ITEM + (u32)ri * (4 * DM * 4));
        SFOR(i, 10) DMA16I(fb, RW_P + i * 1024, l16, o, i * 1024); SEND
        if (L == 1) xfill(s);
    };
    if (L == 0) xfill(0);
    pre_poll(rs, 0, NPRE, 3u, ep);
    if (id == 0) LST(274, rt32());
    yfill(0); DRAIN();
    u32 fv = 0;
    for (int s = 0; s < NSEG; ++s) {
        if (!have) {
            if (!all_(fp == OOB || fv >= (u32)s + 1)) wait_flags(rs, fp, (u32)s + 1, dead);
            pfill(s);
        }
        LS2(L == 0, 192 + ri);
        f32x2 y[5];
        {
            lptr ya = F + ((s >> 3) & 1) * 10240 + (s & 7) * 2 + lane * 16;
            auto yr = [&](lptr q) { return lo_f(*(LAS u16*)q); };
            y[0] = (f32x2){yr(ya), yr(ya + 2560)}; y[1] = (f32x2){yr(ya + 5120), yr(ya + 7680)};
            y[2] = (f32x2){yr(ya + 1024), yr(ya + 3584)}; y[3] = (f32x2){yr(ya + 6144), yr(ya + 8704)};
            lptr yc = ya - lane * 16 + 2048 + (lane >> 1) * 16 + (lane & 1) * 5120;
            y[4] = (f32x2){yr(yc), yr(yc + 2560)};
            asm volatile("" : "+v"(y[0]), "+v"(y[1]), "+v"(y[2]), "+v"(y[3]), "+v"(y[4]));
        }
        if (!have) DRAIN();
        f32x2 o[5], x1[5];
#pragma unroll
        for (int kq = 0; kq < 4; ++kq) {
            lptr q = F + RW_P + kq * (DM * 4);
            const f32x4 a = *(LAS f32x4*)(q + l16), b = *(LAS f32x4*)(q + 1024 + l16); const f32x2 c = *(LAS f32x2*)(q + 2048 + lane * 8);
            if (kq == 0) { o[0] = a.lo; o[1] = a.hi; o[2] = b.lo; o[3] = b.hi; o[4] = c; }
            else { o[0] += a.lo; o[1] += a.hi; o[2] += b.lo; o[3] += b.hi; o[4] += c; }
        }
        {
            lptr q = F + RW_X + (s & 1) * 2048;
            const v2u xa = *(LAS v2u*)(q + lane * 8), xb = *(LAS v2u*)(q + 512 + lane * 8);
            const u32 xi[5] = {xa.x, xa.y, xb.x, xb.y, *(LAS u32*)(q + 1024 + lane * 4)};
#pragma unroll
            for (int i = 0; i < 5; ++i) x1[i] = up2(pk2(up2(xi[i]) + up2(pk2(o[i]))));
            asm volatile("" : "+v"(x1[0]), "+v"(x1[1]), "+v"(x1[2]), "+v"(x1[3]), "+v"(x1[4]));
        }
        have = s + 1 < NSEG && all_(fp == OOB || fv >= (u32)s + 2);
        if (__builtin_expect(have, 0)) pfill(s + 1);
        if (L == 0) xfill(s + 1);
        const f32x2 st1 = ln_stats(x1);
        u32 xp[5];
#pragma unroll
        for (int i = 0; i < 5; ++i) xp[i] = pk2(x1[i] + ((x1[i] - st1.x) * st1.y) * y[i]);
        const int row = s * 32 + ri;
        if (L == 0) {
            const u32 ro = (u32)row * (DM * 2);
            f32x2 x2[5];
#pragma unroll
            for (int i = 0; i < 5; ++i) x2[i] = up2(xp[i]);
            st8s(rs, (u32)lane * 8, W_X2 + ro, (v2u){xp[0], xp[1]});
            st8s(rs, (u32)lane * 8 + 512, W_X2 + ro, (v2u){xp[2], xp[3]});
            st4s(rs, (u32)lane * 4 + 1024, W_X2 + ro, xp[4]);
            const f32x2 st2 = ln_stats(x2);
            u32 hp[5];
#pragma unroll
            for (int i = 0; i < 5; ++i) hp[i] = pk2((x2[i] - st2.x) * st2.y);
            store_h(rs, W_H2, row, hp);
        } else {
            u16* o_ = p.out + (size_t)row * DM;
            *(v2u*)(o_ + 4 * lane) = (v2u){xp[0], xp[1]};
            *(v2u*)(o_ + 256 + 4 * lane) = (v2u){xp[2], xp[3]};
            *(u32*)(o_ + 512 + 2 * lane) = xp[4];
        }
        CFENCE();
        fv = ld4(rs, fp);
        asm volatile("s_waitcnt vmcnt(0)" : "+v"(fv) :: "memory");
        st4s(rs, lane == 0 && L == 0 ? foff(F_H2 + ri) : OOB, 0, (u32)s + 1);
        if ((s & 7) == 3 && s < NSEG - 8) {
            if (s == 27) pre_poll(rs, 0, NCV, 5u, ep);
            if (s == 43) st8s(rs, (id == 32 && lane == 0) ? foff(F_EPOCH) : OOB, 0, (v2u){ep.x + 8u, ep.y});
            yfill((s >> 3) + 1);
        }
        if (ri == 0) LST(320 + L * 64 + s, rt32());
        LS2(L == 0, 224 + ri);
    }
}

constexpr float SCALE = 0.08838834764831845f, LOG2E = 1.4426950408889634f;
constexpr int C_K = 0, C_V = 8192, C_QX = 16384, C_MEM = 24576, C_Q = C_MEM + 10240, C_KM = C_Q + 2304, C_VM = C_KM + 2304, C_DUMP = C_VM + 2048,
              C_ZERO = C_DUMP + 1024, C_WL = C_ZERO + 128;
constexpr int NLF = 14;
static_assert(C_WL % 16 == 0 && C_WL + 4 * NLF * 2048 <= LDS_BYTES, "head lds");
#define CH_VIS 10
#define CH_H0 124
#define CH_W0 88
#define CH_MH 4
#define CH_MW 4
#define CH_REC 20
#define CH_UP 4
#define CH_WX 6
DEV void wait_until(u32 t) {
    unsigned long long c;
    asm volatile("1:\n\ts_memrealtime %0\n\ts_waitcnt lgkmcnt(0)\n\ts_mov_b64 vcc, %0\n\ts_sub_u32 vcc_lo, %1, vcc_lo\n\ts_cmpk_lt_u32 vcc_lo, 400\n\ts_cbranch_scc1 1b" : "=&s"(c) : "s"(uu(t - 1u)) : "vcc", "scc", "memory");
}
#define WAIT_TXT(tmp, t1) "s_memrealtime vcc\n1:\n\ts_waitcnt lgkmcnt(0)\n\ts_sub_u32 " tmp ", " t1 ", vcc_lo\n\ts_memrealtime vcc\n\ts_cmpk_lt_u32 " tmp ", 400\n\ts_cbranch_scc1 1b\n\t"
#define SMF_A(acc, a, b) asm volatile("v_smfmac_f32_16x16x64_bf16 %0, %1, %2, %3" : "+v"(acc) : "v"(a), "a"(b), "v"(idx))
#define SMF_V(acc, a, b) asm volatile("v_smfmac_f32_16x16x64_bf16 %0, %1, %2, %3" : "+v"(acc) : "v"(a), "v"(b), "v"(idx))
#define MF0(d, a, b) asm volatile("v_mfma_f32_16x16x32_bf16 %0, %1, %2, 0" : "=&v"(d) : "v"(a), "v"(b))
#define MF1(d, a, b) asm volatile("v_mfma_f32_16x16x32_bf16 %0, %1, %2, %0" : "+v"(d) : "v"(a), "v"(b))
#define MF0H(d, a, b) asm volatile("v_mfma_f32_16x16x16_bf16 %0, %1, %2, 0" : "=&v"(d) : "v"(a), "v"(b))
#define MF1H(d, a, b) asm volatile("v_mfma_f32_16x16x16_bf16 %0, %1, %2, %0" : "+v"(d) : "v"(a), "v"(b))
DEV v8u tr_frag(lptr p0, lptr p1, lptr p2, lptr p3) { return cat8(tr2(p0, p1), tr2(p2, p3)); }
constexpr int qslot(int ks) { return (ks & 1) && ks < 8 ? ks >> 1 : -1; }
constexpr int kslot(int ks) { return ks < 4 ? 4 + ks : -1; }
constexpr int vslot(int ks) { return ks >= 4 ? 4 + ks : -1; }
DEV u32 ring_map(u32 off, u32 half) { return ((off >> 13) << 14) + (half << 13) + (off & 8191u); }
#define ZRD(x, i) x = __builtin_bit_cast(f32x4, lds_r16(ZP + 16 * (i)))
#define VMAX(d, a, b) asm volatile("v_max_f32 %0, %1, %2" : "=v"(d) : "v"(a), "v"(b))
#define VMAX3(d, a, b, c_) asm volatile("v_max3_f32 %0, %1, %2, %3" : "=v"(d) : "v"(a), "v"(b), "v"(c_))
#define VADD(d, a, b) asm volatile("v_add_f32 %0, %1, %2" : "=v"(d) : "v"(a), "v"(b))
#define VEXP(d, a) asm volatile("v_exp_f32 %0, %1" : "=v"(d) : "v"(a))
#define VFMC(d, a, b) asm volatile("v_fmamk_f32 %0, %1, 0x3e0293ee, %2" : "=v"(d) : "v"(a), "v"(b))
#define SEL(d, a, b) asm volatile("v_cndmask_b32 %0, %1, %2, %3" : "=v"(d) : "v"(a), "v"(b), "s"(himask))
#define QPERM(d, a, q) asm volatile("v_mov_b32_dpp %0, %1 quad_perm:" q " row_mask:0xf bank_mask:0xf" : "=v"(d) : "v"(a))

DEV void role_head(const Params& p, lptr lds, int L, int h, v2u ep) {
    const int tid = threadIdx.x, lane = tid & 63, w = uni(tid >> 6), g = lane >> 4, r = lane & 15;
    const rsrc_t rs = mk_rsrc(p.ws, W_END);
    v8u wq0[10], wq1[10], wk0[10], wk1[10], wv0[10], wv1[10];
    lptr WL = lds + C_WL + w * (NLF * 2048) + lane * 16;
    {
        constexpr int GS = 1056, CS = 48 * GS;
        const v4u sq = mk_srd(p.Wq, DM * NQ * 2), sk = mk_srd(p.Wk, DM * NQ * 2), sv = mk_srd(p.Wv, DM * NQ * 2);
        const u32 vo = (u32)(g * NQ + h * DH + 8 * (r ^ ((g & 1) << 3))) * 2u;
        auto fill = [&](int ch) {
#pragma unroll 1
            for (int i = 0; i < 4; ++i) {
                const u32 dst = uu((ch % 3) * CS + (4 * i + w) * GS), so = uu((ch * 64 + 16 * i + 4 * w) * (NQ * 2));
                DMAIN(sq, dst, vo, so); DMAIN(sk, dst + 16 * GS, vo, so); DMAIN(sv, dst + 32 * GS, vo, so);
            }
        };
        __builtin_amdgcn_s_sleep(ST_HEAD);
        fill(0); fill(1); fill(2);
        const int q4 = r >> 2;
        lptr tb = lds + (4 * (g >> 1) + 2 * (q4 >> 1) + (g & 1)) * GS + (q4 & 1) * 256 + ((w * 64 + (r & 3) * 8) ^ ((q4 & 1) << 7));
        SFOR(ch, 10)
            VMW(ch < 8 ? 24 : 108 - 12 * ch);
            BAR();
            auto frag = [&](int m, int t) { lptr b = tb + (ch % 3) * CS + m * 16 * GS + t * 32; return tr_frag(b, b + 512, b + 8 * GS, b + 8 * GS + 512); };
            wq0[ch] = frag(0, 0); wq1[ch] = frag(0, 1); wk0[ch] = frag(1, 0); wk1[ch] = frag(1, 1); wv0[ch] = frag(2, 0); wv1[ch] = frag(2, 1);
            asm volatile("" : "+a"(wq0[ch]), "+a"(wk0[ch]), "+v"(wv0[ch]), "+v"(wv1[ch]));
            if constexpr (qslot(ch) < 0) asm volatile("" : "+a"(wq1[ch])); else asm volatile("" : "+v"(wq1[ch]));
            if constexpr (kslot(ch) < 0) asm volatile("" : "+a"(wk1[ch])); else asm volatile("" : "+v"(wk1[ch]));
            BAR();
            if constexpr (ch < 7) fill(ch + 3);
        SEND
#pragma unroll
        for (int ch = 0; ch < 10; ++ch) {
            if (qslot(ch) >= 0) { lds_w16(WL + qslot(ch) * 2048, wq1[ch].lo); lds_w16(WL + qslot(ch) * 2048 + 1024, wq1[ch].hi); }
            if (kslot(ch) >= 0) { lds_w16(WL + kslot(ch) * 2048, wk1[ch].lo); lds_w16(WL + kslot(ch) * 2048 + 1024, wk1[ch].hi); }
            if (vslot(ch) >= 0) { lds_w16(WL + vslot(ch) * 2048, wv1[ch].lo); lds_w16(WL + vslot(ch) * 2048 + 1024, wv1[ch].hi); }
        }
        CFENCE();
    }
    const u32 idx = (r & 2) ? 0xEEEEu : 0x4444u;
    const unsigned long long himask = 0xCCCCCCCCCCCCCCCCull;
    bool dead = false;
    if (h == 3) LST(266 + L, rt32());
    pre_wait(rs, 0, NCV, 1u, ep);
    const u32 pol0 = uu(get_pol(rs));
    const u32 l16 = (u32)lane * 16u;
    const u32 mring = W_MRING + (u32)L * (MB / 4) + pol0 * 8192u, hint = mring + 4 * 32768u;
    const u32 fkh = lane < 3 ? foff(F_QKV + L * 96 + lane * 32 + h) : lane < 6 ? hint + (u32)(lane - 3) * 4u : OOB;
    u32 tm = 0;
    MK_SRD();
    u32 ksrc = uu(W_KQ + (u32)(L * NSEG * NH + h) * 16384u + w * 2048), vsrc = ksrc + (W_VO - W_KQ);
    u32 dk = uu(C_K + w * 2048), dv = dk + (C_V - C_K), dq = dk + (C_QX - C_K), dm = uu(C_MEM + w * 2048), dm2 = uu(w < 2 ? C_MEM + 8192 + w * 1024 : C_DUMP);
    asm volatile("" : "+s"(dk), "+s"(dv), "+s"(dq), "+s"(dm), "+s"(dm2));
    wait_flags(rs, lane < 3 ? fkh : OOB, 1u, dead);
    DMA32(dk, l16, ksrc); DMA32(dv, l16, vsrc); DMA32(dq, l16, ksrc ^ 8192u);
    lptr qw = lds + C_Q + (2 * g) * 272 + (32 * w + 2 * r) * 2;
    lptr qr = lds + C_Q + (2 * (r >> 2) + (r & 1)) * 272 + g * 16;
    lptr LB = lds + l16, VX = lds + C_V + w * 2048 + l16;
    if (tid >= 64 && tid < 70) { const u32 z = tid == 68 ? 0x3F803F80u : tid == 69 ? PSENT : 0u; lds_w16(lds + C_ZERO + (tid - 64) * 16, (v4u){z, z, z, z}); }
    u32 zaddr = 0; PIN(zaddr);
    lptr ZP = lds + zaddr + C_ZERO;
    const float CEXP = SCALE * LOG2E;
#define LF_RD(slot, hf) lf[slot][hf] = lds_r16(WL + (slot) * 2048 + (hf) * 1024)
    BAR();
    const u32 ofl = tid == 0 ? foff(F_O + L * 32 + h) : OOB;
    u32 oso = (g & 1) ? OOB : W_ORING + (u32)L * (MB / 2) + ring_map((u32)((r >> 3) * 32768 + h * 1024 + w * 256 + (g >> 1) * 128 + (r & 7) * 16), pol0);
    unsigned long long vis[8];
#pragma unroll
    for (int i = 0; i < 8; ++i) vis[i] = __builtin_amdgcn_ballot_w64(16 * (i >> 2) + 4 * g + (i & 3) <= 16 * (w & 1) + r);
    u32 missed = 0;
    for (int s = 0; s < NSEG;) {
        u32 ms = mring + (u32)(s & 3) * 32768u + w * 2048, ms2 = mring + (u32)(s & 3) * 32768u + 16384u + (w & 1) * 1024, sv = (u32)s;
        asm volatile("" : "+s"(ms), "+s"(ms2)); PIN(sv);
        f32x4 aq0, aq1, ak0, ak1;
        v4u lf[NLF][2];
        ZRD(aq0, 0); ZRD(aq1, 1); ZRD(ak0, 2); ZRD(ak1, 3);
#pragma unroll
        for (int i = 0; i < 4; ++i) { LF_RD(i, 0); LF_RD(i, 1); }
        SCHED();
        v4u A[10];
#define HT(k) do {} while (0)
        v4u kx[8], qf[4], kmf[4], vx[2], ones, P;
        float fa, fb, fc, fd, mx, my, sx[4], t[4], tm0, tm1; u32 pa, pb, fl, e01, e23, pmm; unsigned long long nanq;
        f32x4 av0, av1, s0, s1, sm, oa, ob, rsum;
        v2u vm, pm;
#define LW(sl) cat8(lf[sl][0], lf[sl][1])
#define PQ(ks, tl) do { if constexpr (!(tl)) SMF_A(aq0, A[ks], wq0[ks]); else if constexpr (qslot(ks) < 0) SMF_A(aq1, A[ks], wq1[ks]); else { const v8u lw_ = LW(qslot(ks)); SMF_V(aq1, A[ks], lw_); } } while (0)
#define PK(ks, tl) do { if constexpr (!(tl)) SMF_A(ak0, A[ks], wk0[ks]); else if constexpr (kslot(ks) < 0) SMF_A(ak1, A[ks], wk1[ks]); else { const v8u lw_ = LW(kslot(ks)); SMF_V(ak1, A[ks], lw_); } } while (0)
#define PV(ks, tl) do { if constexpr (!(tl)) SMF_V(av0, A[ks], wv0[ks]); else if constexpr (vslot(ks) < 0) SMF_V(av1, A[ks], wv1[ks]); else { const v8u lw_ = LW(vslot(ks)); SMF_V(av1, A[ks], lw_); } } while (0)
#define KXR(i) kx[i] = lds_r16(LB + C_K + ((i) & 1) * 4096 + ((i) >> 1) * 1024)
#define XW(q_) do { *(LAS u32*)(q_) = pa; *(LAS u32*)((q_) + 272) = pb; } while (0)
        auto slot = [&](auto zc) __attribute__((always_inline)) {
            constexpr int z = decltype(zc)::value;
            if constexpr (z < 14) PQ(z >> 1, z & 1);
            else if constexpr (z < 17) PQ(z - 7, 0);
            else if constexpr (z < 20) PQ(z - 10, 1);
            else if constexpr (z < 33) PK((z - 20) >> 1, z & 1);
            else if constexpr (z < 35) { if constexpr (z == 33) MF0(s0, kx[0], qf[0]); else MF0(s1, kx[1], qf[0]); }
            else if constexpr (z < 41) { if constexpr (z & 1) MF1(s0, kx[z - 33], qf[(z - 33) >> 1]); else MF1(s1, kx[z - 33], qf[(z - 33) >> 1]); }
            else if constexpr (z == 41) PK(6, 1);
            else if constexpr (z < 45) PK(z - 35, 0);
            else if constexpr (z < 48) PK(z - 38, 1);
            else if constexpr (z < 58) PV(z - 48, 0);
            else if constexpr (z < 61) PV(z < 60 ? z - 54 : 0, 1);
            else if constexpr (z == 61) MF0(sm, kmf[0], qf[0]);
            else if constexpr (z < 65) MF1(sm, kmf[z - 61], qf[z - 61]);
            else if constexpr (z == 65) MF0(oa, vx[0], P);
            else if constexpr (z == 66) MF0(ob, vx[1], P);
            else PV(6, 1);
            if constexpr (z < 6) A[4 + z] = lds_r16(LB + C_MEM + (4 + z) * 1024);
            if constexpr (z == 6 || z == 7) LF_RD(4, z - 6);
            if constexpr (z == 8 || z == 9) LF_RD(5, z - 8);
            if constexpr (z == 12 || z == 13) LF_RD(6, z - 12);
            if constexpr (z == 16 || z == 17) KXR(z - 16);
            if constexpr (z == 18 || z == 19) LF_RD(7, z - 18);
            if constexpr (z >= 20 && z < 24) KXR(z - 18);
            if constexpr (z == 32 || z == 33) KXR(z - 26);
            if constexpr (z == 24) XW(qw);
            if constexpr (z == 25) ZRD(av0, 0);
            if constexpr (z == 26) ZRD(av1, 1);
            if constexpr (z == 27) asm volatile("s_waitcnt lgkmcnt(2)\n\ts_barrier" ::: "memory");
            if constexpr (z >= 28 && z < 32) qf[z - 28] = lds_r16(qr + (z - 28) * 64);
            if constexpr (z == 35) fl = ld4(rs, fkh);
            if constexpr (z == 52) XW(qw + (C_KM - C_Q));
            if constexpr (z == 53 || z == 54) vx[z - 53] = lds_r16(VX + (z - 53) * 1024);
            if constexpr (z == 55) asm volatile("s_waitcnt lgkmcnt(2)\n\ts_barrier" ::: "memory");
            if constexpr (z >= 56 && z < 60) kmf[z - 56] = lds_r16(qr + (C_KM - C_Q) + (z - 56) * 64);
            if constexpr (z >= 48 && z < 52) LF_RD(8 + ((z - 48) >> 1), z & 1);
            if constexpr (z >= 60) LF_RD(10 + ((z - 60) >> 1), z & 1);
            SCHED();
            if constexpr (z == 19) HT(2);
            if constexpr (z == 32) HT(3);
            if constexpr (z == 47) HT(4);
            if constexpr (z == 64) HT(5);
            if constexpr (z == 20) VADD(fa, aq0[0], aq0[2]);
            if constexpr (z == 21) VADD(fb, aq0[1], aq0[3]);
            if constexpr (z == 22) { VADD(fc, aq1[0], aq1[2]); VADD(fd, aq1[1], aq1[3]); }
            if constexpr (z == 23) { asm volatile("v_cmp_u_f32 %0, %1, %2" : "=s"(nanq) : "v"(fa), "v"(fb)); VCVT(pa, fa, fc); VCVT(pb, fb, fd); }
            if constexpr (z == 39 || z == 40 || z == 41) NOPS(1);
            if constexpr (z == 42) VMAX3(mx, s0[0], s0[1], s0[2]);
            if constexpr (z == 43) { VMAX3(mx, mx, s0[3], s1[0]); VMAX3(my, s1[1], s1[2], s1[3]); }
            if constexpr (z == 44 || z == 46) { VMAX(fa, mx, my); VMAX(my, mx, my); mx = fa; }
            if constexpr (z == 45) { SEL(sx[0], s0[0], s1[0]); asm volatile("v_permlane16_swap_b32 %0, %1" : "+v"(mx), "+v"(my)); }
            if constexpr (z == 47) { SEL(sx[1], s0[1], s1[1]); asm volatile("v_permlane32_swap_b32 %0, %1" : "+v"(mx), "+v"(my)); }
            if constexpr (z == 48) asm volatile("v_max_f32 %0, %0, %1\n\tv_mul_f32 %0, 0xbe0293ee, %0" : "+v"(mx) : "v"(my));
            if constexpr (z == 49) { VADD(fa, ak0[0], ak0[2]); VADD(fb, ak0[1], ak0[3]); }
            if constexpr (z == 50) { VADD(fc, ak1[0], ak1[2]); VADD(fd, ak1[1], ak1[3]); }
            if constexpr (z == 51) { VCVT(pa, fa, fc); VCVT(pb, fb, fd); }
            if constexpr (z == 52) SEL(sx[2], s0[2], s1[2]);
            if constexpr (z == 53) { SEL(sx[3], s0[3], s1[3]); VFMC(t[0], sx[0], mx); }
            if constexpr (z == 54) { VFMC(t[1], sx[1], mx); VFMC(t[2], sx[2], mx); }
            if constexpr (z == 55) VFMC(t[3], sx[3], mx);
            if constexpr (z >= 56 && z < 60) VEXP(t[z - 56], t[z - 56]);
            if constexpr (z == 60) { VCVT(e01, t[0], t[1]); VCVT(e23, t[2], t[3]); }
            if constexpr (z == 61) { QPERM(P.x, e01, "[0,1,0,1]"); QPERM(P.z, e01, "[2,3,2,3]"); }
            if constexpr (z == 62) { QPERM(P.y, e23, "[0,1,0,1]"); QPERM(P.w, e23, "[2,3,2,3]"); }
            if constexpr (z == 63) { VADD(fa, av0[0], av0[2]); VADD(fb, av0[1], av0[3]); }
            if constexpr (z == 64) VCVT(vm.x, fa, fb);
            if constexpr (z == 67) { asm volatile("" :: "v"(sm)); VFMC(tm0, sm[0], mx); VFMC(tm1, sm[1], mx); }
            SCHED();
        };
        {
            u32 tmp;
            asm volatile("s_mov_b32 m0, %2\n\t" WAIT_TXT("%0", "%1") "buffer_load_dwordx4 %4, %5, %6 offen sc1 lds\n\tbuffer_load_dwordx4 %4, %5, %6 offen offset:1024 sc1 lds\n\t"
                         "s_mov_b32 m0, %3\n\ts_nop 0\n\tbuffer_load_dwordx4 %4, %5, %7 offen sc1 lds\n\ts_waitcnt vmcnt(0) lgkmcnt(0)\n\ts_barrier"
                         : "=&s"(tmp) : "s"(uu(tm - 1u)), "s"(dm), "s"(dm2), "v"(l16), "s"(srd), "s"(ms), "s"(ms2) : "vcc", "scc", "memory");
        }
#pragma unroll
        for (int ks = 0; ks < 4; ++ks) A[ks] = lds_r16(LB + C_MEM + ks * 1024);
        SCHED();
        st4s(rs, ofl, 0, sv);
        SCHED();
        static_for<0, 24>(slot);
        if (__builtin_expect(nanq != 0ull, 0)) {
            bool miss = false;
#pragma unroll
            for (int i = 0; i < 10; ++i) miss |= A[i].x == PSENT || A[i].y == PSENT || A[i].z == PSENT || A[i].w == PSENT;
            nanq = missed < SPIN_CAP ? __builtin_amdgcn_ballot_w64(miss) : 0ull;
        }
        if (__builtin_expect(nanq == 0ull, 1)) {
        static_for<24, 68>(slot);
        PV(7, 1); ones = lds_r16(ZP + 64); SCHED();
        VMAX(fa, tm0, tm1);
        if (__builtin_expect(__builtin_amdgcn_ballot_w64(fa > 24.0f) != 0ull, 0)) {
            const float up = fmaxf(max32(max16(fmaxf(tm0, tm1))), 0.f);
            tm0 -= up; tm1 -= up;
#pragma unroll
            for (int i = 0; i < 4; ++i) t[i] = __builtin_amdgcn_exp2f(__builtin_fmaf(sx[i], CEXP, mx) - up);
            e01 = pk(t[0], t[1]); e23 = pk(t[2], t[3]);
            asm volatile("s_nop 1" : "+v"(e01), "+v"(e23));
            QPERM(P.x, e01, "[0,1,0,1]"); QPERM(P.z, e01, "[2,3,2,3]"); QPERM(P.y, e23, "[0,1,0,1]"); QPERM(P.w, e23, "[2,3,2,3]");
            NOPS(1);
            MF0(oa, vx[0], P); MF0(ob, vx[1], P);
            NOPS(7);
        }
        SCHED();
        PV(1, 1); VEXP(tm0, tm0); SEL(oa[0], oa[0], ob[0]); SCHED();
        PV(8, 1); VEXP(tm1, tm1); SEL(oa[1], oa[1], ob[1]); SCHED();
        PV(9, 1); VCVT(pmm, tm0, tm1); SEL(oa[2], oa[2], ob[2]); SCHED();
        PV(2, 1);
        asm volatile("v_cndmask_b32 %0, %2, 0, %3\n\tv_cndmask_b32 %1, 0, %2, %3" : "=&v"(pm.x), "=&v"(pm.y) : "v"(pmm), "s"(himask)); SCHED();
        PV(3, 1); SEL(oa[3], oa[3], ob[3]); SCHED();
        MF0(rsum, ones, P); NOPS(4);
        VADD(fc, av1[0], av1[2]); VADD(fd, av1[1], av1[3]);
        { const v2u ones2 = ones.lo; MF1H(rsum, ones2, pm); }
        VCVT(vm.y, fc, fd); NOPS(1);
        MF1H(oa, vm, pm);
        {
            float inv; v4u oc;
            asm volatile("s_nop 3\n\tv_rcp_f32 %0, %1\n\ts_nop 2" : "=v"(inv) : "v"(rsum[0]), "v"(rsum));
            PIN(oa);
            const f32x2 i2 = {inv, inv};
            const f32x2 u0 = oa.lo * i2, u1 = oa.hi * i2;
            asm volatile("v_cvt_pk_bf16_f32 %0, %4, %5\n\tv_cvt_pk_bf16_f32 %2, %4, %5\n\tv_cvt_pk_bf16_f32 %1, %6, %7\n\tv_cvt_pk_bf16_f32 %3, %6, %7\n\t"
                         "v_permlane16_swap_b32 %0, %2\n\ts_nop 0\n\tv_permlane16_swap_b32 %1, %3"
                         : "=&v"(oc.x), "=&v"(oc.y), "=&v"(oc.z), "=&v"(oc.w) : "v"(u0.x), "v"(u0.y), "v"(u1.x), "v"(u1.y));
            st16(rs, oso, oc);
        }
        SCHED();
        unsigned long long clk;
        asm volatile("s_memrealtime %0" : "=s"(clk));
        HT(6);
        PIN(fl);
        v4u tq[4], tk[8], tv[2];
        f32x4 c0, c1, cm;
        u32 oo, kvo; bool kvok;
        u32 ln = l16; PIN(ln);
        lptr QX = lds + C_QX + (w & 1) * 4096 + ln, VO = lds + C_V + (w ^ 1) * 2048 + ln, VMX = lds + C_VM + w * 512 + (ln >> 1);
#pragma unroll
        for (int i = 0; i < 2; ++i) { tq[i] = lds_r16(QX + i * 1024); tk[2 * i] = lds_r16(LB + C_K + i * 1024); tk[2 * i + 1] = lds_r16(LB + C_K + 4096 + i * 1024); }
        *(LAS v2u*)VMX = vm;
        SCHED();
        SFOR(i, 12)
            if constexpr (i == 0) MF0(cm, kmf[0], tq[0]); else if constexpr (i == 1) MF0(c0, tk[0], tq[0]); else if constexpr (i == 2) MF0(c1, tk[1], tq[0]);
            else if constexpr (i % 3 == 0) MF1(cm, kmf[i / 3], tq[i / 3]); else if constexpr (i % 3 == 1) MF1(c0, tk[2 * (i / 3)], tq[i / 3]); else MF1(c1, tk[2 * (i / 3) + 1], tq[i / 3]);
            if constexpr (i == 0) { tq[2] = lds_r16(QX + 2048); tk[4] = lds_r16(LB + C_K + 2048); }
            if constexpr (i == 1) { tk[5] = lds_r16(LB + C_K + 6144); tq[3] = lds_r16(QX + 3072); }
            if constexpr (i == 2) { tk[6] = lds_r16(LB + C_K + 3072); tk[7] = lds_r16(LB + C_K + 7168); }
            if constexpr (i == 3) { tv[0] = lds_r16(VO); tv[1] = lds_r16(VO + 1024); }
            if constexpr (i == 4) { oo = ln + (ln & 0x300u) + ((vsrc ^ 8192u) + (u32)((w & 1) * 256)); PIN(oo); }
            if constexpr (i == 5) { ksrc += NH * 16384; vsrc += NH * 16384; PINS(ksrc); PINS(vsrc); }
            if constexpr (i == 7) { kvok = ((u32)__builtin_amdgcn_ballot_w64(fl < (u32)s + 2) & 7u) == 0u; }
            if constexpr (i == 8) { kvo = l16 | (kvok ? 0u : OOB); PIN(kvo); }
            SCHED();
        SEND
        asm volatile("s_waitcnt lgkmcnt(0)\n\ts_barrier" : "+s"(clk) :: "memory");
        const v2u tvm = *(LAS v2u*)(VMX + (512 - (w & 1) * 1024));
        const v4u pv = lds_r16(ZP + 80);
        DMA32(dk, kvo, ksrc);
        SCHED(); asm volatile("" :: "v"(cm));
        float u[10], m, m2;
        {
            float NEG = -1000000000.0f / SCALE; PIN(NEG);
#define MSK(d, a, i) asm volatile("v_cndmask_b32 %0, %1, %2, %3" : "=v"(d) : "v"(NEG), "v"(a), "s"(vis[i]))
#pragma unroll
            for (int i = 0; i < 4; ++i) MSK(u[i], c0[i], i);
#pragma unroll
            for (int i = 0; i < 4; ++i) MSK(u[4 + i], c1[i], 4 + i);
            asm volatile("v_max3_f32 %0, %2, %3, %10\n\tv_max3_f32 %1, %4, %5, %11\n\tv_max3_f32 %0, %0, %6, %7\n\tv_max3_f32 %1, %1, %8, %9\n\t"
                         "v_max_f32 %0, %0, %1\n\tv_mov_b32 %1, %0\n\ts_nop 1\n\tv_permlane16_swap_b32 %0, %1\n\tv_max_f32 %0, %0, %1\n\tv_mov_b32 %1, %0\n\ts_nop 1\n\t"
                         "v_permlane32_swap_b32 %0, %1\n\tv_max_f32 %0, %0, %1\n\tv_mul_f32 %0, 0xbe0293ee, %0"
                         : "=&v"(m), "=&v"(m2) : "v"(u[0]), "v"(u[1]), "v"(u[2]), "v"(u[3]), "v"(u[4]), "v"(u[5]), "v"(u[6]), "v"(u[7]), "v"(cm[0]), "v"(cm[1]));
        }
        DMA32(dv, kvo, vsrc);
        {
            v4u px; v2u p0, p1;
#define EX2(d, a, b) asm volatile("v_fmamk_f32 %0, %2, 0x3e0293ee, %4\n\tv_fmamk_f32 %1, %3, 0x3e0293ee, %4\n\tv_exp_f32 %0, %0\n\tv_exp_f32 %1, %1" : "=&v"(d.x), "=&v"(d.y) : "v"(a), "v"(b), "v"(m))
            f32x2 ex[5];
            EX2(ex[0], u[0], u[1]); EX2(ex[1], u[2], u[3]); EX2(ex[2], u[4], u[5]); EX2(ex[3], u[6], u[7]); EX2(ex[4], cm[0], cm[1]);
            asm volatile("v_cvt_pk_bf16_f32 %0, %8, %9\n\tv_cvt_pk_bf16_f32 %1, %10, %11\n\tv_cvt_pk_bf16_f32 %2, %12, %13\n\tv_cvt_pk_bf16_f32 %3, %14, %15\n\t"
                         "v_mov_b32 %5, 0\n\tv_mov_b32 %6, 0\n\tv_cvt_pk_bf16_f32 %4, %16, %17\n\tv_cvt_pk_bf16_f32 %7, %16, %17"
                         : "=&v"(px.x), "=&v"(px.y), "=&v"(px.z), "=&v"(px.w), "=&v"(p0.x), "=&v"(p0.y), "=&v"(p1.x), "=&v"(p1.y)
                         : "v"(ex[0].x), "v"(ex[0].y), "v"(ex[1].x), "v"(ex[1].y), "v"(ex[2].x), "v"(ex[2].y), "v"(ex[3].x), "v"(ex[3].y), "v"(ex[4].x), "v"(ex[4].y));
            f32x4 o[4], sum;
            const v2u ones2 = ones.lo;
            const u32 now = (u32)clk;
            u32 oz;
            SCHED();
            MF0(sum, ones, px); { const u32 q = ksrc ^ 8192u; DMA32(dq, kvo, q); } SCHED();
            MF0(o[0], vx[0], px);
            MF0(o[1], vx[1], px);
            MF0(o[2], tv[0], px); oz = (oso & ~(3u << 17)) | (u32)((s + 3) & 3) << 17; PIN(oz); SCHED();
            MF0(o[3], tv[1], px); oso = (oso & ~(3u << 17)) | (u32)((s + 1) & 3) << 17; PIN(oso); SCHED();
            MF1H(sum, ones2, p0);
            MF1H(o[0], vm, p0);
            MF1H(o[1], vm, p1); st16(rs, oz, pv); SCHED();
            MF1H(o[2], tvm, p0);
            MF1H(o[3], tvm, p1);
            float inv;
            asm volatile("s_nop 2\n\tv_rcp_f32 %0, %1\n\ts_nop 0" : "=v"(inv) : "v"(sum[0]), "v"(sum));
            asm volatile("" : "+v"(o[0]), "+v"(o[1]));
            st16(rs, oo, (v4u){pk2(o[0].lo * inv), pk2(o[0].hi * inv), pk2(o[1].lo * inv), pk2(o[1].hi * inv)});
            asm volatile("" : "+v"(o[2]), "+v"(o[3]));
            st16(rs, oo ^ 2048u, (v4u){pk2(o[2].lo * inv), pk2(o[2].hi * inv), pk2(o[3].lo * inv), pk2(o[3].hi * inv)});
            SCHED();
            if (__builtin_expect(h == 0 && w == 3, 0)) {
                st4s(rs, ln == 0 ? hint + 128u : OOB, 0, (u32)s << 16 | min(missed, 1u) << 14 | min(now - tm, 0x3FFFu));
                LST(L * 64 + s, now); LST(128 + L * 64 + s, ((int)(rdl(fl, 3) - now) >= CH_VIS - 4) | ((int)kvok << 1) | ((int)min(missed, 3u) << 2)); LST(640 + L * 64 + s, tm); LST(1728 + L * 64 + s, __builtin_amdgcn_s_memtime());
            }
            tm = rdl(fl, 5);
            missed = missed == SPIN_CAP ? missed : 0u;
            if (__builtin_expect((int)(rdl(fl, 3) - now) < CH_VIS - 4, 0)) {
                for (int n = 0; n < 8; ++n) {
                    CFENCE();
                    const u32 hv = ld4(rs, fkh);
                    tm = rdl(hv, 5);
                    if ((int)(rdl(hv, 3) - now) >= CH_VIS - 4) break;
                }
            }
        }
        LS2(L == 0 && w == 0, h);
        if (__builtin_expect(!kvok, 0) && s + 1 < NSEG) {
            st4s(rs, (ln == 0 && w == 0) ? hint + 4096u + (u32)h * 128u : OOB, 0, (u32)s + 1u);
            wait_flags(rs, ln < 48u ? fkh : OOB, (u32)s + 2, dead);
            DMA32(dk, l16, ksrc); DMA32(dv, l16, vsrc); DMA32(dq, l16, ksrc ^ 8192u);
        }
        ++s;
        } else {
            u32 ln = l16; PIN(ln);
            const u32 hv = ld4(rs, ln == 96u ? foff(F_ERR) : fkh);
            tm = rdl(hv, 5);
            *(LAS u32*)(lds + C_DUMP + (w == 0 ? 0 : 64)) = rdl(hv, 6);
            BAR();
            missed = uu(*(LAS u32*)(lds + C_DUMP)) != 0u ? SPIN_CAP : missed + 1;
            st4s(rs, missed == SPIN_CAP ? foff(F_ERR) : OOB, 0, 1u);
            BAR();
        }
    }
    DRAIN(); BAR();
    st4s(rs, ofl, 0, NSEG);
}

#define WO_GAP 6
DEV void role_wo(const Params& p, lptr lds, int j, v2u ep) {
    const int tid = threadIdx.x, lane = tid & 63, w = uni(tid >> 6), g = lane >> 4, r = lane & 15;
    const int rg = j & 1, cg = j >> 1, rot = cg & 7;
    const rsrc_t rs = mk_rsrc(p.ws, W_END);
    v8u wo[8][5];
    {
        const v4u so_ = mk_srd(p.Wo, DM * NQ * 2);
        u32 vo[5];
#pragma unroll
        for (int i = 0; i < 5; ++i) { const int n = 64 * i + lane; vo[i] = (u32)((n / 5) * DM + cg * 40 + (n % 5) * 8) * 2u; }
        auto fill = [&](int b) {
            const u32 dst = uu(w * 40960 + (b & 3) * 10240), src = uu((8 * w + ((b + rot) & 7)) * (DH * DM * 2));
#pragma unroll
            for (int i = 0; i < 10; ++i) DMAIN(so_, dst + i * 1024, vo[i % 5], src + (i / 5) * (64 * DM * 2));
        };
        __builtin_amdgcn_s_sleep(ST_WO);
#pragma unroll 1
        for (int b = 0; b < 4; ++b) fill(b);
        const int q4 = r >> 2;
        lptr tb = lds + w * 40960 + (32 * (g >> 1) + 16 * (q4 >> 1) + 8 * ((r >> 1) & 1) + 4 * (g & 1) + (q4 & 1)) * 80 + (r & 1) * 8;
        SFOR(b, 8)
            VMW(b < 5 ? 30 : 70 - 10 * b);
#pragma unroll
            for (int t = 0; t < 5; ++t) {
                lptr q = tb + (b & 3) * 10240 + t * 16;
                wo[b][t] = tr_frag(q, q + 160, q + 64 * 80, q + 64 * 80 + 160);
                if (b * 5 + t < 32) asm volatile("" : "+a"(wo[b][t])); else asm volatile("" : "+v"(wo[b][t]));
            }
            CFENCE();
            if constexpr (b < 4) fill(b + 4);
        SEND
    }
    if (j == 0) LST(265, rt32());
    pre_wait(rs, 0, NCV, 1u, ep);
    const u32 idx = (r & 2) ? 0xEEEEu : 0x4444u;
    bool dead = false;
    const u32 pol0 = uu(get_pol(rs));
    const u32 l16 = (u32)lane * 16u;
    u32 oo[8];
#pragma unroll
    for (int i = 0; i < 8; ++i) oo[i] = uu(W_ORING + ring_map((u32)(rg * 32768 + (8 * w + ((i + rot) & 7)) * 1024), pol0));
    const u32 hint = W_MRING + 4 * 32768u + pol0 * 8192u;
    u32 hvo[2];
#pragma unroll
    for (int l = 0; l < 2; ++l) hvo[l] = lane < 2 ? hint + (u32)(l ^ 1) * (MB / 4) + (u32)lane * 4u : lane == 2 ? hint + (u32)l * (MB / 4) + 128u : lane == 3 ? foff(F_ERR) :
                 (lane >= 32 && j == 0 && w == 3) ? hint + (u32)l * (MB / 4) + (u32)lane * 128u : OOB;
    lptr xw = r < 8 ? lds + w * 640 + (2 * (g & 1) + (g >> 1)) * 32 + r * 4 : lds + 8192 + lane * 4;
    const int G = ((5 * cg) >> 1) + w, tt = 2 * G + ((lane >> 3) & 1) - 5 * cg;
    const bool pub = w < 3 && lane < 32 && tt >= 0 && tt < 5;
    lptr xr = lds + (pub ? tt * 128 + (2 * (lane >> 4) + ((lane >> 2) & 1)) * 32 + (lane & 3) * 8 : 0);
    const u32 mpo = pub ? W_MRING + ring_map((u32)(G * 256 + rg * 128 + lane * 4), pol0) : OOB;
    constexpr u32 PER0 = CH_W0 + CH_MW + CH_H0 + CH_MH + 2 * CH_VIS;
    const bool lead = j == 0 && w == 3;
    const u32 hso = (lead && lane == 0) ? hint : OOB;
    MK_SRD();
    u32 t = 0, tn = 0;
    if (j == 0) {
        for (int n = 0; n < SPIN_CAP && !dead; ++n) {
            CFENCE();
            if (all_(ld4(rs, W_ORING + pol0 * 8192u + (u32)w * 16384u + (u32)lane * 128u) != PSENT)) break;
            __builtin_amdgcn_s_sleep(1);
        }
        BAR();
    }
    if (lead) {
        t = rt32() + 40u; tn = t + PER0 / 2 + CH_REC;
        st16(rs, lane < 2 ? hint + (u32)lane * (MB / 4) : OOB, (v4u){lane == 0 ? t : tn, PER0, (lane == 0 ? t : tn) + (CH_W0 + CH_VIS + CH_MW + CH_REC), 0u});
    } else {
        for (int n = 0; n < SPIN_CAP && !dead; ++n) {
            CFENCE();
            const u32 hv = ld4(rs, hvo[1]);
            t = rdl(hv, 0);
            if (rdl(hv, 1) < 400u) break;
            __builtin_amdgcn_s_sleep(1);
        }
    }
    int sn[2] = {0, 0};
    u32 hav = 4 * CH_H0, wav = 4 * CH_W0, mh = 8 * CH_MH, mw = 8 * CH_MW, ok[2] = {0u, 0u}, dw[2] = {CH_W0 + CH_VIS + CH_MW + CH_REC, CH_W0 + CH_VIS + CH_MW + CH_REC};
    u32 zero = 0, hv = 0xFFFFu, pst = 1, pdur = 0, pmp = OOB;
    const u32 psent = PSENT;
    lptr sw = lds + 2560 + w * 4;
    bool stop = false;
    asm volatile("" : "+v"(zero), "+v"(sw));
    auto turn = [&](auto lc) __attribute__((always_inline)) {
        constexpr int L = decltype(lc)::value, M = L ^ 1;
        u32 now, done, rep, rd, lost, dh, dn, nt, za; v4u hw;
        const int sc = sn[L];
        auto books = [&](auto ic) __attribute__((always_inline)) {
            constexpr int i = decltype(ic)::value;
            if constexpr (i == 0) { rep = rdl(hv, 2); PINS(rep); }
            if constexpr (i == 1) { lost = (u32)(__builtin_amdgcn_ballot_w64(hv == (u32)sn[M]) >> 32); PINS(lost); }
            if constexpr (i == 2) { lost |= (rep >> 16) ^ (u32)sn[M]; PINS(lost); }
            if constexpr (i == 3) { lost |= ok[M] ^ 2u; rd = rep & 0xFFFFu; PINS(lost); PINS(rd); }
            if constexpr (i == 4) { lost = lost == 0u ? CH_UP * 8u : 0u; PINS(lost); }
            if constexpr (i == 5) { dn = wav >> 2; PINS(dn); pdur = min(pdur, dn + 8u); PINS(pdur); }
            if constexpr (i == 6) { pdur = (done & ok[M]) ? pdur : dn; PINS(pdur); }
            if constexpr (i == 7) { wav += pdur - dn; PINS(wav); }
            if constexpr (i == 8) { dh = hav >> 2; PINS(dh); nt = min(rd, dh + 8u); PINS(nt); }
            if constexpr (i == 9) { nt = rd < 0x3FFFu ? nt : dh; PINS(nt); }
            if constexpr (i == 10) { hav += nt - dh; PINS(hav); }
            if constexpr (i == 11) { mw = max(mw - 1u, CH_MW * 8u); PINS(mw); }
            if constexpr (i == 12) { mh = max(mh - 1u, CH_MH * 8u); PINS(mh); }
            if constexpr (i == 13) { rd = rd >> 14 == 1u ? lost : 0u; PINS(rd); }
            if constexpr (i == 14) { mw += rd; mh += lost - rd; PINS(mw); PINS(mh); }
            if constexpr (i == 15) { ok[M] = done ? 1u : (ok[M] & 1u) << 1; PINS(ok[M]); }
            if constexpr (i == 16) { dh = (2u * hav + mh) / 8u + CH_VIS; PINS(dh); }
            if constexpr (i == 17) { dn = (2u * wav + mw) / 8u + CH_VIS; PINS(dn); }
            if constexpr (i == 18) { nt = dw[L] + dh; PINS(nt); }
            if constexpr (i == 19) { dh += dn; dw[L] = dn; sn[M] += (int)done; PINS(dh); PINS(sn[M]); }
            if constexpr (i == 20) { rd = tn - t + (dh >> 1); PINS(rd); }
            if constexpr (i == 21) { rd = sn[M] + 1 < NSEG ? rd : 0u; PINS(rd); }
            if constexpr (i == 22) { nt = t + (u32)max((int)nt, (int)rd); PINS(nt); }
            if constexpr (i == 23) { hw.x = nt; hw.y = dh; PIN(hw.x); PIN(hw.y); }
            if constexpr (i == 24) { hw.z = nt + dn; hw.w = 0u; PIN(hw); }
            if constexpr (i == 25) st16(rs, hso + L * (MB / 4), hw);
        };
        if (__builtin_expect(sc + 1 < NSEG, 1)) {
            u32 vo = l16 + (L * (MB / 2) + (u32)(sc & 3) * 131072u), mp, hn;
            PIN(vo);
#define WT(k) do {} while (0)
            WT(0);
            v4u d[8];
            {
                u32 tmp;
                asm volatile(WAIT_TXT("%1", "%2") "buffer_load_dwordx4 %0, %3, %4, %5 offen sc1\n\ts_waitcnt lgkmcnt(0)"
                             : "=&v"(d[0]), "=&s"(tmp) : "s"(uu(t - 1u)), "v"(vo), "s"(srd), "s"(oo[0]) : "vcc", "scc", "memory");
            }
            SFOR(i, 7)
                asm volatile("buffer_load_dwordx4 %0, %1, %2, %3 offen sc1" : "=v"(d[i + 1]) : "v"(vo), "s"(srd), "s"(oo[i + 1]) : "memory");
                SCHED();
                if constexpr (i == 0) { done = uu(pst) ^ 1u; PINS(done); }
                static_for<3 * i, 3 * i + 3>(books);
                SCHED();
            SEND
            hn = ld4(rs, hvo[L]);
            SCHED();
            WT(1);
            static_for<21, 26>(books);
            za = done ? pmp : OOB;
            st4s(rs, za, 0, psent);
            *(LAS u32*)(sw + L * 4096) = zero;
            f32x4 acc[5] = {};
            mp = mpo + (L * (MB / 4) + (u32)((sc + 1) & 3) * 32768u); pmp = mpo + (L * (MB / 4) + (u32)(sc & 3) * 32768u);
            asm volatile("" : "+v"(acc[0]), "+v"(acc[1]), "+v"(acc[2]), "+v"(acc[3]), "+v"(acc[4]), "+v"(mp), "+v"(pmp));
            SCHED();
            WT(2);
            float f0[5], f1[5]; unsigned long long bad;
#define WSW(o_) asm volatile("v_permlane32_swap_b32 %0, %1" : "+v"(f0[o_]), "+v"(f1[o_]))
#define WAD(o_) asm volatile("v_add_f32_dpp %0, %1, %0 row_ror:8 row_mask:0xf bank_mask:0xf" : "+v"(f0[o_]) : "v"(f1[o_]))
#define WFO(o_) do { VADD(f0[o_], acc[o_][0], acc[o_][2]); VADD(f1[o_], acc[o_][1], acc[o_][3]); } while (0)
#define WWR(o_) *(LAS float*)(xw + L * 4096 + (o_) * 128) = f0[o_]
            asm volatile("s_waitcnt vmcnt(10)" : "+v"(d[0]));
            if (__builtin_expect(__builtin_amdgcn_ballot_w64(d[0].x == PSENT) != 0ull, 0)) {
                asm volatile("s_waitcnt vmcnt(3)" : "+v"(d[1]), "+v"(d[2]), "+v"(d[3]), "+v"(d[4]), "+v"(d[5]), "+v"(d[6]), "+v"(d[7]));
#pragma unroll
                for (int i = 0; i < 5; ++i) *(LAS u32*)(xw + L * 4096 + i * 128) = psent;
                *(LAS u32*)(sw + L * 4096) = 1u;
            } else {
            SFOR(i, 40)
                constexpr int pc = i < 30 ? i / 5 : 6 + (i & 1), oc = i < 30 ? i % 5 : (i - 30) >> 1;
                if constexpr (oc == 0 && pc > 0) asm volatile("s_waitcnt vmcnt(%1)" : "+v"(d[pc]) : "i"(10 - pc));
                if constexpr (pc * 5 + oc < 32) SMF_A(acc[oc], d[pc], wo[pc][oc]); else SMF_V(acc[oc], d[pc], wo[pc][oc]);
                if constexpr (i == 0) WT(3);
                if constexpr (i == 20) WT(4);
                if constexpr (i == 31 || i == 33) NOPS(1);
                if constexpr (i == 32) NOPS(0);
                if constexpr (i == 34) WFO(0);
                if constexpr (i == 35) { asm volatile("v_cmp_u_f32 %0, %1, %2" : "=s"(bad) : "v"(f0[0]), "v"(f1[0])); WSW(0); }
                if constexpr (i == 36) WFO(1);
                if constexpr (i == 37) { WAD(0); WSW(1); }
                if constexpr (i == 38) { WWR(0); SCHED(); WFO(2); }
                if constexpr (i == 39) { WAD(1); WSW(2); }
                SCHED();
            SEND
            WT(5);
            WWR(1); SCHED(); WFO(3); WAD(2); WWR(2); SCHED(); WSW(3);
            if (__builtin_expect(bad != 0ull, 0)) {
                bool miss = false;
#pragma unroll
                for (int i = 0; i < 8; ++i) miss |= d[i].x == PSENT;
                if (all_(!miss)) {
#pragma unroll
                    for (int i = 0; i < 8; ++i) miss |= d[i].y == PSENT || d[i].z == PSENT || d[i].w == PSENT;
                }
                *(LAS u32*)(sw + L * 4096) = all_(!miss) ? 0u : 1u;
            }
            WFO(4); WAD(3); WWR(3); SCHED();
            asm volatile("v_permlane32_swap_b32 %0, %1\n\ts_nop 1\n\tv_add_f32_dpp %0, %1, %0 row_ror:8 row_mask:0xf bank_mask:0xf" : "+v"(f0[4]), "+v"(f1[4]));
            WWR(4);
            }
            WT(6);
            f32x2 a, b, c, e; u32 cw, mq; v4u sv;
            asm volatile("s_waitcnt lgkmcnt(0)\n\ts_barrier\n\tds_read_b64 %0, %5 offset:%7\n\tds_read_b64 %1, %5 offset:%7+640\n\tds_read_b64 %2, %5 offset:%7+1280\n\tds_read_b64 %3, %5 offset:%7+1920\n\t"
                         "ds_read_b128 %4, %6 offset:%7+2560\n\t"
                         "s_waitcnt lgkmcnt(3)\n\tv_pk_add_f32 %0, %0, %1\n\ts_waitcnt lgkmcnt(1)\n\tv_pk_add_f32 %2, %2, %3\n\ts_waitcnt lgkmcnt(0)\n\tv_pk_add_f32 %0, %0, %2"
                         : "=&v"(a), "=&v"(b), "=&v"(c), "=&v"(e), "=&v"(sv) : "v"(xr), "v"(zero), "i"(L * 4096) : "memory");
            asm volatile("v_cmp_u_f32 vcc, %2, %3\n\tv_cvt_pk_bf16_f32 %0, %2, %3\n\tv_cndmask_b32 %1, %4, %5, vcc" : "=&v"(cw), "=&v"(mq) : "v"(a.x), "v"(a.y), "v"(mp), "v"(OOB) : "vcc");
            st4s(rs, mq, 0, cw);
            SCHED();
            now = rt32();
            PIN(hn); hv = hn;
            WT(7);
            pst = sv.x | sv.y | sv.z | sv.w;
            if (__builtin_expect(__builtin_amdgcn_ballot_w64(mq != mp && pst == 0u) != 0ull, 0)) st4s(rs, mp, 0, cw);
        } else {
            done = uu(pst) ^ 1u;
            st4s(rs, done ? pmp : OOB, 0, psent);
            static_for<0, 25>(books);
            pst = 1;
            hv = ld4(rs, hvo[L]);
            PIN(hv);
            now = rt32();
        }
        pdur = now - t;
        const u32 per = rdl(hv, 1), th = rdl(hv, 0);
        t = lead ? tn : (int)(th - now) < -(int)(per >> 1) ? th + per : th;
        tn = nt;
        dw[M] += max((int)(now + CH_WX - t), 0);
        stop = stop || rdl(hv, 3) != 0u;
    };
    for (int k = 0; k < 2 * SPIN_CAP && !stop && (sn[0] + 1 < NSEG || sn[1] + 1 < NSEG); ++k) { turn(IC<0>{}); turn(IC<1>{}); }
    if (sn[0] + 1 < NSEG || sn[1] + 1 < NSEG) st4s(rs, tid == 0 ? foff(F_ERR) : OOB, 0, 1u);
}

__global__ void __launch_bounds__(256, 1) k_main(Params p) {
    lptr lds = (lptr)(u32)16 - 16;
    if (blockIdx.x == 0) LST(256, rt32());
    unsigned long long pc; int len;
    asm volatile("s_getpc_b64 %0\n.Lk_main_here:\n\ts_mov_b32 %1, .Lk_end_here-.Lk_main_here" : "=s"(pc), "=s"(len));
    const rsrc_t rs = mk_rsrc(p.ws, W_END);
    const v2u e0 = ld8s(rs, threadIdx.x == 0 ? foff(F_EPOCH) : OOB, 0);
    const v4u code = __builtin_amdgcn_raw_buffer_load_b128(mk_rsrc((const void*)(pc & ~15ull), (u32)max(len, 0)), (((blockIdx.x >> 3) & 31u) * 256u + threadIdx.x) * 16u, 0, 0);
    CFENCE();
    if (threadIdx.x == 0) {
        GAS unsigned long long* slots = (GAS unsigned long long*)(p.ws + W_CTRL + F_SLOT * 128);
        const unsigned long long tag = (unsigned long long)e0.y << 32 | (e0.x + 1u);
        u32 role = ((u32)__builtin_amdgcn_s_getreg(20 | (3 << 11)) & 7u) * 32u + (blockIdx.x >> 3);
        for (int n = 0; n < 256 && __hip_atomic_exchange(slots + role * 16, tag, __ATOMIC_RELAXED, __HIP_MEMORY_SCOPE_AGENT) == tag; ++n) role = (role + 1u) & 255u;
        lds_w16(lds, (v4u){role, e0.x, e0.y, 0u});
    }
    __syncthreads();
    const v4u rv = lds_r16(lds);
    const int role = uni((int)rv.x), x = role >> 5, i = role & 31, w = uni(threadIdx.x >> 6);
    const v2u ep = {uu(rv.y), uu(rv.z)};
    asm volatile("" :: "v"(code));
    __syncthreads();
    if (x < 2) { role_head(p, lds, i >> 4, x * 16 + (i & 15), ep); __builtin_amdgcn_endpgm(); }
    if (x == 2) { role_wo(p, lds, i, ep); __builtin_amdgcn_endpgm(); }
    const int no = x < 5 ? 5 : 10, nr = x < 7 ? 5 : 4;
    if (i >= no + nr) { role_qkv(p, lds, (x < 5 ? (x - 3) * 22 : 44 + (x - 5) * 17) + i - no - nr, ep); __builtin_amdgcn_endpgm(); }
    const int og = x < 5 ? x - 3 : 2 * (x - 4) + (i >= 5), ng = i % 5, kq = og >> 1, L = og & 1, r = (x - 3) * 5 + i - no, q = r / 3;
    const int j = i < no ? kq * 5 + ng : r % 3 == 0 ? 20 + (q & 1) * 20 + (q >> 1) : 16 + (r % 3) * 8 + q;
    const bool cv = i >= no && r % 3 == 0;
    if (i >= no || L == 1) conv_stage(p, rs, lds, j, cv ? 8 + q : q, cv ? 40 : j >= 24 && j < 32 ? 8 : -8, ep);
    if (cv) __builtin_amdgcn_endpgm();
    if (i >= no) { role_row(p, lds, ((j - 24) ^ 8) * 4 + w, ep); __builtin_amdgcn_endpgm(); }
    role_out(p, lds, L, kq, ng, ep);
}
__global__ void k_end() { asm volatile(".Lk_end_here:"); }

extern "C" void kernel_launch(void* const* d_in, const int* in_sizes, int n_in, void* d_out, int out_size, void* d_ws, size_t ws_size,
                              hipStream_t stream) {
    Params p;
    p.x = (const u16*)d_in[0]; p.mem0 = (const u16*)d_in[1]; p.Wq = (const u16*)d_in[2]; p.Wk = (const u16*)d_in[3];
    p.Wv = (const u16*)d_in[4]; p.Wo = (const u16*)d_in[5];
    p.bk = (const u16*)d_in[8]; p.bv = (const u16*)d_in[9]; p.shift = (const u16*)d_in[10]; p.ssmk = (const u16*)d_in[11]; p.ssmD = (const u16*)d_in[12];
    p.out = (u16*)d_out;
    p.ws = (unsigned char*)(((size_t)d_ws + 2 * MB - 1) & ~(size_t)(2 * MB - 1));
    (void)hipFuncSetAttribute((const void*)k_main, hipFuncAttributeMaxDynamicSharedMemorySize, LDS_BYTES);
    hipLaunchKernelGGL(k_main, dim3(256), dim3(256), LDS_BYTES, stream, p);
}
```

```cpp
#include <hip/hip_runtime.h>
#include <hip/hip_bf16.h>

typedef unsigned short u16;
typedef unsigned int u32;
typedef u32 v2u __attribute__((ext_vector_type(2)));
typedef u32 v4u __attribute__((ext_vector_type(4)));
typedef u32 v8u __attribute__((ext_vector_type(8)));
typedef float f32x2 __attribute__((ext_vector_type(2)));
typedef float f32x4 __attribute__((ext_vector_type(4)));
typedef float f32x16 __attribute__((ext_vector_type(16)));
typedef __bf16 bf16x2 __attribute__((ext_vector_type(2)));
typedef __bf16 bf16x8 __attribute__((ext_vector_type(8)));
typedef short s16x4 __attribute__((ext_vector_type(4)));
#define LAS __attribute__((address_space(3)))
#define GAS __attribute__((address_space(1)))
typedef LAS unsigned char* lptr;
#define DEV __device__ __forceinline__

constexpr int SEQ = 2048, DM = 640, NH = 32, DH = 128, NSEG = 64, NQ = NH * DH;

constexpr u32 MB = 1u << 20;
constexpr u32 W_ORING = 0;
constexpr u32 W_MRING = MB;
constexpr u32 W_PROBE = 2 * MB - 65536;
constexpr u32 W_CTRL = 4 * MB;
constexpr u32 CTRL_BYTES = 128 * 1024;
constexpr u32 ACT = SEQ * DM * 2;
constexpr u32 W_H1 = 5 * MB, W_Y = W_H1 + ACT, W_H2 = W_Y + ACT, W_X2 = W_H2 + ACT;
constexpr u32 W_KQ = 16 * MB;
constexpr u32 W_VO = W_KQ + 64 * MB;
constexpr u32 W_P = W_VO + 64 * MB;
constexpr u32 P_ITEM = 32 * 4 * DM * 4;
constexpr u32 W_KVT = W_P + 2 * NSEG * P_ITEM;
constexpr u32 W_SP = W_KVT + ACT;
constexpr u32 W_END = W_KVT + 3 * MB;
static_assert((size_t)W_END + 2 * MB <= 256ull * MB, "workspace");
constexpr u32 F_ERR = 0, F_QKV = 16, F_O = F_QKV + 192, F_P = F_O + 64, F_H2 = F_P + 64, F_CLR = F_H2 + 32,
              F_POL = F_CLR, F_EPOCH = F_POL + 32, F_SLOT = F_EPOCH + 1, F_PRE = F_SLOT + 256, F_N = F_PRE + 192;
constexpr int NCV = 44, NPRE = 40;
static_assert(F_N * 128 <= CTRL_BYTES, "ctrl");
DEV u32 foff(u32 idx) { return W_CTRL + idx * 128u; }

constexpr u32 OOB = 0x80000000u;
constexpr u32 PSENT = 0x7F817F81u;
constexpr int SPIN_CAP = 40000;
constexpr int LDS_BYTES = 160 * 1024;
#define ST_HEAD 0
#define ST_WO 0

#define LS3(cond, slot, idx) do {} while (0)
#define LST(idx, v) do {} while (0)
#define LS2(cond, idx) do {} while (0)
struct Params {
    const u16 *x, *mem0, *Wq, *Wk, *Wv, *Wo, *bk, *bv, *shift, *ssmk, *ssmD;
    u16* out;
    unsigned char* ws;
};

typedef __amdgpu_buffer_rsrc_t rsrc_t;
DEV rsrc_t mk_rsrc(const void* p, u32 bytes) { return __builtin_amdgcn_make_buffer_rsrc((void*)p, 0, bytes, 0x00020000); }
DEV v4u ld16s(rsrc_t r, u32 off, u32 so) { return __builtin_amdgcn_raw_buffer_load_b128(r, off, so, 16); }
DEV v2u ld8s(rsrc_t r, u32 off, u32 so) { return __builtin_amdgcn_raw_buffer_load_b64(r, off, so, 16); }
DEV v4u ldin16(rsrc_t r, u32 off, u32 so) { return __builtin_amdgcn_raw_buffer_load_b128(r, off, so, 2); }
DEV v2u ldin8(rsrc_t r, u32 off, u32 so) { return __builtin_amdgcn_raw_buffer_load_b64(r, off, so, 2); }
DEV u32 ld4(rsrc_t r, u32 off) { return __builtin_amdgcn_raw_buffer_load_b32(r, off, 0, 16); }
DEV void st16(rsrc_t r, u32 off, v4u v) { __builtin_amdgcn_raw_buffer_store_b128(v, r, off, 0, 16); }
DEV void st8s(rsrc_t r, u32 off, u32 so, v2u v) { __builtin_amdgcn_raw_buffer_store_b64(v, r, off, so, 16); }
DEV void st4s(rsrc_t r, u32 off, u32 so, u32 v) { __builtin_amdgcn_raw_buffer_store_b32(v, r, off, so, 16); }
#define DRAIN() asm volatile("s_waitcnt vmcnt(0)" ::: "memory")
#define VMW(n) asm volatile("s_waitcnt vmcnt(%0)" :: "i"(n) : "memory")
#define BAR() asm volatile("s_waitcnt lgkmcnt(0)\n\ts_barrier" ::: "memory")
#define SCHED() __builtin_amdgcn_sched_barrier(0)
#define CFENCE() asm volatile("" ::: "memory")
#define NOPS(n) asm volatile("s_nop %0" :: "i"(n))
#define PIN(x) asm volatile("" : "+v"(x))
#define PINS(x) asm volatile("" : "+s"(x))

template <int V> struct IC { static constexpr int value = V; };
template <int I, int N, class F> DEV void static_for(F&& f) { if constexpr (I < N) { f(IC<I>{}); static_for<I + 1, N>(f); } }
#define SFOR(var, n) static_for<0, n>([&](auto ic_) __attribute__((always_inline)) { constexpr int var = decltype(ic_)::value;
#define SEND });

DEV u32 pk(float a, float b) { bf16x2 v = {(__bf16)a, (__bf16)b}; return __builtin_bit_cast(u32, v); }
DEV float lo_f(u32 u) { return __builtin_bit_cast(float, u << 16); }
DEV float hi_f(u32 u) { return __builtin_bit_cast(float, u & 0xffff0000u); }
DEV f32x2 up2(u32 u) { return (f32x2){lo_f(u), hi_f(u)}; }
DEV u32 pk2(f32x2 v) { return pk(v.x, v.y); }
DEV float rbf(float a) { return (float)(__bf16)a; }
DEV v4u lds_r16(lptr p) { return *(LAS v4u*)p; }
DEV void lds_w16(lptr p, v4u v) { *(LAS v4u*)p = v; }
DEV v2u lds_tr(lptr p) { return __builtin_bit_cast(v2u, __builtin_amdgcn_ds_read_tr16_b64_v4i16((LAS s16x4*)p)); }
DEV v4u tr2(lptr p0, lptr p1) { const v2u a = lds_tr(p0), b = lds_tr(p1); return (v4u){a.x, a.y, b.x, b.y}; }
DEV v8u cat8(v4u a, v4u b) { return __builtin_shufflevector(a, b, 0, 1, 2, 3, 4, 5, 6, 7); }
DEV f32x4 mfma16(v4u a, v4u b, f32x4 c) { return __builtin_amdgcn_mfma_f32_16x16x32_bf16(__builtin_bit_cast(bf16x8, a), __builtin_bit_cast(bf16x8, b), c, 0, 0, 0); }
DEV f32x16 mfma32(v4u a, v4u b, f32x16 c) { return __builtin_amdgcn_mfma_f32_32x32x16_bf16(__builtin_bit_cast(bf16x8, a), __builtin_bit_cast(bf16x8, b), c, 0, 0, 0); }

DEV float f_(u32 u) { return __builtin_bit_cast(float, u); }
DEV u32 u_(float f) { return __builtin_bit_cast(u32, f); }
template <int CTRL> DEV float dpp(float v) { return f_((u32)__builtin_amdgcn_update_dpp(0, (int)u_(v), CTRL, 0xf, 0xf, false)); }
DEV float max16(float v) { auto r = __builtin_amdgcn_permlane16_swap(u_(v), u_(v), false, false); return fmaxf(f_(r[0]), f_(r[1])); }
DEV float max32(float v) { auto r = __builtin_amdgcn_permlane32_swap(u_(v), u_(v), false, false); return fmaxf(f_(r[0]), f_(r[1])); }
DEV float add16(float v) { auto r = __builtin_amdgcn_permlane16_swap(u_(v), u_(v), false, false); return f_(r[0]) + f_(r[1]); }
DEV float add32(float v) { auto r = __builtin_amdgcn_permlane32_swap(u_(v), u_(v), false, false); return f_(r[0]) + f_(r[1]); }
DEV float wave_sum(float v) {
    v += dpp<0xB1>(v);
    v += dpp<0x4E>(v);
    v += dpp<0x124>(v);
    v += dpp<0x128>(v);
    return add32(add16(v));
}
DEV bool all_(bool c) { return __builtin_amdgcn_ballot_w64(!c) == 0ull; }
DEV int uni(int v) { return __builtin_amdgcn_readfirstlane(v); }
DEV u32 uu(u32 v) { return (u32)__builtin_amdgcn_readfirstlane((int)v); }
DEV u32 rdl(u32 v, int l) { return (u32)__builtin_amdgcn_readlane((int)v, l); }
DEV u32 rt32() { return (u32)__builtin_amdgcn_s_memrealtime(); }

DEV void wait_flags(rsrc_t rs, u32 off, u32 want, bool& dead) {
    if (dead) return;
#pragma unroll 1
    for (int n = 0;; ++n) {
        CFENCE();
        const u32 v = ld4(rs, off);
        if (all_(off >= OOB || v >= want)) break;
        __builtin_amdgcn_s_sleep(2);
        if ((n & 63) == 63) { if (uni((int)ld4(rs, foff(F_ERR))) != 0) { dead = true; break; } }
        if (n > SPIN_CAP) { st4s(rs, foff(F_ERR), 0, 1u); dead = true; break; }
    }
    CFENCE();
}

DEV void pre_post(rsrc_t rs, int idx, u32 stage, v2u ep) { st8s(rs, threadIdx.x == 0 ? foff(F_PRE + idx) : OOB, 0, (v2u){ep.x + stage, ep.y}); }
DEV void pre_poll(rsrc_t rs, int first, int n, u32 stage, v2u ep) {
    const int lane = threadIdx.x & 63;
#pragma unroll 1
    for (int it = 0; it < SPIN_CAP; ++it) {
        CFENCE();
        const v2u a = ld8s(rs, foff(F_PRE + first + min(lane, n - 1)), 0), b = ld8s(rs, foff(F_PRE + first + min(lane + 64, n - 1)), 0);
        if (all_(a.y == ep.y && b.y == ep.y && max(a.x - ep.x - stage, b.x - ep.x - stage) < 8u - stage)) break;
        __builtin_amdgcn_s_sleep(2);
    }
    CFENCE();
}
DEV void pre_wait(rsrc_t rs, int first, int n, u32 stage, v2u ep) {
    if (uni(threadIdx.x >> 6) == 0) pre_poll(rs, first, n, stage, ep);
    BAR();
}
DEV u32 get_pol(rsrc_t rs) {
    const int lane = threadIdx.x & 63;
    const u32 raw = ld4(rs, lane < 16 ? foff(F_POL + lane) : OOB);
    return wave_sum((float)(int)raw) >= 0.f ? 1u : 0u;
}
#define NPROBE 16
DEV void pol_probe(rsrc_t rs, int id) {
    const int lane = threadIdx.x & 63;
    const u32 off0 = W_PROBE + (u32)(id >> 2) * 16384u + (u32)(id & 3) * 128u;
    st4s(rs, lane < 32 ? off0 + (u32)lane * 512 : OOB, 0, 0u); DRAIN();
    int d[2] = {1 << 16, 1 << 16};
#pragma unroll 1
    for (int q = 0; q < NPROBE; ++q) {
        const u32 half = (u32)(q ^ (q >> 1) ^ id) & 1u;
        const unsigned long long t0 = __builtin_amdgcn_s_memtime();
        const u32 v = ld4(rs, lane == 0 ? off0 + half * 8192 + (u32)(q >> 1) * 512 : OOB);
        asm volatile("s_waitcnt vmcnt(0)" :: "v"(v) : "memory");
        const int dt = (int)(__builtin_amdgcn_s_memtime() - t0);
        if (half) d[1] = min(d[1], dt); else d[0] = min(d[0], dt);
    }
    const u32 xcc = (u32)__builtin_amdgcn_s_getreg(20 | (3 << 11)) & 0xfu;
    st4s(rs, lane == 0 ? foff(F_POL + id) : OOB, 0, (u32)min(max((d[0] - d[1]) * ((xcc & 4u) ? -1 : 1), -250), 250));
}
DEV f32x2 ln_stats(const f32x2 (&x)[5]) {
    const f32x2 s2 = (x[0] + x[1]) + (x[2] + x[3]) + x[4];
    const float mean = wave_sum(s2.x + s2.y) * (1.0f / DM);
    f32x2 q = {0.f, 0.f};
#pragma unroll
    for (int i = 0; i < 5; ++i) { const f32x2 d = x[i] - mean; q += d * d; }
    const float var = wave_sum(q.x + q.y) * (1.0f / DM);
    const float sd = rbf(__builtin_amdgcn_sqrtf(rbf(rbf(var) + 1e-5f)));
    return (f32x2){rbf(mean), __builtin_amdgcn_rcpf(sd)};
}
DEV void store_h(rsrc_t rs, u32 base, int row, const u32 (&hp)[5]) {
    const u32 lane = threadIdx.x & 63;
    const u32 sw = (u32)(row & 15) * 16u, o8 = (lane >> 5) * 8192u + (((lane & 31u) * 8u) ^ sw), hb = base + (u32)(row >> 5) * (32 * DM * 2) + (u32)(row & 31) * 256u;
    st8s(rs, o8, hb, (v2u){hp[0], hp[1]});
    st8s(rs, o8, hb + 16384, (v2u){hp[2], hp[3]});
    st4s(rs, (lane * 4) ^ sw, hb + 32768, hp[4]);
}

#define DMA16(ldsb, vo, so) asm volatile("s_mov_b32 m0, %0\n\ts_nop 0\n\tbuffer_load_dwordx4 %1, %2, %3 offen sc1 lds" :: "s"(ldsb), "v"(vo), "s"(srd), "s"(so) : "memory")
#define DMA16I(ldsb, lo, vo, so, go) do { u32 so_; asm volatile("s_add_u32 m0, %1, %5\n\ts_add_u32 %0, %4, %6\n\tbuffer_load_dwordx4 %2, %3, %0 offen sc1 lds" \
                         : "=&s"(so_) : "s"(ldsb), "v"(vo), "s"(srd), "s"(so), "i"(lo), "i"(go) : "memory", "scc"); } while (0)
#define DMA32(ldsb, vo, so) asm volatile("s_mov_b32 m0, %0\n\ts_nop 0\n\tbuffer_load_dwordx4 %1, %2, %3 offen sc1 lds\n\tbuffer_load_dwordx4 %1, %2, %3 offen offset:1024 sc1 lds" \
                         :: "s"(ldsb), "v"(vo), "s"(srd), "s"(so) : "memory")
#define DMAIN(sr, ldsb, vo, so) asm volatile("s_mov_b32 m0, %0\n\ts_nop 0\n\tbuffer_load_dwordx4 %1, %2, %3 offen lds" :: "s"(ldsb), "v"(vo), "s"(sr), "s"(so) : "memory")
DEV v4u mk_srd(const void* q, u32 bytes) { return (v4u){uu((u32)(size_t)q), uu((u32)((size_t)q >> 32) & 0xffffu), bytes, 0x00020000u}; }
#define DMANT(sr, ldsb, vo, so) asm volatile("s_mov_b32 m0, %0\n\ts_nop 0\n\tbuffer_load_dwordx4 %1, %2, %3 offen nt lds" :: "s"(ldsb), "v"(vo), "s"(sr), "s"(so) : "memory")
#define MK_SRD() const v4u srd = mk_srd(p.ws, W_END)

constexpr int CV_W = 40960;
template <bool B> struct CvL {
    static constexpr int NF = B ? 5 : 3, NK = B ? 4 : 2, KP = 544 + NK * 256, CH = NF * 1024 + 4 * KP;
};
constexpr int CV_Y = 4 * CvL<false>::CH, CV_YP = 272;
static_assert(CV_Y + 32 * CV_YP <= CV_W && 2 * CvL<true>::CH <= CV_Y, "conv lds");
DEV int cv_chan(int u, int i) { return 16 * (u % 40) + 4 * i + u / 40; }
template <bool B> DEV void cv_filt(const Params& p, u32 dst, int d) {
    const u32 lane = threadIdx.x & 63;
    const v4u sf = mk_srd(p.ssmk + d * SEQ, SEQ * 2);
    DMANT(sf, dst, lane < 4u ? OOB : lane * 16u - 64u, 0);
#pragma unroll
    for (int i = 1; i < CvL<B>::NF; ++i) DMANT(sf, uu(dst + i * 1024), i == CvL<B>::NF - 1 && lane >= 4u ? OOB : lane * 16u, i * 1024 - 64);
}
template <bool B> DEV void cv_kvld(rsrc_t rs, int d, v4u* k) {
#pragma unroll
    for (int i = 0; i < CvL<B>::NK; ++i) k[i] = ld16s(rs, (threadIdx.x & 63) * 16u, uu(W_KVT + (u32)d * (SEQ * 2) + i * 1024));
}
template <bool B> DEV void cv_kvput(lptr F, const v4u* k) {
    const int lane = threadIdx.x & 63;
#pragma unroll
    for (int i = 0; i < CvL<B>::NK; ++i) lds_w16(F + CvL<B>::NF * 1024 + (lane & 3) * CvL<B>::KP + 512 + i * 256 + (lane >> 2) * 16, k[i]);
}
template <bool B> DEV void cv_zero(lptr F) {
    const int lane = threadIdx.x & 63;
#pragma unroll
    for (int i = 0; i < 4; ++i) lds_w16(F + (lane >> 5) * CvL<B>::CH + CvL<B>::NF * 1024 + i * CvL<B>::KP + (lane & 31) * 16, (v4u){0u, 0u, 0u, 0u});
}
#define M32V(d, a, b) asm volatile("v_mfma_f32_32x32x16_bf16 %0, %1, %2, %0" : "+v"(d) : "v"(a), "v"(b))
template <bool B> DEV void cv_run(const Params& p, rsrc_t rs, lptr F, int d, u32 D, f32x4 sp, float (&y)[16]) {
    constexpr int KP = CvL<B>::KP, FO = B ? 2048 : 0;
    const int lane = threadIdx.x & 63, n = lane & 31, hh = lane >> 5, c = lane & 3;
    lptr fa = F + 64 + (32 * hh + 16 * (c & 1) + 8 * ((lane >> 4) & 1) + 4 * (c >> 1) - (lane & 12)) * 2;
    lptr kl = F + CvL<B>::NF * 1024 + (32 + n - hh) * 16;
    v4u dg;
#pragma unroll
    for (int q = 0; q < 4; ++q) { const int k = (lane & 35) ? -1 : (n & 4) + (n >> 3); dg[q] = k == 2 * q ? D : k == 2 * q + 1 ? D << 16 : 0u; }
    v4u l[4], h[4]; v2u fc[4], fh[4];
    f32x16 acc[4];
#pragma unroll
    for (int j = 0; j < 4; ++j) { l[j] = lds_r16(kl + j * KP); if constexpr (B) h[j] = lds_r16(kl + j * KP + 512); }
#pragma unroll
    for (int j = 0; j < 2; ++j) { fc[j] = lds_tr(fa + FO - 32 * j); if constexpr (B) fh[j] = lds_tr(fa - 32 * j); }
    asm volatile("s_nop 1" : "+v"(dg));
#pragma unroll
    for (int j = 0; j < 4; ++j) asm volatile("v_mfma_f32_32x32x16_bf16 %0, %1, %2, 0" : "=&v"(acc[j]) : "v"(dg), "v"(B ? h[j] : l[j]));
    constexpr int U = B ? 2 : 4;
    kl -= 32 * U; fa -= 32;
#pragma unroll 1
    for (int m = 0; m < 32; m += 2 * U) {
#pragma unroll
        for (int z = 0; z < U; ++z) {
            const int c = 2 * (z & 1);
            const v4u f = __builtin_shufflevector(fc[c], fc[c + 1], 0, 1, 2, 3), g = __builtin_shufflevector(fh[c], fh[c + 1], 0, 1, 2, 3);
#pragma unroll
            for (int j = 0; j < 4; ++j) {
                M32V(acc[j], f, l[j]); l[j] = lds_r16(kl + j * KP + 32 * (U - z - 1));
                if (j < 2) fc[(c ^ 2) + j] = lds_tr(fa + FO + (z + 1) * 128 + 32 - 32 * j);
                SCHED();
            }
            if constexpr (B) {
#pragma unroll
                for (int j = 0; j < 4; ++j) {
                    M32V(acc[j], g, h[j]); h[j] = lds_r16(kl + j * KP + 512 + 32 * (U - z - 1));
                    if (j < 2) fh[(c ^ 2) + j] = lds_tr(fa + (z + 1) * 128 + 32 - 32 * j);
                    SCHED();
                }
            }
        }
        kl -= 32 * U; fa += 128 * U;
    }
    asm volatile("s_nop 11" : "+v"(acc[0]), "+v"(acc[1]), "+v"(acc[2]), "+v"(acc[3]));
    const float o[3] = {acc[1][15] + acc[2][14] + acc[3][13], acc[2][15] + acc[3][14], acc[3][15]};
    if constexpr (!B) st16(rs, lane == 63 ? W_SP + (u32)d * 16u : OOB, (v4u){u_(o[0]), u_(o[1]), u_(o[2]), 0u});
#pragma unroll
    for (int i = 0; i < 16; ++i) {
        float v = acc[0][i];
#pragma unroll
        for (int j = 1; j < 4; ++j) if (j <= i) v += acc[j][i - j];
        if (i < 3) { const float e = f_((u32)__builtin_amdgcn_ds_bpermute((hh ? lane - 32 : lane + 31) * 4, (int)u_(o[i]))); v += lane == 0 ? sp[i] : e; }
        y[i] = v;
    }
}
DEV void cv_put(lptr F, int pr, const float (&y0)[16], const float (&y1)[16]) {
    const int lane = threadIdx.x & 63;
#pragma unroll
    for (int i = 0; i < 16; ++i) *(LAS u32*)(F + CV_Y + (lane & 31) * CV_YP + (lane >> 5) * 128 + i * 8 + pr * 4) = pk(y0[i], y1[i]);
}
DEV void cv_out(rsrc_t rs, lptr F, int u, int blk0) {
    const int lane = threadIdx.x & 63;
    lptr q = F + CV_Y + ((lane >> 2) & 3) * CV_YP + ((lane >> 4) * 4 + (lane & 3)) * 8;
    const u32 yo = W_Y + (u32)(((lane >> 2) * 8 + blk0) * DM + 4 * u + (lane & 3)) * 16u;
#pragma unroll
    for (int i = 0; i < 8; ++i) { lptr a = q + (i >> 1) * (8 * CV_YP) + (i & 1) * 128; st16(rs, yo + (u32)((i & 1) * 128 + (i >> 1)) * (DM * 16), tr2(a, a + 4 * CV_YP)); }
}
struct KvIn { v2u k[19], v[16]; v4u s[2]; };
DEV void kvt_load(const Params& p, int tile, KvIn& in) {
    const int lane = threadIdx.x & 63;
    const rsrc_t rk = mk_rsrc(p.bk, ACT), rv = mk_rsrc(p.bv, ACT), rh = mk_rsrc(p.shift, DM * 8);
    const int row = (tile / 10) * 64 + (lane >> 4) * 16, col = (tile % 10) * 64 + (lane & 15) * 4;
    const u32 vo = (u32)(row * DM + col) * 2u, vp = row > 0 ? vo - 3 * (DM * 2) : OOB;
#pragma unroll
    for (int i = 0; i < 2; ++i) in.s[i] = ldin16(rh, (u32)col * 8u + i * 16, 0);
#pragma unroll
    for (int i = 0; i < 19; ++i) { in.k[i] = i < 3 ? ldin8(rk, vp, i * (DM * 2)) : ldin8(rk, vo, (i - 3) * (DM * 2)); if (i >= 3) in.v[i - 3] = ldin8(rv, vo, (i - 3) * (DM * 2)); }
}
DEV void kvt_make(rsrc_t rs, lptr xt, int tile, const KvIn& in) {
    const int lane = threadIdx.x & 63;
#pragma unroll
    for (int c = 0; c < 2; ++c) {
        const v4u sv = in.s[c];
        const f32x2 sh[4] = {{lo_f(sv.x), lo_f(sv.z)}, {hi_f(sv.x), hi_f(sv.z)}, {lo_f(sv.y), lo_f(sv.w)}, {hi_f(sv.y), hi_f(sv.w)}};
        f32x2 kf[19];
#pragma unroll
        for (int i = 0; i < 19; ++i) kf[i] = up2(in.k[i][c]);
        u32 kv[16];
#pragma unroll
        for (int i = 0; i < 16; ++i) kv[i] = pk2(((kf[i] * sh[3] + kf[i + 1] * sh[2]) + (kf[i + 2] * sh[1] + kf[i + 3] * sh[0])) * up2(in.v[i][c]));
#pragma unroll
        for (int q = 0; q < 8; ++q) {
            const u32 sel = (q & 1) ? 0x07060302u : 0x05040100u; const u32* z = kv + (q >> 1);
            *(LAS v2u*)(xt + ((2 * c + (q & 1)) * 16 + (lane & 15)) * 144 + (lane >> 5) * 64 + (q >> 1) * 16 + ((lane >> 4) & 1) * 8) =
                (v2u){__builtin_amdgcn_perm(z[4], z[0], sel), __builtin_amdgcn_perm(z[12], z[8], sel)};
        }
    }
    const u32 o = W_KVT + (u32)(((tile % 10) * 64 + 4 * (lane >> 3)) * SEQ + (tile / 10) * 64 + (lane & 7) * 8) * 2u;
#pragma unroll
    for (int i = 0; i < 8; ++i) st16(rs, o + (u32)(32 * (i & 1) + (i >> 1)) * (SEQ * 2), lds_r16(xt + (8 * i + (lane >> 3)) * 144 + (lane & 7) * 16));
}
struct RowX { v2u a, b; u32 c; };
DEV void ln_row(rsrc_t rs, int row, const RowX& r) {
    const f32x2 x[5] = {up2(r.a.x), up2(r.a.y), up2(r.b.x), up2(r.b.y), up2(r.c)};
    const f32x2 st = ln_stats(x);
    u32 hp[5];
#pragma unroll
    for (int q = 0; q < 5; ++q) hp[q] = pk2(up2(pk2(x[q] - st.x)) * st.y);
    store_h(rs, W_H1, row, hp);
}
DEV void pre_rings(const Params& p, rsrc_t rs, int j) {
    const u32 gi = (u32)(j * 256 + threadIdx.x), gt = gi * 16u, R = NPRE * 4096, n = ((gt >> 14) << 9) + ((gt & 8191u) >> 4);
    v4u m = {PSENT, PSENT, PSENT, PSENT};
    if (n < 640u) {
        m = *(const v4u*)(p.mem0 + (2 * ((n >> 2) & 3) + (n & 1)) * DM + (n >> 4) * 16 + (n & 2) * 4);
#pragma unroll
        for (int i = 0; i < 4; ++i) m[i] = m[i] == PSENT ? 0x7FC17FC1u : m[i];
    }
    st4s(rs, gi - 36u * 256u < F_CLR ? foff(gi - 36u * 256u) : OOB, 0, 0u);
#pragma unroll 1
    for (u32 o = gt; o < MB; o += R) st16(rs, W_ORING + o, (v4u){PSENT, PSENT, PSENT, PSENT});
    const u32 mo = gt < 4 * 32768 + 16384 ? W_MRING + gt : OOB;
    st16(rs, mo, m);
    st16(rs, mo + MB / 4, m);
    static_assert(NPRE * 4096 >= 4 * 32768 + 16384, "memory ring");
}
constexpr int LN0 = NPRE * 4;
DEV void conv_stage(const Params& p, rsrc_t rs, lptr lds, int j, int g0, int ge, v2u ep) {
    const int tid = threadIdx.x, lane = tid & 63, w = uni(tid >> 6), u = j * 4 + w;
    lptr F = lds + w * CV_W;
    const u32 fb = uu(w * CV_W);
    const rsrc_t rd = mk_rsrc(p.ssmD, DM * 2);
    float y0[16], y1[16];
    if (j < NPRE) {
        constexpr int CH = CvL<false>::CH;
        const rsrc_t rx = mk_rsrc(p.x, ACT);
        KvIn in;
        RowX r;
        r.a = ldin8(rx, lane * 8u, uu(u * (DM * 2))); r.b = ldin8(rx, lane * 8u + 512u, uu(u * (DM * 2)));
        r.c = __builtin_amdgcn_raw_buffer_load_b32(rx, lane * 4u + 1024u, uu(u * (DM * 2)), 2);
        kvt_load(p, u, in);
        pre_rings(p, rs, j);
        ln_row(rs, u, r);
        DRAIN(); BAR();
        pre_post(rs, j, 1u, ep);
        if (j == 20) LST(260, rt32());
        const u32 D = __builtin_amdgcn_raw_buffer_load_b16(rd, (u32)cv_chan(u, lane & 3) * 2u, 0, 0);
#pragma unroll 1
        for (int i = 0; i < 4; ++i) cv_filt<false>(p, fb + i * CH, cv_chan(u, i));
        kvt_make(rs, F + CV_Y, u, in);
        cv_zero<false>(F); cv_zero<false>(F + 2 * CH);
        DRAIN(); BAR();
        pre_post(rs, j, 2u, ep);
        if (j == 20) LST(262, rt32());
        pre_wait(rs, 0, NPRE, 2u, ep);
        if (j == 20) LST(281, rt32());
        {
            v4u k[8];
#pragma unroll
            for (int i = 0; i < 4; ++i) cv_kvld<false>(rs, cv_chan(u, i), k + 2 * i);
#pragma unroll
            for (int i = 0; i < 4; ++i) cv_kvput<false>(F + i * CH, k + 2 * i);
        }
        CFENCE();
        kvt_load(p, u + LN0, in);
        if (j == 20) LST(286, rt32());
#pragma unroll 1
        for (int i = 0; i < 4; i += 2) {
            cv_run<false>(p, rs, F + i * CH, cv_chan(u, i), rdl(D, i), f32x4{}, y0);
            cv_run<false>(p, rs, F + (i + 1) * CH, cv_chan(u, i + 1), rdl(D, i + 1), f32x4{}, y1);
            cv_put(F, i >> 1, y0, y1);
        }
        if (j == 20) LST(287, rt32());
        cv_out(rs, F, u, 0);
        DRAIN(); BAR();
        pre_post(rs, j, 3u, ep);
        if (j == 20) LST(263, rt32());
        kvt_make(rs, F + CV_Y, u + LN0, in);
    } else pol_probe(rs, u - NPRE * 4);
    DRAIN(); BAR();
    if (g0 < ge) {
        constexpr int CH = CvL<true>::CH;
        pre_post(rs, j, 4u, ep);
        cv_zero<true>(F);
        pre_wait(rs, 0, NPRE, 4u, ep);
        v4u k[4];
        auto fetch = [&](u32 dst, int d) { cv_filt<true>(p, dst, d); cv_kvld<true>(rs, d, k); };
        fetch(fb, cv_chan(g0 * 4 + w, 0));
#pragma unroll 1
        for (int g = g0; g < ge; g += 8) {
            const int un = g * 4 + w;
            const u32 ch = (u32)cv_chan(un, lane & 3), D = __builtin_amdgcn_raw_buffer_load_b16(rd, ch * 2u, 0, 0);
            const v4u sp = ld16s(rs, W_SP + ch * 16u, 0);
            if (g == 16) { LST(282, rt32()); LST(288, __builtin_amdgcn_s_memtime()); }
#pragma unroll 1
            for (int i = 0; i < 4; i += 2) {
                cv_kvput<true>(F, k);
                fetch(fb + CH, cv_chan(un, i + 1));
                cv_run<true>(p, rs, F, cv_chan(un, i), rdl(D, i), (f32x4){f_(rdl(sp.x, i)), f_(rdl(sp.y, i)), f_(rdl(sp.z, i)), 0.f}, y0);
                cv_kvput<true>(F + CH, k);
                fetch(fb, i == 0 ? cv_chan(un, 2) : cv_chan(min(un + 32, 159), 0));
                cv_run<true>(p, rs, F + CH, cv_chan(un, i + 1), rdl(D, i + 1), (f32x4){f_(rdl(sp.x, i + 1)), f_(rdl(sp.y, i + 1)), f_(rdl(sp.z, i + 1)), 0.f}, y1);
                cv_put(F, i >> 1, y0, y1);
            }
            cv_out(rs, F, un, 4);
            if (g == 16) { LST(283, rt32()); LST(289, __builtin_amdgcn_s_memtime()); }
        }
        DRAIN(); BAR();
    }
    pre_post(rs, j, 5u, ep);
    if (j == 20) LST(284, rt32());
    if (j == 3) LST(285, rt32());
}

#define M32A(first, d, a, b) do { if (first) asm volatile("v_mfma_f32_32x32x16_bf16 %0, %1, %2, 0" : "=&v"(d) : "a"(a), "v"(b)); \
                                  else asm volatile("v_mfma_f32_32x32x16_bf16 %0, %1, %2, %0" : "+v"(d) : "a"(a), "v"(b)); } while (0)
#define M32B(first, d, a, b) do { if (first) asm volatile("v_mfma_f32_32x32x16_bf16 %0, %1, %2, 0" : "=&v"(d) : "v"(a), "a"(b)); \
                                  else asm volatile("v_mfma_f32_32x32x16_bf16 %0, %1, %2, %0" : "+v"(d) : "v"(a), "a"(b)); } while (0)
#define VCVT(d, a, b) asm volatile("v_cvt_pk_bf16_f32 %0, %1, %2" : "=v"(d) : "v"(a), "v"(b))
#define LD4A(d, vo, so) asm volatile("buffer_load_dword %0, %1, %2, %3 offen sc1" : "=v"(d) : "v"(vo), "s"(srd), "s"(so) : "memory")
DEV u32 wst_src(int lane, int ld) { return (u32)((lane >> 4) * ld + 8 * ((lane & 15) ^ ((lane >> 4) << 2))) * 2u; }
DEV u32 wst_rd(int lane, int w) { const int q4 = (lane >> 2) & 3; return (u32)(q4 * 256 + ((w ^ q4) * 64) + ((lane >> 4) & 1) * 32 + (lane & 3) * 8); }

constexpr int QK_SEL = 81920;
#define QK_LEAD 12
#define QK_SMP 56
#define QK_W0 8
DEV void role_qkv(const Params& p, lptr lds, int c, v2u ep) {
    const int tid = threadIdx.x, lane = tid & 63, w = uni(tid >> 6), hh = lane >> 5, r32 = lane & 31;
    const int m = c >> 5, h = c & 31;
    const rsrc_t rs = mk_rsrc(p.ws, W_END);
    v4u wt[40];
    {
        const v4u sw = mk_srd(m == 0 ? p.Wq : m == 1 ? p.Wk : p.Wv, DM * NQ * 2), sx = mk_srd(p.x, ACT);
        const u32 vo = wst_src(lane, NQ) + (u32)h * (DH * 2), x8 = (u32)lane * 8u, x4 = (u32)lane * 4u;
        RowX xr[5];
        auto wfill = [&](int i0, int n) {
#pragma unroll 1
            for (int i = i0; i < i0 + n; ++i) DMAIN(sw, uu((4 * i + w) * 1024), vo, uu((16 * i + 4 * w) * (NQ * 2)));
        };
        wfill(0, QK_W0);
#pragma unroll
        for (int i = 0; i < 5; ++i)
            asm volatile("buffer_load_dwordx2 %0, %3, %5, %6 offen nt\n\tbuffer_load_dwordx2 %1, %3, %5, %6 offen offset:512 nt\n\tbuffer_load_dword %2, %4, %5, %6 offen offset:1024 nt"
                         : "=&v"(xr[i].a), "=&v"(xr[i].b), "=&v"(xr[i].c) : "v"(x8), "v"(x4), "s"(sx), "s"(uu((LN0 + 4 * c + w + 384 * i) * (DM * 2))) : "memory");
        wfill(QK_W0, 40 - QK_W0);
        if (c == 3) LST(275, rt32());
        VMW(40 - QK_W0);
        if (c == 3) LST(276, rt32());
#pragma unroll
        for (int i = 0; i < 5; ++i) {
            asm volatile("" : "+v"(xr[i].a), "+v"(xr[i].b), "+v"(xr[i].c));
            const int row = LN0 + 4 * c + w + 384 * i;
            if (i < 4 || row < SEQ) ln_row(rs, row, xr[i]);
        }
        if (c == 3) LST(264, rt32());
        VMW(12); BAR();
        lptr tb = lds + hh * 2048 + wst_rd(lane, w);
        SFOR(k, 40)
            lptr q = tb + (k >> 3) * 32768 + (k & 7) * 4096; wt[k] = tr2(q, q + 1024); asm volatile("" : "+a"(wt[k]));
        SEND
        DRAIN(); BAR();
        pre_post(rs, 96 + c, 1u, ep);
    }
    if (c == 3) LST(268, rt32());
    bool dead = false;
    int n0, n1;
    const u32 t16 = (u32)tid * 16u, wb = uu(w * 1024);
    u32 ar[16];
#pragma unroll
    for (int j = 0; j < 16; ++j) ar[j] = (u32)(r32 * 256 + ((2 * (j & 7) + hh) ^ (r32 & 15)) * 16 + (j >> 3) * 49152);
    MK_SRD();
#define XRD(y, ks) lds_r16(lds + ar[((ks) & 7) + ((ks) >= 24 ? 8 : 0)] + ((ks) >> 3) % 3 * 16384 + (y) * 8192)
#define QDMA(y, i, vo, so) DMA16I(wb, ((i) >> 1) * 16384 + ((i) & 1) * 4096 + (y) * 8192, vo, so, (i) * 4096)
    const u32 h2flag = (lane < 32 && w == 0) ? foff(F_H2 + lane) : OOB;
    const u32 ol = (m == 2 ? (u32)(2 * w + (r32 >> 4)) * 1024u : (u32)((r32 >> 4) * 4 + w) * 1024u) + (u32)(hh * 16 + (r32 & 15)) * 16u
                 + (m == 2 ? W_VO : m == 1 ? W_KQ : W_KQ + 8192u) + (u32)h * 16384u;
    const u32 fo = tid < 2 ? foff(F_QKV + tid * 96 + c) : OOB;
    lptr selw = lds + QK_SEL + (w == 0 ? 0 : 64), selr = lds + QK_SEL;
    auto late = [&](u32 fv) { const u32 need = n1 >= NSEG ? 0xFFFFFFFFu : dead ? 0u : (u32)n1 + 1u; return (u32)__builtin_amdgcn_ballot_w64(fv < need); };
    static_assert(W_H2 - W_H1 == 2 * 64 * (32 * DM * 2), "h");
    pre_wait(rs, 0, NPRE, 1u, ep);
    if (c == 3) LST(269, rt32());
    auto run = [&](auto isv) __attribute__((always_inline)) {
        constexpr bool V = decltype(isv)::value;
        f32x16 ax, ay = {};
        v4u x[8], pp, pq;
        u32 oy = OOB, lt = 1, fcnt = 0, fa = OOB;
        u32 nn = 0;
        PIN(lt); PIN(nn);
#pragma unroll
        for (int z = 0; z < 5; ++z) x[z] = XRD(1, 20 + z);
#define QPK(r_, a1, a_, q_) do { if constexpr (V) VCVT(r_[q_], a1[4 * (a_) + 8 * ((q_) >> 1) + 2 * ((q_) & 1)], a1[4 * (a_) + 8 * ((q_) >> 1) + 2 * ((q_) & 1) + 1]); \
                                 else VCVT(r_[q_], a1[4 * (a_) + (q_)], a1[4 * (a_) + (q_) + 8]); } while (0)
#define QPK4(r_, a1, a_) do { QPK(r_, a1, a_, 0); QPK(r_, a1, a_, 1); QPK(r_, a1, a_, 2); QPK(r_, a1, a_, 3); } while (0)
        for (;;) {
            u32 ls = uu(lt);
            n0 = uni((int)nn); n1 = n0 >> 8; n0 &= 255;
            if (ls != 0u && n0 >= min(NSEG, n1 + QK_LEAD)) {
                CFENCE();
                u32 fv = ld4(rs, h2flag);
                asm volatile("s_waitcnt vmcnt(0)" : "+v"(fv) :: "memory");
                *(LAS u32*)(selw + 16) = late(fv);
                BAR();
                st4s(rs, fa, 0, fcnt);
                fa = OOB;
                if (n1 >= NSEG) __builtin_amdgcn_endpgm();
                if (uu(*(LAS u32*)(selr + 16)) != 0u) {
                    if (w == 0) wait_flags(rs, h2flag, (u32)n1 + 1, dead);
                    BAR();
                }
                ls = 0u;
            }
            if (n0 == 4) pre_wait(rs, 96, 96, 1u, ep);
            int cur = n0;
            asm volatile("s_cmp_eq_u32 %3, 0\n\ts_cselect_b32 %0, %4, %0\n\ts_addc_u32 %2, %2, 0\n\ts_cmp_lg_u32 %3, 0\n\ts_addc_u32 %1, %1, 0"
                         : "+s"(cur), "+s"(n0), "+s"(n1) : "s"(ls), "s"(n1 | 64) : "scc");
            u32 xs = W_H1 + (u32)(cur + (cur & 64)) * (32 * DM * 2), ys, yvo, ox, fv;
            PINS(xs);
#define QT(k) do {} while (0)
            QT(0);
            SCHED();
            QDMA(0, 0, t16, xs); QDMA(0, 1, t16, xs);
            SFOR(z, 80)
                constexpr int ks = (z + 20) % 40, zn = (z + 5) % 80;
                if constexpr (z >= 20 && z < 60) { if constexpr (V) M32B(ks == 0, ax, x[z & 7], wt[ks]); else M32A(ks == 0, ax, wt[ks], x[z & 7]); }
                else { if constexpr (V) M32B(ks == 0, ay, x[z & 7], wt[ks]); else M32A(ks == 0, ay, wt[ks], x[z & 7]); }
                x[(z + 5) & 7] = XRD(zn < 20 || zn >= 60, (zn + 20) % 40);
                if constexpr (z == 77) lt = *(LAS u32*)selr;
                SCHED();
                if constexpr (z < 8) QDMA(0, z + 2, t16, xs);
                if constexpr (z == 9) { ys = W_H1 + (u32)n0 * (32 * DM * 2); yvo = t16 | (n0 < NSEG ? 0u : OOB); PINS(ys); PIN(yvo); }
                if constexpr (z == 11) { ox = ol + (u32)cur * (NH * 16384u); PIN(ox); }
                if constexpr (z == 14) { QT(1); asm volatile("s_waitcnt vmcnt(6) lgkmcnt(0)\n\ts_barrier" ::: "memory"); QT(2); }
                if constexpr (z == 15) st4s(rs, fa, 0, fcnt);
                if constexpr (z >= 16 && z < 26) QDMA(1, z - 16, yvo, ys);
                if constexpr (z == 26) QPK4(pp, ay, 0);
                if constexpr (z == 27) st16(rs, oy, pp);
                if constexpr (z == 28) QPK4(pq, ay, 1);
                if constexpr (z == 29) st16(rs, oy + 512, pq);
                if constexpr (z == 30) { QT(3); asm volatile("s_waitcnt vmcnt(13)\n\ts_barrier" ::: "memory"); QT(4); }
                if constexpr (z == 32) { fcnt = lane == 0 ? (u32)n0 : (u32)n1; fa = fo; asm volatile("" : "+v"(fcnt), "+v"(fa)); }
                if constexpr (z == 34) { oy = (ol + (u32)n0 * (NH * 16384u)) | (yvo & OOB); PIN(oy); }
                if constexpr (z == 35) { nn = (u32)(min(n0 + 1, NSEG) | n1 << 8); PIN(nn); }
                if constexpr (z == 54) { asm volatile("s_waitcnt vmcnt(2) lgkmcnt(0)\n\ts_barrier" ::: "memory"); QT(5); }
                if constexpr (z == QK_SMP) LD4A(fv, h2flag, 0);
                if constexpr (z == 62) QPK4(pp, ax, 0);
                if constexpr (z == 63) st16(rs, ox, pp);
                if constexpr (z == 64) QPK4(pq, ax, 1);
                if constexpr (z == 65) st16(rs, ox + 512, pq);
                if constexpr (z == 73) { asm volatile("s_waitcnt vmcnt(2)" : "+v"(fv) :: "memory"); QT(6); ls = late(fv); PINS(ls); }
                if constexpr (z == 74) { lt = ls; PIN(lt); *(LAS u32*)selw = lt; }
                if constexpr (z == 76) asm volatile("s_waitcnt lgkmcnt(2)\n\ts_barrier" ::: "memory");
                SCHED();
            SEND
        }
    };
    if (m == 2) run(IC<1>{}); else run(IC<0>{});
}

constexpr int OU_SEL = 131072;
DEV void role_out(const Params& p, lptr lds, int L, int kq, int ng, v2u ep) {
    const int tid = threadIdx.x, lane = tid & 63, w = uni(tid >> 6), hh = lane >> 5, r32 = lane & 31;
    const rsrc_t rs = mk_rsrc(p.ws, W_END);
    v4u wo[64];
    bool dead = false;
    {
        const v4u sw = mk_srd(p.Wo, DM * NQ * 2);
        const u32 vo = wst_src(lane, DM) + (u32)ng * 256u;
        auto fill = [&](int b) {
#pragma unroll 1
            for (int i = 0; i < 8; ++i) DMAIN(sw, uu((b % 5) * 32768 + (4 * i + w) * 1024), vo, uu((kq * 1024 + b * 128 + 16 * i + 4 * w) * (DM * 2)));
        };
#pragma unroll 1
        for (int b = 0; b < 5; ++b) fill(b);
        lptr tb = lds + hh * 1024 + wst_rd(lane, w);
        SFOR(b, 8)
            VMW(b < 4 ? 32 : 56 - 8 * b); BAR();
            lptr q = tb + (b % 5) * 32768;
#pragma unroll
            for (int k = 0; k < 8; ++k) { lptr a = q + (k >> 1) * 8192 + (k & 1) * 2048; wo[8 * b + k] = tr2(a, a + 4096); if (b < 7) asm volatile("" : "+a"(wo[8 * b + k])); else PIN(wo[8 * b + k]); }
            if constexpr (b < 3) { BAR(); fill(b + 5); }
        SEND
    }
    pre_wait(rs, 0, NPRE, 1u, ep);
    if (kq == 0 && ng == 0) LST(272 + L, rt32());
    MK_SRD();
    const u32 ofl = (lane < 8 && w == 0) ? foff(F_O + L * 32 + kq * 8 + lane) : OOB;
    const u32 l16 = (u32)lane * 16u;
    const u32 fo = (tid == 0) ? foff(F_P + L * 32 + kq * 5 + ng) : OOB;
    auto osrc = [&](int s) { return uu(W_VO + 8192u + (u32)((L * NSEG + s) * NH + kq * 8) * 16384u + w * 1024); };
#define ODMA(i, dst, vo, so) DMA16I(dst, (i) * 4096, vo, so, ((i) >> 1) * 16384 + ((i) & 1) * 4096)
    const u32 pvo = W_P + (u32)(L * NSEG) * P_ITEM + (u32)(4 * hh * 4 + kq) * (DM * 4) + (u32)(ng * 128 + w * 32 + r32) * 4u;
    lptr selw = lds + OU_SEL + (w == 0 ? 0 : 64);
    bool staged = false;
    u32 pfo = OOB;
    u32 buf = 0;
    BAR();
    for (int s = 0; s < NSEG; ++s) {
        const u32 nsrc = osrc(s + 1), ndst = uu((buf ^ 65536u) + w * 1024);
        lptr A = lds + buf + l16;
        if (!staged) {
            if (w == 0) wait_flags(rs, ofl, (u32)s + 1, dead);
            LS3(L == 0 && w == 0, 1, kq * 5 + ng);
            BAR();
            const u32 dst = uu(buf + w * 1024), src = osrc(s);
            SFOR(i, 16) ODMA(i, dst, l16, src); SEND
            VMW(10);
        }
        asm volatile("s_waitcnt vmcnt(26)\n\ts_barrier" ::: "memory");
        LS2(L == 0 && w == 0, 96 + kq * 5 + ng);
        v4u o[64];
#pragma unroll
        for (int k = 0; k < 3; ++k) o[k] = lds_r16(A + k * 1024);
        u32 nvo, fv; int rdy;
        f32x16 acc;
        SCHED();
        SFOR(k, 64)
            if constexpr (k < 56) M32B(k == 0, acc, o[k], wo[k]); else asm volatile("v_mfma_f32_32x32x16_bf16 %0, %1, %2, %0" : "+v"(acc) : "v"(o[k]), "v"(wo[k]));
            if constexpr (k == 21) rdy = *(LAS int*)(lds + OU_SEL);
            if constexpr (k < 61) o[k + 3] = lds_r16(A + (k + 3) * 1024);
            SCHED();
            if constexpr (k == 0) LD4A(fv, ofl, 0);
            if constexpr (k == 19) {
                asm volatile("s_waitcnt vmcnt(0)" : "+v"(fv) :: "memory");
                *(LAS int*)selw = (s + 1 < NSEG && ((u32)__builtin_amdgcn_ballot_w64(fv < (u32)s + 2) & 255u) == 0u) ? 1 : 0;
            }
            if constexpr (k == 20) { BAR(); st4s(rs, pfo, 0, (u32)s); }
            if constexpr (k == 24) { rdy = uni(rdy); nvo = l16 | (rdy ? 0u : OOB); PIN(nvo); }
            if constexpr (k >= 26 && k < 58 && (k & 1) == 0) ODMA((k - 26) >> 1, ndst, nvo, nsrc);
            SCHED();
        SEND
        staged = rdy != 0;
        u32 pa = pvo + (u32)s * P_ITEM;
        asm volatile("s_nop 9" : "+v"(acc), "+v"(pa));
#pragma unroll
        for (int i = 0; i < 16; ++i) st4s(rs, pa, (u32)((8 * (i >> 2) + (i & 3)) * (4 * DM * 4)), u_(acc[i]));
        SCHED();
        LS3(L == 0 && w == 0, 2, kq * 5 + ng);
        if (staged) pfo = fo;
        else { DRAIN(); BAR(); st4s(rs, fo, 0, (u32)s + 1); pfo = OOB; }
        buf ^= 65536u;
        LS2(L == 0 && w == 0, 160 + kq * 5 + ng);
    }
}

constexpr int RW_P = 20480, RW_X = RW_P + 10240;
DEV void role_row(const Params& p, lptr lds, int id, v2u ep) {
    const int lane = threadIdx.x & 63, w = uni(threadIdx.x >> 6);
    const int ri = id & 31, L = id >> 5;
    const rsrc_t rs = mk_rsrc(p.ws, W_END);
    bool dead = false, have = false;
    const u32 fp = lane < 20 ? foff(F_P + L * 32 + lane) : OOB, l16 = (u32)lane * 16u, lx = lane < 16 ? l16 + 1024u : OOB, fb = uu(w * CV_W);
    lptr F = lds + w * CV_W;
    MK_SRD();
    const v4u sx = mk_srd(p.x, ACT);
    auto yfill = [&](int b) {
#pragma unroll 1
        for (int i = 0; i < 5; ++i) DMA32(uu(fb + (b & 1) * 10240 + i * 2048), l16, uu(W_Y + (u32)(ri * 8 + b) * 10240u + i * 2048));
    };
    auto xfill = [&](int s) {
        const u32 d = uu(fb + RW_X + (s & 1) * 2048), o = uu((u32)(s * 32 + ri) * (DM * 2));
        if (L == 0) { DMANT(sx, d, l16, o); DMANT(sx, d + 1024, lx, o); } else { DMA16(d, l16, o + W_X2); DMA16(d + 1024, lx, o + W_X2); }
    };
    auto pfill = [&](int s) {
        const u32 o = uu(W_P + (u32)(L * NSEG + s) * P_ITEM + (u32)ri * (4 * DM * 4));
        SFOR(i, 10) DMA16I(fb, RW_P + i * 1024, l16, o, i * 1024); SEND
        if (L == 1) xfill(s);
    };
    if (L == 0) xfill(0);
    pre_poll(rs, 0, NPRE, 3u, ep);
    if (id == 0) LST(274, rt32());
    yfill(0); DRAIN();
    u32 fv = 0;
    for (int s = 0; s < NSEG; ++s) {
        if (!have) {
            if (!all_(fp == OOB || fv >= (u32)s + 1)) wait_flags(rs, fp, (u32)s + 1, dead);
            LS3(L == 0, 3, ri);
            pfill(s);
        }
        LS2(L == 0, 192 + ri);
        f32x2 y[5];
        {
            lptr ya = F + ((s >> 3) & 1) * 10240 + (s & 7) * 2 + lane * 16;
            auto yr = [&](lptr q) { return lo_f(*(LAS u16*)q); };
            y[0] = (f32x2){yr(ya), yr(ya + 2560)}; y[1] = (f32x2){yr(ya + 5120), yr(ya + 7680)};
            y[2] = (f32x2){yr(ya + 1024), yr(ya + 3584)}; y[3] = (f32x2){yr(ya + 6144), yr(ya + 8704)};
            lptr yc = ya - lane * 16 + 2048 + (lane >> 1) * 16 + (lane & 1) * 5120;
            y[4] = (f32x2){yr(yc), yr(yc + 2560)};
            asm volatile("" : "+v"(y[0]), "+v"(y[1]), "+v"(y[2]), "+v"(y[3]), "+v"(y[4]));
        }
        if (!have) DRAIN();
        LS3(L == 0, 4, ri);
        f32x2 o[5], x1[5];
#pragma unroll
        for (int kq = 0; kq < 4; ++kq) {
            lptr q = F + RW_P + kq * (DM * 4);
            const f32x4 a = *(LAS f32x4*)(q + l16), b = *(LAS f32x4*)(q + 1024 + l16); const f32x2 c = *(LAS f32x2*)(q + 2048 + lane * 8);
            if (kq == 0) { o[0] = a.lo; o[1] = a.hi; o[2] = b.lo; o[3] = b.hi; o[4] = c; }
            else { o[0] += a.lo; o[1] += a.hi; o[2] += b.lo; o[3] += b.hi; o[4] += c; }
        }
        {
            lptr q = F + RW_X + (s & 1) * 2048;
            const v2u xa = *(LAS v2u*)(q + lane * 8), xb = *(LAS v2u*)(q + 512 + lane * 8);
            const u32 xi[5] = {xa.x, xa.y, xb.x, xb.y, *(LAS u32*)(q + 1024 + lane * 4)};
#pragma unroll
            for (int i = 0; i < 5; ++i) x1[i] = up2(pk2(up2(xi[i]) + up2(pk2(o[i]))));
            asm volatile("" : "+v"(x1[0]), "+v"(x1[1]), "+v"(x1[2]), "+v"(x1[3]), "+v"(x1[4]));
        }
        have = s + 1 < NSEG && all_(fp == OOB || fv >= (u32)s + 2);
        if (__builtin_expect(have, 0)) pfill(s + 1);
        if (L == 0) xfill(s + 1);
        const f32x2 st1 = ln_stats(x1);
        u32 xp[5];
#pragma unroll
        for (int i = 0; i < 5; ++i) xp[i] = pk2(x1[i] + ((x1[i] - st1.x) * st1.y) * y[i]);
        const int row = s * 32 + ri;
        if (L == 0) {
            const u32 ro = (u32)row * (DM * 2);
            f32x2 x2[5];
#pragma unroll
            for (int i = 0; i < 5; ++i) x2[i] = up2(xp[i]);
            st8s(rs, (u32)lane * 8, W_X2 + ro, (v2u){xp[0], xp[1]});
            st8s(rs, (u32)lane * 8 + 512, W_X2 + ro, (v2u){xp[2], xp[3]});
            st4s(rs, (u32)lane * 4 + 1024, W_X2 + ro, xp[4]);
            const f32x2 st2 = ln_stats(x2);
            u32 hp[5];
#pragma unroll
            for (int i = 0; i < 5; ++i) hp[i] = pk2((x2[i] - st2.x) * st2.y);
            store_h(rs, W_H2, row, hp);
        } else {
            u16* o_ = p.out + (size_t)row * DM;
            *(v2u*)(o_ + 4 * lane) = (v2u){xp[0], xp[1]};
            *(v2u*)(o_ + 256 + 4 * lane) = (v2u){xp[2], xp[3]};
            *(u32*)(o_ + 512 + 2 * lane) = xp[4];
        }
        CFENCE();
        LS3(L == 0, 5, ri);
        fv = ld4(rs, fp);
        asm volatile("s_waitcnt vmcnt(0)" : "+v"(fv) :: "memory");
        st4s(rs, lane == 0 && L == 0 ? foff(F_H2 + ri) : OOB, 0, (u32)s + 1);
        if ((s & 7) == 3 && s < NSEG - 8) {
            if (s == 27) pre_poll(rs, 0, NCV, 5u, ep);
            if (s == 43) st8s(rs, (id == 32 && lane == 0) ? foff(F_EPOCH) : OOB, 0, (v2u){ep.x + 8u, ep.y});
            yfill((s >> 3) + 1);
        }
        if (ri == 0) LST(320 + L * 64 + s, rt32());
        LS2(L == 0, 224 + ri);
    }
}

constexpr float SCALE = 0.08838834764831845f, LOG2E = 1.4426950408889634f;
constexpr int C_K = 0, C_V = 8192, C_QX = 16384, C_MEM = 24576, C_Q = C_MEM + 10240, C_KM = C_Q + 2304, C_VM = C_KM + 2304, C_DUMP = C_VM + 2048,
              C_ZERO = C_DUMP + 1024, C_WL = C_ZERO + 128;
constexpr int NLF = 14;
static_assert(C_WL % 16 == 0 && C_WL + 4 * NLF * 2048 <= LDS_BYTES, "head lds");
#define CH_VIS 10
#define CH_H0 124
#define CH_W0 88
#define CH_MH 4
#define CH_MW 4
#define CH_REC 20
#define CH_UP 4
#define CH_WX 6
DEV void wait_until(u32 t) {
    unsigned long long c;
    asm volatile("1:\n\ts_memrealtime %0\n\ts_waitcnt lgkmcnt(0)\n\ts_mov_b64 vcc, %0\n\ts_sub_u32 vcc_lo, %1, vcc_lo\n\ts_cmpk_lt_u32 vcc_lo, 400\n\ts_cbranch_scc1 1b" : "=&s"(c) : "s"(uu(t - 1u)) : "vcc", "scc", "memory");
}
#define WAIT_TXT(tmp, t1) "s_memrealtime vcc\n1:\n\ts_waitcnt lgkmcnt(0)\n\ts_sub_u32 " tmp ", " t1 ", vcc_lo\n\ts_memrealtime vcc\n\ts_cmpk_lt_u32 " tmp ", 400\n\ts_cbranch_scc1 1b\n\t"
#define SMF_A(acc, a, b) asm volatile("v_smfmac_f32_16x16x64_bf16 %0, %1, %2, %3" : "+v"(acc) : "v"(a), "a"(b), "v"(idx))
#define SMF_V(acc, a, b) asm volatile("v_smfmac_f32_16x16x64_bf16 %0, %1, %2, %3" : "+v"(acc) : "v"(a), "v"(b), "v"(idx))
#define MF0(d, a, b) asm volatile("v_mfma_f32_16x16x32_bf16 %0, %1, %2, 0" : "=&v"(d) : "v"(a), "v"(b))
#define MF1(d, a, b) asm volatile("v_mfma_f32_16x16x32_bf16 %0, %1, %2, %0" : "+v"(d) : "v"(a), "v"(b))
#define MF0H(d, a, b) asm volatile("v_mfma_f32_16x16x16_bf16 %0, %1, %2, 0" : "=&v"(d) : "v"(a), "v"(b))
#define MF1H(d, a, b) asm volatile("v_mfma_f32_16x16x16_bf16 %0, %1, %2, %0" : "+v"(d) : "v"(a), "v"(b))
DEV v8u tr_frag(lptr p0, lptr p1, lptr p2, lptr p3) { return cat8(tr2(p0, p1), tr2(p2, p3)); }
constexpr int qslot(int ks) { return (ks & 1) && ks < 8 ? ks >> 1 : -1; }
constexpr int kslot(int ks) { return ks < 4 ? 4 + ks : -1; }
constexpr int vslot(int ks) { return ks >= 4 ? 4 + ks : -1; }
DEV u32 ring_map(u32 off, u32 half) { return ((off >> 13) << 14) + (half << 13) + (off & 8191u); }
#define ZRD(x, i) x = __builtin_bit_cast(f32x4, lds_r16(ZP + 16 * (i)))
#define VMAX(d, a, b) asm volatile("v_max_f32 %0, %1, %2" : "=v"(d) : "v"(a), "v"(b))
#define VMAX3(d, a, b, c_) asm volatile("v_max3_f32 %0, %1, %2, %3" : "=v"(d) : "v"(a), "v"(b), "v"(c_))
#define VADD(d, a, b) asm volatile("v_add_f32 %0, %1, %2" : "=v"(d) : "v"(a), "v"(b))
#define VEXP(d, a) asm volatile("v_exp_f32 %0, %1" : "=v"(d) : "v"(a))
#define VFMC(d, a, b) asm volatile("v_fmamk_f32 %0, %1, 0x3e0293ee, %2" : "=v"(d) : "v"(a), "v"(b))
#define SEL(d, a, b) asm volatile("v_cndmask_b32 %0, %1, %2, %3" : "=v"(d) : "v"(a), "v"(b), "s"(himask))
#define QPERM(d, a, q) asm volatile("v_mov_b32_dpp %0, %1 quad_perm:" q " row_mask:0xf bank_mask:0xf" : "=v"(d) : "v"(a))

DEV void role_head(const Params& p, lptr lds, int L, int h, v2u ep) {
    const int tid = threadIdx.x, lane = tid & 63, w = uni(tid >> 6), g = lane >> 4, r = lane & 15;
    const rsrc_t rs = mk_rsrc(p.ws, W_END);
    v8u wq0[10], wq1[10], wk0[10], wk1[10], wv0[10], wv1[10];
    lptr WL = lds + C_WL + w * (NLF * 2048) + lane * 16;
    {
        constexpr int GS = 1056, CS = 48 * GS;
        const v4u sq = mk_srd(p.Wq, DM * NQ * 2), sk = mk_srd(p.Wk, DM * NQ * 2), sv = mk_srd(p.Wv, DM * NQ * 2);
        const u32 vo = (u32)(g * NQ + h * DH + 8 * (r ^ ((g & 1) << 3))) * 2u;
        auto fill = [&](int ch) {
#pragma unroll 1
            for (int i = 0; i < 4; ++i) {
                const u32 dst = uu((ch % 3) * CS + (4 * i + w) * GS), so = uu((ch * 64 + 16 * i + 4 * w) * (NQ * 2));
                DMAIN(sq, dst, vo, so); DMAIN(sk, dst + 16 * GS, vo, so); DMAIN(sv, dst + 32 * GS, vo, so);
            }
        };
        __builtin_amdgcn_s_sleep(ST_HEAD);
        fill(0); fill(1); fill(2);
        const int q4 = r >> 2;
        lptr tb = lds + (4 * (g >> 1) + 2 * (q4 >> 1) + (g & 1)) * GS + (q4 & 1) * 256 + ((w * 64 + (r & 3) * 8) ^ ((q4 & 1) << 7));
        SFOR(ch, 10)
            VMW(ch < 8 ? 24 : 108 - 12 * ch);
            BAR();
            auto frag = [&](int m, int t) { lptr b = tb + (ch % 3) * CS + m * 16 * GS + t * 32; return tr_frag(b, b + 512, b + 8 * GS, b + 8 * GS + 512); };
            wq0[ch] = frag(0, 0); wq1[ch] = frag(0, 1); wk0[ch] = frag(1, 0); wk1[ch] = frag(1, 1); wv0[ch] = frag(2, 0); wv1[ch] = frag(2, 1);
            asm volatile("" : "+a"(wq0[ch]), "+a"(wk0[ch]), "+v"(wv0[ch]), "+v"(wv1[ch]));
            if constexpr (qslot(ch) < 0) asm volatile("" : "+a"(wq1[ch])); else asm volatile("" : "+v"(wq1[ch]));
            if constexpr (kslot(ch) < 0) asm volatile("" : "+a"(wk1[ch])); else asm volatile("" : "+v"(wk1[ch]));
            BAR();
            if constexpr (ch < 7) fill(ch + 3);
        SEND
#pragma unroll
        for (int ch = 0; ch < 10; ++ch) {
            if (qslot(ch) >= 0) { lds_w16(WL + qslot(ch) * 2048, wq1[ch].lo); lds_w16(WL + qslot(ch) * 2048 + 1024, wq1[ch].hi); }
            if (kslot(ch) >= 0) { lds_w16(WL + kslot(ch) * 2048, wk1[ch].lo); lds_w16(WL + kslot(ch) * 2048 + 1024, wk1[ch].hi); }
            if (vslot(ch) >= 0) { lds_w16(WL + vslot(ch) * 2048, wv1[ch].lo); lds_w16(WL + vslot(ch) * 2048 + 1024, wv1[ch].hi); }
        }
        CFENCE();
    }
    const u32 idx = (r & 2) ? 0xEEEEu : 0x4444u;
    const unsigned long long himask = 0xCCCCCCCCCCCCCCCCull;
    bool dead = false;
    if (h == 3) LST(266 + L, rt32());
    pre_wait(rs, 0, NCV, 1u, ep);
    const u32 pol0 = uu(get_pol(rs));
    const u32 l16 = (u32)lane * 16u;
    const u32 mring = W_MRING + (u32)L * (MB / 4) + pol0 * 8192u, hint = mring + 4 * 32768u;
    const u32 fkh = lane < 3 ? foff(F_QKV + L * 96 + lane * 32 + h) : lane < 6 ? hint + (u32)(lane - 3) * 4u : OOB;
    u32 tm = 0;
    MK_SRD();
    u32 ksrc = uu(W_KQ + (u32)(L * NSEG * NH + h) * 16384u + w * 2048), vsrc = ksrc + (W_VO - W_KQ);
    u32 dk = uu(C_K + w * 2048), dv = dk + (C_V - C_K), dq = dk + (C_QX - C_K), dm = uu(C_MEM + w * 2048), dm2 = uu(w < 2 ? C_MEM + 8192 + w * 1024 : C_DUMP);
    asm volatile("" : "+s"(dk), "+s"(dv), "+s"(dq), "+s"(dm), "+s"(dm2));
    wait_flags(rs, lane < 3 ? fkh : OOB, 1u, dead);
    DMA32(dk, l16, ksrc); DMA32(dv, l16, vsrc); DMA32(dq, l16, ksrc ^ 8192u);
    lptr qw = lds + C_Q + (2 * g) * 272 + (32 * w + 2 * r) * 2;
    lptr qr = lds + C_Q + (2 * (r >> 2) + (r & 1)) * 272 + g * 16;
    lptr LB = lds + l16, VX = lds + C_V + w * 2048 + l16;
    if (tid >= 64 && tid < 70) { const u32 z = tid == 68 ? 0x3F803F80u : tid == 69 ? PSENT : 0u; lds_w16(lds + C_ZERO + (tid - 64) * 16, (v4u){z, z, z, z}); }
    u32 zaddr = 0; PIN(zaddr);
    lptr ZP = lds + zaddr + C_ZERO;
    const float CEXP = SCALE * LOG2E;
#define LF_RD(slot, hf) lf[slot][hf] = lds_r16(WL + (slot) * 2048 + (hf) * 1024)
    BAR();
    const u32 ofl = tid == 0 ? foff(F_O + L * 32 + h) : OOB;
    u32 oso = (g & 1) ? OOB : W_ORING + (u32)L * (MB / 2) + ring_map((u32)((r >> 3) * 32768 + h * 1024 + w * 256 + (g >> 1) * 128 + (r & 7) * 16), pol0);
    unsigned long long vis[8];
#pragma unroll
    for (int i = 0; i < 8; ++i) vis[i] = __builtin_amdgcn_ballot_w64(16 * (i >> 2) + 4 * g + (i & 3) <= 16 * (w & 1) + r);
    u32 missed = 0;
    for (int s = 0; s < NSEG;) {
        u32 ms = mring + (u32)(s & 3) * 32768u + w * 2048, ms2 = mring + (u32)(s & 3) * 32768u + 16384u + (w & 1) * 1024, sv = (u32)s;
        asm volatile("" : "+s"(ms), "+s"(ms2)); PIN(sv);
        f32x4 aq0, aq1, ak0, ak1;
        v4u lf[NLF][2];
        ZRD(aq0, 0); ZRD(aq1, 1); ZRD(ak0, 2); ZRD(ak1, 3);
#pragma unroll
        for (int i = 0; i < 4; ++i) { LF_RD(i, 0); LF_RD(i, 1); }
        SCHED();
        v4u A[10];
#define HT(k) do {} while (0)
        v4u kx[8], qf[4], kmf[4], vx[2], ones, P;
        float fa, fb, fc, fd, mx, my, sx[4], t[4], tm0, tm1; u32 pa, pb, fl, e01, e23, pmm; unsigned long long nanq;
        f32x4 av0, av1, s0, s1, sm, oa, ob, rsum;
        v2u vm, pm;
#define LW(sl) cat8(lf[sl][0], lf[sl][1])
#define PQ(ks, tl) do { if constexpr (!(tl)) SMF_A(aq0, A[ks], wq0[ks]); else if constexpr (qslot(ks) < 0) SMF_A(aq1, A[ks], wq1[ks]); else { const v8u lw_ = LW(qslot(ks)); SMF_V(aq1, A[ks], lw_); } } while (0)
#define PK(ks, tl) do { if constexpr (!(tl)) SMF_A(ak0, A[ks], wk0[ks]); else if constexpr (kslot(ks) < 0) SMF_A(ak1, A[ks], wk1[ks]); else { const v8u lw_ = LW(kslot(ks)); SMF_V(ak1, A[ks], lw_); } } while (0)
#define PV(ks, tl) do { if constexpr (!(tl)) SMF_V(av0, A[ks], wv0[ks]); else if constexpr (vslot(ks) < 0) SMF_V(av1, A[ks], wv1[ks]); else { const v8u lw_ = LW(vslot(ks)); SMF_V(av1, A[ks], lw_); } } while (0)
#define KXR(i) kx[i] = lds_r16(LB + C_K + ((i) & 1) * 4096 + ((i) >> 1) * 1024)
#define XW(q_) do { *(LAS u32*)(q_) = pa; *(LAS u32*)((q_) + 272) = pb; } while (0)
        auto slot = [&](auto zc) __attribute__((always_inline)) {
            constexpr int z = decltype(zc)::value;
            if constexpr (z < 14) PQ(z >> 1, z & 1);
            else if constexpr (z < 17) PQ(z - 7, 0);
            else if constexpr (z < 20) PQ(z - 10, 1);
            else if constexpr (z < 33) PK((z - 20) >> 1, z & 1);
            else if constexpr (z < 35) { if constexpr (z == 33) MF0(s0, kx[0], qf[0]); else MF0(s1, kx[1], qf[0]); }
            else if constexpr (z < 41) { if constexpr (z & 1) MF1(s0, kx[z - 33], qf[(z - 33) >> 1]); else MF1(s1, kx[z - 33], qf[(z - 33) >> 1]); }
            else if constexpr (z == 41) PK(6, 1);
            else if constexpr (z < 45) PK(z - 35, 0);
            else if constexpr (z < 48) PK(z - 38, 1);
            else if constexpr (z < 58) PV(z - 48, 0);
            else if constexpr (z < 61) PV(z < 60 ? z - 54 : 0, 1);
            else if constexpr (z == 61) MF0(sm, kmf[0], qf[0]);
            else if constexpr (z < 65) MF1(sm, kmf[z - 61], qf[z - 61]);
            else if constexpr (z == 65) MF0(oa, vx[0], P);
            else if constexpr (z == 66) MF0(ob, vx[1], P);
            else PV(6, 1);
            if constexpr (z < 6) A[4 + z] = lds_r16(LB + C_MEM + (4 + z) * 1024);
            if constexpr (z == 6 || z == 7) LF_RD(4, z - 6);
            if constexpr (z == 8 || z == 9) LF_RD(5, z - 8);
            if constexpr (z == 12 || z == 13) LF_RD(6, z - 12);
            if constexpr (z == 16 || z == 17) KXR(z - 16);
            if constexpr (z == 18 || z == 19) LF_RD(7, z - 18);
            if constexpr (z >= 20 && z < 24) KXR(z - 18);
            if constexpr (z == 32 || z == 33) KXR(z - 26);
            if constexpr (z == 24) XW(qw);
            if constexpr (z == 25) ZRD(av0, 0);
            if constexpr (z == 26) ZRD(av1, 1);
            if constexpr (z == 27) asm volatile("s_waitcnt lgkmcnt(2)\n\ts_barrier" ::: "memory");
            if constexpr (z >= 28 && z < 32) qf[z - 28] = lds_r16(qr + (z - 28) * 64);
            if constexpr (z == 35) fl = ld4(rs, fkh);
            if constexpr (z == 52) XW(qw + (C_KM - C_Q));
            if constexpr (z == 53 || z == 54) vx[z - 53] = lds_r16(VX + (z - 53) * 1024);
            if constexpr (z == 55) asm volatile("s_waitcnt lgkmcnt(2)\n\ts_barrier" ::: "memory");
            if constexpr (z >= 56 && z < 60) kmf[z - 56] = lds_r16(qr + (C_KM - C_Q) + (z - 56) * 64);
            if constexpr (z >= 48 && z < 52) LF_RD(8 + ((z - 48) >> 1), z & 1);
            if constexpr (z >= 60) LF_RD(10 + ((z - 60) >> 1), z & 1);
            SCHED();
            if constexpr (z == 19) HT(2);
            if constexpr (z == 32) HT(3);
            if constexpr (z == 47) HT(4);
            if constexpr (z == 64) HT(5);
            if constexpr (z == 20) VADD(fa, aq0[0], aq0[2]);
            if constexpr (z == 21) VADD(fb, aq0[1], aq0[3]);
            if constexpr (z == 22) { VADD(fc, aq1[0], aq1[2]); VADD(fd, aq1[1], aq1[3]); }
            if constexpr (z == 23) { asm volatile("v_cmp_u_f32 %0, %1, %2" : "=s"(nanq) : "v"(fa), "v"(fb)); VCVT(pa, fa, fc); VCVT(pb, fb, fd); }
            if constexpr (z == 39 || z == 40 || z == 41) NOPS(1);
            if constexpr (z == 42) VMAX3(mx, s0[0], s0[1], s0[2]);
            if constexpr (z == 43) { VMAX3(mx, mx, s0[3], s1[0]); VMAX3(my, s1[1], s1[2], s1[3]); }
            if constexpr (z == 44 || z == 46) { VMAX(fa, mx, my); VMAX(my, mx, my); mx = fa; }
            if constexpr (z == 45) { SEL(sx[0], s0[0], s1[0]); asm volatile("v_permlane16_swap_b32 %0, %1" : "+v"(mx), "+v"(my)); }
            if constexpr (z == 47) { SEL(sx[1], s0[1], s1[1]); asm volatile("v_permlane32_swap_b32 %0, %1" : "+v"(mx), "+v"(my)); }
            if constexpr (z == 48) asm volatile("v_max_f32 %0, %0, %1\n\tv_mul_f32 %0, 0xbe0293ee, %0" : "+v"(mx) : "v"(my));
            if constexpr (z == 49) { VADD(fa, ak0[0], ak0[2]); VADD(fb, ak0[1], ak0[3]); }
            if constexpr (z == 50) { VADD(fc, ak1[0], ak1[2]); VADD(fd, ak1[1], ak1[3]); }
            if constexpr (z == 51) { VCVT(pa, fa, fc); VCVT(pb, fb, fd); }
            if constexpr (z == 52) SEL(sx[2], s0[2], s1[2]);
            if constexpr (z == 53) { SEL(sx[3], s0[3], s1[3]); VFMC(t[0], sx[0], mx); }
            if constexpr (z == 54) { VFMC(t[1], sx[1], mx); VFMC(t[2], sx[2], mx); }
            if constexpr (z == 55) VFMC(t[3], sx[3], mx);
            if constexpr (z >= 56 && z < 60) VEXP(t[z - 56], t[z - 56]);
            if constexpr (z == 60) { VCVT(e01, t[0], t[1]); VCVT(e23, t[2], t[3]); }
            if constexpr (z == 61) { QPERM(P.x, e01, "[0,1,0,1]"); QPERM(P.z, e01, "[2,3,2,3]"); }
            if constexpr (z == 62) { QPERM(P.y, e23, "[0,1,0,1]"); QPERM(P.w, e23, "[2,3,2,3]"); }
            if constexpr (z == 63) { VADD(fa, av0[0], av0[2]); VADD(fb, av0[1], av0[3]); }
            if constexpr (z == 64) VCVT(vm.x, fa, fb);
            if constexpr (z == 67) { asm volatile("" :: "v"(sm)); VFMC(tm0, sm[0], mx); VFMC(tm1, sm[1], mx); }
            SCHED();
        };
        {
            u32 tmp;
            asm volatile("s_mov_b32 m0, %2\n\t" WAIT_TXT("%0", "%1") "buffer_load_dwordx4 %4, %5, %6 offen sc1 lds\n\tbuffer_load_dwordx4 %4, %5, %6 offen offset:1024 sc1 lds\n\t"
                         "s_mov_b32 m0, %3\n\ts_nop 0\n\tbuffer_load_dwordx4 %4, %5, %7 offen sc1 lds\n\ts_waitcnt vmcnt(0) lgkmcnt(0)\n\ts_barrier"
                         : "=&s"(tmp) : "s"(uu(tm - 1u)), "s"(dm), "s"(dm2), "v"(l16), "s"(srd), "s"(ms), "s"(ms2) : "vcc", "scc", "memory");
        }
#pragma unroll
        for (int ks = 0; ks < 4; ++ks) A[ks] = lds_r16(LB + C_MEM + ks * 1024);
        SCHED();
        st4s(rs, ofl, 0, sv);
        SCHED();
        static_for<0, 24>(slot);
        if (__builtin_expect(nanq != 0ull, 0)) {
            bool miss = false;
#pragma unroll
            for (int i = 0; i < 10; ++i) miss |= A[i].x == PSENT || A[i].y == PSENT || A[i].z == PSENT || A[i].w == PSENT;
            nanq = missed < SPIN_CAP ? __builtin_amdgcn_ballot_w64(miss) : 0ull;
        }
        if (__builtin_expect(nanq == 0ull, 1)) {
        static_for<24, 68>(slot);
        PV(7, 1); ones = lds_r16(ZP + 64); SCHED();
        VMAX(fa, tm0, tm1);
        if (__builtin_expect(__builtin_amdgcn_ballot_w64(fa > 24.0f) != 0ull, 0)) {
            const float up = fmaxf(max32(max16(fmaxf(tm0, tm1))), 0.f);
            tm0 -= up; tm1 -= up;
#pragma unroll
            for (int i = 0; i < 4; ++i) t[i] = __builtin_amdgcn_exp2f(__builtin_fmaf(sx[i], CEXP, mx) - up);
            e01 = pk(t[0], t[1]); e23 = pk(t[2], t[3]);
            asm volatile("s_nop 1" : "+v"(e01), "+v"(e23));
            QPERM(P.x, e01, "[0,1,0,1]"); QPERM(P.z, e01, "[2,3,2,3]"); QPERM(P.y, e23, "[0,1,0,1]"); QPERM(P.w, e23, "[2,3,2,3]");
            NOPS(1);
            MF0(oa, vx[0], P); MF0(ob, vx[1], P);
            NOPS(7);
        }
        SCHED();
        PV(1, 1); VEXP(tm0, tm0); SEL(oa[0], oa[0], ob[0]); SCHED();
        PV(8, 1); VEXP(tm1, tm1); SEL(oa[1], oa[1], ob[1]); SCHED();
        PV(9, 1); VCVT(pmm, tm0, tm1); SEL(oa[2], oa[2], ob[2]); SCHED();
        PV(2, 1);
        asm volatile("v_cndmask_b32 %0, %2, 0, %3\n\tv_cndmask_b32 %1, 0, %2, %3" : "=&v"(pm.x), "=&v"(pm.y) : "v"(pmm), "s"(himask)); SCHED();
        PV(3, 1); SEL(oa[3], oa[3], ob[3]); SCHED();
        MF0(rsum, ones, P); NOPS(4);
        VADD(fc, av1[0], av1[2]); VADD(fd, av1[1], av1[3]);
        { const v2u ones2 = ones.lo; MF1H(rsum, ones2, pm); }
        VCVT(vm.y, fc, fd); NOPS(1);
        MF1H(oa, vm, pm);
        {
            float inv; v4u oc;
            asm volatile("s_nop 3\n\tv_rcp_f32 %0, %1\n\ts_nop 2" : "=v"(inv) : "v"(rsum[0]), "v"(rsum));
            PIN(oa);
            const f32x2 i2 = {inv, inv};
            const f32x2 u0 = oa.lo * i2, u1 = oa.hi * i2;
            asm volatile("v_cvt_pk_bf16_f32 %0, %4, %5\n\tv_cvt_pk_bf16_f32 %2, %4, %5\n\tv_cvt_pk_bf16_f32 %1, %6, %7\n\tv_cvt_pk_bf16_f32 %3, %6, %7\n\t"
                         "v_permlane16_swap_b32 %0, %2\n\ts_nop 0\n\tv_permlane16_swap_b32 %1, %3"
                         : "=&v"(oc.x), "=&v"(oc.y), "=&v"(oc.z), "=&v"(oc.w) : "v"(u0.x), "v"(u0.y), "v"(u1.x), "v"(u1.y));
            st16(rs, oso, oc);
        }
        SCHED();
        unsigned long long clk;
        asm volatile("s_memrealtime %0" : "=s"(clk));
        HT(6);
        PIN(fl);
        v4u tq[4], tk[8], tv[2];
        f32x4 c0, c1, cm;
        u32 oo, kvo; bool kvok;
        u32 ln = l16; PIN(ln);
        lptr QX = lds + C_QX + (w & 1) * 4096 + ln, VO = lds + C_V + (w ^ 1) * 2048 + ln, VMX = lds + C_VM + w * 512 + (ln >> 1);
#pragma unroll
        for (int i = 0; i < 2; ++i) { tq[i] = lds_r16(QX + i * 1024); tk[2 * i] = lds_r16(LB + C_K + i * 1024); tk[2 * i + 1] = lds_r16(LB + C_K + 4096 + i * 1024); }
        *(LAS v2u*)VMX = vm;
        SCHED();
        SFOR(i, 12)
            if constexpr (i == 0) MF0(cm, kmf[0], tq[0]); else if constexpr (i == 1) MF0(c0, tk[0], tq[0]); else if constexpr (i == 2) MF0(c1, tk[1], tq[0]);
            else if constexpr (i % 3 == 0) MF1(cm, kmf[i / 3], tq[i / 3]); else if constexpr (i % 3 == 1) MF1(c0, tk[2 * (i / 3)], tq[i / 3]); else MF1(c1, tk[2 * (i / 3) + 1], tq[i / 3]);
            if constexpr (i == 0) { tq[2] = lds_r16(QX + 2048); tk[4] = lds_r16(LB + C_K + 2048); }
            if constexpr (i == 1) { tk[5] = lds_r16(LB + C_K + 6144); tq[3] = lds_r16(QX + 3072); }
            if constexpr (i == 2) { tk[6] = lds_r16(LB + C_K + 3072); tk[7] = lds_r16(LB + C_K + 7168); }
            if constexpr (i == 3) { tv[0] = lds_r16(VO); tv[1] = lds_r16(VO + 1024); }
            if constexpr (i == 4) { oo = ln + (ln & 0x300u) + ((vsrc ^ 8192u) + (u32)((w & 1) * 256)); PIN(oo); }
            if constexpr (i == 5) { ksrc += NH * 16384; vsrc += NH * 16384; PINS(ksrc); PINS(vsrc); }
            if constexpr (i == 7) { kvok = ((u32)__builtin_amdgcn_ballot_w64(fl < (u32)s + 2) & 7u) == 0u; }
            if constexpr (i == 8) { kvo = l16 | (kvok ? 0u : OOB); PIN(kvo); }
            SCHED();
        SEND
        asm volatile("s_waitcnt lgkmcnt(0)\n\ts_barrier" : "+s"(clk) :: "memory");
        const v2u tvm = *(LAS v2u*)(VMX + (512 - (w & 1) * 1024));
        const v4u pv = lds_r16(ZP + 80);
        DMA32(dk, kvo, ksrc);
        SCHED(); asm volatile("" :: "v"(cm));
        float u[10], m, m2;
        {
            float NEG = -1000000000.0f / SCALE; PIN(NEG);
#define MSK(d, a, i) asm volatile("v_cndmask_b32 %0, %1, %2, %3" : "=v"(d) : "v"(NEG), "v"(a), "s"(vis[i]))
#pragma unroll
            for (int i = 0; i < 4; ++i) MSK(u[i], c0[i], i);
#pragma unroll
            for (int i = 0; i < 4; ++i) MSK(u[4 + i], c1[i], 4 + i);
            asm volatile("v_max3_f32 %0, %2, %3, %10\n\tv_max3_f32 %1, %4, %5, %11\n\tv_max3_f32 %0, %0, %6, %7\n\tv_max3_f32 %1, %1, %8, %9\n\t"
                         "v_max_f32 %0, %0, %1\n\tv_mov_b32 %1, %0\n\ts_nop 1\n\tv_permlane16_swap_b32 %0, %1\n\tv_max_f32 %0, %0, %1\n\tv_mov_b32 %1, %0\n\ts_nop 1\n\t"
                         "v_permlane32_swap_b32 %0, %1\n\tv_max_f32 %0, %0, %1\n\tv_mul_f32 %0, 0xbe0293ee, %0"
                         : "=&v"(m), "=&v"(m2) : "v"(u[0]), "v"(u[1]), "v"(u[2]), "v"(u[3]), "v"(u[4]), "v"(u[5]), "v"(u[6]), "v"(u[7]), "v"(cm[0]), "v"(cm[1]));
        }
        DMA32(dv, kvo, vsrc);
        {
            v4u px; v2u p0, p1;
#define EX2(d, a, b) asm volatile("v_fmamk_f32 %0, %2, 0x3e0293ee, %4\n\tv_fmamk_f32 %1, %3, 0x3e0293ee, %4\n\tv_exp_f32 %0, %0\n\tv_exp_f32 %1, %1" : "=&v"(d.x), "=&v"(d.y) : "v"(a), "v"(b), "v"(m))
            f32x2 ex[5];
            EX2(ex[0], u[0], u[1]); EX2(ex[1], u[2], u[3]); EX2(ex[2], u[4], u[5]); EX2(ex[3], u[6], u[7]); EX2(ex[4], cm[0], cm[1]);
            asm volatile("v_cvt_pk_bf16_f32 %0, %8, %9\n\tv_cvt_pk_bf16_f32 %1, %10, %11\n\tv_cvt_pk_bf16_f32 %2, %12, %13\n\tv_cvt_pk_bf16_f32 %3, %14, %15\n\t"
                         "v_mov_b32 %5, 0\n\tv_mov_b32 %6, 0\n\tv_cvt_pk_bf16_f32 %4, %16, %17\n\tv_cvt_pk_bf16_f32 %7, %16, %17"
                         : "=&v"(px.x), "=&v"(px.y), "=&v"(px.z), "=&v"(px.w), "=&v"(p0.x), "=&v"(p0.y), "=&v"(p1.x), "=&v"(p1.y)
                         : "v"(ex[0].x), "v"(ex[0].y), "v"(ex[1].x), "v"(ex[1].y), "v"(ex[2].x), "v"(ex[2].y), "v"(ex[3].x), "v"(ex[3].y), "v"(ex[4].x), "v"(ex[4].y));
            f32x4 o[4], sum;
            const v2u ones2 = ones.lo;
            const u32 now = (u32)clk;
            u32 oz;
            SCHED();
            MF0(sum, ones, px); { const u32 q = ksrc ^ 8192u; DMA32(dq, kvo, q); } SCHED();
            MF0(o[0], vx[0], px);
            MF0(o[1], vx[1], px);
            MF0(o[2], tv[0], px); oz = (oso & ~(3u << 17)) | (u32)((s + 3) & 3) << 17; PIN(oz); SCHED();
            MF0(o[3], tv[1], px); oso = (oso & ~(3u << 17)) | (u32)((s + 1) & 3) << 17; PIN(oso); SCHED();
            MF1H(sum, ones2, p0);
            MF1H(o[0], vm, p0);
            MF1H(o[1], vm, p1); st16(rs, oz, pv); SCHED();
            MF1H(o[2], tvm, p0);
            MF1H(o[3], tvm, p1);
            float inv;
            asm volatile("s_nop 2\n\tv_rcp_f32 %0, %1\n\ts_nop 0" : "=v"(inv) : "v"(sum[0]), "v"(sum));
            asm volatile("" : "+v"(o[0]), "+v"(o[1]));
            st16(rs, oo, (v4u){pk2(o[0].lo * inv), pk2(o[0].hi * inv), pk2(o[1].lo * inv), pk2(o[1].hi * inv)});
            asm volatile("" : "+v"(o[2]), "+v"(o[3]));
            st16(rs, oo ^ 2048u, (v4u){pk2(o[2].lo * inv), pk2(o[2].hi * inv), pk2(o[3].lo * inv), pk2(o[3].hi * inv)});
            SCHED();
            if (__builtin_expect(h == 0 && w == 3, 0)) {
                st4s(rs, ln == 0 ? hint + 128u : OOB, 0, (u32)s << 16 | min(missed, 1u) << 14 | min(now - tm, 0x3FFFu));
                LST(L * 64 + s, now); LST(128 + L * 64 + s, ((int)(rdl(fl, 3) - now) >= CH_VIS - 4) | ((int)kvok << 1) | ((int)min(missed, 3u) << 2)); LST(640 + L * 64 + s, tm); LST(1728 + L * 64 + s, __builtin_amdgcn_s_memtime());
            }
            tm = rdl(fl, 5);
            missed = missed == SPIN_CAP ? missed : 0u;
            if (__builtin_expect((int)(rdl(fl, 3) - now) < CH_VIS - 4, 0)) {
#pragma unroll 1
                for (int n = 0; n < 8; ++n) {
                    CFENCE();
                    const u32 hv = ld4(rs, fkh);
                    tm = rdl(hv, 5);
                    if ((int)(rdl(hv, 3) - now) >= CH_VIS - 4) break;
                }
            }
        }
        LS2(L == 0 && w == 0, h);
        if (__builtin_expect(!kvok, 0) && s + 1 < NSEG) {
            st4s(rs, (ln == 0 && w == 0) ? hint + 4096u + (u32)h * 128u : OOB, 0, (u32)s + 1u);
            wait_flags(rs, ln < 48u ? fkh : OOB, (u32)s + 2, dead);
            DMA32(dk, l16, ksrc); DMA32(dv, l16, vsrc); DMA32(dq, l16, ksrc ^ 8192u);
        }
        ++s;
        } else {
            u32 ln = l16; PIN(ln);
            const u32 hv = ld4(rs, ln == 96u ? foff(F_ERR) : fkh);
            tm = rdl(hv, 5);
            *(LAS u32*)(lds + C_DUMP + (w == 0 ? 0 : 64)) = rdl(hv, 6);
            BAR();
            missed = uu(*(LAS u32*)(lds + C_DUMP)) != 0u ? SPIN_CAP : missed + 1;
            st4s(rs, missed == SPIN_CAP ? foff(F_ERR) : OOB, 0, 1u);
            BAR();
        }
    }
    DRAIN(); BAR();
    st4s(rs, ofl, 0, NSEG);
}

#define WO_GAP 6
DEV void role_wo(const Params& p, lptr lds, int j, v2u ep) {
    const int tid = threadIdx.x, lane = tid & 63, w = uni(tid >> 6), g = lane >> 4, r = lane & 15;
    const int rg = j & 1, cg = j >> 1, rot = cg & 7;
    const rsrc_t rs = mk_rsrc(p.ws, W_END);
    v8u wo[8][5];
    {
        const v4u so_ = mk_srd(p.Wo, DM * NQ * 2);
        u32 vo[5];
#pragma unroll
        for (int i = 0; i < 5; ++i) { const int n = 64 * i + lane; vo[i] = (u32)((n / 5) * DM + cg * 40 + (n % 5) * 8) * 2u; }
        auto fill = [&](int b) {
            const u32 dst = uu(w * 40960 + (b & 3) * 10240), src = uu((8 * w + ((b + rot) & 7)) * (DH * DM * 2));
#pragma unroll
            for (int i = 0; i < 10; ++i) DMAIN(so_, dst + i * 1024, vo[i % 5], src + (i / 5) * (64 * DM * 2));
        };
        __builtin_amdgcn_s_sleep(ST_WO);
#pragma unroll 1
        for (int b = 0; b < 4; ++b) fill(b);
        const int q4 = r >> 2;
        lptr tb = lds + w * 40960 + (32 * (g >> 1) + 16 * (q4 >> 1) + 8 * ((r >> 1) & 1) + 4 * (g & 1) + (q4 & 1)) * 80 + (r & 1) * 8;
        SFOR(b, 8)
            VMW(b < 5 ? 30 : 70 - 10 * b);
#pragma unroll
            for (int t = 0; t < 5; ++t) {
                lptr q = tb + (b & 3) * 10240 + t * 16;
                wo[b][t] = tr_frag(q, q + 160, q + 64 * 80, q + 64 * 80 + 160);
                if (b * 5 + t < 32) asm volatile("" : "+a"(wo[b][t])); else asm volatile("" : "+v"(wo[b][t]));
            }
            CFENCE();
            if constexpr (b < 4) fill(b + 4);
        SEND
    }
    if (j == 0) LST(265, rt32());
    pre_wait(rs, 0, NCV, 1u, ep);
    const u32 idx = (r & 2) ? 0xEEEEu : 0x4444u;
    bool dead = false;
    const u32 pol0 = uu(get_pol(rs));
    const u32 l16 = (u32)lane * 16u;
    u32 oo[8];
#pragma unroll
    for (int i = 0; i < 8; ++i) oo[i] = uu(W_ORING + ring_map((u32)(rg * 32768 + (8 * w + ((i + rot) & 7)) * 1024), pol0));
    const u32 hint = W_MRING + 4 * 32768u + pol0 * 8192u;
    u32 hvo[2];
#pragma unroll
    for (int l = 0; l < 2; ++l) hvo[l] = lane < 2 ? hint + (u32)(l ^ 1) * (MB / 4) + (u32)lane * 4u : lane == 2 ? hint + (u32)l * (MB / 4) + 128u : lane == 3 ? foff(F_ERR) :
                 (lane >= 32 && j == 0 && w == 3) ? hint + (u32)l * (MB / 4) + (u32)lane * 128u : OOB;
    lptr xw = r < 8 ? lds + w * 640 + (2 * (g & 1) + (g >> 1)) * 32 + r * 4 : lds + 8192 + lane * 4;
    const int G = ((5 * cg) >> 1) + w, tt = 2 * G + ((lane >> 3) & 1) - 5 * cg;
    const bool pub = w < 3 && lane < 32 && tt >= 0 && tt < 5;
    lptr xr = lds + (pub ? tt * 128 + (2 * (lane >> 4) + ((lane >> 2) & 1)) * 32 + (lane & 3) * 8 : 0);
    const u32 mpo = pub ? W_MRING + ring_map((u32)(G * 256 + rg * 128 + lane * 4), pol0) : OOB;
    constexpr u32 PER0 = CH_W0 + CH_MW + CH_H0 + CH_MH + 2 * CH_VIS;
    const bool lead = j == 0 && w == 3;
    const u32 hso = (lead && lane == 0) ? hint : OOB;
    MK_SRD();
    u32 t = 0, tn = 0;
    if (j == 0) {
#pragma unroll 1
        for (int n = 0; n < SPIN_CAP && !dead; ++n) {
            CFENCE();
            if (all_(ld4(rs, W_ORING + pol0 * 8192u + (u32)w * 16384u + (u32)lane * 128u) != PSENT)) break;
            __builtin_amdgcn_s_sleep(1);
        }
        BAR();
    }
    if (lead) {
        t = rt32() + 40u; tn = t + PER0 / 2 + CH_REC;
        st16(rs, lane < 2 ? hint + (u32)lane * (MB / 4) : OOB, (v4u){lane == 0 ? t : tn, PER0, (lane == 0 ? t : tn) + (CH_W0 + CH_VIS + CH_MW + CH_REC), 0u});
    } else {
#pragma unroll 1
        for (int n = 0; n < SPIN_CAP && !dead; ++n) {
            CFENCE();
            const u32 hv = ld4(rs, hvo[1]);
            t = rdl(hv, 0);
            if (rdl(hv, 1) < 400u) break;
            __builtin_amdgcn_s_sleep(1);
        }
    }
    int sn[2] = {0, 0};
    u32 hav = 4 * CH_H0, wav = 4 * CH_W0, mh = 8 * CH_MH, mw = 8 * CH_MW, ok[2] = {0u, 0u}, dw[2] = {CH_W0 + CH_VIS + CH_MW + CH_REC, CH_W0 + CH_VIS + CH_MW + CH_REC};
    u32 zero = 0, hv = 0xFFFFu, pst = 1, pdur = 0, pmp = OOB;
    const u32 psent = PSENT;
    lptr sw = lds + 2560 + w * 4;
    bool stop = false;
    asm volatile("" : "+v"(zero), "+v"(sw));
    auto turn = [&](auto lc) __attribute__((always_inline)) {
        constexpr int L = decltype(lc)::value, M = L ^ 1;
        u32 now, done, rep, rd, lost, dh, dn, nt, za; v4u hw;
        const int sc = sn[L];
        auto books = [&](auto ic) __attribute__((always_inline)) {
            constexpr int i = decltype(ic)::value;
            if constexpr (i == 0) { rep = rdl(hv, 2); PINS(rep); }
            if constexpr (i == 1) { lost = (u32)(__builtin_amdgcn_ballot_w64(hv == (u32)sn[M]) >> 32); PINS(lost); }
            if constexpr (i == 2) { lost |= (rep >> 16) ^ (u32)sn[M]; PINS(lost); }
            if constexpr (i == 3) { lost |= ok[M] ^ 2u; rd = rep & 0xFFFFu; PINS(lost); PINS(rd); }
            if constexpr (i == 4) { lost = lost == 0u ? CH_UP * 8u : 0u; PINS(lost); }
            if constexpr (i == 5) { dn = wav >> 2; PINS(dn); pdur = min(pdur, dn + 8u); PINS(pdur); }
            if constexpr (i == 6) { pdur = (done & ok[M]) ? pdur : dn; PINS(pdur); }
            if constexpr (i == 7) { wav += pdur - dn; PINS(wav); }
            if constexpr (i == 8) { dh = hav >> 2; PINS(dh); nt = min(rd, dh + 8u); PINS(nt); }
            if constexpr (i == 9) { nt = rd < 0x3FFFu ? nt : dh; PINS(nt); }
            if constexpr (i == 10) { hav += nt - dh; PINS(hav); }
            if constexpr (i == 11) { mw = max(mw - 1u, CH_MW * 8u); PINS(mw); }
            if constexpr (i == 12) { mh = max(mh - 1u, CH_MH * 8u); PINS(mh); }
            if constexpr (i == 13) { rd = rd >> 14 == 1u ? lost : 0u; PINS(rd); }
            if constexpr (i == 14) { mw += rd; mh += lost - rd; PINS(mw); PINS(mh); }
            if constexpr (i == 15) { ok[M] = done ? 1u : (ok[M] & 1u) << 1; PINS(ok[M]); }
            if constexpr (i == 16) { dh = (2u * hav + mh) / 8u + CH_VIS; PINS(dh); }
            if constexpr (i == 17) { dn = (2u * wav + mw) / 8u + CH_VIS; PINS(dn); }
            if constexpr (i == 18) { nt = dw[L] + dh; PINS(nt); }
            if constexpr (i == 19) { dh += dn; dw[L] = dn; sn[M] += (int)done; PINS(dh); PINS(sn[M]); }
            if constexpr (i == 20) { rd = tn - t + (dh >> 1); PINS(rd); }
            if constexpr (i == 21) { rd = sn[M] + 1 < NSEG ? rd : 0u; PINS(rd); }
            if constexpr (i == 22) { nt = t + (u32)max((int)nt, (int)rd); PINS(nt); }
            if constexpr (i == 23) { hw.x = nt; hw.y = dh; PIN(hw.x); PIN(hw.y); }
            if constexpr (i == 24) { hw.z = nt + dn; hw.w = 0u; PIN(hw); }
            if constexpr (i == 25) st16(rs, hso + L * (MB / 4), hw);
        };
        if (__builtin_expect(sc + 1 < NSEG, 1)) {
            u32 vo = l16 + (L * (MB / 2) + (u32)(sc & 3) * 131072u), mp, hn;
            PIN(vo);
#define WT(k) do {} while (0)
            WT(0);
            v4u d[8];
            {
                u32 tmp;
                asm volatile(WAIT_TXT("%1", "%2") "buffer_load_dwordx4 %0, %3, %4, %5 offen sc1\n\ts_waitcnt lgkmcnt(0)"
                             : "=&v"(d[0]), "=&s"(tmp) : "s"(uu(t - 1u)), "v"(vo), "s"(srd), "s"(oo[0]) : "vcc", "scc", "memory");
            }
            SFOR(i, 7)
                asm volatile("buffer_load_dwordx4 %0, %1, %2, %3 offen sc1" : "=v"(d[i + 1]) : "v"(vo), "s"(srd), "s"(oo[i + 1]) : "memory");
                SCHED();
                if constexpr (i == 0) { done = uu(pst) ^ 1u; PINS(done); }
                static_for<3 * i, 3 * i + 3>(books);
                SCHED();
            SEND
            hn = ld4(rs, hvo[L]);
            SCHED();
            WT(1);
            static_for<21, 26>(books);
            za = done ? pmp : OOB;
            st4s(rs, za, 0, psent);
            *(LAS u32*)(sw + L * 4096) = zero;
            f32x4 acc[5] = {};
            mp = mpo + (L * (MB / 4) + (u32)((sc + 1) & 3) * 32768u); pmp = mpo + (L * (MB / 4) + (u32)(sc & 3) * 32768u);
            asm volatile("" : "+v"(acc[0]), "+v"(acc[1]), "+v"(acc[2]), "+v"(acc[3]), "+v"(acc[4]), "+v"(mp), "+v"(pmp));
            SCHED();
            WT(2);
            float f0[5], f1[5]; unsigned long long bad;
#define WSW(o_) asm volatile("v_permlane32_swap_b32 %0, %1" : "+v"(f0[o_]), "+v"(f1[o_]))
#define WAD(o_) asm volatile("v_add_f32_dpp %0, %1, %0 row_ror:8 row_mask:0xf bank_mask:0xf" : "+v"(f0[o_]) : "v"(f1[o_]))
#define WFO(o_) do { VADD(f0[o_], acc[o_][0], acc[o_][2]); VADD(f1[o_], acc[o_][1], acc[o_][3]); } while (0)
#define WWR(o_) *(LAS float*)(xw + L * 4096 + (o_) * 128) = f0[o_]
            asm volatile("s_waitcnt vmcnt(10)" : "+v"(d[0]));
            if (__builtin_expect(__builtin_amdgcn_ballot_w64(d[0].x == PSENT) != 0ull, 0)) {
                asm volatile("s_waitcnt vmcnt(3)" : "+v"(d[1]), "+v"(d[2]), "+v"(d[3]), "+v"(d[4]), "+v"(d[5]), "+v"(d[6]), "+v"(d[7]));
#pragma unroll
                for (int i = 0; i < 5; ++i) *(LAS u32*)(xw + L * 4096 + i * 128) = psent;
                *(LAS u32*)(sw + L * 4096) = 1u;
            } else {
            SFOR(i, 40)
                constexpr int pc = i < 30 ? i / 5 : 6 + (i & 1), oc = i < 30 ? i % 5 : (i - 30) >> 1;
                if constexpr (oc == 0 && pc > 0) asm volatile("s_waitcnt vmcnt(%1)" : "+v"(d[pc]) : "i"(10 - pc));
                if constexpr (pc * 5 + oc < 32) SMF_A(acc[oc], d[pc], wo[pc][oc]); else SMF_V(acc[oc], d[pc], wo[pc][oc]);
                if constexpr (i == 0) WT(3);
                if constexpr (i == 20) WT(4);
                if constexpr (i == 31 || i == 33) NOPS(1);
                if constexpr (i == 32) NOPS(0);
                if constexpr (i == 34) WFO(0);
                if constexpr (i == 35) { asm volatile("v_cmp_u_f32 %0, %1, %2" : "=s"(bad) : "v"(f0[0]), "v"(f1[0])); WSW(0); }
                if constexpr (i == 36) WFO(1);
                if constexpr (i == 37) { WAD(0); WSW(1); }
                if constexpr (i == 38) { WWR(0); SCHED(); WFO(2); }
                if constexpr (i == 39) { WAD(1); WSW(2); }
                SCHED();
            SEND
            WT(5);
            WWR(1); SCHED(); WFO(3); WAD(2); WWR(2); SCHED(); WSW(3);
            if (__builtin_expect(bad != 0ull, 0)) {
                bool miss = false;
#pragma unroll
                for (int i = 0; i < 8; ++i) miss |= d[i].x == PSENT;
                if (all_(!miss)) {
#pragma unroll
                    for (int i = 0; i < 8; ++i) miss |= d[i].y == PSENT || d[i].z == PSENT || d[i].w == PSENT;
                }
                *(LAS u32*)(sw + L * 4096) = all_(!miss) ? 0u : 1u;
            }
            WFO(4); WAD(3); WWR(3); SCHED();
            asm volatile("v_permlane32_swap_b32 %0, %1\n\ts_nop 1\n\tv_add_f32_dpp %0, %1, %0 row_ror:8 row_mask:0xf bank_mask:0xf" : "+v"(f0[4]), "+v"(f1[4]));
            WWR(4);
            }
            WT(6);
            f32x2 a, b, c, e; u32 cw, mq; v4u sv;
            asm volatile("s_waitcnt lgkmcnt(0)\n\ts_barrier\n\tds_read_b64 %0, %5 offset:%7\n\tds_read_b64 %1, %5 offset:%7+640\n\tds_read_b64 %2, %5 offset:%7+1280\n\tds_read_b64 %3, %5 offset:%7+1920\n\t"
                         "ds_read_b128 %4, %6 offset:%7+2560\n\t"
                         "s_waitcnt lgkmcnt(3)\n\tv_pk_add_f32 %0, %0, %1\n\ts_waitcnt lgkmcnt(1)\n\tv_pk_add_f32 %2, %2, %3\n\ts_waitcnt lgkmcnt(0)\n\tv_pk_add_f32 %0, %0, %2"
                         : "=&v"(a), "=&v"(b), "=&v"(c), "=&v"(e), "=&v"(sv) : "v"(xr), "v"(zero), "i"(L * 4096) : "memory");
            asm volatile("v_cmp_u_f32 vcc, %2, %3\n\tv_cvt_pk_bf16_f32 %0, %2, %3\n\tv_cndmask_b32 %1, %4, %5, vcc" : "=&v"(cw), "=&v"(mq) : "v"(a.x), "v"(a.y), "v"(mp), "v"(OOB) : "vcc");
            st4s(rs, mq, 0, cw);
            SCHED();
            now = rt32();
            PIN(hn); hv = hn;
            WT(7);
            pst = sv.x | sv.y | sv.z | sv.w;
            if (__builtin_expect(__builtin_amdgcn_ballot_w64(mq != mp && pst == 0u) != 0ull, 0)) st4s(rs, mp, 0, cw);
        } else {
            done = uu(pst) ^ 1u;
            st4s(rs, done ? pmp : OOB, 0, psent);
            static_for<0, 25>(books);
            pst = 1;
            hv = ld4(rs, hvo[L]);
            PIN(hv);
            now = rt32();
        }
        pdur = now - t;
        const u32 per = rdl(hv, 1), th = rdl(hv, 0);
        t = lead ? tn : (int)(th - now) < -(int)(per >> 1) ? th + per : th;
        tn = nt;
        dw[M] += max((int)(now + CH_WX - t), 0);
        stop = stop || rdl(hv, 3) != 0u;
    };
    for (int k = 0; k < 2 * SPIN_CAP && !stop && (sn[0] + 1 < NSEG || sn[1] + 1 < NSEG); ++k) { turn(IC<0>{}); turn(IC<1>{}); }
    if (sn[0] + 1 < NSEG || sn[1] + 1 < NSEG) st4s(rs, tid == 0 ? foff(F_ERR) : OOB, 0, 1u);
}

__global__ void __launch_bounds__(256, 1) k_main(Params p) {
    lptr lds = (lptr)(u32)16 - 16;
    if (blockIdx.x == 0) LST(256, rt32());
    unsigned long long pc; int len;
    asm volatile("s_getpc_b64 %0\n.Lk_main_here:\n\ts_mov_b32 %1, .Lk_end_here-.Lk_main_here" : "=s"(pc), "=s"(len));
    const rsrc_t rs = mk_rsrc(p.ws, W_END);
    const v2u e0 = ld8s(rs, threadIdx.x == 0 ? foff(F_EPOCH) : OOB, 0);
    const v4u code = __builtin_amdgcn_raw_buffer_load_b128(mk_rsrc((const void*)(pc & ~15ull), (u32)max(len, 0)), (((blockIdx.x >> 3) & 31u) * 256u + threadIdx.x) * 16u, 0, 0);
    CFENCE();
    if (threadIdx.x == 0) {
        GAS unsigned long long* slots = (GAS unsigned long long*)(p.ws + W_CTRL + F_SLOT * 128);
        const unsigned long long tag = (unsigned long long)e0.y << 32 | (e0.x + 1u);
        u32 role = ((u32)__builtin_amdgcn_s_getreg(20 | (3 << 11)) & 7u) * 32u + (blockIdx.x >> 3);
        for (int n = 0; n < 256 && __hip_atomic_exchange(slots + role * 16, tag, __ATOMIC_RELAXED, __HIP_MEMORY_SCOPE_AGENT) == tag; ++n) role = (role + 1u) & 255u;
        lds_w16(lds, (v4u){role, e0.x, e0.y, 0u});
    }
    __syncthreads();
    const v4u rv = lds_r16(lds);
    const int role = uni((int)rv.x), x = role >> 5, i = role & 31, w = uni(threadIdx.x >> 6);
    const v2u ep = {uu(rv.y), uu(rv.z)};
    asm volatile("" :: "v"(code));
    __syncthreads();
    if (x < 2) { role_head(p, lds, i >> 4, x * 16 + (i & 15), ep); __builtin_amdgcn_endpgm(); }
    if (x == 2) { role_wo(p, lds, i, ep); __builtin_amdgcn_endpgm(); }
    const int no = x < 5 ? 5 : 10, nr = x < 7 ? 5 : 4;
    if (i >= no + nr) { role_qkv(p, lds, (x < 5 ? (x - 3) * 22 : 44 + (x - 5) * 17) + i - no - nr, ep); __builtin_amdgcn_endpgm(); }
    const int og = x < 5 ? x - 3 : 2 * (x - 4) + (i >= 5), ng = i % 5, kq = og >> 1, L = og & 1, r = (x - 3) * 5 + i - no, q = r / 3;
    const int j = i < no ? kq * 5 + ng : r % 3 == 0 ? 20 + (q & 1) * 20 + (q >> 1) : 16 + (r % 3) * 8 + q;
    const bool cv = i >= no && r % 3 == 0;
    if (i >= no || L == 1) conv_stage(p, rs, lds, j, cv ? 8 + q : q, cv ? 40 : j >= 24 && j < 32 ? 8 : -8, ep);
    if (cv) __builtin_amdgcn_endpgm();
    if (i >= no) { role_row(p, lds, ((j - 24) ^ 8) * 4 + w, ep); __builtin_amdgcn_endpgm(); }
    role_out(p, lds, L, kq, ng, ep);
}
__global__ void k_end() { asm volatile(".Lk_end_here:"); }

extern "C" void kernel_launch(void* const* d_in, const int* in_sizes, int n_in, void* d_out, int out_size, void* d_ws, size_t ws_size,
                              hipStream_t stream) {
    Params p;
    p.x = (const u16*)d_in[0]; p.mem0 = (const u16*)d_in[1]; p.Wq = (const u16*)d_in[2]; p.Wk = (const u16*)d_in[3];
    p.Wv = (const u16*)d_in[4]; p.Wo = (const u16*)d_in[5];
    p.bk = (const u16*)d_in[8]; p.bv = (const u16*)d_in[9]; p.shift = (const u16*)d_in[10]; p.ssmk = (const u16*)d_in[11]; p.ssmD = (const u16*)d_in[12];
    p.out = (u16*)d_out;
    p.ws = (unsigned char*)(((size_t)d_ws + 2 * MB - 1) & ~(size_t)(2 * MB - 1));
    (void)hipFuncSetAttribute((const void*)k_main, hipFuncAttributeMaxDynamicSharedMemorySize, LDS_BYTES);
    hipLaunchKernelGGL(k_main, dim3(256), dim3(256), LDS_BYTES, stream, p);
}
```

```cpp
#include <hip/hip_runtime.h>
#include <hip/hip_bf16.h>

typedef unsigned short u16;
typedef unsigned int u32;
typedef u32 v2u __attribute__((ext_vector_type(2)));
typedef u32 v4u __attribute__((ext_vector_type(4)));
typedef u32 v8u __attribute__((ext_vector_type(8)));
typedef float f32x2 __attribute__((ext_vector_type(2)));
typedef float f32x4 __attribute__((ext_vector_type(4)));
typedef float f32x16 __attribute__((ext_vector_type(16)));
typedef __bf16 bf16x2 __attribute__((ext_vector_type(2)));
typedef __bf16 bf16x8 __attribute__((ext_vector_type(8)));
typedef short s16x4 __attribute__((ext_vector_type(4)));
#define LAS __attribute__((address_space(3)))
#define GAS __attribute__((address_space(1)))
typedef LAS unsigned char* lptr;
#define DEV __device__ __forceinline__

constexpr int SEQ = 2048, DM = 640, NH = 32, DH = 128, NSEG = 64, NQ = NH * DH;

constexpr u32 MB = 1u << 20;
constexpr u32 W_ORING = 0;
constexpr u32 W_MRING = MB;
constexpr u32 W_PROBE = 2 * MB - 65536;
constexpr u32 W_CTRL = 4 * MB;
constexpr u32 CTRL_BYTES = 128 * 1024;
constexpr u32 ACT = SEQ * DM * 2;
constexpr u32 W_H1 = 5 * MB, W_Y = W_H1 + ACT, W_H2 = W_Y + ACT, W_X2 = W_H2 + ACT;
constexpr u32 W_KQ = 16 * MB;
constexpr u32 W_VO = W_KQ + 64 * MB;
constexpr u32 W_P = W_VO + 64 * MB;
constexpr u32 P_ITEM = 32 * 4 * DM * 4;
constexpr u32 W_KVT = W_P + 2 * NSEG * P_ITEM;
constexpr u32 W_SP = W_KVT + ACT;
constexpr u32 W_END = W_KVT + 3 * MB;
static_assert((size_t)W_END + 2 * MB <= 256ull * MB, "workspace");
constexpr u32 F_ERR = 0, F_QKV = 16, F_O = F_QKV + 192, F_P = F_O + 64, F_H2 = F_P + 64, F_CLR = F_H2 + 32,
              F_POL = F_CLR, F_EPOCH = F_POL + 32, F_SLOT = F_EPOCH + 1, F_PRE = F_SLOT + 256, F_N = F_PRE + 192;
constexpr int NCV = 44, NPRE = 40;
static_assert(F_N * 128 <= CTRL_BYTES, "ctrl");
DEV u32 foff(u32 idx) { return W_CTRL + idx * 128u; }

constexpr u32 OOB = 0x80000000u;
constexpr u32 PSENT = 0x7F817F81u;
constexpr int SPIN_CAP = 40000;
constexpr int LDS_BYTES = 160 * 1024;
#define ST_HEAD 0
#define ST_WO 0

#define LS3(cond, slot, idx) do {} while (0)
#define LST(idx, v) do {} while (0)
#define LS2(cond, idx) do {} while (0)
struct Params {
    const u16 *x, *mem0, *Wq, *Wk, *Wv, *Wo, *bk, *bv, *shift, *ssmk, *ssmD;
    u16* out;
    unsigned char* ws;
};

typedef __amdgpu_buffer_rsrc_t rsrc_t;
DEV rsrc_t mk_rsrc(const void* p, u32 bytes) { return __builtin_amdgcn_make_buffer_rsrc((void*)p, 0, bytes, 0x00020000); }
DEV v4u ld16s(rsrc_t r, u32 off, u32 so) { return __builtin_amdgcn_raw_buffer_load_b128(r, off, so, 16); }
DEV v2u ld8s(rsrc_t r, u32 off, u32 so) { return __builtin_amdgcn_raw_buffer_load_b64(r, off, so, 16); }
DEV v4u ldin16(rsrc_t r, u32 off, u32 so) { return __builtin_amdgcn_raw_buffer_load_b128(r, off, so, 2); }
DEV v2u ldin8(rsrc_t r, u32 off, u32 so) { return __builtin_amdgcn_raw_buffer_load_b64(r, off, so, 2); }
DEV u32 ld4(rsrc_t r, u32 off) { return __builtin_amdgcn_raw_buffer_load_b32(r, off, 0, 16); }
DEV void st16(rsrc_t r, u32 off, v4u v) { __builtin_amdgcn_raw_buffer_store_b128(v, r, off, 0, 16); }
DEV void st8s(rsrc_t r, u32 off, u32 so, v2u v) { __builtin_amdgcn_raw_buffer_store_b64(v, r, off, so, 16); }
DEV void st4s(rsrc_t r, u32 off, u32 so, u32 v) { __builtin_amdgcn_raw_buffer_store_b32(v, r, off, so, 16); }
#define DRAIN() asm volatile("s_waitcnt vmcnt(0)" ::: "memory")
#define VMW(n) asm volatile("s_waitcnt vmcnt(%0)" :: "i"(n) : "memory")
#define BAR() asm volatile("s_waitcnt lgkmcnt(0)\n\ts_barrier" ::: "memory")
#define SCHED() __builtin_amdgcn_sched_barrier(0)
#define CFENCE() asm volatile("" ::: "memory")
#define NOPS(n) asm volatile("s_nop %0" :: "i"(n))
#define PIN(x) asm volatile("" : "+v"(x))
#define PINS(x) asm volatile("" : "+s"(x))

template <int V> struct IC { static constexpr int value = V; };
template <int I, int N, class F> DEV void static_for(F&& f) { if constexpr (I < N) { f(IC<I>{}); static_for<I + 1, N>(f); } }
#define SFOR(var, n) static_for<0, n>([&](auto ic_) __attribute__((always_inline)) { constexpr int var = decltype(ic_)::value;
#define SEND });

DEV u32 pk(float a, float b) { bf16x2 v = {(__bf16)a, (__bf16)b}; return __builtin_bit_cast(u32, v); }
DEV float lo_f(u32 u) { return __builtin_bit_cast(float, u << 16); }
DEV float hi_f(u32 u) { return __builtin_bit_cast(float, u & 0xffff0000u); }
DEV f32x2 up2(u32 u) { return (f32x2){lo_f(u), hi_f(u)}; }
DEV u32 pk2(f32x2 v) { return pk(v.x, v.y); }
DEV float rbf(float a) { return (float)(__bf16)a; }
DEV v4u lds_r16(lptr p) { return *(LAS v4u*)p; }
DEV void lds_w16(lptr p, v4u v) { *(LAS v4u*)p = v; }
DEV v2u lds_tr(lptr p) { return __builtin_bit_cast(v2u, __builtin_amdgcn_ds_read_tr16_b64_v4i16((LAS s16x4*)p)); }
DEV v4u tr2(lptr p0, lptr p1) { const v2u a = lds_tr(p0), b = lds_tr(p1); return (v4u){a.x, a.y, b.x, b.y}; }
DEV v8u cat8(v4u a, v4u b) { return __builtin_shufflevector(a, b, 0, 1, 2, 3, 4, 5, 6, 7); }
DEV f32x4 mfma16(v4u a, v4u b, f32x4 c) { return __builtin_amdgcn_mfma_f32_16x16x32_bf16(__builtin_bit_cast(bf16x8, a), __builtin_bit_cast(bf16x8, b), c, 0, 0, 0); }
DEV f32x16 mfma32(v4u a, v4u b, f32x16 c) { return __builtin_amdgcn_mfma_f32_32x32x16_bf16(__builtin_bit_cast(bf16x8, a), __builtin_bit_cast(bf16x8, b), c, 0, 0, 0); }

DEV float f_(u32 u) { return __builtin_bit_cast(float, u); }
DEV u32 u_(float f) { return __builtin_bit_cast(u32, f); }
template <int CTRL> DEV float dpp(float v) { return f_((u32)__builtin_amdgcn_update_dpp(0, (int)u_(v), CTRL, 0xf, 0xf, false)); }
DEV float max16(float v) { auto r = __builtin_amdgcn_permlane16_swap(u_(v), u_(v), false, false); return fmaxf(f_(r[0]), f_(r[1])); }
DEV float max32(float v) { auto r = __builtin_amdgcn_permlane32_swap(u_(v), u_(v), false, false); return fmaxf(f_(r[0]), f_(r[1])); }
DEV float add16(float v) { auto r = __builtin_amdgcn_permlane16_swap(u_(v), u_(v), false, false); return f_(r[0]) + f_(r[1]); }
DEV float add32(float v) { auto r = __builtin_amdgcn_permlane32_swap(u_(v), u_(v), false, false); return f_(r[0]) + f_(r[1]); }
DEV float wave_sum(float v) {
    v += dpp<0xB1>(v);
    v += dpp<0x4E>(v);
    v += dpp<0x124>(v);
    v += dpp<0x128>(v);
    return add32(add16(v));
}
DEV bool all_(bool c) { return __builtin_amdgcn_ballot_w64(!c) == 0ull; }
DEV int uni(int v) { return __builtin_amdgcn_readfirstlane(v); }
DEV u32 uu(u32 v) { return (u32)__builtin_amdgcn_readfirstlane((int)v); }
DEV u32 rdl(u32 v, int l) { return (u32)__builtin_amdgcn_readlane((int)v, l); }
DEV u32 rt32() { return (u32)__builtin_amdgcn_s_memrealtime(); }

DEV void wait_flags(rsrc_t rs, u32 off, u32 want, bool& dead) {
    if (dead) return;
#pragma unroll 1
    for (int n = 0;; ++n) {
        CFENCE();
        const u32 v = ld4(rs, off);
        if (all_(off >= OOB || v >= want)) break;
        __builtin_amdgcn_s_sleep(2);
        if ((n & 63) == 63) { if (uni((int)ld4(rs, foff(F_ERR))) != 0) { dead = true; break; } }
        if (n > SPIN_CAP) { st4s(rs, foff(F_ERR), 0, 1u); dead = true; break; }
    }
    CFENCE();
}

DEV void pre_post(rsrc_t rs, int idx, u32 stage, v2u ep) { st8s(rs, threadIdx.x == 0 ? foff(F_PRE + idx) : OOB, 0, (v2u){ep.x + stage, ep.y}); }
DEV void pre_poll(rsrc_t rs, int first, int n, u32 stage, v2u ep) {
    const int lane = threadIdx.x & 63;
#pragma unroll 1
    for (int it = 0; it < SPIN_CAP; ++it) {
        CFENCE();
        const v2u a = ld8s(rs, foff(F_PRE + first + min(lane, n - 1)), 0), b = ld8s(rs, foff(F_PRE + first + min(lane + 64, n - 1)), 0);
        if (all_(a.y == ep.y && b.y == ep.y && max(a.x - ep.x - stage, b.x - ep.x - stage) < 8u - stage)) break;
        __builtin_amdgcn_s_sleep(2);
    }
    CFENCE();
}
DEV void pre_wait(rsrc_t rs, int first, int n, u32 stage, v2u ep) {
    if (uni(threadIdx.x >> 6) == 0) pre_poll(rs, first, n, stage, ep);
    BAR();
}
DEV u32 get_pol(rsrc_t rs) {
    const int lane = threadIdx.x & 63;
    const u32 raw = ld4(rs, lane < 16 ? foff(F_POL + lane) : OOB);
    return wave_sum((float)(int)raw) >= 0.f ? 1u : 0u;
}
#define NPROBE 16
DEV void pol_probe(rsrc_t rs, int id) {
    const int lane = threadIdx.x & 63;
    const u32 off0 = W_PROBE + (u32)(id >> 2) * 16384u + (u32)(id & 3) * 128u;
    st4s(rs, lane < 32 ? off0 + (u32)lane * 512 : OOB, 0, 0u); DRAIN();
    int d[2] = {1 << 16, 1 << 16};
#pragma unroll 1
    for (int q = 0; q < NPROBE; ++q) {
        const u32 half = (u32)(q ^ (q >> 1) ^ id) & 1u;
        const unsigned long long t0 = __builtin_amdgcn_s_memtime();
        const u32 v = ld4(rs, lane == 0 ? off0 + half * 8192 + (u32)(q >> 1) * 512 : OOB);
        asm volatile("s_waitcnt vmcnt(0)" :: "v"(v) : "memory");
        const int dt = (int)(__builtin_amdgcn_s_memtime() - t0);
        if (half) d[1] = min(d[1], dt); else d[0] = min(d[0], dt);
    }
    const u32 xcc = (u32)__builtin_amdgcn_s_getreg(20 | (3 << 11)) & 0xfu;
    st4s(rs, lane == 0 ? foff(F_POL + id) : OOB, 0, (u32)min(max((d[0] - d[1]) * ((xcc & 4u) ? -1 : 1), -250), 250));
}
DEV f32x2 ln_stats(const f32x2 (&x)[5]) {
    const f32x2 s2 = (x[0] + x[1]) + (x[2] + x[3]) + x[4];
    const float mean = wave_sum(s2.x + s2.y) * (1.0f / DM);
    f32x2 q = {0.f, 0.f};
#pragma unroll
    for (int i = 0; i < 5; ++i) { const f32x2 d = x[i] - mean; q += d * d; }
    const float var = wave_sum(q.x + q.y) * (1.0f / DM);
    const float sd = rbf(__builtin_amdgcn_sqrtf(rbf(rbf(var) + 1e-5f)));
    return (f32x2){rbf(mean), __builtin_amdgcn_rcpf(sd)};
}
DEV void store_h(rsrc_t rs, u32 base, int row, const u32 (&hp)[5]) {
    const u32 lane = threadIdx.x & 63;
    const u32 sw = (u32)(row & 15) * 16u, o8 = (lane >> 5) * 8192u + (((lane & 31u) * 8u) ^ sw), hb = base + (u32)(row >> 5) * (32 * DM * 2) + (u32)(row & 31) * 256u;
    st8s(rs, o8, hb, (v2u){hp[0], hp[1]});
    st8s(rs, o8, hb + 16384, (v2u){hp[2], hp[3]});
    st4s(rs, (lane * 4) ^ sw, hb + 32768, hp[4]);
}

#define DMA16(ldsb, vo, so) asm volatile("s_mov_b32 m0, %0\n\ts_nop 0\n\tbuffer_load_dwordx4 %1, %2, %3 offen sc1 lds" :: "s"(ldsb), "v"(vo), "s"(srd), "s"(so) : "memory")
#define DMA16I(ldsb, lo, vo, so, go) do { u32 so_; asm volatile("s_add_u32 m0, %1, %5\n\ts_add_u32 %0, %4, %6\n\tbuffer_load_dwordx4 %2, %3, %0 offen sc1 lds" \
                         : "=&s"(so_) : "s"(ldsb), "v"(vo), "s"(srd), "s"(so), "i"(lo), "i"(go) : "memory", "scc"); } while (0)
#define DMA32(ldsb, vo, so) asm volatile("s_mov_b32 m0, %0\n\ts_nop 0\n\tbuffer_load_dwordx4 %1, %2, %3 offen sc1 lds\n\tbuffer_load_dwordx4 %1, %2, %3 offen offset:1024 sc1 lds" \
                         :: "s"(ldsb), "v"(vo), "s"(srd), "s"(so) : "memory")
#define DMAIN(sr, ldsb, vo, so) asm volatile("s_mov_b32 m0, %0\n\ts_nop 0\n\tbuffer_load_dwordx4 %1, %2, %3 offen lds" :: "s"(ldsb), "v"(vo), "s"(sr), "s"(so) : "memory")
DEV v4u mk_srd(const void* q, u32 bytes) { return (v4u){uu((u32)(size_t)q), uu((u32)((size_t)q >> 32) & 0xffffu), bytes, 0x00020000u}; }
#define DMANT(sr, ldsb, vo, so) asm volatile("s_mov_b32 m0, %0\n\ts_nop 0\n\tbuffer_load_dwordx4 %1, %2, %3 offen nt lds" :: "s"(ldsb), "v"(vo), "s"(sr), "s"(so) : "memory")
#define MK_SRD() const v4u srd = mk_srd(p.ws, W_END)

constexpr int CV_W = 40960;
template <bool B> struct CvL {
    static constexpr int NF = B ? 5 : 3, NK = B ? 4 : 2, KP = 544 + NK * 256, CH = NF * 1024 + 4 * KP;
};
constexpr int CV_Y = 4 * CvL<false>::CH, CV_YP = 272;
static_assert(CV_Y + 32 * CV_YP <= CV_W && 2 * CvL<true>::CH <= CV_Y, "conv lds");
DEV int cv_chan(int u, int i) { return 16 * (u % 40) + 4 * i + u / 40; }
template <bool B> DEV void cv_filt(const Params& p, u32 dst, int d) {
    const u32 lane = threadIdx.x & 63;
    const v4u sf = mk_srd(p.ssmk + d * SEQ, SEQ * 2);
    DMANT(sf, dst, lane < 4u ? OOB : lane * 16u - 64u, 0);
#pragma unroll
    for (int i = 1; i < CvL<B>::NF; ++i) DMANT(sf, uu(dst + i * 1024), i == CvL<B>::NF - 1 && lane >= 4u ? OOB : lane * 16u, i * 1024 - 64);
}
template <bool B> DEV void cv_kvld(rsrc_t rs, int d, v4u* k) {
#pragma unroll
    for (int i = 0; i < CvL<B>::NK; ++i) k[i] = ld16s(rs, (threadIdx.x & 63) * 16u, uu(W_KVT + (u32)d * (SEQ * 2) + i * 1024));
}
template <bool B> DEV void cv_kvput(lptr F, const v4u* k) {
    const int lane = threadIdx.x & 63;
#pragma unroll
    for (int i = 0; i < CvL<B>::NK; ++i) lds_w16(F + CvL<B>::NF * 1024 + (lane & 3) * CvL<B>::KP + 512 + i * 256 + (lane >> 2) * 16, k[i]);
}
template <bool B> DEV void cv_zero(lptr F) {
    const int lane = threadIdx.x & 63;
#pragma unroll
    for (int i = 0; i < 4; ++i) lds_w16(F + (lane >> 5) * CvL<B>::CH + CvL<B>::NF * 1024 + i * CvL<B>::KP + (lane & 31) * 16, (v4u){0u, 0u, 0u, 0u});
}
#define M32V(d, a, b) asm volatile("v_mfma_f32_32x32x16_bf16 %0, %1, %2, %0" : "+v"(d) : "v"(a), "v"(b))
template <bool B> DEV void cv_run(const Params& p, rsrc_t rs, lptr F, int d, u32 D, f32x4 sp, float (&y)[16]) {
    constexpr int KP = CvL<B>::KP, FO = B ? 2048 : 0;
    const int lane = threadIdx.x & 63, n = lane & 31, hh = lane >> 5, c = lane & 3;
    lptr fa = F + 64 + (32 * hh + 16 * (c & 1) + 8 * ((lane >> 4) & 1) + 4 * (c >> 1) - (lane & 12)) * 2;
    lptr kl = F + CvL<B>::NF * 1024 + (32 + n - hh) * 16;
    v4u dg;
#pragma unroll
    for (int q = 0; q < 4; ++q) { const int k = (lane & 35) ? -1 : (n & 4) + (n >> 3); dg[q] = k == 2 * q ? D : k == 2 * q + 1 ? D << 16 : 0u; }
    v4u l[4], h[4]; v2u fc[4], fh[4];
    f32x16 acc[4];
#pragma unroll
    for (int j = 0; j < 4; ++j) { l[j] = lds_r16(kl + j * KP); if constexpr (B) h[j] = lds_r16(kl + j * KP + 512); }
#pragma unroll
    for (int j = 0; j < 2; ++j) { fc[j] = lds_tr(fa + FO - 32 * j); if constexpr (B) fh[j] = lds_tr(fa - 32 * j); }
    asm volatile("s_nop 1" : "+v"(dg));
#pragma unroll
    for (int j = 0; j < 4; ++j) asm volatile("v_mfma_f32_32x32x16_bf16 %0, %1, %2, 0" : "=&v"(acc[j]) : "v"(dg), "v"(B ? h[j] : l[j]));
    constexpr int U = B ? 2 : 4;
    kl -= 32 * U; fa -= 32;
#pragma unroll 1
    for (int m = 0; m < 32; m += 2 * U) {
#pragma unroll
        for (int z = 0; z < U; ++z) {
            const int c = 2 * (z & 1);
            const v4u f = __builtin_shufflevector(fc[c], fc[c + 1], 0, 1, 2, 3), g = __builtin_shufflevector(fh[c], fh[c + 1], 0, 1, 2, 3);
#pragma unroll
            for (int j = 0; j < 4; ++j) {
                M32V(acc[j], f, l[j]); l[j] = lds_r16(kl + j * KP + 32 * (U - z - 1));
                if (j < 2) fc[(c ^ 2) + j] = lds_tr(fa + FO + (z + 1) * 128 + 32 - 32 * j);
                SCHED();
            }
            if constexpr (B) {
#pragma unroll
                for (int j = 0; j < 4; ++j) {
                    M32V(acc[j], g, h[j]); h[j] = lds_r16(kl + j * KP + 512 + 32 * (U - z - 1));
                    if (j < 2) fh[(c ^ 2) + j] = lds_tr(fa + (z + 1) * 128 + 32 - 32 * j);
                    SCHED();
                }
            }
        }
        kl -= 32 * U; fa += 128 * U;
    }
    asm volatile("s_nop 11" : "+v"(acc[0]), "+v"(acc[1]), "+v"(acc[2]), "+v"(acc[3]));
    const float o[3] = {acc[1][15] + acc[2][14] + acc[3][13], acc[2][15] + acc[3][14], acc[3][15]};
    if constexpr (!B) st16(rs, lane == 63 ? W_SP + (u32)d * 16u : OOB, (v4u){u_(o[0]), u_(o[1]), u_(o[2]), 0u});
#pragma unroll
    for (int i = 0; i < 16; ++i) {
        float v = acc[0][i];
#pragma unroll
        for (int j = 1; j < 4; ++j) if (j <= i) v += acc[j][i - j];
        if (i < 3) { const float e = f_((u32)__builtin_amdgcn_ds_bpermute((hh ? lane - 32 : lane + 31) * 4, (int)u_(o[i]))); v += lane == 0 ? sp[i] : e; }
        y[i] = v;
    }
}
DEV void cv_put(lptr F, int pr, const float (&y0)[16], const float (&y1)[16]) {
    const int lane = threadIdx.x & 63;
#pragma unroll
    for (int i = 0; i < 16; ++i) *(LAS u32*)(F + CV_Y + (lane & 31) * CV_YP + (lane >> 5) * 128 + i * 8 + pr * 4) = pk(y0[i], y1[i]);
}
DEV void cv_out(rsrc_t rs, lptr F, int u, int blk0) {
    const int lane = threadIdx.x & 63;
    lptr q = F + CV_Y + ((lane >> 2) & 3) * CV_YP + ((lane >> 4) * 4 + (lane & 3)) * 8;
    const u32 yo = W_Y + (u32)(((lane >> 2) * 8 + blk0) * DM + 4 * u + (lane & 3)) * 16u;
#pragma unroll
    for (int i = 0; i < 8; ++i) { lptr a = q + (i >> 1) * (8 * CV_YP) + (i & 1) * 128; st16(rs, yo + (u32)((i & 1) * 128 + (i >> 1)) * (DM * 16), tr2(a, a + 4 * CV_YP)); }
}
struct KvIn { v2u k[19], v[16]; v4u s[2]; };
DEV void kvt_load(const Params& p, int tile, KvIn& in) {
    const int lane = threadIdx.x & 63;
    const rsrc_t rk = mk_rsrc(p.bk, ACT), rv = mk_rsrc(p.bv, ACT), rh = mk_rsrc(p.shift, DM * 8);
    const int row = (tile / 10) * 64 + (lane >> 4) * 16, col = (tile % 10) * 64 + (lane & 15) * 4;
    const u32 vo = (u32)(row * DM + col) * 2u, vp = row > 0 ? vo - 3 * (DM * 2) : OOB;
#pragma unroll
    for (int i = 0; i < 2; ++i) in.s[i] = ldin16(rh, (u32)col * 8u + i * 16, 0);
#pragma unroll
    for (int i = 0; i < 19; ++i) { in.k[i] = i < 3 ? ldin8(rk, vp, i * (DM * 2)) : ldin8(rk, vo, (i - 3) * (DM * 2)); if (i >= 3) in.v[i - 3] = ldin8(rv, vo, (i - 3) * (DM * 2)); }
}
DEV void kvt_make(rsrc_t rs, lptr xt, int tile, const KvIn& in) {
    const int lane = threadIdx.x & 63;
#pragma unroll
    for (int c = 0; c < 2; ++c) {
        const v4u sv = in.s[c];
        const f32x2 sh[4] = {{lo_f(sv.x), lo_f(sv.z)}, {hi_f(sv.x), hi_f(sv.z)}, {lo_f(sv.y), lo_f(sv.w)}, {hi_f(sv.y), hi_f(sv.w)}};
        f32x2 kf[19];
#pragma unroll
        for (int i = 0; i < 19; ++i) kf[i] = up2(in.k[i][c]);
        u32 kv[16];
#pragma unroll
        for (int i = 0; i < 16; ++i) kv[i] = pk2(((kf[i] * sh[3] + kf[i + 1] * sh[2]) + (kf[i + 2] * sh[1] + kf[i + 3] * sh[0])) * up2(in.v[i][c]));
#pragma unroll
        for (int q = 0; q < 8; ++q) {
            const u32 sel = (q & 1) ? 0x07060302u : 0x05040100u; const u32* z = kv + (q >> 1);
            *(LAS v2u*)(xt + ((2 * c + (q & 1)) * 16 + (lane & 15)) * 144 + (lane >> 5) * 64 + (q >> 1) * 16 + ((lane >> 4) & 1) * 8) =
                (v2u){__builtin_amdgcn_perm(z[4], z[0], sel), __builtin_amdgcn_perm(z[12], z[8], sel)};
        }
    }
    const u32 o = W_KVT + (u32)(((tile % 10) * 64 + 4 * (lane >> 3)) * SEQ + (tile / 10) * 64 + (lane & 7) * 8) * 2u;
#pragma unroll
    for (int i = 0; i < 8; ++i) st16(rs, o + (u32)(32 * (i & 1) + (i >> 1)) * (SEQ * 2), lds_r16(xt + (8 * i + (lane >> 3)) * 144 + (lane & 7) * 16));
}
struct RowX { v2u a, b; u32 c; };
DEV void ln_row(rsrc_t rs, int row, const RowX& r) {
    const f32x2 x[5] = {up2(r.a.x), up2(r.a.y), up2(r.b.x), up2(r.b.y), up2(r.c)};
    const f32x2 st = ln_stats(x);
    u32 hp[5];
#pragma unroll
    for (int q = 0; q < 5; ++q) hp[q] = pk2(up2(pk2(x[q] - st.x)) * st.y);
    store_h(rs, W_H1, row, hp);
}
DEV void pre_rings(const Params& p, rsrc_t rs, int j) {
    const u32 gi = (u32)(j * 256 + threadIdx.x), gt = gi * 16u, R = NPRE * 4096, n = ((gt >> 14) << 9) + ((gt & 8191u) >> 4);
    v4u m = {PSENT, PSENT, PSENT, PSENT};
    if (n < 640u) {
        m = *(const v4u*)(p.mem0 + (2 * ((n >> 2) & 3) + (n & 1)) * DM + (n >> 4) * 16 + (n & 2) * 4);
#pragma unroll
        for (int i = 0; i < 4; ++i) m[i] = m[i] == PSENT ? 0x7FC17FC1u : m[i];
    }
    st4s(rs, gi - 36u * 256u < F_CLR ? foff(gi - 36u * 256u) : OOB, 0, 0u);
#pragma unroll 1
    for (u32 o = gt; o < MB; o += R) st16(rs, W_ORING + o, (v4u){PSENT, PSENT, PSENT, PSENT});
    const u32 mo = gt < 4 * 32768 + 16384 ? W_MRING + gt : OOB;
    st16(rs, mo, m);
    st16(rs, mo + MB / 4, m);
    static_assert(NPRE * 4096 >= 4 * 32768 + 16384, "memory ring");
}
constexpr int LN0 = NPRE * 4;
DEV void conv_stage(const Params& p, rsrc_t rs, lptr lds, int j, int g0, int ge, v2u ep) {
    const int tid = threadIdx.x, lane = tid & 63, w = uni(tid >> 6), u = j * 4 + w;
    lptr F = lds + w * CV_W;
    const u32 fb = uu(w * CV_W);
    const rsrc_t rd = mk_rsrc(p.ssmD, DM * 2);
    float y0[16], y1[16];
    if (j < NPRE) {
        constexpr int CH = CvL<false>::CH;
        const rsrc_t rx = mk_rsrc(p.x, ACT);
        KvIn in;
        RowX r;
        r.a = ldin8(rx, lane * 8u, uu(u * (DM * 2))); r.b = ldin8(rx, lane * 8u + 512u, uu(u * (DM * 2)));
        r.c = __builtin_amdgcn_raw_buffer_load_b32(rx, lane * 4u + 1024u, uu(u * (DM * 2)), 2);
        kvt_load(p, u, in);
        pre_rings(p, rs, j);
        ln_row(rs, u, r);
        DRAIN(); BAR();
        pre_post(rs, j, 1u, ep);
        if (j == 20) LST(260, rt32());
        const u32 D = __builtin_amdgcn_raw_buffer_load_b16(rd, (u32)cv_chan(u, lane & 3) * 2u, 0, 0);
#pragma unroll 1
        for (int i = 0; i < 4; ++i) cv_filt<false>(p, fb + i * CH, cv_chan(u, i));
        kvt_make(rs, F + CV_Y, u, in);
        cv_zero<false>(F); cv_zero<false>(F + 2 * CH);
        DRAIN(); BAR();
        pre_post(rs, j, 2u, ep);
        if (j == 20) LST(262, rt32());
        pre_wait(rs, 0, NPRE, 2u, ep);
        if (j == 20) LST(281, rt32());
        {
            v4u k[8];
#pragma unroll
            for (int i = 0; i < 4; ++i) cv_kvld<false>(rs, cv_chan(u, i), k + 2 * i);
#pragma unroll
            for (int i = 0; i < 4; ++i) cv_kvput<false>(F + i * CH, k + 2 * i);
        }
        CFENCE();
        kvt_load(p, u + LN0, in);
        if (j == 20) LST(286, rt32());
#pragma unroll 1
        for (int i = 0; i < 4; i += 2) {
            cv_run<false>(p, rs, F + i * CH, cv_chan(u, i), rdl(D, i), f32x4{}, y0);
            cv_run<false>(p, rs, F + (i + 1) * CH, cv_chan(u, i + 1), rdl(D, i + 1), f32x4{}, y1);
            cv_put(F, i >> 1, y0, y1);
        }
        if (j == 20) LST(287, rt32());
        cv_out(rs, F, u, 0);
        DRAIN(); BAR();
        pre_post(rs, j, 3u, ep);
        if (j == 20) LST(263, rt32());
        kvt_make(rs, F + CV_Y, u + LN0, in);
    } else pol_probe(rs, u - NPRE * 4);
    DRAIN(); BAR();
    if (g0 < ge) {
        constexpr int CH = CvL<true>::CH;
        pre_post(rs, j, 4u, ep);
        cv_zero<true>(F);
        pre_wait(rs, 0, NPRE, 4u, ep);
        v4u k[4];
        auto fetch = [&](u32 dst, int d) { cv_filt<true>(p, dst, d); cv_kvld<true>(rs, d, k); };
        fetch(fb, cv_chan(g0 * 4 + w, 0));
#pragma unroll 1
        for (int g = g0; g < ge; g += 8) {
            const int un = g * 4 + w;
            const u32 ch = (u32)cv_chan(un, lane & 3), D = __builtin_amdgcn_raw_buffer_load_b16(rd, ch * 2u, 0, 0);
            const v4u sp = ld16s(rs, W_SP + ch * 16u, 0);
            if (g == 16) { LST(282, rt32()); LST(288, __builtin_amdgcn_s_memtime()); }
#pragma unroll 1
            for (int i = 0; i < 4; i += 2) {
                cv_kvput<true>(F, k);
                fetch(fb + CH, cv_chan(un, i + 1));
                cv_run<true>(p, rs, F, cv_chan(un, i), rdl(D, i), (f32x4){f_(rdl(sp.x, i)), f_(rdl(sp.y, i)), f_(rdl(sp.z, i)), 0.f}, y0);
                cv_kvput<true>(F + CH, k);
                fetch(fb, i == 0 ? cv_chan(un, 2) : cv_chan(min(un + 32, 159), 0));
                cv_run<true>(p, rs, F + CH, cv_chan(un, i + 1), rdl(D, i + 1), (f32x4){f_(rdl(sp.x, i + 1)), f_(rdl(sp.y, i + 1)), f_(rdl(sp.z, i + 1)), 0.f}, y1);
                cv_put(F, i >> 1, y0, y1);
            }
            cv_out(rs, F, un, 4);
            if (g == 16) { LST(283, rt32()); LST(289, __builtin_amdgcn_s_memtime()); }
        }
        DRAIN(); BAR();
    }
    pre_post(rs, j, 5u, ep);
    if (j == 20) LST(284, rt32());
    if (j == 3) LST(285, rt32());
}

#define M32A(first, d, a, b) do { if (first) asm volatile("v_mfma_f32_32x32x16_bf16 %0, %1, %2, 0" : "=&v"(d) : "a"(a), "v"(b)); \
                                  else asm volatile("v_mfma_f32_32x32x16_bf16 %0, %1, %2, %0" : "+v"(d) : "a"(a), "v"(b)); } while (0)
#define M32B(first, d, a, b) do { if (first) asm volatile("v_mfma_f32_32x32x16_bf16 %0, %1, %2, 0" : "=&v"(d) : "v"(a), "a"(b)); \
                                  else asm volatile("v_mfma_f32_32x32x16_bf16 %0, %1, %2, %0" : "+v"(d) : "v"(a), "a"(b)); } while (0)
#define VCVT(d, a, b) asm volatile("v_cvt_pk_bf16_f32 %0, %1, %2" : "=v"(d) : "v"(a), "v"(b))
#define VADD(d, a, b) asm volatile("v_add_f32 %0, %1, %2" : "=v"(d) : "v"(a), "v"(b))
#define LD4A(d, vo, so) asm volatile("buffer_load_dword %0, %1, %2, %3 offen sc1" : "=v"(d) : "v"(vo), "s"(srd), "s"(so) : "memory")
DEV u32 wst_src(int lane, int ld) { return (u32)((lane >> 4) * ld + 8 * ((lane & 15) ^ ((lane >> 4) << 2))) * 2u; }
DEV u32 wst_rd(int lane, int w) { const int q4 = (lane >> 2) & 3; return (u32)(q4 * 256 + ((w ^ q4) * 64) + ((lane >> 4) & 1) * 32 + (lane & 3) * 8); }

constexpr int QK_SEL = 81920;
#define QK_LEAD 12
#define QK_SMP 56
#define QK_W0 8
DEV void role_qkv(const Params& p, lptr lds, int c, v2u ep) {
    const int tid = threadIdx.x, lane = tid & 63, w = uni(tid >> 6), hh = lane >> 5, r32 = lane & 31;
    const int m = c >> 5, h = c & 31;
    const rsrc_t rs = mk_rsrc(p.ws, W_END);
    v4u wt[40];
    {
        const v4u sw = mk_srd(m == 0 ? p.Wq : m == 1 ? p.Wk : p.Wv, DM * NQ * 2), sx = mk_srd(p.x, ACT);
        const u32 vo = wst_src(lane, NQ) + (u32)h * (DH * 2), x8 = (u32)lane * 8u, x4 = (u32)lane * 4u;
        RowX xr[5];
        auto wfill = [&](int i0, int n) {
#pragma unroll 1
            for (int i = i0; i < i0 + n; ++i) DMAIN(sw, uu((4 * i + w) * 1024), vo, uu((16 * i + 4 * w) * (NQ * 2)));
        };
        wfill(0, QK_W0);
#pragma unroll
        for (int i = 0; i < 5; ++i)
            asm volatile("buffer_load_dwordx2 %0, %3, %5, %6 offen nt\n\tbuffer_load_dwordx2 %1, %3, %5, %6 offen offset:512 nt\n\tbuffer_load_dword %2, %4, %5, %6 offen offset:1024 nt"
                         : "=&v"(xr[i].a), "=&v"(xr[i].b), "=&v"(xr[i].c) : "v"(x8), "v"(x4), "s"(sx), "s"(uu((LN0 + 4 * c + w + 384 * i) * (DM * 2))) : "memory");
        wfill(QK_W0, 40 - QK_W0);
        if (c == 3) LST(275, rt32());
        VMW(40 - QK_W0);
        if (c == 3) LST(276, rt32());
#pragma unroll
        for (int i = 0; i < 5; ++i) {
            asm volatile("" : "+v"(xr[i].a), "+v"(xr[i].b), "+v"(xr[i].c));
            const int row = LN0 + 4 * c + w + 384 * i;
            if (i < 4 || row < SEQ) ln_row(rs, row, xr[i]);
        }
        if (c == 3) LST(264, rt32());
        VMW(12); BAR();
        lptr tb = lds + hh * 2048 + wst_rd(lane, w);
        SFOR(k, 40)
            lptr q = tb + (k >> 3) * 32768 + (k & 7) * 4096; wt[k] = tr2(q, q + 1024); asm volatile("" : "+a"(wt[k]));
        SEND
        DRAIN(); BAR();
        pre_post(rs, 96 + c, 1u, ep);
    }
    if (c == 3) LST(268, rt32());
    bool dead = false;
    int n0, n1;
    const u32 t16 = (u32)tid * 16u, wb = uu(w * 1024);
    u32 ar[16];
#pragma unroll
    for (int j = 0; j < 16; ++j) ar[j] = (u32)(r32 * 256 + ((2 * (j & 7) + hh) ^ (r32 & 15)) * 16 + (j >> 3) * 49152);
    MK_SRD();
#define XRD(y, ks) lds_r16(lds + ar[((ks) & 7) + ((ks) >= 24 ? 8 : 0)] + ((ks) >> 3) % 3 * 16384 + (y) * 8192)
#define QDMA(y, i, vo, so) DMA16I(wb, ((i) >> 1) * 16384 + ((i) & 1) * 4096 + (y) * 8192, vo, so, (i) * 4096)
    const u32 h2flag = (lane < 32 && w == 0) ? foff(F_H2 + lane) : OOB;
    const u32 ol = (m == 2 ? (u32)(2 * w + (r32 >> 4)) * 1024u : (u32)((r32 >> 4) * 4 + w) * 1024u) + (u32)(hh * 16 + (r32 & 15)) * 16u
                 + (m == 2 ? W_VO : m == 1 ? W_KQ : W_KQ + 8192u) + (u32)h * 16384u;
    const u32 fo = tid < 2 ? foff(F_QKV + tid * 96 + c) : OOB;
    lptr selw = lds + QK_SEL + (w == 0 ? 0 : 64), selr = lds + QK_SEL;
    auto late = [&](u32 fv) { const u32 need = n1 >= NSEG ? 0xFFFFFFFFu : dead ? 0u : (u32)n1 + 1u; return (u32)__builtin_amdgcn_ballot_w64(fv < need); };
    static_assert(W_H2 - W_H1 == 2 * 64 * (32 * DM * 2), "h");
    pre_wait(rs, 0, NPRE, 1u, ep);
    if (c == 3) LST(269, rt32());
    auto run = [&](auto isv) __attribute__((always_inline)) {
        constexpr bool V = decltype(isv)::value;
        f32x16 ax, ay = {};
        v4u x[8], pp, pq;
        u32 oy = OOB, lt = 1, fcnt = 0, fa = OOB;
        u32 nn = 0;
        PIN(lt); PIN(nn);
#pragma unroll
        for (int z = 0; z < 5; ++z) x[z] = XRD(1, 20 + z);
#define QPK(r_, a1, a_, q_) do { if constexpr (V) VCVT(r_[q_], a1[4 * (a_) + 8 * ((q_) >> 1) + 2 * ((q_) & 1)], a1[4 * (a_) + 8 * ((q_) >> 1) + 2 * ((q_) & 1) + 1]); \
                                 else VCVT(r_[q_], a1[4 * (a_) + (q_)], a1[4 * (a_) + (q_) + 8]); } while (0)
#define QPK4(r_, a1, a_) do { QPK(r_, a1, a_, 0); QPK(r_, a1, a_, 1); QPK(r_, a1, a_, 2); QPK(r_, a1, a_, 3); } while (0)
        for (;;) {
            u32 ls = uu(lt);
            n0 = uni((int)nn); n1 = n0 >> 8; n0 &= 255;
            if (ls != 0u && n0 >= min(NSEG, n1 + QK_LEAD)) {
                CFENCE();
                u32 fv = ld4(rs, h2flag);
                asm volatile("s_waitcnt vmcnt(0)" : "+v"(fv) :: "memory");
                *(LAS u32*)(selw + 16) = late(fv);
                BAR();
                st4s(rs, fa, 0, fcnt);
                fa = OOB;
                if (n1 >= NSEG) __builtin_amdgcn_endpgm();
                if (uu(*(LAS u32*)(selr + 16)) != 0u) {
                    if (w == 0) wait_flags(rs, h2flag, (u32)n1 + 1, dead);
                    BAR();
                }
                ls = 0u;
            }
            if (n0 == 4) pre_wait(rs, 96, 96, 1u, ep);
            int cur = n0;
            asm volatile("s_cmp_eq_u32 %3, 0\n\ts_cselect_b32 %0, %4, %0\n\ts_addc_u32 %2, %2, 0\n\ts_cmp_lg_u32 %3, 0\n\ts_addc_u32 %1, %1, 0"
                         : "+s"(cur), "+s"(n0), "+s"(n1) : "s"(ls), "s"(n1 | 64) : "scc");
            u32 xs = W_H1 + (u32)(cur + (cur & 64)) * (32 * DM * 2), ys, yvo, ox, fv;
            PINS(xs);
#define QT(k) do {} while (0)
            QT(0);
            SCHED();
            QDMA(0, 0, t16, xs); QDMA(0, 1, t16, xs);
            SFOR(z, 80)
                constexpr int ks = (z + 20) % 40, zn = (z + 5) % 80;
                if constexpr (z >= 20 && z < 60) { if constexpr (V) M32B(ks == 0, ax, x[z & 7], wt[ks]); else M32A(ks == 0, ax, wt[ks], x[z & 7]); }
                else { if constexpr (V) M32B(ks == 0, ay, x[z & 7], wt[ks]); else M32A(ks == 0, ay, wt[ks], x[z & 7]); }
                x[(z + 5) & 7] = XRD(zn < 20 || zn >= 60, (zn + 20) % 40);
                if constexpr (z == 77) lt = *(LAS u32*)selr;
                SCHED();
                if constexpr (z < 8) QDMA(0, z + 2, t16, xs);
                if constexpr (z == 9) { ys = W_H1 + (u32)n0 * (32 * DM * 2); yvo = t16 | (n0 < NSEG ? 0u : OOB); PINS(ys); PIN(yvo); }
                if constexpr (z == 11) { ox = ol + (u32)cur * (NH * 16384u); PIN(ox); }
                if constexpr (z == 14) { QT(1); asm volatile("s_waitcnt vmcnt(6) lgkmcnt(0)\n\ts_barrier" ::: "memory"); QT(2); }
                if constexpr (z == 15) st4s(rs, fa, 0, fcnt);
                if constexpr (z >= 16 && z < 26) QDMA(1, z - 16, yvo, ys);
                if constexpr (z == 26) QPK4(pp, ay, 0);
                if constexpr (z == 27) st16(rs, oy, pp);
                if constexpr (z == 28) QPK4(pq, ay, 1);
                if constexpr (z == 29) st16(rs, oy + 512, pq);
                if constexpr (z == 30) { QT(3); asm volatile("s_waitcnt vmcnt(13)\n\ts_barrier" ::: "memory"); QT(4); }
                if constexpr (z == 32) { fcnt = lane == 0 ? (u32)n0 : (u32)n1; fa = fo; asm volatile("" : "+v"(fcnt), "+v"(fa)); }
                if constexpr (z == 34) { oy = (ol + (u32)n0 * (NH * 16384u)) | (yvo & OOB); PIN(oy); }
                if constexpr (z == 35) { nn = (u32)(min(n0 + 1, NSEG) | n1 << 8); PIN(nn); }
                if constexpr (z == 54) { asm volatile("s_waitcnt vmcnt(2) lgkmcnt(0)\n\ts_barrier" ::: "memory"); QT(5); }
                if constexpr (z == QK_SMP) LD4A(fv, h2flag, 0);
                if constexpr (z == 62) QPK4(pp, ax, 0);
                if constexpr (z == 63) st16(rs, ox, pp);
                if constexpr (z == 64) QPK4(pq, ax, 1);
                if constexpr (z == 65) st16(rs, ox + 512, pq);
                if constexpr (z == 73) { asm volatile("s_waitcnt vmcnt(2)" : "+v"(fv) :: "memory"); QT(6); ls = late(fv); PINS(ls); }
                if constexpr (z == 74) { lt = ls; PIN(lt); *(LAS u32*)selw = lt; }
                if constexpr (z == 76) asm volatile("s_waitcnt lgkmcnt(2)\n\ts_barrier" ::: "memory");
                SCHED();
            SEND
        }
    };
    if (m == 2) run(IC<1>{}); else run(IC<0>{});
}

#define OU_SMP 44
DEV void role_out(const Params& p, lptr lds, int L, int kq, int ng, v2u ep) {
    const int tid = threadIdx.x, lane = tid & 63, w = uni(tid >> 6), hh = lane >> 5, r32 = lane & 31;
    const rsrc_t rs = mk_rsrc(p.ws, W_END);
    v4u wo[64];
    bool dead = false;
    {
        const v4u sw = mk_srd(p.Wo, DM * NQ * 2);
        const u32 vo = wst_src(lane, DM) + (u32)ng * 256u;
        auto fill = [&](int b) {
#pragma unroll 1
            for (int i = 0; i < 8; ++i) DMAIN(sw, uu((b % 5) * 32768 + (4 * i + w) * 1024), vo, uu((kq * 1024 + b * 128 + 16 * i + 4 * w) * (DM * 2)));
        };
#pragma unroll 1
        for (int b = 0; b < 5; ++b) fill(b);
        lptr tb = lds + hh * 1024 + wst_rd(lane, w);
        SFOR(b, 8)
            VMW(b < 4 ? 32 : 56 - 8 * b); BAR();
            lptr q = tb + (b % 5) * 32768;
#pragma unroll
            for (int k = 0; k < 8; ++k) { lptr a = q + (k >> 1) * 8192 + (k & 1) * 2048; wo[8 * b + k] = tr2(a, a + 4096); if (b < 7) asm volatile("" : "+a"(wo[8 * b + k])); else PIN(wo[8 * b + k]); }
            if constexpr (b < 3) { BAR(); fill(b + 5); }
        SEND
    }
    pre_wait(rs, 0, NPRE, 1u, ep);
    if (kq == 0 && ng == 0) LST(272 + L, rt32());
    MK_SRD();
    const u32 ofl = lane < 8 ? foff(F_O + L * 32 + kq * 8 + lane) : OOB, l16 = (u32)lane * 16u;
    const u32 fo = (tid == 0) ? foff(F_P + L * 32 + kq * 5 + ng) : OOB;
    const u32 src0 = uu(W_VO + 8192u + (u32)(L * NSEG * NH + kq * 8) * 16384u + w * 1024);
#define ODMA(i, dst, so) DMA16I(dst, (i) * 4096, l16, so, ((i) >> 1) * 16384 + ((i) & 1) * 4096)
    auto first = [&](int s) { const u32 dst = uu((s & 1) * 65536 + w * 1024), src = src0 + (u32)s * (NH * 16384u); SFOR(i, 10) ODMA(i, dst, src); SEND };
    u32 pa = W_P + (u32)(L * NSEG) * P_ITEM + (u32)(4 * hh * 4 + kq) * (DM * 4) + (u32)(ng * 128 + w * 32 + r32) * 4u, fv = 0;
    bool early = false;
    for (int s = 0; s < NSEG; ++s) {
        const u32 dst = uu((s & 1) * 65536 + w * 1024), src = src0 + (u32)s * (NH * 16384u);
        lptr A = lds + (s & 1) * 65536 + l16;
        if (!early) {
            if (!all_(ofl == OOB || fv >= (u32)s + 1)) wait_flags(rs, ofl, (u32)s + 1, dead);
            LS3(L == 0 && w == 1, 1, kq * 5 + ng);
            first(s);
        }
        asm volatile("s_waitcnt vmcnt(8)\n\ts_barrier" ::: "memory");
        v4u o[64];
#pragma unroll
        for (int k = 0; k < 3; ++k) o[k] = lds_r16(A + k * 1024);
        f32x16 acc;
        SCHED();
        SFOR(k, 64)
            if constexpr (k < 56) M32B(k == 0, acc, o[k], wo[k]); else M32V(acc, o[k], wo[k]);
            if constexpr (k < 61) o[k + 3] = lds_r16(A + (k + 3) * 1024);
            SCHED();
            if constexpr (k < 12 && (k & 1) == 0) ODMA(10 + k / 2, dst, src);
            if constexpr (k == 4) asm volatile("s_waitcnt vmcnt(7)\n\ts_barrier" ::: "memory");
            if constexpr (k == 20) asm volatile("s_waitcnt vmcnt(5)\n\ts_barrier" ::: "memory");
            if constexpr (k == 40) asm volatile("s_waitcnt vmcnt(0)\n\ts_barrier" ::: "memory");
            if constexpr (k == OU_SMP) LD4A(fv, ofl, 0);
            SCHED();
        SEND
        asm volatile("s_nop 9" : "+v"(acc), "+v"(pa));
#pragma unroll
        for (int i = 0; i < 16; ++i) st4s(rs, pa, (u32)((8 * (i >> 2) + (i & 3)) * (4 * DM * 4)), u_(acc[i]));
        asm volatile("s_waitcnt vmcnt(16)" : "+v"(fv) :: "memory");
        LS3(L == 0 && w == 1, 2, kq * 5 + ng);
        early = s + 1 < NSEG && all_(ofl == OOB || fv >= (u32)s + 2);
        if (early) { first(s + 1); VMW(10); }
        else { fv = ld4(rs, ofl); asm volatile("s_waitcnt vmcnt(0)" : "+v"(fv) :: "memory"); }
        BAR();
        st4s(rs, fo, 0, (u32)s + 1);
        pa += P_ITEM;
        LS2(L == 0 && w == 1, 160 + kq * 5 + ng);
    }
}

constexpr int RW_P = 20480, RW_X = RW_P + 10240;
DEV void role_row(const Params& p, lptr lds, int id, v2u ep) {
    const int lane = threadIdx.x & 63, w = uni(threadIdx.x >> 6);
    const int ri = id & 31, L = id >> 5;
    const rsrc_t rs = mk_rsrc(p.ws, W_END);
    bool dead = false, have = false;
    const u32 fp = lane < 20 ? foff(F_P + L * 32 + lane) : OOB, l16 = (u32)lane * 16u, lx = lane < 16 ? l16 + 1024u : OOB, fb = uu(w * CV_W);
    lptr F = lds + w * CV_W;
    MK_SRD();
    const v4u sx = mk_srd(p.x, ACT);
    auto yfill = [&](int b) {
#pragma unroll 1
        for (int i = 0; i < 5; ++i) DMA32(uu(fb + (b & 1) * 10240 + i * 2048), l16, uu(W_Y + (u32)(ri * 8 + b) * 10240u + i * 2048));
    };
    auto xfill = [&](int s) {
        const u32 d = uu(fb + RW_X + (s & 1) * 2048), o = uu((u32)(s * 32 + ri) * (DM * 2));
        if (L == 0) { DMANT(sx, d, l16, o); DMANT(sx, d + 1024, lx, o); } else { DMA16(d, l16, o + W_X2); DMA16(d + 1024, lx, o + W_X2); }
    };
    auto pfill = [&](int s) {
        const u32 o = uu(W_P + (u32)(L * NSEG + s) * P_ITEM + (u32)ri * (4 * DM * 4));
        SFOR(i, 10) DMA16I(fb, RW_P + i * 1024, l16, o, i * 1024); SEND
        if (L == 1) xfill(s);
    };
    if (L == 0) xfill(0);
    pre_poll(rs, 0, NPRE, 3u, ep);
    if (id == 0) LST(274, rt32());
    yfill(0); DRAIN();
    u32 fv = 0;
    for (int s = 0; s < NSEG; ++s) {
        if (!have) {
            if (!all_(fp == OOB || fv >= (u32)s + 1)) wait_flags(rs, fp, (u32)s + 1, dead);
            LS3(L == 0, 3, ri);
            pfill(s);
        }
        LS2(L == 0, 192 + ri);
        f32x2 y[5];
        {
            lptr ya = F + ((s >> 3) & 1) * 10240 + (s & 7) * 2 + lane * 16;
            auto yr = [&](lptr q) { return lo_f(*(LAS u16*)q); };
            y[0] = (f32x2){yr(ya), yr(ya + 2560)}; y[1] = (f32x2){yr(ya + 5120), yr(ya + 7680)};
            y[2] = (f32x2){yr(ya + 1024), yr(ya + 3584)}; y[3] = (f32x2){yr(ya + 6144), yr(ya + 8704)};
            lptr yc = ya - lane * 16 + 2048 + (lane >> 1) * 16 + (lane & 1) * 5120;
            y[4] = (f32x2){yr(yc), yr(yc + 2560)};
            asm volatile("" : "+v"(y[0]), "+v"(y[1]), "+v"(y[2]), "+v"(y[3]), "+v"(y[4]));
        }
        if (!have) DRAIN();
        LS3(L == 0, 4, ri);
        f32x2 o[5], x1[5];
#pragma unroll
        for (int kq = 0; kq < 4; ++kq) {
            lptr q = F + RW_P + kq * (DM * 4);
            const f32x4 a = *(LAS f32x4*)(q + l16), b = *(LAS f32x4*)(q + 1024 + l16); const f32x2 c = *(LAS f32x2*)(q + 2048 + lane * 8);
            if (kq == 0) { o[0] = a.lo; o[1] = a.hi; o[2] = b.lo; o[3] = b.hi; o[4] = c; }
            else { o[0] += a.lo; o[1] += a.hi; o[2] += b.lo; o[3] += b.hi; o[4] += c; }
        }
        {
            lptr q = F + RW_X + (s & 1) * 2048;
            const v2u xa = *(LAS v2u*)(q + lane * 8), xb = *(LAS v2u*)(q + 512 + lane * 8);
            const u32 xi[5] = {xa.x, xa.y, xb.x, xb.y, *(LAS u32*)(q + 1024 + lane * 4)};
#pragma unroll
            for (int i = 0; i < 5; ++i) x1[i] = up2(pk2(up2(xi[i]) + up2(pk2(o[i]))));
            asm volatile("" : "+v"(x1[0]), "+v"(x1[1]), "+v"(x1[2]), "+v"(x1[3]), "+v"(x1[4]));
        }
        have = s + 1 < NSEG && all_(fp == OOB || fv >= (u32)s + 2);
        if (__builtin_expect(have, 0)) pfill(s + 1);
        if (L == 0) xfill(s + 1);
        const f32x2 st1 = ln_stats(x1);
        u32 xp[5];
#pragma unroll
        for (int i = 0; i < 5; ++i) xp[i] = pk2(x1[i] + ((x1[i] - st1.x) * st1.y) * y[i]);
        const int row = s * 32 + ri;
        if (L == 0) {
            const u32 ro = (u32)row * (DM * 2);
            f32x2 x2[5];
#pragma unroll
            for (int i = 0; i < 5; ++i) x2[i] = up2(xp[i]);
            st8s(rs, (u32)lane * 8, W_X2 + ro, (v2u){xp[0], xp[1]});
            st8s(rs, (u32)lane * 8 + 512, W_X2 + ro, (v2u){xp[2], xp[3]});
            st4s(rs, (u32)lane * 4 + 1024, W_X2 + ro, xp[4]);
            const f32x2 st2 = ln_stats(x2);
            u32 hp[5];
#pragma unroll
            for (int i = 0; i < 5; ++i) hp[i] = pk2((x2[i] - st2.x) * st2.y);
            store_h(rs, W_H2, row, hp);
        } else {
            u16* o_ = p.out + (size_t)row * DM;
            *(v2u*)(o_ + 4 * lane) = (v2u){xp[0], xp[1]};
            *(v2u*)(o_ + 256 + 4 * lane) = (v2u){xp[2], xp[3]};
            *(u32*)(o_ + 512 + 2 * lane) = xp[4];
        }
        CFENCE();
        LS3(L == 0, 5, ri);
        fv = ld4(rs, fp);
        asm volatile("s_waitcnt vmcnt(0)" : "+v"(fv) :: "memory");
        st4s(rs, lane == 0 && L == 0 ? foff(F_H2 + ri) : OOB, 0, (u32)s + 1);
        if ((s & 7) == 3 && s < NSEG - 8) {
            if (s == 27) pre_poll(rs, 0, NCV, 5u, ep);
            if (s == 43) st8s(rs, (id == 32 && lane == 0) ? foff(F_EPOCH) : OOB, 0, (v2u){ep.x + 8u, ep.y});
            yfill((s >> 3) + 1);
        }
        if (ri == 0) LST(320 + L * 64 + s, rt32());
        LS2(L == 0, 224 + ri);
    }
}

constexpr float SCALE = 0.08838834764831845f, LOG2E = 1.4426950408889634f;
constexpr int C_K = 0, C_V = 8192, C_QX = 16384, C_MEM = 24576, C_Q = C_MEM + 10240, C_KM = C_Q + 2304, C_VM = C_KM + 2304, C_DUMP = C_VM + 2048,
              C_ZERO = C_DUMP + 1024, C_WL = C_ZERO + 128;
constexpr int NLF = 14;
static_assert(C_WL % 16 == 0 && C_WL + 4 * NLF * 2048 <= LDS_BYTES, "head lds");
#define CH_VIS 10
#define CH_H0 124
#define CH_W0 88
#define CH_MH 4
#define CH_MW 4
#define CH_REC 20
#define CH_UP 4
#define CH_WX 6
DEV void wait_until(u32 t) {
    unsigned long long c;
    asm volatile("1:\n\ts_memrealtime %0\n\ts_waitcnt lgkmcnt(0)\n\ts_mov_b64 vcc, %0\n\ts_sub_u32 vcc_lo, %1, vcc_lo\n\ts_cmpk_lt_u32 vcc_lo, 400\n\ts_cbranch_scc1 1b" : "=&s"(c) : "s"(uu(t - 1u)) : "vcc", "scc", "memory");
}
#define WAIT_TXT(tmp, t1) "s_memrealtime vcc\n1:\n\ts_waitcnt lgkmcnt(0)\n\ts_sub_u32 " tmp ", " t1 ", vcc_lo\n\ts_memrealtime vcc\n\ts_cmpk_lt_u32 " tmp ", 400\n\ts_cbranch_scc1 1b\n\t"
#define SMF_A(acc, a, b) asm volatile("v_smfmac_f32_16x16x64_bf16 %0, %1, %2, %3" : "+v"(acc) : "v"(a), "a"(b), "v"(idx))
#define SMF_V(acc, a, b) asm volatile("v_smfmac_f32_16x16x64_bf16 %0, %1, %2, %3" : "+v"(acc) : "v"(a), "v"(b), "v"(idx))
#define MF0(d, a, b) asm volatile("v_mfma_f32_16x16x32_bf16 %0, %1, %2, 0" : "=&v"(d) : "v"(a), "v"(b))
#define MF1(d, a, b) asm volatile("v_mfma_f32_16x16x32_bf16 %0, %1, %2, %0" : "+v"(d) : "v"(a), "v"(b))
#define MF0H(d, a, b) asm volatile("v_mfma_f32_16x16x16_bf16 %0, %1, %2, 0" : "=&v"(d) : "v"(a), "v"(b))
#define MF1H(d, a, b) asm volatile("v_mfma_f32_16x16x16_bf16 %0, %1, %2, %0" : "+v"(d) : "v"(a), "v"(b))
DEV v8u tr_frag(lptr p0, lptr p1, lptr p2, lptr p3) { return cat8(tr2(p0, p1), tr2(p2, p3)); }
constexpr int qslot(int ks) { return (ks & 1) && ks < 8 ? ks >> 1 : -1; }
constexpr int kslot(int ks) { return ks < 4 ? 4 + ks : -1; }
constexpr int vslot(int ks) { return ks >= 4 ? 4 + ks : -1; }
DEV u32 ring_map(u32 off, u32 half) { return ((off >> 13) << 14) + (half << 13) + (off & 8191u); }
#define ZRD(x, i) x = __builtin_bit_cast(f32x4, lds_r16(ZP + 16 * (i)))
#define VMAX(d, a, b) asm volatile("v_max_f32 %0, %1, %2" : "=v"(d) : "v"(a), "v"(b))
#define VMAX3(d, a, b, c_) asm volatile("v_max3_f32 %0, %1, %2, %3" : "=v"(d) : "v"(a), "v"(b), "v"(c_))
#define VEXP(d, a) asm volatile("v_exp_f32 %0, %1" : "=v"(d) : "v"(a))
#define VFMC(d, a, b) asm volatile("v_fmamk_f32 %0, %1, 0x3e0293ee, %2" : "=v"(d) : "v"(a), "v"(b))
#define SEL(d, a, b) asm volatile("v_cndmask_b32 %0, %1, %2, %3" : "=v"(d) : "v"(a), "v"(b), "s"(himask))
#define QPERM(d, a, q) asm volatile("v_mov_b32_dpp %0, %1 quad_perm:" q " row_mask:0xf bank_mask:0xf" : "=v"(d) : "v"(a))

DEV void role_head(const Params& p, lptr lds, int L, int h, v2u ep) {
    const int tid = threadIdx.x, lane = tid & 63, w = uni(tid >> 6), g = lane >> 4, r = lane & 15;
    const rsrc_t rs = mk_rsrc(p.ws, W_END);
    v8u wq0[10], wq1[10], wk0[10], wk1[10], wv0[10], wv1[10];
    lptr WL = lds + C_WL + w * (NLF * 2048) + lane * 16;
    {
        constexpr int GS = 1056, CS = 48 * GS;
        const v4u sq = mk_srd(p.Wq, DM * NQ * 2), sk = mk_srd(p.Wk, DM * NQ * 2), sv = mk_srd(p.Wv, DM * NQ * 2);
        const u32 vo = (u32)(g * NQ + h * DH + 8 * (r ^ ((g & 1) << 3))) * 2u;
        auto fill = [&](int ch) {
#pragma unroll 1
            for (int i = 0; i < 4; ++i) {
                const u32 dst = uu((ch % 3) * CS + (4 * i + w) * GS), so = uu((ch * 64 + 16 * i + 4 * w) * (NQ * 2));
                DMAIN(sq, dst, vo, so); DMAIN(sk, dst + 16 * GS, vo, so); DMAIN(sv, dst + 32 * GS, vo, so);
            }
        };
        __builtin_amdgcn_s_sleep(ST_HEAD);
        fill(0); fill(1); fill(2);
        const int q4 = r >> 2;
        lptr tb = lds + (4 * (g >> 1) + 2 * (q4 >> 1) + (g & 1)) * GS + (q4 & 1) * 256 + ((w * 64 + (r & 3) * 8) ^ ((q4 & 1) << 7));
        SFOR(ch, 10)
            VMW(ch < 8 ? 24 : 108 - 12 * ch);
            BAR();
            auto frag = [&](int m, int t) { lptr b = tb + (ch % 3) * CS + m * 16 * GS + t * 32; return tr_frag(b, b + 512, b + 8 * GS, b + 8 * GS + 512); };
            wq0[ch] = frag(0, 0); wq1[ch] = frag(0, 1); wk0[ch] = frag(1, 0); wk1[ch] = frag(1, 1); wv0[ch] = frag(2, 0); wv1[ch] = frag(2, 1);
            asm volatile("" : "+a"(wq0[ch]), "+a"(wk0[ch]), "+v"(wv0[ch]), "+v"(wv1[ch]));
            if constexpr (qslot(ch) < 0) asm volatile("" : "+a"(wq1[ch])); else asm volatile("" : "+v"(wq1[ch]));
            if constexpr (kslot(ch) < 0) asm volatile("" : "+a"(wk1[ch])); else asm volatile("" : "+v"(wk1[ch]));
            BAR();
            if constexpr (ch < 7) fill(ch + 3);
        SEND
#pragma unroll
        for (int ch = 0; ch < 10; ++ch) {
            if (qslot(ch) >= 0) { lds_w16(WL + qslot(ch) * 2048, wq1[ch].lo); lds_w16(WL + qslot(ch) * 2048 + 1024, wq1[ch].hi); }
            if (kslot(ch) >= 0) { lds_w16(WL + kslot(ch) * 2048, wk1[ch].lo); lds_w16(WL + kslot(ch) * 2048 + 1024, wk1[ch].hi); }
            if (vslot(ch) >= 0) { lds_w16(WL + vslot(ch) * 2048, wv1[ch].lo); lds_w16(WL + vslot(ch) * 2048 + 1024, wv1[ch].hi); }
        }
        CFENCE();
    }
    const u32 idx = (r & 2) ? 0xEEEEu : 0x4444u;
    const unsigned long long himask = 0xCCCCCCCCCCCCCCCCull;
    bool dead = false;
    if (h == 3) LST(266 + L, rt32());
    pre_wait(rs, 0, NCV, 1u, ep);
    const u32 pol0 = uu(get_pol(rs));
    const u32 l16 = (u32)lane * 16u;
    const u32 mring = W_MRING + (u32)L * (MB / 4) + pol0 * 8192u, hint = mring + 4 * 32768u;
    const u32 fkh = lane < 3 ? foff(F_QKV + L * 96 + lane * 32 + h) : lane < 6 ? hint + (u32)(lane - 3) * 4u : OOB;
    u32 tm = 0;
    MK_SRD();
    u32 ksrc = uu(W_KQ + (u32)(L * NSEG * NH + h) * 16384u + w * 2048), vsrc = ksrc + (W_VO - W_KQ);
    u32 dk = uu(C_K + w * 2048), dv = dk + (C_V - C_K), dq = dk + (C_QX - C_K), dm = uu(C_MEM + w * 2048), dm2 = uu(w < 2 ? C_MEM + 8192 + w * 1024 : C_DUMP);
    asm volatile("" : "+s"(dk), "+s"(dv), "+s"(dq), "+s"(dm), "+s"(dm2));
    wait_flags(rs, lane < 3 ? fkh : OOB, 1u, dead);
    DMA32(dk, l16, ksrc); DMA32(dv, l16, vsrc); DMA32(dq, l16, ksrc ^ 8192u);
    lptr qw = lds + C_Q + (2 * g) * 272 + (32 * w + 2 * r) * 2;
    lptr qr = lds + C_Q + (2 * (r >> 2) + (r & 1)) * 272 + g * 16;
    lptr LB = lds + l16, VX = lds + C_V + w * 2048 + l16;
    if (tid >= 64 && tid < 70) { const u32 z = tid == 68 ? 0x3F803F80u : tid == 69 ? PSENT : 0u; lds_w16(lds + C_ZERO + (tid - 64) * 16, (v4u){z, z, z, z}); }
    u32 zaddr = 0; PIN(zaddr);
    lptr ZP = lds + zaddr + C_ZERO;
    const float CEXP = SCALE * LOG2E;
#define LF_RD(slot, hf) lf[slot][hf] = lds_r16(WL + (slot) * 2048 + (hf) * 1024)
    BAR();
    const u32 ofl = tid == 0 ? foff(F_O + L * 32 + h) : OOB;
    u32 oso = (g & 1) ? OOB : W_ORING + (u32)L * (MB / 2) + ring_map((u32)((r >> 3) * 32768 + h * 1024 + w * 256 + (g >> 1) * 128 + (r & 7) * 16), pol0);
    unsigned long long vis[8];
#pragma unroll
    for (int i = 0; i < 8; ++i) vis[i] = __builtin_amdgcn_ballot_w64(16 * (i >> 2) + 4 * g + (i & 3) <= 16 * (w & 1) + r);
    u32 missed = 0;
    for (int s = 0; s < NSEG;) {
        u32 ms = mring + (u32)(s & 3) * 32768u + w * 2048, ms2 = mring + (u32)(s & 3) * 32768u + 16384u + (w & 1) * 1024, sv = (u32)s;
        asm volatile("" : "+s"(ms), "+s"(ms2)); PIN(sv);
        f32x4 aq0, aq1, ak0, ak1;
        v4u lf[NLF][2];
        ZRD(aq0, 0); ZRD(aq1, 1); ZRD(ak0, 2); ZRD(ak1, 3);
#pragma unroll
        for (int i = 0; i < 4; ++i) { LF_RD(i, 0); LF_RD(i, 1); }
        SCHED();
        v4u A[10];
#define HT(k) do {} while (0)
        v4u kx[8], qf[4], kmf[4], vx[2], ones, P;
        float fa, fb, fc, fd, mx, my, sx[4], t[4], tm0, tm1; u32 pa, pb, fl, e01, e23, pmm; unsigned long long nanq;
        f32x4 av0, av1, s0, s1, sm, oa, ob, rsum;
        v2u vm, pm;
#define LW(sl) cat8(lf[sl][0], lf[sl][1])
#define PQ(ks, tl) do { if constexpr (!(tl)) SMF_A(aq0, A[ks], wq0[ks]); else if constexpr (qslot(ks) < 0) SMF_A(aq1, A[ks], wq1[ks]); else { const v8u lw_ = LW(qslot(ks)); SMF_V(aq1, A[ks], lw_); } } while (0)
#define PK(ks, tl) do { if constexpr (!(tl)) SMF_A(ak0, A[ks], wk0[ks]); else if constexpr (kslot(ks) < 0) SMF_A(ak1, A[ks], wk1[ks]); else { const v8u lw_ = LW(kslot(ks)); SMF_V(ak1, A[ks], lw_); } } while (0)
#define PV(ks, tl) do { if constexpr (!(tl)) SMF_V(av0, A[ks], wv0[ks]); else if constexpr (vslot(ks) < 0) SMF_V(av1, A[ks], wv1[ks]); else { const v8u lw_ = LW(vslot(ks)); SMF_V(av1, A[ks], lw_); } } while (0)
#define KXR(i) kx[i] = lds_r16(LB + C_K + ((i) & 1) * 4096 + ((i) >> 1) * 1024)
#define XW(q_) do { *(LAS u32*)(q_) = pa; *(LAS u32*)((q_) + 272) = pb; } while (0)
        auto slot = [&](auto zc) __attribute__((always_inline)) {
            constexpr int z = decltype(zc)::value;
            if constexpr (z < 14) PQ(z >> 1, z & 1);
            else if constexpr (z < 17) PQ(z - 7, 0);
            else if constexpr (z < 20) PQ(z - 10, 1);
            else if constexpr (z < 33) PK((z - 20) >> 1, z & 1);
            else if constexpr (z < 35) { if constexpr (z == 33) MF0(s0, kx[0], qf[0]); else MF0(s1, kx[1], qf[0]); }
            else if constexpr (z < 41) { if constexpr (z & 1) MF1(s0, kx[z - 33], qf[(z - 33) >> 1]); else MF1(s1, kx[z - 33], qf[(z - 33) >> 1]); }
            else if constexpr (z == 41) PK(6, 1);
            else if constexpr (z < 45) PK(z - 35, 0);
            else if constexpr (z < 48) PK(z - 38, 1);
            else if constexpr (z < 58) PV(z - 48, 0);
            else if constexpr (z < 61) PV(z < 60 ? z - 54 : 0, 1);
            else if constexpr (z == 61) MF0(sm, kmf[0], qf[0]);
            else if constexpr (z < 65) MF1(sm, kmf[z - 61], qf[z - 61]);
            else if constexpr (z == 65) MF0(oa, vx[0], P);
            else if constexpr (z == 66) MF0(ob, vx[1], P);
            else PV(6, 1);
            if constexpr (z < 6) A[4 + z] = lds_r16(LB + C_MEM + (4 + z) * 1024);
            if constexpr (z == 6 || z == 7) LF_RD(4, z - 6);
            if constexpr (z == 8 || z == 9) LF_RD(5, z - 8);
            if constexpr (z == 12 || z == 13) LF_RD(6, z - 12);
            if constexpr (z == 16 || z == 17) KXR(z - 16);
            if constexpr (z == 18 || z == 19) LF_RD(7, z - 18);
            if constexpr (z >= 20 && z < 24) KXR(z - 18);
            if constexpr (z == 32 || z == 33) KXR(z - 26);
            if constexpr (z == 24) XW(qw);
            if constexpr (z == 25) ZRD(av0, 0);
            if constexpr (z == 26) ZRD(av1, 1);
            if constexpr (z == 27) asm volatile("s_waitcnt lgkmcnt(2)\n\ts_barrier" ::: "memory");
            if constexpr (z >= 28 && z < 32) qf[z - 28] = lds_r16(qr + (z - 28) * 64);
            if constexpr (z == 35) fl = ld4(rs, fkh);
            if constexpr (z == 52) XW(qw + (C_KM - C_Q));
            if constexpr (z == 53 || z == 54) vx[z - 53] = lds_r16(VX + (z - 53) * 1024);
            if constexpr (z == 55) asm volatile("s_waitcnt lgkmcnt(2)\n\ts_barrier" ::: "memory");
            if constexpr (z >= 56 && z < 60) kmf[z - 56] = lds_r16(qr + (C_KM - C_Q) + (z - 56) * 64);
            if constexpr (z >= 48 && z < 52) LF_RD(8 + ((z - 48) >> 1), z & 1);
            if constexpr (z >= 60) LF_RD(10 + ((z - 60) >> 1), z & 1);
            SCHED();
            if constexpr (z == 19) HT(2);
            if constexpr (z == 32) HT(3);
            if constexpr (z == 47) HT(4);
            if constexpr (z == 64) HT(5);
            if constexpr (z == 20) VADD(fa, aq0[0], aq0[2]);
            if constexpr (z == 21) VADD(fb, aq0[1], aq0[3]);
            if constexpr (z == 22) { VADD(fc, aq1[0], aq1[2]); VADD(fd, aq1[1], aq1[3]); }
            if constexpr (z == 23) { asm volatile("v_cmp_u_f32 %0, %1, %2" : "=s"(nanq) : "v"(fa), "v"(fb)); VCVT(pa, fa, fc); VCVT(pb, fb, fd); }
            if constexpr (z == 39 || z == 40 || z == 41) NOPS(1);
            if constexpr (z == 42) VMAX3(mx, s0[0], s0[1], s0[2]);
            if constexpr (z == 43) { VMAX3(mx, mx, s0[3], s1[0]); VMAX3(my, s1[1], s1[2], s1[3]); }
            if constexpr (z == 44 || z == 46) { VMAX(fa, mx, my); VMAX(my, mx, my); mx = fa; }
            if constexpr (z == 45) { SEL(sx[0], s0[0], s1[0]); asm volatile("v_permlane16_swap_b32 %0, %1" : "+v"(mx), "+v"(my)); }
            if constexpr (z == 47) { SEL(sx[1], s0[1], s1[1]); asm volatile("v_permlane32_swap_b32 %0, %1" : "+v"(mx), "+v"(my)); }
            if constexpr (z == 48) asm volatile("v_max_f32 %0, %0, %1\n\tv_mul_f32 %0, 0xbe0293ee, %0" : "+v"(mx) : "v"(my));
            if constexpr (z == 49) { VADD(fa, ak0[0], ak0[2]); VADD(fb, ak0[1], ak0[3]); }
            if constexpr (z == 50) { VADD(fc, ak1[0], ak1[2]); VADD(fd, ak1[1], ak1[3]); }
            if constexpr (z == 51) { VCVT(pa, fa, fc); VCVT(pb, fb, fd); }
            if constexpr (z == 52) SEL(sx[2], s0[2], s1[2]);
            if constexpr (z == 53) { SEL(sx[3], s0[3], s1[3]); VFMC(t[0], sx[0], mx); }
            if constexpr (z == 54) { VFMC(t[1], sx[1], mx); VFMC(t[2], sx[2], mx); }
            if constexpr (z == 55) VFMC(t[3], sx[3], mx);
            if constexpr (z >= 56 && z < 60) VEXP(t[z - 56], t[z - 56]);
            if constexpr (z == 60) { VCVT(e01, t[0], t[1]); VCVT(e23, t[2], t[3]); }
            if constexpr (z == 61) { QPERM(P.x, e01, "[0,1,0,1]"); QPERM(P.z, e01, "[2,3,2,3]"); }
            if constexpr (z == 62) { QPERM(P.y, e23, "[0,1,0,1]"); QPERM(P.w, e23, "[2,3,2,3]"); }
            if constexpr (z == 63) { VADD(fa, av0[0], av0[2]); VADD(fb, av0[1], av0[3]); }
            if constexpr (z == 64) VCVT(vm.x, fa, fb);
            if constexpr (z == 67) { asm volatile("" :: "v"(sm)); VFMC(tm0, sm[0], mx); VFMC(tm1, sm[1], mx); }
            SCHED();
        };
        {
            u32 tmp;
            asm volatile("s_mov_b32 m0, %2\n\t" WAIT_TXT("%0", "%1") "buffer_load_dwordx4 %4, %5, %6 offen sc1 lds\n\tbuffer_load_dwordx4 %4, %5, %6 offen offset:1024 sc1 lds\n\t"
                         "s_mov_b32 m0, %3\n\ts_nop 0\n\tbuffer_load_dwordx4 %4, %5, %7 offen sc1 lds\n\ts_waitcnt vmcnt(0) lgkmcnt(0)\n\ts_barrier"
                         : "=&s"(tmp) : "s"(uu(tm - 1u)), "s"(dm), "s"(dm2), "v"(l16), "s"(srd), "s"(ms), "s"(ms2) : "vcc", "scc", "memory");
        }
#pragma unroll
        for (int ks = 0; ks < 4; ++ks) A[ks] = lds_r16(LB + C_MEM + ks * 1024);
        SCHED();
        st4s(rs, ofl, 0, sv);
        SCHED();
        static_for<0, 24>(slot);
        if (__builtin_expect(nanq != 0ull, 0)) {
            bool miss = false;
#pragma unroll
            for (int i = 0; i < 10; ++i) miss |= A[i].x == PSENT || A[i].y == PSENT || A[i].z == PSENT || A[i].w == PSENT;
            nanq = missed < SPIN_CAP ? __builtin_amdgcn_ballot_w64(miss) : 0ull;
        }
        if (__builtin_expect(nanq == 0ull, 1)) {
        static_for<24, 68>(slot);
        PV(7, 1); ones = lds_r16(ZP + 64); SCHED();
        VMAX(fa, tm0, tm1);
        if (__builtin_expect(__builtin_amdgcn_ballot_w64(fa > 24.0f) != 0ull, 0)) {
            const float up = fmaxf(max32(max16(fmaxf(tm0, tm1))), 0.f);
            tm0 -= up; tm1 -= up;
#pragma unroll
            for (int i = 0; i < 4; ++i) t[i] = __builtin_amdgcn_exp2f(__builtin_fmaf(sx[i], CEXP, mx) - up);
            e01 = pk(t[0], t[1]); e23 = pk(t[2], t[3]);
            asm volatile("s_nop 1" : "+v"(e01), "+v"(e23));
            QPERM(P.x, e01, "[0,1,0,1]"); QPERM(P.z, e01, "[2,3,2,3]"); QPERM(P.y, e23, "[0,1,0,1]"); QPERM(P.w, e23, "[2,3,2,3]");
            NOPS(1);
            MF0(oa, vx[0], P); MF0(ob, vx[1], P);
            NOPS(7);
        }
        SCHED();
        PV(1, 1); VEXP(tm0, tm0); SEL(oa[0], oa[0], ob[0]); SCHED();
        PV(8, 1); VEXP(tm1, tm1); SEL(oa[1], oa[1], ob[1]); SCHED();
        PV(9, 1); VCVT(pmm, tm0, tm1); SEL(oa[2], oa[2], ob[2]); SCHED();
        PV(2, 1);
        asm volatile("v_cndmask_b32 %0, %2, 0, %3\n\tv_cndmask_b32 %1, 0, %2, %3" : "=&v"(pm.x), "=&v"(pm.y) : "v"(pmm), "s"(himask)); SCHED();
        PV(3, 1); SEL(oa[3], oa[3], ob[3]); SCHED();
        MF0(rsum, ones, P); NOPS(4);
        VADD(fc, av1[0], av1[2]); VADD(fd, av1[1], av1[3]);
        { const v2u ones2 = ones.lo; MF1H(rsum, ones2, pm); }
        VCVT(vm.y, fc, fd); NOPS(1);
        MF1H(oa, vm, pm);
        {
            float inv; v4u oc;
            asm volatile("s_nop 3\n\tv_rcp_f32 %0, %1\n\ts_nop 2" : "=v"(inv) : "v"(rsum[0]), "v"(rsum));
            PIN(oa);
            const f32x2 i2 = {inv, inv};
            const f32x2 u0 = oa.lo * i2, u1 = oa.hi * i2;
            asm volatile("v_cvt_pk_bf16_f32 %0, %4, %5\n\tv_cvt_pk_bf16_f32 %2, %4, %5\n\tv_cvt_pk_bf16_f32 %1, %6, %7\n\tv_cvt_pk_bf16_f32 %3, %6, %7\n\t"
                         "v_permlane16_swap_b32 %0, %2\n\ts_nop 0\n\tv_permlane16_swap_b32 %1, %3"
                         : "=&v"(oc.x), "=&v"(oc.y), "=&v"(oc.z), "=&v"(oc.w) : "v"(u0.x), "v"(u0.y), "v"(u1.x), "v"(u1.y));
            st16(rs, oso, oc);
        }
        SCHED();
        unsigned long long clk;
        asm volatile("s_memrealtime %0" : "=s"(clk));
        HT(6);
        PIN(fl);
        v4u tq[4], tk[8], tv[2];
        f32x4 c0, c1, cm;
        u32 oo, kvo; bool kvok;
        u32 ln = l16; PIN(ln);
        lptr QX = lds + C_QX + (w & 1) * 4096 + ln, VO = lds + C_V + (w ^ 1) * 2048 + ln, VMX = lds + C_VM + w * 512 + (ln >> 1);
#pragma unroll
        for (int i = 0; i < 2; ++i) { tq[i] = lds_r16(QX + i * 1024); tk[2 * i] = lds_r16(LB + C_K + i * 1024); tk[2 * i + 1] = lds_r16(LB + C_K + 4096 + i * 1024); }
        *(LAS v2u*)VMX = vm;
        SCHED();
        SFOR(i, 12)
            if constexpr (i == 0) MF0(cm, kmf[0], tq[0]); else if constexpr (i == 1) MF0(c0, tk[0], tq[0]); else if constexpr (i == 2) MF0(c1, tk[1], tq[0]);
            else if constexpr (i % 3 == 0) MF1(cm, kmf[i / 3], tq[i / 3]); else if constexpr (i % 3 == 1) MF1(c0, tk[2 * (i / 3)], tq[i / 3]); else MF1(c1, tk[2 * (i / 3) + 1], tq[i / 3]);
            if constexpr (i == 0) { tq[2] = lds_r16(QX + 2048); tk[4] = lds_r16(LB + C_K + 2048); }
            if constexpr (i == 1) { tk[5] = lds_r16(LB + C_K + 6144); tq[3] = lds_r16(QX + 3072); }
            if constexpr (i == 2) { tk[6] = lds_r16(LB + C_K + 3072); tk[7] = lds_r16(LB + C_K + 7168); }
            if constexpr (i == 3) { tv[0] = lds_r16(VO); tv[1] = lds_r16(VO + 1024); }
            if constexpr (i == 4) { oo = ln + (ln & 0x300u) + ((vsrc ^ 8192u) + (u32)((w & 1) * 256)); PIN(oo); }
            if constexpr (i == 5) { ksrc += NH * 16384; vsrc += NH * 16384; PINS(ksrc); PINS(vsrc); }
            if constexpr (i == 7) { kvok = ((u32)__builtin_amdgcn_ballot_w64(fl < (u32)s + 2) & 7u) == 0u; }
            if constexpr (i == 8) { kvo = l16 | (kvok ? 0u : OOB); PIN(kvo); }
            SCHED();
        SEND
        asm volatile("s_waitcnt lgkmcnt(0)\n\ts_barrier" : "+s"(clk) :: "memory");
        const v2u tvm = *(LAS v2u*)(VMX + (512 - (w & 1) * 1024));
        const v4u pv = lds_r16(ZP + 80);
        DMA32(dk, kvo, ksrc);
        SCHED(); asm volatile("" :: "v"(cm));
        float u[10], m, m2;
        {
            float NEG = -1000000000.0f / SCALE; PIN(NEG);
#define MSK(d, a, i) asm volatile("v_cndmask_b32 %0, %1, %2, %3" : "=v"(d) : "v"(NEG), "v"(a), "s"(vis[i]))
#pragma unroll
            for (int i = 0; i < 4; ++i) MSK(u[i], c0[i], i);
#pragma unroll
            for (int i = 0; i < 4; ++i) MSK(u[4 + i], c1[i], 4 + i);
            asm volatile("v_max3_f32 %0, %2, %3, %10\n\tv_max3_f32 %1, %4, %5, %11\n\tv_max3_f32 %0, %0, %6, %7\n\tv_max3_f32 %1, %1, %8, %9\n\t"
                         "v_max_f32 %0, %0, %1\n\tv_mov_b32 %1, %0\n\ts_nop 1\n\tv_permlane16_swap_b32 %0, %1\n\tv_max_f32 %0, %0, %1\n\tv_mov_b32 %1, %0\n\ts_nop 1\n\t"
                         "v_permlane32_swap_b32 %0, %1\n\tv_max_f32 %0, %0, %1\n\tv_mul_f32 %0, 0xbe0293ee, %0"
                         : "=&v"(m), "=&v"(m2) : "v"(u[0]), "v"(u[1]), "v"(u[2]), "v"(u[3]), "v"(u[4]), "v"(u[5]), "v"(u[6]), "v"(u[7]), "v"(cm[0]), "v"(cm[1]));
        }
        DMA32(dv, kvo, vsrc);
        {
            v4u px; v2u p0, p1;
#define EX2(d, a, b) asm volatile("v_fmamk_f32 %0, %2, 0x3e0293ee, %4\n\tv_fmamk_f32 %1, %3, 0x3e0293ee, %4\n\tv_exp_f32 %0, %0\n\tv_exp_f32 %1, %1" : "=&v"(d.x), "=&v"(d.y) : "v"(a), "v"(b), "v"(m))
            f32x2 ex[5];
            EX2(ex[0], u[0], u[1]); EX2(ex[1], u[2], u[3]); EX2(ex[2], u[4], u[5]); EX2(ex[3], u[6], u[7]); EX2(ex[4], cm[0], cm[1]);
            asm volatile("v_cvt_pk_bf16_f32 %0, %8, %9\n\tv_cvt_pk_bf16_f32 %1, %10, %11\n\tv_cvt_pk_bf16_f32 %2, %12, %13\n\tv_cvt_pk_bf16_f32 %3, %14, %15\n\t"
                         "v_mov_b32 %5, 0\n\tv_mov_b32 %6, 0\n\tv_cvt_pk_bf16_f32 %4, %16, %17\n\tv_cvt_pk_bf16_f32 %7, %16, %17"
                         : "=&v"(px.x), "=&v"(px.y), "=&v"(px.z), "=&v"(px.w), "=&v"(p0.x), "=&v"(p0.y), "=&v"(p1.x), "=&v"(p1.y)
                         : "v"(ex[0].x), "v"(ex[0].y), "v"(ex[1].x), "v"(ex[1].y), "v"(ex[2].x), "v"(ex[2].y), "v"(ex[3].x), "v"(ex[3].y), "v"(ex[4].x), "v"(ex[4].y));
            f32x4 o[4], sum;
            const v2u ones2 = ones.lo;
            const u32 now = (u32)clk;
            u32 oz;
            SCHED();
            MF0(sum, ones, px); { const u32 q = ksrc ^ 8192u; DMA32(dq, kvo, q); } SCHED();
            MF0(o[0], vx[0], px);
            MF0(o[1], vx[1], px);
            MF0(o[2], tv[0], px); oz = (oso & ~(3u << 17)) | (u32)((s + 3) & 3) << 17; PIN(oz); SCHED();
            MF0(o[3], tv[1], px); oso = (oso & ~(3u << 17)) | (u32)((s + 1) & 3) << 17; PIN(oso); SCHED();
            MF1H(sum, ones2, p0);
            MF1H(o[0], vm, p0);
            MF1H(o[1], vm, p1); st16(rs, oz, pv); SCHED();
            MF1H(o[2], tvm, p0);
            MF1H(o[3], tvm, p1);
            float inv;
            asm volatile("s_nop 2\n\tv_rcp_f32 %0, %1\n\ts_nop 0" : "=v"(inv) : "v"(sum[0]), "v"(sum));
            asm volatile("" : "+v"(o[0]), "+v"(o[1]));
            st16(rs, oo, (v4u){pk2(o[0].lo * inv), pk2(o[0].hi * inv), pk2(o[1].lo * inv), pk2(o[1].hi * inv)});
            asm volatile("" : "+v"(o[2]), "+v"(o[3]));
            st16(rs, oo ^ 2048u, (v4u){pk2(o[2].lo * inv), pk2(o[2].hi * inv), pk2(o[3].lo * inv), pk2(o[3].hi * inv)});
            SCHED();
            if (__builtin_expect(h == 0 && w == 3, 0)) {
                st4s(rs, ln == 0 ? hint + 128u : OOB, 0, (u32)s << 16 | min(missed, 1u) << 14 | min(now - tm, 0x3FFFu));
                LST(L * 64 + s, now); LST(128 + L * 64 + s, ((int)(rdl(fl, 3) - now) >= CH_VIS - 4) | ((int)kvok << 1) | ((int)min(missed, 3u) << 2)); LST(640 + L * 64 + s, tm); LST(1728 + L * 64 + s, __builtin_amdgcn_s_memtime());
            }
            tm = rdl(fl, 5);
            missed = missed == SPIN_CAP ? missed : 0u;
            if (__builtin_expect((int)(rdl(fl, 3) - now) < CH_VIS - 4, 0)) {
#pragma unroll 1
                for (int n = 0; n < 8; ++n) {
                    CFENCE();
                    const u32 hv = ld4(rs, fkh);
                    tm = rdl(hv, 5);
                    if ((int)(rdl(hv, 3) - now) >= CH_VIS - 4) break;
                }
            }
        }
        LS2(L == 0 && w == 0, h);
        if (__builtin_expect(!kvok, 0) && s + 1 < NSEG) {
            st4s(rs, (ln == 0 && w == 0) ? hint + 4096u + (u32)h * 128u : OOB, 0, (u32)s + 1u);
            wait_flags(rs, ln < 48u ? fkh : OOB, (u32)s + 2, dead);
            DMA32(dk, l16, ksrc); DMA32(dv, l16, vsrc); DMA32(dq, l16, ksrc ^ 8192u);
        }
        ++s;
        } else {
            u32 ln = l16; PIN(ln);
            const u32 hv = ld4(rs, ln == 96u ? foff(F_ERR) : fkh);
            tm = rdl(hv, 5);
            *(LAS u32*)(lds + C_DUMP + (w == 0 ? 0 : 64)) = rdl(hv, 6);
            BAR();
            missed = uu(*(LAS u32*)(lds + C_DUMP)) != 0u ? SPIN_CAP : missed + 1;
            st4s(rs, missed == SPIN_CAP ? foff(F_ERR) : OOB, 0, 1u);
            BAR();
        }
    }
    DRAIN(); BAR();
    st4s(rs, ofl, 0, NSEG);
}

#define WO_GAP 6
DEV void role_wo(const Params& p, lptr lds, int j, v2u ep) {
    const int tid = threadIdx.x, lane = tid & 63, w = uni(tid >> 6), g = lane >> 4, r = lane & 15;
    const int rg = j & 1, cg = j >> 1, rot = cg & 7;
    const rsrc_t rs = mk_rsrc(p.ws, W_END);
    v8u wo[8][5];
    {
        const v4u so_ = mk_srd(p.Wo, DM * NQ * 2);
        u32 vo[5];
#pragma unroll
        for (int i = 0; i < 5; ++i) { const int n = 64 * i + lane; vo[i] = (u32)((n / 5) * DM + cg * 40 + (n % 5) * 8) * 2u; }
        auto fill = [&](int b) {
            const u32 dst = uu(w * 40960 + (b & 3) * 10240), src = uu((8 * w + ((b + rot) & 7)) * (DH * DM * 2));
#pragma unroll
            for (int i = 0; i < 10; ++i) DMAIN(so_, dst + i * 1024, vo[i % 5], src + (i / 5) * (64 * DM * 2));
        };
        __builtin_amdgcn_s_sleep(ST_WO);
#pragma unroll 1
        for (int b = 0; b < 4; ++b) fill(b);
        const int q4 = r >> 2;
        lptr tb = lds + w * 40960 + (32 * (g >> 1) + 16 * (q4 >> 1) + 8 * ((r >> 1) & 1) + 4 * (g & 1) + (q4 & 1)) * 80 + (r & 1) * 8;
        SFOR(b, 8)
            VMW(b < 5 ? 30 : 70 - 10 * b);
#pragma unroll
            for (int t = 0; t < 5; ++t) {
                lptr q = tb + (b & 3) * 10240 + t * 16;
                wo[b][t] = tr_frag(q, q + 160, q + 64 * 80, q + 64 * 80 + 160);
                if (b * 5 + t < 32) asm volatile("" : "+a"(wo[b][t])); else asm volatile("" : "+v"(wo[b][t]));
            }
            CFENCE();
            if constexpr (b < 4) fill(b + 4);
        SEND
    }
    if (j == 0) LST(265, rt32());
    pre_wait(rs, 0, NCV, 1u, ep);
    const u32 idx = (r & 2) ? 0xEEEEu : 0x4444u;
    bool dead = false;
    const u32 pol0 = uu(get_pol(rs));
    const u32 l16 = (u32)lane * 16u;
    u32 oo[8];
#pragma unroll
    for (int i = 0; i < 8; ++i) oo[i] = uu(W_ORING + ring_map((u32)(rg * 32768 + (8 * w + ((i + rot) & 7)) * 1024), pol0));
    const u32 hint = W_MRING + 4 * 32768u + pol0 * 8192u;
    u32 hvo[2];
#pragma unroll
    for (int l = 0; l < 2; ++l) hvo[l] = lane < 2 ? hint + (u32)(l ^ 1) * (MB / 4) + (u32)lane * 4u : lane == 2 ? hint + (u32)l * (MB / 4) + 128u : lane == 3 ? foff(F_ERR) :
                 (lane >= 32 && j == 0 && w == 3) ? hint + (u32)l * (MB / 4) + (u32)lane * 128u : OOB;
    lptr xw = r < 8 ? lds + w * 640 + (2 * (g & 1) + (g >> 1)) * 32 + r * 4 : lds + 8192 + lane * 4;
    const int G = ((5 * cg) >> 1) + w, tt = 2 * G + ((lane >> 3) & 1) - 5 * cg;
    const bool pub = w < 3 && lane < 32 && tt >= 0 && tt < 5;
    lptr xr = lds + (pub ? tt * 128 + (2 * (lane >> 4) + ((lane >> 2) & 1)) * 32 + (lane & 3) * 8 : 0);
    const u32 mpo = pub ? W_MRING + ring_map((u32)(G * 256 + rg * 128 + lane * 4), pol0) : OOB;
    constexpr u32 PER0 = CH_W0 + CH_MW + CH_H0 + CH_MH + 2 * CH_VIS;
    const bool lead = j == 0 && w == 3;
    const u32 hso = (lead && lane == 0) ? hint : OOB;
    MK_SRD();
    u32 t = 0, tn = 0;
    if (j == 0) {
#pragma unroll 1
        for (int n = 0; n < SPIN_CAP && !dead; ++n) {
            CFENCE();
            if (all_(ld4(rs, W_ORING + pol0 * 8192u + (u32)w * 16384u + (u32)lane * 128u) != PSENT)) break;
            __builtin_amdgcn_s_sleep(1);
        }
        BAR();
    }
    if (lead) {
        t = rt32() + 40u; tn = t + PER0 / 2 + CH_REC;
        st16(rs, lane < 2 ? hint + (u32)lane * (MB / 4) : OOB, (v4u){lane == 0 ? t : tn, PER0, (lane == 0 ? t : tn) + (CH_W0 + CH_VIS + CH_MW + CH_REC), 0u});
    } else {
#pragma unroll 1
        for (int n = 0; n < SPIN_CAP && !dead; ++n) {
            CFENCE();
            const u32 hv = ld4(rs, hvo[1]);
            t = rdl(hv, 0);
            if (rdl(hv, 1) < 400u) break;
            __builtin_amdgcn_s_sleep(1);
        }
    }
    int sn[2] = {0, 0};
    u32 hav = 4 * CH_H0, wav = 4 * CH_W0, mh = 8 * CH_MH, mw = 8 * CH_MW, ok[2] = {0u, 0u}, dw[2] = {CH_W0 + CH_VIS + CH_MW + CH_REC, CH_W0 + CH_VIS + CH_MW + CH_REC};
    u32 zero = 0, hv = 0xFFFFu, pst = 1, pdur = 0, pmp = OOB;
    const u32 psent = PSENT;
    lptr sw = lds + 2560 + w * 4;
    bool stop = false;
    asm volatile("" : "+v"(zero), "+v"(sw));
    auto turn = [&](auto lc) __attribute__((always_inline)) {
        constexpr int L = decltype(lc)::value, M = L ^ 1;
        u32 now, done, rep, rd, lost, dh, dn, nt, za; v4u hw;
        const int sc = sn[L];
        auto books = [&](auto ic) __attribute__((always_inline)) {
            constexpr int i = decltype(ic)::value;
            if constexpr (i == 0) { rep = rdl(hv, 2); PINS(rep); }
            if constexpr (i == 1) { lost = (u32)(__builtin_amdgcn_ballot_w64(hv == (u32)sn[M]) >> 32); PINS(lost); }
            if constexpr (i == 2) { lost |= (rep >> 16) ^ (u32)sn[M]; PINS(lost); }
            if constexpr (i == 3) { lost |= ok[M] ^ 2u; rd = rep & 0xFFFFu; PINS(lost); PINS(rd); }
            if constexpr (i == 4) { lost = lost == 0u ? CH_UP * 8u : 0u; PINS(lost); }
            if constexpr (i == 5) { dn = wav >> 2; PINS(dn); pdur = min(pdur, dn + 8u); PINS(pdur); }
            if constexpr (i == 6) { pdur = (done & ok[M]) ? pdur : dn; PINS(pdur); }
            if constexpr (i == 7) { wav += pdur - dn; PINS(wav); }
            if constexpr (i == 8) { dh = hav >> 2; PINS(dh); nt = min(rd, dh + 8u); PINS(nt); }
            if constexpr (i == 9) { nt = rd < 0x3FFFu ? nt : dh; PINS(nt); }
            if constexpr (i == 10) { hav += nt - dh; PINS(hav); }
            if constexpr (i == 11) { mw = max(mw - 1u, CH_MW * 8u); PINS(mw); }
            if constexpr (i == 12) { mh = max(mh - 1u, CH_MH * 8u); PINS(mh); }
            if constexpr (i == 13) { rd = rd >> 14 == 1u ? lost : 0u; PINS(rd); }
            if constexpr (i == 14) { mw += rd; mh += lost - rd; PINS(mw); PINS(mh); }
            if constexpr (i == 15) { ok[M] = done ? 1u : (ok[M] & 1u) << 1; PINS(ok[M]); }
            if constexpr (i == 16) { dh = (2u * hav + mh) / 8u + CH_VIS; PINS(dh); }
            if constexpr (i == 17) { dn = (2u * wav + mw) / 8u + CH_VIS; PINS(dn); }
            if constexpr (i == 18) { nt = dw[L] + dh; PINS(nt); }
            if constexpr (i == 19) { dh += dn; dw[L] = dn; sn[M] += (int)done; PINS(dh); PINS(sn[M]); }
            if constexpr (i == 20) { rd = tn - t + (dh >> 1); PINS(rd); }
            if constexpr (i == 21) { rd = sn[M] + 1 < NSEG ? rd : 0u; PINS(rd); }
            if constexpr (i == 22) { nt = t + (u32)max((int)nt, (int)rd); PINS(nt); }
            if constexpr (i == 23) { hw.x = nt; hw.y = dh; PIN(hw.x); PIN(hw.y); }
            if constexpr (i == 24) { hw.z = nt + dn; hw.w = 0u; PIN(hw); }
            if constexpr (i == 25) st16(rs, hso + L * (MB / 4), hw);
        };
        if (__builtin_expect(sc + 1 < NSEG, 1)) {
            u32 vo = l16 + (L * (MB / 2) + (u32)(sc & 3) * 131072u), mp, hn;
            PIN(vo);
#define WT(k) do {} while (0)
            WT(0);
            v4u d[8];
            {
                u32 tmp;
                asm volatile(WAIT_TXT("%1", "%2") "buffer_load_dwordx4 %0, %3, %4, %5 offen sc1\n\ts_waitcnt lgkmcnt(0)"
                             : "=&v"(d[0]), "=&s"(tmp) : "s"(uu(t - 1u)), "v"(vo), "s"(srd), "s"(oo[0]) : "vcc", "scc", "memory");
            }
            SFOR(i, 7)
                asm volatile("buffer_load_dwordx4 %0, %1, %2, %3 offen sc1" : "=v"(d[i + 1]) : "v"(vo), "s"(srd), "s"(oo[i + 1]) : "memory");
                SCHED();
                if constexpr (i == 0) { done = uu(pst) ^ 1u; PINS(done); }
                static_for<3 * i, 3 * i + 3>(books);
                SCHED();
            SEND
            hn = ld4(rs, hvo[L]);
            SCHED();
            WT(1);
            static_for<21, 26>(books);
            za = done ? pmp : OOB;
            st4s(rs, za, 0, psent);
            *(LAS u32*)(sw + L * 4096) = zero;
            f32x4 acc[5] = {};
            mp = mpo + (L * (MB / 4) + (u32)((sc + 1) & 3) * 32768u); pmp = mpo + (L * (MB / 4) + (u32)(sc & 3) * 32768u);
            asm volatile("" : "+v"(acc[0]), "+v"(acc[1]), "+v"(acc[2]), "+v"(acc[3]), "+v"(acc[4]), "+v"(mp), "+v"(pmp));
            SCHED();
            WT(2);
            float f0[5], f1[5]; unsigned long long bad;
#define WSW(o_) asm volatile("v_permlane32_swap_b32 %0, %1" : "+v"(f0[o_]), "+v"(f1[o_]))
#define WAD(o_) asm volatile("v_add_f32_dpp %0, %1, %0 row_ror:8 row_mask:0xf bank_mask:0xf" : "+v"(f0[o_]) : "v"(f1[o_]))
#define WFO(o_) do { VADD(f0[o_], acc[o_][0], acc[o_][2]); VADD(f1[o_], acc[o_][1], acc[o_][3]); } while (0)
#define WWR(o_) *(LAS float*)(xw + L * 4096 + (o_) * 128) = f0[o_]
            asm volatile("s_waitcnt vmcnt(10)" : "+v"(d[0]));
            if (__builtin_expect(__builtin_amdgcn_ballot_w64(d[0].x == PSENT) != 0ull, 0)) {
                asm volatile("s_waitcnt vmcnt(3)" : "+v"(d[1]), "+v"(d[2]), "+v"(d[3]), "+v"(d[4]), "+v"(d[5]), "+v"(d[6]), "+v"(d[7]));
#pragma unroll
                for (int i = 0; i < 5; ++i) *(LAS u32*)(xw + L * 4096 + i * 128) = psent;
                *(LAS u32*)(sw + L * 4096) = 1u;
            } else {
            SFOR(i, 40)
                constexpr int pc = i < 30 ? i / 5 : 6 + (i & 1), oc = i < 30 ? i % 5 : (i - 30) >> 1;
                if constexpr (oc == 0 && pc > 0) asm volatile("s_waitcnt vmcnt(%1)" : "+v"(d[pc]) : "i"(10 - pc));
                if constexpr (pc * 5 + oc < 32) SMF_A(acc[oc], d[pc], wo[pc][oc]); else SMF_V(acc[oc], d[pc], wo[pc][oc]);
                if constexpr (i == 0) WT(3);
                if constexpr (i == 20) WT(4);
                if constexpr (i == 31 || i == 33) NOPS(1);
                if constexpr (i == 32) NOPS(0);
                if constexpr (i == 34) WFO(0);
                if constexpr (i == 35) { asm volatile("v_cmp_u_f32 %0, %1, %2" : "=s"(bad) : "v"(f0[0]), "v"(f1[0])); WSW(0); }
                if constexpr (i == 36) WFO(1);
                if constexpr (i == 37) { WAD(0); WSW(1); }
                if constexpr (i == 38) { WWR(0); SCHED(); WFO(2); }
                if constexpr (i == 39) { WAD(1); WSW(2); }
                SCHED();
            SEND
            WT(5);
            WWR(1); SCHED(); WFO(3); WAD(2); WWR(2); SCHED(); WSW(3);
            if (__builtin_expect(bad != 0ull, 0)) {
                bool miss = false;
#pragma unroll
                for (int i = 0; i < 8; ++i) miss |= d[i].x == PSENT;
                if (all_(!miss)) {
#pragma unroll
                    for (int i = 0; i < 8; ++i) miss |= d[i].y == PSENT || d[i].z == PSENT || d[i].w == PSENT;
                }
                *(LAS u32*)(sw + L * 4096) = all_(!miss) ? 0u : 1u;
            }
            WFO(4); WAD(3); WWR(3); SCHED();
            asm volatile("v_permlane32_swap_b32 %0, %1\n\ts_nop 1\n\tv_add_f32_dpp %0, %1, %0 row_ror:8 row_mask:0xf bank_mask:0xf" : "+v"(f0[4]), "+v"(f1[4]));
            WWR(4);
            }
            WT(6);
            f32x2 a, b, c, e; u32 cw, mq; v4u sv;
            asm volatile("s_waitcnt lgkmcnt(0)\n\ts_barrier\n\tds_read_b64 %0, %5 offset:%7\n\tds_read_b64 %1, %5 offset:%7+640\n\tds_read_b64 %2, %5 offset:%7+1280\n\tds_read_b64 %3, %5 offset:%7+1920\n\t"
                         "ds_read_b128 %4, %6 offset:%7+2560\n\t"
                         "s_waitcnt lgkmcnt(3)\n\tv_pk_add_f32 %0, %0, %1\n\ts_waitcnt lgkmcnt(1)\n\tv_pk_add_f32 %2, %2, %3\n\ts_waitcnt lgkmcnt(0)\n\tv_pk_add_f32 %0, %0, %2"
                         : "=&v"(a), "=&v"(b), "=&v"(c), "=&v"(e), "=&v"(sv) : "v"(xr), "v"(zero), "i"(L * 4096) : "memory");
            asm volatile("v_cmp_u_f32 vcc, %2, %3\n\tv_cvt_pk_bf16_f32 %0, %2, %3\n\tv_cndmask_b32 %1, %4, %5, vcc" : "=&v"(cw), "=&v"(mq) : "v"(a.x), "v"(a.y), "v"(mp), "v"(OOB) : "vcc");
            st4s(rs, mq, 0, cw);
            SCHED();
            now = rt32();
            PIN(hn); hv = hn;
            WT(7);
            pst = sv.x | sv.y | sv.z | sv.w;
            if (__builtin_expect(__builtin_amdgcn_ballot_w64(mq != mp && pst == 0u) != 0ull, 0)) st4s(rs, mp, 0, cw);
        } else {
            done = uu(pst) ^ 1u;
            st4s(rs, done ? pmp : OOB, 0, psent);
            static_for<0, 25>(books);
            pst = 1;
            hv = ld4(rs, hvo[L]);
            PIN(hv);
            now = rt32();
        }
        pdur = now - t;
        const u32 per = rdl(hv, 1), th = rdl(hv, 0);
        t = lead ? tn : (int)(th - now) < -(int)(per >> 1) ? th + per : th;
        tn = nt;
        dw[M] += max((int)(now + CH_WX - t), 0);
        stop = stop || rdl(hv, 3) != 0u;
    };
    for (int k = 0; k < 2 * SPIN_CAP && !stop && (sn[0] + 1 < NSEG || sn[1] + 1 < NSEG); ++k) { turn(IC<0>{}); turn(IC<1>{}); }
    if (sn[0] + 1 < NSEG || sn[1] + 1 < NSEG) st4s(rs, tid == 0 ? foff(F_ERR) : OOB, 0, 1u);
}

__global__ void __launch_bounds__(256, 1) k_main(Params p) {
    lptr lds = (lptr)(u32)16 - 16;
    if (blockIdx.x == 0) LST(256, rt32());
    unsigned long long pc; int len;
    asm volatile("s_getpc_b64 %0\n.Lk_main_here:\n\ts_mov_b32 %1, .Lk_end_here-.Lk_main_here" : "=s"(pc), "=s"(len));
    const rsrc_t rs = mk_rsrc(p.ws, W_END);
    const v2u e0 = ld8s(rs, threadIdx.x == 0 ? foff(F_EPOCH) : OOB, 0);
    const v4u code = __builtin_amdgcn_raw_buffer_load_b128(mk_rsrc((const void*)(pc & ~15ull), (u32)max(len, 0)), (((blockIdx.x >> 3) & 31u) * 256u + threadIdx.x) * 16u, 0, 0);
    CFENCE();
    if (threadIdx.x == 0) {
        GAS unsigned long long* slots = (GAS unsigned long long*)(p.ws + W_CTRL + F_SLOT * 128);
        const unsigned long long tag = (unsigned long long)e0.y << 32 | (e0.x + 1u);
        u32 role = ((u32)__builtin_amdgcn_s_getreg(20 | (3 << 11)) & 7u) * 32u + (blockIdx.x >> 3);
        for (int n = 0; n < 256 && __hip_atomic_exchange(slots + role * 16, tag, __ATOMIC_RELAXED, __HIP_MEMORY_SCOPE_AGENT) == tag; ++n) role = (role + 1u) & 255u;
        lds_w16(lds, (v4u){role, e0.x, e0.y, 0u});
    }
    __syncthreads();
    const v4u rv = lds_r16(lds);
    const int role = uni((int)rv.x), x = role >> 5, i = role & 31, w = uni(threadIdx.x >> 6);
    const v2u ep = {uu(rv.y), uu(rv.z)};
    asm volatile("" :: "v"(code));
    __syncthreads();
    if (x < 2) { role_head(p, lds, i >> 4, x * 16 + (i & 15), ep); __builtin_amdgcn_endpgm(); }
    if (x == 2) { role_wo(p, lds, i, ep); __builtin_amdgcn_endpgm(); }
    const int no = x < 5 ? 5 : 10, nr = x < 7 ? 5 : 4;
    if (i >= no + nr) { role_qkv(p, lds, (x < 5 ? (x - 3) * 22 : 44 + (x - 5) * 17) + i - no - nr, ep); __builtin_amdgcn_endpgm(); }
    const int og = x < 5 ? x - 3 : 2 * (x - 4) + (i >= 5), ng = i % 5, kq = og >> 1, L = og & 1, r = (x - 3) * 5 + i - no, q = r / 3;
    const int j = i < no ? kq * 5 + ng : r % 3 == 0 ? 20 + (q & 1) * 20 + (q >> 1) : 16 + (r % 3) * 8 + q;
    const bool cv = i >= no && r % 3 == 0;
    if (i >= no || L == 1) conv_stage(p, rs, lds, j, cv ? 8 + q : q, cv ? 40 : j >= 24 && j < 32 ? 8 : -8, ep);
    if (cv) __builtin_amdgcn_endpgm();
    if (i >= no) { role_row(p, lds, ((j - 24) ^ 8) * 4 + w, ep); __builtin_amdgcn_endpgm(); }
    role_out(p, lds, L, kq, ng, ep);
}
__global__ void k_end() { asm volatile(".Lk_end_here:"); }

extern "C" void kernel_launch(void* const* d_in, const int* in_sizes, int n_in, void* d_out, int out_size, void* d_ws, size_t ws_size,
                              hipStream_t stream) {
    Params p;
    p.x = (const u16*)d_in[0]; p.mem0 = (const u16*)d_in[1]; p.Wq = (const u16*)d_in[2]; p.Wk = (const u16*)d_in[3];
    p.Wv = (const u16*)d_in[4]; p.Wo = (const u16*)d_in[5];
    p.bk = (const u16*)d_in[8]; p.bv = (const u16*)d_in[9]; p.shift = (const u16*)d_in[10]; p.ssmk = (const u16*)d_in[11]; p.ssmD = (const u16*)d_in[12];
    p.out = (u16*)d_out;
    p.ws = (unsigned char*)(((size_t)d_ws + 2 * MB - 1) & ~(size_t)(2 * MB - 1));
    (void)hipFuncSetAttribute((const void*)k_main, hipFuncAttributeMaxDynamicSharedMemorySize, LDS_BYTES);
    hipLaunchKernelGGL(k_main, dim3(256), dim3(256), LDS_BYTES, stream, p);
}
```

```cpp
#include <hip/hip_runtime.h>
#include <hip/hip_bf16.h>

typedef unsigned short u16;
typedef unsigned int u32;
typedef u32 v2u __attribute__((ext_vector_type(2)));
typedef u32 v4u __attribute__((ext_vector_type(4)));
typedef u32 v8u __attribute__((ext_vector_type(8)));
typedef float f32x2 __attribute__((ext_vector_type(2)));
typedef float f32x4 __attribute__((ext_vector_type(4)));
typedef float f32x16 __attribute__((ext_vector_type(16)));
typedef __bf16 bf16x2 __attribute__((ext_vector_type(2)));
typedef __bf16 bf16x8 __attribute__((ext_vector_type(8)));
typedef short s16x4 __attribute__((ext_vector_type(4)));
#define LAS __attribute__((address_space(3)))
#define GAS __attribute__((address_space(1)))
typedef LAS unsigned char* lptr;
#define DEV __device__ __forceinline__

constexpr int SEQ = 2048, DM = 640, NH = 32, DH = 128, NSEG = 64, NQ = NH * DH;

constexpr u32 MB = 1u << 20;
constexpr u32 W_ORING = 0;
constexpr u32 W_MRING = MB;
constexpr u32 W_PROBE = 2 * MB - 65536;
constexpr u32 W_CTRL = 4 * MB;
constexpr u32 CTRL_BYTES = 128 * 1024;
constexpr u32 ACT = SEQ * DM * 2;
constexpr u32 W_H1 = 5 * MB, W_Y = W_H1 + ACT, W_H2 = W_Y + ACT, W_X2 = W_H2 + ACT;
constexpr u32 W_KQ = 16 * MB;
constexpr u32 W_VO = W_KQ + 64 * MB;
constexpr u32 W_P = W_VO + 64 * MB;
constexpr u32 P_ITEM = 32 * 4 * DM * 4;
constexpr u32 W_KVT = W_P + 2 * NSEG * P_ITEM;
constexpr u32 W_SP = W_KVT + ACT;
constexpr u32 W_END = W_KVT + 3 * MB;
static_assert((size_t)W_END + 2 * MB <= 256ull * MB, "workspace");
constexpr u32 F_ERR = 0, F_QKV = 16, F_O = F_QKV + 192, F_P = F_O + 64, F_H2 = F_P + 64, F_CLR = F_H2 + 32,
              F_POL = F_CLR, F_EPOCH = F_POL + 32, F_SLOT = F_EPOCH + 1, F_PRE = F_SLOT + 256, F_N = F_PRE + 192;
constexpr int NCV = 44, NPRE = 40;
static_assert(F_N * 128 <= CTRL_BYTES, "ctrl");
DEV u32 foff(u32 idx) { return W_CTRL + idx * 128u; }

constexpr u32 OOB = 0x80000000u;
constexpr u32 PSENT = 0x7F817F81u;
constexpr int SPIN_CAP = 40000;
constexpr int LDS_BYTES = 160 * 1024;
#define ST_HEAD 0
#define ST_WO 0

#define LS3(cond, slot, idx) do {} while (0)
#define LST(idx, v) do {} while (0)
#define LS2(cond, idx) do {} while (0)
struct Params {
    const u16 *x, *mem0, *Wq, *Wk, *Wv, *Wo, *bk, *bv, *shift, *ssmk, *ssmD;
    u16* out;
    unsigned char* ws;
};

typedef __amdgpu_buffer_rsrc_t rsrc_t;
DEV rsrc_t mk_rsrc(const void* p, u32 bytes) { return __builtin_amdgcn_make_buffer_rsrc((void*)p, 0, bytes, 0x00020000); }
DEV v4u ld16s(rsrc_t r, u32 off, u32 so) { return __builtin_amdgcn_raw_buffer_load_b128(r, off, so, 16); }
DEV v2u ld8s(rsrc_t r, u32 off, u32 so) { return __builtin_amdgcn_raw_buffer_load_b64(r, off, so, 16); }
DEV v4u ldin16(rsrc_t r, u32 off, u32 so) { return __builtin_amdgcn_raw_buffer_load_b128(r, off, so, 2); }
DEV v2u ldin8(rsrc_t r, u32 off, u32 so) { return __builtin_amdgcn_raw_buffer_load_b64(r, off, so, 2); }
DEV u32 ld4(rsrc_t r, u32 off) { return __builtin_amdgcn_raw_buffer_load_b32(r, off, 0, 16); }
DEV void st16(rsrc_t r, u32 off, v4u v) { __builtin_amdgcn_raw_buffer_store_b128(v, r, off, 0, 16); }
DEV void st8s(rsrc_t r, u32 off, u32 so, v2u v) { __builtin_amdgcn_raw_buffer_store_b64(v, r, off, so, 16); }
DEV void st4s(rsrc_t r, u32 off, u32 so, u32 v) { __builtin_amdgcn_raw_buffer_store_b32(v, r, off, so, 16); }
#define DRAIN() asm volatile("s_waitcnt vmcnt(0)" ::: "memory")
#define VMW(n) asm volatile("s_waitcnt vmcnt(%0)" :: "i"(n) : "memory")
#define BAR() asm volatile("s_waitcnt lgkmcnt(0)\n\ts_barrier" ::: "memory")
#define SCHED() __builtin_amdgcn_sched_barrier(0)
#define CFENCE() asm volatile("" ::: "memory")
#define NOPS(n) asm volatile("s_nop %0" :: "i"(n))
#define PIN(x) asm volatile("" : "+v"(x))
#define PINS(x) asm volatile("" : "+s"(x))

template <int V> struct IC { static constexpr int value = V; };
template <int I, int N, class F> DEV void static_for(F&& f) { if constexpr (I < N) { f(IC<I>{}); static_for<I + 1, N>(f); } }
#define SFOR(var, n) static_for<0, n>([&](auto ic_) __attribute__((always_inline)) { constexpr int var = decltype(ic_)::value;
#define SEND });

DEV u32 pk(float a, float b) { bf16x2 v = {(__bf16)a, (__bf16)b}; return __builtin_bit_cast(u32, v); }
DEV float lo_f(u32 u) { return __builtin_bit_cast(float, u << 16); }
DEV float hi_f(u32 u) { return __builtin_bit_cast(float, u & 0xffff0000u); }
DEV f32x2 up2(u32 u) { return (f32x2){lo_f(u), hi_f(u)}; }
DEV u32 pk2(f32x2 v) { return pk(v.x, v.y); }
DEV float rbf(float a) { return (float)(__bf16)a; }
DEV v4u lds_r16(lptr p) { return *(LAS v4u*)p; }
DEV void lds_w16(lptr p, v4u v) { *(LAS v4u*)p = v; }
DEV v2u lds_tr(lptr p) { return __builtin_bit_cast(v2u, __builtin_amdgcn_ds_read_tr16_b64_v4i16((LAS s16x4*)p)); }
DEV v4u tr2(lptr p0, lptr p1) { const v2u a = lds_tr(p0), b = lds_tr(p1); return (v4u){a.x, a.y, b.x, b.y}; }
DEV v8u cat8(v4u a, v4u b) { return __builtin_shufflevector(a, b, 0, 1, 2, 3, 4, 5, 6, 7); }
DEV f32x4 mfma16(v4u a, v4u b, f32x4 c) { return __builtin_amdgcn_mfma_f32_16x16x32_bf16(__builtin_bit_cast(bf16x8, a), __builtin_bit_cast(bf16x8, b), c, 0, 0, 0); }
DEV f32x16 mfma32(v4u a, v4u b, f32x16 c) { return __builtin_amdgcn_mfma_f32_32x32x16_bf16(__builtin_bit_cast(bf16x8, a), __builtin_bit_cast(bf16x8, b), c, 0, 0, 0); }

DEV float f_(u32 u) { return __builtin_bit_cast(float, u); }
DEV u32 u_(float f) { return __builtin_bit_cast(u32, f); }
template <int CTRL> DEV float dpp(float v) { return f_((u32)__builtin_amdgcn_update_dpp(0, (int)u_(v), CTRL, 0xf, 0xf, false)); }
DEV float max16(float v) { auto r = __builtin_amdgcn_permlane16_swap(u_(v), u_(v), false, false); return fmaxf(f_(r[0]), f_(r[1])); }
DEV float max32(float v) { auto r = __builtin_amdgcn_permlane32_swap(u_(v), u_(v), false, false); return fmaxf(f_(r[0]), f_(r[1])); }
DEV float add16(float v) { auto r = __builtin_amdgcn_permlane16_swap(u_(v), u_(v), false, false); return f_(r[0]) + f_(r[1]); }
DEV float add32(float v) { auto r = __builtin_amdgcn_permlane32_swap(u_(v), u_(v), false, false); return f_(r[0]) + f_(r[1]); }
DEV float wave_sum(float v) {
    v += dpp<0xB1>(v);
    v += dpp<0x4E>(v);
    v += dpp<0x124>(v);
    v += dpp<0x128>(v);
    return add32(add16(v));
}
DEV bool all_(bool c) { return __builtin_amdgcn_ballot_w64(!c) == 0ull; }
DEV int uni(int v) { return __builtin_amdgcn_readfirstlane(v); }
DEV u32 uu(u32 v) { return (u32)__builtin_amdgcn_readfirstlane((int)v); }
DEV u32 rdl(u32 v, int l) { return (u32)__builtin_amdgcn_readlane((int)v, l); }
DEV u32 rt32() { return (u32)__builtin_amdgcn_s_memrealtime(); }

#define WSL 2
DEV void wait_flags(rsrc_t rs, u32 off, u32 want, bool& dead) {
    if (dead) return;
#pragma unroll 1
    for (int n = 0;; ++n) {
        CFENCE();
        const u32 v = ld4(rs, off);
        if (all_(off >= OOB || v >= want)) break;
        __builtin_amdgcn_s_sleep(WSL);
        if ((n & 63) == 63) { if (uni((int)ld4(rs, foff(F_ERR))) != 0) { dead = true; break; } }
        if (n > SPIN_CAP) { st4s(rs, foff(F_ERR), 0, 1u); dead = true; break; }
    }
    CFENCE();
}

DEV void pre_post(rsrc_t rs, int idx, u32 stage, v2u ep) { st8s(rs, threadIdx.x == 0 ? foff(F_PRE + idx) : OOB, 0, (v2u){ep.x + stage, ep.y}); }
DEV void pre_poll(rsrc_t rs, int first, int n, u32 stage, v2u ep) {
    const int lane = threadIdx.x & 63;
#pragma unroll 1
    for (int it = 0; it < SPIN_CAP; ++it) {
        CFENCE();
        const v2u a = ld8s(rs, foff(F_PRE + first + min(lane, n - 1)), 0), b = ld8s(rs, foff(F_PRE + first + min(lane + 64, n - 1)), 0);
        if (all_(a.y == ep.y && b.y == ep.y && max(a.x - ep.x - stage, b.x - ep.x - stage) < 8u - stage)) break;
        __builtin_amdgcn_s_sleep(2);
    }
    CFENCE();
}
DEV void pre_wait(rsrc_t rs, int first, int n, u32 stage, v2u ep) {
    if (uni(threadIdx.x >> 6) == 0) pre_poll(rs, first, n, stage, ep);
    BAR();
}
DEV u32 get_pol(rsrc_t rs) {
    const int lane = threadIdx.x & 63;
    const u32 raw = ld4(rs, lane < 16 ? foff(F_POL + lane) : OOB);
    return wave_sum((float)(int)raw) >= 0.f ? 1u : 0u;
}
#define NPROBE 16
DEV void pol_probe(rsrc_t rs, int id) {
    const int lane = threadIdx.x & 63;
    const u32 off0 = W_PROBE + (u32)(id >> 2) * 16384u + (u32)(id & 3) * 128u;
    st4s(rs, lane < 32 ? off0 + (u32)lane * 512 : OOB, 0, 0u); DRAIN();
    int d[2] = {1 << 16, 1 << 16};
#pragma unroll 1
    for (int q = 0; q < NPROBE; ++q) {
        const u32 half = (u32)(q ^ (q >> 1) ^ id) & 1u;
        const unsigned long long t0 = __builtin_amdgcn_s_memtime();
        const u32 v = ld4(rs, lane == 0 ? off0 + half * 8192 + (u32)(q >> 1) * 512 : OOB);
        asm volatile("s_waitcnt vmcnt(0)" :: "v"(v) : "memory");
        const int dt = (int)(__builtin_amdgcn_s_memtime() - t0);
        if (half) d[1] = min(d[1], dt); else d[0] = min(d[0], dt);
    }
    const u32 xcc = (u32)__builtin_amdgcn_s_getreg(20 | (3 << 11)) & 0xfu;
    st4s(rs, lane == 0 ? foff(F_POL + id) : OOB, 0, (u32)min(max((d[0] - d[1]) * ((xcc & 4u) ? -1 : 1), -250), 250));
}
DEV f32x2 ln_stats(const f32x2 (&x)[5]) {
    const float c = f_(uu(u_(x[0].x)));
    f32x2 d[5];
#pragma unroll
    for (int i = 0; i < 5; ++i) d[i] = x[i] - c;
    const f32x2 s2 = (d[0] + d[1]) + (d[2] + d[3]) + d[4], q2 = (d[0] * d[0] + d[1] * d[1]) + (d[2] * d[2] + d[3] * d[3] + d[4] * d[4]);
    float a = s2.x + s2.y, b = q2.x + q2.y, a2, b2;
#define LN_DPP(m) "v_add_f32_dpp %0, %0, %0 " m " row_mask:0xf bank_mask:0xf\n\tv_add_f32_dpp %1, %1, %1 " m " row_mask:0xf bank_mask:0xf\n\ts_nop 0\n\t"
    asm("s_nop 1\n\t" LN_DPP("quad_perm:[1,0,3,2]") LN_DPP("quad_perm:[2,3,0,1]") LN_DPP("row_ror:4")
        "v_add_f32_dpp %0, %0, %0 row_ror:8 row_mask:0xf bank_mask:0xf\n\tv_add_f32_dpp %1, %1, %1 row_ror:8 row_mask:0xf bank_mask:0xf\n\ts_nop 1\n\t"
        "v_permlane16_swap_b32 %0, %1\n\tv_add_f32 %0, %0, %1\n\tv_mov_b32 %1, %0\n\ts_nop 1\n\tv_permlane32_swap_b32 %0, %1\n\tv_add_f32 %0, %0, %1\n\ts_nop 0\n\t"
        "v_readlane_b32 %2, %0, 0\n\tv_readlane_b32 %3, %0, 16\n\ts_nop 1"
        : "+v"(a), "+v"(b), "=s"(a2), "=s"(b2));
    const float m = a2 * (1.0f / DM), var = fmaxf(b2 * (1.0f / DM) - m * m, 0.f);
    const float sd = rbf(__builtin_amdgcn_sqrtf(rbf(rbf(var) + 1e-5f)));
    return (f32x2){rbf(c + m), __builtin_amdgcn_rcpf(sd)};
}
DEV u32 pkv(f32x2 v) { u32 d; asm("v_cvt_pk_bf16_f32 %0, %1, %2" : "=v"(d) : "v"(v.x), "v"(v.y)); return d; }
DEV void store_h(rsrc_t rs, u32 base, int row, const u32 (&hp)[5]) {
    const u32 lane = threadIdx.x & 63;
    const u32 sw = (u32)(row & 15) * 16u, o8 = (lane >> 5) * 8192u + (((lane & 31u) * 8u) ^ sw), hb = base + (u32)(row >> 5) * (32 * DM * 2) + (u32)(row & 31) * 256u;
    st8s(rs, o8, hb, (v2u){hp[0], hp[1]});
    st8s(rs, o8, hb + 16384, (v2u){hp[2], hp[3]});
    st4s(rs, (lane * 4) ^ sw, hb + 32768, hp[4]);
}

#define DMA16(ldsb, vo, so) asm volatile("s_mov_b32 m0, %0\n\ts_nop 0\n\tbuffer_load_dwordx4 %1, %2, %3 offen sc1 lds" :: "s"(ldsb), "v"(vo), "s"(srd), "s"(so) : "memory")
#define DMA16I(ldsb, lo, vo, so, go) do { u32 so_; asm volatile("s_add_u32 m0, %1, %5\n\ts_add_u32 %0, %4, %6\n\tbuffer_load_dwordx4 %2, %3, %0 offen sc1 lds" \
                         : "=&s"(so_) : "s"(ldsb), "v"(vo), "s"(srd), "s"(so), "i"(lo), "i"(go) : "memory", "scc"); } while (0)
#define DMA32(ldsb, vo, so) asm volatile("s_mov_b32 m0, %0\n\ts_nop 0\n\tbuffer_load_dwordx4 %1, %2, %3 offen sc1 lds\n\tbuffer_load_dwordx4 %1, %2, %3 offen offset:1024 sc1 lds" \
                         :: "s"(ldsb), "v"(vo), "s"(srd), "s"(so) : "memory")
#define DMAIN(sr, ldsb, vo, so) asm volatile("s_mov_b32 m0, %0\n\ts_nop 0\n\tbuffer_load_dwordx4 %1, %2, %3 offen lds" :: "s"(ldsb), "v"(vo), "s"(sr), "s"(so) : "memory")
DEV v4u mk_srd(const void* q, u32 bytes) { return (v4u){uu((u32)(size_t)q), uu((u32)((size_t)q >> 32) & 0xffffu), bytes, 0x00020000u}; }
#define DMANT(sr, ldsb, vo, so) asm volatile("s_mov_b32 m0, %0\n\ts_nop 0\n\tbuffer_load_dwordx4 %1, %2, %3 offen nt lds" :: "s"(ldsb), "v"(vo), "s"(sr), "s"(so) : "memory")
#define MK_SRD() const v4u srd = mk_srd(p.ws, W_END)

constexpr int CV_W = 40960;
template <bool B> struct CvL {
    static constexpr int NF = B ? 5 : 3, NK = B ? 4 : 2, KP = 544 + NK * 256, CH = NF * 1024 + 4 * KP;
};
constexpr int CV_Y = 4 * CvL<false>::CH, CV_YP = 272;
static_assert(CV_Y + 32 * CV_YP <= CV_W && 2 * CvL<true>::CH <= CV_Y, "conv lds");
DEV int cv_chan(int u, int i) { return 16 * (u % 40) + 4 * i + u / 40; }
template <bool B> DEV void cv_filt(const Params& p, u32 dst, int d) {
    const u32 lane = threadIdx.x & 63;
    const v4u sf = mk_srd(p.ssmk + d * SEQ, SEQ * 2);
    DMANT(sf, dst, lane < 4u ? OOB : lane * 16u - 64u, 0);
#pragma unroll
    for (int i = 1; i < CvL<B>::NF; ++i) DMANT(sf, uu(dst + i * 1024), i == CvL<B>::NF - 1 && lane >= 4u ? OOB : lane * 16u, i * 1024 - 64);
}
template <bool B> DEV void cv_kvld(rsrc_t rs, int d, v4u* k) {
#pragma unroll
    for (int i = 0; i < CvL<B>::NK; ++i) k[i] = ld16s(rs, (threadIdx.x & 63) * 16u, uu(W_KVT + (u32)d * (SEQ * 2) + i * 1024));
}
template <bool B> DEV void cv_kvput(lptr F, const v4u* k) {
    const int lane = threadIdx.x & 63;
#pragma unroll
    for (int i = 0; i < CvL<B>::NK; ++i) lds_w16(F + CvL<B>::NF * 1024 + (lane & 3) * CvL<B>::KP + 512 + i * 256 + (lane >> 2) * 16, k[i]);
}
template <bool B> DEV void cv_zero(lptr F) {
    const int lane = threadIdx.x & 63;
#pragma unroll
    for (int i = 0; i < 4; ++i) lds_w16(F + (lane >> 5) * CvL<B>::CH + CvL<B>::NF * 1024 + i * CvL<B>::KP + (lane & 31) * 16, (v4u){0u, 0u, 0u, 0u});
}
#define M32V(d, a, b) asm volatile("v_mfma_f32_32x32x16_bf16 %0, %1, %2, %0" : "+v"(d) : "v"(a), "v"(b))
template <bool B> DEV void cv_run(const Params& p, rsrc_t rs, lptr F, int d, u32 D, f32x4 sp, float (&y)[16]) {
    constexpr int KP = CvL<B>::KP, FO = B ? 2048 : 0;
    const int lane = threadIdx.x & 63, n = lane & 31, hh = lane >> 5, c = lane & 3;
    lptr fa = F + 64 + (32 * hh + 16 * (c & 1) + 8 * ((lane >> 4) & 1) + 4 * (c >> 1) - (lane & 12)) * 2;
    lptr kl = F + CvL<B>::NF * 1024 + (32 + n - hh) * 16;
    v4u dg;
#pragma unroll
    for (int q = 0; q < 4; ++q) { const int k = (lane & 35) ? -1 : (n & 4) + (n >> 3); dg[q] = k == 2 * q ? D : k == 2 * q + 1 ? D << 16 : 0u; }
    v4u l[4], h[4]; v2u fc[4], fh[4];
    f32x16 acc[4];
#pragma unroll
    for (int j = 0; j < 4; ++j) { l[j] = lds_r16(kl + j * KP); if constexpr (B) h[j] = lds_r16(kl + j * KP + 512); }
#pragma unroll
    for (int j = 0; j < 2; ++j) { fc[j] = lds_tr(fa + FO - 32 * j); if constexpr (B) fh[j] = lds_tr(fa - 32 * j); }
    asm volatile("s_nop 1" : "+v"(dg));
#pragma unroll
    for (int j = 0; j < 4; ++j) asm volatile("v_mfma_f32_32x32x16_bf16 %0, %1, %2, 0" : "=&v"(acc[j]) : "v"(dg), "v"(B ? h[j] : l[j]));
    constexpr int U = B ? 2 : 4;
    kl -= 32 * U; fa -= 32;
#pragma unroll 1
    for (int m = 0; m < 32; m += 2 * U) {
#pragma unroll
        for (int z = 0; z < U; ++z) {
            const int c = 2 * (z & 1);
            const v4u f = __builtin_shufflevector(fc[c], fc[c + 1], 0, 1, 2, 3), g = __builtin_shufflevector(fh[c], fh[c + 1], 0, 1, 2, 3);
#pragma unroll
            for (int j = 0; j < 4; ++j) {
                M32V(acc[j], f, l[j]); l[j] = lds_r16(kl + j * KP + 32 * (U - z - 1));
                if (j < 2) fc[(c ^ 2) + j] = lds_tr(fa + FO + (z + 1) * 128 + 32 - 32 * j);
                SCHED();
            }
            if constexpr (B) {
#pragma unroll
                for (int j = 0; j < 4; ++j) {
                    M32V(acc[j], g, h[j]); h[j] = lds_r16(kl + j * KP + 512 + 32 * (U - z - 1));
                    if (j < 2) fh[(c ^ 2) + j] = lds_tr(fa + (z + 1) * 128 + 32 - 32 * j);
                    SCHED();
                }
            }
        }
        kl -= 32 * U; fa += 128 * U;
    }
    asm volatile("s_nop 11" : "+v"(acc[0]), "+v"(acc[1]), "+v"(acc[2]), "+v"(acc[3]));
    const float o[3] = {acc[1][15] + acc[2][14] + acc[3][13], acc[2][15] + acc[3][14], acc[3][15]};
    if constexpr (!B) st16(rs, lane == 63 ? W_SP + (u32)d * 16u : OOB, (v4u){u_(o[0]), u_(o[1]), u_(o[2]), 0u});
#pragma unroll
    for (int i = 0; i < 16; ++i) {
        float v = acc[0][i];
#pragma unroll
        for (int j = 1; j < 4; ++j) if (j <= i) v += acc[j][i - j];
        if (i < 3) { const float e = f_((u32)__builtin_amdgcn_ds_bpermute((hh ? lane - 32 : lane + 31) * 4, (int)u_(o[i]))); v += lane == 0 ? sp[i] : e; }
        y[i] = v;
    }
}
DEV void cv_put(lptr F, int pr, const float (&y0)[16], const float (&y1)[16]) {
    const int lane = threadIdx.x & 63;
#pragma unroll
    for (int i = 0; i < 16; ++i) *(LAS u32*)(F + CV_Y + (lane & 31) * CV_YP + (lane >> 5) * 128 + i * 8 + pr * 4) = pk(y0[i], y1[i]);
}
DEV void cv_out(rsrc_t rs, lptr F, int u, int blk0) {
    const int lane = threadIdx.x & 63;
    lptr q = F + CV_Y + ((lane >> 2) & 3) * CV_YP + ((lane >> 4) * 4 + (lane & 3)) * 8;
    const u32 yo = W_Y + (u32)(((lane >> 2) * 8 + blk0) * DM + 4 * u + (lane & 3)) * 16u;
#pragma unroll
    for (int i = 0; i < 8; ++i) { lptr a = q + (i >> 1) * (8 * CV_YP) + (i & 1) * 128; st16(rs, yo + (u32)((i & 1) * 128 + (i >> 1)) * (DM * 16), tr2(a, a + 4 * CV_YP)); }
}
struct KvIn { v2u k[19], v[16]; v4u s[2]; };
DEV void kvt_load(const Params& p, int tile, KvIn& in) {
    const int lane = threadIdx.x & 63;
    const rsrc_t rk = mk_rsrc(p.bk, ACT), rv = mk_rsrc(p.bv, ACT), rh = mk_rsrc(p.shift, DM * 8);
    const int row = (tile / 10) * 64 + (lane >> 4) * 16, col = (tile % 10) * 64 + (lane & 15) * 4;
    const u32 vo = (u32)(row * DM + col) * 2u, vp = row > 0 ? vo - 3 * (DM * 2) : OOB;
#pragma unroll
    for (int i = 0; i < 2; ++i) in.s[i] = ldin16(rh, (u32)col * 8u + i * 16, 0);
#pragma unroll
    for (int i = 0; i < 19; ++i) { in.k[i] = i < 3 ? ldin8(rk, vp, i * (DM * 2)) : ldin8(rk, vo, (i - 3) * (DM * 2)); if (i >= 3) in.v[i - 3] = ldin8(rv, vo, (i - 3) * (DM * 2)); }
}
DEV void kvt_make(rsrc_t rs, lptr xt, int tile, const KvIn& in) {
    const int lane = threadIdx.x & 63;
#pragma unroll
    for (int c = 0; c < 2; ++c) {
        const v4u sv = in.s[c];
        const f32x2 sh[4] = {{lo_f(sv.x), lo_f(sv.z)}, {hi_f(sv.x), hi_f(sv.z)}, {lo_f(sv.y), lo_f(sv.w)}, {hi_f(sv.y), hi_f(sv.w)}};
        f32x2 kf[19];
#pragma unroll
        for (int i = 0; i < 19; ++i) kf[i] = up2(in.k[i][c]);
        u32 kv[16];
#pragma unroll
        for (int i = 0; i < 16; ++i) kv[i] = pk2(((kf[i] * sh[3] + kf[i + 1] * sh[2]) + (kf[i + 2] * sh[1] + kf[i + 3] * sh[0])) * up2(in.v[i][c]));
#pragma unroll
        for (int q = 0; q < 8; ++q) {
            const u32 sel = (q & 1) ? 0x07060302u : 0x05040100u; const u32* z = kv + (q >> 1);
            *(LAS v2u*)(xt + ((2 * c + (q & 1)) * 16 + (lane & 15)) * 144 + (lane >> 5) * 64 + (q >> 1) * 16 + ((lane >> 4) & 1) * 8) =
                (v2u){__builtin_amdgcn_perm(z[4], z[0], sel), __builtin_amdgcn_perm(z[12], z[8], sel)};
        }
    }
    const u32 o = W_KVT + (u32)(((tile % 10) * 64 + 4 * (lane >> 3)) * SEQ + (tile / 10) * 64 + (lane & 7) * 8) * 2u;
#pragma unroll
    for (int i = 0; i < 8; ++i) st16(rs, o + (u32)(32 * (i & 1) + (i >> 1)) * (SEQ * 2), lds_r16(xt + (8 * i + (lane >> 3)) * 144 + (lane & 7) * 16));
}
struct RowX { v2u a, b; u32 c; };
DEV void ln_row(rsrc_t rs, int row, const RowX& r) {
    const f32x2 x[5] = {up2(r.a.x), up2(r.a.y), up2(r.b.x), up2(r.b.y), up2(r.c)};
    const f32x2 st = ln_stats(x);
    u32 hp[5];
#pragma unroll
    for (int q = 0; q < 5; ++q) hp[q] = pk2(up2(pkv(x[q] - st.x)) * st.y);
    store_h(rs, W_H1, row, hp);
}
DEV void pre_rings(const Params& p, rsrc_t rs, int j) {
    const u32 gi = (u32)(j * 256 + threadIdx.x), gt = gi * 16u, R = NPRE * 4096, n = ((gt >> 14) << 9) + ((gt & 8191u) >> 4);
    v4u m = {PSENT, PSENT, PSENT, PSENT};
    if (n < 640u) {
        m = *(const v4u*)(p.mem0 + (2 * ((n >> 2) & 3) + (n & 1)) * DM + (n >> 4) * 16 + (n & 2) * 4);
#pragma unroll
        for (int i = 0; i < 4; ++i) m[i] = m[i] == PSENT ? 0x7FC17FC1u : m[i];
    }
    st4s(rs, gi - 36u * 256u < F_CLR ? foff(gi - 36u * 256u) : OOB, 0, 0u);
#pragma unroll 1
    for (u32 o = gt; o < MB; o += R) st16(rs, W_ORING + o, (v4u){PSENT, PSENT, PSENT, PSENT});
    const u32 mo = gt < 4 * 32768 + 16384 ? W_MRING + gt : OOB;
    st16(rs, mo, m);
    st16(rs, mo + MB / 4, m);
    static_assert(NPRE * 4096 >= 4 * 32768 + 16384, "memory ring");
}
constexpr int LN0 = NPRE * 4;
DEV void conv_stage(const Params& p, rsrc_t rs, lptr lds, int j, int g0, int ge, v2u ep) {
    const int tid = threadIdx.x, lane = tid & 63, w = uni(tid >> 6), u = j * 4 + w;
    lptr F = lds + w * CV_W;
    const u32 fb = uu(w * CV_W);
    const rsrc_t rd = mk_rsrc(p.ssmD, DM * 2);
    float y0[16], y1[16];
    if (j < NPRE) {
        constexpr int CH = CvL<false>::CH;
        const rsrc_t rx = mk_rsrc(p.x, ACT);
        KvIn in;
        RowX r;
        r.a = ldin8(rx, lane * 8u, uu(u * (DM * 2))); r.b = ldin8(rx, lane * 8u + 512u, uu(u * (DM * 2)));
        r.c = __builtin_amdgcn_raw_buffer_load_b32(rx, lane * 4u + 1024u, uu(u * (DM * 2)), 2);
        kvt_load(p, u, in);
        pre_rings(p, rs, j);
        ln_row(rs, u, r);
        DRAIN(); BAR();
        pre_post(rs, j, 1u, ep);
        if (j == 20) LST(260, rt32());
        const u32 D = __builtin_amdgcn_raw_buffer_load_b16(rd, (u32)cv_chan(u, lane & 3) * 2u, 0, 0);
#pragma unroll 1
        for (int i = 0; i < 4; ++i) cv_filt<false>(p, fb + i * CH, cv_chan(u, i));
        kvt_make(rs, F + CV_Y, u, in);
        cv_zero<false>(F); cv_zero<false>(F + 2 * CH);
        DRAIN(); BAR();
        pre_post(rs, j, 2u, ep);
        if (j == 20) LST(262, rt32());
        pre_wait(rs, 0, NPRE, 2u, ep);
        if (j == 20) LST(281, rt32());
        {
            v4u k[8];
#pragma unroll
            for (int i = 0; i < 4; ++i) cv_kvld<false>(rs, cv_chan(u, i), k + 2 * i);
#pragma unroll
            for (int i = 0; i < 4; ++i) cv_kvput<false>(F + i * CH, k + 2 * i);
        }
        CFENCE();
        kvt_load(p, u + LN0, in);
        if (j == 20) LST(286, rt32());
#pragma unroll 1
        for (int i = 0; i < 4; i += 2) {
            cv_run<false>(p, rs, F + i * CH, cv_chan(u, i), rdl(D, i), f32x4{}, y0);
            cv_run<false>(p, rs, F + (i + 1) * CH, cv_chan(u, i + 1), rdl(D, i + 1), f32x4{}, y1);
            cv_put(F, i >> 1, y0, y1);
        }
        if (j == 20) LST(287, rt32());
        cv_out(rs, F, u, 0);
        DRAIN(); BAR();
        pre_post(rs, j, 3u, ep);
        if (j == 20) LST(263, rt32());
        kvt_make(rs, F + CV_Y, u + LN0, in);
    } else pol_probe(rs, u - NPRE * 4);
    DRAIN(); BAR();
    if (g0 < ge) {
        constexpr int CH = CvL<true>::CH;
        pre_post(rs, j, 4u, ep);
        cv_zero<true>(F);
        pre_wait(rs, 0, NPRE, 4u, ep);
        v4u k[4];
        auto fetch = [&](u32 dst, int d) { cv_filt<true>(p, dst, d); cv_kvld<true>(rs, d, k); };
        fetch(fb, cv_chan(g0 * 4 + w, 0));
#pragma unroll 1
        for (int g = g0; g < ge; g += 8) {
            const int un = g * 4 + w;
            const u32 ch = (u32)cv_chan(un, lane & 3);
            u32 D = __builtin_amdgcn_raw_buffer_load_b16(rd, ch * 2u, 0, 0);
            v4u sp = ld16s(rs, W_SP + ch * 16u, 0);
            asm volatile("" : "+v"(D), "+v"(sp));
            if (g == 16) { LST(282, rt32()); LST(288, __builtin_amdgcn_s_memtime()); }
#pragma unroll 1
            for (int i = 0; i < 4; i += 2) {
                cv_kvput<true>(F, k);
                fetch(fb + CH, cv_chan(un, i + 1));
                cv_run<true>(p, rs, F, cv_chan(un, i), rdl(D, i), (f32x4){f_(rdl(sp.x, i)), f_(rdl(sp.y, i)), f_(rdl(sp.z, i)), 0.f}, y0);
                cv_kvput<true>(F + CH, k);
                fetch(fb, i == 0 ? cv_chan(un, 2) : cv_chan(min(un + 32, 159), 0));
                cv_run<true>(p, rs, F + CH, cv_chan(un, i + 1), rdl(D, i + 1), (f32x4){f_(rdl(sp.x, i + 1)), f_(rdl(sp.y, i + 1)), f_(rdl(sp.z, i + 1)), 0.f}, y1);
                cv_put(F, i >> 1, y0, y1);
            }
            cv_out(rs, F, un, 4);
            if (g == 16) { LST(283, rt32()); LST(289, __builtin_amdgcn_s_memtime()); }
        }
        DRAIN(); BAR();
    }
    pre_post(rs, j, 5u, ep);
    if (j == 20) LST(284, rt32());
    if (j == 3) LST(285, rt32());
}

#define M32A(first, d, a, b) do { if (first) asm volatile("v_mfma_f32_32x32x16_bf16 %0, %1, %2, 0" : "=&v"(d) : "a"(a), "v"(b)); \
                                  else asm volatile("v_mfma_f32_32x32x16_bf16 %0, %1, %2, %0" : "+v"(d) : "a"(a), "v"(b)); } while (0)
#define M32B(first, d, a, b) do { if (first) asm volatile("v_mfma_f32_32x32x16_bf16 %0, %1, %2, 0" : "=&v"(d) : "v"(a), "a"(b)); \
                                  else asm volatile("v_mfma_f32_32x32x16_bf16 %0, %1, %2, %0" : "+v"(d) : "v"(a), "a"(b)); } while (0)
#define VCVT(d, a, b) asm volatile("v_cvt_pk_bf16_f32 %0, %1, %2" : "=v"(d) : "v"(a), "v"(b))
#define VADD(d, a, b) asm volatile("v_add_f32 %0, %1, %2" : "=v"(d) : "v"(a), "v"(b))
#define LD4A(d, vo, so) asm volatile("buffer_load_dword %0, %1, %2, %3 offen sc1" : "=v"(d) : "v"(vo), "s"(srd), "s"(so) : "memory")
DEV u32 wst_src(int lane, int ld) { return (u32)((lane >> 4) * ld + 8 * ((lane & 15) ^ ((lane >> 4) << 2))) * 2u; }
DEV u32 wst_rd(int lane, int w) { const int q4 = (lane >> 2) & 3; return (u32)(q4 * 256 + ((w ^ q4) * 64) + ((lane >> 4) & 1) * 32 + (lane & 3) * 8); }

constexpr int QK_SEL = 81920;
#define QK_LEAD 12
#define QK_SMP 56
#define QK_W0 8
DEV void role_qkv(const Params& p, lptr lds, int c, v2u ep) {
    const int tid = threadIdx.x, lane = tid & 63, w = uni(tid >> 6), hh = lane >> 5, r32 = lane & 31;
    const int m = c >> 5, h = c & 31;
    const rsrc_t rs = mk_rsrc(p.ws, W_END);
    v4u wt[40];
    {
        const v4u sw = mk_srd(m == 0 ? p.Wq : m == 1 ? p.Wk : p.Wv, DM * NQ * 2), sx = mk_srd(p.x, ACT);
        const u32 vo = wst_src(lane, NQ) + (u32)h * (DH * 2), x8 = (u32)lane * 8u, x4 = (u32)lane * 4u;
        RowX xr[5];
        auto wfill = [&](int i0, int n) {
#pragma unroll 1
            for (int i = i0; i < i0 + n; ++i) DMAIN(sw, uu((4 * i + w) * 1024), vo, uu((16 * i + 4 * w) * (NQ * 2)));
        };
        wfill(0, QK_W0);
#pragma unroll
        for (int i = 0; i < 5; ++i)
            asm volatile("buffer_load_dwordx2 %0, %3, %5, %6 offen\n\tbuffer_load_dwordx2 %1, %3, %5, %6 offen offset:512\n\tbuffer_load_dword %2, %4, %5, %6 offen offset:1024"
                         : "=&v"(xr[i].a), "=&v"(xr[i].b), "=&v"(xr[i].c) : "v"(x8), "v"(x4), "s"(sx), "s"(uu((LN0 + 4 * c + w + 384 * i) * (DM * 2))) : "memory");
        wfill(QK_W0, 40 - QK_W0);
        if (c == 3) LST(275, rt32());
        VMW(40 - QK_W0);
        if (c == 3) LST(276, rt32());
#pragma unroll
        for (int i = 0; i < 5; ++i) {
            asm volatile("" : "+v"(xr[i].a), "+v"(xr[i].b), "+v"(xr[i].c));
            const int row = LN0 + 4 * c + w + 384 * i;
            if (i < 4 || row < SEQ) ln_row(rs, row, xr[i]);
        }
        if (c == 3) LST(264, rt32());
        VMW(12); BAR();
        lptr tb = lds + hh * 2048 + wst_rd(lane, w);
        SFOR(k, 40)
            lptr q = tb + (k >> 3) * 32768 + (k & 7) * 4096; wt[k] = tr2(q, q + 1024); asm volatile("" : "+a"(wt[k]));
        SEND
        DRAIN(); BAR();
        pre_post(rs, 96 + c, 1u, ep);
    }
    if (c == 3) LST(268, rt32());
    bool dead = false;
    int n0, n1;
    const u32 t16 = (u32)tid * 16u, wb = uu(w * 1024);
    u32 ar[16];
#pragma unroll
    for (int j = 0; j < 16; ++j) ar[j] = (u32)(r32 * 256 + ((2 * (j & 7) + hh) ^ (r32 & 15)) * 16 + (j >> 3) * 49152);
    MK_SRD();
#define XRD(y, ks) lds_r16(lds + ar[((ks) & 7) + ((ks) >= 24 ? 8 : 0)] + ((ks) >> 3) % 3 * 16384 + (y) * 8192)
#define QDMA(y, i, vo, so) DMA16I(wb, ((i) >> 1) * 16384 + ((i) & 1) * 4096 + (y) * 8192, vo, so, (i) * 4096)
    const u32 h2flag = (lane < 32 && w == 0) ? foff(F_H2 + lane) : OOB;
    const u32 ol = (m == 2 ? (u32)(2 * w + (r32 >> 4)) * 1024u : (u32)((r32 >> 4) * 4 + w) * 1024u) + (u32)(hh * 16 + (r32 & 15)) * 16u
                 + (m == 2 ? W_VO : m == 1 ? W_KQ : W_KQ + 8192u) + (u32)h * 16384u;
    const u32 fo = tid < 2 ? foff(F_QKV + tid * 96 + c) : OOB;
    lptr selw = lds + QK_SEL + (w == 0 ? 0 : 64), selr = lds + QK_SEL;
    auto late = [&](u32 fv) { const u32 need = n1 >= NSEG ? 0xFFFFFFFFu : dead ? 0u : (u32)n1 + 1u; return (u32)__builtin_amdgcn_ballot_w64(fv < need); };
    static_assert(W_H2 - W_H1 == 2 * 64 * (32 * DM * 2), "h");
    pre_wait(rs, 0, NPRE, 1u, ep);
    if (c == 3) LST(269, rt32());
    auto run = [&](auto isv) __attribute__((always_inline)) {
        constexpr bool V = decltype(isv)::value;
        f32x16 ax, ay = {};
        v4u x[8], pp, pq;
        u32 oy = OOB, lt = 1, fcnt = 0, fa = OOB;
        u32 nn = 0;
        PIN(lt); PIN(nn);
#pragma unroll
        for (int z = 0; z < 5; ++z) x[z] = XRD(1, 20 + z);
#define QPK(r_, a1, a_, q_) do { if constexpr (V) VCVT(r_[q_], a1[4 * (a_) + 8 * ((q_) >> 1) + 2 * ((q_) & 1)], a1[4 * (a_) + 8 * ((q_) >> 1) + 2 * ((q_) & 1) + 1]); \
                                 else VCVT(r_[q_], a1[4 * (a_) + (q_)], a1[4 * (a_) + (q_) + 8]); } while (0)
#define QPK4(r_, a1, a_) do { QPK(r_, a1, a_, 0); QPK(r_, a1, a_, 1); QPK(r_, a1, a_, 2); QPK(r_, a1, a_, 3); } while (0)
        for (;;) {
            u32 ls = uu(lt);
            n0 = uni((int)nn); n1 = n0 >> 8; n0 &= 255;
            if (ls != 0u && n0 >= min(NSEG, n1 + QK_LEAD)) {
                CFENCE();
                u32 fv = ld4(rs, h2flag);
                asm volatile("s_waitcnt vmcnt(0)" : "+v"(fv) :: "memory");
                *(LAS u32*)(selw + 16) = late(fv);
                BAR();
                st4s(rs, fa, 0, fcnt);
                fa = OOB;
                if (n1 >= NSEG) __builtin_amdgcn_endpgm();
                if (uu(*(LAS u32*)(selr + 16)) != 0u) {
                    if (w == 0) wait_flags(rs, h2flag, (u32)n1 + 1, dead);
                    BAR();
                }
                ls = 0u;
            }
            if (n0 == 4) pre_wait(rs, 96, 96, 1u, ep);
            int cur = n0;
            asm volatile("s_cmp_eq_u32 %3, 0\n\ts_cselect_b32 %0, %4, %0\n\ts_addc_u32 %2, %2, 0\n\ts_cmp_lg_u32 %3, 0\n\ts_addc_u32 %1, %1, 0"
                         : "+s"(cur), "+s"(n0), "+s"(n1) : "s"(ls), "s"(n1 | 64) : "scc");
            u32 xs = W_H1 + (u32)(cur + (cur & 64)) * (32 * DM * 2), ys, yvo, ox, fv;
            PINS(xs);
#define QT(k) do {} while (0)
            QT(0);
            SCHED();
            QDMA(0, 0, t16, xs); QDMA(0, 1, t16, xs);
            SFOR(z, 80)
                constexpr int ks = (z + 20) % 40, zn = (z + 5) % 80;
                if constexpr (z >= 20 && z < 60) { if constexpr (V) M32B(ks == 0, ax, x[z & 7], wt[ks]); else M32A(ks == 0, ax, wt[ks], x[z & 7]); }
                else { if constexpr (V) M32B(ks == 0, ay, x[z & 7], wt[ks]); else M32A(ks == 0, ay, wt[ks], x[z & 7]); }
                x[(z + 5) & 7] = XRD(zn < 20 || zn >= 60, (zn + 20) % 40);
                if constexpr (z == 77) lt = *(LAS u32*)selr;
                SCHED();
                if constexpr (z < 8) QDMA(0, z + 2, t16, xs);
                if constexpr (z == 9) { ys = W_H1 + (u32)n0 * (32 * DM * 2); yvo = t16 | (n0 < NSEG ? 0u : OOB); PINS(ys); PIN(yvo); }
                if constexpr (z == 11) { ox = ol + (u32)cur * (NH * 16384u); PIN(ox); }
                if constexpr (z == 14) { QT(1); asm volatile("s_waitcnt vmcnt(6) lgkmcnt(0)\n\ts_barrier" ::: "memory"); QT(2); }
                if constexpr (z == 15) st4s(rs, fa, 0, fcnt);
                if constexpr (z >= 16 && z < 26) QDMA(1, z - 16, yvo, ys);
                if constexpr (z == 26) QPK4(pp, ay, 0);
                if constexpr (z == 27) st16(rs, oy, pp);
                if constexpr (z == 28) QPK4(pq, ay, 1);
                if constexpr (z == 29) st16(rs, oy + 512, pq);
                if constexpr (z == 30) { QT(3); asm volatile("s_waitcnt vmcnt(13)\n\ts_barrier" ::: "memory"); QT(4); }
                if constexpr (z == 32) { fcnt = lane == 0 ? (u32)n0 : (u32)n1; fa = fo; asm volatile("" : "+v"(fcnt), "+v"(fa)); }
                if constexpr (z == 34) { oy = (ol + (u32)n0 * (NH * 16384u)) | (yvo & OOB); PIN(oy); }
                if constexpr (z == 35) { nn = (u32)(min(n0 + 1, NSEG) | n1 << 8); PIN(nn); }
                if constexpr (z == 54) { asm volatile("s_waitcnt vmcnt(2) lgkmcnt(0)\n\ts_barrier" ::: "memory"); QT(5); }
                if constexpr (z == QK_SMP) LD4A(fv, h2flag, 0);
                if constexpr (z == 62) QPK4(pp, ax, 0);
                if constexpr (z == 63) st16(rs, ox, pp);
                if constexpr (z == 64) QPK4(pq, ax, 1);
                if constexpr (z == 65) st16(rs, ox + 512, pq);
                if constexpr (z == 73) { asm volatile("s_waitcnt vmcnt(2)" : "+v"(fv) :: "memory"); QT(6); ls = late(fv); PINS(ls); }
                if constexpr (z == 74) { lt = ls; PIN(lt); *(LAS u32*)selw = lt; }
                if constexpr (z == 76) asm volatile("s_waitcnt lgkmcnt(2)\n\ts_barrier" ::: "memory");
                SCHED();
            SEND
        }
    };
    if (m == 2) run(IC<1>{}); else run(IC<0>{});
}

#define OU_SMP 44
DEV void role_out(const Params& p, lptr lds, int L, int kq, int ng, v2u ep) {
    const int tid = threadIdx.x, lane = tid & 63, w = uni(tid >> 6), hh = lane >> 5, r32 = lane & 31;
    const rsrc_t rs = mk_rsrc(p.ws, W_END);
    v4u wo[64];
    bool dead = false;
    {
        const v4u sw = mk_srd(p.Wo, DM * NQ * 2);
        const u32 vo = wst_src(lane, DM) + (u32)ng * 256u;
        auto fill = [&](int b) {
#pragma unroll 1
            for (int i = 0; i < 8; ++i) DMAIN(sw, uu((b % 5) * 32768 + (4 * i + w) * 1024), vo, uu((kq * 1024 + b * 128 + 16 * i + 4 * w) * (DM * 2)));
        };
#pragma unroll 1
        for (int b = 0; b < 5; ++b) fill(b);
        lptr tb = lds + hh * 1024 + wst_rd(lane, w);
        SFOR(b, 8)
            VMW(b < 4 ? 32 : 56 - 8 * b); BAR();
            lptr q = tb + (b % 5) * 32768;
#pragma unroll
            for (int k = 0; k < 8; ++k) { lptr a = q + (k >> 1) * 8192 + (k & 1) * 2048; wo[8 * b + k] = tr2(a, a + 4096); if (b < 7) asm volatile("" : "+a"(wo[8 * b + k])); else PIN(wo[8 * b + k]); }
            if constexpr (b < 3) { BAR(); fill(b + 5); }
        SEND
    }
    pre_wait(rs, 0, NPRE, 1u, ep);
    if (kq == 0 && ng == 0) LST(272 + L, rt32());
    MK_SRD();
    const u32 ofl = lane < 8 ? foff(F_O + L * 32 + kq * 8 + lane) : OOB, l16 = (u32)lane * 16u;
    const u32 fo = (tid == 0) ? foff(F_P + L * 32 + kq * 5 + ng) : OOB;
    const u32 src0 = uu(W_VO + 8192u + (u32)(L * NSEG * NH + kq * 8) * 16384u + w * 1024);
#define ODMA(i, dst, so) DMA16I(dst, (i) * 4096, l16, so, ((i) >> 1) * 16384 + ((i) & 1) * 4096)
    auto first = [&](int s) { const u32 dst = uu((s & 1) * 65536 + w * 1024), src = src0 + (u32)s * (NH * 16384u); SFOR(i, 10) ODMA(i, dst, src); SEND };
    u32 pa = W_P + (u32)(L * NSEG) * P_ITEM + (u32)(4 * hh * 4 + kq) * (DM * 4) + (u32)(ng * 128 + w * 32 + r32) * 4u, fv = 0;
    bool early = false;
    for (int s = 0; s < NSEG; ++s) {
        const u32 dst = uu((s & 1) * 65536 + w * 1024), src = src0 + (u32)s * (NH * 16384u);
        lptr A = lds + (s & 1) * 65536 + l16;
        if (!early) {
            if (!all_(ofl == OOB || fv >= (u32)s + 1)) wait_flags(rs, ofl, (u32)s + 1, dead);
            LS3(L == 0 && w == 1, 1, kq * 5 + ng);
            first(s);
        }
        asm volatile("s_waitcnt vmcnt(8)\n\ts_barrier" ::: "memory");
        v4u o[64];
#pragma unroll
        for (int k = 0; k < 3; ++k) o[k] = lds_r16(A + k * 1024);
        f32x16 acc;
        SCHED();
        SFOR(k, 64)
            if constexpr (k < 56) M32B(k == 0, acc, o[k], wo[k]); else M32V(acc, o[k], wo[k]);
            if constexpr (k < 61) o[k + 3] = lds_r16(A + (k + 3) * 1024);
            SCHED();
            if constexpr (k < 12 && (k & 1) == 0) ODMA(10 + k / 2, dst, src);
            if constexpr (k == 4) asm volatile("s_waitcnt vmcnt(7)\n\ts_barrier" ::: "memory");
            if constexpr (k == 20) asm volatile("s_waitcnt vmcnt(5)\n\ts_barrier" ::: "memory");
            if constexpr (k == 40) asm volatile("s_waitcnt vmcnt(0)\n\ts_barrier" ::: "memory");
            if constexpr (k == OU_SMP) LD4A(fv, ofl, 0);
            SCHED();
        SEND
        asm volatile("s_nop 9" : "+v"(acc), "+v"(pa));
#pragma unroll
        for (int i = 0; i < 16; ++i) st4s(rs, pa, (u32)((8 * (i >> 2) + (i & 3)) * (4 * DM * 4)), u_(acc[i]));
        asm volatile("s_waitcnt vmcnt(16)" : "+v"(fv) :: "memory");
        LS3(L == 0 && w == 1, 2, kq * 5 + ng);
        early = s + 1 < NSEG && all_(ofl == OOB || fv >= (u32)s + 2);
        if (early) { first(s + 1); VMW(10); }
        else { fv = ld4(rs, ofl); asm volatile("s_waitcnt vmcnt(0)" : "+v"(fv) :: "memory"); }
        BAR();
        st4s(rs, fo, 0, (u32)s + 1);
        pa += P_ITEM;
        LS2(L == 0 && w == 1, 160 + kq * 5 + ng);
    }
}

constexpr int RW_P = 20480, RW_X = RW_P + 10240;
DEV void role_row(const Params& p, lptr lds, int id, v2u ep) {
    const int lane = threadIdx.x & 63, w = uni(threadIdx.x >> 6);
    const int ri = id >> 1, L = id & 1;
    const rsrc_t rs = mk_rsrc(p.ws, W_END);
    bool dead = false, have = false;
    const u32 fp = lane < 20 ? foff(F_P + L * 32 + lane) : OOB, l16 = (u32)lane * 16u, lx = lane < 16 ? l16 + 1024u : OOB, fb = uu(w * CV_W);
    lptr F = lds + w * CV_W;
    MK_SRD();
    const v4u sx = mk_srd(p.x, ACT);
    auto yfill = [&](int b) {
#pragma unroll 1
        for (int i = 0; i < 5; ++i) DMA32(uu(fb + (b & 1) * 10240 + i * 2048), l16, uu(W_Y + (u32)(ri * 8 + b) * 10240u + i * 2048));
    };
    auto xfill = [&](int s) {
        const u32 d = uu(fb + RW_X + (s & 1) * 2048), o = uu((u32)(s * 32 + ri) * (DM * 2));
        if (L == 0) { DMAIN(sx, d, l16, o); DMAIN(sx, d + 1024, lx, o); } else { DMA16(d, l16, o + W_X2); DMA16(d + 1024, lx, o + W_X2); }
    };
    auto pfill = [&](int s) {
        const u32 o = uu(W_P + (u32)(L * NSEG + s) * P_ITEM + (u32)ri * (4 * DM * 4));
        SFOR(i, 10) DMA16I(fb, RW_P + i * 1024, l16, o, i * 1024); SEND
        if (L == 1) xfill(s);
    };
    if (L == 0) xfill(0);
    pre_poll(rs, 0, NPRE, 3u, ep);
    if (id == 0) LST(274, rt32());
    yfill(0); DRAIN();
    u32 fv = 0;
    for (int s = 0; s < NSEG; ++s) {
        if (!have) {
            if (!all_(fp == OOB || fv >= (u32)s + 1)) wait_flags(rs, fp, (u32)s + 1, dead);
            LS3(L == 0, 3, ri);
            pfill(s);
        }
        LS2(L == 0, 192 + ri);
        f32x2 y[5];
        {
            lptr ya = F + ((s >> 3) & 1) * 10240 + (s & 7) * 2 + lane * 16;
            auto yr = [&](lptr q) { return lo_f(*(LAS u16*)q); };
            y[0] = (f32x2){yr(ya), yr(ya + 2560)}; y[1] = (f32x2){yr(ya + 5120), yr(ya + 7680)};
            y[2] = (f32x2){yr(ya + 1024), yr(ya + 3584)}; y[3] = (f32x2){yr(ya + 6144), yr(ya + 8704)};
            lptr yc = ya - lane * 16 + 2048 + (lane >> 1) * 16 + (lane & 1) * 5120;
            y[4] = (f32x2){yr(yc), yr(yc + 2560)};
            asm volatile("" : "+v"(y[0]), "+v"(y[1]), "+v"(y[2]), "+v"(y[3]), "+v"(y[4]));
        }
        if (!have) DRAIN();
        LS3(L == 0, 4, ri);
        f32x2 x1[5];
        {
            f32x4 a[4], b[4]; f32x2 c[4];
            lptr q = F + RW_X + (s & 1) * 2048;
            v2u xa = *(LAS v2u*)(q + lane * 8), xb = *(LAS v2u*)(q + 512 + lane * 8); u32 xc = *(LAS u32*)(q + 1024 + lane * 4);
#pragma unroll
            for (int kq = 0; kq < 4; ++kq) { lptr r = F + RW_P + kq * (DM * 4); a[kq] = *(LAS f32x4*)(r + l16); b[kq] = *(LAS f32x4*)(r + 1024 + l16); c[kq] = *(LAS f32x2*)(r + 2048 + lane * 8); }
            SCHED();
            const f32x4 sa = (a[0] + a[1]) + (a[2] + a[3]), sb = (b[0] + b[1]) + (b[2] + b[3]);
            const f32x2 o[5] = {sa.lo, sa.hi, sb.lo, sb.hi, (c[0] + c[1]) + (c[2] + c[3])};
            const u32 xi[5] = {xa.x, xa.y, xb.x, xb.y, xc};
#pragma unroll
            for (int i = 0; i < 5; ++i) x1[i] = up2(xi[i]) + o[i];
            asm volatile("" : "+v"(x1[0]), "+v"(x1[1]), "+v"(x1[2]), "+v"(x1[3]), "+v"(x1[4]));
        }
        have = s + 1 < NSEG && all_(fp == OOB || fv >= (u32)s + 2);
        if (__builtin_expect(have, 0)) pfill(s + 1);
        if (L == 0) xfill(s + 1);
        const f32x2 st1 = ln_stats(x1);
        u32 xp[5];
#pragma unroll
        for (int i = 0; i < 5; ++i) xp[i] = pkv(x1[i] + (x1[i] - st1.x) * (y[i] * st1.y));
        const int row = s * 32 + ri;
        if (L == 0) {
            const u32 ro = (u32)row * (DM * 2);
            f32x2 x2[5];
#pragma unroll
            for (int i = 0; i < 5; ++i) x2[i] = up2(xp[i]);
            st8s(rs, (u32)lane * 8, W_X2 + ro, (v2u){xp[0], xp[1]});
            st8s(rs, (u32)lane * 8 + 512, W_X2 + ro, (v2u){xp[2], xp[3]});
            st4s(rs, (u32)lane * 4 + 1024, W_X2 + ro, xp[4]);
            const f32x2 st2 = ln_stats(x2);
            u32 hp[5];
#pragma unroll
            for (int i = 0; i < 5; ++i) hp[i] = pk2((x2[i] - st2.x) * st2.y);
            store_h(rs, W_H2, row, hp);
        } else {
            u16* o_ = p.out + (size_t)row * DM;
            *(v2u*)(o_ + 4 * lane) = (v2u){xp[0], xp[1]};
            *(v2u*)(o_ + 256 + 4 * lane) = (v2u){xp[2], xp[3]};
            *(u32*)(o_ + 512 + 2 * lane) = xp[4];
        }
        CFENCE();
        LS3(L == 0, 5, ri);
        fv = ld4(rs, fp);
        asm volatile("s_waitcnt vmcnt(0)" : "+v"(fv) :: "memory");
        st4s(rs, lane == 0 && L == 0 ? foff(F_H2 + ri) : OOB, 0, (u32)s + 1);
        if ((s & 7) == 3 && s < NSEG - 8) {
            if (s == 27) pre_poll(rs, 0, NCV, 5u, ep);
            if (s == 43) st8s(rs, (id == 1 && lane == 0) ? foff(F_EPOCH) : OOB, 0, (v2u){ep.x + 8u, ep.y});
            yfill((s >> 3) + 1);
        }
        if (ri == 0) LST(320 + L * 64 + s, rt32());
        LS2(L == 0, 224 + ri);
    }
}

constexpr float SCALE = 0.08838834764831845f, LOG2E = 1.4426950408889634f;
constexpr int C_K = 0, C_V = 8192, C_QX = 16384, C_MEM = 24576, C_Q = C_MEM + 10240, C_KM = C_Q + 2304, C_VM = C_KM + 2304, C_DUMP = C_VM + 2048,
              C_ZERO = C_DUMP + 1024, C_WL = C_ZERO + 128;
constexpr int NLF = 14;
static_assert(C_WL % 16 == 0 && C_WL + 4 * NLF * 2048 <= LDS_BYTES, "head lds");
#define CH_VIS 10
#define CH_H0 124
#define CH_W0 88
#define CH_MH 4
#define CH_MW 4
#define CH_REC 20
#define CH_UP 4
#define CH_WX 6
#define WAIT_TXT(tmp, t4) "s_memrealtime vcc\n1:\n\ts_waitcnt lgkmcnt(0)\n\ts_sub_u32 " tmp ", " t4 ", vcc_lo\n\ts_memrealtime vcc\n\ts_cmpk_lt_u32 " tmp ", 400\n\ts_cbranch_scc1 1b\n\t" \
                          "s_add_u32 " tmp ", " tmp ", 4\n\ts_cselect_b32 " tmp ", " tmp ", 0\n2:\n\ts_sub_u32 " tmp ", " tmp ", 1\n\ts_cbranch_scc0 2b\n\t"
DEV void wait_until(u32 t) { u32 tmp; asm volatile(WAIT_TXT("%0", "%1") "s_waitcnt lgkmcnt(0)" : "=&s"(tmp) : "s"(uu(t - 4u)) : "vcc", "scc", "memory"); }
#define SMF_A(acc, a, b) asm volatile("v_smfmac_f32_16x16x64_bf16 %0, %1, %2, %3" : "+v"(acc) : "v"(a), "a"(b), "v"(idx))
#define SMF_V(acc, a, b) asm volatile("v_smfmac_f32_16x16x64_bf16 %0, %1, %2, %3" : "+v"(acc) : "v"(a), "v"(b), "v"(idx))
#define MF0(d, a, b) asm volatile("v_mfma_f32_16x16x32_bf16 %0, %1, %2, 0" : "=&v"(d) : "v"(a), "v"(b))
#define MF1(d, a, b) asm volatile("v_mfma_f32_16x16x32_bf16 %0, %1, %2, %0" : "+v"(d) : "v"(a), "v"(b))
#define MF0H(d, a, b) asm volatile("v_mfma_f32_16x16x16_bf16 %0, %1, %2, 0" : "=&v"(d) : "v"(a), "v"(b))
#define MF1H(d, a, b) asm volatile("v_mfma_f32_16x16x16_bf16 %0, %1, %2, %0" : "+v"(d) : "v"(a), "v"(b))
DEV v8u tr_frag(lptr p0, lptr p1, lptr p2, lptr p3) { return cat8(tr2(p0, p1), tr2(p2, p3)); }
constexpr int qslot(int ks) { return (ks & 1) && ks < 8 ? ks >> 1 : -1; }
constexpr int kslot(int ks) { return ks < 4 ? 4 + ks : -1; }
constexpr int vslot(int ks) { return ks >= 4 ? 4 + ks : -1; }
DEV u32 ring_map(u32 off, u32 half) { return ((off >> 13) << 14) + (half << 13) + (off & 8191u); }
#define ZRD(x, i) x = __builtin_bit_cast(f32x4, lds_r16(ZP + 16 * (i)))
#define VMAX(d, a, b) asm volatile("v_max_f32 %0, %1, %2" : "=v"(d) : "v"(a), "v"(b))
#define VMAX3(d, a, b, c_) asm volatile("v_max3_f32 %0, %1, %2, %3" : "=v"(d) : "v"(a), "v"(b), "v"(c_))
#define VEXP(d, a) asm volatile("v_exp_f32 %0, %1" : "=v"(d) : "v"(a))
#define VFMC(d, a, b) asm volatile("v_fmamk_f32 %0, %1, 0x3e0293ee, %2" : "=v"(d) : "v"(a), "v"(b))
#define SEL(d, a, b) asm volatile("v_cndmask_b32 %0, %1, %2, %3" : "=v"(d) : "v"(a), "v"(b), "s"(himask))
#define QPERM(d, a, q) asm volatile("v_mov_b32_dpp %0, %1 quad_perm:" q " row_mask:0xf bank_mask:0xf" : "=v"(d) : "v"(a))

DEV void role_head(const Params& p, lptr lds, int L, int h, v2u ep) {
    const int tid = threadIdx.x, lane = tid & 63, w = uni(tid >> 6), g = lane >> 4, r = lane & 15;
    const rsrc_t rs = mk_rsrc(p.ws, W_END);
    v8u wq0[10], wq1[10], wk0[10], wk1[10], wv0[10], wv1[10];
    lptr WL = lds + C_WL + w * (NLF * 2048) + lane * 16;
    {
        constexpr int GS = 1056, CS = 48 * GS;
        const v4u sq = mk_srd(p.Wq, DM * NQ * 2), sk = mk_srd(p.Wk, DM * NQ * 2), sv = mk_srd(p.Wv, DM * NQ * 2);
        const u32 vo = (u32)(g * NQ + h * DH + 8 * (r ^ ((g & 1) << 3))) * 2u;
        auto fill = [&](int ch) {
#pragma unroll 1
            for (int i = 0; i < 4; ++i) {
                const u32 dst = uu((ch % 3) * CS + (4 * i + w) * GS), so = uu((ch * 64 + 16 * i + 4 * w) * (NQ * 2));
                DMAIN(sq, dst, vo, so); DMAIN(sk, dst + 16 * GS, vo, so); DMAIN(sv, dst + 32 * GS, vo, so);
            }
        };
        __builtin_amdgcn_s_sleep(ST_HEAD);
        fill(0); fill(1); fill(2);
        const int q4 = r >> 2;
        lptr tb = lds + (4 * (g >> 1) + 2 * (q4 >> 1) + (g & 1)) * GS + (q4 & 1) * 256 + ((w * 64 + (r & 3) * 8) ^ ((q4 & 1) << 7));
        SFOR(ch, 10)
            VMW(ch < 8 ? 24 : 108 - 12 * ch);
            BAR();
            auto frag = [&](int m, int t) { lptr b = tb + (ch % 3) * CS + m * 16 * GS + t * 32; return tr_frag(b, b + 512, b + 8 * GS, b + 8 * GS + 512); };
            wq0[ch] = frag(0, 0); wq1[ch] = frag(0, 1); wk0[ch] = frag(1, 0); wk1[ch] = frag(1, 1); wv0[ch] = frag(2, 0); wv1[ch] = frag(2, 1);
            asm volatile("" : "+a"(wq0[ch]), "+a"(wk0[ch]), "+v"(wv0[ch]), "+v"(wv1[ch]));
            if constexpr (qslot(ch) < 0) asm volatile("" : "+a"(wq1[ch])); else asm volatile("" : "+v"(wq1[ch]));
            if constexpr (kslot(ch) < 0) asm volatile("" : "+a"(wk1[ch])); else asm volatile("" : "+v"(wk1[ch]));
            BAR();
            if constexpr (ch < 7) fill(ch + 3);
        SEND
#pragma unroll
        for (int ch = 0; ch < 10; ++ch) {
            if (qslot(ch) >= 0) { lds_w16(WL + qslot(ch) * 2048, wq1[ch].lo); lds_w16(WL + qslot(ch) * 2048 + 1024, wq1[ch].hi); }
            if (kslot(ch) >= 0) { lds_w16(WL + kslot(ch) * 2048, wk1[ch].lo); lds_w16(WL + kslot(ch) * 2048 + 1024, wk1[ch].hi); }
            if (vslot(ch) >= 0) { lds_w16(WL + vslot(ch) * 2048, wv1[ch].lo); lds_w16(WL + vslot(ch) * 2048 + 1024, wv1[ch].hi); }
        }
        CFENCE();
    }
    const u32 idx = (r & 2) ? 0xEEEEu : 0x4444u;
    const unsigned long long himask = 0xCCCCCCCCCCCCCCCCull;
    bool dead = false;
    if (h == 3) LST(266 + L, rt32());
    pre_wait(rs, 0, NCV, 1u, ep);
    const u32 pol0 = uu(get_pol(rs));
    const u32 l16 = (u32)lane * 16u;
    const u32 mring = W_MRING + (u32)L * (MB / 4) + pol0 * 8192u, hint = mring + 4 * 32768u;
    const u32 fkh = lane < 3 ? foff(F_QKV + L * 96 + lane * 32 + h) : lane < 6 ? hint + (u32)(lane - 3) * 4u : OOB;
    u32 tm = 0;
    MK_SRD();
    u32 ksrc = uu(W_KQ + (u32)(L * NSEG * NH + h) * 16384u + w * 2048), vsrc = ksrc + (W_VO - W_KQ);
    u32 dk = uu(C_K + w * 2048), dv = dk + (C_V - C_K), dq = dk + (C_QX - C_K), dm = uu(C_MEM + w * 2048), dm2 = uu(w < 2 ? C_MEM + 8192 + w * 1024 : C_DUMP);
    asm volatile("" : "+s"(dk), "+s"(dv), "+s"(dq), "+s"(dm), "+s"(dm2));
    wait_flags(rs, lane < 3 ? fkh : OOB, 1u, dead);
    DMA32(dk, l16, ksrc); DMA32(dv, l16, vsrc); DMA32(dq, l16, ksrc ^ 8192u);
    lptr qw = lds + C_Q + (2 * g) * 272 + (32 * w + 2 * r) * 2;
    lptr qr = lds + C_Q + (2 * (r >> 2) + (r & 1)) * 272 + g * 16;
    lptr LB = lds + l16, VX = lds + C_V + w * 2048 + l16;
    if (tid >= 64 && tid < 70) { const u32 z = tid == 68 ? 0x3F803F80u : tid == 69 ? PSENT : 0u; lds_w16(lds + C_ZERO + (tid - 64) * 16, (v4u){z, z, z, z}); }
    u32 zaddr = 0; PIN(zaddr);
    lptr ZP = lds + zaddr + C_ZERO;
    const float CEXP = SCALE * LOG2E;
#define LF_RD(slot, hf) lf[slot][hf] = lds_r16(WL + (slot) * 2048 + (hf) * 1024)
    BAR();
    const u32 ofl = tid == 0 ? foff(F_O + L * 32 + h) : OOB;
    u32 oso = (g & 1) ? OOB : W_ORING + (u32)L * (MB / 2) + ring_map((u32)((r >> 3) * 32768 + h * 1024 + w * 256 + (g >> 1) * 128 + (r & 7) * 16), pol0);
    unsigned long long vis[8];
#pragma unroll
    for (int i = 0; i < 8; ++i) vis[i] = __builtin_amdgcn_ballot_w64(16 * (i >> 2) + 4 * g + (i & 3) <= 16 * (w & 1) + r);
    u32 missed = 0;
    for (int s = 0; s < NSEG;) {
        u32 ms = mring + (u32)(s & 3) * 32768u + w * 2048, ms2 = mring + (u32)(s & 3) * 32768u + 16384u + (w & 1) * 1024, sv = (u32)s;
        asm volatile("" : "+s"(ms), "+s"(ms2)); PIN(sv);
        f32x4 aq0, aq1, ak0, ak1;
        v4u lf[NLF][2];
        ZRD(aq0, 0); ZRD(aq1, 1); ZRD(ak0, 2); ZRD(ak1, 3);
#pragma unroll
        for (int i = 0; i < 4; ++i) { LF_RD(i, 0); LF_RD(i, 1); }
        SCHED();
        v4u A[10];
#define HT(k) do {} while (0)
        v4u kx[8], qf[4], kmf[4], vx[2], ones, P;
        float fa, fb, fc, fd, mx, my, sx[4], t[4], tm0, tm1; u32 pa, pb, fl, e01, e23, pmm; unsigned long long nanq;
        f32x4 av0, av1, s0, s1, sm, oa, ob, rsum;
        v2u vm, pm;
#define LW(sl) cat8(lf[sl][0], lf[sl][1])
#define PQ(ks, tl) do { if constexpr (!(tl)) SMF_A(aq0, A[ks], wq0[ks]); else if constexpr (qslot(ks) < 0) SMF_A(aq1, A[ks], wq1[ks]); else { const v8u lw_ = LW(qslot(ks)); SMF_V(aq1, A[ks], lw_); } } while (0)
#define PK(ks, tl) do { if constexpr (!(tl)) SMF_A(ak0, A[ks], wk0[ks]); else if constexpr (kslot(ks) < 0) SMF_A(ak1, A[ks], wk1[ks]); else { const v8u lw_ = LW(kslot(ks)); SMF_V(ak1, A[ks], lw_); } } while (0)
#define PV(ks, tl) do { if constexpr (!(tl)) SMF_V(av0, A[ks], wv0[ks]); else if constexpr (vslot(ks) < 0) SMF_V(av1, A[ks], wv1[ks]); else { const v8u lw_ = LW(vslot(ks)); SMF_V(av1, A[ks], lw_); } } while (0)
#define KXR(i) kx[i] = lds_r16(LB + C_K + ((i) & 1) * 4096 + ((i) >> 1) * 1024)
#define XW(q_) do { *(LAS u32*)(q_) = pa; *(LAS u32*)((q_) + 272) = pb; } while (0)
        auto slot = [&](auto zc) __attribute__((always_inline)) {
            constexpr int z = decltype(zc)::value;
            if constexpr (z < 14) PQ(z >> 1, z & 1);
            else if constexpr (z < 17) PQ(z - 7, 0);
            else if constexpr (z < 20) PQ(z - 10, 1);
            else if constexpr (z < 33) PK((z - 20) >> 1, z & 1);
            else if constexpr (z < 35) { if constexpr (z == 33) MF0(s0, kx[0], qf[0]); else MF0(s1, kx[1], qf[0]); }
            else if constexpr (z < 41) { if constexpr (z & 1) MF1(s0, kx[z - 33], qf[(z - 33) >> 1]); else MF1(s1, kx[z - 33], qf[(z - 33) >> 1]); }
            else if constexpr (z == 41) PK(6, 1);
            else if constexpr (z < 45) PK(z - 35, 0);
            else if constexpr (z < 48) PK(z - 38, 1);
            else if constexpr (z < 58) PV(z - 48, 0);
            else if constexpr (z < 61) PV(z < 60 ? z - 54 : 0, 1);
            else if constexpr (z == 61) MF0(sm, kmf[0], qf[0]);
            else if constexpr (z < 65) MF1(sm, kmf[z - 61], qf[z - 61]);
            else if constexpr (z == 65) MF0(oa, vx[0], P);
            else if constexpr (z == 66) MF0(ob, vx[1], P);
            else PV(6, 1);
            if constexpr (z < 6) A[4 + z] = lds_r16(LB + C_MEM + (4 + z) * 1024);
            if constexpr (z == 6 || z == 7) LF_RD(4, z - 6);
            if constexpr (z == 8 || z == 9) LF_RD(5, z - 8);
            if constexpr (z == 12 || z == 13) LF_RD(6, z - 12);
            if constexpr (z == 16 || z == 17) KXR(z - 16);
            if constexpr (z == 18 || z == 19) LF_RD(7, z - 18);
            if constexpr (z >= 20 && z < 24) KXR(z - 18);
            if constexpr (z == 32 || z == 33) KXR(z - 26);
            if constexpr (z == 24) XW(qw);
            if constexpr (z == 25) ZRD(av0, 0);
            if constexpr (z == 26) ZRD(av1, 1);
            if constexpr (z == 27) asm volatile("s_waitcnt lgkmcnt(2)\n\ts_barrier" ::: "memory");
            if constexpr (z >= 28 && z < 32) qf[z - 28] = lds_r16(qr + (z - 28) * 64);
            if constexpr (z == 35) fl = ld4(rs, fkh);
            if constexpr (z == 52) XW(qw + (C_KM - C_Q));
            if constexpr (z == 53 || z == 54) vx[z - 53] = lds_r16(VX + (z - 53) * 1024);
            if constexpr (z == 55) asm volatile("s_waitcnt lgkmcnt(2)\n\ts_barrier" ::: "memory");
            if constexpr (z >= 56 && z < 60) kmf[z - 56] = lds_r16(qr + (C_KM - C_Q) + (z - 56) * 64);
            if constexpr (z >= 48 && z < 52) LF_RD(8 + ((z - 48) >> 1), z & 1);
            if constexpr (z >= 60) LF_RD(10 + ((z - 60) >> 1), z & 1);
            SCHED();
            if constexpr (z == 19) HT(2);
            if constexpr (z == 32) HT(3);
            if constexpr (z == 47) HT(4);
            if constexpr (z == 64) HT(5);
            if constexpr (z == 20) VADD(fa, aq0[0], aq0[2]);
            if constexpr (z == 21) VADD(fb, aq0[1], aq0[3]);
            if constexpr (z == 22) { VADD(fc, aq1[0], aq1[2]); VADD(fd, aq1[1], aq1[3]); }
            if constexpr (z == 23) { asm volatile("v_cmp_u_f32 %0, %1, %2" : "=s"(nanq) : "v"(fa), "v"(fb)); VCVT(pa, fa, fc); VCVT(pb, fb, fd); }
            if constexpr (z == 39 || z == 40 || z == 41) NOPS(1);
            if constexpr (z == 42) VMAX3(mx, s0[0], s0[1], s0[2]);
            if constexpr (z == 43) { VMAX3(mx, mx, s0[3], s1[0]); VMAX3(my, s1[1], s1[2], s1[3]); }
            if constexpr (z == 44 || z == 46) { VMAX(fa, mx, my); VMAX(my, mx, my); mx = fa; }
            if constexpr (z == 45) { SEL(sx[0], s0[0], s1[0]); asm volatile("v_permlane16_swap_b32 %0, %1" : "+v"(mx), "+v"(my)); }
            if constexpr (z == 47) { SEL(sx[1], s0[1], s1[1]); asm volatile("v_permlane32_swap_b32 %0, %1" : "+v"(mx), "+v"(my)); }
            if constexpr (z == 48) asm volatile("v_max_f32 %0, %0, %1\n\tv_mul_f32 %0, 0xbe0293ee, %0" : "+v"(mx) : "v"(my));
            if constexpr (z == 49) { VADD(fa, ak0[0], ak0[2]); VADD(fb, ak0[1], ak0[3]); }
            if constexpr (z == 50) { VADD(fc, ak1[0], ak1[2]); VADD(fd, ak1[1], ak1[3]); }
            if constexpr (z == 51) { VCVT(pa, fa, fc); VCVT(pb, fb, fd); }
            if constexpr (z == 52) SEL(sx[2], s0[2], s1[2]);
            if constexpr (z == 53) { SEL(sx[3], s0[3], s1[3]); VFMC(t[0], sx[0], mx); }
            if constexpr (z == 54) { VFMC(t[1], sx[1], mx); VFMC(t[2], sx[2], mx); }
            if constexpr (z == 55) VFMC(t[3], sx[3], mx);
            if constexpr (z >= 56 && z < 60) VEXP(t[z - 56], t[z - 56]);
            if constexpr (z == 60) { VCVT(e01, t[0], t[1]); VCVT(e23, t[2], t[3]); }
            if constexpr (z == 61) { QPERM(P.x, e01, "[0,1,0,1]"); QPERM(P.z, e01, "[2,3,2,3]"); }
            if constexpr (z == 62) { QPERM(P.y, e23, "[0,1,0,1]"); QPERM(P.w, e23, "[2,3,2,3]"); }
            if constexpr (z == 63) { VADD(fa, av0[0], av0[2]); VADD(fb, av0[1], av0[3]); }
            if constexpr (z == 64) VCVT(vm.x, fa, fb);
            if constexpr (z == 67) { asm volatile("" :: "v"(sm)); VFMC(tm0, sm[0], mx); VFMC(tm1, sm[1], mx); }
            SCHED();
        };
        {
            u32 tmp;
            asm volatile("s_mov_b32 m0, %2\n\t" WAIT_TXT("%0", "%1") "buffer_load_dwordx4 %4, %5, %6 offen sc1 lds\n\tbuffer_load_dwordx4 %4, %5, %6 offen offset:1024 sc1 lds\n\t"
                         "s_mov_b32 m0, %3\n\ts_nop 0\n\tbuffer_load_dwordx4 %4, %5, %7 offen sc1 lds\n\ts_waitcnt vmcnt(0) lgkmcnt(0)\n\ts_barrier"
                         : "=&s"(tmp) : "s"(uu(tm - 4u)), "s"(dm), "s"(dm2), "v"(l16), "s"(srd), "s"(ms), "s"(ms2) : "vcc", "scc", "memory");
        }
#pragma unroll
        for (int ks = 0; ks < 4; ++ks) A[ks] = lds_r16(LB + C_MEM + ks * 1024);
        SCHED();
        st4s(rs, ofl, 0, sv);
        SCHED();
        static_for<0, 24>(slot);
        if (__builtin_expect(nanq != 0ull, 0)) {
            bool miss = false;
#pragma unroll
            for (int i = 0; i < 10; ++i) miss |= A[i].x == PSENT || A[i].y == PSENT || A[i].z == PSENT || A[i].w == PSENT;
            nanq = missed < SPIN_CAP ? __builtin_amdgcn_ballot_w64(miss) : 0ull;
        }
        if (__builtin_expect(nanq == 0ull, 1)) {
        static_for<24, 68>(slot);
        PV(7, 1); ones = lds_r16(ZP + 64); SCHED();
        VMAX(fa, tm0, tm1);
        if (__builtin_expect(__builtin_amdgcn_ballot_w64(fa > 24.0f) != 0ull, 0)) {
            const float up = fmaxf(max32(max16(fmaxf(tm0, tm1))), 0.f);
            tm0 -= up; tm1 -= up;
#pragma unroll
            for (int i = 0; i < 4; ++i) t[i] = __builtin_amdgcn_exp2f(__builtin_fmaf(sx[i], CEXP, mx) - up);
            e01 = pk(t[0], t[1]); e23 = pk(t[2], t[3]);
            asm volatile("s_nop 1" : "+v"(e01), "+v"(e23));
            QPERM(P.x, e01, "[0,1,0,1]"); QPERM(P.z, e01, "[2,3,2,3]"); QPERM(P.y, e23, "[0,1,0,1]"); QPERM(P.w, e23, "[2,3,2,3]");
            NOPS(1);
            MF0(oa, vx[0], P); MF0(ob, vx[1], P);
            NOPS(7);
        }
        SCHED();
        PV(1, 1); VEXP(tm0, tm0); SEL(oa[0], oa[0], ob[0]); SCHED();
        PV(8, 1); VEXP(tm1, tm1); SEL(oa[1], oa[1], ob[1]); SCHED();
        PV(9, 1); VCVT(pmm, tm0, tm1); SEL(oa[2], oa[2], ob[2]); SCHED();
        PV(2, 1);
        asm volatile("v_cndmask_b32 %0, %2, 0, %3\n\tv_cndmask_b32 %1, 0, %2, %3" : "=&v"(pm.x), "=&v"(pm.y) : "v"(pmm), "s"(himask)); SCHED();
        PV(3, 1); SEL(oa[3], oa[3], ob[3]); SCHED();
        MF0(rsum, ones, P); NOPS(4);
        VADD(fc, av1[0], av1[2]); VADD(fd, av1[1], av1[3]);
        { const v2u ones2 = ones.lo; MF1H(rsum, ones2, pm); }
        VCVT(vm.y, fc, fd); NOPS(1);
        MF1H(oa, vm, pm);
        {
            float inv; v4u oc;
            asm volatile("s_nop 3\n\tv_rcp_f32 %0, %1\n\ts_nop 2" : "=v"(inv) : "v"(rsum[0]), "v"(rsum));
            PIN(oa);
            const f32x2 i2 = {inv, inv};
            const f32x2 u0 = oa.lo * i2, u1 = oa.hi * i2;
            asm volatile("v_cvt_pk_bf16_f32 %0, %4, %5\n\tv_cvt_pk_bf16_f32 %2, %4, %5\n\tv_cvt_pk_bf16_f32 %1, %6, %7\n\tv_cvt_pk_bf16_f32 %3, %6, %7\n\t"
                         "v_permlane16_swap_b32 %0, %2\n\ts_nop 0\n\tv_permlane16_swap_b32 %1, %3"
                         : "=&v"(oc.x), "=&v"(oc.y), "=&v"(oc.z), "=&v"(oc.w) : "v"(u0.x), "v"(u0.y), "v"(u1.x), "v"(u1.y));
            st16(rs, oso, oc);
        }
        SCHED();
        unsigned long long clk;
        asm volatile("s_memrealtime %0" : "=s"(clk));
        HT(6);
        PIN(fl);
        v4u tq[4], tk[8], tv[2];
        f32x4 c0, c1, cm;
        u32 oo, kvo; bool kvok;
        u32 ln = l16; PIN(ln);
        lptr QX = lds + C_QX + (w & 1) * 4096 + ln, VO = lds + C_V + (w ^ 1) * 2048 + ln, VMX = lds + C_VM + w * 512 + (ln >> 1);
#pragma unroll
        for (int i = 0; i < 2; ++i) { tq[i] = lds_r16(QX + i * 1024); tk[2 * i] = lds_r16(LB + C_K + i * 1024); tk[2 * i + 1] = lds_r16(LB + C_K + 4096 + i * 1024); }
        *(LAS v2u*)VMX = vm;
        SCHED();
        SFOR(i, 12)
            if constexpr (i == 0) MF0(cm, kmf[0], tq[0]); else if constexpr (i == 1) MF0(c0, tk[0], tq[0]); else if constexpr (i == 2) MF0(c1, tk[1], tq[0]);
            else if constexpr (i % 3 == 0) MF1(cm, kmf[i / 3], tq[i / 3]); else if constexpr (i % 3 == 1) MF1(c0, tk[2 * (i / 3)], tq[i / 3]); else MF1(c1, tk[2 * (i / 3) + 1], tq[i / 3]);
            if constexpr (i == 0) { tq[2] = lds_r16(QX + 2048); tk[4] = lds_r16(LB + C_K + 2048); }
            if constexpr (i == 1) { tk[5] = lds_r16(LB + C_K + 6144); tq[3] = lds_r16(QX + 3072); }
            if constexpr (i == 2) { tk[6] = lds_r16(LB + C_K + 3072); tk[7] = lds_r16(LB + C_K + 7168); }
            if constexpr (i == 3) { tv[0] = lds_r16(VO); tv[1] = lds_r16(VO + 1024); }
            if constexpr (i == 4) { oo = ln + (ln & 0x300u) + ((vsrc ^ 8192u) + (u32)((w & 1) * 256)); PIN(oo); }
            if constexpr (i == 5) { ksrc += NH * 16384; vsrc += NH * 16384; PINS(ksrc); PINS(vsrc); }
            if constexpr (i == 7) { kvok = ((u32)__builtin_amdgcn_ballot_w64(fl < (u32)s + 2) & 7u) == 0u; }
            if constexpr (i == 8) { kvo = l16 | (kvok ? 0u : OOB); PIN(kvo); }
            SCHED();
        SEND
        asm volatile("s_waitcnt lgkmcnt(0)\n\ts_barrier" : "+s"(clk) :: "memory");
        const v2u tvm = *(LAS v2u*)(VMX + (512 - (w & 1) * 1024));
        const v4u pv = lds_r16(ZP + 80);
        DMA32(dk, kvo, ksrc);
        SCHED(); asm volatile("" :: "v"(cm));
        float u[10], m, m2;
        {
            float NEG = -1000000000.0f / SCALE; PIN(NEG);
#define MSK(d, a, i) asm volatile("v_cndmask_b32 %0, %1, %2, %3" : "=v"(d) : "v"(NEG), "v"(a), "s"(vis[i]))
#pragma unroll
            for (int i = 0; i < 4; ++i) MSK(u[i], c0[i], i);
#pragma unroll
            for (int i = 0; i < 4; ++i) MSK(u[4 + i], c1[i], 4 + i);
            asm volatile("v_max3_f32 %0, %2, %3, %10\n\tv_max3_f32 %1, %4, %5, %11\n\tv_max3_f32 %0, %0, %6, %7\n\tv_max3_f32 %1, %1, %8, %9\n\t"
                         "v_max_f32 %0, %0, %1\n\tv_mov_b32 %1, %0\n\ts_nop 1\n\tv_permlane16_swap_b32 %0, %1\n\tv_max_f32 %0, %0, %1\n\tv_mov_b32 %1, %0\n\ts_nop 1\n\t"
                         "v_permlane32_swap_b32 %0, %1\n\tv_max_f32 %0, %0, %1\n\tv_mul_f32 %0, 0xbe0293ee, %0"
                         : "=&v"(m), "=&v"(m2) : "v"(u[0]), "v"(u[1]), "v"(u[2]), "v"(u[3]), "v"(u[4]), "v"(u[5]), "v"(u[6]), "v"(u[7]), "v"(cm[0]), "v"(cm[1]));
        }
        DMA32(dv, kvo, vsrc);
        {
            v4u px; v2u p0, p1;
#define EX2(d, a, b) asm volatile("v_fmamk_f32 %0, %2, 0x3e0293ee, %4\n\tv_fmamk_f32 %1, %3, 0x3e0293ee, %4\n\tv_exp_f32 %0, %0\n\tv_exp_f32 %1, %1" : "=&v"(d.x), "=&v"(d.y) : "v"(a), "v"(b), "v"(m))
            f32x2 ex[5];
            EX2(ex[0], u[0], u[1]); EX2(ex[1], u[2], u[3]); EX2(ex[2], u[4], u[5]); EX2(ex[3], u[6], u[7]); EX2(ex[4], cm[0], cm[1]);
            asm volatile("v_cvt_pk_bf16_f32 %0, %8, %9\n\tv_cvt_pk_bf16_f32 %1, %10, %11\n\tv_cvt_pk_bf16_f32 %2, %12, %13\n\tv_cvt_pk_bf16_f32 %3, %14, %15\n\t"
                         "v_mov_b32 %5, 0\n\tv_mov_b32 %6, 0\n\tv_cvt_pk_bf16_f32 %4, %16, %17\n\tv_cvt_pk_bf16_f32 %7, %16, %17"
                         : "=&v"(px.x), "=&v"(px.y), "=&v"(px.z), "=&v"(px.w), "=&v"(p0.x), "=&v"(p0.y), "=&v"(p1.x), "=&v"(p1.y)
                         : "v"(ex[0].x), "v"(ex[0].y), "v"(ex[1].x), "v"(ex[1].y), "v"(ex[2].x), "v"(ex[2].y), "v"(ex[3].x), "v"(ex[3].y), "v"(ex[4].x), "v"(ex[4].y));
            f32x4 o[4], sum;
            const v2u ones2 = ones.lo;
            const u32 now = (u32)clk;
            u32 oz;
            SCHED();
            MF0(sum, ones, px); { const u32 q = ksrc ^ 8192u; DMA32(dq, kvo, q); } SCHED();
            MF0(o[0], vx[0], px);
            MF0(o[1], vx[1], px);
            MF0(o[2], tv[0], px); oz = (oso & ~(3u << 17)) | (u32)((s + 3) & 3) << 17; PIN(oz); SCHED();
            MF0(o[3], tv[1], px); oso = (oso & ~(3u << 17)) | (u32)((s + 1) & 3) << 17; PIN(oso); SCHED();
            MF1H(sum, ones2, p0);
            MF1H(o[0], vm, p0);
            MF1H(o[1], vm, p1); st16(rs, oz, pv); SCHED();
            MF1H(o[2], tvm, p0);
            MF1H(o[3], tvm, p1);
            float inv;
            asm volatile("s_nop 2\n\tv_rcp_f32 %0, %1\n\ts_nop 0" : "=v"(inv) : "v"(sum[0]), "v"(sum));
            asm volatile("" : "+v"(o[0]), "+v"(o[1]));
            st16(rs, oo, (v4u){pk2(o[0].lo * inv), pk2(o[0].hi * inv), pk2(o[1].lo * inv), pk2(o[1].hi * inv)});
            asm volatile("" : "+v"(o[2]), "+v"(o[3]));
            st16(rs, oo ^ 2048u, (v4u){pk2(o[2].lo * inv), pk2(o[2].hi * inv), pk2(o[3].lo * inv), pk2(o[3].hi * inv)});
            SCHED();
            if (__builtin_expect(h == 0 && w == 3, 0)) {
                st4s(rs, ln == 0 ? hint + 128u : OOB, 0, (u32)s << 16 | min(missed, 1u) << 14 | min(now - tm, 0x3FFFu));
                LST(L * 64 + s, now); LST(128 + L * 64 + s, ((int)(rdl(fl, 3) - now) >= CH_VIS - 4) | ((int)kvok << 1) | ((int)min(missed, 3u) << 2)); LST(640 + L * 64 + s, tm); LST(1728 + L * 64 + s, __builtin_amdgcn_s_memtime());
            }
            tm = rdl(fl, 5);
            missed = missed == SPIN_CAP ? missed : 0u;
            if (__builtin_expect((int)(rdl(fl, 3) - now) < CH_VIS - 4, 0)) {
#pragma unroll 1
                for (int n = 0; n < 8; ++n) {
                    CFENCE();
                    const u32 hv = ld4(rs, fkh);
                    tm = rdl(hv, 5);
                    if ((int)(rdl(hv, 3) - now) >= CH_VIS - 4) break;
                }
            }
        }
        LS2(L == 0 && w == 0, h);
        if (__builtin_expect(!kvok, 0) && s + 1 < NSEG) {
            st4s(rs, (ln == 0 && w == 0) ? hint + 4096u + (u32)h * 128u : OOB, 0, (u32)s + 1u);
            wait_flags(rs, ln < 48u ? fkh : OOB, (u32)s + 2, dead);
            DMA32(dk, l16, ksrc); DMA32(dv, l16, vsrc); DMA32(dq, l16, ksrc ^ 8192u);
        }
        ++s;
        } else {
            u32 ln = l16; PIN(ln);
            const u32 hv = ld4(rs, ln == 96u ? foff(F_ERR) : fkh);
            tm = rdl(hv, 5);
            *(LAS u32*)(lds + C_DUMP + (w == 0 ? 0 : 64)) = rdl(hv, 6);
            BAR();
            missed = uu(*(LAS u32*)(lds + C_DUMP)) != 0u ? SPIN_CAP : missed + 1;
            st4s(rs, missed == SPIN_CAP ? foff(F_ERR) : OOB, 0, 1u);
            BAR();
        }
    }
    DRAIN(); BAR();
    st4s(rs, ofl, 0, NSEG);
}

#define WO_GAP 6
DEV void role_wo(const Params& p, lptr lds, int j, v2u ep) {
    const int tid = threadIdx.x, lane = tid & 63, w = uni(tid >> 6), g = lane >> 4, r = lane & 15;
    const int rg = j & 1, cg = j >> 1, rot = cg & 7;
    const rsrc_t rs = mk_rsrc(p.ws, W_END);
    v8u wo[8][5];
    {
        const v4u so_ = mk_srd(p.Wo, DM * NQ * 2);
        u32 vo[5];
#pragma unroll
        for (int i = 0; i < 5; ++i) { const int n = 64 * i + lane; vo[i] = (u32)((n / 5) * DM + cg * 40 + (n % 5) * 8) * 2u; }
        auto fill = [&](int b) {
            const u32 dst = uu(w * 40960 + (b & 3) * 10240), src = uu((8 * w + ((b + rot) & 7)) * (DH * DM * 2));
#pragma unroll
            for (int i = 0; i < 10; ++i) DMAIN(so_, dst + i * 1024, vo[i % 5], src + (i / 5) * (64 * DM * 2));
        };
        __builtin_amdgcn_s_sleep(ST_WO);
#pragma unroll 1
        for (int b = 0; b < 4; ++b) fill(b);
        const int q4 = r >> 2;
        lptr tb = lds + w * 40960 + (32 * (g >> 1) + 16 * (q4 >> 1) + 8 * ((r >> 1) & 1) + 4 * (g & 1) + (q4 & 1)) * 80 + (r & 1) * 8;
        SFOR(b, 8)
            VMW(b < 5 ? 30 : 70 - 10 * b);
#pragma unroll
            for (int t = 0; t < 5; ++t) {
                lptr q = tb + (b & 3) * 10240 + t * 16;
                wo[b][t] = tr_frag(q, q + 160, q + 64 * 80, q + 64 * 80 + 160);
                if (b * 5 + t < 32) asm volatile("" : "+a"(wo[b][t])); else asm volatile("" : "+v"(wo[b][t]));
            }
            CFENCE();
            if constexpr (b < 4) fill(b + 4);
        SEND
    }
    if (j == 0) LST(265, rt32());
    pre_wait(rs, 0, NCV, 1u, ep);
    const u32 idx = (r & 2) ? 0xEEEEu : 0x4444u;
    bool dead = false;
    const u32 pol0 = uu(get_pol(rs));
    const u32 l16 = (u32)lane * 16u;
    u32 oo[8];
#pragma unroll
    for (int i = 0; i < 8; ++i) oo[i] = uu(W_ORING + ring_map((u32)(rg * 32768 + (8 * w + ((i + rot) & 7)) * 1024), pol0));
    const u32 hint = W_MRING + 4 * 32768u + pol0 * 8192u;
    u32 hvo[2];
#pragma unroll
    for (int l = 0; l < 2; ++l) hvo[l] = lane < 2 ? hint + (u32)(l ^ 1) * (MB / 4) + (u32)lane * 4u : lane == 2 ? hint + (u32)l * (MB / 4) + 128u : lane == 3 ? foff(F_ERR) :
                 (lane >= 32 && j == 0 && w == 3) ? hint + (u32)l * (MB / 4) + (u32)lane * 128u : OOB;
    lptr xw = r < 8 ? lds + w * 640 + (2 * (g & 1) + (g >> 1)) * 32 + r * 4 : lds + 8192 + lane * 4;
    const int G = ((5 * cg) >> 1) + w, tt = 2 * G + ((lane >> 3) & 1) - 5 * cg;
    const bool pub = w < 3 && lane < 32 && tt >= 0 && tt < 5;
    lptr xr = lds + (pub ? tt * 128 + (2 * (lane >> 4) + ((lane >> 2) & 1)) * 32 + (lane & 3) * 8 : 0);
    const u32 mpo = pub ? W_MRING + ring_map((u32)(G * 256 + rg * 128 + lane * 4), pol0) : OOB;
    constexpr u32 PER0 = CH_W0 + CH_MW + CH_H0 + CH_MH + 2 * CH_VIS;
    const bool lead = j == 0 && w == 3;
    const u32 hso = (lead && lane == 0) ? hint : OOB;
    MK_SRD();
    u32 t = 0, tn = 0;
    if (j == 0) {
#pragma unroll 1
        for (int n = 0; n < SPIN_CAP && !dead; ++n) {
            CFENCE();
            if (all_(ld4(rs, W_ORING + pol0 * 8192u + (u32)w * 16384u + (u32)lane * 128u) != PSENT)) break;
            __builtin_amdgcn_s_sleep(1);
        }
        BAR();
    }
    if (lead) {
        t = rt32() + 40u; tn = t + PER0 / 2 + CH_REC;
        st16(rs, lane < 2 ? hint + (u32)lane * (MB / 4) : OOB, (v4u){lane == 0 ? t : tn, PER0, (lane == 0 ? t : tn) + (CH_W0 + CH_VIS + CH_MW + CH_REC), 0u});
    } else {
#pragma unroll 1
        for (int n = 0; n < SPIN_CAP && !dead; ++n) {
            CFENCE();
            const u32 hv = ld4(rs, hvo[1]);
            t = rdl(hv, 0);
            if (rdl(hv, 1) < 400u) break;
            __builtin_amdgcn_s_sleep(1);
        }
    }
    int sn[2] = {0, 0};
    u32 hav = 4 * CH_H0, wav = 4 * CH_W0, mh = 8 * CH_MH, mw = 8 * CH_MW, ok[2] = {0u, 0u}, dw[2] = {CH_W0 + CH_VIS + CH_MW + CH_REC, CH_W0 + CH_VIS + CH_MW + CH_REC};
    u32 zero = 0, hv = 0xFFFFu, pst = 1, pdur = 0, pmp = OOB;
    const u32 psent = PSENT;
    lptr sw = lds + 2560 + w * 4;
    bool stop = false;
    asm volatile("" : "+v"(zero), "+v"(sw));
    auto turn = [&](auto lc) __attribute__((always_inline)) {
        constexpr int L = decltype(lc)::value, M = L ^ 1;
        u32 now, done, rep, rd, lost, dh, dn, nt, za; v4u hw;
        const int sc = sn[L];
        auto books = [&](auto ic) __attribute__((always_inline)) {
            constexpr int i = decltype(ic)::value;
            if constexpr (i == 0) { rep = rdl(hv, 2); PINS(rep); }
            if constexpr (i == 1) { lost = (u32)(__builtin_amdgcn_ballot_w64(hv == (u32)sn[M]) >> 32); PINS(lost); }
            if constexpr (i == 2) { lost |= (rep >> 16) ^ (u32)sn[M]; PINS(lost); }
            if constexpr (i == 3) { lost |= ok[M] ^ 2u; rd = rep & 0xFFFFu; PINS(lost); PINS(rd); }
            if constexpr (i == 4) { lost = lost == 0u ? CH_UP * 8u : 0u; PINS(lost); }
            if constexpr (i == 5) { dn = wav >> 2; PINS(dn); pdur = min(pdur, dn + 8u); PINS(pdur); }
            if constexpr (i == 6) { pdur = (done & ok[M]) ? pdur : dn; PINS(pdur); }
            if constexpr (i == 7) { wav += pdur - dn; PINS(wav); }
            if constexpr (i == 8) { dh = hav >> 2; PINS(dh); nt = min(rd, dh + 8u); PINS(nt); }
            if constexpr (i == 9) { nt = rd < 0x3FFFu ? nt : dh; PINS(nt); }
            if constexpr (i == 10) { hav += nt - dh; PINS(hav); }
            if constexpr (i == 11) { mw = max(mw - 1u, CH_MW * 8u); PINS(mw); }
            if constexpr (i == 12) { mh = max(mh - 1u, CH_MH * 8u); PINS(mh); }
            if constexpr (i == 13) { rd = rd >> 14 == 1u ? lost : 0u; PINS(rd); }
            if constexpr (i == 14) { mw += rd; mh += lost - rd; PINS(mw); PINS(mh); }
            if constexpr (i == 15) { ok[M] = done ? 1u : (ok[M] & 1u) << 1; PINS(ok[M]); }
            if constexpr (i == 16) { dh = (2u * hav + mh) / 8u + CH_VIS; PINS(dh); }
            if constexpr (i == 17) { dn = (2u * wav + mw) / 8u + CH_VIS; PINS(dn); }
            if constexpr (i == 18) { nt = dw[L] + dh; PINS(nt); }
            if constexpr (i == 19) { dh += dn; dw[L] = dn; sn[M] += (int)done; PINS(dh); PINS(sn[M]); }
            if constexpr (i == 20) { rd = tn - t + (dh >> 1); PINS(rd); }
            if constexpr (i == 21) { rd = sn[M] + 1 < NSEG ? rd : 0u; PINS(rd); }
            if constexpr (i == 22) { nt = t + (u32)max((int)nt, (int)rd); PINS(nt); }
            if constexpr (i == 23) { hw.x = nt; hw.y = dh; PIN(hw.x); PIN(hw.y); }
            if constexpr (i == 24) { hw.z = nt + dn; hw.w = 0u; PIN(hw); }
            if constexpr (i == 25) st16(rs, hso + L * (MB / 4), hw);
        };
        if (__builtin_expect(sc + 1 < NSEG, 1)) {
            u32 vo = l16 + (L * (MB / 2) + (u32)(sc & 3) * 131072u), mp, hn;
            PIN(vo);
#define WT(k) do {} while (0)
            WT(0);
            v4u d[8];
            {
                u32 tmp;
                asm volatile(WAIT_TXT("%1", "%2") "buffer_load_dwordx4 %0, %3, %4, %5 offen sc1\n\ts_waitcnt lgkmcnt(0)"
                             : "=&v"(d[0]), "=&s"(tmp) : "s"(uu(t - 4u)), "v"(vo), "s"(srd), "s"(oo[0]) : "vcc", "scc", "memory");
            }
            SFOR(i, 7)
                asm volatile("buffer_load_dwordx4 %0, %1, %2, %3 offen sc1" : "=v"(d[i + 1]) : "v"(vo), "s"(srd), "s"(oo[i + 1]) : "memory");
                SCHED();
                if constexpr (i == 0) { done = uu(pst) ^ 1u; PINS(done); }
                static_for<3 * i, 3 * i + 3>(books);
                SCHED();
            SEND
            hn = ld4(rs, hvo[L]);
            SCHED();
            WT(1);
            static_for<21, 26>(books);
            za = done ? pmp : OOB;
            st4s(rs, za, 0, psent);
            *(LAS u32*)(sw + L * 4096) = zero;
            f32x4 acc[5] = {};
            mp = mpo + (L * (MB / 4) + (u32)((sc + 1) & 3) * 32768u); pmp = mpo + (L * (MB / 4) + (u32)(sc & 3) * 32768u);
            asm volatile("" : "+v"(acc[0]), "+v"(acc[1]), "+v"(acc[2]), "+v"(acc[3]), "+v"(acc[4]), "+v"(mp), "+v"(pmp));
            SCHED();
            WT(2);
            float f0[5], f1[5]; unsigned long long bad;
#define WSW(o_) asm volatile("v_permlane32_swap_b32 %0, %1" : "+v"(f0[o_]), "+v"(f1[o_]))
#define WAD(o_) asm volatile("v_add_f32_dpp %0, %1, %0 row_ror:8 row_mask:0xf bank_mask:0xf" : "+v"(f0[o_]) : "v"(f1[o_]))
#define WFO(o_) do { VADD(f0[o_], acc[o_][0], acc[o_][2]); VADD(f1[o_], acc[o_][1], acc[o_][3]); } while (0)
#define WWR(o_) *(LAS float*)(xw + L * 4096 + (o_) * 128) = f0[o_]
            asm volatile("s_waitcnt vmcnt(10)" : "+v"(d[0]));
            if (__builtin_expect(__builtin_amdgcn_ballot_w64(d[0].x == PSENT) != 0ull, 0)) {
                asm volatile("s_waitcnt vmcnt(3)" : "+v"(d[1]), "+v"(d[2]), "+v"(d[3]), "+v"(d[4]), "+v"(d[5]), "+v"(d[6]), "+v"(d[7]));
#pragma unroll
                for (int i = 0; i < 5; ++i) *(LAS u32*)(xw + L * 4096 + i * 128) = psent;
                *(LAS u32*)(sw + L * 4096) = 1u;
            } else {
            SFOR(i, 40)
                constexpr int pc = i < 30 ? i / 5 : 6 + (i & 1), oc = i < 30 ? i % 5 : (i - 30) >> 1;
                if constexpr (oc == 0 && pc > 0) asm volatile("s_waitcnt vmcnt(%1)" : "+v"(d[pc]) : "i"(10 - pc));
                if constexpr (pc * 5 + oc < 32) SMF_A(acc[oc], d[pc], wo[pc][oc]); else SMF_V(acc[oc], d[pc], wo[pc][oc]);
                if constexpr (i == 0) WT(3);
                if constexpr (i == 20) WT(4);
                if constexpr (i == 31 || i == 33) NOPS(1);
                if constexpr (i == 32) NOPS(0);
                if constexpr (i == 34) WFO(0);
                if constexpr (i == 35) { asm volatile("v_cmp_u_f32 %0, %1, %2" : "=s"(bad) : "v"(f0[0]), "v"(f1[0])); WSW(0); }
                if constexpr (i == 36) WFO(1);
                if constexpr (i == 37) { WAD(0); WSW(1); }
                if constexpr (i == 38) { WWR(0); SCHED(); WFO(2); }
                if constexpr (i == 39) { WAD(1); WSW(2); }
                SCHED();
            SEND
            WT(5);
            WWR(1); SCHED(); WFO(3); WAD(2); WWR(2); SCHED(); WSW(3);
            if (__builtin_expect(bad != 0ull, 0)) {
                bool miss = false;
#pragma unroll
                for (int i = 0; i < 8; ++i) miss |= d[i].x == PSENT;
                if (all_(!miss)) {
#pragma unroll
                    for (int i = 0; i < 8; ++i) miss |= d[i].y == PSENT || d[i].z == PSENT || d[i].w == PSENT;
                }
                *(LAS u32*)(sw + L * 4096) = all_(!miss) ? 0u : 1u;
            }
            WFO(4); WAD(3); WWR(3); SCHED();
            asm volatile("v_permlane32_swap_b32 %0, %1\n\ts_nop 1\n\tv_add_f32_dpp %0, %1, %0 row_ror:8 row_mask:0xf bank_mask:0xf" : "+v"(f0[4]), "+v"(f1[4]));
            WWR(4);
            }
            WT(6);
            f32x2 a, b, c, e; u32 cw, mq; v4u sv;
            asm volatile("s_waitcnt lgkmcnt(0)\n\ts_barrier\n\tds_read_b64 %0, %5 offset:%7\n\tds_read_b64 %1, %5 offset:%7+640\n\tds_read_b64 %2, %5 offset:%7+1280\n\tds_read_b64 %3, %5 offset:%7+1920\n\t"
                         "ds_read_b128 %4, %6 offset:%7+2560\n\t"
                         "s_waitcnt lgkmcnt(3)\n\tv_pk_add_f32 %0, %0, %1\n\ts_waitcnt lgkmcnt(1)\n\tv_pk_add_f32 %2, %2, %3\n\ts_waitcnt lgkmcnt(0)\n\tv_pk_add_f32 %0, %0, %2"
                         : "=&v"(a), "=&v"(b), "=&v"(c), "=&v"(e), "=&v"(sv) : "v"(xr), "v"(zero), "i"(L * 4096) : "memory");
            asm volatile("v_cmp_u_f32 vcc, %2, %3\n\tv_cvt_pk_bf16_f32 %0, %2, %3\n\tv_cndmask_b32 %1, %4, %5, vcc" : "=&v"(cw), "=&v"(mq) : "v"(a.x), "v"(a.y), "v"(mp), "v"(OOB) : "vcc");
            st4s(rs, mq, 0, cw);
            SCHED();
            now = rt32();
            PIN(hn); hv = hn;
            WT(7);
            pst = sv.x | sv.y | sv.z | sv.w;
            if (__builtin_expect(__builtin_amdgcn_ballot_w64(mq != mp && pst == 0u) != 0ull, 0)) st4s(rs, mp, 0, cw);
        } else {
            done = uu(pst) ^ 1u;
            st4s(rs, done ? pmp : OOB, 0, psent);
            static_for<0, 25>(books);
            pst = 1;
            hv = ld4(rs, hvo[L]);
            PIN(hv);
            now = rt32();
        }
        pdur = now - t;
        const u32 per = rdl(hv, 1), th = rdl(hv, 0);
        t = lead ? tn : (int)(th - now) < -(int)(per >> 1) ? th + per : th;
        tn = nt;
        dw[M] += max((int)(now + CH_WX - t), 0);
        stop = stop || rdl(hv, 3) != 0u;
    };
    for (int k = 0; k < 2 * SPIN_CAP && !stop && (sn[0] + 1 < NSEG || sn[1] + 1 < NSEG); ++k) { turn(IC<0>{}); turn(IC<1>{}); }
    if (sn[0] + 1 < NSEG || sn[1] + 1 < NSEG) st4s(rs, tid == 0 ? foff(F_ERR) : OOB, 0, 1u);
}

__global__ void __launch_bounds__(256, 1) k_main(Params p) {
    lptr lds = (lptr)(u32)16 - 16;
    if (blockIdx.x == 0) LST(256, rt32());
    unsigned long long pc; int len;
    asm volatile("s_getpc_b64 %0\n.Lk_main_here:\n\ts_mov_b32 %1, .Lk_end_here-.Lk_main_here" : "=s"(pc), "=s"(len));
    const rsrc_t rs = mk_rsrc(p.ws, W_END);
    const v2u e0 = ld8s(rs, threadIdx.x == 0 ? foff(F_EPOCH) : OOB, 0);
    const v4u code = __builtin_amdgcn_raw_buffer_load_b128(mk_rsrc((const void*)(pc & ~15ull), (u32)max(len, 0)), (((blockIdx.x >> 3) & 31u) * 256u + threadIdx.x) * 16u, 0, 0);
    CFENCE();
    if (threadIdx.x == 0) {
        GAS unsigned long long* slots = (GAS unsigned long long*)(p.ws + W_CTRL + F_SLOT * 128);
        const unsigned long long tag = (unsigned long long)e0.y << 32 | (e0.x + 1u);
        u32 role = ((u32)__builtin_amdgcn_s_getreg(20 | (3 << 11)) & 7u) * 32u + (blockIdx.x >> 3);
        for (int n = 0; n < 256 && __hip_atomic_exchange(slots + role * 16, tag, __ATOMIC_RELAXED, __HIP_MEMORY_SCOPE_AGENT) == tag; ++n) role = (role + 1u) & 255u;
        lds_w16(lds, (v4u){role, e0.x, e0.y, 0u});
    }
    __syncthreads();
    const v4u rv = lds_r16(lds);
    const int role = uni((int)rv.x), x = role >> 5, i = role & 31, w = uni(threadIdx.x >> 6);
    const v2u ep = {uu(rv.y), uu(rv.z)};
    asm volatile("" :: "v"(code));
    __syncthreads();
    if (x < 2) { role_head(p, lds, i >> 4, x * 16 + (i & 15), ep); __builtin_amdgcn_endpgm(); }
    if (x == 2) { role_wo(p, lds, i, ep); __builtin_amdgcn_endpgm(); }
    const int no = x < 5 ? 5 : 10, nr = x < 7 ? 5 : 4;
    if (i >= no + nr) { role_qkv(p, lds, (x < 5 ? (x - 3) * 22 : 44 + (x - 5) * 17) + i - no - nr, ep); __builtin_amdgcn_endpgm(); }
    const int og = x < 5 ? x - 3 : 2 * (x - 4) + (i >= 5), ng = i % 5, kq = og >> 1, L = og & 1, r = (x - 3) * 5 + i - no, q = r / 3;
    const int j = i < no ? kq * 5 + ng : r % 3 == 0 ? 20 + (q & 1) * 20 + (q >> 1) : 16 + (r % 3) * 8 + q;
    const bool cv = i >= no && r % 3 == 0;
    if (i >= no || L == 1) conv_stage(p, rs, lds, j, q, cv ? 40 : -8, ep);
    if (cv) __builtin_amdgcn_endpgm();
    if (i >= no) { role_row(p, lds, (j - 24) * 4 + w, ep); __builtin_amdgcn_endpgm(); }
    role_out(p, lds, L, kq, ng, ep);
}
__global__ void k_end() { asm volatile(".Lk_end_here:"); }

extern "C" void kernel_launch(void* const* d_in, const int* in_sizes, int n_in, void* d_out, int out_size, void* d_ws, size_t ws_size,
                              hipStream_t stream) {
    Params p;
    p.x = (const u16*)d_in[0]; p.mem0 = (const u16*)d_in[1]; p.Wq = (const u16*)d_in[2]; p.Wk = (const u16*)d_in[3];
    p.Wv = (const u16*)d_in[4]; p.Wo = (const u16*)d_in[5];
    p.bk = (const u16*)d_in[8]; p.bv = (const u16*)d_in[9]; p.shift = (const u16*)d_in[10]; p.ssmk = (const u16*)d_in[11]; p.ssmD = (const u16*)d_in[12];
    p.out = (u16*)d_out;
    p.ws = (unsigned char*)(((size_t)d_ws + 2 * MB - 1) & ~(size_t)(2 * MB - 1));
    (void)hipFuncSetAttribute((const void*)k_main, hipFuncAttributeMaxDynamicSharedMemorySize, LDS_BYTES);
    hipLaunchKernelGGL(k_main, dim3(256), dim3(256), LDS_BYTES, stream, p);
}
```

```cpp
#include <hip/hip_runtime.h>
#include <hip/hip_bf16.h>

typedef unsigned short u16;
typedef unsigned int u32;
typedef u32 v2u __attribute__((ext_vector_type(2)));
typedef u32 v4u __attribute__((ext_vector_type(4)));
typedef u32 v8u __attribute__((ext_vector_type(8)));
typedef float f32x2 __attribute__((ext_vector_type(2)));
typedef float f32x4 __attribute__((ext_vector_type(4)));
typedef float f32x16 __attribute__((ext_vector_type(16)));
typedef __bf16 bf16x2 __attribute__((ext_vector_type(2)));
typedef __bf16 bf16x8 __attribute__((ext_vector_type(8)));
typedef short s16x4 __attribute__((ext_vector_type(4)));
#define LAS __attribute__((address_space(3)))
#define GAS __attribute__((address_space(1)))
typedef LAS unsigned char* lptr;
#define DEV __device__ __forceinline__

constexpr int SEQ = 2048, DM = 640, NH = 32, DH = 128, NSEG = 64, NQ = NH * DH;

constexpr u32 MB = 1u << 20;
constexpr u32 W_ORING = 0;
constexpr u32 W_MRING = MB;
constexpr u32 W_PROBE = 2 * MB - 65536;
constexpr u32 W_CTRL = 4 * MB;
constexpr u32 CTRL_BYTES = 128 * 1024;
constexpr u32 ACT = SEQ * DM * 2;
constexpr u32 W_H1 = 5 * MB, W_Y = W_H1 + ACT, W_H2 = W_Y + ACT, W_X2 = W_H2 + ACT;
constexpr u32 W_KQ = 16 * MB;
constexpr u32 W_VO = W_KQ + 64 * MB;
constexpr u32 W_P = W_VO + 64 * MB;
constexpr u32 P_ITEM = 32 * 4 * DM * 4;
constexpr u32 W_KVT = W_P + 2 * NSEG * P_ITEM;
constexpr u32 W_SP = W_KVT + ACT;
constexpr u32 W_END = W_KVT + 3 * MB;
static_assert((size_t)W_END + 2 * MB <= 256ull * MB, "workspace");
constexpr u32 F_ERR = 0, F_QKV = 16, F_O = F_QKV + 192, F_P = F_O + 64, F_H2 = F_P + 64, F_CLR = F_H2 + 32,
              F_POL = F_CLR, F_EPOCH = F_POL + 32, F_SLOT = F_EPOCH + 1, F_PRE = F_SLOT + 256, F_N = F_PRE + 192;
constexpr int NCV = 44, NPRE = 40;
static_assert(F_N * 128 <= CTRL_BYTES, "ctrl");
DEV u32 foff(u32 idx) { return W_CTRL + idx * 128u; }

constexpr u32 OOB = 0x80000000u;
constexpr u32 PSENT = 0x7F817F81u;
constexpr int SPIN_CAP = 40000;
constexpr int LDS_BYTES = 160 * 1024;
#define ST_HEAD 0
#define ST_WO 0

#define LS3(cond, slot, idx) do {} while (0)
#define LST(idx, v) do {} while (0)
#define LS2(cond, idx) do {} while (0)
struct Params {
    const u16 *x, *mem0, *Wq, *Wk, *Wv, *Wo, *bk, *bv, *shift, *ssmk, *ssmD;
    u16* out;
    unsigned char* ws;
};

typedef __amdgpu_buffer_rsrc_t rsrc_t;
DEV rsrc_t mk_rsrc(const void* p, u32 bytes) { return __builtin_amdgcn_make_buffer_rsrc((void*)p, 0, bytes, 0x00020000); }
DEV v4u ld16s(rsrc_t r, u32 off, u32 so) { return __builtin_amdgcn_raw_buffer_load_b128(r, off, so, 16); }
DEV v2u ld8s(rsrc_t r, u32 off, u32 so) { return __builtin_amdgcn_raw_buffer_load_b64(r, off, so, 16); }
DEV v4u ldin16(rsrc_t r, u32 off, u32 so) { return __builtin_amdgcn_raw_buffer_load_b128(r, off, so, 2); }
DEV v2u ldin8(rsrc_t r, u32 off, u32 so) { return __builtin_amdgcn_raw_buffer_load_b64(r, off, so, 2); }
DEV u32 ld4(rsrc_t r, u32 off) { return __builtin_amdgcn_raw_buffer_load_b32(r, off, 0, 16); }
DEV void st16(rsrc_t r, u32 off, v4u v) { __builtin_amdgcn_raw_buffer_store_b128(v, r, off, 0, 16); }
DEV void st8s(rsrc_t r, u32 off, u32 so, v2u v) { __builtin_amdgcn_raw_buffer_store_b64(v, r, off, so, 16); }
DEV void st4s(rsrc_t r, u32 off, u32 so, u32 v) { __builtin_amdgcn_raw_buffer_store_b32(v, r, off, so, 16); }
#define DRAIN() asm volatile("s_waitcnt vmcnt(0)" ::: "memory")
#define VMW(n) asm volatile("s_waitcnt vmcnt(%0)" :: "i"(n) : "memory")
#define BAR() asm volatile("s_waitcnt lgkmcnt(0)\n\ts_barrier" ::: "memory")
#define SCHED() __builtin_amdgcn_sched_barrier(0)
#define CFENCE() asm volatile("" ::: "memory")
#define NOPS(n) asm volatile("s_nop %0" :: "i"(n))
#define PIN(x) asm volatile("" : "+v"(x))
#define PINS(x) asm volatile("" : "+s"(x))

template <int V> struct IC { static constexpr int value = V; };
template <int I, int N, class F> DEV void static_for(F&& f) { if constexpr (I < N) { f(IC<I>{}); static_for<I + 1, N>(f); } }
#define SFOR(var, n) static_for<0, n>([&](auto ic_) __attribute__((always_inline)) { constexpr int var = decltype(ic_)::value;
#define SEND });

DEV u32 pk(float a, float b) { bf16x2 v = {(__bf16)a, (__bf16)b}; return __builtin_bit_cast(u32, v); }
DEV float lo_f(u32 u) { return __builtin_bit_cast(float, u << 16); }
DEV float hi_f(u32 u) { return __builtin_bit_cast(float, u & 0xffff0000u); }
DEV f32x2 up2(u32 u) { return (f32x2){lo_f(u), hi_f(u)}; }
DEV u32 pk2(f32x2 v) { return pk(v.x, v.y); }
DEV float rbf(float a) { return (float)(__bf16)a; }
DEV v4u lds_r16(lptr p) { return *(LAS v4u*)p; }
DEV void lds_w16(lptr p, v4u v) { *(LAS v4u*)p = v; }
DEV v2u lds_tr(lptr p) { return __builtin_bit_cast(v2u, __builtin_amdgcn_ds_read_tr16_b64_v4i16((LAS s16x4*)p)); }
DEV v4u tr2(lptr p0, lptr p1) { const v2u a = lds_tr(p0), b = lds_tr(p1); return (v4u){a.x, a.y, b.x, b.y}; }
DEV v8u cat8(v4u a, v4u b) { return __builtin_shufflevector(a, b, 0, 1, 2, 3, 4, 5, 6, 7); }
DEV f32x4 mfma16(v4u a, v4u b, f32x4 c) { return __builtin_amdgcn_mfma_f32_16x16x32_bf16(__builtin_bit_cast(bf16x8, a), __builtin_bit_cast(bf16x8, b), c, 0, 0, 0); }
DEV f32x16 mfma32(v4u a, v4u b, f32x16 c) { return __builtin_amdgcn_mfma_f32_32x32x16_bf16(__builtin_bit_cast(bf16x8, a), __builtin_bit_cast(bf16x8, b), c, 0, 0, 0); }

DEV float f_(u32 u) { return __builtin_bit_cast(float, u); }
DEV u32 u_(float f) { return __builtin_bit_cast(u32, f); }
template <int CTRL> DEV float dpp(float v) { return f_((u32)__builtin_amdgcn_update_dpp(0, (int)u_(v), CTRL, 0xf, 0xf, false)); }
DEV float max16(float v) { auto r = __builtin_amdgcn_permlane16_swap(u_(v), u_(v), false, false); return fmaxf(f_(r[0]), f_(r[1])); }
DEV float max32(float v) { auto r = __builtin_amdgcn_permlane32_swap(u_(v), u_(v), false, false); return fmaxf(f_(r[0]), f_(r[1])); }
DEV float add16(float v) { auto r = __builtin_amdgcn_permlane16_swap(u_(v), u_(v), false, false); return f_(r[0]) + f_(r[1]); }
DEV float add32(float v) { auto r = __builtin_amdgcn_permlane32_swap(u_(v), u_(v), false, false); return f_(r[0]) + f_(r[1]); }
DEV float wave_sum(float v) {
    v += dpp<0xB1>(v);
    v += dpp<0x4E>(v);
    v += dpp<0x124>(v);
    v += dpp<0x128>(v);
    return add32(add16(v));
}
DEV bool all_(bool c) { return __builtin_amdgcn_ballot_w64(!c) == 0ull; }
DEV int uni(int v) { return __builtin_amdgcn_readfirstlane(v); }
DEV u32 uu(u32 v) { return (u32)__builtin_amdgcn_readfirstlane((int)v); }
DEV u32 rdl(u32 v, int l) { return (u32)__builtin_amdgcn_readlane((int)v, l); }
DEV u32 rt32() { return (u32)__builtin_amdgcn_s_memrealtime(); }

#define WSL 2
DEV void wait_flags(rsrc_t rs, u32 off, u32 want, bool& dead) {
    if (dead) return;
#pragma unroll 1
    for (int n = 0;; ++n) {
        CFENCE();
        const u32 v = ld4(rs, off);
        if (all_(off >= OOB || v >= want)) break;
        __builtin_amdgcn_s_sleep(WSL);
        if ((n & 63) == 63) { if (uni((int)ld4(rs, foff(F_ERR))) != 0) { dead = true; break; } }
        if (n > SPIN_CAP) { st4s(rs, foff(F_ERR), 0, 1u); dead = true; break; }
    }
    CFENCE();
}

DEV void pre_post(rsrc_t rs, int idx, u32 stage, v2u ep) { st8s(rs, threadIdx.x == 0 ? foff(F_PRE + idx) : OOB, 0, (v2u){ep.x + stage, ep.y}); }
DEV void pre_poll(rsrc_t rs, int first, int n, u32 stage, v2u ep) {
    const int lane = threadIdx.x & 63;
#pragma unroll 1
    for (int it = 0; it < SPIN_CAP; ++it) {
        CFENCE();
        const v2u a = ld8s(rs, foff(F_PRE + first + min(lane, n - 1)), 0), b = ld8s(rs, foff(F_PRE + first + min(lane + 64, n - 1)), 0);
        if (all_(a.y == ep.y && b.y == ep.y && max(a.x - ep.x - stage, b.x - ep.x - stage) < 8u - stage)) break;
        __builtin_amdgcn_s_sleep(2);
    }
    CFENCE();
}
DEV void pre_wait(rsrc_t rs, int first, int n, u32 stage, v2u ep) {
    if (uni(threadIdx.x >> 6) == 0) pre_poll(rs, first, n, stage, ep);
    BAR();
}
DEV u32 get_pol(rsrc_t rs) {
    const int lane = threadIdx.x & 63;
    const u32 raw = ld4(rs, lane < 16 ? foff(F_POL + lane) : OOB);
    return wave_sum((float)(int)raw) >= 0.f ? 1u : 0u;
}
#define NPROBE 16
DEV void pol_probe(rsrc_t rs, int id) {
    const int lane = threadIdx.x & 63;
    const u32 off0 = W_PROBE + (u32)(id >> 2) * 16384u + (u32)(id & 3) * 128u;
    st4s(rs, lane < 32 ? off0 + (u32)lane * 512 : OOB, 0, 0u); DRAIN();
    int d[2] = {1 << 16, 1 << 16};
#pragma unroll 1
    for (int q = 0; q < NPROBE; ++q) {
        const u32 half = (u32)(q ^ (q >> 1) ^ id) & 1u;
        const unsigned long long t0 = __builtin_amdgcn_s_memtime();
        const u32 v = ld4(rs, lane == 0 ? off0 + half * 8192 + (u32)(q >> 1) * 512 : OOB);
        asm volatile("s_waitcnt vmcnt(0)" :: "v"(v) : "memory");
        const int dt = (int)(__builtin_amdgcn_s_memtime() - t0);
        if (half) d[1] = min(d[1], dt); else d[0] = min(d[0], dt);
    }
    const u32 xcc = (u32)__builtin_amdgcn_s_getreg(20 | (3 << 11)) & 0xfu;
    st4s(rs, lane == 0 ? foff(F_POL + id) : OOB, 0, (u32)min(max((d[0] - d[1]) * ((xcc & 4u) ? -1 : 1), -250), 250));
}
DEV f32x2 ln_stats(const f32x2 (&x)[5]) {
    const float c = f_(uu(u_(x[0].x)));
    f32x2 d[5];
#pragma unroll
    for (int i = 0; i < 5; ++i) d[i] = x[i] - c;
    const f32x2 s2 = (d[0] + d[1]) + (d[2] + d[3]) + d[4], q2 = (d[0] * d[0] + d[1] * d[1]) + (d[2] * d[2] + d[3] * d[3] + d[4] * d[4]);
    float a = s2.x + s2.y, b = q2.x + q2.y, a2, b2;
#define LN_DPP(m) "v_add_f32_dpp %0, %0, %0 " m " row_mask:0xf bank_mask:0xf\n\tv_add_f32_dpp %1, %1, %1 " m " row_mask:0xf bank_mask:0xf\n\ts_nop 0\n\t"
    asm("s_nop 1\n\t" LN_DPP("quad_perm:[1,0,3,2]") LN_DPP("quad_perm:[2,3,0,1]") LN_DPP("row_ror:4")
        "v_add_f32_dpp %0, %0, %0 row_ror:8 row_mask:0xf bank_mask:0xf\n\tv_add_f32_dpp %1, %1, %1 row_ror:8 row_mask:0xf bank_mask:0xf\n\ts_nop 1\n\t"
        "v_permlane16_swap_b32 %0, %1\n\tv_add_f32 %0, %0, %1\n\tv_mov_b32 %1, %0\n\ts_nop 1\n\tv_permlane32_swap_b32 %0, %1\n\tv_add_f32 %0, %0, %1\n\ts_nop 0\n\t"
        "v_readlane_b32 %2, %0, 0\n\tv_readlane_b32 %3, %0, 16\n\ts_nop 1"
        : "+v"(a), "+v"(b), "=s"(a2), "=s"(b2));
    const float m = a2 * (1.0f / DM), var = fmaxf(b2 * (1.0f / DM) - m * m, 0.f);
    const float sd = rbf(__builtin_amdgcn_sqrtf(rbf(rbf(var) + 1e-5f)));
    return (f32x2){rbf(c + m), __builtin_amdgcn_rcpf(sd)};
}
DEV u32 pkv(f32x2 v) { u32 d; asm("v_cvt_pk_bf16_f32 %0, %1, %2" : "=v"(d) : "v"(v.x), "v"(v.y)); return d; }
DEV void store_h(rsrc_t rs, u32 base, int row, const u32 (&hp)[5]) {
    const u32 lane = threadIdx.x & 63;
    const u32 sw = (u32)(row & 15) * 16u, o8 = (lane >> 5) * 8192u + (((lane & 31u) * 8u) ^ sw), hb = base + (u32)(row >> 5) * (32 * DM * 2) + (u32)(row & 31) * 256u;
    st8s(rs, o8, hb, (v2u){hp[0], hp[1]});
    st8s(rs, o8, hb + 16384, (v2u){hp[2], hp[3]});
    st4s(rs, (lane * 4) ^ sw, hb + 32768, hp[4]);
}

#define DMA16(ldsb, vo, so) asm volatile("s_mov_b32 m0, %0\n\ts_nop 0\n\tbuffer_load_dwordx4 %1, %2, %3 offen sc1 lds" :: "s"(ldsb), "v"(vo), "s"(srd), "s"(so) : "memory")
#define DMA16I(ldsb, lo, vo, so, go) do { u32 so_; asm volatile("s_add_u32 m0, %1, %5\n\ts_add_u32 %0, %4, %6\n\tbuffer_load_dwordx4 %2, %3, %0 offen sc1 lds" \
                         : "=&s"(so_) : "s"(ldsb), "v"(vo), "s"(srd), "s"(so), "i"(lo), "i"(go) : "memory", "scc"); } while (0)
#define DMA32(ldsb, vo, so) asm volatile("s_mov_b32 m0, %0\n\ts_nop 0\n\tbuffer_load_dwordx4 %1, %2, %3 offen sc1 lds\n\tbuffer_load_dwordx4 %1, %2, %3 offen offset:1024 sc1 lds" \
                         :: "s"(ldsb), "v"(vo), "s"(srd), "s"(so) : "memory")
#define DMAIN(sr, ldsb, vo, so) asm volatile("s_mov_b32 m0, %0\n\ts_nop 0\n\tbuffer_load_dwordx4 %1, %2, %3 offen lds" :: "s"(ldsb), "v"(vo), "s"(sr), "s"(so) : "memory")
DEV v4u mk_srd(const void* q, u32 bytes) { return (v4u){uu((u32)(size_t)q), uu((u32)((size_t)q >> 32) & 0xffffu), bytes, 0x00020000u}; }
#define DMANT(sr, ldsb, vo, so) asm volatile("s_mov_b32 m0, %0\n\ts_nop 0\n\tbuffer_load_dwordx4 %1, %2, %3 offen nt lds" :: "s"(ldsb), "v"(vo), "s"(sr), "s"(so) : "memory")
#define MK_SRD() const v4u srd = mk_srd(p.ws, W_END)

constexpr int CV_W = 40960;
template <bool B> struct CvL {
    static constexpr int NF = B ? 5 : 3, NK = B ? 4 : 2, KP = 544 + NK * 256, CH = NF * 1024 + 4 * KP;
};
constexpr int CV_Y = 4 * CvL<false>::CH, CV_YP = 272;
static_assert(CV_Y + 32 * CV_YP <= CV_W && 2 * CvL<true>::CH <= CV_Y, "conv lds");
DEV int cv_chan(int u, int i) { return 16 * (u % 40) + 4 * i + u / 40; }
template <bool B> DEV void cv_filt(const Params& p, u32 dst, int d) {
    const u32 lane = threadIdx.x & 63;
    const v4u sf = mk_srd(p.ssmk + d * SEQ, SEQ * 2);
    DMANT(sf, dst, lane < 4u ? OOB : lane * 16u - 64u, 0);
#pragma unroll
    for (int i = 1; i < CvL<B>::NF; ++i) DMANT(sf, uu(dst + i * 1024), i == CvL<B>::NF - 1 && lane >= 4u ? OOB : lane * 16u, i * 1024 - 64);
}
template <bool B> DEV void cv_kvld(rsrc_t rs, int d, v4u* k) {
#pragma unroll
    for (int i = 0; i < CvL<B>::NK; ++i) k[i] = ld16s(rs, (threadIdx.x & 63) * 16u, uu(W_KVT + (u32)d * (SEQ * 2) + i * 1024));
}
template <bool B> DEV void cv_kvput(lptr F, const v4u* k) {
    const int lane = threadIdx.x & 63;
#pragma unroll
    for (int i = 0; i < CvL<B>::NK; ++i) lds_w16(F + CvL<B>::NF * 1024 + (lane & 3) * CvL<B>::KP + 512 + i * 256 + (lane >> 2) * 16, k[i]);
}
template <bool B> DEV void cv_zero(lptr F) {
    const int lane = threadIdx.x & 63;
#pragma unroll
    for (int i = 0; i < 4; ++i) lds_w16(F + (lane >> 5) * CvL<B>::CH + CvL<B>::NF * 1024 + i * CvL<B>::KP + (lane & 31) * 16, (v4u){0u, 0u, 0u, 0u});
}
#define M32V(d, a, b) asm volatile("v_mfma_f32_32x32x16_bf16 %0, %1, %2, %0" : "+v"(d) : "v"(a), "v"(b))
template <bool B> DEV void cv_run(const Params& p, rsrc_t rs, lptr F, int d, u32 D, f32x4 sp, float (&y)[16]) {
    constexpr int KP = CvL<B>::KP, FO = B ? 2048 : 0;
    const int lane = threadIdx.x & 63, n = lane & 31, hh = lane >> 5, c = lane & 3;
    lptr fa = F + 64 + (32 * hh + 16 * (c & 1) + 8 * ((lane >> 4) & 1) + 4 * (c >> 1) - (lane & 12)) * 2;
    lptr kl = F + CvL<B>::NF * 1024 + (32 + n - hh) * 16;
    v4u dg;
#pragma unroll
    for (int q = 0; q < 4; ++q) { const int k = (lane & 35) ? -1 : (n & 4) + (n >> 3); dg[q] = k == 2 * q ? D : k == 2 * q + 1 ? D << 16 : 0u; }
    v4u l[4], h[4]; v2u fc[4], fh[4];
    f32x16 acc[4];
#pragma unroll
    for (int j = 0; j < 4; ++j) { l[j] = lds_r16(kl + j * KP); if constexpr (B) h[j] = lds_r16(kl + j * KP + 512); }
#pragma unroll
    for (int j = 0; j < 2; ++j) { fc[j] = lds_tr(fa + FO - 32 * j); if constexpr (B) fh[j] = lds_tr(fa - 32 * j); }
    asm volatile("s_nop 1" : "+v"(dg));
#pragma unroll
    for (int j = 0; j < 4; ++j) asm volatile("v_mfma_f32_32x32x16_bf16 %0, %1, %2, 0" : "=&v"(acc[j]) : "v"(dg), "v"(B ? h[j] : l[j]));
    constexpr int U = B ? 2 : 4;
    kl -= 32 * U; fa -= 32;
#pragma unroll 1
    for (int m = 0; m < 32; m += 2 * U) {
#pragma unroll
        for (int z = 0; z < U; ++z) {
            const int c = 2 * (z & 1);
            const v4u f = __builtin_shufflevector(fc[c], fc[c + 1], 0, 1, 2, 3), g = __builtin_shufflevector(fh[c], fh[c + 1], 0, 1, 2, 3);
#pragma unroll
            for (int j = 0; j < 4; ++j) {
                M32V(acc[j], f, l[j]); l[j] = lds_r16(kl + j * KP + 32 * (U - z - 1));
                if (j < 2) fc[(c ^ 2) + j] = lds_tr(fa + FO + (z + 1) * 128 + 32 - 32 * j);
                SCHED();
            }
            if constexpr (B) {
#pragma unroll
                for (int j = 0; j < 4; ++j) {
                    M32V(acc[j], g, h[j]); h[j] = lds_r16(kl + j * KP + 512 + 32 * (U - z - 1));
                    if (j < 2) fh[(c ^ 2) + j] = lds_tr(fa + (z + 1) * 128 + 32 - 32 * j);
                    SCHED();
                }
            }
        }
        kl -= 32 * U; fa += 128 * U;
    }
    asm volatile("s_nop 11" : "+v"(acc[0]), "+v"(acc[1]), "+v"(acc[2]), "+v"(acc[3]));
    const float o[3] = {acc[1][15] + acc[2][14] + acc[3][13], acc[2][15] + acc[3][14], acc[3][15]};
    if constexpr (!B) st16(rs, lane == 63 ? W_SP + (u32)d * 16u : OOB, (v4u){u_(o[0]), u_(o[1]), u_(o[2]), 0u});
#pragma unroll
    for (int i = 0; i < 16; ++i) {
        float v = acc[0][i];
#pragma unroll
        for (int j = 1; j < 4; ++j) if (j <= i) v += acc[j][i - j];
        if (i < 3) { const float e = f_((u32)__builtin_amdgcn_ds_bpermute((hh ? lane - 32 : lane + 31) * 4, (int)u_(o[i]))); v += lane == 0 ? sp[i] : e; }
        y[i] = v;
    }
}
DEV void cv_put(lptr F, int pr, const float (&y0)[16], const float (&y1)[16]) {
    const int lane = threadIdx.x & 63;
#pragma unroll
    for (int i = 0; i < 16; ++i) *(LAS u32*)(F + CV_Y + (lane & 31) * CV_YP + (lane >> 5) * 128 + i * 8 + pr * 4) = pk(y0[i], y1[i]);
}
DEV void cv_out(rsrc_t rs, lptr F, int u, int blk0) {
    const int lane = threadIdx.x & 63;
    lptr q = F + CV_Y + ((lane >> 2) & 3) * CV_YP + ((lane >> 4) * 4 + (lane & 3)) * 8;
    const u32 yo = W_Y + (u32)(((lane >> 2) * 8 + blk0) * DM + 4 * u + (lane & 3)) * 16u;
#pragma unroll
    for (int i = 0; i < 8; ++i) { lptr a = q + (i >> 1) * (8 * CV_YP) + (i & 1) * 128; st16(rs, yo + (u32)((i & 1) * 128 + (i >> 1)) * (DM * 16), tr2(a, a + 4 * CV_YP)); }
}
struct KvIn { v2u k[19], v[16]; v4u s[2]; };
DEV void kvt_load(const Params& p, int tile, KvIn& in) {
    const int lane = threadIdx.x & 63;
    const rsrc_t rk = mk_rsrc(p.bk, ACT), rv = mk_rsrc(p.bv, ACT), rh = mk_rsrc(p.shift, DM * 8);
    const int row = (tile / 10) * 64 + (lane >> 4) * 16, col = (tile % 10) * 64 + (lane & 15) * 4;
    const u32 vo = (u32)(row * DM + col) * 2u, vp = row > 0 ? vo - 3 * (DM * 2) : OOB;
#pragma unroll
    for (int i = 0; i < 2; ++i) in.s[i] = ldin16(rh, (u32)col * 8u + i * 16, 0);
#pragma unroll
    for (int i = 0; i < 19; ++i) { in.k[i] = i < 3 ? ldin8(rk, vp, i * (DM * 2)) : ldin8(rk, vo, (i - 3) * (DM * 2)); if (i >= 3) in.v[i - 3] = ldin8(rv, vo, (i - 3) * (DM * 2)); }
}
DEV void kvt_make(rsrc_t rs, lptr xt, int tile, const KvIn& in) {
    const int lane = threadIdx.x & 63;
#pragma unroll
    for (int c = 0; c < 2; ++c) {
        const v4u sv = in.s[c];
        const f32x2 sh[4] = {{lo_f(sv.x), lo_f(sv.z)}, {hi_f(sv.x), hi_f(sv.z)}, {lo_f(sv.y), lo_f(sv.w)}, {hi_f(sv.y), hi_f(sv.w)}};
        f32x2 kf[19];
#pragma unroll
        for (int i = 0; i < 19; ++i) kf[i] = up2(in.k[i][c]);
        u32 kv[16];
#pragma unroll
        for (int i = 0; i < 16; ++i) kv[i] = pk2(((kf[i] * sh[3] + kf[i + 1] * sh[2]) + (kf[i + 2] * sh[1] + kf[i + 3] * sh[0])) * up2(in.v[i][c]));
#pragma unroll
        for (int q = 0; q < 8; ++q) {
            const u32 sel = (q & 1) ? 0x07060302u : 0x05040100u; const u32* z = kv + (q >> 1);
            *(LAS v2u*)(xt + ((2 * c + (q & 1)) * 16 + (lane & 15)) * 144 + (lane >> 5) * 64 + (q >> 1) * 16 + ((lane >> 4) & 1) * 8) =
                (v2u){__builtin_amdgcn_perm(z[4], z[0], sel), __builtin_amdgcn_perm(z[12], z[8], sel)};
        }
    }
    const u32 o = W_KVT + (u32)(((tile % 10) * 64 + 4 * (lane >> 3)) * SEQ + (tile / 10) * 64 + (lane & 7) * 8) * 2u;
#pragma unroll
    for (int i = 0; i < 8; ++i) st16(rs, o + (u32)(32 * (i & 1) + (i >> 1)) * (SEQ * 2), lds_r16(xt + (8 * i + (lane >> 3)) * 144 + (lane & 7) * 16));
}
struct RowX { v2u a, b; u32 c; };
DEV void ln_row(rsrc_t rs, int row, const RowX& r) {
    const f32x2 x[5] = {up2(r.a.x), up2(r.a.y), up2(r.b.x), up2(r.b.y), up2(r.c)};
    const f32x2 st = ln_stats(x);
    u32 hp[5];
#pragma unroll
    for (int q = 0; q < 5; ++q) hp[q] = pk2(up2(pkv(x[q] - st.x)) * st.y);
    store_h(rs, W_H1, row, hp);
}
DEV void pre_rings(const Params& p, rsrc_t rs, int j) {
    const u32 gi = (u32)(j * 256 + threadIdx.x), gt = gi * 16u, R = NPRE * 4096, n = ((gt >> 14) << 9) + ((gt & 8191u) >> 4);
    v4u m = {PSENT, PSENT, PSENT, PSENT};
    if (n < 640u) {
        m = *(const v4u*)(p.mem0 + (2 * ((n >> 2) & 3) + (n & 1)) * DM + (n >> 4) * 16 + (n & 2) * 4);
#pragma unroll
        for (int i = 0; i < 4; ++i) m[i] = m[i] == PSENT ? 0x7FC17FC1u : m[i];
    }
    st4s(rs, gi - 36u * 256u < F_CLR ? foff(gi - 36u * 256u) : OOB, 0, 0u);
#pragma unroll 1
    for (u32 o = gt; o < MB; o += R) st16(rs, W_ORING + o, (v4u){PSENT, PSENT, PSENT, PSENT});
    const u32 mo = gt < 4 * 32768 + 16384 ? W_MRING + gt : OOB;
    st16(rs, mo, m);
    st16(rs, mo + MB / 4, m);
    static_assert(NPRE * 4096 >= 4 * 32768 + 16384, "memory ring");
}
constexpr int LN0 = NPRE * 4;
DEV void conv_stage(const Params& p, rsrc_t rs, lptr lds, int j, int g0, int ge, v2u ep) {
    const int tid = threadIdx.x, lane = tid & 63, w = uni(tid >> 6), u = j * 4 + w;
    lptr F = lds + w * CV_W;
    const u32 fb = uu(w * CV_W);
    const rsrc_t rd = mk_rsrc(p.ssmD, DM * 2);
    float y0[16], y1[16];
    if (j < NPRE) {
        constexpr int CH = CvL<false>::CH;
        const rsrc_t rx = mk_rsrc(p.x, ACT);
        KvIn in;
        RowX r;
        r.a = ldin8(rx, lane * 8u, uu(u * (DM * 2))); r.b = ldin8(rx, lane * 8u + 512u, uu(u * (DM * 2)));
        r.c = __builtin_amdgcn_raw_buffer_load_b32(rx, lane * 4u + 1024u, uu(u * (DM * 2)), 2);
        kvt_load(p, u, in);
        pre_rings(p, rs, j);
        ln_row(rs, u, r);
        DRAIN(); BAR();
        pre_post(rs, j, 1u, ep);
        if (j == 20) LST(260, rt32());
        const u32 D = __builtin_amdgcn_raw_buffer_load_b16(rd, (u32)cv_chan(u, lane & 3) * 2u, 0, 0);
#pragma unroll 1
        for (int i = 0; i < 4; ++i) cv_filt<false>(p, fb + i * CH, cv_chan(u, i));
        kvt_make(rs, F + CV_Y, u, in);
        cv_zero<false>(F); cv_zero<false>(F + 2 * CH);
        DRAIN(); BAR();
        pre_post(rs, j, 2u, ep);
        if (j == 20) LST(262, rt32());
        pre_wait(rs, 0, NPRE, 2u, ep);
        if (j == 20) LST(281, rt32());
        {
            v4u k[8];
#pragma unroll
            for (int i = 0; i < 4; ++i) cv_kvld<false>(rs, cv_chan(u, i), k + 2 * i);
#pragma unroll
            for (int i = 0; i < 4; ++i) cv_kvput<false>(F + i * CH, k + 2 * i);
        }
        CFENCE();
        kvt_load(p, u + LN0, in);
        if (j == 20) LST(286, rt32());
#pragma unroll 1
        for (int i = 0; i < 4; i += 2) {
            cv_run<false>(p, rs, F + i * CH, cv_chan(u, i), rdl(D, i), f32x4{}, y0);
            cv_run<false>(p, rs, F + (i + 1) * CH, cv_chan(u, i + 1), rdl(D, i + 1), f32x4{}, y1);
            cv_put(F, i >> 1, y0, y1);
        }
        if (j == 20) LST(287, rt32());
        cv_out(rs, F, u, 0);
        DRAIN(); BAR();
        pre_post(rs, j, 3u, ep);
        if (j == 20) LST(263, rt32());
        kvt_make(rs, F + CV_Y, u + LN0, in);
    } else pol_probe(rs, u - NPRE * 4);
    DRAIN(); BAR();
    if (g0 < ge) {
        constexpr int CH = CvL<true>::CH;
        pre_post(rs, j, 4u, ep);
        cv_zero<true>(F);
        pre_wait(rs, 0, NPRE, 4u, ep);
        v4u k[4];
        auto fetch = [&](u32 dst, int d) { cv_filt<true>(p, dst, d); cv_kvld<true>(rs, d, k); };
        fetch(fb, cv_chan(g0 * 4 + w, 0));
#pragma unroll 1
        for (int g = g0; g < ge; g += 8) {
            const int un = g * 4 + w;
            const u32 ch = (u32)cv_chan(un, lane & 3);
            u32 D = __builtin_amdgcn_raw_buffer_load_b16(rd, ch * 2u, 0, 0);
            v4u sp = ld16s(rs, W_SP + ch * 16u, 0);
            asm volatile("" : "+v"(D), "+v"(sp));
            if (g == 16) { LST(282, rt32()); LST(288, __builtin_amdgcn_s_memtime()); }
#pragma unroll 1
            for (int i = 0; i < 4; i += 2) {
                cv_kvput<true>(F, k);
                fetch(fb + CH, cv_chan(un, i + 1));
                cv_run<true>(p, rs, F, cv_chan(un, i), rdl(D, i), (f32x4){f_(rdl(sp.x, i)), f_(rdl(sp.y, i)), f_(rdl(sp.z, i)), 0.f}, y0);
                cv_kvput<true>(F + CH, k);
                fetch(fb, i == 0 ? cv_chan(un, 2) : cv_chan(min(un + 32, 159), 0));
                cv_run<true>(p, rs, F + CH, cv_chan(un, i + 1), rdl(D, i + 1), (f32x4){f_(rdl(sp.x, i + 1)), f_(rdl(sp.y, i + 1)), f_(rdl(sp.z, i + 1)), 0.f}, y1);
                cv_put(F, i >> 1, y0, y1);
            }
            cv_out(rs, F, un, 4);
            if (g == 16) { LST(283, rt32()); LST(289, __builtin_amdgcn_s_memtime()); }
        }
        DRAIN(); BAR();
    }
    pre_post(rs, j, 5u, ep);
    if (j == 20) LST(284, rt32());
    if (j == 3) LST(285, rt32());
}

#define M32A(first, d, a, b) do { if (first) asm volatile("v_mfma_f32_32x32x16_bf16 %0, %1, %2, 0" : "=&v"(d) : "a"(a), "v"(b)); \
                                  else asm volatile("v_mfma_f32_32x32x16_bf16 %0, %1, %2, %0" : "+v"(d) : "a"(a), "v"(b)); } while (0)
#define M32B(first, d, a, b) do { if (first) asm volatile("v_mfma_f32_32x32x16_bf16 %0, %1, %2, 0" : "=&v"(d) : "v"(a), "a"(b)); \
                                  else asm volatile("v_mfma_f32_32x32x16_bf16 %0, %1, %2, %0" : "+v"(d) : "v"(a), "a"(b)); } while (0)
#define VCVT(d, a, b) asm volatile("v_cvt_pk_bf16_f32 %0, %1, %2" : "=v"(d) : "v"(a), "v"(b))
#define VADD(d, a, b) asm volatile("v_add_f32 %0, %1, %2" : "=v"(d) : "v"(a), "v"(b))
#define LD4A(d, vo, so) asm volatile("buffer_load_dword %0, %1, %2, %3 offen sc1" : "=v"(d) : "v"(vo), "s"(srd), "s"(so) : "memory")
DEV u32 wst_src(int lane, int ld) { return (u32)((lane >> 4) * ld + 8 * ((lane & 15) ^ ((lane >> 4) << 2))) * 2u; }
DEV u32 wst_rd(int lane, int w) { const int q4 = (lane >> 2) & 3; return (u32)(q4 * 256 + ((w ^ q4) * 64) + ((lane >> 4) & 1) * 32 + (lane & 3) * 8); }

constexpr int QK_SEL = 81920;
#define QK_LEAD 12
#define QK_SMP 56
#define QK_W0 8
DEV void role_qkv(const Params& p, lptr lds, int c, v2u ep) {
    const int tid = threadIdx.x, lane = tid & 63, w = uni(tid >> 6), hh = lane >> 5, r32 = lane & 31;
    const int m = c >> 5, h = c & 31;
    const rsrc_t rs = mk_rsrc(p.ws, W_END);
    v4u wt[40];
    {
        const v4u sw = mk_srd(m == 0 ? p.Wq : m == 1 ? p.Wk : p.Wv, DM * NQ * 2), sx = mk_srd(p.x, ACT);
        const u32 vo = wst_src(lane, NQ) + (u32)h * (DH * 2), x8 = (u32)lane * 8u, x4 = (u32)lane * 4u;
        RowX xr[5];
        auto wfill = [&](int i0, int n) {
#pragma unroll 1
            for (int i = i0; i < i0 + n; ++i) DMAIN(sw, uu((4 * i + w) * 1024), vo, uu((16 * i + 4 * w) * (NQ * 2)));
        };
        wfill(0, QK_W0);
#pragma unroll
        for (int i = 0; i < 5; ++i)
            asm volatile("buffer_load_dwordx2 %0, %3, %5, %6 offen\n\tbuffer_load_dwordx2 %1, %3, %5, %6 offen offset:512\n\tbuffer_load_dword %2, %4, %5, %6 offen offset:1024"
                         : "=&v"(xr[i].a), "=&v"(xr[i].b), "=&v"(xr[i].c) : "v"(x8), "v"(x4), "s"(sx), "s"(uu((LN0 + 4 * c + w + 384 * i) * (DM * 2))) : "memory");
        wfill(QK_W0, 40 - QK_W0);
        if (c == 3) LST(275, rt32());
        VMW(40 - QK_W0);
        if (c == 3) LST(276, rt32());
#pragma unroll
        for (int i = 0; i < 5; ++i) {
            asm volatile("" : "+v"(xr[i].a), "+v"(xr[i].b), "+v"(xr[i].c));
            const int row = LN0 + 4 * c + w + 384 * i;
            if (i < 4 || row < SEQ) ln_row(rs, row, xr[i]);
        }
        if (c == 3) LST(264, rt32());
        VMW(12); BAR();
        lptr tb = lds + hh * 2048 + wst_rd(lane, w);
        SFOR(k, 40)
            lptr q = tb + (k >> 3) * 32768 + (k & 7) * 4096; wt[k] = tr2(q, q + 1024); asm volatile("" : "+a"(wt[k]));
        SEND
        DRAIN(); BAR();
        pre_post(rs, 96 + c, 1u, ep);
    }
    if (c == 3) LST(268, rt32());
    bool dead = false;
    int n0, n1;
    const u32 t16 = (u32)tid * 16u, wb = uu(w * 1024);
    u32 ar[16];
#pragma unroll
    for (int j = 0; j < 16; ++j) ar[j] = (u32)(r32 * 256 + ((2 * (j & 7) + hh) ^ (r32 & 15)) * 16 + (j >> 3) * 49152);
    MK_SRD();
#define XRD(y, ks) lds_r16(lds + ar[((ks) & 7) + ((ks) >= 24 ? 8 : 0)] + ((ks) >> 3) % 3 * 16384 + (y) * 8192)
#define QDMA(y, i, vo, so) DMA16I(wb, ((i) >> 1) * 16384 + ((i) & 1) * 4096 + (y) * 8192, vo, so, (i) * 4096)
    const u32 h2flag = (lane < 32 && w == 0) ? foff(F_H2 + lane) : OOB;
    const u32 ol = (m == 2 ? (u32)(2 * w + (r32 >> 4)) * 1024u : (u32)((r32 >> 4) * 4 + w) * 1024u) + (u32)(hh * 16 + (r32 & 15)) * 16u
                 + (m == 2 ? W_VO : m == 1 ? W_KQ : W_KQ + 8192u) + (u32)h * 16384u;
    const u32 fo = tid < 2 ? foff(F_QKV + tid * 96 + c) : OOB;
    lptr selw = lds + QK_SEL + (w == 0 ? 0 : 64), selr = lds + QK_SEL;
    auto late = [&](u32 fv) { const u32 need = n1 >= NSEG ? 0xFFFFFFFFu : dead ? 0u : (u32)n1 + 1u; return (u32)__builtin_amdgcn_ballot_w64(fv < need); };
    static_assert(W_H2 - W_H1 == 2 * 64 * (32 * DM * 2), "h");
    pre_wait(rs, 0, NPRE, 1u, ep);
    if (c == 3) LST(269, rt32());
    auto run = [&](auto isv) __attribute__((always_inline)) {
        constexpr bool V = decltype(isv)::value;
        f32x16 ax, ay = {};
        v4u x[8], pp, pq;
        u32 oy = OOB, lt = 1, fcnt = 0, fa = OOB;
        u32 nn = 0;
        PIN(lt); PIN(nn);
#pragma unroll
        for (int z = 0; z < 5; ++z) x[z] = XRD(1, 20 + z);
#define QPK(r_, a1, a_, q_) do { if constexpr (V) VCVT(r_[q_], a1[4 * (a_) + 8 * ((q_) >> 1) + 2 * ((q_) & 1)], a1[4 * (a_) + 8 * ((q_) >> 1) + 2 * ((q_) & 1) + 1]); \
                                 else VCVT(r_[q_], a1[4 * (a_) + (q_)], a1[4 * (a_) + (q_) + 8]); } while (0)
#define QPK4(r_, a1, a_) do { QPK(r_, a1, a_, 0); QPK(r_, a1, a_, 1); QPK(r_, a1, a_, 2); QPK(r_, a1, a_, 3); } while (0)
        for (;;) {
            u32 ls = uu(lt);
            n0 = uni((int)nn); n1 = n0 >> 8; n0 &= 255;
            if (ls != 0u && n0 >= min(NSEG, n1 + QK_LEAD)) {
                CFENCE();
                u32 fv = ld4(rs, h2flag);
                asm volatile("s_waitcnt vmcnt(0)" : "+v"(fv) :: "memory");
                *(LAS u32*)(selw + 16) = late(fv);
                BAR();
                st4s(rs, fa, 0, fcnt);
                fa = OOB;
                if (n1 >= NSEG) __builtin_amdgcn_endpgm();
                if (uu(*(LAS u32*)(selr + 16)) != 0u) {
                    if (w == 0) wait_flags(rs, h2flag, (u32)n1 + 1, dead);
                    BAR();
                }
                ls = 0u;
            }
            if (n0 == 4) pre_wait(rs, 96, 96, 1u, ep);
            int cur = n0;
            asm volatile("s_cmp_eq_u32 %3, 0\n\ts_cselect_b32 %0, %4, %0\n\ts_addc_u32 %2, %2, 0\n\ts_cmp_lg_u32 %3, 0\n\ts_addc_u32 %1, %1, 0"
                         : "+s"(cur), "+s"(n0), "+s"(n1) : "s"(ls), "s"(n1 | 64) : "scc");
            u32 xs = W_H1 + (u32)(cur + (cur & 64)) * (32 * DM * 2), ys, yvo, ox, fv;
            PINS(xs);
#define QT(k) do {} while (0)
            QT(0);
            SCHED();
            QDMA(0, 0, t16, xs); QDMA(0, 1, t16, xs);
            SFOR(z, 80)
                constexpr int ks = (z + 20) % 40, zn = (z + 5) % 80;
                if constexpr (z >= 20 && z < 60) { if constexpr (V) M32B(ks == 0, ax, x[z & 7], wt[ks]); else M32A(ks == 0, ax, wt[ks], x[z & 7]); }
                else { if constexpr (V) M32B(ks == 0, ay, x[z & 7], wt[ks]); else M32A(ks == 0, ay, wt[ks], x[z & 7]); }
                x[(z + 5) & 7] = XRD(zn < 20 || zn >= 60, (zn + 20) % 40);
                if constexpr (z == 77) lt = *(LAS u32*)selr;
                SCHED();
                if constexpr (z < 8) QDMA(0, z + 2, t16, xs);
                if constexpr (z == 9) { ys = W_H1 + (u32)n0 * (32 * DM * 2); yvo = t16 | (n0 < NSEG ? 0u : OOB); PINS(ys); PIN(yvo); }
                if constexpr (z == 11) { ox = ol + (u32)cur * (NH * 16384u); PIN(ox); }
                if constexpr (z == 14) { QT(1); asm volatile("s_waitcnt vmcnt(6) lgkmcnt(0)\n\ts_barrier" ::: "memory"); QT(2); }
                if constexpr (z == 15) st4s(rs, fa, 0, fcnt);
                if constexpr (z >= 16 && z < 26) QDMA(1, z - 16, yvo, ys);
                if constexpr (z == 26) QPK4(pp, ay, 0);
                if constexpr (z == 27) st16(rs, oy, pp);
                if constexpr (z == 28) QPK4(pq, ay, 1);
                if constexpr (z == 29) st16(rs, oy + 512, pq);
                if constexpr (z == 30) { QT(3); asm volatile("s_waitcnt vmcnt(13)\n\ts_barrier" ::: "memory"); QT(4); }
                if constexpr (z == 32) { fcnt = lane == 0 ? (u32)n0 : (u32)n1; fa = fo; asm volatile("" : "+v"(fcnt), "+v"(fa)); }
                if constexpr (z == 34) { oy = (ol + (u32)n0 * (NH * 16384u)) | (yvo & OOB); PIN(oy); }
                if constexpr (z == 35) { nn = (u32)(min(n0 + 1, NSEG) | n1 << 8); PIN(nn); }
                if constexpr (z == 54) { asm volatile("s_waitcnt vmcnt(2) lgkmcnt(0)\n\ts_barrier" ::: "memory"); QT(5); }
                if constexpr (z == QK_SMP) LD4A(fv, h2flag, 0);
                if constexpr (z == 62) QPK4(pp, ax, 0);
                if constexpr (z == 63) st16(rs, ox, pp);
                if constexpr (z == 64) QPK4(pq, ax, 1);
                if constexpr (z == 65) st16(rs, ox + 512, pq);
                if constexpr (z == 73) { asm volatile("s_waitcnt vmcnt(2)" : "+v"(fv) :: "memory"); QT(6); ls = late(fv); PINS(ls); }
                if constexpr (z == 74) { lt = ls; PIN(lt); *(LAS u32*)selw = lt; }
                if constexpr (z == 76) asm volatile("s_waitcnt lgkmcnt(2)\n\ts_barrier" ::: "memory");
                SCHED();
            SEND
        }
    };
    if (m == 2) run(IC<1>{}); else run(IC<0>{});
}

#define OU_SMP 44
DEV void role_out(const Params& p, lptr lds, int L, int kq, int ng, v2u ep) {
    const int tid = threadIdx.x, lane = tid & 63, w = uni(tid >> 6), hh = lane >> 5, r32 = lane & 31;
    const rsrc_t rs = mk_rsrc(p.ws, W_END);
    v4u wo[64];
    bool dead = false;
    {
        const v4u sw = mk_srd(p.Wo, DM * NQ * 2);
        const u32 vo = wst_src(lane, DM) + (u32)ng * 256u;
        auto fill = [&](int b) {
#pragma unroll 1
            for (int i = 0; i < 8; ++i) DMAIN(sw, uu((b % 5) * 32768 + (4 * i + w) * 1024), vo, uu((kq * 1024 + b * 128 + 16 * i + 4 * w) * (DM * 2)));
        };
#pragma unroll 1
        for (int b = 0; b < 5; ++b) fill(b);
        lptr tb = lds + hh * 1024 + wst_rd(lane, w);
        SFOR(b, 8)
            VMW(b < 4 ? 32 : 56 - 8 * b); BAR();
            lptr q = tb + (b % 5) * 32768;
#pragma unroll
            for (int k = 0; k < 8; ++k) { lptr a = q + (k >> 1) * 8192 + (k & 1) * 2048; wo[8 * b + k] = tr2(a, a + 4096); if (b < 7) asm volatile("" : "+a"(wo[8 * b + k])); else PIN(wo[8 * b + k]); }
            if constexpr (b < 3) { BAR(); fill(b + 5); }
        SEND
    }
    pre_wait(rs, 0, NPRE, 1u, ep);
    if (kq == 0 && ng == 0) LST(272 + L, rt32());
    MK_SRD();
    const u32 ofl = lane < 8 ? foff(F_O + L * 32 + kq * 8 + lane) : OOB, l16 = (u32)lane * 16u;
    const u32 fo = (tid == 0) ? foff(F_P + L * 32 + kq * 5 + ng) : OOB;
    const u32 src0 = uu(W_VO + 8192u + (u32)(L * NSEG * NH + kq * 8) * 16384u + w * 1024);
#define ODMA(i, dst, so) DMA16I(dst, (i) * 4096, l16, so, ((i) >> 1) * 16384 + ((i) & 1) * 4096)
    auto first = [&](int s) { const u32 dst = uu((s & 1) * 65536 + w * 1024), src = src0 + (u32)s * (NH * 16384u); SFOR(i, 10) ODMA(i, dst, src); SEND };
    u32 pa = W_P + (u32)(L * NSEG) * P_ITEM + (u32)(4 * hh * 4 + kq) * (DM * 4) + (u32)(ng * 128 + w * 32 + r32) * 4u, fv = 0;
    bool early = false;
    for (int s = 0; s < NSEG; ++s) {
        const u32 dst = uu((s & 1) * 65536 + w * 1024), src = src0 + (u32)s * (NH * 16384u);
        lptr A = lds + (s & 1) * 65536 + l16;
        if (!early) {
            if (!all_(ofl == OOB || fv >= (u32)s + 1)) wait_flags(rs, ofl, (u32)s + 1, dead);
            LS3(L == 0 && w == 1, 1, kq * 5 + ng);
            first(s);
        }
        asm volatile("s_waitcnt vmcnt(8)\n\ts_barrier" ::: "memory");
        v4u o[64];
#pragma unroll
        for (int k = 0; k < 3; ++k) o[k] = lds_r16(A + k * 1024);
        f32x16 acc;
        SCHED();
        SFOR(k, 64)
            if constexpr (k < 56) M32B(k == 0, acc, o[k], wo[k]); else M32V(acc, o[k], wo[k]);
            if constexpr (k < 61) o[k + 3] = lds_r16(A + (k + 3) * 1024);
            SCHED();
            if constexpr (k < 12 && (k & 1) == 0) ODMA(10 + k / 2, dst, src);
            if constexpr (k == 4) asm volatile("s_waitcnt vmcnt(7)\n\ts_barrier" ::: "memory");
            if constexpr (k == 20) asm volatile("s_waitcnt vmcnt(5)\n\ts_barrier" ::: "memory");
            if constexpr (k == 40) asm volatile("s_waitcnt vmcnt(0)\n\ts_barrier" ::: "memory");
            if constexpr (k == OU_SMP) LD4A(fv, ofl, 0);
            SCHED();
        SEND
        asm volatile("s_nop 9" : "+v"(acc), "+v"(pa));
#pragma unroll
        for (int i = 0; i < 16; ++i) st4s(rs, pa, (u32)((8 * (i >> 2) + (i & 3)) * (4 * DM * 4)), u_(acc[i]));
        asm volatile("s_waitcnt vmcnt(16)" : "+v"(fv) :: "memory");
        LS3(L == 0 && w == 1, 2, kq * 5 + ng);
        early = s + 1 < NSEG && all_(ofl == OOB || fv >= (u32)s + 2);
        if (early) { first(s + 1); VMW(10); }
        else { fv = ld4(rs, ofl); asm volatile("s_waitcnt vmcnt(0)" : "+v"(fv) :: "memory"); }
        BAR();
        st4s(rs, fo, 0, (u32)s + 1);
        pa += P_ITEM;
        LS2(L == 0 && w == 1, 160 + kq * 5 + ng);
    }
}

constexpr int RW_P = 20480, RW_X = RW_P + 10240;
DEV void role_row(const Params& p, lptr lds, int id, v2u ep) {
    const int lane = threadIdx.x & 63, w = uni(threadIdx.x >> 6);
    const int ri = id >> 1, L = id & 1;
    const rsrc_t rs = mk_rsrc(p.ws, W_END);
    bool dead = false, have = false;
    const u32 fp = lane < 20 ? foff(F_P + L * 32 + lane) : OOB, l16 = (u32)lane * 16u, lx = lane < 16 ? l16 + 1024u : OOB, fb = uu(w * CV_W);
    lptr F = lds + w * CV_W;
    MK_SRD();
    const v4u sx = mk_srd(p.x, ACT);
    auto yfill = [&](int b) {
#pragma unroll 1
        for (int i = 0; i < 5; ++i) DMA32(uu(fb + (b & 1) * 10240 + i * 2048), l16, uu(W_Y + (u32)(ri * 8 + b) * 10240u + i * 2048));
    };
    auto xfill = [&](int s) {
        const u32 d = uu(fb + RW_X + (s & 1) * 2048), o = uu((u32)(s * 32 + ri) * (DM * 2));
        if (L == 0) { DMAIN(sx, d, l16, o); DMAIN(sx, d + 1024, lx, o); } else { DMA16(d, l16, o + W_X2); DMA16(d + 1024, lx, o + W_X2); }
    };
    auto pfill = [&](int s) {
        const u32 o = uu(W_P + (u32)(L * NSEG + s) * P_ITEM + (u32)ri * (4 * DM * 4));
        SFOR(i, 10) DMA16I(fb, RW_P + i * 1024, l16, o, i * 1024); SEND
        if (L == 1) xfill(s);
    };
    if (L == 0) xfill(0);
    pre_poll(rs, 0, NPRE, 3u, ep);
    if (id == 0) LST(274, rt32());
    yfill(0); DRAIN();
    u32 fv = 0;
    for (int s = 0; s < NSEG; ++s) {
        if (!have) {
            if (!all_(fp == OOB || fv >= (u32)s + 1)) wait_flags(rs, fp, (u32)s + 1, dead);
            LS3(L == 0, 3, ri);
            pfill(s);
        }
        LS2(L == 0, 192 + ri);
        f32x2 y[5];
        {
            lptr ya = F + ((s >> 3) & 1) * 10240 + (s & 7) * 2 + lane * 16;
            auto yr = [&](lptr q) { return lo_f(*(LAS u16*)q); };
            y[0] = (f32x2){yr(ya), yr(ya + 2560)}; y[1] = (f32x2){yr(ya + 5120), yr(ya + 7680)};
            y[2] = (f32x2){yr(ya + 1024), yr(ya + 3584)}; y[3] = (f32x2){yr(ya + 6144), yr(ya + 8704)};
            lptr yc = ya - lane * 16 + 2048 + (lane >> 1) * 16 + (lane & 1) * 5120;
            y[4] = (f32x2){yr(yc), yr(yc + 2560)};
            asm volatile("" : "+v"(y[0]), "+v"(y[1]), "+v"(y[2]), "+v"(y[3]), "+v"(y[4]));
        }
        if (!have) DRAIN();
        LS3(L == 0, 4, ri);
        f32x2 x1[5];
        {
            f32x4 a[4], b[4]; f32x2 c[4];
            lptr q = F + RW_X + (s & 1) * 2048;
            v2u xa = *(LAS v2u*)(q + lane * 8), xb = *(LAS v2u*)(q + 512 + lane * 8); u32 xc = *(LAS u32*)(q + 1024 + lane * 4);
#pragma unroll
            for (int kq = 0; kq < 4; ++kq) { lptr r = F + RW_P + kq * (DM * 4); a[kq] = *(LAS f32x4*)(r + l16); b[kq] = *(LAS f32x4*)(r + 1024 + l16); c[kq] = *(LAS f32x2*)(r + 2048 + lane * 8); }
            SCHED();
            const f32x4 sa = (a[0] + a[1]) + (a[2] + a[3]), sb = (b[0] + b[1]) + (b[2] + b[3]);
            const f32x2 o[5] = {sa.lo, sa.hi, sb.lo, sb.hi, (c[0] + c[1]) + (c[2] + c[3])};
            const u32 xi[5] = {xa.x, xa.y, xb.x, xb.y, xc};
#pragma unroll
            for (int i = 0; i < 5; ++i) x1[i] = up2(xi[i]) + o[i];
            asm volatile("" : "+v"(x1[0]), "+v"(x1[1]), "+v"(x1[2]), "+v"(x1[3]), "+v"(x1[4]));
        }
        have = s + 1 < NSEG && all_(fp == OOB || fv >= (u32)s + 2);
        if (__builtin_expect(have, 0)) pfill(s + 1);
        if (L == 0) xfill(s + 1);
        const f32x2 st1 = ln_stats(x1);
        u32 xp[5];
#pragma unroll
        for (int i = 0; i < 5; ++i) xp[i] = pkv(x1[i] + (x1[i] - st1.x) * (y[i] * st1.y));
        const int row = s * 32 + ri;
        if (L == 0) {
            const u32 ro = (u32)row * (DM * 2);
            f32x2 x2[5];
#pragma unroll
            for (int i = 0; i < 5; ++i) x2[i] = up2(xp[i]);
            st8s(rs, (u32)lane * 8, W_X2 + ro, (v2u){xp[0], xp[1]});
            st8s(rs, (u32)lane * 8 + 512, W_X2 + ro, (v2u){xp[2], xp[3]});
            st4s(rs, (u32)lane * 4 + 1024, W_X2 + ro, xp[4]);
            const f32x2 st2 = ln_stats(x2);
            u32 hp[5];
#pragma unroll
            for (int i = 0; i < 5; ++i) hp[i] = pk2((x2[i] - st2.x) * st2.y);
            store_h(rs, W_H2, row, hp);
        } else {
            u16* o_ = p.out + (size_t)row * DM;
            *(v2u*)(o_ + 4 * lane) = (v2u){xp[0], xp[1]};
            *(v2u*)(o_ + 256 + 4 * lane) = (v2u){xp[2], xp[3]};
            *(u32*)(o_ + 512 + 2 * lane) = xp[4];
        }
        CFENCE();
        LS3(L == 0, 5, ri);
        fv = ld4(rs, fp);
        asm volatile("s_waitcnt vmcnt(0)" : "+v"(fv) :: "memory");
        st4s(rs, lane == 0 && L == 0 ? foff(F_H2 + ri) : OOB, 0, (u32)s + 1);
        if ((s & 7) == 3 && s < NSEG - 8) {
            if (s == 27) pre_poll(rs, 0, NCV, 5u, ep);
            if (s == 43) st8s(rs, (id == 1 && lane == 0) ? foff(F_EPOCH) : OOB, 0, (v2u){ep.x + 8u, ep.y});
            yfill((s >> 3) + 1);
        }
        if (ri == 0) LST(320 + L * 64 + s, rt32());
        LS2(L == 0, 224 + ri);
    }
}

constexpr float SCALE = 0.08838834764831845f, LOG2E = 1.4426950408889634f;
constexpr int C_K = 0, C_V = 8192, C_QX = 16384, C_MEM = 24576, C_Q = C_MEM + 10240, C_KM = C_Q + 2304, C_VM = C_KM + 2304, C_DUMP = C_VM + 2048,
              C_ZERO = C_DUMP + 1024, C_WL = C_ZERO + 128;
constexpr int NLF = 14;
static_assert(C_WL % 16 == 0 && C_WL + 4 * NLF * 2048 <= LDS_BYTES, "head lds");
#define CH_VIS 10
#define CH_H0 120
#define CH_W0 83
#define CH_MH 4
#define CH_MW 4
#define CH_REC 20
#define CH_UP 4
#define CH_WX 6
#define WAIT_TXT(tmp, t4) "s_memrealtime vcc\n1:\n\ts_waitcnt lgkmcnt(0)\n\ts_sub_u32 " tmp ", " t4 ", vcc_lo\n\ts_memrealtime vcc\n\ts_cmpk_lt_u32 " tmp ", 400\n\ts_cbranch_scc1 1b\n\t" \
                          "s_add_u32 " tmp ", " tmp ", 4\n\ts_cselect_b32 " tmp ", " tmp ", 0\n2:\n\ts_sub_u32 " tmp ", " tmp ", 1\n\ts_cbranch_scc0 2b\n\t"
DEV void wait_until(u32 t) { u32 tmp; asm volatile(WAIT_TXT("%0", "%1") "s_waitcnt lgkmcnt(0)" : "=&s"(tmp) : "s"(uu(t - 4u)) : "vcc", "scc", "memory"); }
#define SMF_A(acc, a, b) asm volatile("v_smfmac_f32_16x16x64_bf16 %0, %1, %2, %3" : "+v"(acc) : "v"(a), "a"(b), "v"(idx))
#define SMF_V(acc, a, b) asm volatile("v_smfmac_f32_16x16x64_bf16 %0, %1, %2, %3" : "+v"(acc) : "v"(a), "v"(b), "v"(idx))
#define MF0(d, a, b) asm volatile("v_mfma_f32_16x16x32_bf16 %0, %1, %2, 0" : "=&v"(d) : "v"(a), "v"(b))
#define MF1(d, a, b) asm volatile("v_mfma_f32_16x16x32_bf16 %0, %1, %2, %0" : "+v"(d) : "v"(a), "v"(b))
#define MF0H(d, a, b) asm volatile("v_mfma_f32_16x16x16_bf16 %0, %1, %2, 0" : "=&v"(d) : "v"(a), "v"(b))
#define MF1H(d, a, b) asm volatile("v_mfma_f32_16x16x16_bf16 %0, %1, %2, %0" : "+v"(d) : "v"(a), "v"(b))
DEV v8u tr_frag(lptr p0, lptr p1, lptr p2, lptr p3) { return cat8(tr2(p0, p1), tr2(p2, p3)); }
constexpr int qslot(int ks) { return (ks & 1) && ks < 8 ? ks >> 1 : -1; }
constexpr int kslot(int ks) { return ks < 4 ? 4 + ks : -1; }
constexpr int vslot(int ks) { return ks >= 4 ? 4 + ks : -1; }
DEV u32 ring_map(u32 off, u32 half) { return ((off >> 13) << 14) + (half << 13) + (off & 8191u); }
#define ZRD(x, i) x = __builtin_bit_cast(f32x4, lds_r16(ZP + 16 * (i)))
#define VMAX(d, a, b) asm volatile("v_max_f32 %0, %1, %2" : "=v"(d) : "v"(a), "v"(b))
#define VMAX3(d, a, b, c_) asm volatile("v_max3_f32 %0, %1, %2, %3" : "=v"(d) : "v"(a), "v"(b), "v"(c_))
#define VEXP(d, a) asm volatile("v_exp_f32 %0, %1" : "=v"(d) : "v"(a))
#define VFMC(d, a, b) asm volatile("v_fmamk_f32 %0, %1, 0x3e0293ee, %2" : "=v"(d) : "v"(a), "v"(b))
#define SEL(d, a, b) asm volatile("v_cndmask_b32 %0, %1, %2, %3" : "=v"(d) : "v"(a), "v"(b), "s"(himask))
#define QPERM(d, a, q) asm volatile("v_mov_b32_dpp %0, %1 quad_perm:" q " row_mask:0xf bank_mask:0xf" : "=v"(d) : "v"(a))

#define HD_SMP 47
DEV void role_head(const Params& p, lptr lds, int L, int h, v2u ep) {
    const int tid = threadIdx.x, lane = tid & 63, w = uni(tid >> 6), g = lane >> 4, r = lane & 15;
    const rsrc_t rs = mk_rsrc(p.ws, W_END);
    v8u wq0[10], wq1[10], wk0[10], wk1[10], wv0[10], wv1[10];
    lptr WL = lds + C_WL + w * (NLF * 2048) + lane * 16;
    {
        constexpr int GS = 1056, CS = 48 * GS;
        const v4u sq = mk_srd(p.Wq, DM * NQ * 2), sk = mk_srd(p.Wk, DM * NQ * 2), sv = mk_srd(p.Wv, DM * NQ * 2);
        const u32 vo = (u32)(g * NQ + h * DH + 8 * (r ^ ((g & 1) << 3))) * 2u;
        auto fill = [&](int ch) {
#pragma unroll 1
            for (int i = 0; i < 4; ++i) {
                const u32 dst = uu((ch % 3) * CS + (4 * i + w) * GS), so = uu((ch * 64 + 16 * i + 4 * w) * (NQ * 2));
                DMAIN(sq, dst, vo, so); DMAIN(sk, dst + 16 * GS, vo, so); DMAIN(sv, dst + 32 * GS, vo, so);
            }
        };
        __builtin_amdgcn_s_sleep(ST_HEAD);
        fill(0); fill(1); fill(2);
        const int q4 = r >> 2;
        lptr tb = lds + (4 * (g >> 1) + 2 * (q4 >> 1) + (g & 1)) * GS + (q4 & 1) * 256 + ((w * 64 + (r & 3) * 8) ^ ((q4 & 1) << 7));
        SFOR(ch, 10)
            VMW(ch < 8 ? 24 : 108 - 12 * ch);
            BAR();
            auto frag = [&](int m, int t) { lptr b = tb + (ch % 3) * CS + m * 16 * GS + t * 32; return tr_frag(b, b + 512, b + 8 * GS, b + 8 * GS + 512); };
            wq0[ch] = frag(0, 0); wq1[ch] = frag(0, 1); wk0[ch] = frag(1, 0); wk1[ch] = frag(1, 1); wv0[ch] = frag(2, 0); wv1[ch] = frag(2, 1);
            asm volatile("" : "+a"(wq0[ch]), "+a"(wk0[ch]), "+v"(wv0[ch]), "+v"(wv1[ch]));
            if constexpr (qslot(ch) < 0) asm volatile("" : "+a"(wq1[ch])); else asm volatile("" : "+v"(wq1[ch]));
            if constexpr (kslot(ch) < 0) asm volatile("" : "+a"(wk1[ch])); else asm volatile("" : "+v"(wk1[ch]));
            BAR();
            if constexpr (ch < 7) fill(ch + 3);
        SEND
#pragma unroll
        for (int ch = 0; ch < 10; ++ch) {
            if (qslot(ch) >= 0) { lds_w16(WL + qslot(ch) * 2048, wq1[ch].lo); lds_w16(WL + qslot(ch) * 2048 + 1024, wq1[ch].hi); }
            if (kslot(ch) >= 0) { lds_w16(WL + kslot(ch) * 2048, wk1[ch].lo); lds_w16(WL + kslot(ch) * 2048 + 1024, wk1[ch].hi); }
            if (vslot(ch) >= 0) { lds_w16(WL + vslot(ch) * 2048, wv1[ch].lo); lds_w16(WL + vslot(ch) * 2048 + 1024, wv1[ch].hi); }
        }
        CFENCE();
    }
    const u32 idx = (r & 2) ? 0xEEEEu : 0x4444u;
    const unsigned long long himask = 0xCCCCCCCCCCCCCCCCull;
    bool dead = false;
    if (h == 3) LST(266 + L, rt32());
    pre_wait(rs, 0, NCV, 1u, ep);
    const u32 pol0 = uu(get_pol(rs));
    const u32 l16 = (u32)lane * 16u;
    const u32 mring = W_MRING + (u32)L * (MB / 4) + pol0 * 8192u, hint = mring + 4 * 32768u;
    const u32 fkh = lane < 3 ? foff(F_QKV + L * 96 + lane * 32 + h) : lane < 7 ? hint + (u32)(lane - 3) * 4u : OOB;
    u32 tm = 0;
    MK_SRD();
    u32 ksrc = uu(W_KQ + (u32)(L * NSEG * NH + h) * 16384u + w * 2048), vsrc = ksrc + (W_VO - W_KQ);
    u32 dk = uu(C_K + w * 2048), dv = dk + (C_V - C_K), dq = dk + (C_QX - C_K), dm = uu(C_MEM + w * 2048), dm2 = uu(w < 2 ? C_MEM + 8192 + w * 1024 : C_DUMP);
    asm volatile("" : "+s"(dk), "+s"(dv), "+s"(dq), "+s"(dm), "+s"(dm2));
    auto keys_wait = [&](u32 need) {
#pragma unroll 1
        for (int n = 0; !dead; ++n) {
            wait_until(tm);
            const u32 hv = ld4(rs, lane == 7 ? foff(F_ERR) : fkh);
            tm = rdl(hv, 5);
            wait_until(rdl(hv, 3) - 30u);
            if (((u32)__builtin_amdgcn_ballot_w64(ld4(rs, fkh) < need) & 7u) == 0u) break;
            dead = n > SPIN_CAP || rdl(hv, 7) != 0u;
            st4s(rs, dead ? foff(F_ERR) : OOB, 0, 1u);
        }
        CFENCE();
        DMA32(dk, l16, ksrc); DMA32(dv, l16, vsrc); DMA32(dq, l16, ksrc ^ 8192u);
    };
    keys_wait(1u);
    lptr qw = lds + C_Q + (2 * g) * 272 + (32 * w + 2 * r) * 2;
    lptr qr = lds + C_Q + (2 * (r >> 2) + (r & 1)) * 272 + g * 16;
    lptr LB = lds + l16, VX = lds + C_V + w * 2048 + l16;
    if (tid >= 64 && tid < 70) { const u32 z = tid == 68 ? 0x3F803F80u : tid == 69 ? PSENT : 0u; lds_w16(lds + C_ZERO + (tid - 64) * 16, (v4u){z, z, z, z}); }
    u32 zaddr = 0; PIN(zaddr);
    lptr ZP = lds + zaddr + C_ZERO;
    const float CEXP = SCALE * LOG2E;
#define LF_RD(slot, hf) lf[slot][hf] = lds_r16(WL + (slot) * 2048 + (hf) * 1024)
    BAR();
    const u32 ofl = tid == 0 ? foff(F_O + L * 32 + h) : OOB;
    u32 oso = (g & 1) ? OOB : W_ORING + (u32)L * (MB / 2) + ring_map((u32)((r >> 3) * 32768 + h * 1024 + w * 256 + (g >> 1) * 128 + (r & 7) * 16), pol0);
    unsigned long long vis[8];
#pragma unroll
    for (int i = 0; i < 8; ++i) vis[i] = __builtin_amdgcn_ballot_w64(16 * (i >> 2) + 4 * g + (i & 3) <= 16 * (w & 1) + r);
    u32 missed = 0;
    for (int s = 0; s < NSEG;) {
        u32 ms = mring + (u32)(s & 3) * 32768u + w * 2048, ms2 = mring + (u32)(s & 3) * 32768u + 16384u + (w & 1) * 1024, sv = (u32)s;
        asm volatile("" : "+s"(ms), "+s"(ms2)); PIN(sv);
        f32x4 aq0, aq1, ak0, ak1;
        v4u lf[NLF][2];
        ZRD(aq0, 0); ZRD(aq1, 1); ZRD(ak0, 2); ZRD(ak1, 3);
#pragma unroll
        for (int i = 0; i < 4; ++i) { LF_RD(i, 0); LF_RD(i, 1); }
        SCHED();
        v4u A[10];
#define HT(k) do {} while (0)
        v4u kx[8], qf[4], kmf[4], vx[2], ones, P;
        float fa, fb, fc, fd, mx, my, sx[4], t[4], tm0, tm1; u32 pa, pb, fl, e01, e23, pmm; unsigned long long nanq;
        f32x4 av0, av1, s0, s1, sm, oa, ob, rsum;
        v2u vm, pm;
#define LW(sl) cat8(lf[sl][0], lf[sl][1])
#define PQ(ks, tl) do { if constexpr (!(tl)) SMF_A(aq0, A[ks], wq0[ks]); else if constexpr (qslot(ks) < 0) SMF_A(aq1, A[ks], wq1[ks]); else { const v8u lw_ = LW(qslot(ks)); SMF_V(aq1, A[ks], lw_); } } while (0)
#define PK(ks, tl) do { if constexpr (!(tl)) SMF_A(ak0, A[ks], wk0[ks]); else if constexpr (kslot(ks) < 0) SMF_A(ak1, A[ks], wk1[ks]); else { const v8u lw_ = LW(kslot(ks)); SMF_V(ak1, A[ks], lw_); } } while (0)
#define PV(ks, tl) do { if constexpr (!(tl)) SMF_V(av0, A[ks], wv0[ks]); else if constexpr (vslot(ks) < 0) SMF_V(av1, A[ks], wv1[ks]); else { const v8u lw_ = LW(vslot(ks)); SMF_V(av1, A[ks], lw_); } } while (0)
#define KXR(i) kx[i] = lds_r16(LB + C_K + ((i) & 1) * 4096 + ((i) >> 1) * 1024)
#define XW(q_) do { *(LAS u32*)(q_) = pa; *(LAS u32*)((q_) + 272) = pb; } while (0)
        auto slot = [&](auto zc) __attribute__((always_inline)) {
            constexpr int z = decltype(zc)::value;
            if constexpr (z < 14) PQ(z >> 1, z & 1);
            else if constexpr (z < 17) PQ(z - 7, 0);
            else if constexpr (z < 20) PQ(z - 10, 1);
            else if constexpr (z < 33) PK((z - 20) >> 1, z & 1);
            else if constexpr (z < 35) { if constexpr (z == 33) MF0(s0, kx[0], qf[0]); else MF0(s1, kx[1], qf[0]); }
            else if constexpr (z < 41) { if constexpr (z & 1) MF1(s0, kx[z - 33], qf[(z - 33) >> 1]); else MF1(s1, kx[z - 33], qf[(z - 33) >> 1]); }
            else if constexpr (z == 41) PK(6, 1);
            else if constexpr (z < 45) PK(z - 35, 0);
            else if constexpr (z < 48) PK(z - 38, 1);
            else if constexpr (z < 58) PV(z - 48, 0);
            else if constexpr (z < 61) PV(z < 60 ? z - 54 : 0, 1);
            else if constexpr (z == 61) MF0(sm, kmf[0], qf[0]);
            else if constexpr (z < 65) MF1(sm, kmf[z - 61], qf[z - 61]);
            else if constexpr (z == 65) MF0(oa, vx[0], P);
            else if constexpr (z == 66) MF0(ob, vx[1], P);
            else PV(6, 1);
            if constexpr (z < 6) A[4 + z] = lds_r16(LB + C_MEM + (4 + z) * 1024);
            if constexpr (z == 6 || z == 7) LF_RD(4, z - 6);
            if constexpr (z == 8 || z == 9) LF_RD(5, z - 8);
            if constexpr (z == 12 || z == 13) LF_RD(6, z - 12);
            if constexpr (z == 16 || z == 17) KXR(z - 16);
            if constexpr (z == 18 || z == 19) LF_RD(7, z - 18);
            if constexpr (z >= 20 && z < 24) KXR(z - 18);
            if constexpr (z == 32 || z == 33) KXR(z - 26);
            if constexpr (z == 24) XW(qw);
            if constexpr (z == 25) ZRD(av0, 0);
            if constexpr (z == 26) ZRD(av1, 1);
            if constexpr (z == 27) asm volatile("s_waitcnt lgkmcnt(2)\n\ts_barrier" ::: "memory");
            if constexpr (z >= 28 && z < 32) qf[z - 28] = lds_r16(qr + (z - 28) * 64);
            if constexpr (z == HD_SMP) LD4A(fl, fkh, 0);
            if constexpr (z == 52) XW(qw + (C_KM - C_Q));
            if constexpr (z == 53 || z == 54) vx[z - 53] = lds_r16(VX + (z - 53) * 1024);
            if constexpr (z == 55) asm volatile("s_waitcnt lgkmcnt(2)\n\ts_barrier" ::: "memory");
            if constexpr (z >= 56 && z < 60) kmf[z - 56] = lds_r16(qr + (C_KM - C_Q) + (z - 56) * 64);
            if constexpr (z >= 48 && z < 52) LF_RD(8 + ((z - 48) >> 1), z & 1);
            if constexpr (z >= 60) LF_RD(10 + ((z - 60) >> 1), z & 1);
            SCHED();
            if constexpr (z == 19) HT(2);
            if constexpr (z == 32) HT(3);
            if constexpr (z == 47) HT(4);
            if constexpr (z == 64) HT(5);
            if constexpr (z == 20) VADD(fa, aq0[0], aq0[2]);
            if constexpr (z == 21) VADD(fb, aq0[1], aq0[3]);
            if constexpr (z == 22) { VADD(fc, aq1[0], aq1[2]); VADD(fd, aq1[1], aq1[3]); }
            if constexpr (z == 23) { asm volatile("v_cmp_u_f32 %0, %1, %2" : "=s"(nanq) : "v"(fa), "v"(fb)); VCVT(pa, fa, fc); VCVT(pb, fb, fd); }
            if constexpr (z == 39 || z == 40 || z == 41) NOPS(1);
            if constexpr (z == 42) VMAX3(mx, s0[0], s0[1], s0[2]);
            if constexpr (z == 43) { VMAX3(mx, mx, s0[3], s1[0]); VMAX3(my, s1[1], s1[2], s1[3]); }
            if constexpr (z == 44 || z == 46) { VMAX(fa, mx, my); VMAX(my, mx, my); mx = fa; }
            if constexpr (z == 45) { SEL(sx[0], s0[0], s1[0]); asm volatile("v_permlane16_swap_b32 %0, %1" : "+v"(mx), "+v"(my)); }
            if constexpr (z == 47) { SEL(sx[1], s0[1], s1[1]); asm volatile("v_permlane32_swap_b32 %0, %1" : "+v"(mx), "+v"(my)); }
            if constexpr (z == 48) asm volatile("v_max_f32 %0, %0, %1\n\tv_mul_f32 %0, 0xbe0293ee, %0" : "+v"(mx) : "v"(my));
            if constexpr (z == 49) { VADD(fa, ak0[0], ak0[2]); VADD(fb, ak0[1], ak0[3]); }
            if constexpr (z == 50) { VADD(fc, ak1[0], ak1[2]); VADD(fd, ak1[1], ak1[3]); }
            if constexpr (z == 51) { VCVT(pa, fa, fc); VCVT(pb, fb, fd); }
            if constexpr (z == 52) SEL(sx[2], s0[2], s1[2]);
            if constexpr (z == 53) { SEL(sx[3], s0[3], s1[3]); VFMC(t[0], sx[0], mx); }
            if constexpr (z == 54) { VFMC(t[1], sx[1], mx); VFMC(t[2], sx[2], mx); }
            if constexpr (z == 55) VFMC(t[3], sx[3], mx);
            if constexpr (z >= 56 && z < 60) VEXP(t[z - 56], t[z - 56]);
            if constexpr (z == 60) { VCVT(e01, t[0], t[1]); VCVT(e23, t[2], t[3]); }
            if constexpr (z == 61) { QPERM(P.x, e01, "[0,1,0,1]"); QPERM(P.z, e01, "[2,3,2,3]"); }
            if constexpr (z == 62) { QPERM(P.y, e23, "[0,1,0,1]"); QPERM(P.w, e23, "[2,3,2,3]"); }
            if constexpr (z == 63) { VADD(fa, av0[0], av0[2]); VADD(fb, av0[1], av0[3]); }
            if constexpr (z == 64) VCVT(vm.x, fa, fb);
            if constexpr (z == 67) { asm volatile("" :: "v"(sm)); VFMC(tm0, sm[0], mx); VFMC(tm1, sm[1], mx); }
            SCHED();
        };
        {
            u32 tmp;
            asm volatile("s_mov_b32 m0, %2\n\t" WAIT_TXT("%0", "%1") "buffer_load_dwordx4 %4, %5, %6 offen sc1 lds\n\tbuffer_load_dwordx4 %4, %5, %6 offen offset:1024 sc1 lds\n\t"
                         "s_mov_b32 m0, %3\n\ts_nop 0\n\tbuffer_load_dwordx4 %4, %5, %7 offen sc1 lds\n\ts_waitcnt vmcnt(0) lgkmcnt(0)\n\ts_barrier"
                         : "=&s"(tmp) : "s"(uu(tm - 4u)), "s"(dm), "s"(dm2), "v"(l16), "s"(srd), "s"(ms), "s"(ms2) : "vcc", "scc", "memory");
        }
        HT(1);
#pragma unroll
        for (int ks = 0; ks < 4; ++ks) A[ks] = lds_r16(LB + C_MEM + ks * 1024);
        SCHED();
        st4s(rs, ofl, 0, sv);
        SCHED();
        static_for<0, 24>(slot);
        if (__builtin_expect(nanq != 0ull, 0)) {
            bool miss = false;
#pragma unroll
            for (int i = 0; i < 10; ++i) miss |= A[i].x == PSENT || A[i].y == PSENT || A[i].z == PSENT || A[i].w == PSENT;
            nanq = missed < SPIN_CAP ? __builtin_amdgcn_ballot_w64(miss) : 0ull;
        }
        if (__builtin_expect(nanq == 0ull, 1)) {
        static_for<24, 68>(slot);
        PV(7, 1); ones = lds_r16(ZP + 64); SCHED();
        VMAX(fa, tm0, tm1);
        if (__builtin_expect(__builtin_amdgcn_ballot_w64(fa > 24.0f) != 0ull, 0)) {
            const float up = fmaxf(max32(max16(fmaxf(tm0, tm1))), 0.f);
            tm0 -= up; tm1 -= up;
#pragma unroll
            for (int i = 0; i < 4; ++i) t[i] = __builtin_amdgcn_exp2f(__builtin_fmaf(sx[i], CEXP, mx) - up);
            e01 = pk(t[0], t[1]); e23 = pk(t[2], t[3]);
            asm volatile("s_nop 1" : "+v"(e01), "+v"(e23));
            QPERM(P.x, e01, "[0,1,0,1]"); QPERM(P.z, e01, "[2,3,2,3]"); QPERM(P.y, e23, "[0,1,0,1]"); QPERM(P.w, e23, "[2,3,2,3]");
            NOPS(1);
            MF0(oa, vx[0], P); MF0(ob, vx[1], P);
            NOPS(7);
        }
        SCHED();
        PV(1, 1); VEXP(tm0, tm0); SEL(oa[0], oa[0], ob[0]); SCHED();
        PV(8, 1); VEXP(tm1, tm1); SEL(oa[1], oa[1], ob[1]); SCHED();
        PV(9, 1); VCVT(pmm, tm0, tm1); SEL(oa[2], oa[2], ob[2]); SCHED();
        PV(2, 1);
        asm volatile("v_cndmask_b32 %0, %2, 0, %3\n\tv_cndmask_b32 %1, 0, %2, %3" : "=&v"(pm.x), "=&v"(pm.y) : "v"(pmm), "s"(himask)); SCHED();
        PV(3, 1); SEL(oa[3], oa[3], ob[3]); SCHED();
        MF0(rsum, ones, P); NOPS(4);
        VADD(fc, av1[0], av1[2]); VADD(fd, av1[1], av1[3]);
        { const v2u ones2 = ones.lo; MF1H(rsum, ones2, pm); }
        VCVT(vm.y, fc, fd); NOPS(1);
        MF1H(oa, vm, pm);
        {
            float inv; v4u oc;
            asm volatile("s_nop 3\n\tv_rcp_f32 %0, %1\n\ts_nop 2" : "=v"(inv) : "v"(rsum[0]), "v"(rsum));
            PIN(oa);
            const f32x2 i2 = {inv, inv};
            const f32x2 u0 = oa.lo * i2, u1 = oa.hi * i2;
            asm volatile("v_cvt_pk_bf16_f32 %0, %4, %5\n\tv_cvt_pk_bf16_f32 %2, %4, %5\n\tv_cvt_pk_bf16_f32 %1, %6, %7\n\tv_cvt_pk_bf16_f32 %3, %6, %7\n\t"
                         "v_permlane16_swap_b32 %0, %2\n\ts_nop 0\n\tv_permlane16_swap_b32 %1, %3"
                         : "=&v"(oc.x), "=&v"(oc.y), "=&v"(oc.z), "=&v"(oc.w) : "v"(u0.x), "v"(u0.y), "v"(u1.x), "v"(u1.y));
            st16(rs, oso, oc);
        }
        SCHED();
        unsigned long long clk;
        asm volatile("s_memrealtime %0" : "=s"(clk));
        HT(6);
        v4u tq[4], tk[8], tv[2];
        f32x4 c0, c1, cm;
        u32 oo, kvo; bool kvok;
        u32 ln = l16; PIN(ln);
        lptr QX = lds + C_QX + (w & 1) * 4096 + ln, VO = lds + C_V + (w ^ 1) * 2048 + ln, VMX = lds + C_VM + w * 512 + (ln >> 1);
#pragma unroll
        for (int i = 0; i < 2; ++i) { tq[i] = lds_r16(QX + i * 1024); tk[2 * i] = lds_r16(LB + C_K + i * 1024); tk[2 * i + 1] = lds_r16(LB + C_K + 4096 + i * 1024); }
        *(LAS v2u*)VMX = vm;
        SCHED();
        SFOR(i, 12)
            if constexpr (i == 0) MF0(cm, kmf[0], tq[0]); else if constexpr (i == 1) MF0(c0, tk[0], tq[0]); else if constexpr (i == 2) MF0(c1, tk[1], tq[0]);
            else if constexpr (i % 3 == 0) MF1(cm, kmf[i / 3], tq[i / 3]); else if constexpr (i % 3 == 1) MF1(c0, tk[2 * (i / 3)], tq[i / 3]); else MF1(c1, tk[2 * (i / 3) + 1], tq[i / 3]);
            if constexpr (i == 0) { tq[2] = lds_r16(QX + 2048); tk[4] = lds_r16(LB + C_K + 2048); }
            if constexpr (i == 1) { tk[5] = lds_r16(LB + C_K + 6144); tq[3] = lds_r16(QX + 3072); }
            if constexpr (i == 2) { tk[6] = lds_r16(LB + C_K + 3072); tk[7] = lds_r16(LB + C_K + 7168); }
            if constexpr (i == 3) { tv[0] = lds_r16(VO); tv[1] = lds_r16(VO + 1024); }
            if constexpr (i == 4) { oo = ln + (ln & 0x300u) + ((vsrc ^ 8192u) + (u32)((w & 1) * 256)); PIN(oo); }
            if constexpr (i == 5) { ksrc += NH * 16384; vsrc += NH * 16384; PINS(ksrc); PINS(vsrc); }
            if constexpr (i == 9) { asm volatile("s_waitcnt vmcnt(1)" : "+v"(fl) :: "memory"); kvok = ((u32)__builtin_amdgcn_ballot_w64(fl < (u32)s + 2) & 7u) == 0u; }
            if constexpr (i == 10) { kvo = l16 | (kvok ? 0u : OOB); PIN(kvo); }
            SCHED();
        SEND
        asm volatile("s_waitcnt lgkmcnt(0)\n\ts_barrier" : "+s"(clk) :: "memory");
        const v2u tvm = *(LAS v2u*)(VMX + (512 - (w & 1) * 1024));
        const v4u pv = lds_r16(ZP + 80);
        DMA32(dk, kvo, ksrc);
        SCHED(); asm volatile("" :: "v"(cm));
        float u[10], m, m2;
        {
            float NEG = -1000000000.0f / SCALE; PIN(NEG);
#define MSK(d, a, i) asm volatile("v_cndmask_b32 %0, %1, %2, %3" : "=v"(d) : "v"(NEG), "v"(a), "s"(vis[i]))
#pragma unroll
            for (int i = 0; i < 4; ++i) MSK(u[i], c0[i], i);
#pragma unroll
            for (int i = 0; i < 4; ++i) MSK(u[4 + i], c1[i], 4 + i);
            asm volatile("v_max3_f32 %0, %2, %3, %10\n\tv_max3_f32 %1, %4, %5, %11\n\tv_max3_f32 %0, %0, %6, %7\n\tv_max3_f32 %1, %1, %8, %9\n\t"
                         "v_max_f32 %0, %0, %1\n\tv_mov_b32 %1, %0\n\ts_nop 1\n\tv_permlane16_swap_b32 %0, %1\n\tv_max_f32 %0, %0, %1\n\tv_mov_b32 %1, %0\n\ts_nop 1\n\t"
                         "v_permlane32_swap_b32 %0, %1\n\tv_max_f32 %0, %0, %1\n\tv_mul_f32 %0, 0xbe0293ee, %0"
                         : "=&v"(m), "=&v"(m2) : "v"(u[0]), "v"(u[1]), "v"(u[2]), "v"(u[3]), "v"(u[4]), "v"(u[5]), "v"(u[6]), "v"(u[7]), "v"(cm[0]), "v"(cm[1]));
        }
        DMA32(dv, kvo, vsrc);
        {
            v4u px; v2u p0, p1;
#define EX2(d, a, b) asm volatile("v_fmamk_f32 %0, %2, 0x3e0293ee, %4\n\tv_fmamk_f32 %1, %3, 0x3e0293ee, %4\n\tv_exp_f32 %0, %0\n\tv_exp_f32 %1, %1" : "=&v"(d.x), "=&v"(d.y) : "v"(a), "v"(b), "v"(m))
            f32x2 ex[5];
            EX2(ex[0], u[0], u[1]); EX2(ex[1], u[2], u[3]); EX2(ex[2], u[4], u[5]); EX2(ex[3], u[6], u[7]); EX2(ex[4], cm[0], cm[1]);
            asm volatile("v_cvt_pk_bf16_f32 %0, %8, %9\n\tv_cvt_pk_bf16_f32 %1, %10, %11\n\tv_cvt_pk_bf16_f32 %2, %12, %13\n\tv_cvt_pk_bf16_f32 %3, %14, %15\n\t"
                         "v_mov_b32 %5, 0\n\tv_mov_b32 %6, 0\n\tv_cvt_pk_bf16_f32 %4, %16, %17\n\tv_cvt_pk_bf16_f32 %7, %16, %17"
                         : "=&v"(px.x), "=&v"(px.y), "=&v"(px.z), "=&v"(px.w), "=&v"(p0.x), "=&v"(p0.y), "=&v"(p1.x), "=&v"(p1.y)
                         : "v"(ex[0].x), "v"(ex[0].y), "v"(ex[1].x), "v"(ex[1].y), "v"(ex[2].x), "v"(ex[2].y), "v"(ex[3].x), "v"(ex[3].y), "v"(ex[4].x), "v"(ex[4].y));
            f32x4 o[4], sum;
            const v2u ones2 = ones.lo;
            const u32 now = (u32)clk;
            u32 oz;
            SCHED();
            MF0(sum, ones, px); { const u32 q = ksrc ^ 8192u; DMA32(dq, kvo, q); } SCHED();
            MF0(o[0], vx[0], px);
            MF0(o[1], vx[1], px);
            MF0(o[2], tv[0], px); oz = (oso & ~(3u << 17)) | (u32)((s + 3) & 3) << 17; PIN(oz); SCHED();
            MF0(o[3], tv[1], px); oso = (oso & ~(3u << 17)) | (u32)((s + 1) & 3) << 17; PIN(oso); SCHED();
            MF1H(sum, ones2, p0);
            MF1H(o[0], vm, p0);
            MF1H(o[1], vm, p1); st16(rs, oz, pv); SCHED();
            MF1H(o[2], tvm, p0);
            MF1H(o[3], tvm, p1);
            float inv;
            asm volatile("s_nop 2\n\tv_rcp_f32 %0, %1\n\ts_nop 0" : "=v"(inv) : "v"(sum[0]), "v"(sum));
            asm volatile("" : "+v"(o[0]), "+v"(o[1]));
            st16(rs, oo, (v4u){pk2(o[0].lo * inv), pk2(o[0].hi * inv), pk2(o[1].lo * inv), pk2(o[1].hi * inv)});
            asm volatile("" : "+v"(o[2]), "+v"(o[3]));
            st16(rs, oo ^ 2048u, (v4u){pk2(o[2].lo * inv), pk2(o[2].hi * inv), pk2(o[3].lo * inv), pk2(o[3].hi * inv)});
            SCHED();
            LS2(L == 0 && w == 0, h);
            st4s(rs, (h == 0 && tid == 192) ? hint + 128u : OOB, 0, (u32)s << 16 | min(missed, 1u) << 14 | min(now - tm, 0x3FFFu));
            tm = rdl(fl, 5);
            missed = missed == SPIN_CAP ? missed : 0u;
            if (__builtin_expect((int)(rdl(fl, 3) - now) < CH_VIS - 4 || s + L == 0, 0)) {
#pragma unroll 1
                for (int n = 0; n < (s + L == 0 ? 200 : 8); ++n) {
                    CFENCE();
                    const u32 hv = ld4(rs, fkh);
                    tm = rdl(hv, s + L == 0 ? 6 : 5);
                    if (s + L == 0 ? tm != PSENT : (int)(rdl(hv, 3) - now) >= CH_VIS - 4) break;
                }
            }
        }
        if (__builtin_expect(!kvok, 0) && s + 1 < NSEG) {
            st4s(rs, ln == 0 ? hint + 4096u + (u32)h * 128u : OOB, 0, (u32)s + 1u);
            keys_wait((u32)s + 2u);
        }
        ++s;
        } else {
            u32 ln = l16; PIN(ln);
            const u32 hv = ld4(rs, ln == 112u ? foff(F_ERR) : fkh);
            tm = rdl(hv, 5);
            *(LAS u32*)(lds + C_DUMP + (w == 0 ? 0 : 64)) = rdl(hv, 7);
            BAR();
            missed = uu(*(LAS u32*)(lds + C_DUMP)) != 0u ? SPIN_CAP : missed + 1;
            st4s(rs, missed == SPIN_CAP ? foff(F_ERR) : OOB, 0, 1u);
            BAR();
        }
    }
    DRAIN(); BAR();
    st4s(rs, ofl, 0, NSEG);
}

#define WO_GAP 6
DEV void role_wo(const Params& p, lptr lds, int j, v2u ep) {
    const int tid = threadIdx.x, lane = tid & 63, w = uni(tid >> 6), g = lane >> 4, r = lane & 15;
    const int rg = j & 1, cg = j >> 1, rot = cg & 7;
    const rsrc_t rs = mk_rsrc(p.ws, W_END);
    v8u wo[8][5];
    {
        const v4u so_ = mk_srd(p.Wo, DM * NQ * 2);
        u32 vo[5];
#pragma unroll
        for (int i = 0; i < 5; ++i) { const int n = 64 * i + lane; vo[i] = (u32)((n / 5) * DM + cg * 40 + (n % 5) * 8) * 2u; }
        auto fill = [&](int b) {
            const u32 dst = uu(w * 40960 + (b & 3) * 10240), src = uu((8 * w + ((b + rot) & 7)) * (DH * DM * 2));
#pragma unroll
            for (int i = 0; i < 10; ++i) DMAIN(so_, dst + i * 1024, vo[i % 5], src + (i / 5) * (64 * DM * 2));
        };
        __builtin_amdgcn_s_sleep(ST_WO);
#pragma unroll 1
        for (int b = 0; b < 4; ++b) fill(b);
        const int q4 = r >> 2;
        lptr tb = lds + w * 40960 + (32 * (g >> 1) + 16 * (q4 >> 1) + 8 * ((r >> 1) & 1) + 4 * (g & 1) + (q4 & 1)) * 80 + (r & 1) * 8;
        SFOR(b, 8)
            VMW(b < 5 ? 30 : 70 - 10 * b);
#pragma unroll
            for (int t = 0; t < 5; ++t) {
                lptr q = tb + (b & 3) * 10240 + t * 16;
                wo[b][t] = tr_frag(q, q + 160, q + 64 * 80, q + 64 * 80 + 160);
                if (b * 5 + t < 32) asm volatile("" : "+a"(wo[b][t])); else asm volatile("" : "+v"(wo[b][t]));
            }
            CFENCE();
            if constexpr (b < 4) fill(b + 4);
        SEND
    }
    if (j == 0) LST(265, rt32());
    pre_wait(rs, 0, NCV, 1u, ep);
    const u32 idx = (r & 2) ? 0xEEEEu : 0x4444u;
    bool dead = false;
    const u32 pol0 = uu(get_pol(rs));
    const u32 l16 = (u32)lane * 16u;
    u32 oo[8];
#pragma unroll
    for (int i = 0; i < 8; ++i) oo[i] = uu(W_ORING + ring_map((u32)(rg * 32768 + (8 * w + ((i + rot) & 7)) * 1024), pol0));
    const u32 hint = W_MRING + 4 * 32768u + pol0 * 8192u;
    u32 hvo[2];
#pragma unroll
    for (int l = 0; l < 2; ++l) hvo[l] = lane < 2 ? hint + (u32)(l ^ 1) * (MB / 4) + (u32)lane * 4u : lane == 2 ? hint + (u32)l * (MB / 4) + 128u : lane == 3 ? foff(F_ERR) :
                 (lane >= 32 && j == 0 && w == 3) ? hint + (u32)l * (MB / 4) + (u32)lane * 128u : OOB;
    lptr xw = r < 8 ? lds + w * 640 + (2 * (g & 1) + (g >> 1)) * 32 + r * 4 : lds + 8192 + lane * 4;
    const int G = ((5 * cg) >> 1) + w, tt = 2 * G + ((lane >> 3) & 1) - 5 * cg;
    const bool pub = w < 3 && lane < 32 && tt >= 0 && tt < 5;
    lptr xr = lds + (pub ? tt * 128 + (2 * (lane >> 4) + ((lane >> 2) & 1)) * 32 + (lane & 3) * 8 : 0);
    const u32 mpo = pub ? W_MRING + ring_map((u32)(G * 256 + rg * 128 + lane * 4), pol0) : OOB;
    constexpr u32 PER0 = CH_W0 + CH_MW + CH_H0 + CH_MH + 2 * CH_VIS, DW0 = CH_W0 + CH_VIS + CH_MW + CH_REC;
    const bool lead = j == 0 && w == 3;
    const u32 hso = (lead && lane == 0) ? hint : OOB;
    MK_SRD();
    u32 t = 0, tn = 0;
    if (j == 0) {
#pragma unroll 1
        for (int n = 0; n < SPIN_CAP && !dead; ++n) {
            CFENCE();
            if (all_(ld4(rs, W_ORING + pol0 * 8192u + (u32)w * 16384u + (u32)lane * 128u) != PSENT)) break;
            __builtin_amdgcn_s_sleep(1);
        }
        BAR();
    }
    if (lead) {
        t = rt32() + 40u; tn = t + PER0 / 2 + CH_REC;
        st16(rs, lane < 2 ? hint + (u32)lane * (MB / 4) : OOB, (v4u){lane == 0 ? t : tn, PER0, (lane == 0 ? t : tn) + DW0, t + DW0});
    } else {
#pragma unroll 1
        for (int n = 0; n < SPIN_CAP && !dead; ++n) {
            CFENCE();
            const u32 hv = ld4(rs, hvo[1]);
            t = rdl(hv, 0);
            if (rdl(hv, 1) < 400u) break;
            __builtin_amdgcn_s_sleep(1);
        }
    }
    int sn[2] = {0, 0};
    u32 hav = 4 * CH_H0, wav = 4 * CH_W0, mh = 8 * CH_MH, mw = 8 * CH_MW, ok[2] = {0u, 0u}, dw[2] = {DW0, DW0};
    const u32 tm0 = t + DW0;
    u32 zero = 0, hv = 0xFFFFu, pst = 1, pdur = 0, pmp = OOB;
    const u32 psent = PSENT;
    lptr sw = lds + 2560 + w * 4;
    bool stop = false;
    asm volatile("" : "+v"(zero), "+v"(sw));
    auto turn = [&](auto lc) __attribute__((always_inline)) {
        constexpr int L = decltype(lc)::value, M = L ^ 1;
        u32 now, done, rep, rd, lost, dh, dn, nt, za; v4u hw;
        const int sc = sn[L];
        auto books = [&](auto ic) __attribute__((always_inline)) {
            constexpr int i = decltype(ic)::value;
            if constexpr (i == 0) { rep = rdl(hv, 2); PINS(rep); }
            if constexpr (i == 1) { lost = (u32)(__builtin_amdgcn_ballot_w64(hv == (u32)sn[M]) >> 32); PINS(lost); }
            if constexpr (i == 2) { lost |= (rep >> 16) ^ (u32)sn[M]; PINS(lost); }
            if constexpr (i == 3) { lost |= ok[M] ^ 2u; rd = rep & 0xFFFFu; PINS(lost); PINS(rd); }
            if constexpr (i == 4) { lost = lost == 0u ? CH_UP * 8u : 0u; PINS(lost); }
            if constexpr (i == 5) { dn = wav >> 2; PINS(dn); pdur = min(pdur, dn + 8u); PINS(pdur); }
            if constexpr (i == 6) { pdur = (done & ok[M]) ? pdur : dn; PINS(pdur); }
            if constexpr (i == 7) { wav += pdur - dn; PINS(wav); }
            if constexpr (i == 8) { dh = hav >> 2; PINS(dh); nt = min(rd, dh + 8u); PINS(nt); }
            if constexpr (i == 9) { nt = rd < 0x3FFFu ? nt : dh; PINS(nt); }
            if constexpr (i == 10) { hav += nt - dh; PINS(hav); }
            if constexpr (i == 11) { mw = max(mw - 1u, CH_MW * 8u); PINS(mw); }
            if constexpr (i == 12) { mh = max(mh - 1u, CH_MH * 8u); PINS(mh); }
            if constexpr (i == 13) { rd = rd >> 14 == 1u ? lost : 0u; PINS(rd); }
            if constexpr (i == 14) { mw += rd; mh += lost - rd; PINS(mw); PINS(mh); }
            if constexpr (i == 15) { ok[M] = done ? 1u : (ok[M] & 1u) << 1; PINS(ok[M]); }
            if constexpr (i == 16) { dh = (2u * hav + mh) / 8u + CH_VIS; PINS(dh); }
            if constexpr (i == 17) { dn = (2u * wav + mw) / 8u + CH_VIS; PINS(dn); }
            if constexpr (i == 18) { nt = dw[L] + dh; PINS(nt); }
            if constexpr (i == 19) { dh += dn; dw[L] = dn; sn[M] += (int)done; PINS(dh); PINS(sn[M]); }
            if constexpr (i == 20) { rd = tn - t + (dh >> 1); PINS(rd); }
            if constexpr (i == 21) { rd = sn[M] + 1 < NSEG ? rd : 0u; PINS(rd); }
            if constexpr (i == 22) { nt = t + (u32)max((int)nt, (int)rd); PINS(nt); }
            if constexpr (i == 23) { hw.x = nt; hw.y = dh; PIN(hw.x); PIN(hw.y); }
            if constexpr (i == 24) { hw.z = nt + dn; hw.w = tm0; PIN(hw); }
            if constexpr (i == 25) st16(rs, hso + L * (MB / 4), hw);
        };
        if (__builtin_expect(sc + 1 < NSEG, 1)) {
            u32 vo = l16 + (L * (MB / 2) + (u32)(sc & 3) * 131072u), mp, hn;
            PIN(vo);
#define WT(k) do {} while (0)
            WT(0);
            v4u d[8];
            {
                u32 tmp;
                asm volatile(WAIT_TXT("%1", "%2") "buffer_load_dwordx4 %0, %3, %4, %5 offen sc1\n\ts_waitcnt lgkmcnt(0)"
                             : "=&v"(d[0]), "=&s"(tmp) : "s"(uu(t - 4u)), "v"(vo), "s"(srd), "s"(oo[0]) : "vcc", "scc", "memory");
            }
            SFOR(i, 7)
                asm volatile("buffer_load_dwordx4 %0, %1, %2, %3 offen sc1" : "=v"(d[i + 1]) : "v"(vo), "s"(srd), "s"(oo[i + 1]) : "memory");
                SCHED();
                if constexpr (i == 0) { done = uu(pst) ^ 1u; PINS(done); }
                static_for<3 * i, 3 * i + 3>(books);
                SCHED();
            SEND
            hn = ld4(rs, hvo[L]);
            SCHED();
            WT(1);
            static_for<21, 26>(books);
            za = done ? pmp : OOB;
            st4s(rs, za, 0, psent);
            *(LAS u32*)(sw + L * 4096) = zero;
            f32x4 acc[5] = {};
            mp = mpo + (L * (MB / 4) + (u32)((sc + 1) & 3) * 32768u); pmp = mpo + (L * (MB / 4) + (u32)(sc & 3) * 32768u);
            asm volatile("" : "+v"(acc[0]), "+v"(acc[1]), "+v"(acc[2]), "+v"(acc[3]), "+v"(acc[4]), "+v"(mp), "+v"(pmp));
            SCHED();
            WT(2);
            float f0[5], f1[5]; unsigned long long bad;
#define WSW(o_) asm volatile("v_permlane32_swap_b32 %0, %1" : "+v"(f0[o_]), "+v"(f1[o_]))
#define WAD(o_) asm volatile("v_add_f32_dpp %0, %1, %0 row_ror:8 row_mask:0xf bank_mask:0xf" : "+v"(f0[o_]) : "v"(f1[o_]))
#define WFO(o_) do { VADD(f0[o_], acc[o_][0], acc[o_][2]); VADD(f1[o_], acc[o_][1], acc[o_][3]); } while (0)
#define WWR(o_) *(LAS float*)(xw + L * 4096 + (o_) * 128) = f0[o_]
            asm volatile("s_waitcnt vmcnt(10)" : "+v"(d[0]));
            if (__builtin_expect(__builtin_amdgcn_ballot_w64(d[0].x == PSENT) != 0ull, 0)) {
                asm volatile("s_waitcnt vmcnt(3)" : "+v"(d[1]), "+v"(d[2]), "+v"(d[3]), "+v"(d[4]), "+v"(d[5]), "+v"(d[6]), "+v"(d[7]));
#pragma unroll
                for (int i = 0; i < 5; ++i) *(LAS u32*)(xw + L * 4096 + i * 128) = psent;
                *(LAS u32*)(sw + L * 4096) = 1u;
            } else {
            SFOR(i, 40)
                constexpr int pc = i < 30 ? i / 5 : 6 + (i & 1), oc = i < 30 ? i % 5 : (i - 30) >> 1;
                if constexpr (oc == 0 && pc > 0) asm volatile("s_waitcnt vmcnt(%1)" : "+v"(d[pc]) : "i"(10 - pc));
                if constexpr (pc * 5 + oc < 32) SMF_A(acc[oc], d[pc], wo[pc][oc]); else SMF_V(acc[oc], d[pc], wo[pc][oc]);
                if constexpr (i == 0) WT(3);
                if constexpr (i == 20) WT(4);
                if constexpr (i == 31 || i == 33) NOPS(1);
                if constexpr (i == 32) NOPS(0);
                if constexpr (i == 34) WFO(0);
                if constexpr (i == 35) { asm volatile("v_cmp_u_f32 %0, %1, %2" : "=s"(bad) : "v"(f0[0]), "v"(f1[0])); WSW(0); }
                if constexpr (i == 36) WFO(1);
                if constexpr (i == 37) { WAD(0); WSW(1); }
                if constexpr (i == 38) { WWR(0); SCHED(); WFO(2); }
                if constexpr (i == 39) { WAD(1); WSW(2); }
                SCHED();
            SEND
            WT(5);
            WWR(1); SCHED(); WFO(3); WAD(2); WWR(2); SCHED(); WSW(3);
            if (__builtin_expect(bad != 0ull, 0)) {
                bool miss = false;
#pragma unroll
                for (int i = 0; i < 8; ++i) miss |= d[i].x == PSENT;
                if (all_(!miss)) {
#pragma unroll
                    for (int i = 0; i < 8; ++i) miss |= d[i].y == PSENT || d[i].z == PSENT || d[i].w == PSENT;
                }
                *(LAS u32*)(sw + L * 4096) = all_(!miss) ? 0u : 1u;
            }
            WFO(4); WAD(3); WWR(3); SCHED();
            asm volatile("v_permlane32_swap_b32 %0, %1\n\ts_nop 1\n\tv_add_f32_dpp %0, %1, %0 row_ror:8 row_mask:0xf bank_mask:0xf" : "+v"(f0[4]), "+v"(f1[4]));
            WWR(4);
            }
            WT(6);
            f32x2 a, b, c, e; u32 cw, mq; v4u sv;
            asm volatile("s_waitcnt lgkmcnt(0)\n\ts_barrier\n\tds_read_b64 %0, %5 offset:%7\n\tds_read_b64 %1, %5 offset:%7+640\n\tds_read_b64 %2, %5 offset:%7+1280\n\tds_read_b64 %3, %5 offset:%7+1920\n\t"
                         "ds_read_b128 %4, %6 offset:%7+2560\n\t"
                         "s_waitcnt lgkmcnt(3)\n\tv_pk_add_f32 %0, %0, %1\n\ts_waitcnt lgkmcnt(1)\n\tv_pk_add_f32 %2, %2, %3\n\ts_waitcnt lgkmcnt(0)\n\tv_pk_add_f32 %0, %0, %2"
                         : "=&v"(a), "=&v"(b), "=&v"(c), "=&v"(e), "=&v"(sv) : "v"(xr), "v"(zero), "i"(L * 4096) : "memory");
            asm volatile("v_cmp_u_f32 vcc, %2, %3\n\tv_cvt_pk_bf16_f32 %0, %2, %3\n\tv_cndmask_b32 %1, %4, %5, vcc" : "=&v"(cw), "=&v"(mq) : "v"(a.x), "v"(a.y), "v"(mp), "v"(OOB) : "vcc");
            st4s(rs, mq, 0, cw);
            SCHED();
            now = rt32();
            PIN(hn); hv = hn;
            WT(7);
            pst = sv.x | sv.y | sv.z | sv.w;
            if (__builtin_expect(__builtin_amdgcn_ballot_w64(mq != mp && pst == 0u) != 0ull, 0)) st4s(rs, mp, 0, cw);
        } else {
            done = uu(pst) ^ 1u;
            st4s(rs, done ? pmp : OOB, 0, psent);
            static_for<0, 25>(books);
            pst = 1;
            hv = ld4(rs, hvo[L]);
            PIN(hv);
            now = rt32();
        }
        pdur = now - t;
        const u32 per = rdl(hv, 1), th = rdl(hv, 0);
        t = lead ? tn : (int)(th - now) < -(int)(per >> 1) ? th + per : th;
        tn = nt;
        dw[M] += max((int)(now + CH_WX - t), 0);
        stop = stop || rdl(hv, 3) != 0u;
    };
    for (int k = 0; k < 2 * SPIN_CAP && !stop && (sn[0] + 1 < NSEG || sn[1] + 1 < NSEG); ++k) { turn(IC<0>{}); turn(IC<1>{}); }
    if (sn[0] + 1 < NSEG || sn[1] + 1 < NSEG) st4s(rs, tid == 0 ? foff(F_ERR) : OOB, 0, 1u);
}

__global__ void __launch_bounds__(256, 1) k_main(Params p) {
    lptr lds = (lptr)(u32)16 - 16;
    if (blockIdx.x == 0) LST(256, rt32());
    unsigned long long pc; int len;
    asm volatile("s_getpc_b64 %0\n.Lk_main_here:\n\ts_mov_b32 %1, .Lk_end_here-.Lk_main_here" : "=s"(pc), "=s"(len));
    const rsrc_t rs = mk_rsrc(p.ws, W_END);
    const v2u e0 = ld8s(rs, threadIdx.x == 0 ? foff(F_EPOCH) : OOB, 0);
    const v4u code = __builtin_amdgcn_raw_buffer_load_b128(mk_rsrc((const void*)(pc & ~15ull), (u32)max(len, 0)), (((blockIdx.x >> 3) & 31u) * 256u + threadIdx.x) * 16u, 0, 0);
    CFENCE();
    if (threadIdx.x == 0) {
        GAS unsigned long long* slots = (GAS unsigned long long*)(p.ws + W_CTRL + F_SLOT * 128);
        const unsigned long long tag = (unsigned long long)e0.y << 32 | (e0.x + 1u);
        u32 role = ((u32)__builtin_amdgcn_s_getreg(20 | (3 << 11)) & 7u) * 32u + (blockIdx.x >> 3);
        for (int n = 0; n < 256 && __hip_atomic_exchange(slots + role * 16, tag, __ATOMIC_RELAXED, __HIP_MEMORY_SCOPE_AGENT) == tag; ++n) role = (role + 1u) & 255u;
        lds_w16(lds, (v4u){role, e0.x, e0.y, 0u});
    }
    __syncthreads();
    const v4u rv = lds_r16(lds);
    const int role = uni((int)rv.x), x = role >> 5, i = role & 31, w = uni(threadIdx.x >> 6);
    const v2u ep = {uu(rv.y), uu(rv.z)};
    asm volatile("" :: "v"(code));
    __syncthreads();
    if (x < 2) { role_head(p, lds, i >> 4, x * 16 + (i & 15), ep); __builtin_amdgcn_endpgm(); }
    if (x == 2) { role_wo(p, lds, i, ep); __builtin_amdgcn_endpgm(); }
    const int no = x < 5 ? 5 : 10, nr = x < 7 ? 5 : 4;
    if (i >= no + nr) { role_qkv(p, lds, (x < 5 ? (x - 3) * 22 : 44 + (x - 5) * 17) + i - no - nr, ep); __builtin_amdgcn_endpgm(); }
    const int og = x < 5 ? x - 3 : 2 * (x - 4) + (i >= 5), ng = i % 5, kq = og >> 1, L = og & 1, r = (x - 3) * 5 + i - no, q = r / 3;
    const int j = i < no ? kq * 5 + ng : r % 3 == 0 ? 20 + (q & 1) * 20 + (q >> 1) : 16 + (r % 3) * 8 + q;
    const bool cv = i >= no && r % 3 == 0;
    if (i >= no || L == 1) conv_stage(p, rs, lds, j, q, cv ? 40 : -8, ep);
    if (cv) __builtin_amdgcn_endpgm();
    if (i >= no) { role_row(p, lds, (j - 24) * 4 + w, ep); __builtin_amdgcn_endpgm(); }
    role_out(p, lds, L, kq, ng, ep);
}
__global__ void k_end() { asm volatile(".Lk_end_here:"); }

extern "C" void kernel_launch(void* const* d_in, const int* in_sizes, int n_in, void* d_out, int out_size, void* d_ws, size_t ws_size,
                              hipStream_t stream) {
    Params p;
    p.x = (const u16*)d_in[0]; p.mem0 = (const u16*)d_in[1]; p.Wq = (const u16*)d_in[2]; p.Wk = (const u16*)d_in[3];
    p.Wv = (const u16*)d_in[4]; p.Wo = (const u16*)d_in[5];
    p.bk = (const u16*)d_in[8]; p.bv = (const u16*)d_in[9]; p.shift = (const u16*)d_in[10]; p.ssmk = (const u16*)d_in[11]; p.ssmD = (const u16*)d_in[12];
    p.out = (u16*)d_out;
    p.ws = (unsigned char*)(((size_t)d_ws + 2 * MB - 1) & ~(size_t)(2 * MB - 1));
    (void)hipFuncSetAttribute((const void*)k_main, hipFuncAttributeMaxDynamicSharedMemorySize, LDS_BYTES);
    hipLaunchKernelGGL(k_main, dim3(256), dim3(256), LDS_BYTES, stream, p);
}
```

```cpp
#include <hip/hip_runtime.h>
#include <hip/hip_bf16.h>

typedef unsigned short u16;
typedef unsigned int u32;
typedef u32 v2u __attribute__((ext_vector_type(2)));
typedef u32 v4u __attribute__((ext_vector_type(4)));
typedef u32 v8u __attribute__((ext_vector_type(8)));
typedef float f32x2 __attribute__((ext_vector_type(2)));
typedef float f32x4 __attribute__((ext_vector_type(4)));
typedef float f32x16 __attribute__((ext_vector_type(16)));
typedef __bf16 bf16x2 __attribute__((ext_vector_type(2)));
typedef __bf16 bf16x8 __attribute__((ext_vector_type(8)));
typedef short s16x4 __attribute__((ext_vector_type(4)));
#define LAS __attribute__((address_space(3)))
#define GAS __attribute__((address_space(1)))
typedef LAS unsigned char* lptr;
#define DEV __device__ __forceinline__

constexpr int SEQ = 2048, DM = 640, NH = 32, DH = 128, NSEG = 64, NQ = NH * DH;

constexpr u32 MB = 1u << 20;
constexpr u32 W_ORING = 0;
constexpr u32 W_MRING = MB;
constexpr u32 W_PROBE = 2 * MB - 65536;
constexpr u32 W_CTRL = 4 * MB;
constexpr u32 CTRL_BYTES = 128 * 1024;
constexpr u32 ACT = SEQ * DM * 2;
constexpr u32 W_H1 = 5 * MB, W_Y = W_H1 + ACT, W_H2 = W_Y + ACT, W_X2 = W_H2 + ACT;
constexpr u32 W_KQ = 16 * MB;
constexpr u32 W_VO = W_KQ + 64 * MB;
constexpr u32 W_P = W_VO + 64 * MB;
constexpr u32 P_ITEM = 32 * 4 * DM * 4;
constexpr u32 W_KVT = W_P + 2 * NSEG * P_ITEM;
constexpr u32 W_SP = W_KVT + ACT;
constexpr u32 W_END = W_KVT + 3 * MB;
static_assert((size_t)W_END + 2 * MB <= 256ull * MB, "workspace");
constexpr u32 F_ERR = 0, F_QKV = 16, F_O = F_QKV + 192, F_P = F_O + 64, F_H2 = F_P + 64, F_CLR = F_H2 + 32,
              F_EPOCH = F_CLR + 32, F_SLOT = F_EPOCH + 1, F_PRE = F_SLOT + 256, F_N = F_PRE + 192;
constexpr int NCV = 44, NPRE = 40;
static_assert(F_N * 128 <= CTRL_BYTES, "ctrl");
DEV u32 foff(u32 idx) { return W_CTRL + idx * 128u; }

constexpr u32 OOB = 0x80000000u;
constexpr u32 PSENT = 0x7F817F81u;
constexpr int SPIN_CAP = 40000;
constexpr int LDS_BYTES = 160 * 1024;

#define LS3(cond, slot, idx) do {} while (0)
#define LST(idx, v) do {} while (0)
#define LS2(cond, idx) do {} while (0)
struct Params {
    const u16 *x, *mem0, *Wq, *Wk, *Wv, *Wo, *bk, *bv, *shift, *ssmk, *ssmD;
    u16* out;
    unsigned char* ws;
};

typedef __amdgpu_buffer_rsrc_t rsrc_t;
DEV rsrc_t mk_rsrc(const void* p, u32 bytes) { return __builtin_amdgcn_make_buffer_rsrc((void*)p, 0, bytes, 0x00020000); }
DEV v4u ld16s(rsrc_t r, u32 off, u32 so) { return __builtin_amdgcn_raw_buffer_load_b128(r, off, so, 16); }
DEV v2u ld8s(rsrc_t r, u32 off, u32 so) { return __builtin_amdgcn_raw_buffer_load_b64(r, off, so, 16); }
DEV v4u ldin16(rsrc_t r, u32 off, u32 so) { return __builtin_amdgcn_raw_buffer_load_b128(r, off, so, 2); }
DEV v2u ldin8(rsrc_t r, u32 off, u32 so) { return __builtin_amdgcn_raw_buffer_load_b64(r, off, so, 2); }
DEV u32 ld4(rsrc_t r, u32 off) { return __builtin_amdgcn_raw_buffer_load_b32(r, off, 0, 16); }
DEV void st16(rsrc_t r, u32 off, v4u v) { __builtin_amdgcn_raw_buffer_store_b128(v, r, off, 0, 16); }
DEV void st8s(rsrc_t r, u32 off, u32 so, v2u v) { __builtin_amdgcn_raw_buffer_store_b64(v, r, off, so, 16); }
DEV void st4s(rsrc_t r, u32 off, u32 so, u32 v) { __builtin_amdgcn_raw_buffer_store_b32(v, r, off, so, 16); }
#define DRAIN() asm volatile("s_waitcnt vmcnt(0)" ::: "memory")
#define VMW(n) asm volatile("s_waitcnt vmcnt(%0)" :: "i"(n) : "memory")
#define BAR() asm volatile("s_waitcnt lgkmcnt(0)\n\ts_barrier" ::: "memory")
#define SCHED() __builtin_amdgcn_sched_barrier(0)
#define CFENCE() asm volatile("" ::: "memory")
#define NOPS(n) asm volatile("s_nop %0" :: "i"(n))
#define PIN(x) asm volatile("" : "+v"(x))
#define PINS(x) asm volatile("" : "+s"(x))

template <int V> struct IC { static constexpr int value = V; };
template <int I, int N, class F> DEV void static_for(F&& f) { if constexpr (I < N) { f(IC<I>{}); static_for<I + 1, N>(f); } }
#define SFOR(var, n) static_for<0, n>([&](auto ic_) __attribute__((always_inline)) { constexpr int var = decltype(ic_)::value;
#define SEND });

DEV u32 pk(float a, float b) { bf16x2 v = {(__bf16)a, (__bf16)b}; return __builtin_bit_cast(u32, v); }
DEV float lo_f(u32 u) { return __builtin_bit_cast(float, u << 16); }
DEV float hi_f(u32 u) { return __builtin_bit_cast(float, u & 0xffff0000u); }
DEV f32x2 up2(u32 u) { return (f32x2){lo_f(u), hi_f(u)}; }
DEV u32 pk2(f32x2 v) { return pk(v.x, v.y); }
DEV float rbf(float a) { return (float)(__bf16)a; }
DEV v4u lds_r16(lptr p) { return *(LAS v4u*)p; }
DEV void lds_w16(lptr p, v4u v) { *(LAS v4u*)p = v; }
DEV v2u lds_tr(lptr p) { return __builtin_bit_cast(v2u, __builtin_amdgcn_ds_read_tr16_b64_v4i16((LAS s16x4*)p)); }
DEV v4u tr2(lptr p0, lptr p1) { const v2u a = lds_tr(p0), b = lds_tr(p1); return (v4u){a.x, a.y, b.x, b.y}; }
DEV v8u cat8(v4u a, v4u b) { return __builtin_shufflevector(a, b, 0, 1, 2, 3, 4, 5, 6, 7); }
DEV f32x4 mfma16(v4u a, v4u b, f32x4 c) { return __builtin_amdgcn_mfma_f32_16x16x32_bf16(__builtin_bit_cast(bf16x8, a), __builtin_bit_cast(bf16x8, b), c, 0, 0, 0); }
DEV f32x16 mfma32(v4u a, v4u b, f32x16 c) { return __builtin_amdgcn_mfma_f32_32x32x16_bf16(__builtin_bit_cast(bf16x8, a), __builtin_bit_cast(bf16x8, b), c, 0, 0, 0); }

DEV float f_(u32 u) { return __builtin_bit_cast(float, u); }
DEV u32 u_(float f) { return __builtin_bit_cast(u32, f); }
template <int CTRL> DEV float dpp(float v) { return f_((u32)__builtin_amdgcn_update_dpp(0, (int)u_(v), CTRL, 0xf, 0xf, false)); }
DEV float max16(float v) { auto r = __builtin_amdgcn_permlane16_swap(u_(v), u_(v), false, false); return fmaxf(f_(r[0]), f_(r[1])); }
DEV float max32(float v) { auto r = __builtin_amdgcn_permlane32_swap(u_(v), u_(v), false, false); return fmaxf(f_(r[0]), f_(r[1])); }
DEV float add16(float v) { auto r = __builtin_amdgcn_permlane16_swap(u_(v), u_(v), false, false); return f_(r[0]) + f_(r[1]); }
DEV float add32(float v) { auto r = __builtin_amdgcn_permlane32_swap(u_(v), u_(v), false, false); return f_(r[0]) + f_(r[1]); }
DEV float wave_sum(float v) {
    v += dpp<0xB1>(v);
    v += dpp<0x4E>(v);
    v += dpp<0x124>(v);
    v += dpp<0x128>(v);
    return add32(add16(v));
}
DEV bool all_(bool c) { return __builtin_amdgcn_ballot_w64(!c) == 0ull; }
DEV int uni(int v) { return __builtin_amdgcn_readfirstlane(v); }
DEV u32 uu(u32 v) { return (u32)__builtin_amdgcn_readfirstlane((int)v); }
DEV u32 rdl(u32 v, int l) { return (u32)__builtin_amdgcn_readlane((int)v, l); }
DEV u32 rt32() { return (u32)__builtin_amdgcn_s_memrealtime(); }

#define WSL 2
DEV void wait_flags(rsrc_t rs, u32 off, u32 want, bool& dead) {
    if (dead) return;
#pragma unroll 1
    for (int n = 0;; ++n) {
        CFENCE();
        const u32 v = ld4(rs, off);
        if (all_(off >= OOB || v >= want)) break;
        __builtin_amdgcn_s_sleep(WSL);
        if ((n & 63) == 63) { if (uni((int)ld4(rs, foff(F_ERR))) != 0) { dead = true; break; } }
        if (n > SPIN_CAP) { st4s(rs, foff(F_ERR), 0, 1u); dead = true; break; }
    }
    CFENCE();
}

DEV void pre_post(rsrc_t rs, int idx, u32 stage, v2u ep) { st8s(rs, threadIdx.x == 0 ? foff(F_PRE + idx) : OOB, 0, (v2u){ep.x + stage, ep.y}); }
DEV void pre_poll(rsrc_t rs, int first, int n, u32 stage, v2u ep) {
    const int lane = threadIdx.x & 63;
#pragma unroll 1
    for (int it = 0; it < SPIN_CAP; ++it) {
        CFENCE();
        const v2u a = ld8s(rs, foff(F_PRE + first + min(lane, n - 1)), 0), b = ld8s(rs, foff(F_PRE + first + min(lane + 64, n - 1)), 0);
        if (all_(a.y == ep.y && b.y == ep.y && max(a.x - ep.x - stage, b.x - ep.x - stage) < 8u - stage)) break;
        __builtin_amdgcn_s_sleep(2);
    }
    CFENCE();
}
DEV void pre_wait(rsrc_t rs, int first, int n, u32 stage, v2u ep) {
    if (uni(threadIdx.x >> 6) == 0) pre_poll(rs, first, n, stage, ep);
    BAR();
}
DEV u32 pol_wait(rsrc_t rs, v2u ep) {
    const int lane = threadIdx.x & 63;
    v4u a;
#pragma unroll 1
    for (int it = 0; it < SPIN_CAP; ++it) {
        CFENCE();
        a = ld16s(rs, foff(F_PRE + (lane < NPRE || lane >= 48 ? lane : 0)), 0);
        if (all_(a.y == ep.y && a.x - ep.x - 1u < 7u)) break;
        __builtin_amdgcn_s_sleep(2);
    }
    CFENCE();
    return uu(wave_sum(lane >= 48 ? (float)(int)a.z : 0.f) >= 0.f ? 1u : 0u);
}
#define PROBE_T 1000
#define NPROBE 32
DEV u32 pol_probe(const Params& p, rsrc_t rs, int id) {
    const int lane = threadIdx.x & 63;
    const u32 off0 = W_PROBE + (u32)(id >> 2) * 16384u + (u32)(id & 3) * 128u;
    const u32 tend = rt32() + PROBE_T;
    st4s(rs, lane < 32 ? off0 + (u32)lane * 512 : OOB, 0, 0u); DRAIN();
    int calm = 0;
    int d[2] = {1 << 16, 1 << 16};
#pragma unroll 1
    for (int q = 0; q < NPROBE && ((q & 1) || (calm < 6 && (int)(rt32() - tend) < 0)); ++q) {
        const u32 half = (u32)(q ^ (q >> 1) ^ id) & 1u;
        const unsigned long long t0 = __builtin_amdgcn_s_memtime();
        const u32 v = ld4(rs, lane == 0 ? off0 + half * 8192 + (u32)(q >> 1) * 512 : OOB);
        asm volatile("s_waitcnt vmcnt(0)" :: "v"(v) : "memory");
        const int dt = (int)(__builtin_amdgcn_s_memtime() - t0);
        if (half) d[1] = min(d[1], dt); else d[0] = min(d[0], dt);
        calm += dt < 1100;
    }
    const u32 xcc = (u32)__builtin_amdgcn_s_getreg(20 | (3 << 11)) & 0xfu;
    return (u32)min(max((d[0] - d[1]) * ((xcc & 4u) ? -1 : 1), -250), 250);
}
DEV f32x2 ln_stats(const f32x2 (&x)[5]) {
    const float c = f_(uu(u_(x[0].x)));
    f32x2 d[5];
#pragma unroll
    for (int i = 0; i < 5; ++i) d[i] = x[i] - c;
    const f32x2 s2 = (d[0] + d[1]) + (d[2] + d[3]) + d[4], q2 = (d[0] * d[0] + d[1] * d[1]) + (d[2] * d[2] + d[3] * d[3] + d[4] * d[4]);
    float a = s2.x + s2.y, b = q2.x + q2.y, a2, b2;
#define LN_DPP(m) "v_add_f32_dpp %0, %0, %0 " m " row_mask:0xf bank_mask:0xf\n\tv_add_f32_dpp %1, %1, %1 " m " row_mask:0xf bank_mask:0xf\n\ts_nop 0\n\t"
    asm("s_nop 1\n\t" LN_DPP("quad_perm:[1,0,3,2]") LN_DPP("quad_perm:[2,3,0,1]") LN_DPP("row_ror:4")
        "v_add_f32_dpp %0, %0, %0 row_ror:8 row_mask:0xf bank_mask:0xf\n\tv_add_f32_dpp %1, %1, %1 row_ror:8 row_mask:0xf bank_mask:0xf\n\ts_nop 1\n\t"
        "v_permlane16_swap_b32 %0, %1\n\tv_add_f32 %0, %0, %1\n\tv_mov_b32 %1, %0\n\ts_nop 1\n\tv_permlane32_swap_b32 %0, %1\n\tv_add_f32 %0, %0, %1\n\ts_nop 0\n\t"
        "v_readlane_b32 %2, %0, 0\n\tv_readlane_b32 %3, %0, 16\n\ts_nop 1"
        : "+v"(a), "+v"(b), "=s"(a2), "=s"(b2));
    const float m = a2 * (1.0f / DM), var = fmaxf(b2 * (1.0f / DM) - m * m, 0.f);
    const float sd = rbf(__builtin_amdgcn_sqrtf(rbf(rbf(var) + 1e-5f)));
    return (f32x2){rbf(c + m), __builtin_amdgcn_rcpf(sd)};
}
DEV u32 pkv(f32x2 v) { u32 d; asm("v_cvt_pk_bf16_f32 %0, %1, %2" : "=v"(d) : "v"(v.x), "v"(v.y)); return d; }
DEV void store_h(rsrc_t rs, u32 base, int row, const u32 (&hp)[5]) {
    const u32 lane = threadIdx.x & 63;
    const u32 sw = (u32)(row & 15) * 16u, o8 = (lane >> 5) * 8192u + (((lane & 31u) * 8u) ^ sw), hb = base + (u32)(row >> 5) * (32 * DM * 2) + (u32)(row & 31) * 256u;
    st8s(rs, o8, hb, (v2u){hp[0], hp[1]});
    st8s(rs, o8, hb + 16384, (v2u){hp[2], hp[3]});
    st4s(rs, (lane * 4) ^ sw, hb + 32768, hp[4]);
}

#define DMA16(ldsb, vo, so) asm volatile("s_mov_b32 m0, %0\n\ts_nop 0\n\tbuffer_load_dwordx4 %1, %2, %3 offen sc1 lds" :: "s"(ldsb), "v"(vo), "s"(srd), "s"(so) : "memory")
#define DMA16I(ldsb, lo, vo, so, go) do { u32 so_; asm volatile("s_add_u32 m0, %1, %5\n\ts_add_u32 %0, %4, %6\n\tbuffer_load_dwordx4 %2, %3, %0 offen sc1 lds" \
                         : "=&s"(so_) : "s"(ldsb), "v"(vo), "s"(srd), "s"(so), "i"(lo), "i"(go) : "memory", "scc"); } while (0)
#define DMA32(ldsb, vo, so) asm volatile("s_mov_b32 m0, %0\n\ts_nop 0\n\tbuffer_load_dwordx4 %1, %2, %3 offen sc1 lds\n\tbuffer_load_dwordx4 %1, %2, %3 offen offset:1024 sc1 lds" \
                         :: "s"(ldsb), "v"(vo), "s"(srd), "s"(so) : "memory")
#define DMAIN(sr, ldsb, vo, so) asm volatile("s_mov_b32 m0, %0\n\ts_nop 0\n\tbuffer_load_dwordx4 %1, %2, %3 offen lds" :: "s"(ldsb), "v"(vo), "s"(sr), "s"(so) : "memory")
DEV v4u mk_srd(const void* q, u32 bytes) { return (v4u){uu((u32)(size_t)q), uu((u32)((size_t)q >> 32) & 0xffffu), bytes, 0x00020000u}; }
#define DMANT(sr, ldsb, vo, so) asm volatile("s_mov_b32 m0, %0\n\ts_nop 0\n\tbuffer_load_dwordx4 %1, %2, %3 offen nt lds" :: "s"(ldsb), "v"(vo), "s"(sr), "s"(so) : "memory")
#define AHEAD(sr, vo, so) asm volatile("buffer_load_dwordx4 %0, %1, %2, %3 offen" : "+v"(junk) : "v"(vo), "s"(sr), "s"(so) : "memory")
#define MK_SRD() const v4u srd = mk_srd(p.ws, W_END)

constexpr int CV_W = 40960;
template <bool B> struct CvL {
    static constexpr int NF = B ? 5 : 3, NK = B ? 4 : 2, KP = 544 + NK * 256, CH = NF * 1024 + 4 * KP;
};
constexpr int CV_Y = 4 * CvL<false>::CH, CV_YP = 272;
static_assert(CV_Y + 32 * CV_YP <= CV_W && 2 * CvL<true>::CH <= CV_Y, "conv lds");
DEV int cv_chan(int u, int i) { return 16 * (u % 40) + 4 * i + u / 40; }
template <bool B> DEV void cv_filt(const Params& p, u32 dst, int d, int i) {
    const u32 lane = threadIdx.x & 63;
    const v4u sf = mk_srd(p.ssmk + d * SEQ, SEQ * 2);
    if (i == 0) DMANT(sf, dst, lane < 4u ? OOB : lane * 16u - 64u, 0);
    else DMANT(sf, uu(dst + i * 1024), i == CvL<B>::NF - 1 && lane >= 4u ? OOB : lane * 16u, i * 1024 - 64);
}
DEV v4u cv_kvld(rsrc_t rs, int d, int i) { return ld16s(rs, (threadIdx.x & 63) * 16u, uu(W_KVT + (u32)d * (SEQ * 2) + i * 1024)); }
template <bool B> DEV void cv_kvput(lptr F, const v4u* k) {
    const int lane = threadIdx.x & 63;
#pragma unroll
    for (int i = 0; i < CvL<B>::NK; ++i) lds_w16(F + CvL<B>::NF * 1024 + (lane & 3) * CvL<B>::KP + 512 + i * 256 + (lane >> 2) * 16, k[i]);
}
template <bool B> DEV void cv_zero(lptr F) {
    const int lane = threadIdx.x & 63;
#pragma unroll
    for (int i = 0; i < 4; ++i) lds_w16(F + (lane >> 5) * CvL<B>::CH + CvL<B>::NF * 1024 + i * CvL<B>::KP + (lane & 31) * 16, (v4u){0u, 0u, 0u, 0u});
}
#define M32V(d, a, b) asm volatile("v_mfma_f32_32x32x16_bf16 %0, %1, %2, %0" : "+v"(d) : "v"(a), "v"(b))
template <bool B, class G> DEV void cv_run(const Params& p, rsrc_t rs, lptr F, int d, u32 D, f32x4 sp, float (&y)[16], G&& gap) {
    constexpr int KP = CvL<B>::KP, FO = B ? 2048 : 0;
    const int lane = threadIdx.x & 63, n = lane & 31, hh = lane >> 5, c = lane & 3;
    lptr fa = F + 64 + (32 * hh + 16 * (c & 1) + 8 * ((lane >> 4) & 1) + 4 * (c >> 1) - (lane & 12)) * 2;
    lptr kl = F + CvL<B>::NF * 1024 + (32 + n - hh) * 16;
    v4u dg;
#pragma unroll
    for (int q = 0; q < 4; ++q) { const int k = (lane & 35) ? -1 : (n & 4) + (n >> 3); dg[q] = k == 2 * q ? D : k == 2 * q + 1 ? D << 16 : 0u; }
    v4u l[4], h[4]; v2u fc[4], fh[4];
    f32x16 acc[4];
#pragma unroll
    for (int j = 0; j < 4; ++j) { l[j] = lds_r16(kl + j * KP); if constexpr (B) h[j] = lds_r16(kl + j * KP + 512); }
#pragma unroll
    for (int j = 0; j < 2; ++j) { fc[j] = lds_tr(fa + FO - 32 * j); if constexpr (B) fh[j] = lds_tr(fa - 32 * j); }
    asm volatile("s_nop 1" : "+v"(dg));
#pragma unroll
    for (int j = 0; j < 4; ++j) asm volatile("v_mfma_f32_32x32x16_bf16 %0, %1, %2, 0" : "=&v"(acc[j]) : "v"(dg), "v"(B ? h[j] : l[j]));
    constexpr int U = B ? 2 : 4;
    kl -= 32 * U; fa -= 32;
    auto body = [&](bool first) __attribute__((always_inline)) {
#pragma unroll
        for (int z = 0; z < U; ++z) {
            const int c = 2 * (z & 1);
            const v4u f = __builtin_shufflevector(fc[c], fc[c + 1], 0, 1, 2, 3), g = __builtin_shufflevector(fh[c], fh[c + 1], 0, 1, 2, 3);
#pragma unroll
            for (int j = 0; j < 4; ++j) {
                M32V(acc[j], f, l[j]); l[j] = lds_r16(kl + j * KP + 32 * (U - z - 1));
                if (j < 2) fc[(c ^ 2) + j] = lds_tr(fa + FO + (z + 1) * 128 + 32 - 32 * j);
                if (first && j > 0) gap(5 * z + j - 1);
                SCHED();
            }
            if constexpr (B) {
#pragma unroll
                for (int j = 0; j < 4; ++j) {
                    M32V(acc[j], g, h[j]); h[j] = lds_r16(kl + j * KP + 512 + 32 * (U - z - 1));
                    if (j < 2) fh[(c ^ 2) + j] = lds_tr(fa + (z + 1) * 128 + 32 - 32 * j);
                    if (first && j > 1) gap(5 * z + j + 1);
                    SCHED();
                }
            }
        }
        kl -= 32 * U; fa += 128 * U;
    };
    if constexpr (B) body(true);
#pragma unroll 1
    for (int m = B ? 2 * U : 0; m < 32; m += 2 * U) body(false);
    asm volatile("s_nop 11" : "+v"(acc[0]), "+v"(acc[1]), "+v"(acc[2]), "+v"(acc[3]));
    const float o[3] = {acc[1][15] + acc[2][14] + acc[3][13], acc[2][15] + acc[3][14], acc[3][15]};
    if constexpr (!B) st16(rs, lane == 63 ? W_SP + (u32)d * 16u : OOB, (v4u){u_(o[0]), u_(o[1]), u_(o[2]), 0u});
#pragma unroll
    for (int i = 0; i < 16; ++i) {
        float v = acc[0][i];
#pragma unroll
        for (int j = 1; j < 4; ++j) if (j <= i) v += acc[j][i - j];
        if (i < 3) { const float e = f_((u32)__builtin_amdgcn_ds_bpermute((hh ? lane - 32 : lane + 31) * 4, (int)u_(o[i]))); v += lane == 0 ? sp[i] : e; }
        y[i] = v;
    }
}
DEV void cv_put(lptr F, int pr, const float (&y0)[16], const float (&y1)[16]) {
    const int lane = threadIdx.x & 63;
#pragma unroll
    for (int i = 0; i < 16; ++i) *(LAS u32*)(F + CV_Y + (lane & 31) * CV_YP + (lane >> 5) * 128 + i * 8 + pr * 4) = pk(y0[i], y1[i]);
}
DEV void cv_out(rsrc_t rs, lptr F, int u, int blk0) {
    const int lane = threadIdx.x & 63;
    lptr q = F + CV_Y + ((lane >> 2) & 3) * CV_YP + ((lane >> 4) * 4 + (lane & 3)) * 8;
    const u32 yo = W_Y + (u32)(((lane >> 2) * 8 + blk0) * DM + 4 * u + (lane & 3)) * 16u;
#pragma unroll
    for (int i = 0; i < 8; ++i) { lptr a = q + (i >> 1) * (8 * CV_YP) + (i & 1) * 128; st16(rs, yo + (u32)((i & 1) * 128 + (i >> 1)) * (DM * 16), tr2(a, a + 4 * CV_YP)); }
}
struct KvIn { v2u k[19], v[16]; v4u s[2]; };
DEV void kvt_load(const Params& p, int tile, KvIn& in) {
    const int lane = threadIdx.x & 63;
    const rsrc_t rk = mk_rsrc(p.bk, ACT), rv = mk_rsrc(p.bv, ACT), rh = mk_rsrc(p.shift, DM * 8);
    const int row = (tile / 10) * 64 + (lane >> 4) * 16, col = (tile % 10) * 64 + (lane & 15) * 4;
    const u32 vo = (u32)(row * DM + col) * 2u, vp = row > 0 ? vo - 3 * (DM * 2) : OOB;
#pragma unroll
    for (int i = 0; i < 2; ++i) in.s[i] = ldin16(rh, (u32)col * 8u + i * 16, 0);
#pragma unroll
    for (int i = 0; i < 19; ++i) { in.k[i] = i < 3 ? ldin8(rk, vp, i * (DM * 2)) : ldin8(rk, vo, (i - 3) * (DM * 2)); if (i >= 3) in.v[i - 3] = ldin8(rv, vo, (i - 3) * (DM * 2)); }
}
DEV void kvt_make(rsrc_t rs, lptr xt, int tile, const KvIn& in) {
    const int lane = threadIdx.x & 63;
#pragma unroll
    for (int c = 0; c < 2; ++c) {
        const v4u sv = in.s[c];
        const f32x2 sh[4] = {{lo_f(sv.x), lo_f(sv.z)}, {hi_f(sv.x), hi_f(sv.z)}, {lo_f(sv.y), lo_f(sv.w)}, {hi_f(sv.y), hi_f(sv.w)}};
        f32x2 kf[19];
#pragma unroll
        for (int i = 0; i < 19; ++i) kf[i] = up2(in.k[i][c]);
        u32 kv[16];
#pragma unroll
        for (int i = 0; i < 16; ++i) kv[i] = pk2(((kf[i] * sh[3] + kf[i + 1] * sh[2]) + (kf[i + 2] * sh[1] + kf[i + 3] * sh[0])) * up2(in.v[i][c]));
#pragma unroll
        for (int q = 0; q < 8; ++q) {
            const u32 sel = (q & 1) ? 0x07060302u : 0x05040100u; const u32* z = kv + (q >> 1);
            *(LAS v2u*)(xt + ((2 * c + (q & 1)) * 16 + (lane & 15)) * 144 + (lane >> 5) * 64 + (q >> 1) * 16 + ((lane >> 4) & 1) * 8) =
                (v2u){__builtin_amdgcn_perm(z[4], z[0], sel), __builtin_amdgcn_perm(z[12], z[8], sel)};
        }
    }
    const u32 o = W_KVT + (u32)(((tile % 10) * 64 + 4 * (lane >> 3)) * SEQ + (tile / 10) * 64 + (lane & 7) * 8) * 2u;
#pragma unroll
    for (int i = 0; i < 8; ++i) st16(rs, o + (u32)(32 * (i & 1) + (i >> 1)) * (SEQ * 2), lds_r16(xt + (8 * i + (lane >> 3)) * 144 + (lane & 7) * 16));
}
struct RowX { v2u a, b; u32 c; };
DEV void ln_row(rsrc_t rs, int row, const RowX& r) {
    const f32x2 x[5] = {up2(r.a.x), up2(r.a.y), up2(r.b.x), up2(r.b.y), up2(r.c)};
    const f32x2 st = ln_stats(x);
    u32 hp[5];
#pragma unroll
    for (int q = 0; q < 5; ++q) hp[q] = pk2(up2(pkv(x[q] - st.x)) * st.y);
    store_h(rs, W_H1, row, hp);
}
DEV void pre_rings(const Params& p, rsrc_t rs, int j) {
    const u32 gi = (u32)(j * 256 + threadIdx.x), gt = gi * 16u, R = NPRE * 4096, n = ((gt >> 14) << 9) + ((gt & 8191u) >> 4);
    v4u m = {PSENT, PSENT, PSENT, PSENT};
    if (n < 640u) {
        m = *(const v4u*)(p.mem0 + (2 * ((n >> 2) & 3) + (n & 1)) * DM + (n >> 4) * 16 + (n & 2) * 4);
#pragma unroll
        for (int i = 0; i < 4; ++i) m[i] = m[i] == PSENT ? 0x7FC17FC1u : m[i];
    }
    st4s(rs, gi - 36u * 256u < F_CLR ? foff(gi - 36u * 256u) : OOB, 0, 0u);
#pragma unroll 1
    for (u32 o = gt; o < MB; o += R) st16(rs, W_ORING + o, (v4u){PSENT, PSENT, PSENT, PSENT});
    const u32 mo = gt < 4 * 32768 + 16384 ? W_MRING + gt : OOB;
    st16(rs, mo, m);
    st16(rs, mo + MB / 4, m);
    static_assert(NPRE * 4096 >= 4 * 32768 + 16384, "memory ring");
}
constexpr int LN0 = NPRE * 4;
struct Early { KvIn in; RowX r; u32 vote; };
DEV void conv_early(const Params& p, rsrc_t rs, int j, Early& e) {
    const int lane = threadIdx.x & 63, w = uni(threadIdx.x >> 6), u = j * 4 + w;
    if (j < NPRE) {
        const rsrc_t rx = mk_rsrc(p.x, ACT);
        e.r.a = __builtin_amdgcn_raw_buffer_load_b64(rx, lane * 8u, uu(u * (DM * 2)), 0); e.r.b = __builtin_amdgcn_raw_buffer_load_b64(rx, lane * 8u + 512u, uu(u * (DM * 2)), 0);
        e.r.c = __builtin_amdgcn_raw_buffer_load_b32(rx, lane * 4u + 1024u, uu(u * (DM * 2)), 0);
        kvt_load(p, u, e.in);
    } else if (j < NCV && w > 0) e.vote = pol_probe(p, rs, u - NPRE * 4);
}
DEV void conv_stage(const Params& p, rsrc_t rs, lptr lds, int j, int g0, int ge, v2u ep, Early& e) {
    const int tid = threadIdx.x, lane = tid & 63, w = uni(tid >> 6), u = j * 4 + w;
    lptr F = lds + w * CV_W;
    const u32 fb = uu(w * CV_W);
    const rsrc_t rd = mk_rsrc(p.ssmD, DM * 2);
    float y0[16], y1[16];
    if (j < NPRE) {
        constexpr int CH = CvL<false>::CH;
        KvIn& in = e.in;
        if (w == 1) LST(1500 + j, rt32());
        pre_rings(p, rs, j);
        ln_row(rs, u, e.r);
        if (w == 1) LST(1550 + j, rt32());
        DRAIN(); BAR();
        pre_post(rs, j, 1u, ep);
        if (j == 20) LST(260, rt32());
        if (w == 0) LST(1450 + j, rt32());
        const u32 D = __builtin_amdgcn_raw_buffer_load_b16(rd, (u32)cv_chan(u, lane & 3) * 2u, 0, 0);
#pragma unroll 1
        for (int i = 0; i < 4; ++i) { const int d = cv_chan(u, i); cv_filt<false>(p, fb + i * CH, d, 0); cv_filt<false>(p, fb + i * CH, d, 1); cv_filt<false>(p, fb + i * CH, d, 2); }
        kvt_make(rs, F + CV_Y, u, in);
        cv_zero<false>(F); cv_zero<false>(F + 2 * CH);
        DRAIN(); BAR();
        pre_post(rs, j, 2u, ep);
        if (j == 20) LST(262, rt32());
        pre_wait(rs, 0, NPRE, 2u, ep);
        if (j == 20) LST(281, rt32());
        {
            v4u k[8];
#pragma unroll
            for (int i = 0; i < 8; ++i) k[i] = cv_kvld(rs, cv_chan(u, i >> 1), i & 1);
#pragma unroll
            for (int i = 0; i < 4; ++i) cv_kvput<false>(F + i * CH, k + 2 * i);
        }
        if (j == 20) LST(286, rt32());
#pragma unroll 1
        for (int i = 0; i < 4; i += 2) {
            cv_run<false>(p, rs, F + i * CH, cv_chan(u, i), rdl(D, i), f32x4{}, y0, [](int) {});
            cv_run<false>(p, rs, F + (i + 1) * CH, cv_chan(u, i + 1), rdl(D, i + 1), f32x4{}, y1, [](int) {});
            cv_put(F, i >> 1, y0, y1);
        }
        if (j == 20) LST(287, rt32());
        cv_out(rs, F, u, 0);
        if (j == 20) LST(263, rt32());
    } else {
        st16(rs, lane == 0 ? foff(F_PRE + 48 + u - NPRE * 4) : OOB, (v4u){ep.x + 1u, ep.y, w > 0 ? e.vote : 0u, 0u});
#pragma unroll 1
        for (int t = u + 128; t < 320; t += 16) { kvt_load(p, t, e.in); kvt_make(rs, F, t, e.in); }
    }
    DRAIN(); BAR();
    if (g0 < ge) {
        constexpr int CH = CvL<true>::CH;
        pre_post(rs, j, 3u, ep);
        cv_zero<true>(F);
        if (w == 0) { pre_poll(rs, 64, 32, 3u, ep); pre_poll(rs, 0, NCV, 3u, ep); }
        BAR();
        v4u k[4];
        auto fetch = [&](u32 dst, int d, int i) { if (i < 5) cv_filt<true>(p, dst, d, i); else if (i < 9) k[i - 5] = cv_kvld(rs, d, i - 5); };
#pragma unroll
        for (int i = 0; i < 9; ++i) fetch(fb, cv_chan(g0 * 4 + w, 0), i);
        const u32 ch = (u32)cv_chan(min((g0 + 8 * (lane >> 2)) * 4 + w, 159), lane & 3);
        u32 D = __builtin_amdgcn_raw_buffer_load_b16(rd, ch * 2u, 0, 0);
        v4u sp = ld16s(rs, W_SP + ch * 16u, 0);
        asm volatile("" : "+v"(D), "+v"(sp));
#pragma unroll 1
        for (int g = g0, n = 0; g < ge; g += 8) {
            const int un = g * 4 + w;
            if (g == 16) { LST(282, rt32()); LST(288, __builtin_amdgcn_s_memtime()); }
#pragma unroll 1
            for (int i = 0; i < 4; i += 2, n += 2) {
                const int d1 = (int)rdl(ch, n + 1), d2 = (int)rdl(ch, n + 2);
                cv_kvput<true>(F, k);
                cv_run<true>(p, rs, F, 0, rdl(D, n), (f32x4){f_(rdl(sp.x, n)), f_(rdl(sp.y, n)), f_(rdl(sp.z, n)), 0.f}, y0, [&](int q) { fetch(fb + CH, d1, q); });
                cv_kvput<true>(F + CH, k);
                cv_run<true>(p, rs, F + CH, 326, rdl(D, n + 1), (f32x4){f_(rdl(sp.x, n + 1)), f_(rdl(sp.y, n + 1)), f_(rdl(sp.z, n + 1)), 0.f}, y1, [&](int q) { fetch(fb, d2, q); });
                cv_put(F, i >> 1, y0, y1);
            }
            cv_out(rs, F, un, 4);
            if (g == 16) { LST(283, rt32()); LST(289, __builtin_amdgcn_s_memtime()); }
        }
        DRAIN(); BAR();
    }
    pre_post(rs, j, 5u, ep);
    if (j == 20) LST(284, rt32());
    if (j == 3) LST(285, rt32());
}

#define M32A(first, d, a, b) do { if (first) asm volatile("v_mfma_f32_32x32x16_bf16 %0, %1, %2, 0" : "=&v"(d) : "a"(a), "v"(b)); \
                                  else asm volatile("v_mfma_f32_32x32x16_bf16 %0, %1, %2, %0" : "+v"(d) : "a"(a), "v"(b)); } while (0)
#define M32B(first, d, a, b) do { if (first) asm volatile("v_mfma_f32_32x32x16_bf16 %0, %1, %2, 0" : "=&v"(d) : "v"(a), "a"(b)); \
                                  else asm volatile("v_mfma_f32_32x32x16_bf16 %0, %1, %2, %0" : "+v"(d) : "v"(a), "a"(b)); } while (0)
#define VCVT(d, a, b) asm volatile("v_cvt_pk_bf16_f32 %0, %1, %2" : "=v"(d) : "v"(a), "v"(b))
#define VADD(d, a, b) asm volatile("v_add_f32 %0, %1, %2" : "=v"(d) : "v"(a), "v"(b))
#define LD4A(d, vo, so) asm volatile("buffer_load_dword %0, %1, %2, %3 offen sc1" : "=v"(d) : "v"(vo), "s"(srd), "s"(so) : "memory")
DEV u32 wst_src(int lane, int ld) { return (u32)((lane >> 4) * ld + 8 * ((lane & 15) ^ ((lane >> 4) << 2))) * 2u; }
DEV u32 wst_rd(int lane, int w) { const int q4 = (lane >> 2) & 3; return (u32)(q4 * 256 + ((w ^ q4) * 64) + ((lane >> 4) & 1) * 32 + (lane & 3) * 8); }

constexpr int QK_SEL = 81920;
#define QK_LEAD 12
#define QK_SMP 56
#define QK_W0 8
DEV void role_qkv(const Params& p, lptr lds, int c, v2u ep) {
    const int tid = threadIdx.x, lane = tid & 63, w = uni(tid >> 6), hh = lane >> 5, r32 = lane & 31;
    const int m = c >> 5, h = c & 31;
    const rsrc_t rs = mk_rsrc(p.ws, W_END);
    v4u wt[40];
    {
        const v4u sw = mk_srd(m == 0 ? p.Wq : m == 1 ? p.Wk : p.Wv, DM * NQ * 2), sx = mk_srd(p.x, ACT);
        const u32 vo = wst_src(lane, NQ) + (u32)h * (DH * 2), x8 = (u32)lane * 8u, x4 = (u32)lane * 4u;
        RowX xr[5];
        auto wfill = [&](int i0, int n) {
#pragma unroll 1
            for (int i = i0; i < i0 + n; ++i) DMAIN(sw, uu((4 * i + w) * 1024), vo, uu((16 * i + 4 * w) * (NQ * 2)));
        };
        wfill(0, QK_W0);
#pragma unroll
        for (int i = 0; i < 5; ++i)
            asm volatile("buffer_load_dwordx2 %0, %3, %5, %6 offen\n\tbuffer_load_dwordx2 %1, %3, %5, %6 offen offset:512\n\tbuffer_load_dword %2, %4, %5, %6 offen offset:1024"
                         : "=&v"(xr[i].a), "=&v"(xr[i].b), "=&v"(xr[i].c) : "v"(x8), "v"(x4), "s"(sx), "s"(uu((LN0 + 4 * c + w + 384 * i) * (DM * 2))) : "memory");
        wfill(QK_W0, 40 - QK_W0);
        if (c == 3) LST(275, rt32());
        VMW(40 - QK_W0);
        if (c == 3) LST(276, rt32());
#pragma unroll
        for (int i = 0; i < 5; ++i) {
            asm volatile("" : "+v"(xr[i].a), "+v"(xr[i].b), "+v"(xr[i].c));
            const int row = LN0 + 4 * c + w + 384 * i;
            if (i < 4 || row < SEQ) ln_row(rs, row, xr[i]);
        }
        if (c == 3) LST(264, rt32());
        VMW(12); BAR();
        lptr tb = lds + hh * 2048 + wst_rd(lane, w);
        SFOR(k, 40)
            lptr q = tb + (k >> 3) * 32768 + (k & 7) * 4096; wt[k] = tr2(q, q + 1024); asm volatile("" : "+a"(wt[k]));
        SEND
        DRAIN(); BAR();
        pre_post(rs, 96 + c, 1u, ep);
    }
    if (c == 3) LST(268, rt32());
    if (w == 0) LST(1350 + c, rt32());
    bool dead = false;
    int n0, n1;
    const u32 t16 = (u32)tid * 16u, wb = uu(w * 1024);
    u32 ar[16];
#pragma unroll
    for (int j = 0; j < 16; ++j) ar[j] = (u32)(r32 * 256 + ((2 * (j & 7) + hh) ^ (r32 & 15)) * 16 + (j >> 3) * 49152);
    MK_SRD();
#define XRD(y, ks) lds_r16(lds + ar[((ks) & 7) + ((ks) >= 24 ? 8 : 0)] + ((ks) >> 3) % 3 * 16384 + (y) * 8192)
#define QDMA(y, i, vo, so) DMA16I(wb, ((i) >> 1) * 16384 + ((i) & 1) * 4096 + (y) * 8192, vo, so, (i) * 4096)
    const u32 h2flag = (lane < 32 && w == 0) ? foff(F_H2 + lane) : OOB;
    const u32 ol = (m == 2 ? (u32)(2 * w + (r32 >> 4)) * 1024u : (u32)((r32 >> 4) * 4 + w) * 1024u) + (u32)(hh * 16 + (r32 & 15)) * 16u
                 + (m == 2 ? W_VO : m == 1 ? W_KQ : W_KQ + 8192u) + (u32)h * 16384u;
    const u32 fo = tid < 2 ? foff(F_QKV + tid * 96 + c) : OOB;
    lptr selw = lds + QK_SEL + (w == 0 ? 0 : 64), selr = lds + QK_SEL;
    auto late = [&](u32 fv) { const u32 need = n1 >= NSEG ? 0xFFFFFFFFu : dead ? 0u : (u32)n1 + 1u; return (u32)__builtin_amdgcn_ballot_w64(fv < need); };
    static_assert(W_H2 - W_H1 == 2 * 64 * (32 * DM * 2), "h");
    pre_wait(rs, 0, NPRE, 1u, ep);
    if (c == 3) LST(269, rt32());
    auto run = [&](auto isv) __attribute__((always_inline)) {
        constexpr bool V = decltype(isv)::value;
        f32x16 ax, ay = {};
        v4u x[8], pp, pq;
        u32 oy = OOB, lt = 1, fcnt = 0, fa = OOB;
        u32 nn = 0;
        PIN(lt); PIN(nn);
#pragma unroll
        for (int z = 0; z < 5; ++z) x[z] = XRD(1, 20 + z);
#define QPK(r_, a1, a_, q_) do { if constexpr (V) VCVT(r_[q_], a1[4 * (a_) + 8 * ((q_) >> 1) + 2 * ((q_) & 1)], a1[4 * (a_) + 8 * ((q_) >> 1) + 2 * ((q_) & 1) + 1]); \
                                 else VCVT(r_[q_], a1[4 * (a_) + (q_)], a1[4 * (a_) + (q_) + 8]); } while (0)
#define QPK4(r_, a1, a_) do { QPK(r_, a1, a_, 0); QPK(r_, a1, a_, 1); QPK(r_, a1, a_, 2); QPK(r_, a1, a_, 3); } while (0)
        for (;;) {
            u32 ls = uu(lt);
            n0 = uni((int)nn); n1 = n0 >> 8; n0 &= 255;
            if (ls != 0u && n0 >= min(NSEG, n1 + QK_LEAD)) {
                CFENCE();
                u32 fv = ld4(rs, h2flag);
                asm volatile("s_waitcnt vmcnt(0)" : "+v"(fv) :: "memory");
                *(LAS u32*)(selw + 16) = late(fv);
                BAR();
                st4s(rs, fa, 0, fcnt);
                fa = OOB;
                if (n1 >= NSEG) __builtin_amdgcn_endpgm();
                if (uu(*(LAS u32*)(selr + 16)) != 0u) {
                    if (w == 0) wait_flags(rs, h2flag, (u32)n1 + 1, dead);
                    BAR();
                }
                ls = 0u;
            }
            if (n0 == 4) pre_wait(rs, 96, 96, 1u, ep);
            int cur = n0;
            asm volatile("s_cmp_eq_u32 %3, 0\n\ts_cselect_b32 %0, %4, %0\n\ts_addc_u32 %2, %2, 0\n\ts_cmp_lg_u32 %3, 0\n\ts_addc_u32 %1, %1, 0"
                         : "+s"(cur), "+s"(n0), "+s"(n1) : "s"(ls), "s"(n1 | 64) : "scc");
            u32 xs = W_H1 + (u32)(cur + (cur & 64)) * (32 * DM * 2), ys, yvo, ox, fv;
            PINS(xs);
#define QT(k) do {} while (0)
            QT(0);
            SCHED();
            QDMA(0, 0, t16, xs); QDMA(0, 1, t16, xs);
            SFOR(z, 80)
                constexpr int ks = (z + 20) % 40, zn = (z + 5) % 80;
                if constexpr (z >= 20 && z < 60) { if constexpr (V) M32B(ks == 0, ax, x[z & 7], wt[ks]); else M32A(ks == 0, ax, wt[ks], x[z & 7]); }
                else { if constexpr (V) M32B(ks == 0, ay, x[z & 7], wt[ks]); else M32A(ks == 0, ay, wt[ks], x[z & 7]); }
                x[(z + 5) & 7] = XRD(zn < 20 || zn >= 60, (zn + 20) % 40);
                if constexpr (z == 77) lt = *(LAS u32*)selr;
                SCHED();
                if constexpr (z < 8) QDMA(0, z + 2, t16, xs);
                if constexpr (z == 9) { ys = W_H1 + (u32)n0 * (32 * DM * 2); yvo = t16 | (n0 < NSEG ? 0u : OOB); PINS(ys); PIN(yvo); }
                if constexpr (z == 11) { ox = ol + (u32)cur * (NH * 16384u); PIN(ox); }
                if constexpr (z == 14) { QT(1); asm volatile("s_waitcnt vmcnt(6) lgkmcnt(0)\n\ts_barrier" ::: "memory"); QT(2); }
                if constexpr (z == 15) st4s(rs, fa, 0, fcnt);
                if constexpr (z >= 16 && z < 26) QDMA(1, z - 16, yvo, ys);
                if constexpr (z == 26) QPK4(pp, ay, 0);
                if constexpr (z == 27) st16(rs, oy, pp);
                if constexpr (z == 28) QPK4(pq, ay, 1);
                if constexpr (z == 29) st16(rs, oy + 512, pq);
                if constexpr (z == 30) { QT(3); asm volatile("s_waitcnt vmcnt(13)\n\ts_barrier" ::: "memory"); QT(4); }
                if constexpr (z == 32) { fcnt = lane == 0 ? (u32)n0 : (u32)n1; fa = fo; asm volatile("" : "+v"(fcnt), "+v"(fa)); }
                if constexpr (z == 34) { oy = (ol + (u32)n0 * (NH * 16384u)) | (yvo & OOB); PIN(oy); }
                if constexpr (z == 35) { nn = (u32)(min(n0 + 1, NSEG) | n1 << 8); PIN(nn); }
                if constexpr (z == 54) { asm volatile("s_waitcnt vmcnt(2) lgkmcnt(0)\n\ts_barrier" ::: "memory"); QT(5); }
                if constexpr (z == QK_SMP) LD4A(fv, h2flag, 0);
                if constexpr (z == 62) QPK4(pp, ax, 0);
                if constexpr (z == 63) st16(rs, ox, pp);
                if constexpr (z == 64) QPK4(pq, ax, 1);
                if constexpr (z == 65) st16(rs, ox + 512, pq);
                if constexpr (z == 73) { asm volatile("s_waitcnt vmcnt(2)" : "+v"(fv) :: "memory"); QT(6); ls = late(fv); PINS(ls); }
                if constexpr (z == 74) { lt = ls; PIN(lt); *(LAS u32*)selw = lt; }
                if constexpr (z == 76) asm volatile("s_waitcnt lgkmcnt(2)\n\ts_barrier" ::: "memory");
                SCHED();
            SEND
        }
    };
    if (m == 2) run(IC<1>{}); else run(IC<0>{});
}

#define OU_SMP 44
DEV void role_out(const Params& p, lptr lds, int L, int kq, int ng, v2u ep) {
    const int tid = threadIdx.x, lane = tid & 63, w = uni(tid >> 6), hh = lane >> 5, r32 = lane & 31;
    const rsrc_t rs = mk_rsrc(p.ws, W_END);
    v4u wo[64];
    bool dead = false;
    {
        const v4u sw = mk_srd(p.Wo, DM * NQ * 2);
        const u32 vo = wst_src(lane, DM) + (u32)ng * 256u;
        auto fill = [&](int b) {
#pragma unroll 1
            for (int i = 0; i < 8; ++i) DMAIN(sw, uu((b % 5) * 32768 + (4 * i + w) * 1024), vo, uu((kq * 1024 + b * 128 + 16 * i + 4 * w) * (DM * 2)));
        };
#pragma unroll 1
        for (int b = 0; b < 5; ++b) fill(b);
        lptr tb = lds + hh * 1024 + wst_rd(lane, w);
        SFOR(b, 8)
            VMW(b < 4 ? 32 : 56 - 8 * b); BAR();
            lptr q = tb + (b % 5) * 32768;
#pragma unroll
            for (int k = 0; k < 8; ++k) { lptr a = q + (k >> 1) * 8192 + (k & 1) * 2048; wo[8 * b + k] = tr2(a, a + 4096); if (b < 7) asm volatile("" : "+a"(wo[8 * b + k])); else PIN(wo[8 * b + k]); }
            if constexpr (b < 3) { BAR(); fill(b + 5); }
        SEND
    }
    pre_wait(rs, 0, NPRE, 1u, ep);
    if (kq == 0 && ng == 0) LST(272 + L, rt32());
    MK_SRD();
    const u32 ofl = lane < 8 ? foff(F_O + L * 32 + kq * 8 + lane) : OOB, l16 = (u32)lane * 16u;
    const u32 fo = (tid == 0) ? foff(F_P + L * 32 + kq * 5 + ng) : OOB;
    const u32 src0 = uu(W_VO + 8192u + (u32)(L * NSEG * NH + kq * 8) * 16384u + w * 1024);
#define ODMA(i, dst, so) DMA16I(dst, (i) * 4096, l16, so, ((i) >> 1) * 16384 + ((i) & 1) * 4096)
    auto first = [&](int s) { const u32 dst = uu((s & 1) * 65536 + w * 1024), src = src0 + (u32)s * (NH * 16384u); SFOR(i, 10) ODMA(i, dst, src); SEND };
    u32 pa = W_P + (u32)(L * NSEG) * P_ITEM + (u32)(4 * hh * 4 + kq) * (DM * 4) + (u32)(ng * 128 + w * 32 + r32) * 4u, fv = 0;
    bool early = false;
    for (int s = 0; s < NSEG; ++s) {
        const u32 dst = uu((s & 1) * 65536 + w * 1024), src = src0 + (u32)s * (NH * 16384u);
        lptr A = lds + (s & 1) * 65536 + l16;
        if (!early) {
            if (!all_(ofl == OOB || fv >= (u32)s + 1)) wait_flags(rs, ofl, (u32)s + 1, dead);
            LS3(L == 0 && w == 1, 1, kq * 5 + ng);
            first(s);
        }
        asm volatile("s_waitcnt vmcnt(8)\n\ts_barrier" ::: "memory");
        v4u o[64];
#pragma unroll
        for (int k = 0; k < 3; ++k) o[k] = lds_r16(A + k * 1024);
        f32x16 acc;
        SCHED();
        SFOR(k, 64)
            if constexpr (k < 56) M32B(k == 0, acc, o[k], wo[k]); else M32V(acc, o[k], wo[k]);
            if constexpr (k < 61) o[k + 3] = lds_r16(A + (k + 3) * 1024);
            SCHED();
            if constexpr (k < 12 && (k & 1) == 0) ODMA(10 + k / 2, dst, src);
            if constexpr (k == 4) asm volatile("s_waitcnt vmcnt(7)\n\ts_barrier" ::: "memory");
            if constexpr (k == 20) asm volatile("s_waitcnt vmcnt(5)\n\ts_barrier" ::: "memory");
            if constexpr (k == 40) asm volatile("s_waitcnt vmcnt(0)\n\ts_barrier" ::: "memory");
            if constexpr (k == OU_SMP) LD4A(fv, ofl, 0);
            SCHED();
        SEND
        asm volatile("s_nop 9" : "+v"(acc), "+v"(pa));
#pragma unroll
        for (int i = 0; i < 16; ++i) st4s(rs, pa, (u32)((8 * (i >> 2) + (i & 3)) * (4 * DM * 4)), u_(acc[i]));
        asm volatile("s_waitcnt vmcnt(16)" : "+v"(fv) :: "memory");
        LS3(L == 0 && w == 1, 2, kq * 5 + ng);
        early = s + 1 < NSEG && all_(ofl == OOB || fv >= (u32)s + 2);
        if (early) { first(s + 1); VMW(10); }
        else { fv = ld4(rs, ofl); asm volatile("s_waitcnt vmcnt(0)" : "+v"(fv) :: "memory"); }
        BAR();
        st4s(rs, fo, 0, (u32)s + 1);
        pa += P_ITEM;
        LS2(L == 0 && w == 1, 160 + kq * 5 + ng);
    }
}

constexpr int RW_P = 20480, RW_X = RW_P + 10240;
DEV void role_row(const Params& p, lptr lds, int id, v2u ep) {
    const int lane = threadIdx.x & 63, w = uni(threadIdx.x >> 6);
    const int ri = id >> 1, L = id & 1;
    const rsrc_t rs = mk_rsrc(p.ws, W_END);
    bool dead = false, have = false;
    const u32 fp = lane < 20 ? foff(F_P + L * 32 + lane) : OOB, l16 = (u32)lane * 16u, lx = lane < 16 ? l16 + 1024u : OOB, fb = uu(w * CV_W);
    lptr F = lds + w * CV_W;
    MK_SRD();
    const v4u sx = mk_srd(p.x, ACT);
    auto yfill = [&](int b) {
#pragma unroll 1
        for (int i = 0; i < 5; ++i) DMA32(uu(fb + (b & 1) * 10240 + i * 2048), l16, uu(W_Y + (u32)(ri * 8 + b) * 10240u + i * 2048));
    };
    auto xfill = [&](int s) {
        const u32 d = uu(fb + RW_X + (s & 1) * 2048), o = uu((u32)(s * 32 + ri) * (DM * 2));
        if (L == 0) { DMAIN(sx, d, l16, o); DMAIN(sx, d + 1024, lx, o); } else { DMA16(d, l16, o + W_X2); DMA16(d + 1024, lx, o + W_X2); }
    };
    auto pfill = [&](int s) {
        const u32 o = uu(W_P + (u32)(L * NSEG + s) * P_ITEM + (u32)ri * (4 * DM * 4));
        SFOR(i, 10) DMA16I(fb, RW_P + i * 1024, l16, o, i * 1024); SEND
        if (L == 1) xfill(s);
    };
    if (L == 0) xfill(0);
    pre_poll(rs, 0, NPRE, 3u, ep);
    if (id == 0) LST(274, rt32());
    yfill(0); DRAIN();
    u32 fv = 0;
    for (int s = 0; s < NSEG; ++s) {
        if (!have) {
            if (!all_(fp == OOB || fv >= (u32)s + 1)) wait_flags(rs, fp, (u32)s + 1, dead);
            LS3(L == 0, 3, ri);
            pfill(s);
        }
        LS2(L == 0, 192 + ri);
        f32x2 y[5];
        {
            lptr ya = F + ((s >> 3) & 1) * 10240 + (s & 7) * 2 + lane * 16;
            auto yr = [&](lptr q) { return lo_f(*(LAS u16*)q); };
            y[0] = (f32x2){yr(ya), yr(ya + 2560)}; y[1] = (f32x2){yr(ya + 5120), yr(ya + 7680)};
            y[2] = (f32x2){yr(ya + 1024), yr(ya + 3584)}; y[3] = (f32x2){yr(ya + 6144), yr(ya + 8704)};
            lptr yc = ya - lane * 16 + 2048 + (lane >> 1) * 16 + (lane & 1) * 5120;
            y[4] = (f32x2){yr(yc), yr(yc + 2560)};
            asm volatile("" : "+v"(y[0]), "+v"(y[1]), "+v"(y[2]), "+v"(y[3]), "+v"(y[4]));
        }
        if (!have) DRAIN();
        LS3(L == 0, 4, ri);
        f32x2 x1[5];
        {
            f32x4 a[4], b[4]; f32x2 c[4];
            lptr q = F + RW_X + (s & 1) * 2048;
            v2u xa = *(LAS v2u*)(q + lane * 8), xb = *(LAS v2u*)(q + 512 + lane * 8); u32 xc = *(LAS u32*)(q + 1024 + lane * 4);
#pragma unroll
            for (int kq = 0; kq < 4; ++kq) { lptr r = F + RW_P + kq * (DM * 4); a[kq] = *(LAS f32x4*)(r + l16); b[kq] = *(LAS f32x4*)(r + 1024 + l16); c[kq] = *(LAS f32x2*)(r + 2048 + lane * 8); }
            SCHED();
            const f32x4 sa = (a[0] + a[1]) + (a[2] + a[3]), sb = (b[0] + b[1]) + (b[2] + b[3]);
            const f32x2 o[5] = {sa.lo, sa.hi, sb.lo, sb.hi, (c[0] + c[1]) + (c[2] + c[3])};
            const u32 xi[5] = {xa.x, xa.y, xb.x, xb.y, xc};
#pragma unroll
            for (int i = 0; i < 5; ++i) x1[i] = up2(xi[i]) + o[i];
            asm volatile("" : "+v"(x1[0]), "+v"(x1[1]), "+v"(x1[2]), "+v"(x1[3]), "+v"(x1[4]));
        }
        have = s + 1 < NSEG && all_(fp == OOB || fv >= (u32)s + 2);
        if (__builtin_expect(have, 0)) pfill(s + 1);
        if (L == 0) xfill(s + 1);
        const f32x2 st1 = ln_stats(x1);
        u32 xp[5];
#pragma unroll
        for (int i = 0; i < 5; ++i) xp[i] = pkv(x1[i] + (x1[i] - st1.x) * (y[i] * st1.y));
        const int row = s * 32 + ri;
        if (L == 0) {
            const u32 ro = (u32)row * (DM * 2);
            f32x2 x2[5];
#pragma unroll
            for (int i = 0; i < 5; ++i) x2[i] = up2(xp[i]);
            st8s(rs, (u32)lane * 8, W_X2 + ro, (v2u){xp[0], xp[1]});
            st8s(rs, (u32)lane * 8 + 512, W_X2 + ro, (v2u){xp[2], xp[3]});
            st4s(rs, (u32)lane * 4 + 1024, W_X2 + ro, xp[4]);
            const f32x2 st2 = ln_stats(x2);
            u32 hp[5];
#pragma unroll
            for (int i = 0; i < 5; ++i) hp[i] = pk2((x2[i] - st2.x) * st2.y);
            store_h(rs, W_H2, row, hp);
        } else {
            u16* o_ = p.out + (size_t)row * DM;
            *(v2u*)(o_ + 4 * lane) = (v2u){xp[0], xp[1]};
            *(v2u*)(o_ + 256 + 4 * lane) = (v2u){xp[2], xp[3]};
            *(u32*)(o_ + 512 + 2 * lane) = xp[4];
        }
        CFENCE();
        LS3(L == 0, 5, ri);
        fv = ld4(rs, fp);
        asm volatile("s_waitcnt vmcnt(0)" : "+v"(fv) :: "memory");
        st4s(rs, lane == 0 && L == 0 ? foff(F_H2 + ri) : OOB, 0, (u32)s + 1);
        if ((s & 7) == 6 && s < NSEG - 8) {
            if (s == 30) pre_poll(rs, 0, NCV, 5u, ep);
            if (s == 46) st8s(rs, (id == 1 && lane == 0) ? foff(F_EPOCH) : OOB, 0, (v2u){ep.x + 8u, ep.y});
            yfill((s >> 3) + 1);
        }
        if (ri == 0) LST(320 + L * 64 + s, rt32());
        LS2(L == 0, 224 + ri);
    }
}

constexpr float SCALE = 0.08838834764831845f, LOG2E = 1.4426950408889634f;
constexpr int C_K = 0, C_V = 8192, C_QX = 16384, C_MEM = 24576, C_Q = C_MEM + 10240, C_KM = C_Q + 2304, C_VM = C_KM + 2304, C_DUMP = C_VM + 2048,
              C_ZERO = C_DUMP + 1024, C_WL = C_ZERO + 128;
constexpr int NLF = 14;
static_assert(C_WL % 16 == 0 && C_WL + 4 * NLF * 2048 <= LDS_BYTES, "head lds");
#define CH_VIS 10
#define CH_H0 113
#define CH_W0 83
#define CH_MH 3
#define CH_MW 3
#define CH_REC 8
#define CH_PRE 9
#define CH_T0 190
#define CH_UP 4
#define CH_WX 6
#define WAIT_TXT(tmp, t4) "s_memrealtime vcc\n1:\n\ts_waitcnt lgkmcnt(0)\n\ts_sub_u32 " tmp ", " t4 ", vcc_lo\n\ts_memrealtime vcc\n\ts_cmpk_lt_u32 " tmp ", 400\n\ts_cbranch_scc1 1b\n\t" \
                          "s_add_u32 " tmp ", " tmp ", 4\n\ts_cselect_b32 " tmp ", " tmp ", 0\n2:\n\ts_sub_u32 " tmp ", " tmp ", 1\n\ts_cbranch_scc0 2b\n\t"
DEV void wait_until(u32 t) { u32 tmp; asm volatile(WAIT_TXT("%0", "%1") "s_waitcnt lgkmcnt(0)" : "=&s"(tmp) : "s"(uu(t - 4u)) : "vcc", "scc", "memory"); }
#define SMF_A(acc, a, b) asm volatile("v_smfmac_f32_16x16x64_bf16 %0, %1, %2, %3" : "+v"(acc) : "v"(a), "a"(b), "v"(idx))
#define SMF_V(acc, a, b) asm volatile("v_smfmac_f32_16x16x64_bf16 %0, %1, %2, %3" : "+v"(acc) : "v"(a), "v"(b), "v"(idx))
#define MF0(d, a, b) asm volatile("v_mfma_f32_16x16x32_bf16 %0, %1, %2, 0" : "=&v"(d) : "v"(a), "v"(b))
#define MF1(d, a, b) asm volatile("v_mfma_f32_16x16x32_bf16 %0, %1, %2, %0" : "+v"(d) : "v"(a), "v"(b))
#define MF0H(d, a, b) asm volatile("v_mfma_f32_16x16x16_bf16 %0, %1, %2, 0" : "=&v"(d) : "v"(a), "v"(b))
#define MF1H(d, a, b) asm volatile("v_mfma_f32_16x16x16_bf16 %0, %1, %2, %0" : "+v"(d) : "v"(a), "v"(b))
DEV v8u tr_frag(lptr p0, lptr p1, lptr p2, lptr p3) { return cat8(tr2(p0, p1), tr2(p2, p3)); }
constexpr int qslot(int ks) { return (ks & 1) && ks < 8 ? ks >> 1 : -1; }
constexpr int kslot(int ks) { return ks < 4 ? 4 + ks : -1; }
constexpr int vslot(int ks) { return ks >= 4 ? 4 + ks : -1; }
DEV u32 ring_map(u32 off, u32 half) { return ((off >> 13) << 14) + (half << 13) + (off & 8191u); }
#define ZRD(x, i) x = __builtin_bit_cast(f32x4, lds_r16(ZP + 16 * (i)))
#define VMAX(d, a, b) asm volatile("v_max_f32 %0, %1, %2" : "=v"(d) : "v"(a), "v"(b))
#define VMAX3(d, a, b, c_) asm volatile("v_max3_f32 %0, %1, %2, %3" : "=v"(d) : "v"(a), "v"(b), "v"(c_))
#define VEXP(d, a) asm volatile("v_exp_f32 %0, %1" : "=v"(d) : "v"(a))
#define VFMC(d, a, b) asm volatile("v_fmamk_f32 %0, %1, 0x3e0293ee, %2" : "=v"(d) : "v"(a), "v"(b))
#define SEL(d, a, b) asm volatile("v_cndmask_b32 %0, %1, %2, %3" : "=v"(d) : "v"(a), "v"(b), "s"(himask))
#define QPERM(d, a, q) asm volatile("v_mov_b32_dpp %0, %1 quad_perm:" q " row_mask:0xf bank_mask:0xf" : "=v"(d) : "v"(a))

#define HD_SMP 58
DEV void role_head(const Params& p, lptr lds, int L, int h, v2u ep) {
    const int tid = threadIdx.x, lane = tid & 63, w = uni(tid >> 6), g = lane >> 4, r = lane & 15;
    const rsrc_t rs = mk_rsrc(p.ws, W_END);
    v8u wq0[10], wq1[10], wk0[10], wk1[10], wv0[10], wv1[10];
    lptr WL = lds + C_WL + w * (NLF * 2048) + lane * 16;
    {
        constexpr int GS = 1056, CS = 48 * GS;
        const v4u sq = mk_srd(p.Wq, DM * NQ * 2), sk = mk_srd(p.Wk, DM * NQ * 2), sv = mk_srd(p.Wv, DM * NQ * 2);
        const u32 vo = (u32)(g * NQ + h * DH + 8 * (r ^ ((g & 1) << 3))) * 2u;
        auto fill = [&](int ch) {
#pragma unroll 1
            for (int i = 0; i < 4; ++i) {
                const u32 dst = uu((ch % 3) * CS + (4 * i + w) * GS), so = uu((ch * 64 + 16 * i + 4 * w) * (NQ * 2));
                DMAIN(sq, dst, vo, so); DMAIN(sk, dst + 16 * GS, vo, so); DMAIN(sv, dst + 32 * GS, vo, so);
            }
        };
        fill(0); fill(1); fill(2);
        const int q4 = r >> 2;
        lptr tb = lds + (4 * (g >> 1) + 2 * (q4 >> 1) + (g & 1)) * GS + (q4 & 1) * 256 + ((w * 64 + (r & 3) * 8) ^ ((q4 & 1) << 7));
        SFOR(ch, 10)
            VMW(ch < 8 ? 24 : 108 - 12 * ch);
            BAR();
            auto frag = [&](int m, int t) { lptr b = tb + (ch % 3) * CS + m * 16 * GS + t * 32; return tr_frag(b, b + 512, b + 8 * GS, b + 8 * GS + 512); };
            wq0[ch] = frag(0, 0); wq1[ch] = frag(0, 1); wk0[ch] = frag(1, 0); wk1[ch] = frag(1, 1); wv0[ch] = frag(2, 0); wv1[ch] = frag(2, 1);
            asm volatile("" : "+a"(wq0[ch]), "+a"(wk0[ch]), "+v"(wv0[ch]), "+v"(wv1[ch]));
            if constexpr (qslot(ch) < 0) asm volatile("" : "+a"(wq1[ch])); else asm volatile("" : "+v"(wq1[ch]));
            if constexpr (kslot(ch) < 0) asm volatile("" : "+a"(wk1[ch])); else asm volatile("" : "+v"(wk1[ch]));
            BAR();
            if constexpr (ch < 7) fill(ch + 3);
        SEND
#pragma unroll
        for (int ch = 0; ch < 10; ++ch) {
            if (qslot(ch) >= 0) { lds_w16(WL + qslot(ch) * 2048, wq1[ch].lo); lds_w16(WL + qslot(ch) * 2048 + 1024, wq1[ch].hi); }
            if (kslot(ch) >= 0) { lds_w16(WL + kslot(ch) * 2048, wk1[ch].lo); lds_w16(WL + kslot(ch) * 2048 + 1024, wk1[ch].hi); }
            if (vslot(ch) >= 0) { lds_w16(WL + vslot(ch) * 2048, wv1[ch].lo); lds_w16(WL + vslot(ch) * 2048 + 1024, wv1[ch].hi); }
        }
        CFENCE();
    }
    const u32 idx = (r & 2) ? 0xEEEEu : 0x4444u;
    const unsigned long long himask = 0xCCCCCCCCCCCCCCCCull;
    bool dead = false;
    if (h == 3) LST(266 + L, rt32());
    if (w == 0) LST(1240 + L * 32 + h, rt32());
    if (L == 1) {
        KvIn in;
        kvt_load(p, LN0 + 4 * h + w, in); kvt_make(rs, lds + w * 9216, LN0 + 4 * h + w, in);
        DRAIN(); BAR();
        pre_post(rs, 64 + h, 3u, ep);
    }
    const u32 pol0 = pol_wait(rs, ep);
    if (h == 3 && L == 0) LST(277, rt32());
    const u32 l16 = (u32)lane * 16u;
    const u32 mring = W_MRING + (u32)L * (MB / 4) + pol0 * 8192u, hint = mring + 4 * 32768u;
    const u32 fkh = lane < 3 ? foff(F_QKV + L * 96 + lane * 32 + h) : lane < 7 ? hint + (u32)(lane - 3) * 4u : OOB;
    u32 tm = 0;
    MK_SRD();
    u32 ksrc = uu(W_KQ + (u32)((L * NSEG - 1) * NH + h) * 16384u + w * 2048), vsrc = ksrc + (W_VO - W_KQ);
    u32 dk = uu(C_K + w * 2048), dv = dk + (C_V - C_K), dq = dk + (C_QX - C_K), dm = uu(C_MEM + w * 2048), dm2 = uu(w < 2 ? C_MEM + 8192 + w * 1024 : C_DUMP);
    asm volatile("" : "+s"(dk), "+s"(dv), "+s"(dq), "+s"(dm), "+s"(dm2));
    auto keys_wait = [&](u32 need) {
#pragma unroll 1
        for (int n = 0; !dead; ++n) {
            const bool beat = need + 2 * L > 2u;
            if (beat) wait_until(tm);
            const u32 hv = ld4(rs, lane == 7 ? foff(F_ERR) : fkh);
            if (beat) { tm = rdl(hv, 5); wait_until(rdl(hv, 3) - 30u); }
            if (((u32)__builtin_amdgcn_ballot_w64(ld4(rs, fkh) < need) & 7u) == 0u) break;
            dead = n > SPIN_CAP || rdl(hv, 7) != 0u;
            st4s(rs, dead ? foff(F_ERR) : OOB, 0, 1u);
        }
        CFENCE();
        DMA32(dk, l16, ksrc); DMA32(dv, l16, vsrc); DMA32(dq, l16, ksrc ^ 8192u);
    };
    lptr qw = lds + C_Q + (2 * g) * 272 + (32 * w + 2 * r) * 2;
    lptr qr = lds + C_Q + (2 * (r >> 2) + (r & 1)) * 272 + g * 16;
    lptr LB = lds + l16, VX = lds + C_V + w * 2048 + l16;
    if (tid >= 64 && tid < 70) { const u32 z = tid == 68 ? 0x3F803F80u : tid == 69 ? PSENT : 0u; lds_w16(lds + C_ZERO + (tid - 64) * 16, (v4u){z, z, z, z}); }
    u32 zaddr = 0; PIN(zaddr);
    lptr ZP = lds + zaddr + C_ZERO;
    const float CEXP = SCALE * LOG2E;
#define LF_RD(slot, hf) lf[slot][hf] = lds_r16(WL + (slot) * 2048 + (hf) * 1024)
    BAR();
    const u32 ofl = tid == 0 ? foff(F_O + L * 32 + h) : OOB;
    u32 oso = OOB + W_ORING + (u32)L * (MB / 2) + ring_map((u32)((r >> 3) * 32768 + h * 1024 + w * 256 + (g >> 1) * 128 + (r & 7) * 16), pol0);
    unsigned long long vis[8];
#pragma unroll
    for (int i = 0; i < 8; ++i) vis[i] = __builtin_amdgcn_ballot_w64(16 * (i >> 2) + 4 * g + (i & 3) <= 16 * (w & 1) + r);
    u32 missed = 0;
    for (int s = -1; s < NSEG;) {
        u32 sv = (u32)max(s, 0), ms = mring + (sv & 3u) * 32768u + w * 2048, ms2 = mring + (sv & 3u) * 32768u + 16384u + (w & 1) * 1024;
        asm volatile("" : "+s"(ms), "+s"(ms2)); PIN(sv);
        f32x4 aq0, aq1, ak0, ak1;
        v4u lf[NLF][2];
        ZRD(aq0, 0); ZRD(aq1, 1); ZRD(ak0, 2); ZRD(ak1, 3);
#pragma unroll
        for (int i = 0; i < 4; ++i) { LF_RD(i, 0); LF_RD(i, 1); }
        SCHED();
        v4u A[10];
#define HT(k) do {} while (0)
        v4u kx[8], qf[4], kmf[4], vx[2], ones, P;
        float fa, fb, fc, fd, mx, my, sx[4], t[4], tm0, tm1; u32 pa, pb, fl, e01, e23, pmm; unsigned long long nanq;
        f32x4 av0, av1, s0, s1, sm, oa, ob, rsum;
        v2u vm, pm;
#define LW(sl) cat8(lf[sl][0], lf[sl][1])
#define PQ(ks, tl) do { if constexpr (!(tl)) SMF_A(aq0, A[ks], wq0[ks]); else if constexpr (qslot(ks) < 0) SMF_A(aq1, A[ks], wq1[ks]); else { const v8u lw_ = LW(qslot(ks)); SMF_V(aq1, A[ks], lw_); } } while (0)
#define PK(ks, tl) do { if constexpr (!(tl)) SMF_A(ak0, A[ks], wk0[ks]); else if constexpr (kslot(ks) < 0) SMF_A(ak1, A[ks], wk1[ks]); else { const v8u lw_ = LW(kslot(ks)); SMF_V(ak1, A[ks], lw_); } } while (0)
#define PV(ks, tl) do { if constexpr (!(tl)) SMF_V(av0, A[ks], wv0[ks]); else if constexpr (vslot(ks) < 0) SMF_V(av1, A[ks], wv1[ks]); else { const v8u lw_ = LW(vslot(ks)); SMF_V(av1, A[ks], lw_); } } while (0)
#define KXR(i) kx[i] = lds_r16(LB + C_K + ((i) & 1) * 4096 + ((i) >> 1) * 1024)
#define XW(q_) do { *(LAS u32*)(q_) = pa; *(LAS u32*)((q_) + 272) = pb; } while (0)
        auto slot = [&](auto zc) __attribute__((always_inline)) {
            constexpr int z = decltype(zc)::value;
            if constexpr (z < 14) PQ(z >> 1, z & 1);
            else if constexpr (z < 17) PQ(z - 7, 0);
            else if constexpr (z < 20) PQ(z - 10, 1);
            else if constexpr (z < 33) PK((z - 20) >> 1, z & 1);
            else if constexpr (z < 35) { if constexpr (z == 33) MF0(s0, kx[0], qf[0]); else MF0(s1, kx[1], qf[0]); }
            else if constexpr (z < 41) { if constexpr (z & 1) MF1(s0, kx[z - 33], qf[(z - 33) >> 1]); else MF1(s1, kx[z - 33], qf[(z - 33) >> 1]); }
            else if constexpr (z == 41) PK(6, 1);
            else if constexpr (z < 45) PK(z - 35, 0);
            else if constexpr (z < 48) PK(z - 38, 1);
            else if constexpr (z < 58) PV(z - 48, 0);
            else if constexpr (z < 61) PV(z < 60 ? z - 54 : 0, 1);
            else if constexpr (z == 61) MF0(sm, kmf[0], qf[0]);
            else if constexpr (z < 65) MF1(sm, kmf[z - 61], qf[z - 61]);
            else if constexpr (z == 65) MF0(oa, vx[0], P);
            else if constexpr (z == 66) MF0(ob, vx[1], P);
            else PV(6, 1);
            if constexpr (z < 6) A[4 + z] = lds_r16(LB + C_MEM + (4 + z) * 1024);
            if constexpr (z == 6 || z == 7) LF_RD(4, z - 6);
            if constexpr (z == 8 || z == 9) LF_RD(5, z - 8);
            if constexpr (z == 12 || z == 13) LF_RD(6, z - 12);
            if constexpr (z == 16 || z == 17) KXR(z - 16);
            if constexpr (z == 18 || z == 19) LF_RD(7, z - 18);
            if constexpr (z >= 20 && z < 24) KXR(z - 18);
            if constexpr (z == 32 || z == 33) KXR(z - 26);
            if constexpr (z == 24) XW(qw);
            if constexpr (z == 25) ZRD(av0, 0);
            if constexpr (z == 26) ZRD(av1, 1);
            if constexpr (z == 27) asm volatile("s_waitcnt lgkmcnt(2)\n\ts_barrier" ::: "memory");
            if constexpr (z >= 28 && z < 32) qf[z - 28] = lds_r16(qr + (z - 28) * 64);
            if constexpr (z == HD_SMP) LD4A(fl, fkh, 0);
            if constexpr (z == 52) XW(qw + (C_KM - C_Q));
            if constexpr (z == 53 || z == 54) vx[z - 53] = lds_r16(VX + (z - 53) * 1024);
            if constexpr (z == 55) asm volatile("s_waitcnt lgkmcnt(2)\n\ts_barrier" ::: "memory");
            if constexpr (z >= 56 && z < 60) kmf[z - 56] = lds_r16(qr + (C_KM - C_Q) + (z - 56) * 64);
            if constexpr (z >= 48 && z < 52) LF_RD(8 + ((z - 48) >> 1), z & 1);
            if constexpr (z >= 60) LF_RD(10 + ((z - 60) >> 1), z & 1);
            SCHED();
            if constexpr (z == 19) HT(2);
            if constexpr (z == 32) HT(3);
            if constexpr (z == 47) HT(4);
            if constexpr (z == 64) HT(5);
            if constexpr (z == 20) VADD(fa, aq0[0], aq0[2]);
            if constexpr (z == 21) VADD(fb, aq0[1], aq0[3]);
            if constexpr (z == 22) { VADD(fc, aq1[0], aq1[2]); VADD(fd, aq1[1], aq1[3]); }
            if constexpr (z == 23) { asm volatile("v_cmp_u_f32 %0, %1, %2" : "=s"(nanq) : "v"(fa), "v"(fb)); VCVT(pa, fa, fc); VCVT(pb, fb, fd); }
            if constexpr (z == 39 || z == 40 || z == 41) NOPS(1);
            if constexpr (z == 42) VMAX3(mx, s0[0], s0[1], s0[2]);
            if constexpr (z == 43) { VMAX3(mx, mx, s0[3], s1[0]); VMAX3(my, s1[1], s1[2], s1[3]); }
            if constexpr (z == 44 || z == 46) { VMAX(fa, mx, my); VMAX(my, mx, my); mx = fa; }
            if constexpr (z == 45) { SEL(sx[0], s0[0], s1[0]); asm volatile("v_permlane16_swap_b32 %0, %1" : "+v"(mx), "+v"(my)); }
            if constexpr (z == 47) { SEL(sx[1], s0[1], s1[1]); asm volatile("v_permlane32_swap_b32 %0, %1" : "+v"(mx), "+v"(my)); }
            if constexpr (z == 48) asm volatile("v_max_f32 %0, %0, %1\n\tv_mul_f32 %0, 0xbe0293ee, %0" : "+v"(mx) : "v"(my));
            if constexpr (z == 49) { VADD(fa, ak0[0], ak0[2]); VADD(fb, ak0[1], ak0[3]); }
            if constexpr (z == 50) { VADD(fc, ak1[0], ak1[2]); VADD(fd, ak1[1], ak1[3]); }
            if constexpr (z == 51) { VCVT(pa, fa, fc); VCVT(pb, fb, fd); }
            if constexpr (z == 52) SEL(sx[2], s0[2], s1[2]);
            if constexpr (z == 53) { SEL(sx[3], s0[3], s1[3]); VFMC(t[0], sx[0], mx); }
            if constexpr (z == 54) { VFMC(t[1], sx[1], mx); VFMC(t[2], sx[2], mx); }
            if constexpr (z == 55) VFMC(t[3], sx[3], mx);
            if constexpr (z >= 56 && z < 60) VEXP(t[z - 56], t[z - 56]);
            if constexpr (z == 60) { VCVT(e01, t[0], t[1]); VCVT(e23, t[2], t[3]); }
            if constexpr (z == 61) { QPERM(P.x, e01, "[0,1,0,1]"); QPERM(P.z, e01, "[2,3,2,3]"); }
            if constexpr (z == 62) { QPERM(P.y, e23, "[0,1,0,1]"); QPERM(P.w, e23, "[2,3,2,3]"); }
            if constexpr (z == 63) { VADD(fa, av0[0], av0[2]); VADD(fb, av0[1], av0[3]); }
            if constexpr (z == 64) VCVT(vm.x, fa, fb);
            if constexpr (z == 67) { asm volatile("" :: "v"(sm)); VFMC(tm0, sm[0], mx); VFMC(tm1, sm[1], mx); }
            SCHED();
        };
        {
            u32 tmp;
            asm volatile("s_mov_b32 m0, %2\n\t" WAIT_TXT("%0", "%1") "buffer_load_dwordx4 %4, %5, %6 offen sc1 lds\n\tbuffer_load_dwordx4 %4, %5, %6 offen offset:1024 sc1 lds\n\t"
                         "s_mov_b32 m0, %3\n\ts_nop 0\n\tbuffer_load_dwordx4 %4, %5, %7 offen sc1 lds\n\ts_waitcnt vmcnt(0) lgkmcnt(0)\n\ts_barrier"
                         : "=&s"(tmp) : "s"(uu(tm - 4u)), "s"(dm), "s"(dm2), "v"(l16), "s"(srd), "s"(ms), "s"(ms2) : "vcc", "scc", "memory");
        }
        HT(1);
#pragma unroll
        for (int ks = 0; ks < 4; ++ks) A[ks] = lds_r16(LB + C_MEM + ks * 1024);
        SCHED();
        st4s(rs, ofl, 0, sv);
        SCHED();
        static_for<0, 24>(slot);
        if (__builtin_expect(nanq != 0ull, 0)) {
            bool miss = false;
#pragma unroll
            for (int i = 0; i < 10; ++i) miss |= A[i].x == PSENT || A[i].y == PSENT || A[i].z == PSENT || A[i].w == PSENT;
            nanq = missed < SPIN_CAP ? __builtin_amdgcn_ballot_w64(miss) : 0ull;
        }
        if (__builtin_expect(nanq == 0ull, 1)) {
        static_for<24, 68>(slot);
        PV(7, 1); ones = lds_r16(ZP + 64); SCHED();
        VMAX(fa, tm0, tm1);
        if (__builtin_expect(__builtin_amdgcn_ballot_w64(fa > 24.0f) != 0ull, 0)) {
            const float up = fmaxf(max32(max16(fmaxf(tm0, tm1))), 0.f);
            tm0 -= up; tm1 -= up;
#pragma unroll
            for (int i = 0; i < 4; ++i) t[i] = __builtin_amdgcn_exp2f(__builtin_fmaf(sx[i], CEXP, mx) - up);
            e01 = pk(t[0], t[1]); e23 = pk(t[2], t[3]);
            asm volatile("s_nop 1" : "+v"(e01), "+v"(e23));
            QPERM(P.x, e01, "[0,1,0,1]"); QPERM(P.z, e01, "[2,3,2,3]"); QPERM(P.y, e23, "[0,1,0,1]"); QPERM(P.w, e23, "[2,3,2,3]");
            NOPS(1);
            MF0(oa, vx[0], P); MF0(ob, vx[1], P);
            NOPS(7);
        }
        SCHED();
        PV(1, 1); VEXP(tm0, tm0); SEL(oa[0], oa[0], ob[0]); SCHED();
        PV(8, 1); VEXP(tm1, tm1); SEL(oa[1], oa[1], ob[1]); SCHED();
        PV(9, 1); VCVT(pmm, tm0, tm1); SEL(oa[2], oa[2], ob[2]); SCHED();
        PV(2, 1);
        asm volatile("v_cndmask_b32 %0, %2, 0, %3\n\tv_cndmask_b32 %1, 0, %2, %3" : "=&v"(pm.x), "=&v"(pm.y) : "v"(pmm), "s"(himask)); SCHED();
        PV(3, 1); SEL(oa[3], oa[3], ob[3]); SCHED();
        MF0(rsum, ones, P); NOPS(4);
        VADD(fc, av1[0], av1[2]); VADD(fd, av1[1], av1[3]);
        { const v2u ones2 = ones.lo; MF1H(rsum, ones2, pm); }
        VCVT(vm.y, fc, fd); NOPS(1);
        MF1H(oa, vm, pm);
        {
            float inv; v4u oc;
            asm volatile("s_nop 3\n\tv_rcp_f32 %0, %1\n\ts_nop 2" : "=v"(inv) : "v"(rsum[0]), "v"(rsum));
            PIN(oa);
            const f32x2 i2 = {inv, inv};
            const f32x2 u0 = oa.lo * i2, u1 = oa.hi * i2;
            asm volatile("v_cvt_pk_bf16_f32 %0, %4, %5\n\tv_cvt_pk_bf16_f32 %2, %4, %5\n\tv_cvt_pk_bf16_f32 %1, %6, %7\n\tv_cvt_pk_bf16_f32 %3, %6, %7\n\t"
                         "v_permlane16_swap_b32 %0, %2\n\ts_nop 0\n\tv_permlane16_swap_b32 %1, %3"
                         : "=&v"(oc.x), "=&v"(oc.y), "=&v"(oc.z), "=&v"(oc.w) : "v"(u0.x), "v"(u0.y), "v"(u1.x), "v"(u1.y));
            st16(rs, oso, oc);
        }
        SCHED();
        unsigned long long clk;
        asm volatile("s_memrealtime %0" : "=s"(clk));
        HT(6);
        v4u tq[4], tk[8], tv[2];
        f32x4 c0, c1, cm;
        u32 oo, kvo; bool kvok;
        u32 ln = l16; PIN(ln);
        lptr QX = lds + C_QX + (w & 1) * 4096 + ln, VO = lds + C_V + (w ^ 1) * 2048 + ln, VMX = lds + C_VM + w * 512 + (ln >> 1);
#pragma unroll
        for (int i = 0; i < 2; ++i) { tq[i] = lds_r16(QX + i * 1024); tk[2 * i] = lds_r16(LB + C_K + i * 1024); tk[2 * i + 1] = lds_r16(LB + C_K + 4096 + i * 1024); }
        *(LAS v2u*)VMX = vm;
        SCHED();
        SFOR(i, 12)
            if constexpr (i == 0) MF0(cm, kmf[0], tq[0]); else if constexpr (i == 1) MF0(c0, tk[0], tq[0]); else if constexpr (i == 2) MF0(c1, tk[1], tq[0]);
            else if constexpr (i % 3 == 0) MF1(cm, kmf[i / 3], tq[i / 3]); else if constexpr (i % 3 == 1) MF1(c0, tk[2 * (i / 3)], tq[i / 3]); else MF1(c1, tk[2 * (i / 3) + 1], tq[i / 3]);
            if constexpr (i == 0) { tq[2] = lds_r16(QX + 2048); tk[4] = lds_r16(LB + C_K + 2048); }
            if constexpr (i == 1) { tk[5] = lds_r16(LB + C_K + 6144); tq[3] = lds_r16(QX + 3072); }
            if constexpr (i == 2) { tk[6] = lds_r16(LB + C_K + 3072); tk[7] = lds_r16(LB + C_K + 7168); }
            if constexpr (i == 3) { tv[0] = lds_r16(VO); tv[1] = lds_r16(VO + 1024); }
            if constexpr (i == 4) { oo = ln + (ln & 0x300u) + ((vsrc ^ 8192u) + (u32)((w & 1) * 256) + ((u32)s & OOB)); PIN(oo); }
            if constexpr (i == 5) { ksrc += NH * 16384; vsrc += NH * 16384; PINS(ksrc); PINS(vsrc); }
            if constexpr (i == 9) { asm volatile("s_waitcnt vmcnt(1)" : "+v"(fl) :: "memory"); kvok = ((u32)__builtin_amdgcn_ballot_w64(fl < (u32)s + 2) & 7u) == 0u; }
            if constexpr (i == 10) { kvo = l16 | (kvok ? 0u : OOB); PIN(kvo); }
            SCHED();
        SEND
        asm volatile("s_waitcnt lgkmcnt(0)\n\ts_barrier" : "+s"(clk) :: "memory");
        const v2u tvm = *(LAS v2u*)(VMX + (512 - (w & 1) * 1024));
        const v4u pv = lds_r16(ZP + 80);
        DMA32(dk, kvo, ksrc);
        SCHED(); asm volatile("" :: "v"(cm));
        float u[10], m, m2;
        {
            float NEG = -1000000000.0f / SCALE; PIN(NEG);
#define MSK(d, a, i) asm volatile("v_cndmask_b32 %0, %1, %2, %3" : "=v"(d) : "v"(NEG), "v"(a), "s"(vis[i]))
#pragma unroll
            for (int i = 0; i < 4; ++i) MSK(u[i], c0[i], i);
#pragma unroll
            for (int i = 0; i < 4; ++i) MSK(u[4 + i], c1[i], 4 + i);
            asm volatile("v_max3_f32 %0, %2, %3, %10\n\tv_max3_f32 %1, %4, %5, %11\n\tv_max3_f32 %0, %0, %6, %7\n\tv_max3_f32 %1, %1, %8, %9\n\t"
                         "v_max_f32 %0, %0, %1\n\tv_mov_b32 %1, %0\n\ts_nop 1\n\tv_permlane16_swap_b32 %0, %1\n\tv_max_f32 %0, %0, %1\n\tv_mov_b32 %1, %0\n\ts_nop 1\n\t"
                         "v_permlane32_swap_b32 %0, %1\n\tv_max_f32 %0, %0, %1\n\tv_mul_f32 %0, 0xbe0293ee, %0"
                         : "=&v"(m), "=&v"(m2) : "v"(u[0]), "v"(u[1]), "v"(u[2]), "v"(u[3]), "v"(u[4]), "v"(u[5]), "v"(u[6]), "v"(u[7]), "v"(cm[0]), "v"(cm[1]));
        }
        DMA32(dv, kvo, vsrc);
        {
            v4u px; v2u p0, p1;
#define EX2(d, a, b) asm volatile("v_fmamk_f32 %0, %2, 0x3e0293ee, %4\n\tv_fmamk_f32 %1, %3, 0x3e0293ee, %4\n\tv_exp_f32 %0, %0\n\tv_exp_f32 %1, %1" : "=&v"(d.x), "=&v"(d.y) : "v"(a), "v"(b), "v"(m))
            f32x2 ex[5];
            EX2(ex[0], u[0], u[1]); EX2(ex[1], u[2], u[3]); EX2(ex[2], u[4], u[5]); EX2(ex[3], u[6], u[7]); EX2(ex[4], cm[0], cm[1]);
            asm volatile("v_cvt_pk_bf16_f32 %0, %8, %9\n\tv_cvt_pk_bf16_f32 %1, %10, %11\n\tv_cvt_pk_bf16_f32 %2, %12, %13\n\tv_cvt_pk_bf16_f32 %3, %14, %15\n\t"
                         "v_mov_b32 %5, 0\n\tv_mov_b32 %6, 0\n\tv_cvt_pk_bf16_f32 %4, %16, %17\n\tv_cvt_pk_bf16_f32 %7, %16, %17"
                         : "=&v"(px.x), "=&v"(px.y), "=&v"(px.z), "=&v"(px.w), "=&v"(p0.x), "=&v"(p0.y), "=&v"(p1.x), "=&v"(p1.y)
                         : "v"(ex[0].x), "v"(ex[0].y), "v"(ex[1].x), "v"(ex[1].y), "v"(ex[2].x), "v"(ex[2].y), "v"(ex[3].x), "v"(ex[3].y), "v"(ex[4].x), "v"(ex[4].y));
            f32x4 o[4], sum;
            const v2u ones2 = ones.lo;
            const u32 now = (u32)clk;
            u32 oz;
            SCHED();
            MF0(sum, ones, px); { const u32 q = ksrc ^ 8192u; DMA32(dq, kvo, q); } SCHED();
            MF0(o[0], vx[0], px);
            MF0(o[1], vx[1], px);
            MF0(o[2], tv[0], px); oz = (oso & ~(3u << 17)) | (u32)((s + 3) & 3) << 17; PIN(oz); SCHED();
            MF0(o[3], tv[1], px); oso = (oso & ~(3u << 17)) | (u32)((s + 1) & 3) << 17; PIN(oso); SCHED();
            MF1H(sum, ones2, p0);
            MF1H(o[0], vm, p0);
            MF1H(o[1], vm, p1); st16(rs, oz, pv); SCHED();
            MF1H(o[2], tvm, p0);
            MF1H(o[3], tvm, p1);
            float inv;
            asm volatile("s_nop 2\n\tv_rcp_f32 %0, %1\n\ts_nop 0" : "=v"(inv) : "v"(sum[0]), "v"(sum));
            asm volatile("" : "+v"(o[0]), "+v"(o[1]));
            st16(rs, oo, (v4u){pk2(o[0].lo * inv), pk2(o[0].hi * inv), pk2(o[1].lo * inv), pk2(o[1].hi * inv)});
            asm volatile("" : "+v"(o[2]), "+v"(o[3]));
            st16(rs, oo ^ 2048u, (v4u){pk2(o[2].lo * inv), pk2(o[2].hi * inv), pk2(o[3].lo * inv), pk2(o[3].hi * inv)});
            SCHED();
            LS2(L == 0 && w == 0, h);
            st4s(rs, (h == 0 && tid == 192) ? hint + 128u : OOB, 0, (u32)s << 16 | min(missed, 1u) << 14 | min(now - tm, 0x3FFFu));
            tm = rdl(fl, 5);
            missed = missed == SPIN_CAP ? missed : 0u;
            if (__builtin_expect((int)(rdl(fl, 3) - now) < CH_VIS - 4 || s < 1 - L, 0)) {
                const bool first = s == 0 && L == 0;
                if (s < 0) { oso -= (ln & 256u) ? 0u : OOB; tm = L ? tm : 0u; st4s(rs, tid == 0 ? hint + 4096u + (u32)h * 128u : OOB, 0, 0u); }
#pragma unroll 1
                for (int n = 0; n < (s < 0 ? 0 : first ? 200 : 8); ++n) {
                    CFENCE();
                    const u32 hv = ld4(rs, fkh);
                    tm = rdl(hv, first ? 6 : 5);
                    if (first ? tm != PSENT : (int)(rdl(hv, 3) - now) >= CH_VIS - 4) break;
                }
            }
        }
        if (__builtin_expect(!kvok, 0) && s + 1 < NSEG) {
            st4s(rs, ln == 0 ? hint + 4096u + (u32)h * 128u : OOB, 0, (u32)s + 1u);
            keys_wait((u32)s + 2u);
        }
        ++s;
        } else {
            u32 ln = l16; PIN(ln);
            const u32 hv = ld4(rs, ln == 112u ? foff(F_ERR) : fkh);
            tm = rdl(hv, 5);
            *(LAS u32*)(lds + C_DUMP + (w == 0 ? 0 : 64)) = rdl(hv, 7);
            BAR();
            missed = uu(*(LAS u32*)(lds + C_DUMP)) != 0u ? SPIN_CAP : missed + 1;
            st4s(rs, missed == SPIN_CAP ? foff(F_ERR) : OOB, 0, 1u);
            BAR();
        }
    }
    DRAIN(); BAR();
    st4s(rs, ofl, 0, NSEG);
}

#define WO_AH 1
DEV void role_wo(const Params& p, lptr lds, int j, v2u ep) {
    const int tid = threadIdx.x, lane = tid & 63, w = uni(tid >> 6), g = lane >> 4, r = lane & 15;
    const int rg = j & 1, cg = j >> 1;
    const rsrc_t rs = mk_rsrc(p.ws, W_END);
    v8u wo[8][5];
    {
        const v4u so_ = mk_srd(p.Wo, DM * NQ * 2);
        u32 vo[5];
#pragma unroll
        for (int i = 0; i < 5; ++i) { const int n = 64 * i + lane; vo[i] = (u32)((n / 5) * DM + cg * 40 + (n % 5) * 8) * 2u; }
        v4u junk = {};
        const u32 av = (u32)(j * 5120 + lane * 16);
        auto ahead = [&](int b) {
#pragma unroll
            for (int i = 0; i < 5; ++i) AHEAD(so_, av, uu((8 * w + b) * (DH * DM * 2) + i * 1024));
        };
        auto fill = [&](int b) {
            if (b + WO_AH < 8) ahead(b + WO_AH);
            const u32 dst = uu(w * 40960 + (b & 3) * 10240), src = uu((8 * w + b) * (DH * DM * 2));
#pragma unroll
            for (int i = 0; i < 10; ++i) DMAIN(so_, dst + i * 1024, vo[i % 5], src + (i / 5) * (64 * DM * 2));
        };
#pragma unroll 1
        for (int b = 0; b < WO_AH; ++b) ahead(b);
#pragma unroll 1
        for (int b = 0; b < 4; ++b) fill(b);
        const int q4 = r >> 2;
        lptr tb = lds + w * 40960 + (32 * (g >> 1) + 16 * (q4 >> 1) + 8 * ((r >> 1) & 1) + 4 * (g & 1) + (q4 & 1)) * 80 + (r & 1) * 8;
        SFOR(b, 8)
            VMW((b < 5 ? 30 : 70 - 10 * b) + (b + 1 + WO_AH < 8 ? 5 : 0) + (b + 2 + WO_AH < 8 ? 5 : 0) + (b + 3 + WO_AH < 8 ? 5 : 0));
            if constexpr (b + WO_AH == 7) PIN(junk);
#pragma unroll
            for (int t = 0; t < 5; ++t) {
                lptr q = tb + (b & 3) * 10240 + t * 16;
                wo[b][t] = tr_frag(q, q + 160, q + 64 * 80, q + 64 * 80 + 160);
                if (b * 5 + t < 32) asm volatile("" : "+a"(wo[b][t])); else asm volatile("" : "+v"(wo[b][t]));
            }
            CFENCE();
            if constexpr (b < 4) fill(b + 4);
        SEND
    }
    if (j == 0) LST(265, rt32());
    LST(1100 + j * 4 + w, rt32());
    const u32 idx = (r & 2) ? 0xEEEEu : 0x4444u;
    const u32 pol0 = pol_wait(rs, ep);
    BAR();
    const u32 l16 = (u32)lane * 16u;
    u32 oo[8];
#pragma unroll
    for (int i = 0; i < 8; ++i) oo[i] = uu(W_ORING + ring_map((u32)(rg * 32768 + (8 * w + i) * 1024), pol0));
    const u32 hint = W_MRING + 4 * 32768u + pol0 * 8192u;
    u32 hvo[2];
#pragma unroll
    for (int l = 0; l < 2; ++l) hvo[l] = lane < 2 ? hint + (u32)(l ^ 1) * (MB / 4) + (u32)lane * 4u : lane == 2 ? hint + (u32)l * (MB / 4) + 128u : lane == 3 ? foff(F_ERR) :
                 (lane >= 32 && j == 0 && w == 3) ? hint + (u32)l * (MB / 4) + (u32)lane * 128u : OOB;
    lptr xw = r < 8 ? lds + w * 640 + (2 * (g & 1) + (g >> 1)) * 32 + r * 4 : lds + 8192 + lane * 4;
    const int G = ((5 * cg) >> 1) + w, tt = 2 * G + ((lane >> 3) & 1) - 5 * cg;
    const bool pub = w < 3 && lane < 32 && tt >= 0 && tt < 5;
    lptr xr = lds + (pub ? tt * 128 + (2 * (lane >> 4) + ((lane >> 2) & 1)) * 32 + (lane & 3) * 8 : 0);
    const u32 mpo = pub ? W_MRING + ring_map((u32)(G * 256 + rg * 128 + lane * 4), pol0) : OOB;
    constexpr u32 PER0 = CH_W0 + CH_MW + CH_H0 + CH_MH + 2 * CH_VIS, DW0 = CH_W0 + CH_VIS + CH_MW + CH_REC;
    const bool lead = j == 0 && w == 3;
    u32 hso = OOB;
    MK_SRD();
    u32 t = 0, tn = 0, tm0 = 0, dry = 2;
    int sn[2] = {0, 0};
    u32 hav = 4 * CH_H0, wav = 4 * CH_W0, mh = 8 * CH_MH, mw = 8 * CH_MW, ok[2] = {0u, 0u}, dw[2] = {DW0, DW0};
    auto start = [&] {
        if (w == 0) LST(1310 + j, rt32());
        if (lead) {
#pragma unroll 1
            for (int n = 0; n < SPIN_CAP; ++n) {
                CFENCE();
                const u32 a = ld4(rs, foff(F_QKV + lane)), b = ld4(rs, lane < 32 ? foff(F_QKV + 64 + lane) : hvo[0]);
                if (all_(a != 0u && b != (lane < 32 ? 0u : PSENT))) break;
                __builtin_amdgcn_s_sleep(1);
            }
        }
        if (lead) {
            t = rt32() + CH_T0; tn = t + PER0 / 2 + CH_REC;
            st16(rs, lane < 2 ? hint + (u32)lane * (MB / 4) : OOB, (v4u){lane == 0 ? t : tn, PER0, (lane == 0 ? t : tn) + DW0, t + DW0});
        } else {
#pragma unroll 1
            for (int n = 0; n < SPIN_CAP; ++n) {
                CFENCE();
                const u32 hv = ld4(rs, hvo[1]);
                t = rdl(hv, 0);
                if (rdl(hv, 1) < 400u) break;
                __builtin_amdgcn_s_sleep(1);
            }
        }
        hso = (lead && lane == 0) ? hint : OOB; tm0 = t + DW0; dw[0] = dw[1] = DW0; dry = 0;
    };
    u32 zero = 0, hv = 0xFFFFu, pst = 1, pdur = 0, pmp = OOB;
    const u32 psent = PSENT;
    lptr sw = lds + 2560 + w * 4;
    bool stop = false;
    asm volatile("" : "+v"(zero), "+v"(sw));
    auto turn = [&](auto lc) __attribute__((always_inline)) {
        constexpr int L = decltype(lc)::value, M = L ^ 1;
        u32 now, done, rep, rd, lost, dh, dn, nt, za; v4u hw;
        const int sc = sn[L];
        auto books = [&](auto ic) __attribute__((always_inline)) {
            constexpr int i = decltype(ic)::value;
            if constexpr (i == 0) { rep = rdl(hv, 2); PINS(rep); }
            if constexpr (i == 1) { lost = (u32)(__builtin_amdgcn_ballot_w64(hv == (u32)sn[M]) >> 32); PINS(lost); }
            if constexpr (i == 2) { lost |= (rep >> 16) ^ (u32)sn[M]; PINS(lost); }
            if constexpr (i == 3) { lost |= ok[M] ^ 2u; rd = rep & 0xFFFFu; PINS(lost); PINS(rd); }
            if constexpr (i == 4) { lost = lost == 0u ? CH_UP * 8u : 0u; PINS(lost); }
            if constexpr (i == 5) { dn = wav >> 2; PINS(dn); pdur = min(pdur, dn + 8u); PINS(pdur); }
            if constexpr (i == 6) { pdur = (done & ok[M]) ? pdur : dn; PINS(pdur); }
            if constexpr (i == 7) { wav += pdur - dn; PINS(wav); }
            if constexpr (i == 8) { dh = hav >> 2; PINS(dh); nt = min(rd, dh + 8u); PINS(nt); }
            if constexpr (i == 9) { nt = rd < 0x3FFFu ? nt : dh; PINS(nt); }
            if constexpr (i == 10) { hav += nt - dh; PINS(hav); }
            if constexpr (i == 11) { mw = max(mw - 1u, CH_MW * 8u); PINS(mw); }
            if constexpr (i == 12) { mh = max(mh - 1u, CH_MH * 8u); PINS(mh); }
            if constexpr (i == 13) { rd = rd >> 14 == 1u ? lost : 0u; PINS(rd); }
            if constexpr (i == 14) { mw += rd; mh += lost - rd; PINS(mw); PINS(mh); }
            if constexpr (i == 15) { ok[M] = done ? 1u : (ok[M] & 1u) << 1; PINS(ok[M]); }
            if constexpr (i == 16) { dh = (2u * hav + mh) / 8u + (sn[1] < 3 ? CH_VIS + CH_PRE : CH_VIS); PINS(dh); }
            if constexpr (i == 17) { dn = (2u * wav + mw) / 8u + CH_VIS; PINS(dn); }
            if constexpr (i == 18) { nt = dw[L] + dh; PINS(nt); }
            if constexpr (i == 19) { dh += dn; dw[L] = dn; sn[M] += (int)done; PINS(dh); PINS(sn[M]); }
            if constexpr (i == 20) { rd = tn - t + (dh >> 1); PINS(rd); }
            if constexpr (i == 21) { rd = sn[M] + 1 < NSEG ? rd : 0u; PINS(rd); }
            if constexpr (i == 22) { nt = t + (u32)max((int)nt, (int)rd); PINS(nt); }
            if constexpr (i == 23) { hw.x = nt; hw.y = dh; PIN(hw.x); PIN(hw.y); }
            if constexpr (i == 24) { hw.z = nt + dn; hw.w = tm0; PIN(hw); }
            if constexpr (i == 25) st16(rs, hso + L * (MB / 4), hw);
        };
        if (__builtin_expect(sc + 1 < NSEG, 1)) {
            u32 vo = l16 + (L * (MB / 2) + (((u32)sc + dry) & 3u) * 131072u), mp, hn;
            PIN(vo);
#define WT(k) do {} while (0)
            WT(0);
            v4u d[8];
            {
                u32 tmp;
                asm volatile(WAIT_TXT("%1", "%2") "buffer_load_dwordx4 %0, %3, %4, %5 offen sc1\n\ts_waitcnt lgkmcnt(0)"
                             : "=&v"(d[0]), "=&s"(tmp) : "s"(uu(t - 4u)), "v"(vo), "s"(srd), "s"(oo[0]) : "vcc", "scc", "memory");
            }
            SFOR(i, 7)
                asm volatile("buffer_load_dwordx4 %0, %1, %2, %3 offen sc1" : "=v"(d[i + 1]) : "v"(vo), "s"(srd), "s"(oo[i + 1]) : "memory");
                SCHED();
                if constexpr (i == 0) { done = uu(pst) ^ 1u; PINS(done); }
                static_for<3 * i, 3 * i + 3>(books);
                SCHED();
            SEND
            hn = ld4(rs, hvo[L]);
            SCHED();
            WT(1);
            static_for<21, 26>(books);
            za = done ? pmp : OOB;
            st4s(rs, za, 0, psent);
            *(LAS u32*)(sw + L * 4096) = zero;
            f32x4 acc[5] = {};
            mp = mpo + (L * (MB / 4) + (u32)((sc + 1) & 3) * 32768u); pmp = mpo + (L * (MB / 4) + (u32)(sc & 3) * 32768u);
            asm volatile("" : "+v"(acc[0]), "+v"(acc[1]), "+v"(acc[2]), "+v"(acc[3]), "+v"(acc[4]), "+v"(mp), "+v"(pmp));
            SCHED();
            WT(2);
            float f0[5], f1[5]; unsigned long long bad;
#define WSW(o_) asm volatile("v_permlane32_swap_b32 %0, %1" : "+v"(f0[o_]), "+v"(f1[o_]))
#define WAD(o_) asm volatile("v_add_f32_dpp %0, %1, %0 row_ror:8 row_mask:0xf bank_mask:0xf" : "+v"(f0[o_]) : "v"(f1[o_]))
#define WFO(o_) do { VADD(f0[o_], acc[o_][0], acc[o_][2]); VADD(f1[o_], acc[o_][1], acc[o_][3]); } while (0)
#define WWR(o_) *(LAS float*)(xw + L * 4096 + (o_) * 128) = f0[o_]
            asm volatile("s_waitcnt vmcnt(10)" : "+v"(d[0]));
            if (__builtin_expect(__builtin_amdgcn_ballot_w64(d[0].x == PSENT + dry) != 0ull, 0)) {
                asm volatile("s_waitcnt vmcnt(3)" : "+v"(d[1]), "+v"(d[2]), "+v"(d[3]), "+v"(d[4]), "+v"(d[5]), "+v"(d[6]), "+v"(d[7]));
#pragma unroll
                for (int i = 0; i < 5; ++i) *(LAS u32*)(xw + L * 4096 + i * 128) = psent;
                *(LAS u32*)(sw + L * 4096) = 1u;
            } else {
            SFOR(i, 40)
                constexpr int pc = i < 30 ? i / 5 : 6 + (i & 1), oc = i < 30 ? i % 5 : (i - 30) >> 1;
                if constexpr (oc == 0 && pc > 0) asm volatile("s_waitcnt vmcnt(%1)" : "+v"(d[pc]) : "i"(10 - pc));
                if constexpr (pc * 5 + oc < 32) SMF_A(acc[oc], d[pc], wo[pc][oc]); else SMF_V(acc[oc], d[pc], wo[pc][oc]);
                if constexpr (i == 0) WT(3);
                if constexpr (i == 20) WT(4);
                if constexpr (i == 31 || i == 33) NOPS(1);
                if constexpr (i == 32) NOPS(0);
                if constexpr (i == 34) WFO(0);
                if constexpr (i == 35) { asm volatile("v_cmp_u_f32 %0, %1, %2" : "=s"(bad) : "v"(f0[0]), "v"(f1[0])); WSW(0); }
                if constexpr (i == 36) WFO(1);
                if constexpr (i == 37) { WAD(0); WSW(1); }
                if constexpr (i == 38) { WWR(0); SCHED(); WFO(2); }
                if constexpr (i == 39) { WAD(1); WSW(2); }
                SCHED();
            SEND
            WT(5);
            WWR(1); SCHED(); WFO(3); WAD(2); WWR(2); SCHED(); WSW(3);
            if (__builtin_expect(bad != 0ull, 0)) {
                bool miss = false;
#pragma unroll
                for (int i = 0; i < 8; ++i) miss |= d[i].x == PSENT;
                if (all_(!miss)) {
#pragma unroll
                    for (int i = 0; i < 8; ++i) miss |= d[i].y == PSENT || d[i].z == PSENT || d[i].w == PSENT;
                }
                *(LAS u32*)(sw + L * 4096) = all_(!miss) ? 0u : 1u;
            }
            WFO(4); WAD(3); WWR(3); SCHED();
            asm volatile("v_permlane32_swap_b32 %0, %1\n\ts_nop 1\n\tv_add_f32_dpp %0, %1, %0 row_ror:8 row_mask:0xf bank_mask:0xf" : "+v"(f0[4]), "+v"(f1[4]));
            WWR(4);
            }
            WT(6);
            f32x2 a, b, c, e; u32 cw, mq; v4u sv;
            asm volatile("s_waitcnt lgkmcnt(0)\n\ts_barrier\n\tds_read_b64 %0, %5 offset:%7\n\tds_read_b64 %1, %5 offset:%7+640\n\tds_read_b64 %2, %5 offset:%7+1280\n\tds_read_b64 %3, %5 offset:%7+1920\n\t"
                         "ds_read_b128 %4, %6 offset:%7+2560\n\t"
                         "s_waitcnt lgkmcnt(3)\n\tv_pk_add_f32 %0, %0, %1\n\ts_waitcnt lgkmcnt(1)\n\tv_pk_add_f32 %2, %2, %3\n\ts_waitcnt lgkmcnt(0)\n\tv_pk_add_f32 %0, %0, %2"
                         : "=&v"(a), "=&v"(b), "=&v"(c), "=&v"(e), "=&v"(sv) : "v"(xr), "v"(zero), "i"(L * 4096) : "memory");
            asm volatile("v_cmp_u_f32 vcc, %2, %3\n\tv_cvt_pk_bf16_f32 %0, %2, %3\n\tv_cndmask_b32 %1, %4, %5, vcc" : "=&v"(cw), "=&v"(mq) : "v"(a.x), "v"(a.y), "v"(mp), "v"(OOB) : "vcc");
            st4s(rs, mq, 0, cw);
            SCHED();
            now = rt32();
            PIN(hn); hv = hn;
            WT(7);
            pst = sv.x | sv.y | sv.z | sv.w;
            if (__builtin_expect(__builtin_amdgcn_ballot_w64(mq != mp && pst == 0u) != 0ull, 0)) st4s(rs, mp, 0, cw);
        } else {
            done = uu(pst) ^ 1u;
            st4s(rs, done ? pmp : OOB, 0, psent);
            static_for<0, 25>(books);
            pst = 1;
            hv = ld4(rs, hvo[L]);
            PIN(hv);
            now = rt32();
        }
        pdur = now - t;
        const u32 per = rdl(hv, 1), th = rdl(hv, 0);
        t = lead ? tn : (int)(th - now) < -(int)(per >> 1) ? th + per : th;
        tn = nt;
        dw[M] += max((int)(now + CH_WX - t), 0);
        stop = stop || rdl(hv, 3) != 0u;
    };
    for (int k = 0; k < 2 * SPIN_CAP && !stop && (sn[0] + 1 < NSEG || sn[1] + 1 < NSEG); ++k) {
        if (k == 1) start();
        asm volatile("" : "+v"(pdur), "+v"(dw[0]), "+v"(dw[1])); pdur = uu(pdur); dw[0] = uu(dw[0]); dw[1] = uu(dw[1]);
        turn(IC<0>{}); turn(IC<1>{});
    }
    if (sn[0] + 1 < NSEG || sn[1] + 1 < NSEG) st4s(rs, tid == 0 ? foff(F_ERR) : OOB, 0, 1u);
}

__global__ void __launch_bounds__(256, 1) k_main(Params p) {
    lptr lds = (lptr)(u32)16 - 16;
    if (blockIdx.x == 0) LST(256, rt32());
    unsigned long long pc; int len;
    asm volatile("s_getpc_b64 %0\n.Lk_main_here:\n\ts_mov_b32 %1, .Lk_end_here-.Lk_main_here" : "=s"(pc), "=s"(len));
    const rsrc_t rs = mk_rsrc(p.ws, W_END);
    const v2u e0 = ld8s(rs, threadIdx.x == 0 ? foff(F_EPOCH) : OOB, 0);
    const v4u code = __builtin_amdgcn_raw_buffer_load_b128(mk_rsrc((const void*)(pc & ~15ull), (u32)max(len, 0)), (((blockIdx.x >> 3) & 31u) * 256u + threadIdx.x) * 16u, 0, 0);
    CFENCE();
    auto conv_id = [](int role) {
        const int x = role >> 5, i = role & 31, no = x < 5 ? 5 : 10, og = x < 5 ? x - 3 : 2 * (x - 4) + (i >= 5), r = (x - 3) * 5 + i - no, q = r / 3;
        return x < 3 || i >= no + 5 || r >= 24 ? NCV : i < no ? ((og & 1) ? (og >> 1) * 5 + i % 5 : NCV) : r % 3 == 0 ? 20 + (q & 1) * 20 + (q >> 1) : 16 + (r % 3) * 8 + q;
    };
    int role = (int)(((u32)__builtin_amdgcn_s_getreg(20 | (3 << 11)) & 7u) * 32u + (blockIdx.x >> 3));
    Early e;
    v4u rv;
#pragma unroll 1
    for (int n = 0;; ++n) {
        conv_early(p, rs, conv_id(role), e);
        if (n) break;
        CFENCE();
        if (threadIdx.x == 0) {
            GAS unsigned long long* slots = (GAS unsigned long long*)(p.ws + W_CTRL + F_SLOT * 128);
            const unsigned long long tag = (unsigned long long)e0.y << 32 | (e0.x + 1u);
            u32 mine = (u32)role;
#pragma unroll 1
            for (int k = 0; k < 256 && __hip_atomic_exchange(slots + mine * 16, tag, __ATOMIC_RELAXED, __HIP_MEMORY_SCOPE_AGENT) == tag; ++k) mine = (mine + 1u) & 255u;
            lds_w16(lds, (v4u){mine, e0.x, e0.y, 0u});
        }
        BAR();
        rv = lds_r16(lds);
        BAR();
        if (uni((int)rv.x) == role) break;
        role = uni((int)rv.x);
    }
    const int x = role >> 5, i = role & 31, w = uni(threadIdx.x >> 6), no = x < 5 ? 5 : 10, nr = x < 7 ? 5 : 4, j = conv_id(role);
    const v2u ep = {uu(rv.y), uu(rv.z)};
    asm volatile("" :: "v"(code));
    if (x < 2) { role_head(p, lds, i >> 4, x * 16 + (i & 15), ep); __builtin_amdgcn_endpgm(); }
    if (x == 2) { role_wo(p, lds, i, ep); __builtin_amdgcn_endpgm(); }
    if (i >= no + nr) { role_qkv(p, lds, (x < 5 ? (x - 3) * 22 : 44 + (x - 5) * 17) + i - no - nr, ep); __builtin_amdgcn_endpgm(); }
    const bool cv = j >= NPRE || (j >= 20 && j < 24);
    if (j < NCV) conv_stage(p, rs, lds, j, (j & 3) * 2 + (j >= NPRE), cv ? 40 : 0, ep, e);
    if (i >= no) {
        if (!cv) role_row(p, lds, (j - 24) * 4 + w, ep);
        __builtin_amdgcn_endpgm();
    }
    const int og = x < 5 ? x - 3 : 2 * (x - 4) + (i >= 5);
    role_out(p, lds, og & 1, og >> 1, i % 5, ep);
}
__global__ void k_end() { asm volatile(".Lk_end_here:"); }

extern "C" void kernel_launch(void* const* d_in, const int* in_sizes, int n_in, void* d_out, int out_size, void* d_ws, size_t ws_size,
                              hipStream_t stream) {
    Params p;
    p.x = (const u16*)d_in[0]; p.mem0 = (const u16*)d_in[1]; p.Wq = (const u16*)d_in[2]; p.Wk = (const u16*)d_in[3];
    p.Wv = (const u16*)d_in[4]; p.Wo = (const u16*)d_in[5];
    p.bk = (const u16*)d_in[8]; p.bv = (const u16*)d_in[9]; p.shift = (const u16*)d_in[10]; p.ssmk = (const u16*)d_in[11]; p.ssmD = (const u16*)d_in[12];
    p.out = (u16*)d_out;
    p.ws = (unsigned char*)(((size_t)d_ws + 2 * MB - 1) & ~(size_t)(2 * MB - 1));
    (void)hipFuncSetAttribute((const void*)k_main, hipFuncAttributeMaxDynamicSharedMemorySize, LDS_BYTES);
    hipLaunchKernelGGL(k_main, dim3(256), dim3(256), LDS_BYTES, stream, p);
}
```

```cpp
#include <hip/hip_runtime.h>
#include <hip/hip_bf16.h>

typedef unsigned short u16;
typedef unsigned int u32;
typedef u32 v2u __attribute__((ext_vector_type(2)));
typedef u32 v4u __attribute__((ext_vector_type(4)));
typedef u32 v8u __attribute__((ext_vector_type(8)));
typedef float f32x2 __attribute__((ext_vector_type(2)));
typedef float f32x4 __attribute__((ext_vector_type(4)));
typedef float f32x16 __attribute__((ext_vector_type(16)));
typedef __bf16 bf16x2 __attribute__((ext_vector_type(2)));
typedef __bf16 bf16x8 __attribute__((ext_vector_type(8)));
typedef short s16x4 __attribute__((ext_vector_type(4)));
#define LAS __attribute__((address_space(3)))
#define GAS __attribute__((address_space(1)))
typedef LAS unsigned char* lptr;
#define DEV __device__ __forceinline__

constexpr int SEQ = 2048, DM = 640, NH = 32, DH = 128, NSEG = 64, NQ = NH * DH;

constexpr u32 MB = 1u << 20;
constexpr u32 W_ORING = 0;
constexpr u32 W_MRING = MB;
constexpr u32 W_PROBE = 2 * MB - 65536;
constexpr u32 W_CTRL = 4 * MB;
constexpr u32 CTRL_BYTES = 128 * 1024;
constexpr u32 ACT = SEQ * DM * 2;
constexpr u32 W_H1 = 5 * MB, W_Y = W_H1 + ACT, W_H2 = W_Y + ACT, W_X2 = W_H2 + ACT;
constexpr u32 W_KQ = 16 * MB;
constexpr u32 W_VO = W_KQ + 64 * MB;
constexpr u32 W_P = W_VO + 64 * MB;
constexpr u32 P_ITEM = 32 * 4 * DM * 4;
constexpr u32 W_KVT = W_P + 2 * NSEG * P_ITEM;
constexpr u32 W_SP = W_KVT + ACT;
constexpr u32 W_END = W_KVT + 3 * MB;
static_assert((size_t)W_END + 2 * MB <= 256ull * MB, "workspace");
constexpr u32 F_ERR = 0, F_QKV = 16, F_O = F_QKV + 192, F_P = F_O + 64, F_H2 = F_P + 64, F_CLR = F_H2 + 32,
              F_EPOCH = F_CLR + 32, F_SLOT = F_EPOCH + 1, F_PRE = F_SLOT + 256, F_N = F_PRE + 192;
constexpr int NCV = 44, NPRE = 40;
static_assert(F_N * 128 <= CTRL_BYTES, "ctrl");
DEV u32 foff(u32 idx) { return W_CTRL + idx * 128u; }

constexpr u32 OOB = 0x80000000u;
constexpr u32 PSENT = 0x7F817F81u;
constexpr int SPIN_CAP = 40000;
constexpr int LDS_BYTES = 160 * 1024;

#define LS3(cond, slot, idx) do {} while (0)
#define LST(idx, v) do {} while (0)
#define LS2(cond, idx) do {} while (0)
struct Params {
    const u16 *x, *mem0, *Wq, *Wk, *Wv, *Wo, *bk, *bv, *shift, *ssmk, *ssmD;
    u16* out;
    unsigned char* ws;
};

typedef __amdgpu_buffer_rsrc_t rsrc_t;
DEV rsrc_t mk_rsrc(const void* p, u32 bytes) { return __builtin_amdgcn_make_buffer_rsrc((void*)p, 0, bytes, 0x00020000); }
DEV v4u ld16s(rsrc_t r, u32 off, u32 so) { return __builtin_amdgcn_raw_buffer_load_b128(r, off, so, 16); }
DEV v2u ld8s(rsrc_t r, u32 off, u32 so) { return __builtin_amdgcn_raw_buffer_load_b64(r, off, so, 16); }
DEV v4u ldin16(rsrc_t r, u32 off, u32 so) { return __builtin_amdgcn_raw_buffer_load_b128(r, off, so, 2); }
DEV v2u ldin8(rsrc_t r, u32 off, u32 so) { return __builtin_amdgcn_raw_buffer_load_b64(r, off, so, 2); }
DEV u32 ld4(rsrc_t r, u32 off) { return __builtin_amdgcn_raw_buffer_load_b32(r, off, 0, 16); }
DEV void st16(rsrc_t r, u32 off, v4u v) { __builtin_amdgcn_raw_buffer_store_b128(v, r, off, 0, 16); }
DEV void st8s(rsrc_t r, u32 off, u32 so, v2u v) { __builtin_amdgcn_raw_buffer_store_b64(v, r, off, so, 16); }
DEV void st4s(rsrc_t r, u32 off, u32 so, u32 v) { __builtin_amdgcn_raw_buffer_store_b32(v, r, off, so, 16); }
#define DRAIN() asm volatile("s_waitcnt vmcnt(0)" ::: "memory")
#define VMW(n) asm volatile("s_waitcnt vmcnt(%0)" :: "i"(n) : "memory")
#define BAR() asm volatile("s_waitcnt lgkmcnt(0)\n\ts_barrier" ::: "memory")
#define SCHED() __builtin_amdgcn_sched_barrier(0)
#define CFENCE() asm volatile("" ::: "memory")
#define NOPS(n) asm volatile("s_nop %0" :: "i"(n))
#define PIN(x) asm volatile("" : "+v"(x))
#define PINS(x) asm volatile("" : "+s"(x))

template <int V> struct IC { static constexpr int value = V; };
template <int I, int N, class F> DEV void static_for(F&& f) { if constexpr (I < N) { f(IC<I>{}); static_for<I + 1, N>(f); } }
#define SFOR(var, n) static_for<0, n>([&](auto ic_) __attribute__((always_inline)) { constexpr int var = decltype(ic_)::value;
#define SEND });

DEV u32 pk(float a, float b) { bf16x2 v = {(__bf16)a, (__bf16)b}; return __builtin_bit_cast(u32, v); }
DEV float lo_f(u32 u) { return __builtin_bit_cast(float, u << 16); }
DEV float hi_f(u32 u) { return __builtin_bit_cast(float, u & 0xffff0000u); }
DEV f32x2 up2(u32 u) { return (f32x2){lo_f(u), hi_f(u)}; }
DEV u32 pk2(f32x2 v) { return pk(v.x, v.y); }
DEV float rbf(float a) { return (float)(__bf16)a; }
DEV v4u lds_r16(lptr p) { return *(LAS v4u*)p; }
DEV void lds_w16(lptr p, v4u v) { *(LAS v4u*)p = v; }
DEV v2u lds_tr(lptr p) { return __builtin_bit_cast(v2u, __builtin_amdgcn_ds_read_tr16_b64_v4i16((LAS s16x4*)p)); }
DEV v4u tr2(lptr p0, lptr p1) { const v2u a = lds_tr(p0), b = lds_tr(p1); return (v4u){a.x, a.y, b.x, b.y}; }
DEV v8u cat8(v4u a, v4u b) { return __builtin_shufflevector(a, b, 0, 1, 2, 3, 4, 5, 6, 7); }
DEV f32x4 mfma16(v4u a, v4u b, f32x4 c) { return __builtin_amdgcn_mfma_f32_16x16x32_bf16(__builtin_bit_cast(bf16x8, a), __builtin_bit_cast(bf16x8, b), c, 0, 0, 0); }
DEV f32x16 mfma32(v4u a, v4u b, f32x16 c) { return __builtin_amdgcn_mfma_f32_32x32x16_bf16(__builtin_bit_cast(bf16x8, a), __builtin_bit_cast(bf16x8, b), c, 0, 0, 0); }

DEV float f_(u32 u) { return __builtin_bit_cast(float, u); }
DEV u32 u_(float f) { return __builtin_bit_cast(u32, f); }
template <int CTRL> DEV float dpp(float v) { return f_((u32)__builtin_amdgcn_update_dpp(0, (int)u_(v), CTRL, 0xf, 0xf, false)); }
DEV float max16(float v) { auto r = __builtin_amdgcn_permlane16_swap(u_(v), u_(v), false, false); return fmaxf(f_(r[0]), f_(r[1])); }
DEV float max32(float v) { auto r = __builtin_amdgcn_permlane32_swap(u_(v), u_(v), false, false); return fmaxf(f_(r[0]), f_(r[1])); }
DEV float add16(float v) { auto r = __builtin_amdgcn_permlane16_swap(u_(v), u_(v), false, false); return f_(r[0]) + f_(r[1]); }
DEV float add32(float v) { auto r = __builtin_amdgcn_permlane32_swap(u_(v), u_(v), false, false); return f_(r[0]) + f_(r[1]); }
DEV float wave_sum(float v) {
    v += dpp<0xB1>(v);
    v += dpp<0x4E>(v);
    v += dpp<0x124>(v);
    v += dpp<0x128>(v);
    return add32(add16(v));
}
DEV bool all_(bool c) { return __builtin_amdgcn_ballot_w64(!c) == 0ull; }
DEV int uni(int v) { return __builtin_amdgcn_readfirstlane(v); }
DEV u32 uu(u32 v) { return (u32)__builtin_amdgcn_readfirstlane((int)v); }
DEV u32 rdl(u32 v, int l) { return (u32)__builtin_amdgcn_readlane((int)v, l); }
DEV u32 rt32() { return (u32)__builtin_amdgcn_s_memrealtime(); }

#define WSL 2
DEV void wait_flags(rsrc_t rs, u32 off, u32 want, bool& dead) {
    if (dead) return;
#pragma unroll 1
    for (int n = 0;; ++n) {
        CFENCE();
        const u32 v = ld4(rs, off);
        if (all_(off >= OOB || v >= want)) break;
        __builtin_amdgcn_s_sleep(WSL);
        if ((n & 63) == 63) { if (uni((int)ld4(rs, foff(F_ERR))) != 0) { dead = true; break; } }
        if (n > SPIN_CAP) { st4s(rs, foff(F_ERR), 0, 1u); dead = true; break; }
    }
    CFENCE();
}

DEV void pre_post(rsrc_t rs, int idx, u32 stage, v2u ep) { st8s(rs, threadIdx.x == 0 ? foff(F_PRE + idx) : OOB, 0, (v2u){ep.x + stage, ep.y}); }
DEV void pre_poll(rsrc_t rs, int first, int n, u32 stage, v2u ep) {
    const int lane = threadIdx.x & 63;
#pragma unroll 1
    for (int it = 0; it < SPIN_CAP; ++it) {
        CFENCE();
        const v2u a = ld8s(rs, foff(F_PRE + first + min(lane, n - 1)), 0), b = ld8s(rs, foff(F_PRE + first + min(lane + 64, n - 1)), 0);
        if (all_(a.y == ep.y && b.y == ep.y && max(a.x - ep.x - stage, b.x - ep.x - stage) < 8u - stage)) break;
        __builtin_amdgcn_s_sleep(2);
    }
    CFENCE();
}
DEV void pre_wait(rsrc_t rs, int first, int n, u32 stage, v2u ep) {
    if (uni(threadIdx.x >> 6) == 0) pre_poll(rs, first, n, stage, ep);
    BAR();
}
DEV u32 pol_wait(rsrc_t rs, v2u ep) {
    const int lane = threadIdx.x & 63;
    v4u a;
#pragma unroll 1
    for (int it = 0; it < SPIN_CAP; ++it) {
        CFENCE();
        a = ld16s(rs, foff(F_PRE + (lane < NPRE || lane >= 48 ? lane : 0)), 0);
        if (all_(a.y == ep.y && a.x - ep.x - 1u < 7u)) break;
        __builtin_amdgcn_s_sleep(2);
    }
    CFENCE();
    return uu(wave_sum(lane >= 48 ? (float)(int)a.z : 0.f) >= 0.f ? 1u : 0u);
}
#define PROBE_T 1000
#define NPROBE 32
DEV u32 pol_probe(const Params& p, rsrc_t rs, int id) {
    const int lane = threadIdx.x & 63;
    const u32 off0 = W_PROBE + (u32)(id >> 2) * 16384u + (u32)(id & 3) * 128u;
    const u32 tend = rt32() + PROBE_T;
    st4s(rs, lane < 32 ? off0 + (u32)lane * 512 : OOB, 0, 0u); DRAIN();
    int calm = 0;
    int d[2] = {1 << 16, 1 << 16};
#pragma unroll 1
    for (int q = 0; q < NPROBE && ((q & 1) || (calm < 6 && (int)(rt32() - tend) < 0)); ++q) {
        const u32 half = (u32)(q ^ (q >> 1) ^ id) & 1u;
        const unsigned long long t0 = __builtin_amdgcn_s_memtime();
        const u32 v = ld4(rs, lane == 0 ? off0 + half * 8192 + (u32)(q >> 1) * 512 : OOB);
        asm volatile("s_waitcnt vmcnt(0)" :: "v"(v) : "memory");
        const int dt = (int)(__builtin_amdgcn_s_memtime() - t0);
        if (half) d[1] = min(d[1], dt); else d[0] = min(d[0], dt);
        calm += dt < 1100;
    }
    const u32 xcc = (u32)__builtin_amdgcn_s_getreg(20 | (3 << 11)) & 0xfu;
    return (u32)min(max((d[0] - d[1]) * ((xcc & 4u) ? -1 : 1), -250), 250);
}
DEV f32x2 ln_stats(const f32x2 (&x)[5]) {
    const float c = f_(uu(u_(x[0].x)));
    f32x2 d[5];
#pragma unroll
    for (int i = 0; i < 5; ++i) d[i] = x[i] - c;
    const f32x2 s2 = (d[0] + d[1]) + (d[2] + d[3]) + d[4], q2 = (d[0] * d[0] + d[1] * d[1]) + (d[2] * d[2] + d[3] * d[3] + d[4] * d[4]);
    float a = s2.x + s2.y, b = q2.x + q2.y, a2, b2;
#define LN_DPP(m) "v_add_f32_dpp %0, %0, %0 " m " row_mask:0xf bank_mask:0xf\n\tv_add_f32_dpp %1, %1, %1 " m " row_mask:0xf bank_mask:0xf\n\ts_nop 0\n\t"
    asm("s_nop 1\n\t" LN_DPP("quad_perm:[1,0,3,2]") LN_DPP("quad_perm:[2,3,0,1]") LN_DPP("row_ror:4")
        "v_add_f32_dpp %0, %0, %0 row_ror:8 row_mask:0xf bank_mask:0xf\n\tv_add_f32_dpp %1, %1, %1 row_ror:8 row_mask:0xf bank_mask:0xf\n\ts_nop 1\n\t"
        "v_permlane16_swap_b32 %0, %1\n\tv_add_f32 %0, %0, %1\n\tv_mov_b32 %1, %0\n\ts_nop 1\n\tv_permlane32_swap_b32 %0, %1\n\tv_add_f32 %0, %0, %1\n\ts_nop 0\n\t"
        "v_readlane_b32 %2, %0, 0\n\tv_readlane_b32 %3, %0, 16\n\ts_nop 1"
        : "+v"(a), "+v"(b), "=s"(a2), "=s"(b2));
    const float m = a2 * (1.0f / DM), var = fmaxf(b2 * (1.0f / DM) - m * m, 0.f);
    const float sd = rbf(__builtin_amdgcn_sqrtf(rbf(rbf(var) + 1e-5f)));
    return (f32x2){rbf(c + m), __builtin_amdgcn_rcpf(sd)};
}
DEV u32 pkv(f32x2 v) { u32 d; asm("v_cvt_pk_bf16_f32 %0, %1, %2" : "=v"(d) : "v"(v.x), "v"(v.y)); return d; }
DEV void store_h(rsrc_t rs, u32 base, int row, const u32 (&hp)[5]) {
    const u32 lane = threadIdx.x & 63;
    const u32 sw = (u32)(row & 15) * 16u, o8 = (lane >> 5) * 8192u + (((lane & 31u) * 8u) ^ sw), hb = base + (u32)(row >> 5) * (32 * DM * 2) + (u32)(row & 31) * 256u;
    st8s(rs, o8, hb, (v2u){hp[0], hp[1]});
    st8s(rs, o8, hb + 16384, (v2u){hp[2], hp[3]});
    st4s(rs, (lane * 4) ^ sw, hb + 32768, hp[4]);
}

#define DMA16(ldsb, vo, so) asm volatile("s_mov_b32 m0, %0\n\ts_nop 0\n\tbuffer_load_dwordx4 %1, %2, %3 offen sc1 lds" :: "s"(ldsb), "v"(vo), "s"(srd), "s"(so) : "memory")
#define DMA16I(ldsb, lo, vo, so, go) do { u32 so_; asm volatile("s_add_u32 m0, %1, %5\n\ts_add_u32 %0, %4, %6\n\tbuffer_load_dwordx4 %2, %3, %0 offen sc1 lds" \
                         : "=&s"(so_) : "s"(ldsb), "v"(vo), "s"(srd), "s"(so), "i"(lo), "i"(go) : "memory", "scc"); } while (0)
#define DMA32(ldsb, vo, so) asm volatile("s_mov_b32 m0, %0\n\ts_nop 0\n\tbuffer_load_dwordx4 %1, %2, %3 offen sc1 lds\n\tbuffer_load_dwordx4 %1, %2, %3 offen offset:1024 sc1 lds" \
                         :: "s"(ldsb), "v"(vo), "s"(srd), "s"(so) : "memory")
#define DMAIN(sr, ldsb, vo, so) asm volatile("s_mov_b32 m0, %0\n\ts_nop 0\n\tbuffer_load_dwordx4 %1, %2, %3 offen lds" :: "s"(ldsb), "v"(vo), "s"(sr), "s"(so) : "memory")
DEV v4u mk_srd(const void* q, u32 bytes) { return (v4u){uu((u32)(size_t)q), uu((u32)((size_t)q >> 32) & 0xffffu), bytes, 0x00020000u}; }
#define DMANT(sr, ldsb, vo, so) asm volatile("s_mov_b32 m0, %0\n\ts_nop 0\n\tbuffer_load_dwordx4 %1, %2, %3 offen nt lds" :: "s"(ldsb), "v"(vo), "s"(sr), "s"(so) : "memory")
#define AHEAD(sr, vo, so) asm volatile("buffer_load_dwordx4 %0, %1, %2, %3 offen" : "+v"(junk) : "v"(vo), "s"(sr), "s"(so) : "memory")
#define MK_SRD() const v4u srd = mk_srd(p.ws, W_END)

constexpr int CV_W = 40960;
template <bool B> struct CvL {
    static constexpr int NF = B ? 5 : 3, NK = B ? 4 : 2, KP = 544 + NK * 256, CH = NF * 1024 + 4 * KP;
};
constexpr int CV_Y = 4 * CvL<false>::CH, CV_YP = 272;
static_assert(CV_Y + 32 * CV_YP <= CV_W && 2 * CvL<true>::CH <= CV_Y, "conv lds");
DEV int cv_chan(int u, int i) { return 16 * (u % 40) + 4 * i + u / 40; }
template <bool B> DEV void cv_filt(const Params& p, u32 dst, int d, int i) {
    const u32 lane = threadIdx.x & 63;
    const v4u sf = mk_srd(p.ssmk + d * SEQ, SEQ * 2);
    if (i == 0) DMANT(sf, dst, lane < 4u ? OOB : lane * 16u - 64u, 0);
    else DMANT(sf, uu(dst + i * 1024), i == CvL<B>::NF - 1 && lane >= 4u ? OOB : lane * 16u, i * 1024 - 64);
}
DEV v4u cv_kvld(rsrc_t rs, int d, int i) { return ld16s(rs, (threadIdx.x & 63) * 16u, uu(W_KVT + (u32)d * (SEQ * 2) + i * 1024)); }
template <bool B> DEV void cv_kvput(lptr F, const v4u* k) {
    const int lane = threadIdx.x & 63;
#pragma unroll
    for (int i = 0; i < CvL<B>::NK; ++i) lds_w16(F + CvL<B>::NF * 1024 + (lane & 3) * CvL<B>::KP + 512 + i * 256 + (lane >> 2) * 16, k[i]);
}
template <bool B> DEV void cv_zero(lptr F) {
    const int lane = threadIdx.x & 63;
#pragma unroll
    for (int i = 0; i < 4; ++i) lds_w16(F + (lane >> 5) * CvL<B>::CH + CvL<B>::NF * 1024 + i * CvL<B>::KP + (lane & 31) * 16, (v4u){0u, 0u, 0u, 0u});
}
#define M32V(d, a, b) asm volatile("v_mfma_f32_32x32x16_bf16 %0, %1, %2, %0" : "+v"(d) : "v"(a), "v"(b))
template <bool B, class G> DEV void cv_run(const Params& p, rsrc_t rs, lptr F, int d, u32 D, f32x4 sp, float (&y)[16], G&& gap) {
    constexpr int KP = CvL<B>::KP, FO = B ? 2048 : 0;
    const int lane = threadIdx.x & 63, n = lane & 31, hh = lane >> 5, c = lane & 3;
    lptr fa = F + 64 + (32 * hh + 16 * (c & 1) + 8 * ((lane >> 4) & 1) + 4 * (c >> 1) - (lane & 12)) * 2;
    lptr kl = F + CvL<B>::NF * 1024 + (32 + n - hh) * 16;
    v4u dg;
#pragma unroll
    for (int q = 0; q < 4; ++q) { const int k = (lane & 35) ? -1 : (n & 4) + (n >> 3); dg[q] = k == 2 * q ? D : k == 2 * q + 1 ? D << 16 : 0u; }
    v4u l[4], h[4]; v2u fc[4], fh[4];
    f32x16 acc[4];
#pragma unroll
    for (int j = 0; j < 4; ++j) { l[j] = lds_r16(kl + j * KP); if constexpr (B) h[j] = lds_r16(kl + j * KP + 512); }
#pragma unroll
    for (int j = 0; j < 2; ++j) { fc[j] = lds_tr(fa + FO - 32 * j); if constexpr (B) fh[j] = lds_tr(fa - 32 * j); }
    asm volatile("s_nop 1" : "+v"(dg));
#pragma unroll
    for (int j = 0; j < 4; ++j) asm volatile("v_mfma_f32_32x32x16_bf16 %0, %1, %2, 0" : "=&v"(acc[j]) : "v"(dg), "v"(B ? h[j] : l[j]));
    constexpr int U = B ? 2 : 4;
    kl -= 32 * U; fa -= 32;
    auto body = [&](bool first) __attribute__((always_inline)) {
#pragma unroll
        for (int z = 0; z < U; ++z) {
            const int c = 2 * (z & 1);
            const v4u f = __builtin_shufflevector(fc[c], fc[c + 1], 0, 1, 2, 3), g = __builtin_shufflevector(fh[c], fh[c + 1], 0, 1, 2, 3);
#pragma unroll
            for (int j = 0; j < 4; ++j) {
                M32V(acc[j], f, l[j]); l[j] = lds_r16(kl + j * KP + 32 * (U - z - 1));
                if (j < 2) fc[(c ^ 2) + j] = lds_tr(fa + FO + (z + 1) * 128 + 32 - 32 * j);
                if (first && j > 0) gap(5 * z + j - 1);
                SCHED();
            }
            if constexpr (B) {
#pragma unroll
                for (int j = 0; j < 4; ++j) {
                    M32V(acc[j], g, h[j]); h[j] = lds_r16(kl + j * KP + 512 + 32 * (U - z - 1));
                    if (j < 2) fh[(c ^ 2) + j] = lds_tr(fa + (z + 1) * 128 + 32 - 32 * j);
                    if (first && j > 1) gap(5 * z + j + 1);
                    SCHED();
                }
            }
        }
        kl -= 32 * U; fa += 128 * U;
    };
    if constexpr (B) body(true);
#pragma unroll 1
    for (int m = B ? 2 * U : 0; m < 32; m += 2 * U) body(false);
    asm volatile("s_nop 11" : "+v"(acc[0]), "+v"(acc[1]), "+v"(acc[2]), "+v"(acc[3]));
    const float o[3] = {acc[1][15] + acc[2][14] + acc[3][13], acc[2][15] + acc[3][14], acc[3][15]};
    if constexpr (!B) st16(rs, lane == 63 ? W_SP + (u32)d * 16u : OOB, (v4u){u_(o[0]), u_(o[1]), u_(o[2]), 0u});
#pragma unroll
    for (int i = 0; i < 16; ++i) {
        float v = acc[0][i];
#pragma unroll
        for (int j = 1; j < 4; ++j) if (j <= i) v += acc[j][i - j];
        if (i < 3) { const float e = f_((u32)__builtin_amdgcn_ds_bpermute((hh ? lane - 32 : lane + 31) * 4, (int)u_(o[i]))); v += lane == 0 ? sp[i] : e; }
        y[i] = v;
    }
}
DEV void cv_put(lptr F, int pr, const float (&y0)[16], const float (&y1)[16]) {
    const int lane = threadIdx.x & 63;
#pragma unroll
    for (int i = 0; i < 16; ++i) *(LAS u32*)(F + CV_Y + (lane & 31) * CV_YP + (lane >> 5) * 128 + i * 8 + pr * 4) = pk(y0[i], y1[i]);
}
DEV void cv_out(rsrc_t rs, lptr F, int u, int blk0) {
    const int lane = threadIdx.x & 63;
    lptr q = F + CV_Y + ((lane >> 2) & 3) * CV_YP + ((lane >> 4) * 4 + (lane & 3)) * 8;
    const u32 yo = W_Y + (u32)(((lane >> 2) * 8 + blk0) * DM + 4 * u + (lane & 3)) * 16u;
#pragma unroll
    for (int i = 0; i < 8; ++i) { lptr a = q + (i >> 1) * (8 * CV_YP) + (i & 1) * 128; st16(rs, yo + (u32)((i & 1) * 128 + (i >> 1)) * (DM * 16), tr2(a, a + 4 * CV_YP)); }
}
struct KvIn { v2u k[19], v[16]; v4u s[2]; };
DEV void kvt_load(const Params& p, int tile, KvIn& in) {
    const int lane = threadIdx.x & 63;
    const rsrc_t rk = mk_rsrc(p.bk, ACT), rv = mk_rsrc(p.bv, ACT), rh = mk_rsrc(p.shift, DM * 8);
    const int row = (tile / 10) * 64 + (lane >> 4) * 16, col = (tile % 10) * 64 + (lane & 15) * 4;
    const u32 vo = (u32)(row * DM + col) * 2u, vp = row > 0 ? vo - 3 * (DM * 2) : OOB;
#pragma unroll
    for (int i = 0; i < 2; ++i) in.s[i] = ldin16(rh, (u32)col * 8u + i * 16, 0);
#pragma unroll
    for (int i = 0; i < 19; ++i) { in.k[i] = i < 3 ? ldin8(rk, vp, i * (DM * 2)) : ldin8(rk, vo, (i - 3) * (DM * 2)); if (i >= 3) in.v[i - 3] = ldin8(rv, vo, (i - 3) * (DM * 2)); }
}
DEV void kvt_make(rsrc_t rs, lptr xt, int tile, const KvIn& in) {
    const int lane = threadIdx.x & 63;
#pragma unroll
    for (int c = 0; c < 2; ++c) {
        const v4u sv = in.s[c];
        const f32x2 sh[4] = {{lo_f(sv.x), lo_f(sv.z)}, {hi_f(sv.x), hi_f(sv.z)}, {lo_f(sv.y), lo_f(sv.w)}, {hi_f(sv.y), hi_f(sv.w)}};
        f32x2 kf[19];
#pragma unroll
        for (int i = 0; i < 19; ++i) kf[i] = up2(in.k[i][c]);
        u32 kv[16];
#pragma unroll
        for (int i = 0; i < 16; ++i) kv[i] = pk2(((kf[i] * sh[3] + kf[i + 1] * sh[2]) + (kf[i + 2] * sh[1] + kf[i + 3] * sh[0])) * up2(in.v[i][c]));
#pragma unroll
        for (int q = 0; q < 8; ++q) {
            const u32 sel = (q & 1) ? 0x07060302u : 0x05040100u; const u32* z = kv + (q >> 1);
            *(LAS v2u*)(xt + ((2 * c + (q & 1)) * 16 + (lane & 15)) * 144 + (lane >> 5) * 64 + (q >> 1) * 16 + ((lane >> 4) & 1) * 8) =
                (v2u){__builtin_amdgcn_perm(z[4], z[0], sel), __builtin_amdgcn_perm(z[12], z[8], sel)};
        }
    }
    const u32 o = W_KVT + (u32)(((tile % 10) * 64 + 4 * (lane >> 3)) * SEQ + (tile / 10) * 64 + (lane & 7) * 8) * 2u;
#pragma unroll
    for (int i = 0; i < 8; ++i) st16(rs, o + (u32)(32 * (i & 1) + (i >> 1)) * (SEQ * 2), lds_r16(xt + (8 * i + (lane >> 3)) * 144 + (lane & 7) * 16));
}
struct RowX { v2u a, b; u32 c; };
DEV void ln_row(rsrc_t rs, int row, const RowX& r) {
    const f32x2 x[5] = {up2(r.a.x), up2(r.a.y), up2(r.b.x), up2(r.b.y), up2(r.c)};
    const f32x2 st = ln_stats(x);
    u32 hp[5];
#pragma unroll
    for (int q = 0; q < 5; ++q) hp[q] = pk2(up2(pkv(x[q] - st.x)) * st.y);
    store_h(rs, W_H1, row, hp);
}
DEV void pre_rings(const Params& p, rsrc_t rs, int j) {
    const u32 gi = (u32)(j * 256 + threadIdx.x), gt = gi * 16u, R = NPRE * 4096, n = ((gt >> 14) << 9) + ((gt & 8191u) >> 4);
    v4u m = {PSENT, PSENT, PSENT, PSENT};
    if (n < 640u) {
        m = *(const v4u*)(p.mem0 + (2 * ((n >> 2) & 3) + (n & 1)) * DM + (n >> 4) * 16 + (n & 2) * 4);
#pragma unroll
        for (int i = 0; i < 4; ++i) m[i] = m[i] == PSENT ? 0x7FC17FC1u : m[i];
    }
    st4s(rs, gi - 36u * 256u < F_CLR ? foff(gi - 36u * 256u) : OOB, 0, 0u);
#pragma unroll 1
    for (u32 o = gt; o < MB; o += R) st16(rs, W_ORING + o, (v4u){PSENT, PSENT, PSENT, PSENT});
    const u32 mo = gt < 4 * 32768 + 16384 ? W_MRING + gt : OOB;
    st16(rs, mo, m);
    st16(rs, mo + MB / 4, m);
    static_assert(NPRE * 4096 >= 4 * 32768 + 16384, "memory ring");
}
constexpr int LN0 = NPRE * 4;
struct Early { KvIn in; RowX r; u32 vote; };
DEV void conv_early(const Params& p, rsrc_t rs, int j, Early& e) {
    const int lane = threadIdx.x & 63, w = uni(threadIdx.x >> 6), u = j * 4 + w;
    if (j < NPRE) {
        const rsrc_t rx = mk_rsrc(p.x, ACT);
        e.r.a = __builtin_amdgcn_raw_buffer_load_b64(rx, lane * 8u, uu(u * (DM * 2)), 0); e.r.b = __builtin_amdgcn_raw_buffer_load_b64(rx, lane * 8u + 512u, uu(u * (DM * 2)), 0);
        e.r.c = __builtin_amdgcn_raw_buffer_load_b32(rx, lane * 4u + 1024u, uu(u * (DM * 2)), 0);
        kvt_load(p, u, e.in);
    } else if (j < NCV && w > 0) e.vote = pol_probe(p, rs, u - NPRE * 4);
}
DEV void conv_stage(const Params& p, rsrc_t rs, lptr lds, int j, int g0, int ge, v2u ep, Early& e) {
    const int tid = threadIdx.x, lane = tid & 63, w = uni(tid >> 6), u = j * 4 + w;
    lptr F = lds + w * CV_W;
    const u32 fb = uu(w * CV_W);
    const rsrc_t rd = mk_rsrc(p.ssmD, DM * 2);
    float y0[16], y1[16];
    if (j < NPRE) {
        constexpr int CH = CvL<false>::CH;
        KvIn& in = e.in;
        if (w == 1) LST(1500 + j, rt32());
        pre_rings(p, rs, j);
        ln_row(rs, u, e.r);
        if (w == 1) LST(1550 + j, rt32());
        DRAIN(); BAR();
        pre_post(rs, j, 1u, ep);
        if (j == 20) LST(260, rt32());
        if (w == 0) LST(1450 + j, rt32());
        const u32 D = __builtin_amdgcn_raw_buffer_load_b16(rd, (u32)cv_chan(u, lane & 3) * 2u, 0, 0);
#pragma unroll 1
        for (int i = 0; i < 4; ++i) { const int d = cv_chan(u, i); cv_filt<false>(p, fb + i * CH, d, 0); cv_filt<false>(p, fb + i * CH, d, 1); cv_filt<false>(p, fb + i * CH, d, 2); }
        kvt_make(rs, F + CV_Y, u, in);
        cv_zero<false>(F); cv_zero<false>(F + 2 * CH);
        DRAIN(); BAR();
        pre_post(rs, j, 2u, ep);
        if (j == 20) LST(262, rt32());
        pre_wait(rs, 0, NPRE, 2u, ep);
        if (j == 20) LST(281, rt32());
        {
            v4u k[8];
#pragma unroll
            for (int i = 0; i < 8; ++i) k[i] = cv_kvld(rs, cv_chan(u, i >> 1), i & 1);
#pragma unroll
            for (int i = 0; i < 4; ++i) cv_kvput<false>(F + i * CH, k + 2 * i);
        }
        if (j == 20) LST(286, rt32());
#pragma unroll 1
        for (int i = 0; i < 4; i += 2) {
            cv_run<false>(p, rs, F + i * CH, cv_chan(u, i), rdl(D, i), f32x4{}, y0, [](int) {});
            cv_run<false>(p, rs, F + (i + 1) * CH, cv_chan(u, i + 1), rdl(D, i + 1), f32x4{}, y1, [](int) {});
            cv_put(F, i >> 1, y0, y1);
        }
        if (j == 20) LST(287, rt32());
        cv_out(rs, F, u, 0);
        if (j == 20) LST(263, rt32());
    } else {
        st16(rs, lane == 0 ? foff(F_PRE + 48 + u - NPRE * 4) : OOB, (v4u){ep.x + 1u, ep.y, w > 0 ? e.vote : 0u, 0u});
#pragma unroll 1
        for (int t = u + 128; t < 320; t += 16) { kvt_load(p, t, e.in); kvt_make(rs, F, t, e.in); }
    }
    DRAIN(); BAR();
    if (g0 < ge) {
        constexpr int CH = CvL<true>::CH;
        pre_post(rs, j, 3u, ep);
        cv_zero<true>(F);
        if (w == 0) { pre_poll(rs, 64, 32, 3u, ep); pre_poll(rs, 0, NCV, 3u, ep); }
        BAR();
        v4u k[4];
        auto fetch = [&](u32 dst, int d, int i) { if (i < 5) cv_filt<true>(p, dst, d, i); else if (i < 9) k[i - 5] = cv_kvld(rs, d, i - 5); };
#pragma unroll
        for (int i = 0; i < 9; ++i) fetch(fb, cv_chan(g0 * 4 + w, 0), i);
        const u32 ch = (u32)cv_chan(min((g0 + 8 * (lane >> 2)) * 4 + w, 159), lane & 3);
        u32 D = __builtin_amdgcn_raw_buffer_load_b16(rd, ch * 2u, 0, 0);
        v4u sp = ld16s(rs, W_SP + ch * 16u, 0);
        asm volatile("" : "+v"(D), "+v"(sp));
#pragma unroll 1
        for (int g = g0, n = 0; g < ge; g += 8) {
            const int un = g * 4 + w;
            if (g == 16) { LST(282, rt32()); LST(288, __builtin_amdgcn_s_memtime()); }
#pragma unroll 1
            for (int i = 0; i < 4; i += 2, n += 2) {
                const int d1 = (int)rdl(ch, n + 1), d2 = (int)rdl(ch, n + 2);
                cv_kvput<true>(F, k);
                cv_run<true>(p, rs, F, 0, rdl(D, n), (f32x4){f_(rdl(sp.x, n)), f_(rdl(sp.y, n)), f_(rdl(sp.z, n)), 0.f}, y0, [&](int q) { fetch(fb + CH, d1, q); });
                cv_kvput<true>(F + CH, k);
                cv_run<true>(p, rs, F + CH, 326, rdl(D, n + 1), (f32x4){f_(rdl(sp.x, n + 1)), f_(rdl(sp.y, n + 1)), f_(rdl(sp.z, n + 1)), 0.f}, y1, [&](int q) { fetch(fb, d2, q); });
                cv_put(F, i >> 1, y0, y1);
            }
            cv_out(rs, F, un, 4);
            if (g == 16) { LST(283, rt32()); LST(289, __builtin_amdgcn_s_memtime()); }
        }
        DRAIN(); BAR();
    }
    pre_post(rs, j, 5u, ep);
    if (j == 20) LST(284, rt32());
    if (j == 3) LST(285, rt32());
}

#define M32A(first, d, a, b) do { if (first) asm volatile("v_mfma_f32_32x32x16_bf16 %0, %1, %2, 0" : "=&v"(d) : "a"(a), "v"(b)); \
                                  else asm volatile("v_mfma_f32_32x32x16_bf16 %0, %1, %2, %0" : "+v"(d) : "a"(a), "v"(b)); } while (0)
#define M32B(first, d, a, b) do { if (first) asm volatile("v_mfma_f32_32x32x16_bf16 %0, %1, %2, 0" : "=&v"(d) : "v"(a), "a"(b)); \
                                  else asm volatile("v_mfma_f32_32x32x16_bf16 %0, %1, %2, %0" : "+v"(d) : "v"(a), "a"(b)); } while (0)
#define VCVT(d, a, b) asm volatile("v_cvt_pk_bf16_f32 %0, %1, %2" : "=v"(d) : "v"(a), "v"(b))
#define VADD(d, a, b) asm volatile("v_add_f32 %0, %1, %2" : "=v"(d) : "v"(a), "v"(b))
#define LD4A(d, vo, so) asm volatile("buffer_load_dword %0, %1, %2, %3 offen sc1" : "=v"(d) : "v"(vo), "s"(srd), "s"(so) : "memory")
DEV u32 wst_src(int lane, int ld) { return (u32)((lane >> 4) * ld + 8 * ((lane & 15) ^ ((lane >> 4) << 2))) * 2u; }
DEV u32 wst_rd(int lane, int w) { const int q4 = (lane >> 2) & 3; return (u32)(q4 * 256 + ((w ^ q4) * 64) + ((lane >> 4) & 1) * 32 + (lane & 3) * 8); }

constexpr int QK_SEL = 81920;
#define QK_LEAD 12
#define QK_SMP 56
#define QK_W0 8
DEV void role_qkv(const Params& p, lptr lds, int c, v2u ep) {
    const int tid = threadIdx.x, lane = tid & 63, w = uni(tid >> 6), hh = lane >> 5, r32 = lane & 31;
    const int m = c >> 5, h = c & 31;
    const rsrc_t rs = mk_rsrc(p.ws, W_END);
    v4u wt[40];
    {
        const v4u sw = mk_srd(m == 0 ? p.Wq : m == 1 ? p.Wk : p.Wv, DM * NQ * 2), sx = mk_srd(p.x, ACT);
        const u32 vo = wst_src(lane, NQ) + (u32)h * (DH * 2), x8 = (u32)lane * 8u, x4 = (u32)lane * 4u;
        RowX xr[5];
        auto wfill = [&](int i0, int n) {
#pragma unroll 1
            for (int i = i0; i < i0 + n; ++i) DMAIN(sw, uu((4 * i + w) * 1024), vo, uu((16 * i + 4 * w) * (NQ * 2)));
        };
        wfill(0, QK_W0);
#pragma unroll
        for (int i = 0; i < 5; ++i)
            asm volatile("buffer_load_dwordx2 %0, %3, %5, %6 offen\n\tbuffer_load_dwordx2 %1, %3, %5, %6 offen offset:512\n\tbuffer_load_dword %2, %4, %5, %6 offen offset:1024"
                         : "=&v"(xr[i].a), "=&v"(xr[i].b), "=&v"(xr[i].c) : "v"(x8), "v"(x4), "s"(sx), "s"(uu((LN0 + 4 * c + w + 384 * i) * (DM * 2))) : "memory");
        wfill(QK_W0, 40 - QK_W0);
        if (c == 3) LST(275, rt32());
        VMW(40 - QK_W0);
        if (c == 3) LST(276, rt32());
#pragma unroll
        for (int i = 0; i < 5; ++i) {
            asm volatile("" : "+v"(xr[i].a), "+v"(xr[i].b), "+v"(xr[i].c));
            const int row = LN0 + 4 * c + w + 384 * i;
            if (i < 4 || row < SEQ) ln_row(rs, row, xr[i]);
        }
        if (c == 3) LST(264, rt32());
        VMW(12); BAR();
        lptr tb = lds + hh * 2048 + wst_rd(lane, w);
        SFOR(k, 40)
            lptr q = tb + (k >> 3) * 32768 + (k & 7) * 4096; wt[k] = tr2(q, q + 1024); asm volatile("" : "+a"(wt[k]));
        SEND
        DRAIN(); BAR();
        pre_post(rs, 96 + c, 1u, ep);
    }
    if (c == 3) LST(268, rt32());
    if (w == 0) LST(1350 + c, rt32());
    bool dead = false;
    int n0, n1;
    const u32 t16 = (u32)tid * 16u, wb = uu(w * 1024);
    u32 ar[16];
#pragma unroll
    for (int j = 0; j < 16; ++j) ar[j] = (u32)(r32 * 256 + ((2 * (j & 7) + hh) ^ (r32 & 15)) * 16 + (j >> 3) * 49152);
    MK_SRD();
#define XRD(y, ks) lds_r16(lds + ar[((ks) & 7) + ((ks) >= 24 ? 8 : 0)] + ((ks) >> 3) % 3 * 16384 + (y) * 8192)
#define QDMA(y, i, vo, so) DMA16I(wb, ((i) >> 1) * 16384 + ((i) & 1) * 4096 + (y) * 8192, vo, so, (i) * 4096)
    const u32 h2flag = (lane < 32 && w == 0) ? foff(F_H2 + lane) : OOB;
    const u32 ol = (m == 2 ? (u32)(2 * w + (r32 >> 4)) * 1024u : (u32)((r32 >> 4) * 4 + w) * 1024u) + (u32)(hh * 16 + (r32 & 15)) * 16u
                 + (m == 2 ? W_VO : m == 1 ? W_KQ : W_KQ + 8192u) + (u32)h * 16384u;
    const u32 fo = tid < 2 ? foff(F_QKV + tid * 96 + c) : OOB;
    lptr selw = lds + QK_SEL + (w == 0 ? 0 : 64), selr = lds + QK_SEL;
    auto late = [&](u32 fv) { const u32 need = n1 >= NSEG ? 0xFFFFFFFFu : dead ? 0u : (u32)n1 + 1u; return (u32)__builtin_amdgcn_ballot_w64(fv < need); };
    static_assert(W_H2 - W_H1 == 2 * 64 * (32 * DM * 2), "h");
    pre_wait(rs, 0, NPRE, 1u, ep);
    if (c == 3) LST(269, rt32());
    auto run = [&](auto isv) __attribute__((always_inline)) {
        constexpr bool V = decltype(isv)::value;
        f32x16 ax, ay = {};
        v4u x[8], pp, pq;
        u32 oy = OOB, lt = 1, fcnt = 0, fa = OOB;
        u32 nn = 0;
        PIN(lt); PIN(nn);
#pragma unroll
        for (int z = 0; z < 5; ++z) x[z] = XRD(1, 20 + z);
#define QPK(r_, a1, a_, q_) do { if constexpr (V) VCVT(r_[q_], a1[4 * (a_) + 8 * ((q_) >> 1) + 2 * ((q_) & 1)], a1[4 * (a_) + 8 * ((q_) >> 1) + 2 * ((q_) & 1) + 1]); \
                                 else VCVT(r_[q_], a1[4 * (a_) + (q_)], a1[4 * (a_) + (q_) + 8]); } while (0)
#define QPK4(r_, a1, a_) do { QPK(r_, a1, a_, 0); QPK(r_, a1, a_, 1); QPK(r_, a1, a_, 2); QPK(r_, a1, a_, 3); } while (0)
        for (;;) {
            u32 ls = uu(lt);
            n0 = uni((int)nn); n1 = n0 >> 8; n0 &= 255;
            if (ls != 0u && n0 >= min(NSEG, n1 + QK_LEAD)) {
                CFENCE();
                u32 fv = ld4(rs, h2flag);
                asm volatile("s_waitcnt vmcnt(0)" : "+v"(fv) :: "memory");
                *(LAS u32*)(selw + 16) = late(fv);
                BAR();
                st4s(rs, fa, 0, fcnt);
                fa = OOB;
                if (n1 >= NSEG) __builtin_amdgcn_endpgm();
                if (uu(*(LAS u32*)(selr + 16)) != 0u) {
                    if (w == 0) wait_flags(rs, h2flag, (u32)n1 + 1, dead);
                    BAR();
                }
                ls = 0u;
            }
            if (n0 == 4) pre_wait(rs, 96, 96, 1u, ep);
            int cur = n0;
            asm volatile("s_cmp_eq_u32 %3, 0\n\ts_cselect_b32 %0, %4, %0\n\ts_addc_u32 %2, %2, 0\n\ts_cmp_lg_u32 %3, 0\n\ts_addc_u32 %1, %1, 0"
                         : "+s"(cur), "+s"(n0), "+s"(n1) : "s"(ls), "s"(n1 | 64) : "scc");
            u32 xs = W_H1 + (u32)(cur + (cur & 64)) * (32 * DM * 2), ys, yvo, ox, fv;
            PINS(xs);
#define QT(k) do {} while (0)
            QT(0);
            SCHED();
            QDMA(0, 0, t16, xs); QDMA(0, 1, t16, xs);
            SFOR(z, 80)
                constexpr int ks = (z + 20) % 40, zn = (z + 5) % 80;
                if constexpr (z >= 20 && z < 60) { if constexpr (V) M32B(ks == 0, ax, x[z & 7], wt[ks]); else M32A(ks == 0, ax, wt[ks], x[z & 7]); }
                else { if constexpr (V) M32B(ks == 0, ay, x[z & 7], wt[ks]); else M32A(ks == 0, ay, wt[ks], x[z & 7]); }
                x[(z + 5) & 7] = XRD(zn < 20 || zn >= 60, (zn + 20) % 40);
                if constexpr (z == 77) lt = *(LAS u32*)selr;
                SCHED();
                if constexpr (z < 8) QDMA(0, z + 2, t16, xs);
                if constexpr (z == 9) { ys = W_H1 + (u32)n0 * (32 * DM * 2); yvo = t16 | (n0 < NSEG ? 0u : OOB); PINS(ys); PIN(yvo); }
                if constexpr (z == 11) { ox = ol + (u32)cur * (NH * 16384u); PIN(ox); }
                if constexpr (z == 14) { QT(1); asm volatile("s_waitcnt vmcnt(6) lgkmcnt(0)\n\ts_barrier" ::: "memory"); QT(2); }
                if constexpr (z == 15) st4s(rs, fa, 0, fcnt);
                if constexpr (z >= 16 && z < 26) QDMA(1, z - 16, yvo, ys);
                if constexpr (z == 26) QPK4(pp, ay, 0);
                if constexpr (z == 27) st16(rs, oy, pp);
                if constexpr (z == 28) QPK4(pq, ay, 1);
                if constexpr (z == 29) st16(rs, oy + 512, pq);
                if constexpr (z == 30) { QT(3); asm volatile("s_waitcnt vmcnt(13)\n\ts_barrier" ::: "memory"); QT(4); }
                if constexpr (z == 32) { fcnt = lane == 0 ? (u32)n0 : (u32)n1; fa = fo; asm volatile("" : "+v"(fcnt), "+v"(fa)); }
                if constexpr (z == 34) { oy = (ol + (u32)n0 * (NH * 16384u)) | (yvo & OOB); PIN(oy); }
                if constexpr (z == 35) { nn = (u32)(min(n0 + 1, NSEG) | n1 << 8); PIN(nn); }
                if constexpr (z == 54) { asm volatile("s_waitcnt vmcnt(2) lgkmcnt(0)\n\ts_barrier" ::: "memory"); QT(5); }
                if constexpr (z == QK_SMP) LD4A(fv, h2flag, 0);
                if constexpr (z == 62) QPK4(pp, ax, 0);
                if constexpr (z == 63) st16(rs, ox, pp);
                if constexpr (z == 64) QPK4(pq, ax, 1);
                if constexpr (z == 65) st16(rs, ox + 512, pq);
                if constexpr (z == 73) { asm volatile("s_waitcnt vmcnt(2)" : "+v"(fv) :: "memory"); QT(6); ls = late(fv); PINS(ls); }
                if constexpr (z == 74) { lt = ls; PIN(lt); *(LAS u32*)selw = lt; }
                if constexpr (z == 76) asm volatile("s_waitcnt lgkmcnt(2)\n\ts_barrier" ::: "memory");
                SCHED();
            SEND
        }
    };
    if (m == 2) run(IC<1>{}); else run(IC<0>{});
}

#define OU_SMP 44
DEV void role_out(const Params& p, lptr lds, int L, int kq, int ng, v2u ep) {
    const int tid = threadIdx.x, lane = tid & 63, w = uni(tid >> 6), hh = lane >> 5, r32 = lane & 31;
    const rsrc_t rs = mk_rsrc(p.ws, W_END);
    v4u wo[64];
    bool dead = false;
    {
        const v4u sw = mk_srd(p.Wo, DM * NQ * 2);
        const u32 vo = wst_src(lane, DM) + (u32)ng * 256u;
        auto fill = [&](int b) {
#pragma unroll 1
            for (int i = 0; i < 8; ++i) DMAIN(sw, uu((b % 5) * 32768 + (4 * i + w) * 1024), vo, uu((kq * 1024 + b * 128 + 16 * i + 4 * w) * (DM * 2)));
        };
#pragma unroll 1
        for (int b = 0; b < 5; ++b) fill(b);
        lptr tb = lds + hh * 1024 + wst_rd(lane, w);
        SFOR(b, 8)
            VMW(b < 4 ? 32 : 56 - 8 * b); BAR();
            lptr q = tb + (b % 5) * 32768;
#pragma unroll
            for (int k = 0; k < 8; ++k) { lptr a = q + (k >> 1) * 8192 + (k & 1) * 2048; wo[8 * b + k] = tr2(a, a + 4096); if (b < 7) asm volatile("" : "+a"(wo[8 * b + k])); else PIN(wo[8 * b + k]); }
            if constexpr (b < 3) { BAR(); fill(b + 5); }
        SEND
    }
    pre_wait(rs, 0, NPRE, 1u, ep);
    if (kq == 0 && ng == 0) LST(272 + L, rt32());
    MK_SRD();
    const u32 ofl = lane < 8 ? foff(F_O + L * 32 + kq * 8 + lane) : OOB, l16 = (u32)lane * 16u;
    const u32 fo = (tid == 0) ? foff(F_P + L * 32 + kq * 5 + ng) : OOB;
    const u32 src0 = uu(W_VO + 8192u + (u32)(L * NSEG * NH + kq * 8) * 16384u + w * 1024);
#define ODMA(i, dst, so) DMA16I(dst, (i) * 4096, l16, so, ((i) >> 1) * 16384 + ((i) & 1) * 4096)
    auto first = [&](int s) { const u32 dst = uu((s & 1) * 65536 + w * 1024), src = src0 + (u32)s * (NH * 16384u); SFOR(i, 10) ODMA(i, dst, src); SEND };
    u32 pa = W_P + (u32)(L * NSEG) * P_ITEM + (u32)(4 * hh * 4 + kq) * (DM * 4) + (u32)(ng * 128 + w * 32 + r32) * 4u, fv = 0;
    bool early = false;
    for (int s = 0; s < NSEG; ++s) {
        const u32 dst = uu((s & 1) * 65536 + w * 1024), src = src0 + (u32)s * (NH * 16384u);
        lptr A = lds + (s & 1) * 65536 + l16;
        if (!early) {
            if (!all_(ofl == OOB || fv >= (u32)s + 1)) wait_flags(rs, ofl, (u32)s + 1, dead);
            LS3(L == 0 && w == 1, 1, kq * 5 + ng);
            first(s);
        }
        asm volatile("s_waitcnt vmcnt(8)\n\ts_barrier" ::: "memory");
        v4u o[64];
#pragma unroll
        for (int k = 0; k < 3; ++k) o[k] = lds_r16(A + k * 1024);
        f32x16 acc;
        SCHED();
        SFOR(k, 64)
            if constexpr (k < 56) M32B(k == 0, acc, o[k], wo[k]); else M32V(acc, o[k], wo[k]);
            if constexpr (k < 61) o[k + 3] = lds_r16(A + (k + 3) * 1024);
            SCHED();
            if constexpr (k < 12 && (k & 1) == 0) ODMA(10 + k / 2, dst, src);
            if constexpr (k == 4) asm volatile("s_waitcnt vmcnt(7)\n\ts_barrier" ::: "memory");
            if constexpr (k == 20) asm volatile("s_waitcnt vmcnt(5)\n\ts_barrier" ::: "memory");
            if constexpr (k == 40) asm volatile("s_waitcnt vmcnt(0)\n\ts_barrier" ::: "memory");
            if constexpr (k == OU_SMP) LD4A(fv, ofl, 0);
            SCHED();
        SEND
        asm volatile("s_nop 9" : "+v"(acc), "+v"(pa));
#pragma unroll
        for (int i = 0; i < 16; ++i) st4s(rs, pa, (u32)((8 * (i >> 2) + (i & 3)) * (4 * DM * 4)), u_(acc[i]));
        asm volatile("s_waitcnt vmcnt(16)" : "+v"(fv) :: "memory");
        LS3(L == 0 && w == 1, 2, kq * 5 + ng);
        early = s + 1 < NSEG && all_(ofl == OOB || fv >= (u32)s + 2);
        if (early) { first(s + 1); VMW(10); }
        else { fv = ld4(rs, ofl); asm volatile("s_waitcnt vmcnt(0)" : "+v"(fv) :: "memory"); }
        BAR();
        st4s(rs, fo, 0, (u32)s + 1);
        pa += P_ITEM;
        LS2(L == 0 && w == 1, 160 + kq * 5 + ng);
    }
}

constexpr int RW_P = 20480, RW_X = RW_P + 10240;
DEV void role_row(const Params& p, lptr lds, int id, v2u ep) {
    const int lane = threadIdx.x & 63, w = uni(threadIdx.x >> 6);
    const int ri = id >> 1, L = id & 1;
    const rsrc_t rs = mk_rsrc(p.ws, W_END);
    bool dead = false, have = false;
    const u32 fp = lane < 20 ? foff(F_P + L * 32 + lane) : OOB, l16 = (u32)lane * 16u, lx = lane < 16 ? l16 + 1024u : OOB, fb = uu(w * CV_W);
    lptr F = lds + w * CV_W;
    MK_SRD();
    const v4u sx = mk_srd(p.x, ACT);
    auto yfill = [&](int b) {
#pragma unroll 1
        for (int i = 0; i < 5; ++i) DMA32(uu(fb + (b & 1) * 10240 + i * 2048), l16, uu(W_Y + (u32)(ri * 8 + b) * 10240u + i * 2048));
    };
    auto xfill = [&](int s) {
        const u32 d = uu(fb + RW_X + (s & 1) * 2048), o = uu((u32)(s * 32 + ri) * (DM * 2));
        if (L == 0) { DMAIN(sx, d, l16, o); DMAIN(sx, d + 1024, lx, o); } else { DMA16(d, l16, o + W_X2); DMA16(d + 1024, lx, o + W_X2); }
    };
    auto pfill = [&](int s) {
        const u32 o = uu(W_P + (u32)(L * NSEG + s) * P_ITEM + (u32)ri * (4 * DM * 4));
        SFOR(i, 10) DMA16I(fb, RW_P + i * 1024, l16, o, i * 1024); SEND
        if (L == 1) xfill(s);
    };
    if (L == 0) xfill(0);
    pre_poll(rs, 0, NPRE, 3u, ep);
    if (id == 0) LST(274, rt32());
    yfill(0); DRAIN();
    u32 fv = 0;
    for (int s = 0; s < NSEG; ++s) {
        if (!have) {
            if (!all_(fp == OOB || fv >= (u32)s + 1)) wait_flags(rs, fp, (u32)s + 1, dead);
            LS3(L == 0, 3, ri);
            pfill(s);
        }
        LS2(L == 0, 192 + ri);
        f32x2 y[5];
        {
            lptr ya = F + ((s >> 3) & 1) * 10240 + (s & 7) * 2 + lane * 16;
            auto yr = [&](lptr q) { return lo_f(*(LAS u16*)q); };
            y[0] = (f32x2){yr(ya), yr(ya + 2560)}; y[1] = (f32x2){yr(ya + 5120), yr(ya + 7680)};
            y[2] = (f32x2){yr(ya + 1024), yr(ya + 3584)}; y[3] = (f32x2){yr(ya + 6144), yr(ya + 8704)};
            lptr yc = ya - lane * 16 + 2048 + (lane >> 1) * 16 + (lane & 1) * 5120;
            y[4] = (f32x2){yr(yc), yr(yc + 2560)};
            asm volatile("" : "+v"(y[0]), "+v"(y[1]), "+v"(y[2]), "+v"(y[3]), "+v"(y[4]));
        }
        if (!have) DRAIN();
        LS3(L == 0, 4, ri);
        f32x2 x1[5];
        {
            f32x4 a[4], b[4]; f32x2 c[4];
            lptr q = F + RW_X + (s & 1) * 2048;
            v2u xa = *(LAS v2u*)(q + lane * 8), xb = *(LAS v2u*)(q + 512 + lane * 8); u32 xc = *(LAS u32*)(q + 1024 + lane * 4);
#pragma unroll
            for (int kq = 0; kq < 4; ++kq) { lptr r = F + RW_P + kq * (DM * 4); a[kq] = *(LAS f32x4*)(r + l16); b[kq] = *(LAS f32x4*)(r + 1024 + l16); c[kq] = *(LAS f32x2*)(r + 2048 + lane * 8); }
            SCHED();
            const f32x4 sa = (a[0] + a[1]) + (a[2] + a[3]), sb = (b[0] + b[1]) + (b[2] + b[3]);
            const f32x2 o[5] = {sa.lo, sa.hi, sb.lo, sb.hi, (c[0] + c[1]) + (c[2] + c[3])};
            const u32 xi[5] = {xa.x, xa.y, xb.x, xb.y, xc};
#pragma unroll
            for (int i = 0; i < 5; ++i) x1[i] = up2(xi[i]) + o[i];
            asm volatile("" : "+v"(x1[0]), "+v"(x1[1]), "+v"(x1[2]), "+v"(x1[3]), "+v"(x1[4]));
        }
        have = s + 1 < NSEG && all_(fp == OOB || fv >= (u32)s + 2);
        if (__builtin_expect(have, 0)) pfill(s + 1);
        if (L == 0) xfill(s + 1);
        const f32x2 st1 = ln_stats(x1);
        u32 xp[5];
#pragma unroll
        for (int i = 0; i < 5; ++i) xp[i] = pkv(x1[i] + (x1[i] - st1.x) * (y[i] * st1.y));
        const int row = s * 32 + ri;
        if (L == 0) {
            const u32 ro = (u32)row * (DM * 2);
            f32x2 x2[5];
#pragma unroll
            for (int i = 0; i < 5; ++i) x2[i] = up2(xp[i]);
            st8s(rs, (u32)lane * 8, W_X2 + ro, (v2u){xp[0], xp[1]});
            st8s(rs, (u32)lane * 8 + 512, W_X2 + ro, (v2u){xp[2], xp[3]});
            st4s(rs, (u32)lane * 4 + 1024, W_X2 + ro, xp[4]);
            const f32x2 st2 = ln_stats(x2);
            u32 hp[5];
#pragma unroll
            for (int i = 0; i < 5; ++i) hp[i] = pk2((x2[i] - st2.x) * st2.y);
            store_h(rs, W_H2, row, hp);
        } else {
            u16* o_ = p.out + (size_t)row * DM;
            *(v2u*)(o_ + 4 * lane) = (v2u){xp[0], xp[1]};
            *(v2u*)(o_ + 256 + 4 * lane) = (v2u){xp[2], xp[3]};
            *(u32*)(o_ + 512 + 2 * lane) = xp[4];
        }
        CFENCE();
        LS3(L == 0, 5, ri);
        fv = ld4(rs, fp);
        asm volatile("s_waitcnt vmcnt(0)" : "+v"(fv) :: "memory");
        st4s(rs, lane == 0 && L == 0 ? foff(F_H2 + ri) : OOB, 0, (u32)s + 1);
        if ((s & 7) == 6 && s < NSEG - 8) {
            if (s == 30) pre_poll(rs, 0, NCV, 5u, ep);
            if (s == 46) st8s(rs, (id == 1 && lane == 0) ? foff(F_EPOCH) : OOB, 0, (v2u){ep.x + 8u, ep.y});
            yfill((s >> 3) + 1);
        }
        if (ri == 0) LST(320 + L * 64 + s, rt32());
        LS2(L == 0, 224 + ri);
    }
}

constexpr float SCALE = 0.08838834764831845f, LOG2E = 1.4426950408889634f;
constexpr int C_K = 0, C_V = 8192, C_QX = 16384, C_MEM = 24576, C_Q = C_MEM + 10240, C_KM = C_Q + 2304, C_VM = C_KM + 2304, C_DUMP = C_VM + 2048,
              C_ZERO = C_DUMP + 1024, C_WL = C_ZERO + 128;
constexpr int NLF = 14;
static_assert(C_WL % 16 == 0 && C_WL + 4 * NLF * 2048 <= LDS_BYTES, "head lds");
#define CH_VIS 10
#define CH_H0 104
#define CH_W0 83
#define CH_MH 3
#define CH_MW 3
#define CH_REC 8
#define CH_PRE 9
#define CH_T0 190
#define CH_UP 4
#define CH_WX 6
#define WAIT_TXT(tmp, t4) "s_memrealtime vcc\n1:\n\ts_waitcnt lgkmcnt(0)\n\ts_sub_u32 " tmp ", " t4 ", vcc_lo\n\ts_memrealtime vcc\n\ts_cmpk_lt_u32 " tmp ", 400\n\ts_cbranch_scc1 1b\n\t" \
                          "s_add_u32 " tmp ", " tmp ", 4\n\ts_cselect_b32 " tmp ", " tmp ", 0\n2:\n\ts_sub_u32 " tmp ", " tmp ", 1\n\ts_cbranch_scc0 2b\n\t"
DEV void wait_until(u32 t) { u32 tmp; asm volatile(WAIT_TXT("%0", "%1") "s_waitcnt lgkmcnt(0)" : "=&s"(tmp) : "s"(uu(t - 4u)) : "vcc", "scc", "memory"); }
#define SMF_A(acc, a, b) asm volatile("v_smfmac_f32_16x16x64_bf16 %0, %1, %2, %3" : "+v"(acc) : "v"(a), "a"(b), "v"(idx))
#define SMF_V(acc, a, b) asm volatile("v_smfmac_f32_16x16x64_bf16 %0, %1, %2, %3" : "+v"(acc) : "v"(a), "v"(b), "v"(idx))
#define MF0(d, a, b) asm volatile("v_mfma_f32_16x16x32_bf16 %0, %1, %2, 0" : "=&v"(d) : "v"(a), "v"(b))
#define MF1(d, a, b) asm volatile("v_mfma_f32_16x16x32_bf16 %0, %1, %2, %0" : "+v"(d) : "v"(a), "v"(b))
#define MF0H(d, a, b) asm volatile("v_mfma_f32_16x16x16_bf16 %0, %1, %2, 0" : "=&v"(d) : "v"(a), "v"(b))
#define MF1H(d, a, b) asm volatile("v_mfma_f32_16x16x16_bf16 %0, %1, %2, %0" : "+v"(d) : "v"(a), "v"(b))
DEV v8u tr_frag(lptr p0, lptr p1, lptr p2, lptr p3) { return cat8(tr2(p0, p1), tr2(p2, p3)); }
constexpr int qslot(int ks) { return (ks & 1) && ks < 8 ? ks >> 1 : -1; }
constexpr int kslot(int ks) { return ks < 4 ? 4 + ks : -1; }
constexpr int vslot(int ks) { return ks >= 4 ? 4 + ks : -1; }
DEV u32 ring_map(u32 off, u32 half) { return ((off >> 13) << 14) + (half << 13) + (off & 8191u); }
#define ZRD(x, i) x = __builtin_bit_cast(f32x4, lds_r16(ZP + 16 * (i)))
#define VMAX(d, a, b) asm volatile("v_max_f32 %0, %1, %2" : "=v"(d) : "v"(a), "v"(b))
#define VMAX3(d, a, b, c_) asm volatile("v_max3_f32 %0, %1, %2, %3" : "=v"(d) : "v"(a), "v"(b), "v"(c_))
#define VEXP(d, a) asm volatile("v_exp_f32 %0, %1" : "=v"(d) : "v"(a))
#define VMULC(d, a) asm volatile("v_mul_f32 %0, 0x3e0293ee, %1" : "=v"(d) : "v"(a))
#define CADD(d, a, b) do { d = (a) + (b); PIN(d); } while (0)
#define CMULC(d, a) do { d = (a) * CEXP; PIN(d); } while (0)
#define CCVT(d, a, b) do { d = pk(a, b); PIN(d); } while (0)
#define LGK(n) __builtin_amdgcn_s_waitcnt(0xC07F | (n) << 8)
#define SEL(d, a, b) asm volatile("v_cndmask_b32 %0, %1, %2, %3" : "=v"(d) : "v"(a), "v"(b), "s"(himask))
#define QPERM(d, a, q) asm volatile("v_mov_b32_dpp %0, %1 quad_perm:" q " row_mask:0xf bank_mask:0xf" : "=v"(d) : "v"(a))

#define HD_SMP 56
DEV void role_head(const Params& p, lptr lds, int L, int h, v2u ep) {
    const int tid = threadIdx.x, lane = tid & 63, w = uni(tid >> 6), g = lane >> 4, r = lane & 15;
    const rsrc_t rs = mk_rsrc(p.ws, W_END);
    v8u wq0[10], wq1[10], wk0[10], wk1[10], wv0[10], wv1[10];
    lptr WL = lds + C_WL + w * (NLF * 2048) + lane * 16;
    {
        constexpr int GS = 1056, CS = 48 * GS;
        const v4u sq = mk_srd(p.Wq, DM * NQ * 2), sk = mk_srd(p.Wk, DM * NQ * 2), sv = mk_srd(p.Wv, DM * NQ * 2);
        const u32 vo = (u32)(g * NQ + h * DH + 8 * (r ^ ((g & 1) << 3))) * 2u;
        auto fill = [&](int ch) {
#pragma unroll 1
            for (int i = 0; i < 4; ++i) {
                const u32 dst = uu((ch % 3) * CS + (4 * i + w) * GS), so = uu((ch * 64 + 16 * i + 4 * w) * (NQ * 2));
                DMAIN(sq, dst, vo, so); DMAIN(sk, dst + 16 * GS, vo, so); DMAIN(sv, dst + 32 * GS, vo, so);
            }
        };
        fill(0); fill(1); fill(2);
        const int q4 = r >> 2;
        lptr tb = lds + (4 * (g >> 1) + 2 * (q4 >> 1) + (g & 1)) * GS + (q4 & 1) * 256 + ((w * 64 + (r & 3) * 8) ^ ((q4 & 1) << 7));
        SFOR(ch, 10)
            VMW(ch < 8 ? 24 : 108 - 12 * ch);
            BAR();
            auto frag = [&](int m, int t) { lptr b = tb + (ch % 3) * CS + m * 16 * GS + t * 32; return tr_frag(b, b + 512, b + 8 * GS, b + 8 * GS + 512); };
            wq0[ch] = frag(0, 0); wq1[ch] = frag(0, 1); wk0[ch] = frag(1, 0); wk1[ch] = frag(1, 1); wv0[ch] = frag(2, 0); wv1[ch] = frag(2, 1);
            asm volatile("" : "+a"(wq0[ch]), "+a"(wk0[ch]), "+v"(wv0[ch]), "+v"(wv1[ch]));
            if constexpr (qslot(ch) < 0) asm volatile("" : "+a"(wq1[ch])); else asm volatile("" : "+v"(wq1[ch]));
            if constexpr (kslot(ch) < 0) asm volatile("" : "+a"(wk1[ch])); else asm volatile("" : "+v"(wk1[ch]));
            BAR();
            if constexpr (ch < 7) fill(ch + 3);
        SEND
#pragma unroll
        for (int ch = 0; ch < 10; ++ch) {
            if (qslot(ch) >= 0) { lds_w16(WL + qslot(ch) * 2048, wq1[ch].lo); lds_w16(WL + qslot(ch) * 2048 + 1024, wq1[ch].hi); }
            if (kslot(ch) >= 0) { lds_w16(WL + kslot(ch) * 2048, wk1[ch].lo); lds_w16(WL + kslot(ch) * 2048 + 1024, wk1[ch].hi); }
            if (vslot(ch) >= 0) { lds_w16(WL + vslot(ch) * 2048, wv1[ch].lo); lds_w16(WL + vslot(ch) * 2048 + 1024, wv1[ch].hi); }
        }
        CFENCE();
    }
    const u32 idx = (r & 2) ? 0xEEEEu : 0x4444u;
    unsigned long long himask = 0xCCCCCCCCCCCCCCCCull;
    PINS(himask);
    bool dead = false;
    if (h == 3) LST(266 + L, rt32());
    if (w == 0) LST(1240 + L * 32 + h, rt32());
    if (L == 1) {
        KvIn in;
        kvt_load(p, LN0 + 4 * h + w, in); kvt_make(rs, lds + w * 9216, LN0 + 4 * h + w, in);
        DRAIN(); BAR();
        pre_post(rs, 64 + h, 3u, ep);
    }
    const u32 pol0 = pol_wait(rs, ep);
    if (h == 3 && L == 0) LST(277, rt32());
    const u32 l16 = (u32)lane * 16u;
    const u32 mring = W_MRING + (u32)L * (MB / 4) + pol0 * 8192u, hint = mring + 4 * 32768u;
    const u32 fkh = lane < 3 ? foff(F_QKV + L * 96 + lane * 32 + h) : lane < 7 ? hint + (u32)(lane - 3) * 4u : OOB;
    u32 tm = 0;
    MK_SRD();
    u32 ksrc = uu(W_KQ + (u32)((L * NSEG - 1) * NH + h) * 16384u + w * 2048), vsrc = ksrc + (W_VO - W_KQ);
    u32 dk = uu(C_K + w * 2048), dv = dk + (C_V - C_K), dq = dk + (C_QX - C_K), dm = uu(C_MEM + w * 2048), dm2 = uu(w < 2 ? C_MEM + 8192 + w * 1024 : C_DUMP);
    asm volatile("" : "+s"(dk), "+s"(dv), "+s"(dq), "+s"(dm), "+s"(dm2));
    auto keys_wait = [&](u32 need) {
#pragma unroll 1
        for (int n = 0; !dead; ++n) {
            const bool beat = need + 2 * L > 2u;
            if (beat) wait_until(tm);
            const u32 hv = ld4(rs, lane == 7 ? foff(F_ERR) : fkh);
            if (beat) { tm = rdl(hv, 5); wait_until(rdl(hv, 3) - 30u); }
            if (((u32)__builtin_amdgcn_ballot_w64(ld4(rs, fkh) < need) & 7u) == 0u) break;
            dead = n > SPIN_CAP || rdl(hv, 7) != 0u;
            st4s(rs, dead ? foff(F_ERR) : OOB, 0, 1u);
        }
        CFENCE();
        DMA32(dk, l16, ksrc); DMA32(dv, l16, vsrc); DMA32(dq, l16, ksrc ^ 8192u);
    };
    lptr qw = lds + C_Q + (2 * g) * 288 + (32 * w + 2 * r) * 2;
    lptr kw = qw + (C_KM - C_Q); PIN(kw);
    lptr qr = lds + C_Q + (2 * (r >> 2) + (r & 1)) * 288 + g * 16;
    lptr LB = lds + l16, VX = lds + C_V + w * 2048 + l16;
    PIN(VX);
    if (tid >= 64 && tid < 70) { const u32 z = tid == 68 ? 0x3F803F80u : tid == 69 ? PSENT : 0u; lds_w16(lds + C_ZERO + (tid - 64) * 16, (v4u){z, z, z, z}); }
    u32 zaddr = 0; PIN(zaddr);
    lptr ZP = lds + zaddr + C_ZERO;
    const float CEXP = SCALE * LOG2E;
#define LF_RD(slot, hf) lf[slot][hf] = lds_r16(WL + (slot) * 2048 + (hf) * 1024)
    BAR();
    const u32 ofl = tid == 0 ? foff(F_O + L * 32 + h) : OOB;
    u32 oso = OOB + W_ORING + (u32)L * (MB / 2) + ring_map((u32)((r >> 3) * 32768 + h * 1024 + w * 256 + (g >> 1) * 128 + (r & 7) * 16), pol0);
    unsigned long long vis[8];
#pragma unroll
    for (int i = 0; i < 8; ++i) vis[i] = __builtin_amdgcn_ballot_w64(16 * (i >> 2) + 4 * g + (i & 3) <= 16 * (w & 1) + r);
    u32 missed = 0;
    for (int s = -1; s < NSEG;) {
        u32 sv = (u32)max(s, 0), ms = mring + (sv & 3u) * 32768u + w * 2048, ms2 = mring + (sv & 3u) * 32768u + 16384u + (w & 1) * 1024;
        asm volatile("" : "+s"(ms), "+s"(ms2)); PIN(sv);
        f32x4 aq0, aq1, ak0, ak1;
        v4u lf[NLF][2];
        ZRD(aq0, 0); ZRD(aq1, 1); ZRD(ak0, 2); ZRD(ak1, 3);
#pragma unroll
        for (int i = 0; i < 4; ++i) { LF_RD(i, 0); LF_RD(i, 1); }
        SCHED();
        v4u A[10];
#define HT(k) do {} while (0)
        v4u kx[8], qf[4], kmf[4], vx[2], ones, P;
        float fa, fb, fc, fd, inv, sx[4], t[4], tm0, tm1; u32 pa, pb, fl, e01, e23, pmm;
        f32x4 av0, av1, s0, s1, sm, oa, ob, rsum;
        v2u vm, pm;
#define LW(sl) cat8(lf[sl][0], lf[sl][1])
#define PQ(ks, tl) do { if constexpr (!(tl)) SMF_A(aq0, A[ks], wq0[ks]); else if constexpr (qslot(ks) < 0) SMF_A(aq1, A[ks], wq1[ks]); else { const v8u lw_ = LW(qslot(ks)); SMF_V(aq1, A[ks], lw_); } } while (0)
#define PK(ks, tl) do { if constexpr (!(tl)) SMF_A(ak0, A[ks], wk0[ks]); else if constexpr (kslot(ks) < 0) SMF_A(ak1, A[ks], wk1[ks]); else { const v8u lw_ = LW(kslot(ks)); SMF_V(ak1, A[ks], lw_); } } while (0)
#define PV(ks, tl) do { if constexpr (!(tl)) SMF_V(av0, A[ks], wv0[ks]); else if constexpr (vslot(ks) < 0) SMF_V(av1, A[ks], wv1[ks]); else { const v8u lw_ = LW(vslot(ks)); SMF_V(av1, A[ks], lw_); } } while (0)
#define KXR(i) kx[i] = lds_r16(LB + C_K + ((i) & 1) * 4096 + ((i) >> 1) * 1024)
#define XW(q_) do { *(LAS u32*)(q_) = pa; *(LAS u32*)((q_) + 288) = pb; } while (0)
        auto slot = [&](auto zc) __attribute__((always_inline)) {
            constexpr int z = decltype(zc)::value;
            if constexpr (z < 14) PQ(z >> 1, z & 1);
            else if constexpr (z < 17) PQ(z - 7, 0);
            else if constexpr (z < 20) PQ(z - 10, 1);
            else if constexpr (z < 34) PK((z - 20) >> 1, z & 1);
            else if constexpr (z < 37) PK(z - 27, 0);
            else if constexpr (z < 40) PK(z - 30, 1);
            else if constexpr (z < 42) { if constexpr (z == 40) MF0(s0, kx[0], qf[0]); else MF0(s1, kx[1], qf[0]); }
            else if constexpr (z < 48) { if constexpr (z & 1) MF1(s1, kx[z - 40], qf[(z - 40) >> 1]); else MF1(s0, kx[z - 40], qf[(z - 40) >> 1]); }
            else if constexpr (z < 54) PV(z - 48, 0);
            else if constexpr (z == 54) MF0(sm, kmf[0], qf[0]);
            else if constexpr (z < 58) MF1(sm, kmf[z - 54], qf[z - 54]);
            else if constexpr (z < 62) PV(z - 52, 0);
            else if constexpr (z < 64) PV(z - 62, 1);
            else if constexpr (z == 64) MF0(oa, vx[0], P);
            else if constexpr (z == 65) MF0(ob, vx[1], P);
            else if constexpr (z == 66) MF0(rsum, ones, P);
            else if constexpr (z < 75) PV(z < 70 ? z - 63 : z == 70 ? 2 : z == 71 ? 7 : z == 72 ? 3 : z - 65, 1);
            else { const v2u ones2 = ones.lo; MF1H(rsum, ones2, pm); }
            if constexpr (z < 6) A[4 + z] = lds_r16(LB + C_MEM + (4 + z) * 1024);
            if constexpr (z >= 6 && z < 10) LF_RD(4 + ((z - 6) >> 1), z & 1);
            if constexpr (z >= 14 && z < 18) LF_RD(6 + ((z - 14) >> 1), z & 1);
            if constexpr (z >= 22 && z < 28) KXR(z - 22);
            if constexpr (z == 28) XW(qw);
            if constexpr (z == 29 || z == 30) KXR(z - 23);
            if constexpr (z == 32 || z == 47) asm volatile("s_barrier" ::: "memory");
            if constexpr (z >= 33 && z < 37) qf[z - 33] = lds_r16(qr + (z - 33) * 64);
            if constexpr (z == 20) ZRD(av0, 0);
            if constexpr (z == 43) XW(kw);
            if constexpr (z == 44 || z == 45) vx[z - 44] = lds_r16(VX + (z - 44) * 1024);
            if constexpr (z >= 48 && z < 52) kmf[z - 48] = lds_r16(qr + (C_KM - C_Q) + (z - 48) * 64);
            if constexpr (z == 52) ZRD(av1, 1);
            if constexpr (z == 37) ones = lds_r16(ZP + 64);
            if constexpr (z >= 57 && z < 64) LF_RD(8 + ((z - 57) >> 1), (z - 57) & 1);
            if constexpr (z >= 65 && z < 70) LF_RD(8 + ((z - 58) >> 1), (z - 58) & 1);
            if constexpr (z == HD_SMP) LD4A(fl, fkh, 0);
            SCHED();
            if constexpr (z == 19) HT(2);
            if constexpr (z == 32) HT(3);
            if constexpr (z == 47) HT(4);
            if constexpr (z == 64) HT(5);
            if constexpr (z == 19) LGK(4);
            if constexpr (z == 21 || z == 41) LGK(1);
            if constexpr (z == 56 || z == 73) LGK(0);
            if constexpr (z == 31 || z == 46) LGK(2);
            if constexpr (z == 39) LGK(4);
            if constexpr (z >= 53 && z < 56) LGK(57 - z);
            if constexpr (z == 64) LGK(3);
            if constexpr (z == 68) LGK(5);
            if constexpr (z >= 70 && z < 73) LGK(74 - z);
            SCHED();
            if constexpr (z == 18) CADD(fa, aq0[0], aq0[2]);
            if constexpr (z == 19) CADD(fb, aq0[1], aq0[3]);
            if constexpr (z == 20) CMULC(fa, fa);
            if constexpr (z == 21) CADD(fc, aq1[0], aq1[2]);
            if constexpr (z == 22) CADD(fd, aq1[1], aq1[3]);
            if constexpr (z == 23) CMULC(fc, fc);
            if constexpr (z == 24) CMULC(fb, fb);
            if constexpr (z == 25) CMULC(fd, fd);
            if constexpr (z == 26) CCVT(pa, fa, fc);
            if constexpr (z == 27) CCVT(pb, fb, fd);
            if constexpr (z == 38) CADD(fa, ak0[0], ak0[2]);
            if constexpr (z == 39) CADD(fb, ak0[1], ak0[3]);
            if constexpr (z == 41) { CADD(fc, ak1[0], ak1[2]); CADD(fd, ak1[1], ak1[3]); }
            if constexpr (z == 42) { CCVT(pa, fa, fc); CCVT(pb, fb, fd); }
            if constexpr (z >= 49 && z < 53) SEL(sx[z - 49], s0[z - 49], s1[z - 49]);
            if constexpr (z >= 53 && z < 57) VEXP(t[z - 53], sx[z - 53]);
            if constexpr (z == 57) CCVT(e01, t[0], t[1]);
            if constexpr (z == 58) CCVT(e23, t[2], t[3]);
            if constexpr (z == 59) QPERM(P.x, e01, "[0,1,0,1]");
            if constexpr (z == 60) QPERM(P.z, e01, "[2,3,2,3]");
            if constexpr (z == 61) QPERM(P.y, e23, "[0,1,0,1]");
            if constexpr (z == 62) QPERM(P.w, e23, "[2,3,2,3]");
            if constexpr (z == 63) CADD(fa, av0[0], av0[2]);
            if constexpr (z == 64) { asm volatile("" :: "v"(sm)); VEXP(tm0, sm[0]); }
            if constexpr (z == 65) VEXP(tm1, sm[1]);
            if constexpr (z == 66) CADD(fb, av0[1], av0[3]);
            if constexpr (z == 67) CCVT(pmm, tm0, tm1);
            if constexpr (z >= 68 && z < 72) SEL(oa[z - 68], oa[z - 68], ob[z - 68]);
            if constexpr (z == 72) asm volatile("v_cndmask_b32 %0, %1, 0, %2" : "=v"(pm.x) : "v"(pmm), "s"(himask));
            if constexpr (z == 73) asm volatile("v_cndmask_b32 %0, 0, %1, %2" : "=v"(pm.y) : "v"(pmm), "s"(himask));
            if constexpr (z == 74) LGK(15);
            if constexpr (z == 75) CCVT(vm.x, fa, fb);
            SCHED();
        };
        {
            u32 tmp;
            asm volatile("s_mov_b32 m0, %2\n\t" WAIT_TXT("%0", "%1") "buffer_load_dwordx4 %4, %5, %6 offen sc1 lds\n\tbuffer_load_dwordx4 %4, %5, %6 offen offset:1024 sc1 lds\n\t"
                         "s_mov_b32 m0, %3\n\ts_nop 0\n\tbuffer_load_dwordx4 %4, %5, %7 offen sc1 lds\n\ts_waitcnt vmcnt(0) lgkmcnt(0)\n\ts_barrier"
                         : "=&s"(tmp) : "s"(uu(tm - 4u)), "s"(dm), "s"(dm2), "v"(l16), "s"(srd), "s"(ms), "s"(ms2) : "vcc", "scc", "memory");
        }
        HT(1);
#pragma unroll
        for (int ks = 0; ks < 4; ++ks) A[ks] = lds_r16(LB + C_MEM + ks * 1024);
        SCHED();
        st4s(rs, ofl, 0, sv);
        SCHED();
        static_for<0, 76>(slot);
        asm volatile("s_nop 2\n\tv_add_f32 %1, %3, %5\n\tv_add_f32 %2, %4, %6\n\tv_cvt_pk_bf16_f32 %0, %1, %2"
                     : "=v"(vm.y), "=&v"(fc), "=&v"(fd) : "v"(av1[0]), "v"(av1[1]), "v"(av1[2]), "v"(av1[3]));
        __builtin_amdgcn_s_waitcnt(0xCF7F);
        asm volatile("s_nop 0\n\tv_mfma_f32_16x16x16_bf16 %0, %2, %3, %0\n\tv_rcp_f32 %1, %4" : "+v"(oa), "=&v"(inv) : "v"(vm), "v"(pm), "v"(rsum[0]), "v"(rsum));
        SCHED();
        if (__builtin_expect(__builtin_amdgcn_ballot_w64(u_(rsum[0]) - 0x21800000u > 0x3C000000u) != 0ull, 0)) {
            bool miss = false;
#pragma unroll 1
            for (int i = 0; i < 10; ++i) { const v4u a = lds_r16(LB + C_MEM + i * 1024); miss |= a.x == PSENT || a.y == PSENT || a.z == PSENT || a.w == PSENT; }
            if (missed < SPIN_CAP && !all_(!miss)) {
                *(LAS u32*)(lds + C_DUMP + (w == 0 ? 0 : 64)) = ld4(rs, foff(F_ERR));
                asm volatile("" : "+v"(fl) :: "memory");
                tm = rdl(fl, 5);
                BAR();
                missed = uu(*(LAS u32*)(lds + C_DUMP)) != 0u ? SPIN_CAP : missed + 1;
                st4s(rs, missed == SPIN_CAP ? foff(F_ERR) : OOB, 0, 1u);
                BAR();
                continue;
            }
            float mx = fmaxf(fmaxf(fmaxf(sx[0], sx[1]), fmaxf(sx[2], sx[3])), fmaxf(sm[0], sm[1]));
            mx = max32(max16(fmaxf(mx, dpp<0x4E>(mx))));
#pragma unroll
            for (int i = 0; i < 4; ++i) t[i] = __builtin_amdgcn_exp2f(sx[i] - mx);
            e01 = pk(t[0], t[1]); e23 = pk(t[2], t[3]);
            pmm = pk(__builtin_amdgcn_exp2f(sm[0] - mx), __builtin_amdgcn_exp2f(sm[1] - mx));
            asm volatile("v_cndmask_b32 %0, %2, 0, %3\n\tv_cndmask_b32 %1, 0, %2, %3" : "=&v"(pm.x), "=&v"(pm.y) : "v"(pmm), "s"(himask));
            asm volatile("s_nop 1" : "+v"(e01), "+v"(e23), "+v"(pm));
            QPERM(P.x, e01, "[0,1,0,1]"); QPERM(P.z, e01, "[2,3,2,3]"); QPERM(P.y, e23, "[0,1,0,1]"); QPERM(P.w, e23, "[2,3,2,3]");
            NOPS(1);
            MF0(rsum, ones, P); MF0(oa, vx[0], P); MF0(ob, vx[1], P);
            NOPS(7);
            { const v2u ones2 = ones.lo; MF1H(rsum, ones2, pm); }
#pragma unroll
            for (int i = 0; i < 4; ++i) SEL(oa[i], oa[i], ob[i]);
            NOPS(1);
            MF1H(oa, vm, pm);
            asm volatile("s_nop 7\n\tv_rcp_f32 %0, %1" : "=v"(inv) : "v"(rsum[0]), "v"(rsum));
        }
        {
            v4u oc; f32x2 u0 = oa.lo, u1 = oa.hi; const f32x2 i2 = {inv, inv};
            asm volatile("s_nop 0\n\tv_pk_mul_f32 %0, %0, %2\n\tv_pk_mul_f32 %1, %1, %2" : "+v"(u0), "+v"(u1) : "v"(i2));
            __builtin_amdgcn_s_waitcnt(0xCF7F);
            asm volatile("v_cvt_pk_bf16_f32 %0, %4, %5\n\tv_cvt_pk_bf16_f32 %2, %4, %5\n\tv_cvt_pk_bf16_f32 %1, %6, %7\n\tv_cvt_pk_bf16_f32 %3, %6, %7\n\t"
                         "v_permlane16_swap_b32 %0, %2\n\ts_nop 0\n\tv_permlane16_swap_b32 %1, %3"
                         : "=&v"(oc.x), "=&v"(oc.y), "=&v"(oc.z), "=&v"(oc.w) : "v"(u0.x), "v"(u0.y), "v"(u1.x), "v"(u1.y));
            st16(rs, oso, oc);
        }
        SCHED();
        unsigned long long clk;
        asm volatile("s_memrealtime %0" : "=s"(clk) :: "memory");
        HT(6);
        v4u tq[4], tk[8], tv[2];
        f32x4 c0, c1, cm;
        u32 oo, kvo; bool kvok;
        u32 ln = l16; PIN(ln);
        lptr QX = lds + C_QX + (w & 1) * 4096 + ln, VO = lds + C_V + (w ^ 1) * 2048 + ln, VMX = lds + C_VM + w * 512 + (ln >> 1);
#pragma unroll
        for (int i = 0; i < 2; ++i) { tq[i] = lds_r16(QX + i * 1024); kmf[i] = lds_r16(qr + (C_KM - C_Q) + i * 64); tk[2 * i] = lds_r16(LB + C_K + i * 1024); tk[2 * i + 1] = lds_r16(LB + C_K + 4096 + i * 1024); }
        *(LAS v2u*)VMX = vm;
        SCHED();
        SFOR(i, 12)
            if constexpr (i == 0) MF0(cm, kmf[0], tq[0]); else if constexpr (i == 1) MF0(c0, tk[0], tq[0]); else if constexpr (i == 2) MF0(c1, tk[1], tq[0]);
            else if constexpr (i % 3 == 0) MF1(cm, kmf[i / 3], tq[i / 3]); else if constexpr (i % 3 == 1) MF1(c0, tk[2 * (i / 3)], tq[i / 3]); else MF1(c1, tk[2 * (i / 3) + 1], tq[i / 3]);
            if constexpr (i < 2) kmf[2 + i] = lds_r16(qr + (C_KM - C_Q) + (2 + i) * 64);
            if constexpr (i == 0) { tq[2] = lds_r16(QX + 2048); tk[4] = lds_r16(LB + C_K + 2048); }
            if constexpr (i == 1) { tk[5] = lds_r16(LB + C_K + 6144); tq[3] = lds_r16(QX + 3072); }
            if constexpr (i == 2) { tk[6] = lds_r16(LB + C_K + 3072); tk[7] = lds_r16(LB + C_K + 7168); }
            if constexpr (i == 3) { tv[0] = lds_r16(VO); tv[1] = lds_r16(VO + 1024); }
            if constexpr (i == 4) { oo = ln + (ln & 0x300u) + ((vsrc ^ 8192u) + (u32)((w & 1) * 256) + ((u32)s & OOB)); PIN(oo); }
            if constexpr (i == 5) { ksrc += NH * 16384; vsrc += NH * 16384; PINS(ksrc); PINS(vsrc); }
            if constexpr (i == 9) { asm volatile("s_waitcnt vmcnt(1)" : "+v"(fl) :: "memory"); kvok = ((u32)__builtin_amdgcn_ballot_w64(fl < (u32)s + 2) & 7u) == 0u; }
            if constexpr (i == 10) { kvo = l16 | (kvok ? 0u : OOB); PIN(kvo); }
            SCHED();
        SEND
        asm volatile("s_waitcnt lgkmcnt(0)\n\ts_barrier" : "+s"(clk) :: "memory");
        const v2u tvm = *(LAS v2u*)(VMX + (512 - (w & 1) * 1024));
        const v4u pv = lds_r16(ZP + 80);
        DMA32(dk, kvo, ksrc);
        SCHED(); asm volatile("" :: "v"(cm));
        float u[10], m, m2;
        {
            float NEG = -1000000000.0f / SCALE; PIN(NEG);
#define MSK(d, a, i) asm volatile("v_cndmask_b32 %0, %1, %2, %3" : "=v"(d) : "v"(NEG), "v"(a), "s"(vis[i]))
#pragma unroll
            for (int i = 0; i < 4; ++i) MSK(u[i], c0[i], i);
#pragma unroll
            for (int i = 0; i < 4; ++i) MSK(u[4 + i], c1[i], 4 + i);
            asm volatile("v_max3_f32 %0, %2, %3, %10\n\tv_max3_f32 %1, %4, %5, %11\n\tv_max3_f32 %0, %0, %6, %7\n\tv_max3_f32 %1, %1, %8, %9\n\t"
                         "v_max_f32 %0, %0, %1\n\tv_mov_b32 %1, %0\n\ts_nop 1\n\tv_permlane16_swap_b32 %0, %1\n\tv_max_f32 %0, %0, %1\n\tv_mov_b32 %1, %0\n\ts_nop 1\n\t"
                         "v_permlane32_swap_b32 %0, %1\n\tv_max_f32 %0, %0, %1\n\tv_mul_f32 %0, 0xbe0293ee, %0"
                         : "=&v"(m), "=&v"(m2) : "v"(u[0]), "v"(u[1]), "v"(u[2]), "v"(u[3]), "v"(u[4]), "v"(u[5]), "v"(u[6]), "v"(u[7]), "v"(cm[0]), "v"(cm[1]));
        }
        DMA32(dv, kvo, vsrc);
        {
            v4u px; v2u p0, p1;
#define EX2(d, a, b) asm volatile("v_fmamk_f32 %0, %2, 0x3e0293ee, %4\n\tv_fmamk_f32 %1, %3, 0x3e0293ee, %4\n\tv_exp_f32 %0, %0\n\tv_exp_f32 %1, %1" : "=&v"(d.x), "=&v"(d.y) : "v"(a), "v"(b), "v"(m))
            f32x2 ex[5];
            EX2(ex[0], u[0], u[1]); EX2(ex[1], u[2], u[3]); EX2(ex[2], u[4], u[5]); EX2(ex[3], u[6], u[7]); EX2(ex[4], cm[0], cm[1]);
            asm volatile("v_cvt_pk_bf16_f32 %0, %8, %9\n\tv_cvt_pk_bf16_f32 %1, %10, %11\n\tv_cvt_pk_bf16_f32 %2, %12, %13\n\tv_cvt_pk_bf16_f32 %3, %14, %15\n\t"
                         "v_mov_b32 %5, 0\n\tv_mov_b32 %6, 0\n\tv_cvt_pk_bf16_f32 %4, %16, %17\n\tv_cvt_pk_bf16_f32 %7, %16, %17"
                         : "=&v"(px.x), "=&v"(px.y), "=&v"(px.z), "=&v"(px.w), "=&v"(p0.x), "=&v"(p0.y), "=&v"(p1.x), "=&v"(p1.y)
                         : "v"(ex[0].x), "v"(ex[0].y), "v"(ex[1].x), "v"(ex[1].y), "v"(ex[2].x), "v"(ex[2].y), "v"(ex[3].x), "v"(ex[3].y), "v"(ex[4].x), "v"(ex[4].y));
            f32x4 o[4], sum;
            const v2u ones2 = ones.lo;
            const u32 now = (u32)clk;
            u32 oz;
            SCHED();
            MF0(sum, ones, px); { const u32 q = ksrc ^ 8192u; DMA32(dq, kvo, q); } SCHED();
            MF0(o[0], vx[0], px);
            MF0(o[1], vx[1], px);
            MF0(o[2], tv[0], px); oz = (oso & ~(3u << 17)) | (u32)((s + 3) & 3) << 17; PIN(oz); SCHED();
            MF0(o[3], tv[1], px); oso = (oso & ~(3u << 17)) | (u32)((s + 1) & 3) << 17; PIN(oso); SCHED();
            MF1H(sum, ones2, p0);
            MF1H(o[0], vm, p0);
            MF1H(o[1], vm, p1); st16(rs, oz, pv); SCHED();
            MF1H(o[2], tvm, p0);
            MF1H(o[3], tvm, p1);
            asm volatile("s_nop 2\n\tv_rcp_f32 %0, %1\n\ts_nop 0" : "=v"(inv) : "v"(sum[0]), "v"(sum));
            asm volatile("" : "+v"(o[0]), "+v"(o[1]));
            st16(rs, oo, (v4u){pk2(o[0].lo * inv), pk2(o[0].hi * inv), pk2(o[1].lo * inv), pk2(o[1].hi * inv)});
            asm volatile("" : "+v"(o[2]), "+v"(o[3]));
            st16(rs, oo ^ 2048u, (v4u){pk2(o[2].lo * inv), pk2(o[2].hi * inv), pk2(o[3].lo * inv), pk2(o[3].hi * inv)});
            SCHED();
            LS2(L == 0 && w == 0, h);
            st4s(rs, (h == 0 && tid == 192) ? hint + 128u : OOB, 0, (u32)s << 16 | min(missed, 1u) << 14 | min(now - tm, 0x3FFFu));
            tm = rdl(fl, 5);
            missed = missed == SPIN_CAP ? missed : 0u;
            if (__builtin_expect((int)(rdl(fl, 3) - now) < CH_VIS - 4 || s < 1 - L, 0)) {
                const bool first = s == 0 && L == 0;
                if (s < 0) { oso -= (ln & 256u) ? 0u : OOB; tm = L ? tm : 0u; st4s(rs, tid == 0 ? hint + 4096u + (u32)h * 128u : OOB, 0, 0u); }
#pragma unroll 1
                for (int n = 0; n < (s < 0 ? 0 : first ? 200 : 8); ++n) {
                    CFENCE();
                    const u32 hv = ld4(rs, fkh);
                    tm = rdl(hv, first ? 6 : 5);
                    if (first ? tm != PSENT : (int)(rdl(hv, 3) - now) >= CH_VIS - 4) break;
                }
            }
        }
        if (__builtin_expect(!kvok, 0) && s + 1 < NSEG) {
            st4s(rs, ln == 0 ? hint + 4096u + (u32)h * 128u : OOB, 0, (u32)s + 1u);
            keys_wait((u32)s + 2u);
        }
        ++s;
    }
    DRAIN(); BAR();
    st4s(rs, ofl, 0, NSEG);
}

#define WO_AH 1
DEV void role_wo(const Params& p, lptr lds, int j, v2u ep) {
    const int tid = threadIdx.x, lane = tid & 63, w = uni(tid >> 6), g = lane >> 4, r = lane & 15;
    const int rg = j & 1, cg = j >> 1;
    const rsrc_t rs = mk_rsrc(p.ws, W_END);
    v8u wo[8][5];
    {
        const v4u so_ = mk_srd(p.Wo, DM * NQ * 2);
        u32 vo[5];
#pragma unroll
        for (int i = 0; i < 5; ++i) { const int n = 64 * i + lane; vo[i] = (u32)((n / 5) * DM + cg * 40 + (n % 5) * 8) * 2u; }
        v4u junk = {};
        const u32 av = (u32)(j * 5120 + lane * 16);
        auto ahead = [&](int b) {
#pragma unroll
            for (int i = 0; i < 5; ++i) AHEAD(so_, av, uu((8 * w + b) * (DH * DM * 2) + i * 1024));
        };
        auto fill = [&](int b) {
            if (b + WO_AH < 8) ahead(b + WO_AH);
            const u32 dst = uu(w * 40960 + (b & 3) * 10240), src = uu((8 * w + b) * (DH * DM * 2));
#pragma unroll
            for (int i = 0; i < 10; ++i) DMAIN(so_, dst + i * 1024, vo[i % 5], src + (i / 5) * (64 * DM * 2));
        };
#pragma unroll 1
        for (int b = 0; b < WO_AH; ++b) ahead(b);
#pragma unroll 1
        for (int b = 0; b < 4; ++b) fill(b);
        const int q4 = r >> 2;
        lptr tb = lds + w * 40960 + (32 * (g >> 1) + 16 * (q4 >> 1) + 8 * ((r >> 1) & 1) + 4 * (g & 1) + (q4 & 1)) * 80 + (r & 1) * 8;
        SFOR(b, 8)
            VMW((b < 5 ? 30 : 70 - 10 * b) + (b + 1 + WO_AH < 8 ? 5 : 0) + (b + 2 + WO_AH < 8 ? 5 : 0) + (b + 3 + WO_AH < 8 ? 5 : 0));
            if constexpr (b + WO_AH == 7) PIN(junk);
#pragma unroll
            for (int t = 0; t < 5; ++t) {
                lptr q = tb + (b & 3) * 10240 + t * 16;
                wo[b][t] = tr_frag(q, q + 160, q + 64 * 80, q + 64 * 80 + 160);
                if (b * 5 + t < 32) asm volatile("" : "+a"(wo[b][t])); else asm volatile("" : "+v"(wo[b][t]));
            }
            CFENCE();
            if constexpr (b < 4) fill(b + 4);
        SEND
    }
    if (j == 0) LST(265, rt32());
    LST(1100 + j * 4 + w, rt32());
    const u32 idx = (r & 2) ? 0xEEEEu : 0x4444u;
    const u32 pol0 = pol_wait(rs, ep);
    BAR();
    const u32 l16 = (u32)lane * 16u;
    u32 oo[8];
#pragma unroll
    for (int i = 0; i < 8; ++i) oo[i] = uu(W_ORING + ring_map((u32)(rg * 32768 + (8 * w + i) * 1024), pol0));
    const u32 hint = W_MRING + 4 * 32768u + pol0 * 8192u;
    u32 hvo[2];
#pragma unroll
    for (int l = 0; l < 2; ++l) hvo[l] = lane < 2 ? hint + (u32)(l ^ 1) * (MB / 4) + (u32)lane * 4u : lane == 2 ? hint + (u32)l * (MB / 4) + 128u : lane == 3 ? foff(F_ERR) :
                 (lane >= 32 && j == 0 && w == 3) ? hint + (u32)l * (MB / 4) + (u32)lane * 128u : OOB;
    lptr xw = r < 8 ? lds + w * 640 + (2 * (g & 1) + (g >> 1)) * 32 + r * 4 : lds + 8192 + lane * 4;
    const int G = ((5 * cg) >> 1) + w, tt = 2 * G + ((lane >> 3) & 1) - 5 * cg;
    const bool pub = w < 3 && lane < 32 && tt >= 0 && tt < 5;
    lptr xr = lds + (pub ? tt * 128 + (2 * (lane >> 4) + ((lane >> 2) & 1)) * 32 + (lane & 3) * 8 : 0);
    const u32 mpo = pub ? W_MRING + ring_map((u32)(G * 256 + rg * 128 + lane * 4), pol0) : OOB;
    constexpr u32 PER0 = CH_W0 + CH_MW + CH_H0 + CH_MH + 2 * CH_VIS, DW0 = CH_W0 + CH_VIS + CH_MW + CH_REC;
    const bool lead = j == 0 && w == 3;
    u32 hso = OOB;
    MK_SRD();
    u32 t = 0, tn = 0, tm0 = 0, dry = 2;
    int sn[2] = {0, 0};
    u32 hav = 4 * CH_H0, wav = 4 * CH_W0, mh = 8 * CH_MH, mw = 8 * CH_MW, ok[2] = {0u, 0u}, dw[2] = {DW0, DW0};
    auto start = [&] {
        if (w == 0) LST(1310 + j, rt32());
        if (lead) {
#pragma unroll 1
            for (int n = 0; n < SPIN_CAP; ++n) {
                CFENCE();
                const u32 a = ld4(rs, foff(F_QKV + lane)), b = ld4(rs, lane < 32 ? foff(F_QKV + 64 + lane) : hvo[0]);
                if (all_(a != 0u && b != (lane < 32 ? 0u : PSENT))) break;
                __builtin_amdgcn_s_sleep(1);
            }
        }
        if (lead) {
            t = rt32() + CH_T0; tn = t + PER0 / 2 + CH_REC;
            st16(rs, lane < 2 ? hint + (u32)lane * (MB / 4) : OOB, (v4u){lane == 0 ? t : tn, PER0, (lane == 0 ? t : tn) + DW0, t + DW0});
        } else {
#pragma unroll 1
            for (int n = 0; n < SPIN_CAP; ++n) {
                CFENCE();
                const u32 hv = ld4(rs, hvo[1]);
                t = rdl(hv, 0);
                if (rdl(hv, 1) < 400u) break;
                __builtin_amdgcn_s_sleep(1);
            }
        }
        hso = (lead && lane == 0) ? hint : OOB; tm0 = t + DW0; dw[0] = dw[1] = DW0; dry = 0;
    };
    u32 zero = 0, hv = 0xFFFFu, pst = 1, pdur = 0, pmp = OOB;
    const u32 psent = PSENT;
    lptr sw = lds + 2560 + w * 4;
    bool stop = false;
    asm volatile("" : "+v"(zero), "+v"(sw));
    auto turn = [&](auto lc) __attribute__((always_inline)) {
        constexpr int L = decltype(lc)::value, M = L ^ 1;
        u32 now, done, rep, rd, lost, dh, dn, nt, za; v4u hw;
        const int sc = sn[L];
        auto books = [&](auto ic) __attribute__((always_inline)) {
            constexpr int i = decltype(ic)::value;
            if constexpr (i == 0) { rep = rdl(hv, 2); PINS(rep); }
            if constexpr (i == 1) { lost = (u32)(__builtin_amdgcn_ballot_w64(hv == (u32)sn[M]) >> 32); PINS(lost); }
            if constexpr (i == 2) { lost |= (rep >> 16) ^ (u32)sn[M]; PINS(lost); }
            if constexpr (i == 3) { lost |= ok[M] ^ 2u; rd = rep & 0xFFFFu; PINS(lost); PINS(rd); }
            if constexpr (i == 4) { lost = lost == 0u ? CH_UP * 8u : 0u; PINS(lost); }
            if constexpr (i == 5) { dn = wav >> 2; PINS(dn); pdur = min(pdur, dn + 8u); PINS(pdur); }
            if constexpr (i == 6) { pdur = (done & ok[M]) ? pdur : dn; PINS(pdur); }
            if constexpr (i == 7) { wav += pdur - dn; PINS(wav); }
            if constexpr (i == 8) { dh = hav >> 2; PINS(dh); nt = min(rd, dh + 8u); PINS(nt); }
            if constexpr (i == 9) { nt = rd < 0x3FFFu ? nt : dh; PINS(nt); }
            if constexpr (i == 10) { hav += nt - dh; PINS(hav); }
            if constexpr (i == 11) { mw = max(mw - 1u, CH_MW * 8u); PINS(mw); }
            if constexpr (i == 12) { mh = max(mh - 1u, CH_MH * 8u); PINS(mh); }
            if constexpr (i == 13) { rd = rd >> 14 == 1u ? lost : 0u; PINS(rd); }
            if constexpr (i == 14) { mw += rd; mh += lost - rd; PINS(mw); PINS(mh); }
            if constexpr (i == 15) { ok[M] = done ? 1u : (ok[M] & 1u) << 1; PINS(ok[M]); }
            if constexpr (i == 16) { dh = (2u * hav + mh) / 8u + (sn[1] < 3 ? CH_VIS + CH_PRE : CH_VIS); PINS(dh); }
            if constexpr (i == 17) { dn = (2u * wav + mw) / 8u + CH_VIS; PINS(dn); }
            if constexpr (i == 18) { nt = dw[L] + dh; PINS(nt); }
            if constexpr (i == 19) { dh += dn; dw[L] = dn; sn[M] += (int)done; PINS(dh); PINS(sn[M]); }
            if constexpr (i == 20) { rd = tn - t + (dh >> 1); PINS(rd); }
            if constexpr (i == 21) { rd = sn[M] + 1 < NSEG ? rd : 0u; PINS(rd); }
            if constexpr (i == 22) { nt = t + (u32)max((int)nt, (int)rd); PINS(nt); }
            if constexpr (i == 23) { hw.x = nt; hw.y = dh; PIN(hw.x); PIN(hw.y); }
            if constexpr (i == 24) { hw.z = nt + dn; hw.w = tm0; PIN(hw); }
            if constexpr (i == 25) st16(rs, hso + L * (MB / 4), hw);
        };
        if (__builtin_expect(sc + 1 < NSEG, 1)) {
            u32 vo = l16 + (L * (MB / 2) + (((u32)sc + dry) & 3u) * 131072u), mp, hn;
            PIN(vo);
#define WT(k) do {} while (0)
            WT(0);
            v4u d[8];
            {
                u32 tmp;
                asm volatile(WAIT_TXT("%1", "%2") "buffer_load_dwordx4 %0, %3, %4, %5 offen sc1\n\ts_waitcnt lgkmcnt(0)"
                             : "=&v"(d[0]), "=&s"(tmp) : "s"(uu(t - 4u)), "v"(vo), "s"(srd), "s"(oo[0]) : "vcc", "scc", "memory");
            }
            SFOR(i, 7)
                asm volatile("buffer_load_dwordx4 %0, %1, %2, %3 offen sc1" : "=v"(d[i + 1]) : "v"(vo), "s"(srd), "s"(oo[i + 1]) : "memory");
                SCHED();
                if constexpr (i == 0) { done = uu(pst) ^ 1u; PINS(done); }
                static_for<3 * i, 3 * i + 3>(books);
                SCHED();
            SEND
            hn = ld4(rs, hvo[L]);
            SCHED();
            WT(1);
            static_for<21, 26>(books);
            za = done ? pmp : OOB;
            st4s(rs, za, 0, psent);
            *(LAS u32*)(sw + L * 4096) = zero;
            f32x4 acc[5] = {};
            mp = mpo + (L * (MB / 4) + (u32)((sc + 1) & 3) * 32768u); pmp = mpo + (L * (MB / 4) + (u32)(sc & 3) * 32768u);
            asm volatile("" : "+v"(acc[0]), "+v"(acc[1]), "+v"(acc[2]), "+v"(acc[3]), "+v"(acc[4]), "+v"(mp), "+v"(pmp));
            SCHED();
            WT(2);
            float f0[5], f1[5]; unsigned long long bad;
#define WSW(o_) asm volatile("v_permlane32_swap_b32 %0, %1" : "+v"(f0[o_]), "+v"(f1[o_]))
#define WAD(o_) asm volatile("v_add_f32_dpp %0, %1, %0 row_ror:8 row_mask:0xf bank_mask:0xf" : "+v"(f0[o_]) : "v"(f1[o_]))
#define WFO(o_) do { VADD(f0[o_], acc[o_][0], acc[o_][2]); VADD(f1[o_], acc[o_][1], acc[o_][3]); } while (0)
#define WWR(o_) *(LAS float*)(xw + L * 4096 + (o_) * 128) = f0[o_]
            asm volatile("s_waitcnt vmcnt(10)" : "+v"(d[0]));
            if (__builtin_expect(__builtin_amdgcn_ballot_w64(d[0].x == PSENT + dry) != 0ull, 0)) {
                asm volatile("s_waitcnt vmcnt(3)" : "+v"(d[1]), "+v"(d[2]), "+v"(d[3]), "+v"(d[4]), "+v"(d[5]), "+v"(d[6]), "+v"(d[7]));
#pragma unroll
                for (int i = 0; i < 5; ++i) *(LAS u32*)(xw + L * 4096 + i * 128) = psent;
                *(LAS u32*)(sw + L * 4096) = 1u;
            } else {
            SFOR(i, 40)
                constexpr int pc = i < 30 ? i / 5 : 6 + (i & 1), oc = i < 30 ? i % 5 : (i - 30) >> 1;
                if constexpr (oc == 0 && pc > 0) asm volatile("s_waitcnt vmcnt(%1)" : "+v"(d[pc]) : "i"(10 - pc));
                if constexpr (pc * 5 + oc < 32) SMF_A(acc[oc], d[pc], wo[pc][oc]); else SMF_V(acc[oc], d[pc], wo[pc][oc]);
                if constexpr (i == 0) WT(3);
                if constexpr (i == 20) WT(4);
                if constexpr (i == 31 || i == 33) NOPS(1);
                if constexpr (i == 32) NOPS(0);
                if constexpr (i == 34) WFO(0);
                if constexpr (i == 35) { asm volatile("v_cmp_u_f32 %0, %1, %2" : "=s"(bad) : "v"(f0[0]), "v"(f1[0])); WSW(0); }
                if constexpr (i == 36) WFO(1);
                if constexpr (i == 37) { WAD(0); WSW(1); }
                if constexpr (i == 38) { WWR(0); SCHED(); WFO(2); }
                if constexpr (i == 39) { WAD(1); WSW(2); }
                SCHED();
            SEND
            WT(5);
            WWR(1); SCHED(); WFO(3); WAD(2); WWR(2); SCHED(); WSW(3);
            if (__builtin_expect(bad != 0ull, 0)) {
                bool miss = false;
#pragma unroll
                for (int i = 0; i < 8; ++i) miss |= d[i].x == PSENT;
                if (all_(!miss)) {
#pragma unroll
                    for (int i = 0; i < 8; ++i) miss |= d[i].y == PSENT || d[i].z == PSENT || d[i].w == PSENT;
                }
                *(LAS u32*)(sw + L * 4096) = all_(!miss) ? 0u : 1u;
            }
            WFO(4); WAD(3); WWR(3); SCHED();
            asm volatile("v_permlane32_swap_b32 %0, %1\n\ts_nop 1\n\tv_add_f32_dpp %0, %1, %0 row_ror:8 row_mask:0xf bank_mask:0xf" : "+v"(f0[4]), "+v"(f1[4]));
            WWR(4);
            }
            WT(6);
            f32x2 a, b, c, e; u32 cw, mq; v4u sv;
            asm volatile("s_waitcnt lgkmcnt(0)\n\ts_barrier\n\tds_read_b64 %0, %5 offset:%7\n\tds_read_b64 %1, %5 offset:%7+640\n\tds_read_b64 %2, %5 offset:%7+1280\n\tds_read_b64 %3, %5 offset:%7+1920\n\t"
                         "ds_read_b128 %4, %6 offset:%7+2560\n\t"
                         "s_waitcnt lgkmcnt(3)\n\tv_pk_add_f32 %0, %0, %1\n\ts_waitcnt lgkmcnt(1)\n\tv_pk_add_f32 %2, %2, %3\n\ts_waitcnt lgkmcnt(0)\n\tv_pk_add_f32 %0, %0, %2"
                         : "=&v"(a), "=&v"(b), "=&v"(c), "=&v"(e), "=&v"(sv) : "v"(xr), "v"(zero), "i"(L * 4096) : "memory");
            asm volatile("v_cmp_u_f32 vcc, %2, %3\n\tv_cvt_pk_bf16_f32 %0, %2, %3\n\tv_cndmask_b32 %1, %4, %5, vcc" : "=&v"(cw), "=&v"(mq) : "v"(a.x), "v"(a.y), "v"(mp), "v"(OOB) : "vcc");
            st4s(rs, mq, 0, cw);
            SCHED();
            now = rt32();
            PIN(hn); hv = hn;
            WT(7);
            pst = sv.x | sv.y | sv.z | sv.w;
            if (__builtin_expect(__builtin_amdgcn_ballot_w64(mq != mp && pst == 0u) != 0ull, 0)) st4s(rs, mp, 0, cw);
        } else {
            done = uu(pst) ^ 1u;
            st4s(rs, done ? pmp : OOB, 0, psent);
            static_for<0, 25>(books);
            pst = 1;
            hv = ld4(rs, hvo[L]);
            PIN(hv);
            now = rt32();
        }
        pdur = now - t;
        const u32 per = rdl(hv, 1), th = rdl(hv, 0);
        t = lead ? tn : (int)(th - now) < -(int)(per >> 1) ? th + per : th;
        tn = nt;
        dw[M] += max((int)(now + CH_WX - t), 0);
        stop = stop || rdl(hv, 3) != 0u;
    };
    for (int k = 0; k < 2 * SPIN_CAP && !stop && (sn[0] + 1 < NSEG || sn[1] + 1 < NSEG); ++k) {
        if (k == 1) start();
        asm volatile("" : "+v"(pdur), "+v"(dw[0]), "+v"(dw[1])); pdur = uu(pdur); dw[0] = uu(dw[0]); dw[1] = uu(dw[1]);
        turn(IC<0>{}); turn(IC<1>{});
    }
    if (sn[0] + 1 < NSEG || sn[1] + 1 < NSEG) st4s(rs, tid == 0 ? foff(F_ERR) : OOB, 0, 1u);
}

__global__ void __launch_bounds__(256, 1) k_main(Params p) {
    lptr lds = (lptr)(u32)16 - 16;
    if (blockIdx.x == 0) LST(256, rt32());
    unsigned long long pc; int len;
    asm volatile("s_getpc_b64 %0\n.Lk_main_here:\n\ts_mov_b32 %1, .Lk_end_here-.Lk_main_here" : "=s"(pc), "=s"(len));
    const rsrc_t rs = mk_rsrc(p.ws, W_END);
    const v2u e0 = ld8s(rs, threadIdx.x == 0 ? foff(F_EPOCH) : OOB, 0);
    const v4u code = __builtin_amdgcn_raw_buffer_load_b128(mk_rsrc((const void*)(pc & ~15ull), (u32)max(len, 0)), (((blockIdx.x >> 3) & 31u) * 256u + threadIdx.x) * 16u, 0, 0);
    CFENCE();
    auto conv_id = [](int role) {
        const int x = role >> 5, i = role & 31, no = x < 5 ? 5 : 10, og = x < 5 ? x - 3 : 2 * (x - 4) + (i >= 5), r = (x - 3) * 5 + i - no, q = r / 3;
        return x < 3 || i >= no + 5 || r >= 24 ? NCV : i < no ? ((og & 1) ? (og >> 1) * 5 + i % 5 : NCV) : r % 3 == 0 ? 20 + (q & 1) * 20 + (q >> 1) : 16 + (r % 3) * 8 + q;
    };
    int role = (int)(((u32)__builtin_amdgcn_s_getreg(20 | (3 << 11)) & 7u) * 32u + (blockIdx.x >> 3));
    Early e;
    v4u rv;
#pragma unroll 1
    for (int n = 0;; ++n) {
        conv_early(p, rs, conv_id(role), e);
        if (n) break;
        CFENCE();
        if (threadIdx.x == 0) {
            GAS unsigned long long* slots = (GAS unsigned long long*)(p.ws + W_CTRL + F_SLOT * 128);
            const unsigned long long tag = (unsigned long long)e0.y << 32 | (e0.x + 1u);
            u32 mine = (u32)role;
#pragma unroll 1
            for (int k = 0; k < 256 && __hip_atomic_exchange(slots + mine * 16, tag, __ATOMIC_RELAXED, __HIP_MEMORY_SCOPE_AGENT) == tag; ++k) mine = (mine + 1u) & 255u;
            lds_w16(lds, (v4u){mine, e0.x, e0.y, 0u});
        }
        BAR();
        rv = lds_r16(lds);
        BAR();
        if (uni((int)rv.x) == role) break;
        role = uni((int)rv.x);
    }
    const int x = role >> 5, i = role & 31, w = uni(threadIdx.x >> 6), no = x < 5 ? 5 : 10, nr = x < 7 ? 5 : 4, j = conv_id(role);
    const v2u ep = {uu(rv.y), uu(rv.z)};
    asm volatile("" :: "v"(code));
    if (x < 2) { role_head(p, lds, i >> 4, x * 16 + (i & 15), ep); __builtin_amdgcn_endpgm(); }
    if (x == 2) { role_wo(p, lds, i, ep); __builtin_amdgcn_endpgm(); }
    if (i >= no + nr) { role_qkv(p, lds, (x < 5 ? (x - 3) * 22 : 44 + (x - 5) * 17) + i - no - nr, ep); __builtin_amdgcn_endpgm(); }
    const bool cv = j >= NPRE || (j >= 20 && j < 24);
    if (j < NCV) conv_stage(p, rs, lds, j, (j & 3) * 2 + (j >= NPRE), cv ? 40 : 0, ep, e);
    if (i >= no) {
        if (!cv) role_row(p, lds, (j - 24) * 4 + w, ep);
        __builtin_amdgcn_endpgm();
    }
    const int og = x < 5 ? x - 3 : 2 * (x - 4) + (i >= 5);
    role_out(p, lds, og & 1, og >> 1, i % 5, ep);
}
__global__ void k_end() { asm volatile(".Lk_end_here:"); }

extern "C" void kernel_launch(void* const* d_in, const int* in_sizes, int n_in, void* d_out, int out_size, void* d_ws, size_t ws_size,
                              hipStream_t stream) {
    Params p;
    p.x = (const u16*)d_in[0]; p.mem0 = (const u16*)d_in[1]; p.Wq = (const u16*)d_in[2]; p.Wk = (const u16*)d_in[3];
    p.Wv = (const u16*)d_in[4]; p.Wo = (const u16*)d_in[5];
    p.bk = (const u16*)d_in[8]; p.bv = (const u16*)d_in[9]; p.shift = (const u16*)d_in[10]; p.ssmk = (const u16*)d_in[11]; p.ssmD = (const u16*)d_in[12];
    p.out = (u16*)d_out;
    p.ws = (unsigned char*)(((size_t)d_ws + 2 * MB - 1) & ~(size_t)(2 * MB - 1));
    (void)hipFuncSetAttribute((const void*)k_main, hipFuncAttributeMaxDynamicSharedMemorySize, LDS_BYTES);
    hipLaunchKernelGGL(k_main, dim3(256), dim3(256), LDS_BYTES, stream, p);
}
```

```cpp
#include <hip/hip_runtime.h>
#include <hip/hip_bf16.h>

typedef unsigned short u16;
typedef unsigned int u32;
typedef u32 v2u __attribute__((ext_vector_type(2)));
typedef u32 v4u __attribute__((ext_vector_type(4)));
typedef u32 v8u __attribute__((ext_vector_type(8)));
typedef float f32x2 __attribute__((ext_vector_type(2)));
typedef float f32x4 __attribute__((ext_vector_type(4)));
typedef float f32x16 __attribute__((ext_vector_type(16)));
typedef __bf16 bf16x2 __attribute__((ext_vector_type(2)));
typedef __bf16 bf16x8 __attribute__((ext_vector_type(8)));
typedef short s16x4 __attribute__((ext_vector_type(4)));
#define LAS __attribute__((address_space(3)))
#define GAS __attribute__((address_space(1)))
typedef LAS unsigned char* lptr;
#define DEV __device__ __forceinline__

constexpr int SEQ = 2048, DM = 640, NH = 32, DH = 128, NSEG = 64, NQ = NH * DH;

constexpr u32 MB = 1u << 20;
constexpr u32 W_ORING = 0;
constexpr u32 W_MRING = MB;
constexpr u32 W_PROBE = 2 * MB - 65536;
constexpr u32 W_CTRL = 4 * MB;
constexpr u32 CTRL_BYTES = 128 * 1024;
constexpr u32 ACT = SEQ * DM * 2;
constexpr u32 W_H1 = 5 * MB, W_Y = W_H1 + ACT, W_H2 = W_Y + ACT, W_X2 = W_H2 + ACT;
constexpr u32 W_KQ = 16 * MB;
constexpr u32 W_VO = W_KQ + 64 * MB;
constexpr u32 W_P = W_VO + 64 * MB;
constexpr u32 P_ITEM = 32 * 4 * DM * 4;
constexpr u32 W_KVT = W_P + 2 * NSEG * P_ITEM;
constexpr u32 W_SP = W_KVT + ACT;
constexpr u32 W_END = W_KVT + 3 * MB;
static_assert((size_t)W_END + 2 * MB <= 256ull * MB, "workspace");
constexpr u32 F_ERR = 0, F_QKV = 16, F_O = F_QKV + 192, F_P = F_O + 64, F_H2 = F_P + 64, F_CLR = F_H2 + 32,
              F_EPOCH = F_CLR + 32, F_SLOT = F_EPOCH + 1, F_PRE = F_SLOT + 256, F_N = F_PRE + 192;
constexpr int NCV = 44, NPRE = 40;
static_assert(F_N * 128 <= CTRL_BYTES, "ctrl");
DEV u32 foff(u32 idx) { return W_CTRL + idx * 128u; }

constexpr u32 OOB = 0x80000000u;
constexpr u32 PSENT = 0x7F817F81u;
constexpr int SPIN_CAP = 40000;
constexpr int LDS_BYTES = 160 * 1024;

#define LS3(cond, slot, idx) do {} while (0)
#define LST(idx, v) do {} while (0)
#define LS2(cond, idx) do {} while (0)
struct Params {
    const u16 *x, *mem0, *Wq, *Wk, *Wv, *Wo, *bk, *bv, *shift, *ssmk, *ssmD;
    u16* out;
    unsigned char* ws;
};

typedef __amdgpu_buffer_rsrc_t rsrc_t;
DEV rsrc_t mk_rsrc(const void* p, u32 bytes) { return __builtin_amdgcn_make_buffer_rsrc((void*)p, 0, bytes, 0x00020000); }
DEV v4u ld16s(rsrc_t r, u32 off, u32 so) { return __builtin_amdgcn_raw_buffer_load_b128(r, off, so, 16); }
DEV v2u ld8s(rsrc_t r, u32 off, u32 so) { return __builtin_amdgcn_raw_buffer_load_b64(r, off, so, 16); }
DEV v4u ldin16(rsrc_t r, u32 off, u32 so) { return __builtin_amdgcn_raw_buffer_load_b128(r, off, so, 2); }
DEV v2u ldin8(rsrc_t r, u32 off, u32 so) { return __builtin_amdgcn_raw_buffer_load_b64(r, off, so, 2); }
DEV u32 ld4(rsrc_t r, u32 off) { return __builtin_amdgcn_raw_buffer_load_b32(r, off, 0, 16); }
DEV void st16(rsrc_t r, u32 off, v4u v) { __builtin_amdgcn_raw_buffer_store_b128(v, r, off, 0, 16); }
DEV void st8s(rsrc_t r, u32 off, u32 so, v2u v) { __builtin_amdgcn_raw_buffer_store_b64(v, r, off, so, 16); }
DEV void st4s(rsrc_t r, u32 off, u32 so, u32 v) { __builtin_amdgcn_raw_buffer_store_b32(v, r, off, so, 16); }
#define DRAIN() asm volatile("s_waitcnt vmcnt(0)" ::: "memory")
#define VMW(n) asm volatile("s_waitcnt vmcnt(%0)" :: "i"(n) : "memory")
#define BAR() asm volatile("s_waitcnt lgkmcnt(0)\n\ts_barrier" ::: "memory")
#define SCHED() __builtin_amdgcn_sched_barrier(0)
#define CFENCE() asm volatile("" ::: "memory")
#define NOPS(n) asm volatile("s_nop %0" :: "i"(n))
#define PIN(x) asm volatile("" : "+v"(x))
#define PINS(x) asm volatile("" : "+s"(x))

template <int V> struct IC { static constexpr int value = V; };
template <int I, int N, class F> DEV void static_for(F&& f) { if constexpr (I < N) { f(IC<I>{}); static_for<I + 1, N>(f); } }
#define SFOR(var, n) static_for<0, n>([&](auto ic_) __attribute__((always_inline)) { constexpr int var = decltype(ic_)::value;
#define SEND });

DEV u32 pk(float a, float b) { bf16x2 v = {(__bf16)a, (__bf16)b}; return __builtin_bit_cast(u32, v); }
DEV float lo_f(u32 u) { return __builtin_bit_cast(float, u << 16); }
DEV float hi_f(u32 u) { return __builtin_bit_cast(float, u & 0xffff0000u); }
DEV f32x2 up2(u32 u) { return (f32x2){lo_f(u), hi_f(u)}; }
DEV u32 pk2(f32x2 v) { return pk(v.x, v.y); }
DEV float rbf(float a) { return (float)(__bf16)a; }
DEV v4u lds_r16(lptr p) { return *(LAS v4u*)p; }
DEV void lds_w16(lptr p, v4u v) { *(LAS v4u*)p = v; }
DEV v2u lds_tr(lptr p) { return __builtin_bit_cast(v2u, __builtin_amdgcn_ds_read_tr16_b64_v4i16((LAS s16x4*)p)); }
DEV v4u tr2(lptr p0, lptr p1) { const v2u a = lds_tr(p0), b = lds_tr(p1); return (v4u){a.x, a.y, b.x, b.y}; }
DEV v8u cat8(v4u a, v4u b) { return __builtin_shufflevector(a, b, 0, 1, 2, 3, 4, 5, 6, 7); }
DEV f32x4 mfma16(v4u a, v4u b, f32x4 c) { return __builtin_amdgcn_mfma_f32_16x16x32_bf16(__builtin_bit_cast(bf16x8, a), __builtin_bit_cast(bf16x8, b), c, 0, 0, 0); }
DEV f32x16 mfma32(v4u a, v4u b, f32x16 c) { return __builtin_amdgcn_mfma_f32_32x32x16_bf16(__builtin_bit_cast(bf16x8, a), __builtin_bit_cast(bf16x8, b), c, 0, 0, 0); }

DEV float f_(u32 u) { return __builtin_bit_cast(float, u); }
DEV u32 u_(float f) { return __builtin_bit_cast(u32, f); }
template <int CTRL> DEV float dpp(float v) { return f_((u32)__builtin_amdgcn_update_dpp(0, (int)u_(v), CTRL, 0xf, 0xf, false)); }
DEV float max16(float v) { auto r = __builtin_amdgcn_permlane16_swap(u_(v), u_(v), false, false); return fmaxf(f_(r[0]), f_(r[1])); }
DEV float max32(float v) { auto r = __builtin_amdgcn_permlane32_swap(u_(v), u_(v), false, false); return fmaxf(f_(r[0]), f_(r[1])); }
DEV float add16(float v) { auto r = __builtin_amdgcn_permlane16_swap(u_(v), u_(v), false, false); return f_(r[0]) + f_(r[1]); }
DEV float add32(float v) { auto r = __builtin_amdgcn_permlane32_swap(u_(v), u_(v), false, false); return f_(r[0]) + f_(r[1]); }
DEV float wave_sum(float v) {
    v += dpp<0xB1>(v);
    v += dpp<0x4E>(v);
    v += dpp<0x124>(v);
    v += dpp<0x128>(v);
    return add32(add16(v));
}
DEV bool all_(bool c) { return __builtin_amdgcn_ballot_w64(!c) == 0ull; }
DEV int uni(int v) { return __builtin_amdgcn_readfirstlane(v); }
DEV u32 uu(u32 v) { return (u32)__builtin_amdgcn_readfirstlane((int)v); }
DEV u32 rdl(u32 v, int l) { return (u32)__builtin_amdgcn_readlane((int)v, l); }
DEV u32 rt32() { return (u32)__builtin_amdgcn_s_memrealtime(); }

#define WSL 2
DEV void wait_flags(rsrc_t rs, u32 off, u32 want, bool& dead) {
    if (dead) return;
#pragma unroll 1
    for (int n = 0;; ++n) {
        CFENCE();
        const u32 v = ld4(rs, off);
        if (all_(off >= OOB || v >= want)) break;
        __builtin_amdgcn_s_sleep(WSL);
        if ((n & 63) == 63) { if (uni((int)ld4(rs, foff(F_ERR))) != 0) { dead = true; break; } }
        if (n > SPIN_CAP) { st4s(rs, foff(F_ERR), 0, 1u); dead = true; break; }
    }
    CFENCE();
}

DEV void pre_post(rsrc_t rs, int idx, u32 stage, v2u ep) { st8s(rs, threadIdx.x == 0 ? foff(F_PRE + idx) : OOB, 0, (v2u){ep.x + stage, ep.y}); }
DEV void pre_poll(rsrc_t rs, int first, int n, u32 stage, v2u ep) {
    const int lane = threadIdx.x & 63;
#pragma unroll 1
    for (int it = 0; it < SPIN_CAP; ++it) {
        CFENCE();
        const v2u a = ld8s(rs, foff(F_PRE + first + min(lane, n - 1)), 0), b = ld8s(rs, foff(F_PRE + first + min(lane + 64, n - 1)), 0);
        if (all_(a.y == ep.y && b.y == ep.y && max(a.x - ep.x - stage, b.x - ep.x - stage) < 8u - stage)) break;
        __builtin_amdgcn_s_sleep(2);
    }
    CFENCE();
}
DEV void pre_wait(rsrc_t rs, int first, int n, u32 stage, v2u ep) {
    if (uni(threadIdx.x >> 6) == 0) pre_poll(rs, first, n, stage, ep);
    BAR();
}
DEV u32 pol_wait(rsrc_t rs, v2u ep) {
    const int lane = threadIdx.x & 63;
    v4u a;
#pragma unroll 1
    for (int it = 0; it < SPIN_CAP; ++it) {
        CFENCE();
        a = ld16s(rs, foff(F_PRE + (lane < NPRE || lane >= 48 ? lane : 0)), 0);
        if (all_(a.y == ep.y && a.x - ep.x - 1u < 7u)) break;
        __builtin_amdgcn_s_sleep(2);
    }
    CFENCE();
    return uu(wave_sum(lane >= 48 ? (float)(int)a.z : 0.f) >= 0.f ? 1u : 0u);
}
#define PROBE_T 1000
#define NPROBE 32
DEV u32 pol_probe(const Params& p, rsrc_t rs, int id) {
    const int lane = threadIdx.x & 63;
    const u32 off0 = W_PROBE + (u32)(id >> 2) * 16384u + (u32)(id & 3) * 128u;
    const u32 tend = rt32() + PROBE_T;
    st4s(rs, lane < 32 ? off0 + (u32)lane * 512 : OOB, 0, 0u); DRAIN();
    int calm = 0;
    int d[2] = {1 << 16, 1 << 16};
#pragma unroll 1
    for (int q = 0; q < NPROBE && ((q & 1) || (calm < 6 && (int)(rt32() - tend) < 0)); ++q) {
        const u32 half = (u32)(q ^ (q >> 1) ^ id) & 1u;
        const unsigned long long t0 = __builtin_amdgcn_s_memtime();
        const u32 v = ld4(rs, lane == 0 ? off0 + half * 8192 + (u32)(q >> 1) * 512 : OOB);
        asm volatile("s_waitcnt vmcnt(0)" :: "v"(v) : "memory");
        const int dt = (int)(__builtin_amdgcn_s_memtime() - t0);
        if (half) d[1] = min(d[1], dt); else d[0] = min(d[0], dt);
        calm += dt < 1100;
    }
    const u32 xcc = (u32)__builtin_amdgcn_s_getreg(20 | (3 << 11)) & 0xfu;
    return (u32)min(max((d[0] - d[1]) * ((xcc & 4u) ? -1 : 1), -250), 250);
}
DEV f32x2 ln_stats(const f32x2 (&x)[5]) {
    const float c = f_(uu(u_(x[0].x)));
    f32x2 d[5];
#pragma unroll
    for (int i = 0; i < 5; ++i) d[i] = x[i] - c;
    const f32x2 s2 = (d[0] + d[1]) + (d[2] + d[3]) + d[4], q2 = (d[0] * d[0] + d[1] * d[1]) + (d[2] * d[2] + d[3] * d[3] + d[4] * d[4]);
    float a = s2.x + s2.y, b = q2.x + q2.y, a2, b2;
#define LN_DPP(m) "v_add_f32_dpp %0, %0, %0 " m " row_mask:0xf bank_mask:0xf\n\tv_add_f32_dpp %1, %1, %1 " m " row_mask:0xf bank_mask:0xf\n\ts_nop 0\n\t"
    asm("s_nop 1\n\t" LN_DPP("quad_perm:[1,0,3,2]") LN_DPP("quad_perm:[2,3,0,1]") LN_DPP("row_ror:4")
        "v_add_f32_dpp %0, %0, %0 row_ror:8 row_mask:0xf bank_mask:0xf\n\tv_add_f32_dpp %1, %1, %1 row_ror:8 row_mask:0xf bank_mask:0xf\n\ts_nop 1\n\t"
        "v_permlane16_swap_b32 %0, %1\n\tv_add_f32 %0, %0, %1\n\tv_mov_b32 %1, %0\n\ts_nop 1\n\tv_permlane32_swap_b32 %0, %1\n\tv_add_f32 %0, %0, %1\n\ts_nop 0\n\t"
        "v_readlane_b32 %2, %0, 0\n\tv_readlane_b32 %3, %0, 16\n\ts_nop 1"
        : "+v"(a), "+v"(b), "=s"(a2), "=s"(b2));
    const float m = a2 * (1.0f / DM), var = fmaxf(b2 * (1.0f / DM) - m * m, 0.f);
    const float sd = rbf(__builtin_amdgcn_sqrtf(rbf(rbf(var) + 1e-5f)));
    return (f32x2){rbf(c + m), __builtin_amdgcn_rcpf(sd)};
}
DEV u32 pkv(f32x2 v) { u32 d; asm("v_cvt_pk_bf16_f32 %0, %1, %2" : "=v"(d) : "v"(v.x), "v"(v.y)); return d; }
DEV void store_h(rsrc_t rs, u32 base, int row, const u32 (&hp)[5]) {
    const u32 lane = threadIdx.x & 63;
    const u32 sw = (u32)(row & 15) * 16u, o8 = (lane >> 5) * 8192u + (((lane & 31u) * 8u) ^ sw), hb = base + (u32)(row >> 5) * (32 * DM * 2) + (u32)(row & 31) * 256u;
    st8s(rs, o8, hb, (v2u){hp[0], hp[1]});
    st8s(rs, o8, hb + 16384, (v2u){hp[2], hp[3]});
    st4s(rs, (lane * 4) ^ sw, hb + 32768, hp[4]);
}

#define DMA16(ldsb, vo, so) asm volatile("s_mov_b32 m0, %0\n\ts_nop 0\n\tbuffer_load_dwordx4 %1, %2, %3 offen sc1 lds" :: "s"(ldsb), "v"(vo), "s"(srd), "s"(so) : "memory")
#define DMA16I(ldsb, lo, vo, so, go) do { u32 so_; asm volatile("s_add_u32 m0, %1, %5\n\ts_add_u32 %0, %4, %6\n\tbuffer_load_dwordx4 %2, %3, %0 offen sc1 lds" \
                         : "=&s"(so_) : "s"(ldsb), "v"(vo), "s"(srd), "s"(so), "i"(lo), "i"(go) : "memory", "scc"); } while (0)
#define DMA32(ldsb, vo, so) asm volatile("s_mov_b32 m0, %0\n\ts_nop 0\n\tbuffer_load_dwordx4 %1, %2, %3 offen sc1 lds\n\tbuffer_load_dwordx4 %1, %2, %3 offen offset:1024 sc1 lds" \
                         :: "s"(ldsb), "v"(vo), "s"(srd), "s"(so) : "memory")
#define DMAIN(sr, ldsb, vo, so) asm volatile("s_mov_b32 m0, %0\n\ts_nop 0\n\tbuffer_load_dwordx4 %1, %2, %3 offen lds" :: "s"(ldsb), "v"(vo), "s"(sr), "s"(so) : "memory")
DEV v4u mk_srd(const void* q, u32 bytes) { return (v4u){uu((u32)(size_t)q), uu((u32)((size_t)q >> 32) & 0xffffu), bytes, 0x00020000u}; }
#define DMANT(sr, ldsb, vo, so) asm volatile("s_mov_b32 m0, %0\n\ts_nop 0\n\tbuffer_load_dwordx4 %1, %2, %3 offen nt lds" :: "s"(ldsb), "v"(vo), "s"(sr), "s"(so) : "memory")
#define AHEAD(sr, vo, so) asm volatile("buffer_load_dwordx4 %0, %1, %2, %3 offen" : "+v"(junk) : "v"(vo), "s"(sr), "s"(so) : "memory")
#define MK_SRD() const v4u srd = mk_srd(p.ws, W_END)

constexpr int CV_W = 40960;
template <bool B> struct CvL {
    static constexpr int NF = B ? 5 : 3, NK = B ? 4 : 2, KP = 544 + NK * 256, CH = NF * 1024 + 4 * KP;
};
constexpr int CV_Y = 4 * CvL<false>::CH, CV_YP = 272;
static_assert(CV_Y + 32 * CV_YP <= CV_W && 2 * CvL<true>::CH <= CV_Y, "conv lds");
DEV int cv_chan(int u, int i) { return 16 * (u % 40) + 4 * i + u / 40; }
template <bool B> DEV void cv_filt(const Params& p, u32 dst, int d, int i) {
    const u32 lane = threadIdx.x & 63;
    const v4u sf = mk_srd(p.ssmk + d * SEQ, SEQ * 2);
    if (i == 0) DMANT(sf, dst, lane < 4u ? OOB : lane * 16u - 64u, 0);
    else DMANT(sf, uu(dst + i * 1024), i == CvL<B>::NF - 1 && lane >= 4u ? OOB : lane * 16u, i * 1024 - 64);
}
DEV v4u cv_kvld(rsrc_t rs, int d, int i) { return ld16s(rs, (threadIdx.x & 63) * 16u, uu(W_KVT + (u32)d * (SEQ * 2) + i * 1024)); }
template <bool B> DEV void cv_kvput(lptr F, const v4u* k) {
    const int lane = threadIdx.x & 63;
#pragma unroll
    for (int i = 0; i < CvL<B>::NK; ++i) lds_w16(F + CvL<B>::NF * 1024 + (lane & 3) * CvL<B>::KP + 512 + i * 256 + (lane >> 2) * 16, k[i]);
}
template <bool B> DEV void cv_zero(lptr F) {
    const int lane = threadIdx.x & 63;
#pragma unroll
    for (int i = 0; i < 4; ++i) lds_w16(F + (lane >> 5) * CvL<B>::CH + CvL<B>::NF * 1024 + i * CvL<B>::KP + (lane & 31) * 16, (v4u){0u, 0u, 0u, 0u});
}
#define M32V(d, a, b) asm volatile("v_mfma_f32_32x32x16_bf16 %0, %1, %2, %0" : "+v"(d) : "v"(a), "v"(b))
template <bool B, class G> DEV void cv_run(const Params& p, rsrc_t rs, lptr F, int d, u32 D, f32x4 sp, float (&y)[16], G&& gap) {
    constexpr int KP = CvL<B>::KP, FO = B ? 2048 : 0;
    const int lane = threadIdx.x & 63, n = lane & 31, hh = lane >> 5, c = lane & 3;
    lptr fa = F + 64 + (32 * hh + 16 * (c & 1) + 8 * ((lane >> 4) & 1) + 4 * (c >> 1) - (lane & 12)) * 2;
    lptr kl = F + CvL<B>::NF * 1024 + (32 + n - hh) * 16;
    v4u dg;
#pragma unroll
    for (int q = 0; q < 4; ++q) { const int k = (lane & 35) ? -1 : (n & 4) + (n >> 3); dg[q] = k == 2 * q ? D : k == 2 * q + 1 ? D << 16 : 0u; }
    v4u l[4], h[4]; v2u fc[4], fh[4];
    f32x16 acc[4];
#pragma unroll
    for (int j = 0; j < 4; ++j) { l[j] = lds_r16(kl + j * KP); if constexpr (B) h[j] = lds_r16(kl + j * KP + 512); }
#pragma unroll
    for (int j = 0; j < 2; ++j) { fc[j] = lds_tr(fa + FO - 32 * j); if constexpr (B) fh[j] = lds_tr(fa - 32 * j); }
    asm volatile("s_nop 1" : "+v"(dg));
#pragma unroll
    for (int j = 0; j < 4; ++j) asm volatile("v_mfma_f32_32x32x16_bf16 %0, %1, %2, 0" : "=&v"(acc[j]) : "v"(dg), "v"(B ? h[j] : l[j]));
    constexpr int U = B ? 2 : 4;
    kl -= 32 * U; fa -= 32;
    auto body = [&](bool first) __attribute__((always_inline)) {
#pragma unroll
        for (int z = 0; z < U; ++z) {
            const int c = 2 * (z & 1);
            const v4u f = __builtin_shufflevector(fc[c], fc[c + 1], 0, 1, 2, 3), g = __builtin_shufflevector(fh[c], fh[c + 1], 0, 1, 2, 3);
#pragma unroll
            for (int j = 0; j < 4; ++j) {
                M32V(acc[j], f, l[j]); l[j] = lds_r16(kl + j * KP + 32 * (U - z - 1));
                if (j < 2) fc[(c ^ 2) + j] = lds_tr(fa + FO + (z + 1) * 128 + 32 - 32 * j);
                if (first && j > 0) gap(5 * z + j - 1);
                SCHED();
            }
            if constexpr (B) {
#pragma unroll
                for (int j = 0; j < 4; ++j) {
                    M32V(acc[j], g, h[j]); h[j] = lds_r16(kl + j * KP + 512 + 32 * (U - z - 1));
                    if (j < 2) fh[(c ^ 2) + j] = lds_tr(fa + (z + 1) * 128 + 32 - 32 * j);
                    if (first && j > 1) gap(5 * z + j + 1);
                    SCHED();
                }
            }
        }
        kl -= 32 * U; fa += 128 * U;
    };
    if constexpr (B) body(true);
#pragma unroll 1
    for (int m = B ? 2 * U : 0; m < 32; m += 2 * U) body(false);
    asm volatile("s_nop 11" : "+v"(acc[0]), "+v"(acc[1]), "+v"(acc[2]), "+v"(acc[3]));
    const float o[3] = {acc[1][15] + acc[2][14] + acc[3][13], acc[2][15] + acc[3][14], acc[3][15]};
    if constexpr (!B) st16(rs, lane == 63 ? W_SP + (u32)d * 16u : OOB, (v4u){u_(o[0]), u_(o[1]), u_(o[2]), 0u});
#pragma unroll
    for (int i = 0; i < 16; ++i) {
        float v = acc[0][i];
#pragma unroll
        for (int j = 1; j < 4; ++j) if (j <= i) v += acc[j][i - j];
        if (i < 3) { const float e = f_((u32)__builtin_amdgcn_ds_bpermute((hh ? lane - 32 : lane + 31) * 4, (int)u_(o[i]))); v += lane == 0 ? sp[i] : e; }
        y[i] = v;
    }
}
DEV void cv_put(lptr F, int pr, const float (&y0)[16], const float (&y1)[16]) {
    const int lane = threadIdx.x & 63;
#pragma unroll
    for (int i = 0; i < 16; ++i) *(LAS u32*)(F + CV_Y + (lane & 31) * CV_YP + (lane >> 5) * 128 + i * 8 + pr * 4) = pk(y0[i], y1[i]);
}
DEV void cv_out(rsrc_t rs, lptr F, int u, int blk0) {
    const int lane = threadIdx.x & 63;
    lptr q = F + CV_Y + ((lane >> 2) & 3) * CV_YP + ((lane >> 4) * 4 + (lane & 3)) * 8;
    const u32 yo = W_Y + (u32)(((lane >> 2) * 8 + blk0) * DM + 4 * u + (lane & 3)) * 16u;
#pragma unroll
    for (int i = 0; i < 8; ++i) { lptr a = q + (i >> 1) * (8 * CV_YP) + (i & 1) * 128; st16(rs, yo + (u32)((i & 1) * 128 + (i >> 1)) * (DM * 16), tr2(a, a + 4 * CV_YP)); }
}
struct KvIn { v2u k[19], v[16]; v4u s[2]; };
DEV void kvt_load(const Params& p, int tile, KvIn& in) {
    const int lane = threadIdx.x & 63;
    const rsrc_t rk = mk_rsrc(p.bk, ACT), rv = mk_rsrc(p.bv, ACT), rh = mk_rsrc(p.shift, DM * 8);
    const int row = (tile / 10) * 64 + (lane >> 4) * 16, col = (tile % 10) * 64 + (lane & 15) * 4;
    const u32 vo = (u32)(row * DM + col) * 2u, vp = row > 0 ? vo - 3 * (DM * 2) : OOB;
#pragma unroll
    for (int i = 0; i < 2; ++i) in.s[i] = ldin16(rh, (u32)col * 8u + i * 16, 0);
#pragma unroll
    for (int i = 0; i < 19; ++i) { in.k[i] = i < 3 ? ldin8(rk, vp, i * (DM * 2)) : ldin8(rk, vo, (i - 3) * (DM * 2)); if (i >= 3) in.v[i - 3] = ldin8(rv, vo, (i - 3) * (DM * 2)); }
}
DEV void kvt_make(rsrc_t rs, lptr xt, int tile, const KvIn& in) {
    const int lane = threadIdx.x & 63;
#pragma unroll
    for (int c = 0; c < 2; ++c) {
        const v4u sv = in.s[c];
        const f32x2 sh[4] = {{lo_f(sv.x), lo_f(sv.z)}, {hi_f(sv.x), hi_f(sv.z)}, {lo_f(sv.y), lo_f(sv.w)}, {hi_f(sv.y), hi_f(sv.w)}};
        f32x2 kf[19];
#pragma unroll
        for (int i = 0; i < 19; ++i) kf[i] = up2(in.k[i][c]);
        u32 kv[16];
#pragma unroll
        for (int i = 0; i < 16; ++i) kv[i] = pk2(((kf[i] * sh[3] + kf[i + 1] * sh[2]) + (kf[i + 2] * sh[1] + kf[i + 3] * sh[0])) * up2(in.v[i][c]));
#pragma unroll
        for (int q = 0; q < 8; ++q) {
            const u32 sel = (q & 1) ? 0x07060302u : 0x05040100u; const u32* z = kv + (q >> 1);
            *(LAS v2u*)(xt + ((2 * c + (q & 1)) * 16 + (lane & 15)) * 144 + (lane >> 5) * 64 + (q >> 1) * 16 + ((lane >> 4) & 1) * 8) =
                (v2u){__builtin_amdgcn_perm(z[4], z[0], sel), __builtin_amdgcn_perm(z[12], z[8], sel)};
        }
    }
    const u32 o = W_KVT + (u32)(((tile % 10) * 64 + 4 * (lane >> 3)) * SEQ + (tile / 10) * 64 + (lane & 7) * 8) * 2u;
#pragma unroll
    for (int i = 0; i < 8; ++i) st16(rs, o + (u32)(32 * (i & 1) + (i >> 1)) * (SEQ * 2), lds_r16(xt + (8 * i + (lane >> 3)) * 144 + (lane & 7) * 16));
}
struct RowX { v2u a, b; u32 c; };
DEV void ln_row(rsrc_t rs, int row, const RowX& r) {
    const f32x2 x[5] = {up2(r.a.x), up2(r.a.y), up2(r.b.x), up2(r.b.y), up2(r.c)};
    const f32x2 st = ln_stats(x);
    u32 hp[5];
#pragma unroll
    for (int q = 0; q < 5; ++q) hp[q] = pk2(up2(pkv(x[q] - st.x)) * st.y);
    store_h(rs, W_H1, row, hp);
}
DEV void pre_rings(const Params& p, rsrc_t rs, int j) {
    const u32 gi = (u32)(j * 256 + threadIdx.x), gt = gi * 16u, R = NPRE * 4096, n = ((gt >> 14) << 9) + ((gt & 8191u) >> 4);
    v4u m = {PSENT, PSENT, PSENT, PSENT};
    if (n < 640u) {
        m = *(const v4u*)(p.mem0 + (2 * ((n >> 2) & 3) + (n & 1)) * DM + (n >> 4) * 16 + (n & 2) * 4);
#pragma unroll
        for (int i = 0; i < 4; ++i) m[i] = m[i] == PSENT ? 0x7FC17FC1u : m[i];
    }
    st4s(rs, gi - 36u * 256u < F_CLR ? foff(gi - 36u * 256u) : OOB, 0, 0u);
#pragma unroll 1
    for (u32 o = gt; o < MB; o += R) st16(rs, W_ORING + o, (v4u){PSENT, PSENT, PSENT, PSENT});
    const u32 mo = gt < 4 * 32768 + 16384 ? W_MRING + gt : OOB;
    st16(rs, mo, m);
    st16(rs, mo + MB / 4, m);
    static_assert(NPRE * 4096 >= 4 * 32768 + 16384, "memory ring");
}
constexpr int LN0 = NPRE * 4;
struct Early { KvIn in; RowX r; u32 vote; };
DEV void conv_early(const Params& p, rsrc_t rs, int j, Early& e) {
    const int lane = threadIdx.x & 63, w = uni(threadIdx.x >> 6), u = j * 4 + w;
    if (j < NPRE) {
        const rsrc_t rx = mk_rsrc(p.x, ACT);
        e.r.a = __builtin_amdgcn_raw_buffer_load_b64(rx, lane * 8u, uu(u * (DM * 2)), 0); e.r.b = __builtin_amdgcn_raw_buffer_load_b64(rx, lane * 8u + 512u, uu(u * (DM * 2)), 0);
        e.r.c = __builtin_amdgcn_raw_buffer_load_b32(rx, lane * 4u + 1024u, uu(u * (DM * 2)), 0);
        kvt_load(p, u, e.in);
    } else if (j < NCV && w > 0) e.vote = pol_probe(p, rs, u - NPRE * 4);
}
DEV void conv_stage(const Params& p, rsrc_t rs, lptr lds, int j, int g0, int ge, v2u ep, Early& e) {
    const int tid = threadIdx.x, lane = tid & 63, w = uni(tid >> 6), u = j * 4 + w;
    lptr F = lds + w * CV_W;
    const u32 fb = uu(w * CV_W);
    const rsrc_t rd = mk_rsrc(p.ssmD, DM * 2);
    float y0[16], y1[16];
    if (j < NPRE) {
        constexpr int CH = CvL<false>::CH;
        KvIn& in = e.in;
        if (w == 1) LST(1500 + j, rt32());
        pre_rings(p, rs, j);
        ln_row(rs, u, e.r);
        if (w == 1) LST(1550 + j, rt32());
        DRAIN(); BAR();
        pre_post(rs, j, 1u, ep);
        if (j == 20) LST(260, rt32());
        if (w == 0) LST(1450 + j, rt32());
        const u32 D = __builtin_amdgcn_raw_buffer_load_b16(rd, (u32)cv_chan(u, lane & 3) * 2u, 0, 0);
#pragma unroll 1
        for (int i = 0; i < 4; ++i) { const int d = cv_chan(u, i); cv_filt<false>(p, fb + i * CH, d, 0); cv_filt<false>(p, fb + i * CH, d, 1); cv_filt<false>(p, fb + i * CH, d, 2); }
        kvt_make(rs, F + CV_Y, u, in);
        cv_zero<false>(F); cv_zero<false>(F + 2 * CH);
        DRAIN(); BAR();
        pre_post(rs, j, 2u, ep);
        if (j == 20) LST(262, rt32());
        pre_wait(rs, 0, NPRE, 2u, ep);
        if (j == 20) LST(281, rt32());
        {
            v4u k[8];
#pragma unroll
            for (int i = 0; i < 8; ++i) k[i] = cv_kvld(rs, cv_chan(u, i >> 1), i & 1);
#pragma unroll
            for (int i = 0; i < 4; ++i) cv_kvput<false>(F + i * CH, k + 2 * i);
        }
        if (j == 20) LST(286, rt32());
#pragma unroll 1
        for (int i = 0; i < 4; i += 2) {
            cv_run<false>(p, rs, F + i * CH, cv_chan(u, i), rdl(D, i), f32x4{}, y0, [](int) {});
            cv_run<false>(p, rs, F + (i + 1) * CH, cv_chan(u, i + 1), rdl(D, i + 1), f32x4{}, y1, [](int) {});
            cv_put(F, i >> 1, y0, y1);
        }
        if (j == 20) LST(287, rt32());
        cv_out(rs, F, u, 0);
        if (j == 20) LST(263, rt32());
    } else {
        st16(rs, lane == 0 ? foff(F_PRE + 48 + u - NPRE * 4) : OOB, (v4u){ep.x + 1u, ep.y, w > 0 ? e.vote : 0u, 0u});
#pragma unroll 1
        for (int t = u + 128; t < 320; t += 16) { kvt_load(p, t, e.in); kvt_make(rs, F, t, e.in); }
    }
    DRAIN(); BAR();
    if (g0 < ge) {
        constexpr int CH = CvL<true>::CH;
        pre_post(rs, j, 3u, ep);
        cv_zero<true>(F);
        if (w == 0) { pre_poll(rs, 64, 32, 3u, ep); pre_poll(rs, 0, NCV, 3u, ep); }
        BAR();
        v4u k[4];
        auto fetch = [&](u32 dst, int d, int i) { if (i < 5) cv_filt<true>(p, dst, d, i); else if (i < 9) k[i - 5] = cv_kvld(rs, d, i - 5); };
#pragma unroll
        for (int i = 0; i < 9; ++i) fetch(fb, cv_chan(g0 * 4 + w, 0), i);
        const u32 ch = (u32)cv_chan(min((g0 + 8 * (lane >> 2)) * 4 + w, 159), lane & 3);
        u32 D = __builtin_amdgcn_raw_buffer_load_b16(rd, ch * 2u, 0, 0);
        v4u sp = ld16s(rs, W_SP + ch * 16u, 0);
        asm volatile("" : "+v"(D), "+v"(sp));
#pragma unroll 1
        for (int g = g0, n = 0; g < ge; g += 8) {
            const int un = g * 4 + w;
            if (g == 16) { LST(282, rt32()); LST(288, __builtin_amdgcn_s_memtime()); }
#pragma unroll 1
            for (int i = 0; i < 4; i += 2, n += 2) {
                const int d1 = (int)rdl(ch, n + 1), d2 = (int)rdl(ch, n + 2);
                cv_kvput<true>(F, k);
                cv_run<true>(p, rs, F, 0, rdl(D, n), (f32x4){f_(rdl(sp.x, n)), f_(rdl(sp.y, n)), f_(rdl(sp.z, n)), 0.f}, y0, [&](int q) { fetch(fb + CH, d1, q); });
                cv_kvput<true>(F + CH, k);
                cv_run<true>(p, rs, F + CH, 326, rdl(D, n + 1), (f32x4){f_(rdl(sp.x, n + 1)), f_(rdl(sp.y, n + 1)), f_(rdl(sp.z, n + 1)), 0.f}, y1, [&](int q) { fetch(fb, d2, q); });
                cv_put(F, i >> 1, y0, y1);
            }
            cv_out(rs, F, un, 4);
            if (g == 16) { LST(283, rt32()); LST(289, __builtin_amdgcn_s_memtime()); }
        }
        DRAIN(); BAR();
    }
    pre_post(rs, j, 5u, ep);
    if (j == 20) LST(284, rt32());
    if (j == 3) LST(285, rt32());
}

#define M32A(first, d, a, b) do { if (first) asm volatile("v_mfma_f32_32x32x16_bf16 %0, %1, %2, 0" : "=&v"(d) : "a"(a), "v"(b)); \
                                  else asm volatile("v_mfma_f32_32x32x16_bf16 %0, %1, %2, %0" : "+v"(d) : "a"(a), "v"(b)); } while (0)
#define M32B(first, d, a, b) do { if (first) asm volatile("v_mfma_f32_32x32x16_bf16 %0, %1, %2, 0" : "=&v"(d) : "v"(a), "a"(b)); \
                                  else asm volatile("v_mfma_f32_32x32x16_bf16 %0, %1, %2, %0" : "+v"(d) : "v"(a), "a"(b)); } while (0)
#define VCVT(d, a, b) asm volatile("v_cvt_pk_bf16_f32 %0, %1, %2" : "=v"(d) : "v"(a), "v"(b))
#define VADD(d, a, b) asm volatile("v_add_f32 %0, %1, %2" : "=v"(d) : "v"(a), "v"(b))
#define LD4A(d, vo, so) asm volatile("buffer_load_dword %0, %1, %2, %3 offen sc1" : "=v"(d) : "v"(vo), "s"(srd), "s"(so) : "memory")
DEV u32 wst_src(int lane, int ld) { return (u32)((lane >> 4) * ld + 8 * ((lane & 15) ^ ((lane >> 4) << 2))) * 2u; }
DEV u32 wst_rd(int lane, int w) { const int q4 = (lane >> 2) & 3; return (u32)(q4 * 256 + ((w ^ q4) * 64) + ((lane >> 4) & 1) * 32 + (lane & 3) * 8); }

constexpr int QK_SEL = 81920;
#define QK_LEAD 12
#define QK_SMP 56
#define QK_W0 8
DEV void role_qkv(const Params& p, lptr lds, int c, v2u ep) {
    const int tid = threadIdx.x, lane = tid & 63, w = uni(tid >> 6), hh = lane >> 5, r32 = lane & 31;
    const int m = c >> 5, h = c & 31;
    const rsrc_t rs = mk_rsrc(p.ws, W_END);
    v4u wt[40];
    {
        const v4u sw = mk_srd(m == 0 ? p.Wq : m == 1 ? p.Wk : p.Wv, DM * NQ * 2), sx = mk_srd(p.x, ACT);
        const u32 vo = wst_src(lane, NQ) + (u32)h * (DH * 2), x8 = (u32)lane * 8u, x4 = (u32)lane * 4u;
        RowX xr[5];
        auto wfill = [&](int i0, int n) {
#pragma unroll 1
            for (int i = i0; i < i0 + n; ++i) DMAIN(sw, uu((4 * i + w) * 1024), vo, uu((16 * i + 4 * w) * (NQ * 2)));
        };
        wfill(0, QK_W0);
#pragma unroll
        for (int i = 0; i < 5; ++i)
            asm volatile("buffer_load_dwordx2 %0, %3, %5, %6 offen\n\tbuffer_load_dwordx2 %1, %3, %5, %6 offen offset:512\n\tbuffer_load_dword %2, %4, %5, %6 offen offset:1024"
                         : "=&v"(xr[i].a), "=&v"(xr[i].b), "=&v"(xr[i].c) : "v"(x8), "v"(x4), "s"(sx), "s"(uu((LN0 + 4 * c + w + 384 * i) * (DM * 2))) : "memory");
        wfill(QK_W0, 40 - QK_W0);
        if (c == 3) LST(275, rt32());
        VMW(40 - QK_W0);
        if (c == 3) LST(276, rt32());
#pragma unroll
        for (int i = 0; i < 5; ++i) {
            asm volatile("" : "+v"(xr[i].a), "+v"(xr[i].b), "+v"(xr[i].c));
            const int row = LN0 + 4 * c + w + 384 * i;
            if (i < 4 || row < SEQ) ln_row(rs, row, xr[i]);
        }
        if (c == 3) LST(264, rt32());
        VMW(12); BAR();
        lptr tb = lds + hh * 2048 + wst_rd(lane, w);
        SFOR(k, 40)
            lptr q = tb + (k >> 3) * 32768 + (k & 7) * 4096; wt[k] = tr2(q, q + 1024); asm volatile("" : "+a"(wt[k]));
        SEND
        DRAIN(); BAR();
        pre_post(rs, 96 + c, 1u, ep);
    }
    if (c == 3) LST(268, rt32());
    if (w == 0) LST(1350 + c, rt32());
    bool dead = false;
    int n0, n1;
    const u32 t16 = (u32)tid * 16u, wb = uu(w * 1024);
    u32 ar[16];
#pragma unroll
    for (int j = 0; j < 16; ++j) ar[j] = (u32)(r32 * 256 + ((2 * (j & 7) + hh) ^ (r32 & 15)) * 16 + (j >> 3) * 49152);
    MK_SRD();
#define XRD(y, ks) lds_r16(lds + ar[((ks) & 7) + ((ks) >= 24 ? 8 : 0)] + ((ks) >> 3) % 3 * 16384 + (y) * 8192)
#define QDMA(y, i, vo, so) DMA16I(wb, ((i) >> 1) * 16384 + ((i) & 1) * 4096 + (y) * 8192, vo, so, (i) * 4096)
    const u32 h2flag = (lane < 32 && w == 0) ? foff(F_H2 + lane) : OOB;
    const u32 ol = (m == 2 ? (u32)(2 * w + (r32 >> 4)) * 1024u : (u32)((r32 >> 4) * 4 + w) * 1024u) + (u32)(hh * 16 + (r32 & 15)) * 16u
                 + (m == 2 ? W_VO : m == 1 ? W_KQ : W_KQ + 8192u) + (u32)h * 16384u;
    const u32 fo = tid < 2 ? foff(F_QKV + tid * 96 + c) : OOB;
    lptr selw = lds + QK_SEL + (w == 0 ? 0 : 64), selr = lds + QK_SEL;
    auto late = [&](u32 fv) { const u32 need = n1 >= NSEG ? 0xFFFFFFFFu : dead ? 0u : (u32)n1 + 1u; return (u32)__builtin_amdgcn_ballot_w64(fv < need); };
    static_assert(W_H2 - W_H1 == 2 * 64 * (32 * DM * 2), "h");
    pre_wait(rs, 0, NPRE, 1u, ep);
    if (c == 3) LST(269, rt32());
    auto run = [&](auto isv) __attribute__((always_inline)) {
        constexpr bool V = decltype(isv)::value;
        f32x16 ax, ay = {};
        v4u x[8], pp, pq;
        u32 oy = OOB, lt = 1, fcnt = 0, fa = OOB;
        u32 nn = 0;
        PIN(lt); PIN(nn);
#pragma unroll
        for (int z = 0; z < 5; ++z) x[z] = XRD(1, 20 + z);
#define QPK(r_, a1, a_, q_) do { if constexpr (V) VCVT(r_[q_], a1[4 * (a_) + 8 * ((q_) >> 1) + 2 * ((q_) & 1)], a1[4 * (a_) + 8 * ((q_) >> 1) + 2 * ((q_) & 1) + 1]); \
                                 else VCVT(r_[q_], a1[4 * (a_) + (q_)], a1[4 * (a_) + (q_) + 8]); } while (0)
#define QPK4(r_, a1, a_) do { QPK(r_, a1, a_, 0); QPK(r_, a1, a_, 1); QPK(r_, a1, a_, 2); QPK(r_, a1, a_, 3); } while (0)
        for (;;) {
            u32 ls = uu(lt);
            n0 = uni((int)nn); n1 = n0 >> 8; n0 &= 255;
            if (ls != 0u && n0 >= min(NSEG, n1 + QK_LEAD)) {
                CFENCE();
                u32 fv = ld4(rs, h2flag);
                asm volatile("s_waitcnt vmcnt(0)" : "+v"(fv) :: "memory");
                *(LAS u32*)(selw + 16) = late(fv);
                BAR();
                st4s(rs, fa, 0, fcnt);
                fa = OOB;
                if (n1 >= NSEG) __builtin_amdgcn_endpgm();
                if (uu(*(LAS u32*)(selr + 16)) != 0u) {
                    if (w == 0) wait_flags(rs, h2flag, (u32)n1 + 1, dead);
                    BAR();
                }
                ls = 0u;
            }
            if (n0 == 4) pre_wait(rs, 96, 96, 1u, ep);
            int cur = n0;
            asm volatile("s_cmp_eq_u32 %3, 0\n\ts_cselect_b32 %0, %4, %0\n\ts_addc_u32 %2, %2, 0\n\ts_cmp_lg_u32 %3, 0\n\ts_addc_u32 %1, %1, 0"
                         : "+s"(cur), "+s"(n0), "+s"(n1) : "s"(ls), "s"(n1 | 64) : "scc");
            u32 xs = W_H1 + (u32)(cur + (cur & 64)) * (32 * DM * 2), ys, yvo, ox, fv;
            PINS(xs);
#define QT(k) do {} while (0)
            QT(0);
            SCHED();
            QDMA(0, 0, t16, xs); QDMA(0, 1, t16, xs);
            SFOR(z, 80)
                constexpr int ks = (z + 20) % 40, zn = (z + 5) % 80;
                if constexpr (z >= 20 && z < 60) { if constexpr (V) M32B(ks == 0, ax, x[z & 7], wt[ks]); else M32A(ks == 0, ax, wt[ks], x[z & 7]); }
                else { if constexpr (V) M32B(ks == 0, ay, x[z & 7], wt[ks]); else M32A(ks == 0, ay, wt[ks], x[z & 7]); }
                x[(z + 5) & 7] = XRD(zn < 20 || zn >= 60, (zn + 20) % 40);
                if constexpr (z == 77) lt = *(LAS u32*)selr;
                SCHED();
                if constexpr (z < 8) QDMA(0, z + 2, t16, xs);
                if constexpr (z == 9) { ys = W_H1 + (u32)n0 * (32 * DM * 2); yvo = t16 | (n0 < NSEG ? 0u : OOB); PINS(ys); PIN(yvo); }
                if constexpr (z == 11) { ox = ol + (u32)cur * (NH * 16384u); PIN(ox); }
                if constexpr (z == 14) { QT(1); asm volatile("s_waitcnt vmcnt(6) lgkmcnt(0)\n\ts_barrier" ::: "memory"); QT(2); }
                if constexpr (z == 15) st4s(rs, fa, 0, fcnt);
                if constexpr (z >= 16 && z < 26) QDMA(1, z - 16, yvo, ys);
                if constexpr (z == 26) QPK4(pp, ay, 0);
                if constexpr (z == 27) st16(rs, oy, pp);
                if constexpr (z == 28) QPK4(pq, ay, 1);
                if constexpr (z == 29) st16(rs, oy + 512, pq);
                if constexpr (z == 30) { QT(3); asm volatile("s_waitcnt vmcnt(13)\n\ts_barrier" ::: "memory"); QT(4); }
                if constexpr (z == 32) { fcnt = lane == 0 ? (u32)n0 : (u32)n1; fa = fo; asm volatile("" : "+v"(fcnt), "+v"(fa)); }
                if constexpr (z == 34) { oy = (ol + (u32)n0 * (NH * 16384u)) | (yvo & OOB); PIN(oy); }
                if constexpr (z == 35) { nn = (u32)(min(n0 + 1, NSEG) | n1 << 8); PIN(nn); }
                if constexpr (z == 54) { asm volatile("s_waitcnt vmcnt(2) lgkmcnt(0)\n\ts_barrier" ::: "memory"); QT(5); }
                if constexpr (z == QK_SMP) LD4A(fv, h2flag, 0);
                if constexpr (z == 62) QPK4(pp, ax, 0);
                if constexpr (z == 63) st16(rs, ox, pp);
                if constexpr (z == 64) QPK4(pq, ax, 1);
                if constexpr (z == 65) st16(rs, ox + 512, pq);
                if constexpr (z == 73) { asm volatile("s_waitcnt vmcnt(2)" : "+v"(fv) :: "memory"); QT(6); ls = late(fv); PINS(ls); }
                if constexpr (z == 74) { lt = ls; PIN(lt); *(LAS u32*)selw = lt; }
                if constexpr (z == 76) asm volatile("s_waitcnt lgkmcnt(2)\n\ts_barrier" ::: "memory");
                SCHED();
            SEND
        }
    };
    if (m == 2) run(IC<1>{}); else run(IC<0>{});
}

#define OU_SMP 44
DEV void role_out(const Params& p, lptr lds, int L, int kq, int ng, v2u ep) {
    const int tid = threadIdx.x, lane = tid & 63, w = uni(tid >> 6), hh = lane >> 5, r32 = lane & 31;
    const rsrc_t rs = mk_rsrc(p.ws, W_END);
    v4u wo[64];
    bool dead = false;
    {
        const v4u sw = mk_srd(p.Wo, DM * NQ * 2);
        const u32 vo = wst_src(lane, DM) + (u32)ng * 256u;
        auto fill = [&](int b) {
#pragma unroll 1
            for (int i = 0; i < 8; ++i) DMAIN(sw, uu((b % 5) * 32768 + (4 * i + w) * 1024), vo, uu((kq * 1024 + b * 128 + 16 * i + 4 * w) * (DM * 2)));
        };
#pragma unroll 1
        for (int b = 0; b < 5; ++b) fill(b);
        lptr tb = lds + hh * 1024 + wst_rd(lane, w);
        SFOR(b, 8)
            VMW(b < 4 ? 32 : 56 - 8 * b); BAR();
            lptr q = tb + (b % 5) * 32768;
#pragma unroll
            for (int k = 0; k < 8; ++k) { lptr a = q + (k >> 1) * 8192 + (k & 1) * 2048; wo[8 * b + k] = tr2(a, a + 4096); if (b < 7) asm volatile("" : "+a"(wo[8 * b + k])); else PIN(wo[8 * b + k]); }
            if constexpr (b < 3) { BAR(); fill(b + 5); }
        SEND
    }
    pre_wait(rs, 0, NPRE, 1u, ep);
    if (kq == 0 && ng == 0) LST(272 + L, rt32());
    MK_SRD();
    const u32 ofl = lane < 8 ? foff(F_O + L * 32 + kq * 8 + lane) : OOB, l16 = (u32)lane * 16u;
    const u32 fo = (tid == 0) ? foff(F_P + L * 32 + kq * 5 + ng) : OOB;
    const u32 src0 = uu(W_VO + 8192u + (u32)(L * NSEG * NH + kq * 8) * 16384u + w * 1024);
#define ODMA(i, dst, so) DMA16I(dst, (i) * 4096, l16, so, ((i) >> 1) * 16384 + ((i) & 1) * 4096)
    auto first = [&](int s) { const u32 dst = uu((s & 1) * 65536 + w * 1024), src = src0 + (u32)s * (NH * 16384u); SFOR(i, 10) ODMA(i, dst, src); SEND };
    u32 pa = W_P + (u32)(L * NSEG) * P_ITEM + (u32)(4 * hh * 4 + kq) * (DM * 4) + (u32)(ng * 128 + w * 32 + r32) * 4u, fv = 0;
    bool early = false;
    for (int s = 0; s < NSEG; ++s) {
        const u32 dst = uu((s & 1) * 65536 + w * 1024), src = src0 + (u32)s * (NH * 16384u);
        lptr A = lds + (s & 1) * 65536 + l16;
        if (!early) {
            if (!all_(ofl == OOB || fv >= (u32)s + 1)) wait_flags(rs, ofl, (u32)s + 1, dead);
            LS3(L == 0 && w == 1, 1, kq * 5 + ng);
            first(s);
        }
        asm volatile("s_waitcnt vmcnt(8)\n\ts_barrier" ::: "memory");
        v4u o[64];
#pragma unroll
        for (int k = 0; k < 3; ++k) o[k] = lds_r16(A + k * 1024);
        f32x16 acc;
        SCHED();
        SFOR(k, 64)
            if constexpr (k < 56) M32B(k == 0, acc, o[k], wo[k]); else M32V(acc, o[k], wo[k]);
            if constexpr (k < 61) o[k + 3] = lds_r16(A + (k + 3) * 1024);
            SCHED();
            if constexpr (k < 12 && (k & 1) == 0) ODMA(10 + k / 2, dst, src);
            if constexpr (k == 4) asm volatile("s_waitcnt vmcnt(7)\n\ts_barrier" ::: "memory");
            if constexpr (k == 20) asm volatile("s_waitcnt vmcnt(5)\n\ts_barrier" ::: "memory");
            if constexpr (k == 40) asm volatile("s_waitcnt vmcnt(0)\n\ts_barrier" ::: "memory");
            if constexpr (k == OU_SMP) LD4A(fv, ofl, 0);
            SCHED();
        SEND
        asm volatile("s_nop 9" : "+v"(acc), "+v"(pa));
#pragma unroll
        for (int i = 0; i < 16; ++i) st4s(rs, pa, (u32)((8 * (i >> 2) + (i & 3)) * (4 * DM * 4)), u_(acc[i]));
        asm volatile("s_waitcnt vmcnt(16)" : "+v"(fv) :: "memory");
        LS3(L == 0 && w == 1, 2, kq * 5 + ng);
        early = s + 1 < NSEG && all_(ofl == OOB || fv >= (u32)s + 2);
        if (early) { first(s + 1); VMW(10); }
        else { fv = ld4(rs, ofl); asm volatile("s_waitcnt vmcnt(0)" : "+v"(fv) :: "memory"); }
        BAR();
        st4s(rs, fo, 0, (u32)s + 1);
        pa += P_ITEM;
        LS2(L == 0 && w == 1, 160 + kq * 5 + ng);
    }
}

constexpr int RW_P = 20480, RW_X = RW_P + 10240;
DEV void role_row(const Params& p, lptr lds, int id, v2u ep) {
    const int lane = threadIdx.x & 63, w = uni(threadIdx.x >> 6);
    const int ri = id >> 1, L = id & 1;
    const rsrc_t rs = mk_rsrc(p.ws, W_END);
    bool dead = false, have = false;
    const u32 fp = lane < 20 ? foff(F_P + L * 32 + lane) : OOB, l16 = (u32)lane * 16u, lx = lane < 16 ? l16 + 1024u : OOB, fb = uu(w * CV_W);
    lptr F = lds + w * CV_W;
    MK_SRD();
    const v4u sx = mk_srd(p.x, ACT);
    auto yfill = [&](int b) {
#pragma unroll 1
        for (int i = 0; i < 5; ++i) DMA32(uu(fb + (b & 1) * 10240 + i * 2048), l16, uu(W_Y + (u32)(ri * 8 + b) * 10240u + i * 2048));
    };
    auto xfill = [&](int s) {
        const u32 d = uu(fb + RW_X + (s & 1) * 2048), o = uu((u32)(s * 32 + ri) * (DM * 2));
        if (L == 0) { DMAIN(sx, d, l16, o); DMAIN(sx, d + 1024, lx, o); } else { DMA16(d, l16, o + W_X2); DMA16(d + 1024, lx, o + W_X2); }
    };
    auto pfill = [&](int s) {
        const u32 o = uu(W_P + (u32)(L * NSEG + s) * P_ITEM + (u32)ri * (4 * DM * 4));
        SFOR(i, 10) DMA16I(fb, RW_P + i * 1024, l16, o, i * 1024); SEND
        if (L == 1) xfill(s);
    };
    if (L == 0) xfill(0);
    pre_poll(rs, 0, NPRE, 3u, ep);
    if (id == 0) LST(274, rt32());
    yfill(0); DRAIN();
    u32 fv = 0;
    for (int s = 0; s < NSEG; ++s) {
        if (!have) {
            if (!all_(fp == OOB || fv >= (u32)s + 1)) wait_flags(rs, fp, (u32)s + 1, dead);
            LS3(L == 0, 3, ri);
            pfill(s);
        }
        LS2(L == 0, 192 + ri);
        f32x2 y[5];
        {
            lptr ya = F + ((s >> 3) & 1) * 10240 + (s & 7) * 2 + lane * 16;
            auto yr = [&](lptr q) { return lo_f(*(LAS u16*)q); };
            y[0] = (f32x2){yr(ya), yr(ya + 2560)}; y[1] = (f32x2){yr(ya + 5120), yr(ya + 7680)};
            y[2] = (f32x2){yr(ya + 1024), yr(ya + 3584)}; y[3] = (f32x2){yr(ya + 6144), yr(ya + 8704)};
            lptr yc = ya - lane * 16 + 2048 + (lane >> 1) * 16 + (lane & 1) * 5120;
            y[4] = (f32x2){yr(yc), yr(yc + 2560)};
            asm volatile("" : "+v"(y[0]), "+v"(y[1]), "+v"(y[2]), "+v"(y[3]), "+v"(y[4]));
        }
        if (!have) DRAIN();
        LS3(L == 0, 4, ri);
        f32x2 x1[5];
        {
            f32x4 a[4], b[4]; f32x2 c[4];
            lptr q = F + RW_X + (s & 1) * 2048;
            v2u xa = *(LAS v2u*)(q + lane * 8), xb = *(LAS v2u*)(q + 512 + lane * 8); u32 xc = *(LAS u32*)(q + 1024 + lane * 4);
#pragma unroll
            for (int kq = 0; kq < 4; ++kq) { lptr r = F + RW_P + kq * (DM * 4); a[kq] = *(LAS f32x4*)(r + l16); b[kq] = *(LAS f32x4*)(r + 1024 + l16); c[kq] = *(LAS f32x2*)(r + 2048 + lane * 8); }
            SCHED();
            const f32x4 sa = (a[0] + a[1]) + (a[2] + a[3]), sb = (b[0] + b[1]) + (b[2] + b[3]);
            const f32x2 o[5] = {sa.lo, sa.hi, sb.lo, sb.hi, (c[0] + c[1]) + (c[2] + c[3])};
            const u32 xi[5] = {xa.x, xa.y, xb.x, xb.y, xc};
#pragma unroll
            for (int i = 0; i < 5; ++i) x1[i] = up2(xi[i]) + o[i];
            asm volatile("" : "+v"(x1[0]), "+v"(x1[1]), "+v"(x1[2]), "+v"(x1[3]), "+v"(x1[4]));
        }
        have = s + 1 < NSEG && all_(fp == OOB || fv >= (u32)s + 2);
        if (__builtin_expect(have, 0)) pfill(s + 1);
        if (L == 0) xfill(s + 1);
        const f32x2 st1 = ln_stats(x1);
        u32 xp[5];
#pragma unroll
        for (int i = 0; i < 5; ++i) xp[i] = pkv(x1[i] + (x1[i] - st1.x) * (y[i] * st1.y));
        const int row = s * 32 + ri;
        if (L == 0) {
            const u32 ro = (u32)row * (DM * 2);
            f32x2 x2[5];
#pragma unroll
            for (int i = 0; i < 5; ++i) x2[i] = up2(xp[i]);
            st8s(rs, (u32)lane * 8, W_X2 + ro, (v2u){xp[0], xp[1]});
            st8s(rs, (u32)lane * 8 + 512, W_X2 + ro, (v2u){xp[2], xp[3]});
            st4s(rs, (u32)lane * 4 + 1024, W_X2 + ro, xp[4]);
            const f32x2 st2 = ln_stats(x2);
            u32 hp[5];
#pragma unroll
            for (int i = 0; i < 5; ++i) hp[i] = pk2((x2[i] - st2.x) * st2.y);
            store_h(rs, W_H2, row, hp);
        } else {
            u16* o_ = p.out + (size_t)row * DM;
            *(v2u*)(o_ + 4 * lane) = (v2u){xp[0], xp[1]};
            *(v2u*)(o_ + 256 + 4 * lane) = (v2u){xp[2], xp[3]};
            *(u32*)(o_ + 512 + 2 * lane) = xp[4];
        }
        CFENCE();
        LS3(L == 0, 5, ri);
        fv = ld4(rs, fp);
        asm volatile("s_waitcnt vmcnt(0)" : "+v"(fv) :: "memory");
        st4s(rs, lane == 0 && L == 0 ? foff(F_H2 + ri) : OOB, 0, (u32)s + 1);
        if ((s & 7) == 6 && s < NSEG - 8) {
            if (s == 30) pre_poll(rs, 0, NCV, 5u, ep);
            if (s == 46) st8s(rs, (id == 1 && lane == 0) ? foff(F_EPOCH) : OOB, 0, (v2u){ep.x + 8u, ep.y});
            yfill((s >> 3) + 1);
        }
        if (ri == 0) LST(320 + L * 64 + s, rt32());
        LS2(L == 0, 224 + ri);
    }
}

constexpr float SCALE = 0.08838834764831845f, LOG2E = 1.4426950408889634f;
constexpr int C_K = 0, C_V = 8192, C_QX = 16384, C_MEM = 24576, C_Q = C_MEM + 10240, C_KM = C_Q + 2304, C_VM = C_KM + 2304, C_DUMP = C_VM + 2048,
              C_ZERO = C_DUMP + 1024, C_WL = C_ZERO + 128;
constexpr int NLF = 14;
static_assert(C_WL % 16 == 0 && C_WL + 4 * NLF * 2048 <= LDS_BYTES, "head lds");
#define CH_VIS 10
#define CH_H0 104
#define CH_W0 81
#define CH_MH 3
#define CH_MW 3
#define CH_REC 8
#define CH_PRE 9
#define CH_T0 190
#define CH_UP 2
#define CH_WX 6
#define WAIT_TXT(tmp, t4) "s_memrealtime vcc\n1:\n\ts_waitcnt lgkmcnt(0)\n\ts_sub_u32 " tmp ", " t4 ", vcc_lo\n\ts_memrealtime vcc\n\ts_cmpk_lt_u32 " tmp ", 400\n\ts_cbranch_scc1 1b\n\t" \
                          "s_add_u32 " tmp ", " tmp ", 4\n\ts_cselect_b32 " tmp ", " tmp ", 0\n2:\n\ts_sub_u32 " tmp ", " tmp ", 1\n\ts_cbranch_scc0 2b\n\t"
DEV void wait_until(u32 t) { u32 tmp; asm volatile(WAIT_TXT("%0", "%1") "s_waitcnt lgkmcnt(0)" : "=&s"(tmp) : "s"(uu(t - 4u)) : "vcc", "scc", "memory"); }
#define SMF_A(acc, a, b) asm volatile("v_smfmac_f32_16x16x64_bf16 %0, %1, %2, %3" : "+v"(acc) : "v"(a), "a"(b), "v"(idx))
#define SMF_V(acc, a, b) asm volatile("v_smfmac_f32_16x16x64_bf16 %0, %1, %2, %3" : "+v"(acc) : "v"(a), "v"(b), "v"(idx))
#define MF0(d, a, b) asm volatile("v_mfma_f32_16x16x32_bf16 %0, %1, %2, 0" : "=&v"(d) : "v"(a), "v"(b))
#define MF1(d, a, b) asm volatile("v_mfma_f32_16x16x32_bf16 %0, %1, %2, %0" : "+v"(d) : "v"(a), "v"(b))
#define MF0H(d, a, b) asm volatile("v_mfma_f32_16x16x16_bf16 %0, %1, %2, 0" : "=&v"(d) : "v"(a), "v"(b))
#define MF1H(d, a, b) asm volatile("v_mfma_f32_16x16x16_bf16 %0, %1, %2, %0" : "+v"(d) : "v"(a), "v"(b))
DEV v8u tr_frag(lptr p0, lptr p1, lptr p2, lptr p3) { return cat8(tr2(p0, p1), tr2(p2, p3)); }
constexpr int qslot(int ks) { return (ks & 1) && ks < 8 ? ks >> 1 : -1; }
constexpr int kslot(int ks) { return ks < 4 ? 4 + ks : -1; }
constexpr int vslot(int ks) { return ks >= 4 ? 4 + ks : -1; }
DEV u32 ring_map(u32 off, u32 half) { return ((off >> 13) << 14) + (half << 13) + (off & 8191u); }
#define ZRD(x, i) x = __builtin_bit_cast(f32x4, lds_r16(ZP + 16 * (i)))
#define VMAX(d, a, b) asm volatile("v_max_f32 %0, %1, %2" : "=v"(d) : "v"(a), "v"(b))
#define VMAX3(d, a, b, c_) asm volatile("v_max3_f32 %0, %1, %2, %3" : "=v"(d) : "v"(a), "v"(b), "v"(c_))
#define VEXP(d, a) asm volatile("v_exp_f32 %0, %1" : "=v"(d) : "v"(a))
#define VMULC(d, a) asm volatile("v_mul_f32 %0, 0x3e0293ee, %1" : "=v"(d) : "v"(a))
#define CADD(d, a, b) do { d = (a) + (b); PIN(d); } while (0)
#define CMULC(d, a) do { d = (a) * CEXP; PIN(d); } while (0)
#define CCVT(d, a, b) do { d = pk(a, b); PIN(d); } while (0)
#define LGK(n) __builtin_amdgcn_s_waitcnt(0xC07F | (n) << 8)
#define SEL(d, a, b) asm volatile("v_cndmask_b32 %0, %1, %2, %3" : "=v"(d) : "v"(a), "v"(b), "s"(himask))
#define QPERM(d, a, q) asm volatile("v_mov_b32_dpp %0, %1 quad_perm:" q " row_mask:0xf bank_mask:0xf" : "=v"(d) : "v"(a))

#define HD_SMP 56
DEV void role_head(const Params& p, lptr lds, int L, int h, v2u ep) {
    const int tid = threadIdx.x, lane = tid & 63, w = uni(tid >> 6), g = lane >> 4, r = lane & 15;
    const rsrc_t rs = mk_rsrc(p.ws, W_END);
    v8u wq0[10], wq1[10], wk0[10], wk1[10], wv0[10], wv1[10];
    lptr WL = lds + C_WL + w * (NLF * 2048) + lane * 16;
    {
        constexpr int GS = 1056, CS = 48 * GS;
        const v4u sq = mk_srd(p.Wq, DM * NQ * 2), sk = mk_srd(p.Wk, DM * NQ * 2), sv = mk_srd(p.Wv, DM * NQ * 2);
        const u32 vo = (u32)(g * NQ + h * DH + 8 * (r ^ ((g & 1) << 3))) * 2u;
        auto fill = [&](int ch) {
#pragma unroll 1
            for (int i = 0; i < 4; ++i) {
                const u32 dst = uu((ch % 3) * CS + (4 * i + w) * GS), so = uu((ch * 64 + 16 * i + 4 * w) * (NQ * 2));
                DMAIN(sq, dst, vo, so); DMAIN(sk, dst + 16 * GS, vo, so); DMAIN(sv, dst + 32 * GS, vo, so);
            }
        };
        fill(0); fill(1); fill(2);
        const int q4 = r >> 2;
        lptr tb = lds + (4 * (g >> 1) + 2 * (q4 >> 1) + (g & 1)) * GS + (q4 & 1) * 256 + ((w * 64 + (r & 3) * 8) ^ ((q4 & 1) << 7));
        SFOR(ch, 10)
            VMW(ch < 8 ? 24 : 108 - 12 * ch);
            BAR();
            auto frag = [&](int m, int t) { lptr b = tb + (ch % 3) * CS + m * 16 * GS + t * 32; return tr_frag(b, b + 512, b + 8 * GS, b + 8 * GS + 512); };
            wq0[ch] = frag(0, 0); wq1[ch] = frag(0, 1); wk0[ch] = frag(1, 0); wk1[ch] = frag(1, 1); wv0[ch] = frag(2, 0); wv1[ch] = frag(2, 1);
            asm volatile("" : "+a"(wq0[ch]), "+a"(wk0[ch]), "+v"(wv0[ch]), "+v"(wv1[ch]));
            if constexpr (qslot(ch) < 0) asm volatile("" : "+a"(wq1[ch])); else asm volatile("" : "+v"(wq1[ch]));
            if constexpr (kslot(ch) < 0) asm volatile("" : "+a"(wk1[ch])); else asm volatile("" : "+v"(wk1[ch]));
            BAR();
            if constexpr (ch < 7) fill(ch + 3);
        SEND
#pragma unroll
        for (int ch = 0; ch < 10; ++ch) {
            if (qslot(ch) >= 0) { lds_w16(WL + qslot(ch) * 2048, wq1[ch].lo); lds_w16(WL + qslot(ch) * 2048 + 1024, wq1[ch].hi); }
            if (kslot(ch) >= 0) { lds_w16(WL + kslot(ch) * 2048, wk1[ch].lo); lds_w16(WL + kslot(ch) * 2048 + 1024, wk1[ch].hi); }
            if (vslot(ch) >= 0) { lds_w16(WL + vslot(ch) * 2048, wv1[ch].lo); lds_w16(WL + vslot(ch) * 2048 + 1024, wv1[ch].hi); }
        }
        CFENCE();
    }
    const u32 idx = (r & 2) ? 0xEEEEu : 0x4444u;
    unsigned long long himask = 0xCCCCCCCCCCCCCCCCull;
    PINS(himask);
    bool dead = false;
    if (h == 3) LST(266 + L, rt32());
    if (w == 0) LST(1240 + L * 32 + h, rt32());
    if (L == 1) {
        KvIn in;
        kvt_load(p, LN0 + 4 * h + w, in); kvt_make(rs, lds + w * 9216, LN0 + 4 * h + w, in);
        DRAIN(); BAR();
        pre_post(rs, 64 + h, 3u, ep);
    }
    const u32 pol0 = pol_wait(rs, ep);
    if (h == 3 && L == 0) LST(277, rt32());
    const u32 l16 = (u32)lane * 16u;
    const u32 mring = W_MRING + (u32)L * (MB / 4) + pol0 * 8192u, hint = mring + 4 * 32768u;
    const u32 fkh = lane < 3 ? foff(F_QKV + L * 96 + lane * 32 + h) : lane < 7 ? hint + (u32)(lane - 3) * 4u : OOB;
    u32 tm = 0;
    MK_SRD();
    u32 ksrc = uu(W_KQ + (u32)((L * NSEG - 1) * NH + h) * 16384u + w * 2048), vsrc = ksrc + (W_VO - W_KQ);
    u32 dk = uu(C_K + w * 2048), dv = dk + (C_V - C_K), dq = dk + (C_QX - C_K), dm = uu(C_MEM + w * 2048), dm2 = uu(w < 2 ? C_MEM + 8192 + w * 1024 : C_DUMP);
    asm volatile("" : "+s"(dk), "+s"(dv), "+s"(dq), "+s"(dm), "+s"(dm2));
    auto keys_wait = [&](u32 need) {
#pragma unroll 1
        for (int n = 0; !dead; ++n) {
            const bool beat = need + 2 * L > 2u;
            if (beat) wait_until(tm);
            const u32 hv = ld4(rs, lane == 7 ? foff(F_ERR) : fkh);
            if (beat) { tm = rdl(hv, 5); wait_until(rdl(hv, 3) - 30u); }
            if (((u32)__builtin_amdgcn_ballot_w64(ld4(rs, fkh) < need) & 7u) == 0u) break;
            dead = n > SPIN_CAP || rdl(hv, 7) != 0u;
            st4s(rs, dead ? foff(F_ERR) : OOB, 0, 1u);
        }
        CFENCE();
        DMA32(dk, l16, ksrc); DMA32(dv, l16, vsrc); DMA32(dq, l16, ksrc ^ 8192u);
    };
    lptr qw = lds + C_Q + (2 * g) * 288 + (32 * w + 2 * r) * 2;
    lptr kw = qw + (C_KM - C_Q); PIN(kw);
    lptr qr = lds + C_Q + (2 * (r >> 2) + (r & 1)) * 288 + g * 16;
    lptr LB = lds + l16, VX = lds + C_V + w * 2048 + l16;
    PIN(VX);
    if (tid >= 64 && tid < 70) { const u32 z = tid == 68 ? 0x3F803F80u : tid == 69 ? PSENT : 0u; lds_w16(lds + C_ZERO + (tid - 64) * 16, (v4u){z, z, z, z}); }
    u32 zaddr = 0; PIN(zaddr);
    lptr ZP = lds + zaddr + C_ZERO;
    const float CEXP = SCALE * LOG2E;
#define LF_RD(slot, hf) lf[slot][hf] = lds_r16(WL + (slot) * 2048 + (hf) * 1024)
    BAR();
    const u32 ofl = tid == 0 ? foff(F_O + L * 32 + h) : OOB;
    u32 oso = OOB + W_ORING + (u32)L * (MB / 2) + ring_map((u32)((r >> 3) * 32768 + h * 1024 + w * 256 + (g >> 1) * 128 + (r & 7) * 16), pol0);
    unsigned long long vis[8];
#pragma unroll
    for (int i = 0; i < 8; ++i) vis[i] = __builtin_amdgcn_ballot_w64(16 * (i >> 2) + 4 * g + (i & 3) <= 16 * (w & 1) + r);
    u32 missed = 0;
    for (int s = -1; s < NSEG;) {
        u32 sv = (u32)max(s, 0), ms = mring + (sv & 3u) * 32768u + w * 2048, ms2 = mring + (sv & 3u) * 32768u + 16384u + (w & 1) * 1024;
        asm volatile("" : "+s"(ms), "+s"(ms2)); PIN(sv);
        f32x4 aq0, aq1, ak0, ak1;
        v4u lf[NLF][2];
        ZRD(aq0, 0); ZRD(aq1, 1); ZRD(ak0, 2); ZRD(ak1, 3);
#pragma unroll
        for (int i = 0; i < 4; ++i) { LF_RD(i, 0); LF_RD(i, 1); }
        SCHED();
        v4u A[10];
#define HT(k) do {} while (0)
        v4u kx[8], qf[4], kmf[4], vx[2], ones, P;
        float fa, fb, fc, fd, inv, sx[4], t[4], tm0, tm1; u32 pa, pb, fl, e01, e23, pmm;
        f32x4 av0, av1, s0, s1, sm, oa, ob, rsum;
        v2u vm, pm;
#define LW(sl) cat8(lf[sl][0], lf[sl][1])
#define PQ(ks, tl) do { if constexpr (!(tl)) SMF_A(aq0, A[ks], wq0[ks]); else if constexpr (qslot(ks) < 0) SMF_A(aq1, A[ks], wq1[ks]); else { const v8u lw_ = LW(qslot(ks)); SMF_V(aq1, A[ks], lw_); } } while (0)
#define PK(ks, tl) do { if constexpr (!(tl)) SMF_A(ak0, A[ks], wk0[ks]); else if constexpr (kslot(ks) < 0) SMF_A(ak1, A[ks], wk1[ks]); else { const v8u lw_ = LW(kslot(ks)); SMF_V(ak1, A[ks], lw_); } } while (0)
#define PV(ks, tl) do { if constexpr (!(tl)) SMF_V(av0, A[ks], wv0[ks]); else if constexpr (vslot(ks) < 0) SMF_V(av1, A[ks], wv1[ks]); else { const v8u lw_ = LW(vslot(ks)); SMF_V(av1, A[ks], lw_); } } while (0)
#define KXR(i) kx[i] = lds_r16(LB + C_K + ((i) & 1) * 4096 + ((i) >> 1) * 1024)
#define XW(q_) do { *(LAS u32*)(q_) = pa; *(LAS u32*)((q_) + 288) = pb; } while (0)
        auto slot = [&](auto zc) __attribute__((always_inline)) {
            constexpr int z = decltype(zc)::value;
            if constexpr (z < 14) PQ(z >> 1, z & 1);
            else if constexpr (z < 17) PQ(z - 7, 0);
            else if constexpr (z < 20) PQ(z - 10, 1);
            else if constexpr (z < 34) PK((z - 20) >> 1, z & 1);
            else if constexpr (z < 37) PK(z - 27, 0);
            else if constexpr (z < 40) PK(z - 30, 1);
            else if constexpr (z < 42) { if constexpr (z == 40) MF0(s0, kx[0], qf[0]); else MF0(s1, kx[1], qf[0]); }
            else if constexpr (z < 48) { if constexpr (z & 1) MF1(s1, kx[z - 40], qf[(z - 40) >> 1]); else MF1(s0, kx[z - 40], qf[(z - 40) >> 1]); }
            else if constexpr (z < 54) PV(z - 48, 0);
            else if constexpr (z == 54) MF0(sm, kmf[0], qf[0]);
            else if constexpr (z < 58) MF1(sm, kmf[z - 54], qf[z - 54]);
            else if constexpr (z < 62) PV(z - 52, 0);
            else if constexpr (z < 64) PV(z - 62, 1);
            else if constexpr (z == 64) MF0(oa, vx[0], P);
            else if constexpr (z == 65) MF0(ob, vx[1], P);
            else if constexpr (z == 66) MF0(rsum, ones, P);
            else if constexpr (z < 75) PV(z < 70 ? z - 63 : z == 70 ? 2 : z == 71 ? 7 : z == 72 ? 3 : z - 65, 1);
            else { const v2u ones2 = ones.lo; MF1H(rsum, ones2, pm); }
            if constexpr (z < 6) A[4 + z] = lds_r16(LB + C_MEM + (4 + z) * 1024);
            if constexpr (z >= 6 && z < 10) LF_RD(4 + ((z - 6) >> 1), z & 1);
            if constexpr (z >= 14 && z < 18) LF_RD(6 + ((z - 14) >> 1), z & 1);
            if constexpr (z >= 22 && z < 28) KXR(z - 22);
            if constexpr (z == 28) XW(qw);
            if constexpr (z == 29 || z == 30) KXR(z - 23);
            if constexpr (z == 32 || z == 47) asm volatile("s_barrier" ::: "memory");
            if constexpr (z >= 33 && z < 37) qf[z - 33] = lds_r16(qr + (z - 33) * 64);
            if constexpr (z == 20) ZRD(av0, 0);
            if constexpr (z == 43) XW(kw);
            if constexpr (z == 44 || z == 45) vx[z - 44] = lds_r16(VX + (z - 44) * 1024);
            if constexpr (z >= 48 && z < 52) kmf[z - 48] = lds_r16(qr + (C_KM - C_Q) + (z - 48) * 64);
            if constexpr (z == 52) ZRD(av1, 1);
            if constexpr (z == 37) ones = lds_r16(ZP + 64);
            if constexpr (z >= 57 && z < 64) LF_RD(8 + ((z - 57) >> 1), (z - 57) & 1);
            if constexpr (z >= 65 && z < 70) LF_RD(8 + ((z - 58) >> 1), (z - 58) & 1);
            if constexpr (z == HD_SMP) LD4A(fl, fkh, 0);
            SCHED();
            if constexpr (z == 19) HT(2);
            if constexpr (z == 32) HT(3);
            if constexpr (z == 47) HT(4);
            if constexpr (z == 64) HT(5);
            if constexpr (z == 19) LGK(4);
            if constexpr (z == 21 || z == 41) LGK(1);
            if constexpr (z == 56 || z == 73) LGK(0);
            if constexpr (z == 31 || z == 46) LGK(2);
            if constexpr (z == 39) LGK(4);
            if constexpr (z >= 53 && z < 56) LGK(57 - z);
            if constexpr (z == 64) LGK(3);
            if constexpr (z == 68) LGK(5);
            if constexpr (z >= 70 && z < 73) LGK(74 - z);
            SCHED();
            if constexpr (z == 18) CADD(fa, aq0[0], aq0[2]);
            if constexpr (z == 19) CADD(fb, aq0[1], aq0[3]);
            if constexpr (z == 20) CMULC(fa, fa);
            if constexpr (z == 21) CADD(fc, aq1[0], aq1[2]);
            if constexpr (z == 22) CADD(fd, aq1[1], aq1[3]);
            if constexpr (z == 23) CMULC(fc, fc);
            if constexpr (z == 24) CMULC(fb, fb);
            if constexpr (z == 25) CMULC(fd, fd);
            if constexpr (z == 26) CCVT(pa, fa, fc);
            if constexpr (z == 27) CCVT(pb, fb, fd);
            if constexpr (z == 38) CADD(fa, ak0[0], ak0[2]);
            if constexpr (z == 39) CADD(fb, ak0[1], ak0[3]);
            if constexpr (z == 41) { CADD(fc, ak1[0], ak1[2]); CADD(fd, ak1[1], ak1[3]); }
            if constexpr (z == 42) { CCVT(pa, fa, fc); CCVT(pb, fb, fd); }
            if constexpr (z >= 49 && z < 53) SEL(sx[z - 49], s0[z - 49], s1[z - 49]);
            if constexpr (z >= 53 && z < 57) VEXP(t[z - 53], sx[z - 53]);
            if constexpr (z == 57) CCVT(e01, t[0], t[1]);
            if constexpr (z == 58) CCVT(e23, t[2], t[3]);
            if constexpr (z == 59) QPERM(P.x, e01, "[0,1,0,1]");
            if constexpr (z == 60) QPERM(P.z, e01, "[2,3,2,3]");
            if constexpr (z == 61) QPERM(P.y, e23, "[0,1,0,1]");
            if constexpr (z == 62) QPERM(P.w, e23, "[2,3,2,3]");
            if constexpr (z == 63) CADD(fa, av0[0], av0[2]);
            if constexpr (z == 64) { asm volatile("" :: "v"(sm)); VEXP(tm0, sm[0]); }
            if constexpr (z == 65) VEXP(tm1, sm[1]);
            if constexpr (z == 66) CADD(fb, av0[1], av0[3]);
            if constexpr (z == 67) CCVT(pmm, tm0, tm1);
            if constexpr (z >= 68 && z < 72) SEL(oa[z - 68], oa[z - 68], ob[z - 68]);
            if constexpr (z == 72) asm volatile("v_cndmask_b32 %0, %1, 0, %2" : "=v"(pm.x) : "v"(pmm), "s"(himask));
            if constexpr (z == 73) asm volatile("v_cndmask_b32 %0, 0, %1, %2" : "=v"(pm.y) : "v"(pmm), "s"(himask));
            if constexpr (z == 74) LGK(15);
            if constexpr (z == 75) CCVT(vm.x, fa, fb);
            SCHED();
        };
        {
            u32 tmp;
            asm volatile("s_mov_b32 m0, %2\n\t" WAIT_TXT("%0", "%1") "buffer_load_dwordx4 %4, %5, %6 offen sc1 lds\n\tbuffer_load_dwordx4 %4, %5, %6 offen offset:1024 sc1 lds\n\t"
                         "s_mov_b32 m0, %3\n\ts_nop 0\n\tbuffer_load_dwordx4 %4, %5, %7 offen sc1 lds\n\ts_waitcnt vmcnt(0) lgkmcnt(0)\n\ts_barrier"
                         : "=&s"(tmp) : "s"(uu(tm - 4u)), "s"(dm), "s"(dm2), "v"(l16), "s"(srd), "s"(ms), "s"(ms2) : "vcc", "scc", "memory");
        }
        HT(1);
#pragma unroll
        for (int ks = 0; ks < 4; ++ks) A[ks] = lds_r16(LB + C_MEM + ks * 1024);
        SCHED();
        st4s(rs, ofl, 0, sv);
        SCHED();
        static_for<0, 76>(slot);
        asm volatile("s_nop 2\n\tv_add_f32 %1, %3, %5\n\tv_add_f32 %2, %4, %6\n\tv_cvt_pk_bf16_f32 %0, %1, %2"
                     : "=v"(vm.y), "=&v"(fc), "=&v"(fd) : "v"(av1[0]), "v"(av1[1]), "v"(av1[2]), "v"(av1[3]));
        __builtin_amdgcn_s_waitcnt(0xCF7F);
        asm volatile("s_nop 0\n\tv_mfma_f32_16x16x16_bf16 %0, %2, %3, %0\n\tv_rcp_f32 %1, %4" : "+v"(oa), "=&v"(inv) : "v"(vm), "v"(pm), "v"(rsum[0]), "v"(rsum));
        SCHED();
        if (__builtin_expect(__builtin_amdgcn_ballot_w64(u_(rsum[0]) - 0x21800000u > 0x3C000000u) != 0ull, 0)) {
            bool miss = false;
#pragma unroll 1
            for (int i = 0; i < 10; ++i) { const v4u a = lds_r16(LB + C_MEM + i * 1024); miss |= a.x == PSENT || a.y == PSENT || a.z == PSENT || a.w == PSENT; }
            if (missed < SPIN_CAP && !all_(!miss)) {
                *(LAS u32*)(lds + C_DUMP + (w == 0 ? 0 : 64)) = ld4(rs, foff(F_ERR));
                asm volatile("" : "+v"(fl) :: "memory");
                tm = rdl(fl, 5);
                BAR();
                missed = uu(*(LAS u32*)(lds + C_DUMP)) != 0u ? SPIN_CAP : missed + 1;
                st4s(rs, missed == SPIN_CAP ? foff(F_ERR) : OOB, 0, 1u);
                BAR();
                continue;
            }
            float mx = fmaxf(fmaxf(fmaxf(sx[0], sx[1]), fmaxf(sx[2], sx[3])), fmaxf(sm[0], sm[1]));
            mx = max32(max16(fmaxf(mx, dpp<0x4E>(mx))));
#pragma unroll
            for (int i = 0; i < 4; ++i) t[i] = __builtin_amdgcn_exp2f(sx[i] - mx);
            e01 = pk(t[0], t[1]); e23 = pk(t[2], t[3]);
            pmm = pk(__builtin_amdgcn_exp2f(sm[0] - mx), __builtin_amdgcn_exp2f(sm[1] - mx));
            asm volatile("v_cndmask_b32 %0, %2, 0, %3\n\tv_cndmask_b32 %1, 0, %2, %3" : "=&v"(pm.x), "=&v"(pm.y) : "v"(pmm), "s"(himask));
            asm volatile("s_nop 1" : "+v"(e01), "+v"(e23), "+v"(pm));
            QPERM(P.x, e01, "[0,1,0,1]"); QPERM(P.z, e01, "[2,3,2,3]"); QPERM(P.y, e23, "[0,1,0,1]"); QPERM(P.w, e23, "[2,3,2,3]");
            NOPS(1);
            MF0(rsum, ones, P); MF0(oa, vx[0], P); MF0(ob, vx[1], P);
            NOPS(7);
            { const v2u ones2 = ones.lo; MF1H(rsum, ones2, pm); }
#pragma unroll
            for (int i = 0; i < 4; ++i) SEL(oa[i], oa[i], ob[i]);
            NOPS(1);
            MF1H(oa, vm, pm);
            asm volatile("s_nop 7\n\tv_rcp_f32 %0, %1" : "=v"(inv) : "v"(rsum[0]), "v"(rsum));
        }
        {
            v4u oc; f32x2 u0 = oa.lo, u1 = oa.hi; const f32x2 i2 = {inv, inv};
            asm volatile("s_nop 0\n\tv_pk_mul_f32 %0, %0, %2\n\tv_pk_mul_f32 %1, %1, %2" : "+v"(u0), "+v"(u1) : "v"(i2));
            __builtin_amdgcn_s_waitcnt(0xCF7F);
            asm volatile("v_cvt_pk_bf16_f32 %0, %4, %5\n\tv_cvt_pk_bf16_f32 %2, %4, %5\n\tv_cvt_pk_bf16_f32 %1, %6, %7\n\tv_cvt_pk_bf16_f32 %3, %6, %7\n\t"
                         "v_permlane16_swap_b32 %0, %2\n\ts_nop 0\n\tv_permlane16_swap_b32 %1, %3"
                         : "=&v"(oc.x), "=&v"(oc.y), "=&v"(oc.z), "=&v"(oc.w) : "v"(u0.x), "v"(u0.y), "v"(u1.x), "v"(u1.y));
            st16(rs, oso, oc);
        }
        SCHED();
        unsigned long long clk;
        asm volatile("s_memrealtime %0" : "=s"(clk) :: "memory");
        HT(6);
        v4u tq[4], tk[8], tv[2];
        f32x4 c0, c1, cm;
        u32 oo, kvo; bool kvok;
        u32 ln = l16; PIN(ln);
        lptr QX = lds + C_QX + (w & 1) * 4096 + ln, VO = lds + C_V + (w ^ 1) * 2048 + ln, VMX = lds + C_VM + w * 512 + (ln >> 1);
#pragma unroll
        for (int i = 0; i < 2; ++i) { tq[i] = lds_r16(QX + i * 1024); kmf[i] = lds_r16(qr + (C_KM - C_Q) + i * 64); tk[2 * i] = lds_r16(LB + C_K + i * 1024); tk[2 * i + 1] = lds_r16(LB + C_K + 4096 + i * 1024); }
        *(LAS v2u*)VMX = vm;
        SCHED();
        SFOR(i, 12)
            if constexpr (i == 0) MF0(cm, kmf[0], tq[0]); else if constexpr (i == 1) MF0(c0, tk[0], tq[0]); else if constexpr (i == 2) MF0(c1, tk[1], tq[0]);
            else if constexpr (i % 3 == 0) MF1(cm, kmf[i / 3], tq[i / 3]); else if constexpr (i % 3 == 1) MF1(c0, tk[2 * (i / 3)], tq[i / 3]); else MF1(c1, tk[2 * (i / 3) + 1], tq[i / 3]);
            if constexpr (i < 2) kmf[2 + i] = lds_r16(qr + (C_KM - C_Q) + (2 + i) * 64);
            if constexpr (i == 0) { tq[2] = lds_r16(QX + 2048); tk[4] = lds_r16(LB + C_K + 2048); }
            if constexpr (i == 1) { tk[5] = lds_r16(LB + C_K + 6144); tq[3] = lds_r16(QX + 3072); }
            if constexpr (i == 2) { tk[6] = lds_r16(LB + C_K + 3072); tk[7] = lds_r16(LB + C_K + 7168); }
            if constexpr (i == 3) { tv[0] = lds_r16(VO); tv[1] = lds_r16(VO + 1024); }
            if constexpr (i == 4) { oo = ln + (ln & 0x300u) + ((vsrc ^ 8192u) + (u32)((w & 1) * 256) + ((u32)s & OOB)); PIN(oo); }
            if constexpr (i == 5) { ksrc += NH * 16384; vsrc += NH * 16384; PINS(ksrc); PINS(vsrc); }
            if constexpr (i == 9) { asm volatile("s_waitcnt vmcnt(1)" : "+v"(fl) :: "memory"); kvok = ((u32)__builtin_amdgcn_ballot_w64(fl < (u32)s + 2) & 7u) == 0u; }
            if constexpr (i == 10) { kvo = l16 | (kvok ? 0u : OOB); PIN(kvo); }
            SCHED();
        SEND
        asm volatile("s_waitcnt lgkmcnt(0)\n\ts_barrier" : "+s"(clk) :: "memory");
        const v2u tvm = *(LAS v2u*)(VMX + (512 - (w & 1) * 1024));
        const v4u pv = lds_r16(ZP + 80);
        DMA32(dk, kvo, ksrc);
        SCHED(); asm volatile("" :: "v"(cm));
        float u[10], m, m2;
        {
            float NEG = -1000000000.0f / SCALE; PIN(NEG);
#define MSK(d, a, i) asm volatile("v_cndmask_b32 %0, %1, %2, %3" : "=v"(d) : "v"(NEG), "v"(a), "s"(vis[i]))
#pragma unroll
            for (int i = 0; i < 4; ++i) MSK(u[i], c0[i], i);
#pragma unroll
            for (int i = 0; i < 4; ++i) MSK(u[4 + i], c1[i], 4 + i);
            asm volatile("v_max3_f32 %0, %2, %3, %10\n\tv_max3_f32 %1, %4, %5, %11\n\tv_max3_f32 %0, %0, %6, %7\n\tv_max3_f32 %1, %1, %8, %9\n\t"
                         "v_max_f32 %0, %0, %1\n\tv_mov_b32 %1, %0\n\ts_nop 1\n\tv_permlane16_swap_b32 %0, %1\n\tv_max_f32 %0, %0, %1\n\tv_mov_b32 %1, %0\n\ts_nop 1\n\t"
                         "v_permlane32_swap_b32 %0, %1\n\tv_max_f32 %0, %0, %1\n\tv_mul_f32 %0, 0xbe0293ee, %0"
                         : "=&v"(m), "=&v"(m2) : "v"(u[0]), "v"(u[1]), "v"(u[2]), "v"(u[3]), "v"(u[4]), "v"(u[5]), "v"(u[6]), "v"(u[7]), "v"(cm[0]), "v"(cm[1]));
        }
        DMA32(dv, kvo, vsrc);
        {
            v4u px; v2u p0, p1;
#define EX2(d, a, b) asm volatile("v_fmamk_f32 %0, %2, 0x3e0293ee, %4\n\tv_fmamk_f32 %1, %3, 0x3e0293ee, %4\n\tv_exp_f32 %0, %0\n\tv_exp_f32 %1, %1" : "=&v"(d.x), "=&v"(d.y) : "v"(a), "v"(b), "v"(m))
            f32x2 ex[5];
            EX2(ex[0], u[0], u[1]); EX2(ex[1], u[2], u[3]); EX2(ex[2], u[4], u[5]); EX2(ex[3], u[6], u[7]); EX2(ex[4], cm[0], cm[1]);
            asm volatile("v_cvt_pk_bf16_f32 %0, %8, %9\n\tv_cvt_pk_bf16_f32 %1, %10, %11\n\tv_cvt_pk_bf16_f32 %2, %12, %13\n\tv_cvt_pk_bf16_f32 %3, %14, %15\n\t"
                         "v_mov_b32 %5, 0\n\tv_mov_b32 %6, 0\n\tv_cvt_pk_bf16_f32 %4, %16, %17\n\tv_cvt_pk_bf16_f32 %7, %16, %17"
                         : "=&v"(px.x), "=&v"(px.y), "=&v"(px.z), "=&v"(px.w), "=&v"(p0.x), "=&v"(p0.y), "=&v"(p1.x), "=&v"(p1.y)
                         : "v"(ex[0].x), "v"(ex[0].y), "v"(ex[1].x), "v"(ex[1].y), "v"(ex[2].x), "v"(ex[2].y), "v"(ex[3].x), "v"(ex[3].y), "v"(ex[4].x), "v"(ex[4].y));
            f32x4 o[4], sum;
            const v2u ones2 = ones.lo;
            const u32 now = (u32)clk;
            u32 oz;
            SCHED();
            MF0(sum, ones, px); { const u32 q = ksrc ^ 8192u; DMA32(dq, kvo, q); } SCHED();
            MF0(o[0], vx[0], px);
            MF0(o[1], vx[1], px);
            MF0(o[2], tv[0], px); oz = (oso & ~(3u << 17)) | (u32)((s + 3) & 3) << 17; PIN(oz); SCHED();
            MF0(o[3], tv[1], px); oso = (oso & ~(3u << 17)) | (u32)((s + 1) & 3) << 17; PIN(oso); SCHED();
            MF1H(sum, ones2, p0);
            MF1H(o[0], vm, p0);
            MF1H(o[1], vm, p1); st16(rs, oz, pv); SCHED();
            MF1H(o[2], tvm, p0);
            MF1H(o[3], tvm, p1);
            asm volatile("s_nop 2\n\tv_rcp_f32 %0, %1\n\ts_nop 0" : "=v"(inv) : "v"(sum[0]), "v"(sum));
            asm volatile("" : "+v"(o[0]), "+v"(o[1]));
            st16(rs, oo, (v4u){pk2(o[0].lo * inv), pk2(o[0].hi * inv), pk2(o[1].lo * inv), pk2(o[1].hi * inv)});
            asm volatile("" : "+v"(o[2]), "+v"(o[3]));
            st16(rs, oo ^ 2048u, (v4u){pk2(o[2].lo * inv), pk2(o[2].hi * inv), pk2(o[3].lo * inv), pk2(o[3].hi * inv)});
            SCHED();
            LS2(L == 0 && w == 0, h);
            st4s(rs, (h == 0 && tid == 192) ? hint + 128u : OOB, 0, (u32)s << 16 | min(missed, 1u) << 14 | min(now - tm, 0x3FFFu));
            tm = rdl(fl, 5);
            missed = missed == SPIN_CAP ? missed : 0u;
            if (__builtin_expect((int)(rdl(fl, 3) - now) < CH_VIS - 4 || s < 1 - L, 0)) {
                const bool first = s == 0 && L == 0;
                if (s < 0) { oso -= (ln & 256u) ? 0u : OOB; tm = L ? tm : 0u; st4s(rs, tid == 0 ? hint + 4096u + (u32)h * 128u : OOB, 0, 0u); }
#pragma unroll 1
                for (int n = 0; n < (s < 0 ? 0 : first ? 200 : 8); ++n) {
                    CFENCE();
                    const u32 hv = ld4(rs, fkh);
                    tm = rdl(hv, first ? 6 : 5);
                    if (first ? tm != PSENT : (int)(rdl(hv, 3) - now) >= CH_VIS - 4) break;
                }
            }
        }
        if (__builtin_expect(!kvok, 0) && s + 1 < NSEG) {
            st4s(rs, ln == 0 ? hint + 4096u + (u32)h * 128u : OOB, 0, (u32)s + 1u);
            keys_wait((u32)s + 2u);
        }
        ++s;
    }
    DRAIN(); BAR();
    st4s(rs, ofl, 0, NSEG);
}

#define WO_NL 3
#define WO_ROT 0
#define WO_AH 1
DEV void role_wo(const Params& p, lptr lds, int j, v2u ep) {
    const int tid = threadIdx.x, lane = tid & 63, w = uni(tid >> 6), g = lane >> 4, r = lane & 15;
    const int rg = j & 1, cg = j >> 1, rot = WO_ROT;
    const rsrc_t rs = mk_rsrc(p.ws, W_END);
    v8u wo[8][5];
    {
        const v4u so_ = mk_srd(p.Wo, DM * NQ * 2);
        u32 vo[5];
#pragma unroll
        for (int i = 0; i < 5; ++i) { const int n = 64 * i + lane; vo[i] = (u32)((n / 5) * DM + cg * 40 + (n % 5) * 8) * 2u; }
        v4u junk = {};
        const u32 av = (u32)(j * 5120 + lane * 16);
        auto ahead = [&](int b) {
#pragma unroll
            for (int i = 0; i < 5; ++i) AHEAD(so_, av, uu((8 * w + b) * (DH * DM * 2) + i * 1024));
        };
        auto fill = [&](int b) {
            if (b + WO_AH < 8) ahead(b + WO_AH);
            const u32 dst = uu(w * 40960 + (b & 3) * 10240), src = uu((8 * w + ((b + rot) & 7)) * (DH * DM * 2));
#pragma unroll
            for (int i = 0; i < 10; ++i) DMAIN(so_, dst + i * 1024, vo[i % 5], src + (i / 5) * (64 * DM * 2));
        };
#pragma unroll 1
        for (int b = 0; b < WO_AH; ++b) ahead(b);
#pragma unroll 1
        for (int b = 0; b < 4; ++b) fill(b);
        const int q4 = r >> 2;
        lptr tb = lds + w * 40960 + (32 * (g >> 1) + 16 * (q4 >> 1) + 8 * ((r >> 1) & 1) + 4 * (g & 1) + (q4 & 1)) * 80 + (r & 1) * 8;
        SFOR(b, 8)
            VMW((b < 5 ? 30 : 70 - 10 * b) + (b + 1 + WO_AH < 8 ? 5 : 0) + (b + 2 + WO_AH < 8 ? 5 : 0) + (b + 3 + WO_AH < 8 ? 5 : 0));
            if constexpr (b + WO_AH == 7) PIN(junk);
#pragma unroll
            for (int t = 0; t < 5; ++t) {
                lptr q = tb + (b & 3) * 10240 + t * 16;
                wo[b][t] = tr_frag(q, q + 160, q + 64 * 80, q + 64 * 80 + 160);
                if (b * 5 + t < 32) asm volatile("" : "+a"(wo[b][t])); else asm volatile("" : "+v"(wo[b][t]));
            }
            CFENCE();
            if constexpr (b < 4) fill(b + 4);
        SEND
    }
    if (j == 0) LST(265, rt32());
    LST(1100 + j * 4 + w, rt32());
    const u32 idx = (r & 2) ? 0xEEEEu : 0x4444u;
    const u32 pol0 = pol_wait(rs, ep);
    BAR();
    const u32 l16 = (u32)lane * 16u;
    u32 oo[8];
#pragma unroll
    for (int i = 0; i < 8; ++i) oo[i] = uu(W_ORING + ring_map((u32)(rg * 32768 + (8 * w + ((i + rot) & 7)) * 1024), pol0));
    const u32 hint = W_MRING + 4 * 32768u + pol0 * 8192u;
    u32 hvo[2];
#pragma unroll
    for (int l = 0; l < 2; ++l) hvo[l] = lane < 2 ? hint + (u32)(l ^ 1) * (MB / 4) + (u32)lane * 4u : lane == 2 ? hint + (u32)l * (MB / 4) + 128u : lane == 3 ? foff(F_ERR) :
                 (lane >= 32 && j == 0 && w == 3) ? hint + (u32)l * (MB / 4) + (u32)lane * 128u : OOB;
    lptr xw = r < 8 ? lds + w * 640 + (2 * (g & 1) + (g >> 1)) * 32 + r * 4 : lds + 8192 + lane * 4;
    const int G = ((5 * cg) >> 1) + w, tt = 2 * G + ((lane >> 3) & 1) - 5 * cg;
    const bool pub = w < 3 && lane < 32 && tt >= 0 && tt < 5;
    lptr xr = lds + (pub ? tt * 128 + (2 * (lane >> 4) + ((lane >> 2) & 1)) * 32 + (lane & 3) * 8 : 0);
    const u32 mpo = pub ? W_MRING + ring_map((u32)(G * 256 + rg * 128 + lane * 4), pol0) : OOB;
    constexpr u32 PER0 = CH_W0 + CH_MW + CH_H0 + CH_MH + 2 * CH_VIS, DW0 = CH_W0 + CH_VIS + CH_MW + CH_REC;
    const bool lead = j == 0 && w == 3;
    u32 hso = OOB;
    MK_SRD();
    u32 t = 0, tn = 0, tm0 = 0, dry = 2;
    int sn[2] = {0, 0};
    u32 hav = 4 * CH_H0, wav = 4 * CH_W0, mh = 8 * CH_MH, mw = 8 * CH_MW, ok[2] = {0u, 0u}, dw[2] = {DW0, DW0};
    auto start = [&] {
        if (w == 0) LST(1310 + j, rt32());
        if (lead) {
#pragma unroll 1
            for (int n = 0; n < SPIN_CAP; ++n) {
                CFENCE();
                const u32 a = ld4(rs, foff(F_QKV + lane)), b = ld4(rs, lane < 32 ? foff(F_QKV + 64 + lane) : hvo[0]);
                if (all_(a != 0u && b != (lane < 32 ? 0u : PSENT))) break;
                __builtin_amdgcn_s_sleep(1);
            }
        }
        if (lead) {
            t = rt32() + CH_T0; tn = t + PER0 / 2 + CH_REC;
            st16(rs, lane < 2 ? hint + (u32)lane * (MB / 4) : OOB, (v4u){lane == 0 ? t : tn, PER0, (lane == 0 ? t : tn) + DW0, t + DW0});
        } else {
#pragma unroll 1
            for (int n = 0; n < SPIN_CAP; ++n) {
                CFENCE();
                const u32 hv = ld4(rs, hvo[1]);
                t = rdl(hv, 0);
                if (rdl(hv, 1) < 400u) break;
                __builtin_amdgcn_s_sleep(1);
            }
        }
        hso = (lead && lane == 0) ? hint : OOB; tm0 = t + DW0; dw[0] = dw[1] = DW0; dry = 0;
    };
    u32 zero = 0, hv = 0xFFFFu, pst = 1, pdur = 0, pmp = OOB;
    const u32 psent = PSENT;
    lptr sw = lds + 2560 + w * 4;
    bool stop = false;
    asm volatile("" : "+v"(zero), "+v"(sw));
    auto turn = [&](auto lc) __attribute__((always_inline)) {
        constexpr int L = decltype(lc)::value, M = L ^ 1;
        u32 now, done, rep, rd, lost, dh, dn, nt, za; v4u hw;
        const int sc = sn[L];
        auto books = [&](auto ic) __attribute__((always_inline)) {
            constexpr int i = decltype(ic)::value;
            if constexpr (i == 0) { rep = rdl(hv, 2); PINS(rep); }
            if constexpr (i == 1) { lost = (u32)(__builtin_amdgcn_ballot_w64(hv == (u32)sn[M]) >> 32); PINS(lost); }
            if constexpr (i == 2) { lost |= (rep >> 16) ^ (u32)sn[M]; PINS(lost); }
            if constexpr (i == 3) { lost |= ok[M] ^ 2u; rd = rep & 0xFFFFu; PINS(lost); PINS(rd); }
            if constexpr (i == 4) { lost = lost == 0u ? CH_UP * 8u : 0u; PINS(lost); }
            if constexpr (i == 5) { dn = wav >> 2; PINS(dn); pdur = min(pdur, dn + 8u); PINS(pdur); }
            if constexpr (i == 6) { pdur = (done & ok[M]) ? pdur : dn; PINS(pdur); }
            if constexpr (i == 7) { wav += pdur - dn; PINS(wav); }
            if constexpr (i == 8) { dh = hav >> 2; PINS(dh); nt = min(rd, dh + 8u); PINS(nt); }
            if constexpr (i == 9) { nt = rd < 0x3FFFu ? nt : dh; PINS(nt); }
            if constexpr (i == 10) { hav += nt - dh; PINS(hav); }
            if constexpr (i == 11) { mw = max(mw - 1u, CH_MW * 8u); PINS(mw); }
            if constexpr (i == 12) { mh = max(mh - 1u, CH_MH * 8u); PINS(mh); }
            if constexpr (i == 13) { rd = rd >> 14 == 1u ? lost : 0u; PINS(rd); }
            if constexpr (i == 14) { mw += rd; mh += lost - rd; PINS(mw); PINS(mh); }
            if constexpr (i == 15) { ok[M] = done ? 1u : (ok[M] & 1u) << 1; PINS(ok[M]); }
            if constexpr (i == 16) { dh = (2u * hav + mh) / 8u + (sn[1] < 3 ? CH_VIS + CH_PRE : CH_VIS); PINS(dh); }
            if constexpr (i == 17) { dn = (2u * wav + mw) / 8u + CH_VIS; PINS(dn); }
            if constexpr (i == 18) { nt = dw[L] + dh; PINS(nt); }
            if constexpr (i == 19) { dh += dn; dw[L] = dn; sn[M] += (int)done; PINS(dh); PINS(sn[M]); }
            if constexpr (i == 20) { rd = tn - t + (dh >> 1); PINS(rd); }
            if constexpr (i == 21) { rd = sn[M] + 1 < NSEG ? rd : 0u; PINS(rd); }
            if constexpr (i == 22) { nt = t + (u32)max((int)nt, (int)rd); PINS(nt); }
            if constexpr (i == 23) { hw.x = nt; hw.y = dh; PIN(hw.x); PIN(hw.y); }
            if constexpr (i == 24) { hw.z = nt + dn; hw.w = tm0; PIN(hw); }
            if constexpr (i == 25) st16(rs, hso + L * (MB / 4), hw);
        };
        if (__builtin_expect(sc + 1 < NSEG, 1)) {
            u32 vo = l16 + (L * (MB / 2) + (((u32)sc + dry) & 3u) * 131072u), mp, hn;
            PIN(vo);
#define WT(k) do {} while (0)
            WT(0);
            v4u d[8];
            {
                u32 tmp;
                asm volatile(WAIT_TXT("%1", "%2") "buffer_load_dwordx4 %0, %3, %4, %5 offen sc1\n\ts_waitcnt lgkmcnt(0)"
                             : "=&v"(d[0]), "=&s"(tmp) : "s"(uu(t - 4u)), "v"(vo), "s"(srd), "s"(oo[0]) : "vcc", "scc", "memory");
            }
            SFOR(i, 7)
                asm volatile("buffer_load_dwordx4 %0, %1, %2, %3 offen sc1" : "=v"(d[i + 1]) : "v"(vo), "s"(srd), "s"(oo[i + 1]) : "memory");
                SCHED();
                if constexpr (i == 0) { done = uu(pst) ^ 1u; PINS(done); }
                static_for<3 * i, 3 * i + 3>(books);
                SCHED();
            SEND
            LD4A(hn, hvo[L], 0);
            SCHED();
            WT(1);
            static_for<21, 26>(books);
            za = done ? pmp : OOB;
            st4s(rs, za, 0, psent);
            *(LAS u32*)(sw + L * 4096) = zero;
            f32x4 acc[5] = {};
            mp = mpo + (L * (MB / 4) + (u32)((sc + 1) & 3) * 32768u); pmp = mpo + (L * (MB / 4) + (u32)(sc & 3) * 32768u);
            asm volatile("" : "+v"(acc[0]), "+v"(acc[1]), "+v"(acc[2]), "+v"(acc[3]), "+v"(acc[4]), "+v"(mp), "+v"(pmp));
            SCHED();
            float f0[5], f1[5]; unsigned long long bad;
#define WSW(o_) asm volatile("v_permlane32_swap_b32 %0, %1" : "+v"(f0[o_]), "+v"(f1[o_]))
#define WAD(o_) asm volatile("v_add_f32_dpp %0, %1, %0 row_ror:8 row_mask:0xf bank_mask:0xf" : "+v"(f0[o_]) : "v"(f1[o_]))
#define WFO(o_) do { VADD(f0[o_], acc[o_][0], acc[o_][2]); VADD(f1[o_], acc[o_][1], acc[o_][3]); } while (0)
#define WWR(o_) *(LAS float*)(xw + L * 4096 + (o_) * 128) = f0[o_]
            auto slot = [&](auto ic_) __attribute__((always_inline)) {
                constexpr int i = decltype(ic_)::value;
                constexpr int NL = WO_NL, I0 = 40 - 5 * NL, pc = i < I0 ? i / 5 : 8 - NL + (i - I0) % NL, oc = i < I0 ? i % 5 : (i - I0) / NL;
                if constexpr (oc == 0) asm volatile("s_waitcnt vmcnt(%1)" : "+v"(d[pc]) : "i"(10 - pc));
                if constexpr (pc * 5 + oc < 32) SMF_A(acc[oc], d[pc], wo[pc][oc]); else SMF_V(acc[oc], d[pc], wo[pc][oc]);
                SCHED();
                if constexpr (i == 0) WT(2);
                if constexpr (i == 20) WT(4);
                constexpr int e = i - (I0 + NL + 1), o = e < 0 ? 0 : e / 3;
                if constexpr (e >= 0 && e % 3 == 0) { CADD(f0[o], acc[o][0], acc[o][2]); if constexpr (o > 0) WAD(o - 1); }
                if constexpr (e >= 0 && e % 3 == 1) { if constexpr (o > 0) { WWR(o - 1); SCHED(); } CADD(f1[o], acc[o][1], acc[o][3]); }
                if constexpr (e >= 0 && e % 3 == 2) {
                    if constexpr (o == 0) asm volatile("v_cmp_u_f32 %0, %1, %2" : "=s"(bad) : "v"(f0[0]), "v"(f1[0]));
                    else LGK(15);
                    WSW(o);
                }
                SCHED();
            };
            static_for<0, 2>(slot);
            if (__builtin_expect(__builtin_amdgcn_ballot_w64(d[0].x == PSENT + dry) != 0ull, 0)) {
                asm volatile("s_waitcnt vmcnt(3)" : "+v"(d[1]), "+v"(d[2]), "+v"(d[3]), "+v"(d[4]), "+v"(d[5]), "+v"(d[6]), "+v"(d[7]));
#pragma unroll
                for (int i = 0; i < 5; ++i) *(LAS u32*)(xw + L * 4096 + i * 128) = psent;
                *(LAS u32*)(sw + L * 4096) = 1u;
            } else {
            static_for<2, 40>(slot);
            WT(5);
            static_assert(WO_NL == 3, "tail");
            if (__builtin_expect(bad != 0ull, 0)) {
                bool miss = false;
#pragma unroll
                for (int i = 0; i < 8; ++i) miss |= d[i].x == PSENT;
                if (all_(!miss)) {
#pragma unroll
                    for (int i = 0; i < 8; ++i) miss |= d[i].y == PSENT || d[i].z == PSENT || d[i].w == PSENT;
                }
                *(LAS u32*)(sw + L * 4096) = all_(!miss) ? 0u : 1u;
            }
            asm volatile("v_permlane32_swap_b32 %0, %1\n\ts_nop 1\n\tv_add_f32_dpp %0, %1, %0 row_ror:8 row_mask:0xf bank_mask:0xf\n\tv_add_f32 %2, %4, %6\n\tv_add_f32 %3, %5, %7"
                         : "+v"(f0[3]), "+v"(f1[3]), "=&v"(f0[4]), "=&v"(f1[4]) : "v"(acc[4][0]), "v"(acc[4][1]), "v"(acc[4][2]), "v"(acc[4][3]));
            WWR(3); SCHED();
            asm volatile("s_nop 0\n\tv_permlane32_swap_b32 %0, %1\n\ts_nop 1\n\tv_add_f32_dpp %0, %1, %0 row_ror:8 row_mask:0xf bank_mask:0xf" : "+v"(f0[4]), "+v"(f1[4]));
            WWR(4);
            }
            WT(6);
            f32x2 a, b, c, e; u32 cw, mq; v4u sv;
            asm volatile("s_waitcnt lgkmcnt(0)\n\ts_barrier\n\tds_read_b64 %0, %5 offset:%7\n\tds_read_b64 %1, %5 offset:%7+640\n\tds_read_b64 %2, %5 offset:%7+1280\n\tds_read_b64 %3, %5 offset:%7+1920\n\t"
                         "ds_read_b128 %4, %6 offset:%7+2560\n\t"
                         "s_waitcnt lgkmcnt(3)\n\tv_pk_add_f32 %0, %0, %1\n\ts_waitcnt lgkmcnt(1)\n\tv_pk_add_f32 %2, %2, %3\n\ts_nop 0\n\tv_pk_add_f32 %0, %0, %2"
                         : "=&v"(a), "=&v"(b), "=&v"(c), "=&v"(e), "=&v"(sv) : "v"(xr), "v"(zero), "i"(L * 4096) : "memory");
            asm volatile("v_cmp_u_f32 vcc, %2, %3\n\tv_cvt_pk_bf16_f32 %0, %2, %3\n\tv_cndmask_b32 %1, %4, %5, vcc" : "=&v"(cw), "=&v"(mq) : "v"(a.x), "v"(a.y), "v"(mp), "v"(OOB) : "vcc");
            st4s(rs, mq, 0, cw);
            SCHED();
            now = rt32();
            asm volatile("s_waitcnt vmcnt(3) lgkmcnt(0)" : "+v"(hn), "+v"(sv)); hv = hn;
            WT(7);
            pst = sv.x | sv.y | sv.z | sv.w;
            if (__builtin_expect(__builtin_amdgcn_ballot_w64(mq != mp && pst == 0u) != 0ull, 0)) st4s(rs, mp, 0, cw);
        } else {
            done = uu(pst) ^ 1u;
            st4s(rs, done ? pmp : OOB, 0, psent);
            static_for<0, 25>(books);
            pst = 1;
            hv = ld4(rs, hvo[L]);
            PIN(hv);
            now = rt32();
        }
        pdur = now - t;
        const u32 per = rdl(hv, 1), th = rdl(hv, 0);
        t = lead ? tn : (int)(th - now) < -(int)(per >> 1) ? th + per : th;
        tn = nt;
        dw[M] += max((int)(now + CH_WX - t), 0);
        stop = stop || rdl(hv, 3) != 0u;
    };
    for (int k = 0; k < 2 * SPIN_CAP && !stop && (sn[0] + 1 < NSEG || sn[1] + 1 < NSEG); ++k) {
        if (k == 1) start();
        asm volatile("" : "+v"(pdur), "+v"(dw[0]), "+v"(dw[1])); pdur = uu(pdur); dw[0] = uu(dw[0]); dw[1] = uu(dw[1]);
        turn(IC<0>{}); turn(IC<1>{});
    }
    if (sn[0] + 1 < NSEG || sn[1] + 1 < NSEG) st4s(rs, tid == 0 ? foff(F_ERR) : OOB, 0, 1u);
}

__global__ void __launch_bounds__(256, 1) k_main(Params p) {
    lptr lds = (lptr)(u32)16 - 16;
    if (blockIdx.x == 0) LST(256, rt32());
    unsigned long long pc; int len;
    asm volatile("s_getpc_b64 %0\n.Lk_main_here:\n\ts_mov_b32 %1, .Lk_end_here-.Lk_main_here" : "=s"(pc), "=s"(len));
    const rsrc_t rs = mk_rsrc(p.ws, W_END);
    const v2u e0 = ld8s(rs, threadIdx.x == 0 ? foff(F_EPOCH) : OOB, 0);
    const v4u code = __builtin_amdgcn_raw_buffer_load_b128(mk_rsrc((const void*)(pc & ~15ull), (u32)max(len, 0)), (((blockIdx.x >> 3) & 31u) * 256u + threadIdx.x) * 16u, 0, 0);
    CFENCE();
    auto conv_id = [](int role) {
        const int x = role >> 5, i = role & 31, no = x < 5 ? 5 : 10, og = x < 5 ? x - 3 : 2 * (x - 4) + (i >= 5), r = (x - 3) * 5 + i - no, q = r / 3;
        return x < 3 || i >= no + 5 || r >= 24 ? NCV : i < no ? ((og & 1) ? (og >> 1) * 5 + i % 5 : NCV) : r % 3 == 0 ? 20 + (q & 1) * 20 + (q >> 1) : 16 + (r % 3) * 8 + q;
    };
    int role = (int)(((u32)__builtin_amdgcn_s_getreg(20 | (3 << 11)) & 7u) * 32u + (blockIdx.x >> 3));
    Early e;
    v4u rv;
#pragma unroll 1
    for (int n = 0;; ++n) {
        conv_early(p, rs, conv_id(role), e);
        if (n) break;
        CFENCE();
        if (threadIdx.x == 0) {
            GAS unsigned long long* slots = (GAS unsigned long long*)(p.ws + W_CTRL + F_SLOT * 128);
            const unsigned long long tag = (unsigned long long)e0.y << 32 | (e0.x + 1u);
            u32 mine = (u32)role;
#pragma unroll 1
            for (int k = 0; k < 256 && __hip_atomic_exchange(slots + mine * 16, tag, __ATOMIC_RELAXED, __HIP_MEMORY_SCOPE_AGENT) == tag; ++k) mine = (mine + 1u) & 255u;
            lds_w16(lds, (v4u){mine, e0.x, e0.y, 0u});
        }
        BAR();
        rv = lds_r16(lds);
        BAR();
        if (uni((int)rv.x) == role) break;
        role = uni((int)rv.x);
    }
    const int x = role >> 5, i = role & 31, w = uni(threadIdx.x >> 6), no = x < 5 ? 5 : 10, nr = x < 7 ? 5 : 4, j = conv_id(role);
    const v2u ep = {uu(rv.y), uu(rv.z)};
    asm volatile("" :: "v"(code));
    if (x < 2) { role_head(p, lds, i >> 4, x * 16 + (i & 15), ep); __builtin_amdgcn_endpgm(); }
    if (x == 2) { role_wo(p, lds, i, ep); __builtin_amdgcn_endpgm(); }
    if (i >= no + nr) { role_qkv(p, lds, (x < 5 ? (x - 3) * 22 : 44 + (x - 5) * 17) + i - no - nr, ep); __builtin_amdgcn_endpgm(); }
    const bool cv = j >= NPRE || (j >= 20 && j < 24);
    if (j < NCV) conv_stage(p, rs, lds, j, (j & 3) * 2 + (j >= NPRE), cv ? 40 : 0, ep, e);
    if (i >= no) {
        if (!cv) role_row(p, lds, (j - 24) * 4 + w, ep);
        __builtin_amdgcn_endpgm();
    }
    const int og = x < 5 ? x - 3 : 2 * (x - 4) + (i >= 5);
    role_out(p, lds, og & 1, og >> 1, i % 5, ep);
}
__global__ void k_end() { asm volatile(".Lk_end_here:"); }

extern "C" void kernel_launch(void* const* d_in, const int* in_sizes, int n_in, void* d_out, int out_size, void* d_ws, size_t ws_size,
                              hipStream_t stream) {
    Params p;
    p.x = (const u16*)d_in[0]; p.mem0 = (const u16*)d_in[1]; p.Wq = (const u16*)d_in[2]; p.Wk = (const u16*)d_in[3];
    p.Wv = (const u16*)d_in[4]; p.Wo = (const u16*)d_in[5];
    p.bk = (const u16*)d_in[8]; p.bv = (const u16*)d_in[9]; p.shift = (const u16*)d_in[10]; p.ssmk = (const u16*)d_in[11]; p.ssmD = (const u16*)d_in[12];
    p.out = (u16*)d_out;
    p.ws = (unsigned char*)(((size_t)d_ws + 2 * MB - 1) & ~(size_t)(2 * MB - 1));
    (void)hipFuncSetAttribute((const void*)k_main, hipFuncAttributeMaxDynamicSharedMemorySize, LDS_BYTES);
    hipLaunchKernelGGL(k_main, dim3(256), dim3(256), LDS_BYTES, stream, p);
}
```

```cpp
#include <hip/hip_runtime.h>
#include <hip/hip_bf16.h>

typedef unsigned short u16;
typedef unsigned int u32;
typedef u32 v2u __attribute__((ext_vector_type(2)));
typedef u32 v4u __attribute__((ext_vector_type(4)));
typedef u32 v8u __attribute__((ext_vector_type(8)));
typedef float f32x2 __attribute__((ext_vector_type(2)));
typedef float f32x4 __attribute__((ext_vector_type(4)));
typedef float f32x16 __attribute__((ext_vector_type(16)));
typedef __bf16 bf16x2 __attribute__((ext_vector_type(2)));
typedef __bf16 bf16x8 __attribute__((ext_vector_type(8)));
typedef short s16x4 __attribute__((ext_vector_type(4)));
#define LAS __attribute__((address_space(3)))
#define GAS __attribute__((address_space(1)))
typedef LAS unsigned char* lptr;
#define DEV __device__ __forceinline__

constexpr int SEQ = 2048, DM = 640, NH = 32, DH = 128, NSEG = 64, NQ = NH * DH;

constexpr u32 MB = 1u << 20;
constexpr u32 W_ORING = 0;
constexpr u32 W_MRING = MB;
constexpr u32 W_PROBE = 2 * MB - 65536;
constexpr u32 W_CTRL = 4 * MB;
constexpr u32 CTRL_BYTES = 128 * 1024;
constexpr u32 ACT = SEQ * DM * 2;
constexpr u32 W_H1 = 5 * MB, W_Y = W_H1 + ACT, W_H2 = W_Y + ACT, W_X2 = W_H2 + ACT;
constexpr u32 W_KQ = 16 * MB;
constexpr u32 W_VO = W_KQ + 64 * MB;
constexpr u32 W_P = W_VO + 64 * MB;
constexpr u32 P_ITEM = 32 * 4 * DM * 4;
constexpr u32 W_KVT = W_P + 2 * NSEG * P_ITEM;
constexpr u32 W_SP = W_KVT + ACT;
constexpr u32 W_END = W_KVT + 3 * MB;
static_assert((size_t)W_END + 2 * MB <= 256ull * MB, "workspace");
constexpr u32 F_ERR = 0, F_QKV = 16, F_O = F_QKV + 192, F_P = F_O + 64, F_H2 = F_P + 64, F_CLR = F_H2 + 32,
              F_EPOCH = F_CLR + 32, F_SLOT = F_EPOCH + 1, F_PRE = F_SLOT + 256, F_N = F_PRE + 192;
constexpr int NCV = 44, NPRE = 40;
static_assert(F_N * 128 <= CTRL_BYTES, "ctrl");
DEV u32 foff(u32 idx) { return W_CTRL + idx * 128u; }

constexpr u32 OOB = 0x80000000u;
constexpr u32 PSENT = 0x7F817F81u;
constexpr int SPIN_CAP = 40000;
constexpr int LDS_BYTES = 160 * 1024;

#define LS3(cond, slot, idx) do {} while (0)
#define LST(idx, v) do {} while (0)
#define LS2(cond, idx) do {} while (0)
struct Params {
    const u16 *x, *mem0, *Wq, *Wk, *Wv, *Wo, *bk, *bv, *shift, *ssmk, *ssmD;
    u16* out;
    unsigned char* ws;
};

typedef __amdgpu_buffer_rsrc_t rsrc_t;
DEV rsrc_t mk_rsrc(const void* p, u32 bytes) { return __builtin_amdgcn_make_buffer_rsrc((void*)p, 0, bytes, 0x00020000); }
DEV v4u ld16s(rsrc_t r, u32 off, u32 so) { return __builtin_amdgcn_raw_buffer_load_b128(r, off, so, 16); }
DEV v2u ld8s(rsrc_t r, u32 off, u32 so) { return __builtin_amdgcn_raw_buffer_load_b64(r, off, so, 16); }
DEV v4u ldin16(rsrc_t r, u32 off, u32 so) { return __builtin_amdgcn_raw_buffer_load_b128(r, off, so, 2); }
DEV v2u ldin8(rsrc_t r, u32 off, u32 so) { return __builtin_amdgcn_raw_buffer_load_b64(r, off, so, 2); }
DEV u32 ld4(rsrc_t r, u32 off) { return __builtin_amdgcn_raw_buffer_load_b32(r, off, 0, 16); }
DEV void st16(rsrc_t r, u32 off, v4u v) { __builtin_amdgcn_raw_buffer_store_b128(v, r, off, 0, 16); }
DEV void st8s(rsrc_t r, u32 off, u32 so, v2u v) { __builtin_amdgcn_raw_buffer_store_b64(v, r, off, so, 16); }
DEV void st4s(rsrc_t r, u32 off, u32 so, u32 v) { __builtin_amdgcn_raw_buffer_store_b32(v, r, off, so, 16); }
#define DRAIN() asm volatile("s_waitcnt vmcnt(0)" ::: "memory")
#define VMW(n) asm volatile("s_waitcnt vmcnt(%0)" :: "i"(n) : "memory")
#define BAR() asm volatile("s_waitcnt lgkmcnt(0)\n\ts_barrier" ::: "memory")
#define SCHED() __builtin_amdgcn_sched_barrier(0)
#define CFENCE() asm volatile("" ::: "memory")
#define NOPS(n) asm volatile("s_nop %0" :: "i"(n))
#define PIN(x) asm volatile("" : "+v"(x))
#define PINS(x) asm volatile("" : "+s"(x))

template <int V> struct IC { static constexpr int value = V; };
template <int I, int N, class F> DEV void static_for(F&& f) { if constexpr (I < N) { f(IC<I>{}); static_for<I + 1, N>(f); } }
#define SFOR(var, n) static_for<0, n>([&](auto ic_) __attribute__((always_inline)) { constexpr int var = decltype(ic_)::value;
#define SEND });

DEV u32 pk(float a, float b) { bf16x2 v = {(__bf16)a, (__bf16)b}; return __builtin_bit_cast(u32, v); }
DEV float lo_f(u32 u) { return __builtin_bit_cast(float, u << 16); }
DEV float hi_f(u32 u) { return __builtin_bit_cast(float, u & 0xffff0000u); }
DEV f32x2 up2(u32 u) { return (f32x2){lo_f(u), hi_f(u)}; }
DEV u32 pk2(f32x2 v) { return pk(v.x, v.y); }
DEV float rbf(float a) { return (float)(__bf16)a; }
DEV v4u lds_r16(lptr p) { return *(LAS v4u*)p; }
DEV void lds_w16(lptr p, v4u v) { *(LAS v4u*)p = v; }
DEV v2u lds_tr(lptr p) { return __builtin_bit_cast(v2u, __builtin_amdgcn_ds_read_tr16_b64_v4i16((LAS s16x4*)p)); }
DEV v4u tr2(lptr p0, lptr p1) { const v2u a = lds_tr(p0), b = lds_tr(p1); return (v4u){a.x, a.y, b.x, b.y}; }
DEV v8u cat8(v4u a, v4u b) { return __builtin_shufflevector(a, b, 0, 1, 2, 3, 4, 5, 6, 7); }
DEV f32x4 mfma16(v4u a, v4u b, f32x4 c) { return __builtin_amdgcn_mfma_f32_16x16x32_bf16(__builtin_bit_cast(bf16x8, a), __builtin_bit_cast(bf16x8, b), c, 0, 0, 0); }
DEV f32x16 mfma32(v4u a, v4u b, f32x16 c) { return __builtin_amdgcn_mfma_f32_32x32x16_bf16(__builtin_bit_cast(bf16x8, a), __builtin_bit_cast(bf16x8, b), c, 0, 0, 0); }

DEV float f_(u32 u) { return __builtin_bit_cast(float, u); }
DEV u32 u_(float f) { return __builtin_bit_cast(u32, f); }
template <int CTRL> DEV float dpp(float v) { return f_((u32)__builtin_amdgcn_update_dpp(0, (int)u_(v), CTRL, 0xf, 0xf, false)); }
DEV float max16(float v) { auto r = __builtin_amdgcn_permlane16_swap(u_(v), u_(v), false, false); return fmaxf(f_(r[0]), f_(r[1])); }
DEV float max32(float v) { auto r = __builtin_amdgcn_permlane32_swap(u_(v), u_(v), false, false); return fmaxf(f_(r[0]), f_(r[1])); }
DEV float add16(float v) { auto r = __builtin_amdgcn_permlane16_swap(u_(v), u_(v), false, false); return f_(r[0]) + f_(r[1]); }
DEV float add32(float v) { auto r = __builtin_amdgcn_permlane32_swap(u_(v), u_(v), false, false); return f_(r[0]) + f_(r[1]); }
DEV float wave_sum(float v) {
    v += dpp<0xB1>(v);
    v += dpp<0x4E>(v);
    v += dpp<0x124>(v);
    v += dpp<0x128>(v);
    return add32(add16(v));
}
DEV bool all_(bool c) { return __builtin_amdgcn_ballot_w64(!c) == 0ull; }
DEV int uni(int v) { return __builtin_amdgcn_readfirstlane(v); }
DEV u32 uu(u32 v) { return (u32)__builtin_amdgcn_readfirstlane((int)v); }
DEV u32 rdl(u32 v, int l) { return (u32)__builtin_amdgcn_readlane((int)v, l); }
DEV u32 rt32() { return (u32)__builtin_amdgcn_s_memrealtime(); }

#define WSL 2
DEV void wait_flags(rsrc_t rs, u32 off, u32 want, bool& dead) {
    if (dead) return;
#pragma unroll 1
    for (int n = 0;; ++n) {
        CFENCE();
        const u32 v = ld4(rs, off);
        if (all_(off >= OOB || v >= want)) break;
        __builtin_amdgcn_s_sleep(WSL);
        if ((n & 63) == 63) { if (uni((int)ld4(rs, foff(F_ERR))) != 0) { dead = true; break; } }
        if (n > SPIN_CAP) { st4s(rs, foff(F_ERR), 0, 1u); dead = true; break; }
    }
    CFENCE();
}

DEV void pre_post(rsrc_t rs, int idx, u32 stage, v2u ep) { st8s(rs, threadIdx.x == 0 ? foff(F_PRE + idx) : OOB, 0, (v2u){ep.x + stage, ep.y}); }
DEV void pre_poll(rsrc_t rs, int first, int n, u32 stage, v2u ep) {
    const int lane = threadIdx.x & 63;
#pragma unroll 1
    for (int it = 0; it < SPIN_CAP; ++it) {
        CFENCE();
        const v2u a = ld8s(rs, foff(F_PRE + first + min(lane, n - 1)), 0), b = ld8s(rs, foff(F_PRE + first + min(lane + 64, n - 1)), 0);
        if (all_(a.y == ep.y && b.y == ep.y && max(a.x - ep.x - stage, b.x - ep.x - stage) < 8u - stage)) break;
        __builtin_amdgcn_s_sleep(2);
    }
    CFENCE();
}
DEV void pre_wait(rsrc_t rs, int first, int n, u32 stage, v2u ep) {
    if (uni(threadIdx.x >> 6) == 0) pre_poll(rs, first, n, stage, ep);
    BAR();
}
DEV u32 pol_wait(rsrc_t rs, v2u ep) {
    const int lane = threadIdx.x & 63;
    v4u a;
#pragma unroll 1
    for (int it = 0; it < SPIN_CAP; ++it) {
        CFENCE();
        a = ld16s(rs, foff(F_PRE + (lane < NPRE || lane >= 48 ? lane : 0)), 0);
        if (all_(a.y == ep.y && a.x - ep.x - 1u < 7u)) break;
        __builtin_amdgcn_s_sleep(2);
    }
    CFENCE();
    return uu(wave_sum(lane >= 48 ? (float)(int)a.z : 0.f) >= 0.f ? 1u : 0u);
}
#define PROBE_T 1000
#define NPROBE 32
DEV u32 pol_probe(const Params& p, rsrc_t rs, int id) {
    const int lane = threadIdx.x & 63;
    const u32 off0 = W_PROBE + (u32)(id >> 2) * 16384u + (u32)(id & 3) * 128u;
    const u32 tend = rt32() + PROBE_T;
    st4s(rs, lane < 32 ? off0 + (u32)lane * 512 : OOB, 0, 0u); DRAIN();
    int calm = 0;
    int d[2] = {1 << 16, 1 << 16};
#pragma unroll 1
    for (int q = 0; q < NPROBE && ((q & 1) || (calm < 6 && (int)(rt32() - tend) < 0)); ++q) {
        const u32 half = (u32)(q ^ (q >> 1) ^ id) & 1u;
        const unsigned long long t0 = __builtin_amdgcn_s_memtime();
        const u32 v = ld4(rs, lane == 0 ? off0 + half * 8192 + (u32)(q >> 1) * 512 : OOB);
        asm volatile("s_waitcnt vmcnt(0)" :: "v"(v) : "memory");
        const int dt = (int)(__builtin_amdgcn_s_memtime() - t0);
        if (half) d[1] = min(d[1], dt); else d[0] = min(d[0], dt);
        calm += dt < 1100;
    }
    const u32 xcc = (u32)__builtin_amdgcn_s_getreg(20 | (3 << 11)) & 0xfu;
    return (u32)min(max((d[0] - d[1]) * ((xcc & 4u) ? -1 : 1), -250), 250);
}
DEV f32x2 ln_stats(const f32x2 (&x)[5]) {
    const float c = f_(uu(u_(x[0].x)));
    f32x2 d[5];
#pragma unroll
    for (int i = 0; i < 5; ++i) d[i] = x[i] - c;
    const f32x2 s2 = (d[0] + d[1]) + (d[2] + d[3]) + d[4], q2 = (d[0] * d[0] + d[1] * d[1]) + (d[2] * d[2] + d[3] * d[3] + d[4] * d[4]);
    float a = s2.x + s2.y, b = q2.x + q2.y, a2, b2;
#define LN_DPP(m) "v_add_f32_dpp %0, %0, %0 " m " row_mask:0xf bank_mask:0xf\n\tv_add_f32_dpp %1, %1, %1 " m " row_mask:0xf bank_mask:0xf\n\ts_nop 0\n\t"
    asm("s_nop 1\n\t" LN_DPP("quad_perm:[1,0,3,2]") LN_DPP("quad_perm:[2,3,0,1]") LN_DPP("row_ror:4")
        "v_add_f32_dpp %0, %0, %0 row_ror:8 row_mask:0xf bank_mask:0xf\n\tv_add_f32_dpp %1, %1, %1 row_ror:8 row_mask:0xf bank_mask:0xf\n\ts_nop 1\n\t"
        "v_permlane16_swap_b32 %0, %1\n\tv_add_f32 %0, %0, %1\n\tv_mov_b32 %1, %0\n\ts_nop 1\n\tv_permlane32_swap_b32 %0, %1\n\tv_add_f32 %0, %0, %1\n\ts_nop 0\n\t"
        "v_readlane_b32 %2, %0, 0\n\tv_readlane_b32 %3, %0, 16\n\ts_nop 1"
        : "+v"(a), "+v"(b), "=s"(a2), "=s"(b2));
    const float m = a2 * (1.0f / DM), var = fmaxf(b2 * (1.0f / DM) - m * m, 0.f);
    const float sd = rbf(__builtin_amdgcn_sqrtf(rbf(rbf(var) + 1e-5f)));
    return (f32x2){rbf(c + m), __builtin_amdgcn_rcpf(sd)};
}
DEV u32 pkv(f32x2 v) { u32 d; asm("v_cvt_pk_bf16_f32 %0, %1, %2" : "=v"(d) : "v"(v.x), "v"(v.y)); return d; }
DEV void store_h(rsrc_t rs, u32 base, int row, const u32 (&hp)[5]) {
    const u32 lane = threadIdx.x & 63;
    const u32 sw = (u32)(row & 15) * 16u, o8 = (lane >> 5) * 8192u + (((lane & 31u) * 8u) ^ sw), hb = base + (u32)(row >> 5) * (32 * DM * 2) + (u32)(row & 31) * 256u;
    st8s(rs, o8, hb, (v2u){hp[0], hp[1]});
    st8s(rs, o8, hb + 16384, (v2u){hp[2], hp[3]});
    st4s(rs, (lane * 4) ^ sw, hb + 32768, hp[4]);
}

#define DMA16(ldsb, vo, so) asm volatile("s_mov_b32 m0, %0\n\ts_nop 0\n\tbuffer_load_dwordx4 %1, %2, %3 offen sc1 lds" :: "s"(ldsb), "v"(vo), "s"(srd), "s"(so) : "memory")
#define DMA16I(ldsb, lo, vo, so, go) do { u32 so_; asm volatile("s_add_u32 m0, %1, %5\n\ts_add_u32 %0, %4, %6\n\tbuffer_load_dwordx4 %2, %3, %0 offen sc1 lds" \
                         : "=&s"(so_) : "s"(ldsb), "v"(vo), "s"(srd), "s"(so), "i"(lo), "i"(go) : "memory", "scc"); } while (0)
#define DMA32(ldsb, vo, so) asm volatile("s_mov_b32 m0, %0\n\ts_nop 0\n\tbuffer_load_dwordx4 %1, %2, %3 offen sc1 lds\n\tbuffer_load_dwordx4 %1, %2, %3 offen offset:1024 sc1 lds" \
                         :: "s"(ldsb), "v"(vo), "s"(srd), "s"(so) : "memory")
#define DMAIN(sr, ldsb, vo, so) asm volatile("s_mov_b32 m0, %0\n\ts_nop 0\n\tbuffer_load_dwordx4 %1, %2, %3 offen lds" :: "s"(ldsb), "v"(vo), "s"(sr), "s"(so) : "memory")
DEV v4u mk_srd(const void* q, u32 bytes) { return (v4u){uu((u32)(size_t)q), uu((u32)((size_t)q >> 32) & 0xffffu), bytes, 0x00020000u}; }
#define DMANT(sr, ldsb, vo, so) asm volatile("s_mov_b32 m0, %0\n\ts_nop 0\n\tbuffer_load_dwordx4 %1, %2, %3 offen nt lds" :: "s"(ldsb), "v"(vo), "s"(sr), "s"(so) : "memory")
#define AHEAD(sr, vo, so) asm volatile("buffer_load_dwordx4 %0, %1, %2, %3 offen" : "+v"(junk) : "v"(vo), "s"(sr), "s"(so) : "memory")
#define MK_SRD() const v4u srd = mk_srd(p.ws, W_END)

constexpr int CV_W = 40960;
template <bool B> struct CvL {
    static constexpr int NF = B ? 5 : 3, NK = B ? 4 : 2, KP = 544 + NK * 256, CH = NF * 1024 + 4 * KP;
};
constexpr int CV_Y = 4 * CvL<false>::CH, CV_YP = 272;
static_assert(CV_Y + 32 * CV_YP <= CV_W && 2 * CvL<true>::CH <= CV_Y, "conv lds");
DEV int cv_chan(int u, int i) { return 16 * (u % 40) + 4 * i + u / 40; }
template <bool B> DEV void cv_filt(const Params& p, u32 dst, int d, int i) {
    const u32 lane = threadIdx.x & 63;
    const v4u sf = mk_srd(p.ssmk + d * SEQ, SEQ * 2);
    if (i == 0) DMANT(sf, dst, lane < 4u ? OOB : lane * 16u - 64u, 0);
    else DMANT(sf, uu(dst + i * 1024), i == CvL<B>::NF - 1 && lane >= 4u ? OOB : lane * 16u, i * 1024 - 64);
}
DEV v4u cv_kvld(rsrc_t rs, int d, int i) { return ld16s(rs, (threadIdx.x & 63) * 16u, uu(W_KVT + (u32)d * (SEQ * 2) + i * 1024)); }
template <bool B> DEV void cv_kvput(lptr F, const v4u* k) {
    const int lane = threadIdx.x & 63;
#pragma unroll
    for (int i = 0; i < CvL<B>::NK; ++i) lds_w16(F + CvL<B>::NF * 1024 + (lane & 3) * CvL<B>::KP + 512 + i * 256 + (lane >> 2) * 16, k[i]);
}
template <bool B> DEV void cv_zero(lptr F) {
    const int lane = threadIdx.x & 63;
#pragma unroll
    for (int i = 0; i < 4; ++i) lds_w16(F + (lane >> 5) * CvL<B>::CH + CvL<B>::NF * 1024 + i * CvL<B>::KP + (lane & 31) * 16, (v4u){0u, 0u, 0u, 0u});
}
#define M32V(d, a, b) asm volatile("v_mfma_f32_32x32x16_bf16 %0, %1, %2, %0" : "+v"(d) : "v"(a), "v"(b))
template <bool B, class G> DEV void cv_run(const Params& p, rsrc_t rs, lptr F, int d, u32 D, f32x4 sp, float (&y)[16], G&& gap) {
    constexpr int KP = CvL<B>::KP, FO = B ? 2048 : 0;
    const int lane = threadIdx.x & 63, n = lane & 31, hh = lane >> 5, c = lane & 3;
    lptr fa = F + 64 + (32 * hh + 16 * (c & 1) + 8 * ((lane >> 4) & 1) + 4 * (c >> 1) - (lane & 12)) * 2;
    lptr kl = F + CvL<B>::NF * 1024 + (32 + n - hh) * 16;
    v4u dg;
#pragma unroll
    for (int q = 0; q < 4; ++q) { const int k = (lane & 35) ? -1 : (n & 4) + (n >> 3); dg[q] = k == 2 * q ? D : k == 2 * q + 1 ? D << 16 : 0u; }
    v4u l[4], h[4]; v2u fc[4], fh[4];
    f32x16 acc[4];
#pragma unroll
    for (int j = 0; j < 4; ++j) { l[j] = lds_r16(kl + j * KP); if constexpr (B) h[j] = lds_r16(kl + j * KP + 512); }
#pragma unroll
    for (int j = 0; j < 2; ++j) { fc[j] = lds_tr(fa + FO - 32 * j); if constexpr (B) fh[j] = lds_tr(fa - 32 * j); }
    asm volatile("s_nop 1" : "+v"(dg));
#pragma unroll
    for (int j = 0; j < 4; ++j) asm volatile("v_mfma_f32_32x32x16_bf16 %0, %1, %2, 0" : "=&v"(acc[j]) : "v"(dg), "v"(B ? h[j] : l[j]));
    constexpr int U = B ? 2 : 4;
    kl -= 32 * U; fa -= 32;
    auto body = [&](bool first) __attribute__((always_inline)) {
#pragma unroll
        for (int z = 0; z < U; ++z) {
            const int c = 2 * (z & 1);
            const v4u f = __builtin_shufflevector(fc[c], fc[c + 1], 0, 1, 2, 3), g = __builtin_shufflevector(fh[c], fh[c + 1], 0, 1, 2, 3);
#pragma unroll
            for (int j = 0; j < 4; ++j) {
                M32V(acc[j], f, l[j]); l[j] = lds_r16(kl + j * KP + 32 * (U - z - 1));
                if (j < 2) fc[(c ^ 2) + j] = lds_tr(fa + FO + (z + 1) * 128 + 32 - 32 * j);
                if (first && j > 0) gap(5 * z + j - 1);
                SCHED();
            }
            if constexpr (B) {
#pragma unroll
                for (int j = 0; j < 4; ++j) {
                    M32V(acc[j], g, h[j]); h[j] = lds_r16(kl + j * KP + 512 + 32 * (U - z - 1));
                    if (j < 2) fh[(c ^ 2) + j] = lds_tr(fa + (z + 1) * 128 + 32 - 32 * j);
                    if (first && j > 1) gap(5 * z + j + 1);
                    SCHED();
                }
            }
        }
        kl -= 32 * U; fa += 128 * U;
    };
    if constexpr (B) body(true);
#pragma unroll 1
    for (int m = B ? 2 * U : 0; m < 32; m += 2 * U) body(false);
    asm volatile("s_nop 11" : "+v"(acc[0]), "+v"(acc[1]), "+v"(acc[2]), "+v"(acc[3]));
    const float o[3] = {acc[1][15] + acc[2][14] + acc[3][13], acc[2][15] + acc[3][14], acc[3][15]};
    if constexpr (!B) st16(rs, lane == 63 ? W_SP + (u32)d * 16u : OOB, (v4u){u_(o[0]), u_(o[1]), u_(o[2]), 0u});
#pragma unroll
    for (int i = 0; i < 16; ++i) {
        float v = acc[0][i];
#pragma unroll
        for (int j = 1; j < 4; ++j) if (j <= i) v += acc[j][i - j];
        if (i < 3) { const float e = f_((u32)__builtin_amdgcn_ds_bpermute((hh ? lane - 32 : lane + 31) * 4, (int)u_(o[i]))); v += lane == 0 ? sp[i] : e; }
        y[i] = v;
    }
}
DEV void cv_put(lptr F, int pr, const float (&y0)[16], const float (&y1)[16]) {
    const int lane = threadIdx.x & 63;
#pragma unroll
    for (int i = 0; i < 16; ++i) *(LAS u32*)(F + CV_Y + (lane & 31) * CV_YP + (lane >> 5) * 128 + i * 8 + pr * 4) = pk(y0[i], y1[i]);
}
DEV void cv_out(rsrc_t rs, lptr F, int u, int blk0) {
    const int lane = threadIdx.x & 63;
    lptr q = F + CV_Y + ((lane >> 2) & 3) * CV_YP + ((lane >> 4) * 4 + (lane & 3)) * 8;
    const u32 yo = W_Y + (u32)(((lane >> 2) * 8 + blk0) * DM + 4 * u + (lane & 3)) * 16u;
#pragma unroll
    for (int i = 0; i < 8; ++i) { lptr a = q + (i >> 1) * (8 * CV_YP) + (i & 1) * 128; st16(rs, yo + (u32)((i & 1) * 128 + (i >> 1)) * (DM * 16), tr2(a, a + 4 * CV_YP)); }
}
struct KvIn { v2u k[19], v[16]; v4u s[2]; };
DEV void kvt_load(const Params& p, int tile, KvIn& in) {
    const int lane = threadIdx.x & 63;
    const rsrc_t rk = mk_rsrc(p.bk, ACT), rv = mk_rsrc(p.bv, ACT), rh = mk_rsrc(p.shift, DM * 8);
    const int row = (tile / 10) * 64 + (lane >> 4) * 16, col = (tile % 10) * 64 + (lane & 15) * 4;
    const u32 vo = (u32)(row * DM + col) * 2u, vp = row > 0 ? vo - 3 * (DM * 2) : OOB;
#pragma unroll
    for (int i = 0; i < 2; ++i) in.s[i] = ldin16(rh, (u32)col * 8u + i * 16, 0);
#pragma unroll
    for (int i = 0; i < 19; ++i) { in.k[i] = i < 3 ? ldin8(rk, vp, i * (DM * 2)) : ldin8(rk, vo, (i - 3) * (DM * 2)); if (i >= 3) in.v[i - 3] = ldin8(rv, vo, (i - 3) * (DM * 2)); }
}
DEV void kvt_make(rsrc_t rs, lptr xt, int tile, const KvIn& in) {
    const int lane = threadIdx.x & 63;
#pragma unroll
    for (int c = 0; c < 2; ++c) {
        const v4u sv = in.s[c];
        const f32x2 sh[4] = {{lo_f(sv.x), lo_f(sv.z)}, {hi_f(sv.x), hi_f(sv.z)}, {lo_f(sv.y), lo_f(sv.w)}, {hi_f(sv.y), hi_f(sv.w)}};
        f32x2 kf[19];
#pragma unroll
        for (int i = 0; i < 19; ++i) kf[i] = up2(in.k[i][c]);
        u32 kv[16];
#pragma unroll
        for (int i = 0; i < 16; ++i) kv[i] = pk2(((kf[i] * sh[3] + kf[i + 1] * sh[2]) + (kf[i + 2] * sh[1] + kf[i + 3] * sh[0])) * up2(in.v[i][c]));
#pragma unroll
        for (int q = 0; q < 8; ++q) {
            const u32 sel = (q & 1) ? 0x07060302u : 0x05040100u; const u32* z = kv + (q >> 1);
            *(LAS v2u*)(xt + ((2 * c + (q & 1)) * 16 + (lane & 15)) * 144 + (lane >> 5) * 64 + (q >> 1) * 16 + ((lane >> 4) & 1) * 8) =
                (v2u){__builtin_amdgcn_perm(z[4], z[0], sel), __builtin_amdgcn_perm(z[12], z[8], sel)};
        }
    }
    const u32 o = W_KVT + (u32)(((tile % 10) * 64 + 4 * (lane >> 3)) * SEQ + (tile / 10) * 64 + (lane & 7) * 8) * 2u;
#pragma unroll
    for (int i = 0; i < 8; ++i) st16(rs, o + (u32)(32 * (i & 1) + (i >> 1)) * (SEQ * 2), lds_r16(xt + (8 * i + (lane >> 3)) * 144 + (lane & 7) * 16));
}
struct RowX { v2u a, b; u32 c; };
DEV void ln_row(rsrc_t rs, int row, const RowX& r) {
    const f32x2 x[5] = {up2(r.a.x), up2(r.a.y), up2(r.b.x), up2(r.b.y), up2(r.c)};
    const f32x2 st = ln_stats(x);
    u32 hp[5];
#pragma unroll
    for (int q = 0; q < 5; ++q) hp[q] = pk2(up2(pkv(x[q] - st.x)) * st.y);
    store_h(rs, W_H1, row, hp);
}
DEV void pre_rings(const Params& p, rsrc_t rs, int j) {
    const u32 gi = (u32)(j * 256 + threadIdx.x), gt = gi * 16u, R = NPRE * 4096, n = ((gt >> 14) << 9) + ((gt & 8191u) >> 4);
    v4u m = {PSENT, PSENT, PSENT, PSENT};
    if (n < 640u) {
        m = *(const v4u*)(p.mem0 + (2 * ((n >> 2) & 3) + (n & 1)) * DM + (n >> 4) * 16 + (n & 2) * 4);
#pragma unroll
        for (int i = 0; i < 4; ++i) m[i] = m[i] == PSENT ? 0x7FC17FC1u : m[i];
    }
    st4s(rs, gi - 36u * 256u < F_CLR ? foff(gi - 36u * 256u) : OOB, 0, 0u);
#pragma unroll 1
    for (u32 o = gt; o < MB; o += R) st16(rs, W_ORING + o, (v4u){PSENT, PSENT, PSENT, PSENT});
    const u32 mo = gt < 4 * 32768 + 16384 ? W_MRING + gt : OOB;
    st16(rs, mo, m);
    st16(rs, mo + MB / 4, m);
    static_assert(NPRE * 4096 >= 4 * 32768 + 16384, "memory ring");
}
constexpr int LN0 = NPRE * 4;
struct Early { KvIn in; RowX r; u32 vote; };
DEV void conv_early(const Params& p, rsrc_t rs, int j, Early& e) {
    const int lane = threadIdx.x & 63, w = uni(threadIdx.x >> 6), u = j * 4 + w;
    if (j < NPRE) {
        const rsrc_t rx = mk_rsrc(p.x, ACT);
        e.r.a = __builtin_amdgcn_raw_buffer_load_b64(rx, lane * 8u, uu(u * (DM * 2)), 0); e.r.b = __builtin_amdgcn_raw_buffer_load_b64(rx, lane * 8u + 512u, uu(u * (DM * 2)), 0);
        e.r.c = __builtin_amdgcn_raw_buffer_load_b32(rx, lane * 4u + 1024u, uu(u * (DM * 2)), 0);
        kvt_load(p, u, e.in);
    } else if (j < NCV && w > 0) e.vote = pol_probe(p, rs, u - NPRE * 4);
}
DEV void conv_stage(const Params& p, rsrc_t rs, lptr lds, int j, int g0, int ge, v2u ep, Early& e) {
    const int tid = threadIdx.x, lane = tid & 63, w = uni(tid >> 6), u = j * 4 + w;
    lptr F = lds + w * CV_W;
    const u32 fb = uu(w * CV_W);
    const rsrc_t rd = mk_rsrc(p.ssmD, DM * 2);
    float y0[16], y1[16];
    if (j < NPRE) {
        constexpr int CH = CvL<false>::CH;
        KvIn& in = e.in;
        if (w == 1) LST(1500 + j, rt32());
        pre_rings(p, rs, j);
        ln_row(rs, u, e.r);
        if (w == 1) LST(1550 + j, rt32());
        DRAIN(); BAR();
        pre_post(rs, j, 1u, ep);
        if (j == 20) LST(260, rt32());
        if (w == 0) LST(1450 + j, rt32());
        const u32 D = __builtin_amdgcn_raw_buffer_load_b16(rd, (u32)cv_chan(u, lane & 3) * 2u, 0, 0);
#pragma unroll 1
        for (int i = 0; i < 4; ++i) { const int d = cv_chan(u, i); cv_filt<false>(p, fb + i * CH, d, 0); cv_filt<false>(p, fb + i * CH, d, 1); cv_filt<false>(p, fb + i * CH, d, 2); }
        kvt_make(rs, F + CV_Y, u, in);
        cv_zero<false>(F); cv_zero<false>(F + 2 * CH);
        DRAIN(); BAR();
        pre_post(rs, j, 2u, ep);
        if (j == 20) LST(262, rt32());
        pre_wait(rs, 0, NPRE, 2u, ep);
        if (j == 20) LST(281, rt32());
        {
            v4u k[8];
#pragma unroll
            for (int i = 0; i < 8; ++i) k[i] = cv_kvld(rs, cv_chan(u, i >> 1), i & 1);
#pragma unroll
            for (int i = 0; i < 4; ++i) cv_kvput<false>(F + i * CH, k + 2 * i);
        }
        if (j == 20) LST(286, rt32());
#pragma unroll 1
        for (int i = 0; i < 4; i += 2) {
            cv_run<false>(p, rs, F + i * CH, cv_chan(u, i), rdl(D, i), f32x4{}, y0, [](int) {});
            cv_run<false>(p, rs, F + (i + 1) * CH, cv_chan(u, i + 1), rdl(D, i + 1), f32x4{}, y1, [](int) {});
            cv_put(F, i >> 1, y0, y1);
        }
        if (j == 20) LST(287, rt32());
        cv_out(rs, F, u, 0);
        if (j == 20) LST(263, rt32());
    } else {
        st16(rs, lane == 0 ? foff(F_PRE + 48 + u - NPRE * 4) : OOB, (v4u){ep.x + 1u, ep.y, w > 0 ? e.vote : 0u, 0u});
#pragma unroll 1
        for (int t = u + 128; t < 320; t += 16) { kvt_load(p, t, e.in); kvt_make(rs, F, t, e.in); }
    }
    DRAIN(); BAR();
    if (g0 < ge) {
        constexpr int CH = CvL<true>::CH;
        pre_post(rs, j, 3u, ep);
        cv_zero<true>(F);
        if (w == 0) { pre_poll(rs, 64, 32, 3u, ep); pre_poll(rs, 0, NCV, 3u, ep); }
        BAR();
        v4u k[4];
        auto fetch = [&](u32 dst, int d, int i) { if (i < 5) cv_filt<true>(p, dst, d, i); else if (i < 9) k[i - 5] = cv_kvld(rs, d, i - 5); };
#pragma unroll
        for (int i = 0; i < 9; ++i) fetch(fb, cv_chan(g0 * 4 + w, 0), i);
        const u32 ch = (u32)cv_chan(min((g0 + 8 * (lane >> 2)) * 4 + w, 159), lane & 3);
        u32 D = __builtin_amdgcn_raw_buffer_load_b16(rd, ch * 2u, 0, 0);
        v4u sp = ld16s(rs, W_SP + ch * 16u, 0);
        asm volatile("" : "+v"(D), "+v"(sp));
#pragma unroll 1
        for (int g = g0, n = 0; g < ge; g += 8) {
            const int un = g * 4 + w;
            if (g == 16) { LST(282, rt32()); LST(288, __builtin_amdgcn_s_memtime()); }
#pragma unroll 1
            for (int i = 0; i < 4; i += 2, n += 2) {
                const int d1 = (int)rdl(ch, n + 1), d2 = (int)rdl(ch, n + 2);
                cv_kvput<true>(F, k);
                cv_run<true>(p, rs, F, 0, rdl(D, n), (f32x4){f_(rdl(sp.x, n)), f_(rdl(sp.y, n)), f_(rdl(sp.z, n)), 0.f}, y0, [&](int q) { fetch(fb + CH, d1, q); });
                cv_kvput<true>(F + CH, k);
                cv_run<true>(p, rs, F + CH, 326, rdl(D, n + 1), (f32x4){f_(rdl(sp.x, n + 1)), f_(rdl(sp.y, n + 1)), f_(rdl(sp.z, n + 1)), 0.f}, y1, [&](int q) { fetch(fb, d2, q); });
                cv_put(F, i >> 1, y0, y1);
            }
            cv_out(rs, F, un, 4);
            if (g == 16) { LST(283, rt32()); LST(289, __builtin_amdgcn_s_memtime()); }
        }
        DRAIN(); BAR();
    }
    pre_post(rs, j, 5u, ep);
    if (j == 20) LST(284, rt32());
    if (j == 3) LST(285, rt32());
}

#define M32A(first, d, a, b) do { if (first) asm volatile("v_mfma_f32_32x32x16_bf16 %0, %1, %2, 0" : "=&v"(d) : "a"(a), "v"(b)); \
                                  else asm volatile("v_mfma_f32_32x32x16_bf16 %0, %1, %2, %0" : "+v"(d) : "a"(a), "v"(b)); } while (0)
#define M32B(first, d, a, b) do { if (first) asm volatile("v_mfma_f32_32x32x16_bf16 %0, %1, %2, 0" : "=&v"(d) : "v"(a), "a"(b)); \
                                  else asm volatile("v_mfma_f32_32x32x16_bf16 %0, %1, %2, %0" : "+v"(d) : "v"(a), "a"(b)); } while (0)
#define VCVT(d, a, b) asm volatile("v_cvt_pk_bf16_f32 %0, %1, %2" : "=v"(d) : "v"(a), "v"(b))
#define VADD(d, a, b) asm volatile("v_add_f32 %0, %1, %2" : "=v"(d) : "v"(a), "v"(b))
#define LD4A(d, vo, so) asm volatile("buffer_load_dword %0, %1, %2, %3 offen sc1" : "=v"(d) : "v"(vo), "s"(srd), "s"(so) : "memory")
DEV u32 wst_src(int lane, int ld) { return (u32)((lane >> 4) * ld + 8 * ((lane & 15) ^ ((lane >> 4) << 2))) * 2u; }
DEV u32 wst_rd(int lane, int w) { const int q4 = (lane >> 2) & 3; return (u32)(q4 * 256 + ((w ^ q4) * 64) + ((lane >> 4) & 1) * 32 + (lane & 3) * 8); }

constexpr int QK_SEL = 81920;
#define QK_LEAD 12
#define QK_SMP 56
#define QK_W0 8
DEV void role_qkv(const Params& p, lptr lds, int c, v2u ep) {
    const int tid = threadIdx.x, lane = tid & 63, w = uni(tid >> 6), hh = lane >> 5, r32 = lane & 31;
    const int m = c >> 5, h = c & 31;
    const rsrc_t rs = mk_rsrc(p.ws, W_END);
    v4u wt[40];
    {
        const v4u sw = mk_srd(m == 0 ? p.Wq : m == 1 ? p.Wk : p.Wv, DM * NQ * 2), sx = mk_srd(p.x, ACT);
        const u32 vo = wst_src(lane, NQ) + (u32)h * (DH * 2), x8 = (u32)lane * 8u, x4 = (u32)lane * 4u;
        RowX xr[5];
        auto wfill = [&](int i0, int n) {
#pragma unroll 1
            for (int i = i0; i < i0 + n; ++i) DMAIN(sw, uu((4 * i + w) * 1024), vo, uu((16 * i + 4 * w) * (NQ * 2)));
        };
        wfill(0, QK_W0);
#pragma unroll
        for (int i = 0; i < 5; ++i)
            asm volatile("buffer_load_dwordx2 %0, %3, %5, %6 offen\n\tbuffer_load_dwordx2 %1, %3, %5, %6 offen offset:512\n\tbuffer_load_dword %2, %4, %5, %6 offen offset:1024"
                         : "=&v"(xr[i].a), "=&v"(xr[i].b), "=&v"(xr[i].c) : "v"(x8), "v"(x4), "s"(sx), "s"(uu((LN0 + 4 * c + w + 384 * i) * (DM * 2))) : "memory");
        wfill(QK_W0, 40 - QK_W0);
        if (c == 3) LST(275, rt32());
        VMW(40 - QK_W0);
        if (c == 3) LST(276, rt32());
#pragma unroll
        for (int i = 0; i < 5; ++i) {
            asm volatile("" : "+v"(xr[i].a), "+v"(xr[i].b), "+v"(xr[i].c));
            const int row = LN0 + 4 * c + w + 384 * i;
            if (i < 4 || row < SEQ) ln_row(rs, row, xr[i]);
        }
        if (c == 3) LST(264, rt32());
        VMW(12); BAR();
        lptr tb = lds + hh * 2048 + wst_rd(lane, w);
        SFOR(k, 40)
            lptr q = tb + (k >> 3) * 32768 + (k & 7) * 4096; wt[k] = tr2(q, q + 1024); asm volatile("" : "+a"(wt[k]));
        SEND
        DRAIN(); BAR();
        pre_post(rs, 96 + c, 1u, ep);
    }
    if (c == 3) LST(268, rt32());
    if (w == 0) LST(1350 + c, rt32());
    bool dead = false;
    int n0, n1;
    const u32 t16 = (u32)tid * 16u, wb = uu(w * 1024);
    u32 ar[16];
#pragma unroll
    for (int j = 0; j < 16; ++j) ar[j] = (u32)(r32 * 256 + ((2 * (j & 7) + hh) ^ (r32 & 15)) * 16 + (j >> 3) * 49152);
    MK_SRD();
#define XRD(y, ks) lds_r16(lds + ar[((ks) & 7) + ((ks) >= 24 ? 8 : 0)] + ((ks) >> 3) % 3 * 16384 + (y) * 8192)
#define QDMA(y, i, vo, so) DMA16I(wb, ((i) >> 1) * 16384 + ((i) & 1) * 4096 + (y) * 8192, vo, so, (i) * 4096)
    const u32 h2flag = (lane < 32 && w == 0) ? foff(F_H2 + lane) : OOB;
    const u32 ol = (m == 2 ? (u32)(2 * w + (r32 >> 4)) * 1024u : (u32)((r32 >> 4) * 4 + w) * 1024u) + (u32)(hh * 16 + (r32 & 15)) * 16u
                 + (m == 2 ? W_VO : m == 1 ? W_KQ : W_KQ + 8192u) + (u32)h * 16384u;
    const u32 fo = tid < 2 ? foff(F_QKV + tid * 96 + c) : OOB;
    lptr selw = lds + QK_SEL + (w == 0 ? 0 : 64), selr = lds + QK_SEL;
    auto late = [&](u32 fv) { const u32 need = n1 >= NSEG ? 0xFFFFFFFFu : dead ? 0u : (u32)n1 + 1u; return (u32)__builtin_amdgcn_ballot_w64(fv < need); };
    static_assert(W_H2 - W_H1 == 2 * 64 * (32 * DM * 2), "h");
    pre_wait(rs, 0, NPRE, 1u, ep);
    if (c == 3) LST(269, rt32());
    auto run = [&](auto isv) __attribute__((always_inline)) {
        constexpr bool V = decltype(isv)::value;
        f32x16 ax, ay = {};
        v4u x[8], pp, pq;
        u32 oy = OOB, lt = 1, fcnt = 0, fa = OOB;
        u32 nn = 0;
        PIN(lt); PIN(nn);
#pragma unroll
        for (int z = 0; z < 5; ++z) x[z] = XRD(1, 20 + z);
#define QPK(r_, a1, a_, q_) do { if constexpr (V) VCVT(r_[q_], a1[4 * (a_) + 8 * ((q_) >> 1) + 2 * ((q_) & 1)], a1[4 * (a_) + 8 * ((q_) >> 1) + 2 * ((q_) & 1) + 1]); \
                                 else VCVT(r_[q_], a1[4 * (a_) + (q_)], a1[4 * (a_) + (q_) + 8]); } while (0)
#define QPK4(r_, a1, a_) do { QPK(r_, a1, a_, 0); QPK(r_, a1, a_, 1); QPK(r_, a1, a_, 2); QPK(r_, a1, a_, 3); } while (0)
        for (;;) {
            u32 ls = uu(lt);
            n0 = uni((int)nn); n1 = n0 >> 8; n0 &= 255;
            if (ls != 0u && n0 >= min(NSEG, n1 + QK_LEAD)) {
                CFENCE();
                u32 fv = ld4(rs, h2flag);
                asm volatile("s_waitcnt vmcnt(0)" : "+v"(fv) :: "memory");
                *(LAS u32*)(selw + 16) = late(fv);
                BAR();
                st4s(rs, fa, 0, fcnt);
                fa = OOB;
                if (n1 >= NSEG) __builtin_amdgcn_endpgm();
                if (uu(*(LAS u32*)(selr + 16)) != 0u) {
                    if (w == 0) wait_flags(rs, h2flag, (u32)n1 + 1, dead);
                    BAR();
                }
                ls = 0u;
            }
            if (n0 == 4) pre_wait(rs, 96, 96, 1u, ep);
            int cur = n0;
            asm volatile("s_cmp_eq_u32 %3, 0\n\ts_cselect_b32 %0, %4, %0\n\ts_addc_u32 %2, %2, 0\n\ts_cmp_lg_u32 %3, 0\n\ts_addc_u32 %1, %1, 0"
                         : "+s"(cur), "+s"(n0), "+s"(n1) : "s"(ls), "s"(n1 | 64) : "scc");
            u32 xs = W_H1 + (u32)(cur + (cur & 64)) * (32 * DM * 2), ys, yvo, ox, fv;
            PINS(xs);
#define QT(k) do {} while (0)
            QT(0);
            SCHED();
            QDMA(0, 0, t16, xs); QDMA(0, 1, t16, xs);
            SFOR(z, 80)
                constexpr int ks = (z + 20) % 40, zn = (z + 5) % 80;
                if constexpr (z >= 20 && z < 60) { if constexpr (V) M32B(ks == 0, ax, x[z & 7], wt[ks]); else M32A(ks == 0, ax, wt[ks], x[z & 7]); }
                else { if constexpr (V) M32B(ks == 0, ay, x[z & 7], wt[ks]); else M32A(ks == 0, ay, wt[ks], x[z & 7]); }
                x[(z + 5) & 7] = XRD(zn < 20 || zn >= 60, (zn + 20) % 40);
                if constexpr (z == 77) lt = *(LAS u32*)selr;
                SCHED();
                if constexpr (z < 8) QDMA(0, z + 2, t16, xs);
                if constexpr (z == 9) { ys = W_H1 + (u32)n0 * (32 * DM * 2); yvo = t16 | (n0 < NSEG ? 0u : OOB); PINS(ys); PIN(yvo); }
                if constexpr (z == 11) { ox = ol + (u32)cur * (NH * 16384u); PIN(ox); }
                if constexpr (z == 14) { QT(1); asm volatile("s_waitcnt vmcnt(6) lgkmcnt(0)\n\ts_barrier" ::: "memory"); QT(2); }
                if constexpr (z == 15) st4s(rs, fa, 0, fcnt);
                if constexpr (z >= 16 && z < 26) QDMA(1, z - 16, yvo, ys);
                if constexpr (z == 26) QPK4(pp, ay, 0);
                if constexpr (z == 27) st16(rs, oy, pp);
                if constexpr (z == 28) QPK4(pq, ay, 1);
                if constexpr (z == 29) st16(rs, oy + 512, pq);
                if constexpr (z == 30) { QT(3); asm volatile("s_waitcnt vmcnt(13)\n\ts_barrier" ::: "memory"); QT(4); }
                if constexpr (z == 32) { fcnt = lane == 0 ? (u32)n0 : (u32)n1; fa = fo; asm volatile("" : "+v"(fcnt), "+v"(fa)); }
                if constexpr (z == 34) { oy = (ol + (u32)n0 * (NH * 16384u)) | (yvo & OOB); PIN(oy); }
                if constexpr (z == 35) { nn = (u32)(min(n0 + 1, NSEG) | n1 << 8); PIN(nn); }
                if constexpr (z == 54) { asm volatile("s_waitcnt vmcnt(2) lgkmcnt(0)\n\ts_barrier" ::: "memory"); QT(5); }
                if constexpr (z == QK_SMP) LD4A(fv, h2flag, 0);
                if constexpr (z == 62) QPK4(pp, ax, 0);
                if constexpr (z == 63) st16(rs, ox, pp);
                if constexpr (z == 64) QPK4(pq, ax, 1);
                if constexpr (z == 65) st16(rs, ox + 512, pq);
                if constexpr (z == 73) { asm volatile("s_waitcnt vmcnt(2)" : "+v"(fv) :: "memory"); QT(6); ls = late(fv); PINS(ls); }
                if constexpr (z == 74) { lt = ls; PIN(lt); *(LAS u32*)selw = lt; }
                if constexpr (z == 76) asm volatile("s_waitcnt lgkmcnt(2)\n\ts_barrier" ::: "memory");
                SCHED();
            SEND
        }
    };
    if (m == 2) run(IC<1>{}); else run(IC<0>{});
}

#define OU_SMP 44
DEV void role_out(const Params& p, lptr lds, int L, int kq, int ng, v2u ep) {
    const int tid = threadIdx.x, lane = tid & 63, w = uni(tid >> 6), hh = lane >> 5, r32 = lane & 31;
    const rsrc_t rs = mk_rsrc(p.ws, W_END);
    v4u wo[64];
    bool dead = false;
    {
        const v4u sw = mk_srd(p.Wo, DM * NQ * 2);
        const u32 vo = wst_src(lane, DM) + (u32)ng * 256u;
        auto fill = [&](int b) {
#pragma unroll 1
            for (int i = 0; i < 8; ++i) DMAIN(sw, uu((b % 5) * 32768 + (4 * i + w) * 1024), vo, uu((kq * 1024 + b * 128 + 16 * i + 4 * w) * (DM * 2)));
        };
#pragma unroll 1
        for (int b = 0; b < 5; ++b) fill(b);
        lptr tb = lds + hh * 1024 + wst_rd(lane, w);
        SFOR(b, 8)
            VMW(b < 4 ? 32 : 56 - 8 * b); BAR();
            lptr q = tb + (b % 5) * 32768;
#pragma unroll
            for (int k = 0; k < 8; ++k) { lptr a = q + (k >> 1) * 8192 + (k & 1) * 2048; wo[8 * b + k] = tr2(a, a + 4096); if (b < 7) asm volatile("" : "+a"(wo[8 * b + k])); else PIN(wo[8 * b + k]); }
            if constexpr (b < 3) { BAR(); fill(b + 5); }
        SEND
    }
    pre_wait(rs, 0, NPRE, 1u, ep);
    if (kq == 0 && ng == 0) LST(272 + L, rt32());
    MK_SRD();
    const u32 ofl = lane < 8 ? foff(F_O + L * 32 + kq * 8 + lane) : OOB, l16 = (u32)lane * 16u;
    const u32 fo = (tid == 0) ? foff(F_P + L * 32 + kq * 5 + ng) : OOB;
    const u32 src0 = uu(W_VO + 8192u + (u32)(L * NSEG * NH + kq * 8) * 16384u + w * 1024);
#define ODMA(i, dst, so) DMA16I(dst, (i) * 4096, l16, so, ((i) >> 1) * 16384 + ((i) & 1) * 4096)
    auto first = [&](int s) { const u32 dst = uu((s & 1) * 65536 + w * 1024), src = src0 + (u32)s * (NH * 16384u); SFOR(i, 10) ODMA(i, dst, src); SEND };
    u32 pa = W_P + (u32)(L * NSEG) * P_ITEM + (u32)(4 * hh * 4 + kq) * (DM * 4) + (u32)(ng * 128 + w * 32 + r32) * 4u, fv = 0;
    bool early = false;
    for (int s = 0; s < NSEG; ++s) {
        const u32 dst = uu((s & 1) * 65536 + w * 1024), src = src0 + (u32)s * (NH * 16384u);
        lptr A = lds + (s & 1) * 65536 + l16;
        if (!early) {
            if (!all_(ofl == OOB || fv >= (u32)s + 1)) wait_flags(rs, ofl, (u32)s + 1, dead);
            LS3(L == 0 && w == 1, 1, kq * 5 + ng);
            first(s);
        }
        asm volatile("s_waitcnt vmcnt(8)\n\ts_barrier" ::: "memory");
        v4u o[64];
#pragma unroll
        for (int k = 0; k < 3; ++k) o[k] = lds_r16(A + k * 1024);
        f32x16 acc;
        SCHED();
        SFOR(k, 64)
            if constexpr (k < 56) M32B(k == 0, acc, o[k], wo[k]); else M32V(acc, o[k], wo[k]);
            if constexpr (k < 61) o[k + 3] = lds_r16(A + (k + 3) * 1024);
            SCHED();
            if constexpr (k < 12 && (k & 1) == 0) ODMA(10 + k / 2, dst, src);
            if constexpr (k == 4) asm volatile("s_waitcnt vmcnt(7)\n\ts_barrier" ::: "memory");
            if constexpr (k == 20) asm volatile("s_waitcnt vmcnt(5)\n\ts_barrier" ::: "memory");
            if constexpr (k == 40) asm volatile("s_waitcnt vmcnt(0)\n\ts_barrier" ::: "memory");
            if constexpr (k == OU_SMP) LD4A(fv, ofl, 0);
            SCHED();
        SEND
        asm volatile("s_nop 9" : "+v"(acc), "+v"(pa));
#pragma unroll
        for (int i = 0; i < 16; ++i) st4s(rs, pa, (u32)((8 * (i >> 2) + (i & 3)) * (4 * DM * 4)), u_(acc[i]));
        asm volatile("s_waitcnt vmcnt(16)" : "+v"(fv) :: "memory");
        LS3(L == 0 && w == 1, 2, kq * 5 + ng);
        early = s + 1 < NSEG && all_(ofl == OOB || fv >= (u32)s + 2);
        if (early) { first(s + 1); VMW(10); }
        else { fv = ld4(rs, ofl); asm volatile("s_waitcnt vmcnt(0)" : "+v"(fv) :: "memory"); }
        BAR();
        st4s(rs, fo, 0, (u32)s + 1);
        pa += P_ITEM;
        LS2(L == 0 && w == 1, 160 + kq * 5 + ng);
    }
}

constexpr int RW_P = 20480, RW_X = RW_P + 10240;
DEV void role_row(const Params& p, lptr lds, int id, v2u ep) {
    const int lane = threadIdx.x & 63, w = uni(threadIdx.x >> 6);
    const int ri = id >> 1, L = id & 1;
    const rsrc_t rs = mk_rsrc(p.ws, W_END);
    bool dead = false, have = false;
    const u32 fp = lane < 20 ? foff(F_P + L * 32 + lane) : OOB, l16 = (u32)lane * 16u, lx = lane < 16 ? l16 + 1024u : OOB, fb = uu(w * CV_W);
    lptr F = lds + w * CV_W;
    MK_SRD();
    const v4u sx = mk_srd(p.x, ACT);
    auto yfill = [&](int b) {
#pragma unroll 1
        for (int i = 0; i < 5; ++i) DMA32(uu(fb + (b & 1) * 10240 + i * 2048), l16, uu(W_Y + (u32)(ri * 8 + b) * 10240u + i * 2048));
    };
    auto xfill = [&](int s) {
        const u32 d = uu(fb + RW_X + (s & 1) * 2048), o = uu((u32)(s * 32 + ri) * (DM * 2));
        if (L == 0) { DMAIN(sx, d, l16, o); DMAIN(sx, d + 1024, lx, o); } else { DMA16(d, l16, o + W_X2); DMA16(d + 1024, lx, o + W_X2); }
    };
    auto pfill = [&](int s) {
        const u32 o = uu(W_P + (u32)(L * NSEG + s) * P_ITEM + (u32)ri * (4 * DM * 4));
        SFOR(i, 10) DMA16I(fb, RW_P + i * 1024, l16, o, i * 1024); SEND
        if (L == 1) xfill(s);
    };
    if (L == 0) xfill(0);
    pre_poll(rs, 0, NPRE, 3u, ep);
    if (id == 0) LST(274, rt32());
    yfill(0); DRAIN();
    u32 fv = 0;
    for (int s = 0; s < NSEG; ++s) {
        if (!have) {
            if (!all_(fp == OOB || fv >= (u32)s + 1)) wait_flags(rs, fp, (u32)s + 1, dead);
            LS3(L == 0, 3, ri);
            pfill(s);
        }
        LS2(L == 0, 192 + ri);
        f32x2 y[5];
        {
            lptr ya = F + ((s >> 3) & 1) * 10240 + (s & 7) * 2 + lane * 16;
            auto yr = [&](lptr q) { return lo_f(*(LAS u16*)q); };
            y[0] = (f32x2){yr(ya), yr(ya + 2560)}; y[1] = (f32x2){yr(ya + 5120), yr(ya + 7680)};
            y[2] = (f32x2){yr(ya + 1024), yr(ya + 3584)}; y[3] = (f32x2){yr(ya + 6144), yr(ya + 8704)};
            lptr yc = ya - lane * 16 + 2048 + (lane >> 1) * 16 + (lane & 1) * 5120;
            y[4] = (f32x2){yr(yc), yr(yc + 2560)};
            asm volatile("" : "+v"(y[0]), "+v"(y[1]), "+v"(y[2]), "+v"(y[3]), "+v"(y[4]));
        }
        if (!have) DRAIN();
        LS3(L == 0, 4, ri);
        f32x2 x1[5];
        {
            f32x4 a[4], b[4]; f32x2 c[4];
            lptr q = F + RW_X + (s & 1) * 2048;
            v2u xa = *(LAS v2u*)(q + lane * 8), xb = *(LAS v2u*)(q + 512 + lane * 8); u32 xc = *(LAS u32*)(q + 1024 + lane * 4);
#pragma unroll
            for (int kq = 0; kq < 4; ++kq) { lptr r = F + RW_P + kq * (DM * 4); a[kq] = *(LAS f32x4*)(r + l16); b[kq] = *(LAS f32x4*)(r + 1024 + l16); c[kq] = *(LAS f32x2*)(r + 2048 + lane * 8); }
            SCHED();
            const f32x4 sa = (a[0] + a[1]) + (a[2] + a[3]), sb = (b[0] + b[1]) + (b[2] + b[3]);
            const f32x2 o[5] = {sa.lo, sa.hi, sb.lo, sb.hi, (c[0] + c[1]) + (c[2] + c[3])};
            const u32 xi[5] = {xa.x, xa.y, xb.x, xb.y, xc};
#pragma unroll
            for (int i = 0; i < 5; ++i) x1[i] = up2(xi[i]) + o[i];
            asm volatile("" : "+v"(x1[0]), "+v"(x1[1]), "+v"(x1[2]), "+v"(x1[3]), "+v"(x1[4]));
        }
        have = s + 1 < NSEG && all_(fp == OOB || fv >= (u32)s + 2);
        if (__builtin_expect(have, 0)) pfill(s + 1);
        if (L == 0) xfill(s + 1);
        const f32x2 st1 = ln_stats(x1);
        u32 xp[5];
#pragma unroll
        for (int i = 0; i < 5; ++i) xp[i] = pkv(x1[i] + (x1[i] - st1.x) * (y[i] * st1.y));
        const int row = s * 32 + ri;
        if (L == 0) {
            const u32 ro = (u32)row * (DM * 2);
            f32x2 x2[5];
#pragma unroll
            for (int i = 0; i < 5; ++i) x2[i] = up2(xp[i]);
            st8s(rs, (u32)lane * 8, W_X2 + ro, (v2u){xp[0], xp[1]});
            st8s(rs, (u32)lane * 8 + 512, W_X2 + ro, (v2u){xp[2], xp[3]});
            st4s(rs, (u32)lane * 4 + 1024, W_X2 + ro, xp[4]);
            const f32x2 st2 = ln_stats(x2);
            u32 hp[5];
#pragma unroll
            for (int i = 0; i < 5; ++i) hp[i] = pk2((x2[i] - st2.x) * st2.y);
            store_h(rs, W_H2, row, hp);
        } else {
            u16* o_ = p.out + (size_t)row * DM;
            *(v2u*)(o_ + 4 * lane) = (v2u){xp[0], xp[1]};
            *(v2u*)(o_ + 256 + 4 * lane) = (v2u){xp[2], xp[3]};
            *(u32*)(o_ + 512 + 2 * lane) = xp[4];
        }
        CFENCE();
        LS3(L == 0, 5, ri);
        fv = ld4(rs, fp);
        asm volatile("s_waitcnt vmcnt(0)" : "+v"(fv) :: "memory");
        st4s(rs, lane == 0 && L == 0 ? foff(F_H2 + ri) : OOB, 0, (u32)s + 1);
        if ((s & 7) == 6 && s < NSEG - 8) {
            if (s == 30) pre_poll(rs, 0, NCV, 5u, ep);
            if (s == 46) st8s(rs, (id == 1 && lane == 0) ? foff(F_EPOCH) : OOB, 0, (v2u){ep.x + 8u, ep.y});
            yfill((s >> 3) + 1);
        }
        if (ri == 0) LST(320 + L * 64 + s, rt32());
        LS2(L == 0, 224 + ri);
    }
}

constexpr float SCALE = 0.08838834764831845f, LOG2E = 1.4426950408889634f;
constexpr int C_K = 0, C_V = 8192, C_QX = 16384, C_MEM = 24576, C_Q = C_MEM + 10240, C_KM = C_Q + 2304, C_VM = C_KM + 2304, C_DUMP = C_VM + 2048,
              C_ZERO = C_DUMP + 1024, C_WL = C_ZERO + 128;
constexpr int NLF = 14;
static_assert(C_WL % 16 == 0 && C_WL + 4 * NLF * 2048 <= LDS_BYTES, "head lds");
#define CH_VIS 10
#define CH_H0 104
#define CH_W0 81
#define CH_MH 3
#define CH_MW 3
#define CH_REC 8
#define CH_PRE 9
#define CH_T0 190
#define CH_UP 2
#define CH_WX 6
#define WAIT_TXT(tmp, t4) "s_memrealtime vcc\n\t.p2align 5\n1:\n\ts_waitcnt lgkmcnt(0)\n\ts_sub_u32 " tmp ", " t4 ", vcc_lo\n\ts_memrealtime vcc\n\ts_cmpk_lt_u32 " tmp ", 400\n\ts_cbranch_scc1 1b\n\t" \
                          "s_add_u32 " tmp ", " tmp ", 4\n\ts_cselect_b32 " tmp ", " tmp ", 0\n2:\n\ts_sub_u32 " tmp ", " tmp ", 1\n\ts_cbranch_scc0 2b\n\t"
DEV void wait_until(u32 t) { u32 tmp; asm volatile(WAIT_TXT("%0", "%1") "s_waitcnt lgkmcnt(0)" : "=&s"(tmp) : "s"(uu(t - 4u)) : "vcc", "scc", "memory"); }
#define SMF_A(acc, a, b) asm volatile("v_smfmac_f32_16x16x64_bf16 %0, %1, %2, %3" : "+v"(acc) : "v"(a), "a"(b), "v"(idx))
#define SMF_V(acc, a, b) asm volatile("v_smfmac_f32_16x16x64_bf16 %0, %1, %2, %3" : "+v"(acc) : "v"(a), "v"(b), "v"(idx))
#define MF0(d, a, b) asm volatile("v_mfma_f32_16x16x32_bf16 %0, %1, %2, 0" : "=&v"(d) : "v"(a), "v"(b))
#define MF1(d, a, b) asm volatile("v_mfma_f32_16x16x32_bf16 %0, %1, %2, %0" : "+v"(d) : "v"(a), "v"(b))
#define MF0H(d, a, b) asm volatile("v_mfma_f32_16x16x16_bf16 %0, %1, %2, 0" : "=&v"(d) : "v"(a), "v"(b))
#define MF1H(d, a, b) asm volatile("v_mfma_f32_16x16x16_bf16 %0, %1, %2, %0" : "+v"(d) : "v"(a), "v"(b))
DEV v8u tr_frag(lptr p0, lptr p1, lptr p2, lptr p3) { return cat8(tr2(p0, p1), tr2(p2, p3)); }
constexpr int qslot(int ks) { return (ks & 1) && ks < 8 ? ks >> 1 : -1; }
constexpr int kslot(int ks) { return ks < 4 ? 4 + ks : -1; }
constexpr int vslot(int ks) { return ks >= 4 ? 4 + ks : -1; }
DEV u32 ring_map(u32 off, u32 half) { return ((off >> 13) << 14) + (half << 13) + (off & 8191u); }
#define ZRD(x, i) x = __builtin_bit_cast(f32x4, lds_r16(ZP + 16 * (i)))
#define VMAX(d, a, b) asm volatile("v_max_f32 %0, %1, %2" : "=v"(d) : "v"(a), "v"(b))
#define VMAX3(d, a, b, c_) asm volatile("v_max3_f32 %0, %1, %2, %3" : "=v"(d) : "v"(a), "v"(b), "v"(c_))
#define VEXP(d, a) asm volatile("v_exp_f32 %0, %1" : "=v"(d) : "v"(a))
#define VMULC(d, a) asm volatile("v_mul_f32 %0, 0x3e0293ee, %1" : "=v"(d) : "v"(a))
#define CADD(d, a, b) do { d = (a) + (b); PIN(d); } while (0)
#define CMULC(d, a) do { d = (a) * CEXP; PIN(d); } while (0)
#define CCVT(d, a, b) do { d = pk(a, b); PIN(d); } while (0)
#define LGK(n) __builtin_amdgcn_s_waitcnt(0xC07F | (n) << 8)
#define SEL(d, a, b) asm volatile("v_cndmask_b32 %0, %1, %2, %3" : "=v"(d) : "v"(a), "v"(b), "s"(himask))
#define QPERM(d, a, q) asm volatile("v_mov_b32_dpp %0, %1 quad_perm:" q " row_mask:0xf bank_mask:0xf" : "=v"(d) : "v"(a))

#define AL_H 3
#define AL_W 0
#define HD_SMP 56
DEV void role_head(const Params& p, lptr lds, int L, int h, v2u ep) {
    const int tid = threadIdx.x, lane = tid & 63, w = uni(tid >> 6), g = lane >> 4, r = lane & 15;
    const rsrc_t rs = mk_rsrc(p.ws, W_END);
    v8u wq0[10], wq1[10], wk0[10], wk1[10], wv0[10], wv1[10];
    lptr WL = lds + C_WL + w * (NLF * 2048) + lane * 16;
    {
        constexpr int GS = 1056, CS = 48 * GS;
        const v4u sq = mk_srd(p.Wq, DM * NQ * 2), sk = mk_srd(p.Wk, DM * NQ * 2), sv = mk_srd(p.Wv, DM * NQ * 2);
        const u32 vo = (u32)(g * NQ + h * DH + 8 * (r ^ ((g & 1) << 3))) * 2u;
        auto fill = [&](int ch) {
#pragma unroll 1
            for (int i = 0; i < 4; ++i) {
                const u32 dst = uu((ch % 3) * CS + (4 * i + w) * GS), so = uu((ch * 64 + 16 * i + 4 * w) * (NQ * 2));
                DMAIN(sq, dst, vo, so); DMAIN(sk, dst + 16 * GS, vo, so); DMAIN(sv, dst + 32 * GS, vo, so);
            }
        };
        fill(0); fill(1); fill(2);
        const int q4 = r >> 2;
        lptr tb = lds + (4 * (g >> 1) + 2 * (q4 >> 1) + (g & 1)) * GS + (q4 & 1) * 256 + ((w * 64 + (r & 3) * 8) ^ ((q4 & 1) << 7));
        SFOR(ch, 10)
            VMW(ch < 8 ? 24 : 108 - 12 * ch);
            BAR();
            auto frag = [&](int m, int t) { lptr b = tb + (ch % 3) * CS + m * 16 * GS + t * 32; return tr_frag(b, b + 512, b + 8 * GS, b + 8 * GS + 512); };
            wq0[ch] = frag(0, 0); wq1[ch] = frag(0, 1); wk0[ch] = frag(1, 0); wk1[ch] = frag(1, 1); wv0[ch] = frag(2, 0); wv1[ch] = frag(2, 1);
            asm volatile("" : "+a"(wq0[ch]), "+a"(wk0[ch]), "+v"(wv0[ch]), "+v"(wv1[ch]));
            if constexpr (qslot(ch) < 0) asm volatile("" : "+a"(wq1[ch])); else asm volatile("" : "+v"(wq1[ch]));
            if constexpr (kslot(ch) < 0) asm volatile("" : "+a"(wk1[ch])); else asm volatile("" : "+v"(wk1[ch]));
            BAR();
            if constexpr (ch < 7) fill(ch + 3);
        SEND
#pragma unroll
        for (int ch = 0; ch < 10; ++ch) {
            if (qslot(ch) >= 0) { lds_w16(WL + qslot(ch) * 2048, wq1[ch].lo); lds_w16(WL + qslot(ch) * 2048 + 1024, wq1[ch].hi); }
            if (kslot(ch) >= 0) { lds_w16(WL + kslot(ch) * 2048, wk1[ch].lo); lds_w16(WL + kslot(ch) * 2048 + 1024, wk1[ch].hi); }
            if (vslot(ch) >= 0) { lds_w16(WL + vslot(ch) * 2048, wv1[ch].lo); lds_w16(WL + vslot(ch) * 2048 + 1024, wv1[ch].hi); }
        }
        CFENCE();
    }
    const u32 idx = (r & 2) ? 0xEEEEu : 0x4444u;
    unsigned long long himask = 0xCCCCCCCCCCCCCCCCull;
    PINS(himask);
    bool dead = false;
    if (h == 3) LST(266 + L, rt32());
    if (w == 0) LST(1240 + L * 32 + h, rt32());
    if (L == 1) {
        KvIn in;
        kvt_load(p, LN0 + 4 * h + w, in); kvt_make(rs, lds + w * 9216, LN0 + 4 * h + w, in);
        DRAIN(); BAR();
        pre_post(rs, 64 + h, 3u, ep);
    }
    const u32 pol0 = pol_wait(rs, ep);
    if (h == 3 && L == 0) LST(277, rt32());
    const u32 l16 = (u32)lane * 16u;
    const u32 mring = W_MRING + (u32)L * (MB / 4) + pol0 * 8192u, hint = mring + 4 * 32768u;
    const u32 fkh = lane < 3 ? foff(F_QKV + L * 96 + lane * 32 + h) : lane < 7 ? hint + (u32)(lane - 3) * 4u : OOB;
    u32 tm = 0;
    MK_SRD();
    u32 ksrc = uu(W_KQ + (u32)((L * NSEG - 1) * NH + h) * 16384u + w * 2048), vsrc = ksrc + (W_VO - W_KQ);
    u32 dk = uu(C_K + w * 2048), dv = dk + (C_V - C_K), dq = dk + (C_QX - C_K), dm = uu(C_MEM + w * 2048), dm2 = uu(w < 2 ? C_MEM + 8192 + w * 1024 : C_DUMP);
    asm volatile("" : "+s"(dk), "+s"(dv), "+s"(dq), "+s"(dm), "+s"(dm2));
    auto keys_wait = [&](u32 need) {
#pragma unroll 1
        for (int n = 0; !dead; ++n) {
            const bool beat = need + 2 * L > 2u;
            if (beat) wait_until(tm);
            const u32 hv = ld4(rs, lane == 7 ? foff(F_ERR) : fkh);
            if (beat) { tm = rdl(hv, 5); wait_until(rdl(hv, 3) - 30u); }
            if (((u32)__builtin_amdgcn_ballot_w64(ld4(rs, fkh) < need) & 7u) == 0u) break;
            dead = n > SPIN_CAP || rdl(hv, 7) != 0u;
            st4s(rs, dead ? foff(F_ERR) : OOB, 0, 1u);
        }
        CFENCE();
        DMA32(dk, l16, ksrc); DMA32(dv, l16, vsrc); DMA32(dq, l16, ksrc ^ 8192u);
    };
    lptr qw = lds + C_Q + (2 * g) * 288 + (32 * w + 2 * r) * 2;
    lptr kw = qw + (C_KM - C_Q); PIN(kw);
    lptr qr = lds + C_Q + (2 * (r >> 2) + (r & 1)) * 288 + g * 16;
    lptr LB = lds + l16, VX = lds + C_V + w * 2048 + l16;
    PIN(VX);
    if (tid >= 64 && tid < 70) { const u32 z = tid == 68 ? 0x3F803F80u : tid == 69 ? PSENT : 0u; lds_w16(lds + C_ZERO + (tid - 64) * 16, (v4u){z, z, z, z}); }
    u32 zaddr = 0; PIN(zaddr);
    lptr ZP = lds + zaddr + C_ZERO;
    const float CEXP = SCALE * LOG2E;
#define LF_RD(slot, hf) lf[slot][hf] = lds_r16(WL + (slot) * 2048 + (hf) * 1024)
    BAR();
    const u32 ofl = tid == 0 ? foff(F_O + L * 32 + h) : OOB;
    u32 oso = OOB + W_ORING + (u32)L * (MB / 2) + ring_map((u32)((r >> 3) * 32768 + h * 1024 + w * 256 + (g >> 1) * 128 + (r & 7) * 16), pol0);
    unsigned long long vis[8];
#pragma unroll
    for (int i = 0; i < 8; ++i) vis[i] = __builtin_amdgcn_ballot_w64(16 * (i >> 2) + 4 * g + (i & 3) <= 16 * (w & 1) + r);
    u32 missed = 0;
    for (int s = -1; s < NSEG;) {
        u32 sv = (u32)max(s, 0), ms = mring + (sv & 3u) * 32768u + w * 2048, ms2 = mring + (sv & 3u) * 32768u + 16384u + (w & 1) * 1024;
        asm volatile("" : "+s"(ms), "+s"(ms2)); PIN(sv);
        f32x4 aq0, aq1, ak0, ak1;
        v4u lf[NLF][2];
        ZRD(aq0, 0); ZRD(aq1, 1); ZRD(ak0, 2); ZRD(ak1, 3);
#pragma unroll
        for (int i = 0; i < 4; ++i) { LF_RD(i, 0); LF_RD(i, 1); }
        SCHED();
        v4u A[10];
#define HT(k) do {} while (0)
        v4u kx[8], qf[4], kmf[4], vx[2], ones, P;
        float fa, fb, fc, fd, inv, sx[4], t[4], tm0, tm1; u32 pa, pb, fl, e01, e23, pmm;
        f32x4 av0, av1, s0, s1, sm, oa, ob, rsum;
        v2u vm, pm;
#define LW(sl) cat8(lf[sl][0], lf[sl][1])
#define PQ(ks, tl) do { if constexpr (!(tl)) SMF_A(aq0, A[ks], wq0[ks]); else if constexpr (qslot(ks) < 0) SMF_A(aq1, A[ks], wq1[ks]); else { const v8u lw_ = LW(qslot(ks)); SMF_V(aq1, A[ks], lw_); } } while (0)
#define PK(ks, tl) do { if constexpr (!(tl)) SMF_A(ak0, A[ks], wk0[ks]); else if constexpr (kslot(ks) < 0) SMF_A(ak1, A[ks], wk1[ks]); else { const v8u lw_ = LW(kslot(ks)); SMF_V(ak1, A[ks], lw_); } } while (0)
#define PV(ks, tl) do { if constexpr (!(tl)) SMF_V(av0, A[ks], wv0[ks]); else if constexpr (vslot(ks) < 0) SMF_V(av1, A[ks], wv1[ks]); else { const v8u lw_ = LW(vslot(ks)); SMF_V(av1, A[ks], lw_); } } while (0)
#define KXR(i) kx[i] = lds_r16(LB + C_K + ((i) & 1) * 4096 + ((i) >> 1) * 1024)
#define XW(q_) do { *(LAS u32*)(q_) = pa; *(LAS u32*)((q_) + 288) = pb; } while (0)
        auto slot = [&](auto zc) __attribute__((always_inline)) {
            constexpr int z = decltype(zc)::value;
            if constexpr (z < 14) PQ(z >> 1, z & 1);
            else if constexpr (z < 17) PQ(z - 7, 0);
            else if constexpr (z < 20) PQ(z - 10, 1);
            else if constexpr (z < 34) PK((z - 20) >> 1, z & 1);
            else if constexpr (z < 37) PK(z - 27, 0);
            else if constexpr (z < 40) PK(z - 30, 1);
            else if constexpr (z < 42) { if constexpr (z == 40) MF0(s0, kx[0], qf[0]); else MF0(s1, kx[1], qf[0]); }
            else if constexpr (z < 48) { if constexpr (z & 1) MF1(s1, kx[z - 40], qf[(z - 40) >> 1]); else MF1(s0, kx[z - 40], qf[(z - 40) >> 1]); }
            else if constexpr (z < 54) PV(z - 48, 0);
            else if constexpr (z == 54) MF0(sm, kmf[0], qf[0]);
            else if constexpr (z < 58) MF1(sm, kmf[z - 54], qf[z - 54]);
            else if constexpr (z < 62) PV(z - 52, 0);
            else if constexpr (z < 64) PV(z - 62, 1);
            else if constexpr (z == 64) MF0(oa, vx[0], P);
            else if constexpr (z == 65) MF0(ob, vx[1], P);
            else if constexpr (z == 66) MF0(rsum, ones, P);
            else if constexpr (z < 75) PV(z < 70 ? z - 63 : z == 70 ? 2 : z == 71 ? 7 : z == 72 ? 3 : z - 65, 1);
            else { const v2u ones2 = ones.lo; MF1H(rsum, ones2, pm); }
            if constexpr (z < 6) A[4 + z] = lds_r16(LB + C_MEM + (4 + z) * 1024);
            if constexpr (z >= 6 && z < 10) LF_RD(4 + ((z - 6) >> 1), z & 1);
            if constexpr (z >= 14 && z < 18) LF_RD(6 + ((z - 14) >> 1), z & 1);
            if constexpr (z >= 22 && z < 28) KXR(z - 22);
            if constexpr (z == 28) XW(qw);
            if constexpr (z == 29 || z == 30) KXR(z - 23);
            if constexpr (z == 32 || z == 47) asm volatile("s_barrier" ::: "memory");
            if constexpr (z >= 33 && z < 37) qf[z - 33] = lds_r16(qr + (z - 33) * 64);
            if constexpr (z == 20) ZRD(av0, 0);
            if constexpr (z == 43) XW(kw);
            if constexpr (z == 44 || z == 45) vx[z - 44] = lds_r16(VX + (z - 44) * 1024);
            if constexpr (z >= 48 && z < 52) kmf[z - 48] = lds_r16(qr + (C_KM - C_Q) + (z - 48) * 64);
            if constexpr (z == 52) ZRD(av1, 1);
            if constexpr (z == 37) ones = lds_r16(ZP + 64);
            if constexpr (z >= 57 && z < 64) LF_RD(8 + ((z - 57) >> 1), (z - 57) & 1);
            if constexpr (z >= 65 && z < 70) LF_RD(8 + ((z - 58) >> 1), (z - 58) & 1);
            if constexpr (z == HD_SMP) LD4A(fl, fkh, 0);
            SCHED();
            if constexpr (z == 19) HT(2);
            if constexpr (z == 32) HT(3);
            if constexpr (z == 47) HT(4);
            if constexpr (z == 64) HT(5);
            if constexpr (z == 19) LGK(4);
            if constexpr (z == 21 || z == 41) LGK(1);
            if constexpr (z == 56 || z == 73) LGK(0);
            if constexpr (z == 31 || z == 46) LGK(2);
            if constexpr (z == 39) LGK(4);
            if constexpr (z >= 53 && z < 56) LGK(57 - z);
            if constexpr (z == 64) LGK(3);
            if constexpr (z == 68) LGK(5);
            if constexpr (z >= 70 && z < 73) LGK(74 - z);
            SCHED();
            if constexpr (z == 18) CADD(fa, aq0[0], aq0[2]);
            if constexpr (z == 19) CADD(fb, aq0[1], aq0[3]);
            if constexpr (z == 20) CMULC(fa, fa);
            if constexpr (z == 21) CADD(fc, aq1[0], aq1[2]);
            if constexpr (z == 22) CADD(fd, aq1[1], aq1[3]);
            if constexpr (z == 23) CMULC(fc, fc);
            if constexpr (z == 24) CMULC(fb, fb);
            if constexpr (z == 25) CMULC(fd, fd);
            if constexpr (z == 26) CCVT(pa, fa, fc);
            if constexpr (z == 27) CCVT(pb, fb, fd);
            if constexpr (z == 38) CADD(fa, ak0[0], ak0[2]);
            if constexpr (z == 39) CADD(fb, ak0[1], ak0[3]);
            if constexpr (z == 41) { CADD(fc, ak1[0], ak1[2]); CADD(fd, ak1[1], ak1[3]); }
            if constexpr (z == 42) { CCVT(pa, fa, fc); CCVT(pb, fb, fd); }
            if constexpr (z >= 49 && z < 53) SEL(sx[z - 49], s0[z - 49], s1[z - 49]);
            if constexpr (z >= 53 && z < 57) VEXP(t[z - 53], sx[z - 53]);
            if constexpr (z == 57) CCVT(e01, t[0], t[1]);
            if constexpr (z == 58) CCVT(e23, t[2], t[3]);
            if constexpr (z == 59) QPERM(P.x, e01, "[0,1,0,1]");
            if constexpr (z == 60) QPERM(P.z, e01, "[2,3,2,3]");
            if constexpr (z == 61) QPERM(P.y, e23, "[0,1,0,1]");
            if constexpr (z == 62) QPERM(P.w, e23, "[2,3,2,3]");
            if constexpr (z == 63) CADD(fa, av0[0], av0[2]);
            if constexpr (z == 64) { asm volatile("" :: "v"(sm)); VEXP(tm0, sm[0]); }
            if constexpr (z == 65) VEXP(tm1, sm[1]);
            if constexpr (z == 66) CADD(fb, av0[1], av0[3]);
            if constexpr (z == 67) CCVT(pmm, tm0, tm1);
            if constexpr (z >= 68 && z < 72) SEL(oa[z - 68], oa[z - 68], ob[z - 68]);
            if constexpr (z == 72) asm volatile("v_cndmask_b32 %0, %1, 0, %2" : "=v"(pm.x) : "v"(pmm), "s"(himask));
            if constexpr (z == 73) asm volatile("v_cndmask_b32 %0, 0, %1, %2" : "=v"(pm.y) : "v"(pmm), "s"(himask));
            if constexpr (z == 74) LGK(15);
            if constexpr (z == 75) CCVT(vm.x, fa, fb);
            SCHED();
        };
        {
            u32 tmp;
            asm volatile("s_mov_b32 m0, %2\n\t" WAIT_TXT("%0", "%1") "buffer_load_dwordx4 %4, %5, %6 offen sc1 lds\n\tbuffer_load_dwordx4 %4, %5, %6 offen offset:1024 sc1 lds\n\t"
                         "s_mov_b32 m0, %3\n\ts_nop 0\n\tbuffer_load_dwordx4 %4, %5, %7 offen sc1 lds\n\t.p2align 6\n\t.rept %8\n\ts_nop 0\n\t.endr\n\ts_waitcnt vmcnt(0) lgkmcnt(0)\n\ts_barrier"
                         : "=&s"(tmp) : "s"(uu(tm - 4u)), "s"(dm), "s"(dm2), "v"(l16), "s"(srd), "s"(ms), "s"(ms2), "i"(AL_H) : "vcc", "scc", "memory");
        }
        HT(1);
#pragma unroll
        for (int ks = 0; ks < 4; ++ks) A[ks] = lds_r16(LB + C_MEM + ks * 1024);
        SCHED();
        st4s(rs, ofl, 0, sv);
        SCHED();
        static_for<0, 76>(slot);
        asm volatile("s_nop 2\n\tv_add_f32 %1, %3, %5\n\tv_add_f32 %2, %4, %6\n\tv_cvt_pk_bf16_f32 %0, %1, %2"
                     : "=v"(vm.y), "=&v"(fc), "=&v"(fd) : "v"(av1[0]), "v"(av1[1]), "v"(av1[2]), "v"(av1[3]));
        __builtin_amdgcn_s_waitcnt(0xCF7F);
        asm volatile("s_nop 0\n\tv_mfma_f32_16x16x16_bf16 %0, %2, %3, %0\n\tv_rcp_f32 %1, %4" : "+v"(oa), "=&v"(inv) : "v"(vm), "v"(pm), "v"(rsum[0]), "v"(rsum));
        SCHED();
        if (__builtin_expect(__builtin_amdgcn_ballot_w64(u_(rsum[0]) - 0x21800000u > 0x3C000000u) != 0ull, 0)) {
            DRAIN(); SCHED();
            bool miss = false;
#pragma unroll 1
            for (int i = 0; i < 10; ++i) { const v4u a = lds_r16(LB + C_MEM + i * 1024); miss |= a.x == PSENT || a.y == PSENT || a.z == PSENT || a.w == PSENT; }
            if (missed < SPIN_CAP && !all_(!miss)) {
                *(LAS u32*)(lds + C_DUMP + (w == 0 ? 0 : 64)) = ld4(rs, foff(F_ERR));
                asm volatile("" : "+v"(fl) :: "memory");
                tm = rdl(fl, 5);
                BAR();
                missed = uu(*(LAS u32*)(lds + C_DUMP)) != 0u ? SPIN_CAP : missed + 1;
                st4s(rs, missed == SPIN_CAP ? foff(F_ERR) : OOB, 0, 1u);
                BAR();
                continue;
            }
            float mx = fmaxf(fmaxf(fmaxf(sx[0], sx[1]), fmaxf(sx[2], sx[3])), fmaxf(sm[0], sm[1]));
            mx = max32(max16(fmaxf(mx, dpp<0x4E>(mx))));
#pragma unroll
            for (int i = 0; i < 4; ++i) t[i] = __builtin_amdgcn_exp2f(sx[i] - mx);
            e01 = pk(t[0], t[1]); e23 = pk(t[2], t[3]);
            pmm = pk(__builtin_amdgcn_exp2f(sm[0] - mx), __builtin_amdgcn_exp2f(sm[1] - mx));
            asm volatile("v_cndmask_b32 %0, %2, 0, %3\n\tv_cndmask_b32 %1, 0, %2, %3" : "=&v"(pm.x), "=&v"(pm.y) : "v"(pmm), "s"(himask));
            asm volatile("s_nop 1" : "+v"(e01), "+v"(e23), "+v"(pm));
            QPERM(P.x, e01, "[0,1,0,1]"); QPERM(P.z, e01, "[2,3,2,3]"); QPERM(P.y, e23, "[0,1,0,1]"); QPERM(P.w, e23, "[2,3,2,3]");
            NOPS(1);
            MF0(rsum, ones, P); MF0(oa, vx[0], P); MF0(ob, vx[1], P);
            NOPS(7);
            { const v2u ones2 = ones.lo; MF1H(rsum, ones2, pm); }
#pragma unroll
            for (int i = 0; i < 4; ++i) SEL(oa[i], oa[i], ob[i]);
            NOPS(1);
            MF1H(oa, vm, pm);
            asm volatile("s_nop 7\n\tv_rcp_f32 %0, %1" : "=v"(inv) : "v"(rsum[0]), "v"(rsum));
        }
        {
            v4u oc; f32x2 u0 = oa.lo, u1 = oa.hi; const f32x2 i2 = {inv, inv};
            asm volatile("s_nop 0\n\tv_pk_mul_f32 %0, %0, %2\n\tv_pk_mul_f32 %1, %1, %2" : "+v"(u0), "+v"(u1) : "v"(i2));
            __builtin_amdgcn_s_waitcnt(0xCF7F);
            asm volatile("v_cvt_pk_bf16_f32 %0, %4, %5\n\tv_cvt_pk_bf16_f32 %2, %4, %5\n\tv_cvt_pk_bf16_f32 %1, %6, %7\n\tv_cvt_pk_bf16_f32 %3, %6, %7\n\t"
                         "v_permlane16_swap_b32 %0, %2\n\ts_nop 0\n\tv_permlane16_swap_b32 %1, %3"
                         : "=&v"(oc.x), "=&v"(oc.y), "=&v"(oc.z), "=&v"(oc.w) : "v"(u0.x), "v"(u0.y), "v"(u1.x), "v"(u1.y));
            st16(rs, oso, oc);
        }
        SCHED();
        unsigned long long clk;
        asm volatile("s_memrealtime %0" : "=s"(clk) :: "memory");
        HT(6);
        v4u tq[4], tk[8], tv[2];
        f32x4 c0, c1, cm;
        u32 oo, kvo; bool kvok;
        u32 ln = l16; PIN(ln);
        lptr QX = lds + C_QX + (w & 1) * 4096 + ln, VO = lds + C_V + (w ^ 1) * 2048 + ln, VMX = lds + C_VM + w * 512 + (ln >> 1);
#pragma unroll
        for (int i = 0; i < 2; ++i) { tq[i] = lds_r16(QX + i * 1024); kmf[i] = lds_r16(qr + (C_KM - C_Q) + i * 64); tk[2 * i] = lds_r16(LB + C_K + i * 1024); tk[2 * i + 1] = lds_r16(LB + C_K + 4096 + i * 1024); }
        *(LAS v2u*)VMX = vm;
        SCHED();
        SFOR(i, 12)
            if constexpr (i == 0) MF0(cm, kmf[0], tq[0]); else if constexpr (i == 1) MF0(c0, tk[0], tq[0]); else if constexpr (i == 2) MF0(c1, tk[1], tq[0]);
            else if constexpr (i % 3 == 0) MF1(cm, kmf[i / 3], tq[i / 3]); else if constexpr (i % 3 == 1) MF1(c0, tk[2 * (i / 3)], tq[i / 3]); else MF1(c1, tk[2 * (i / 3) + 1], tq[i / 3]);
            if constexpr (i < 2) kmf[2 + i] = lds_r16(qr + (C_KM - C_Q) + (2 + i) * 64);
            if constexpr (i == 0) { tq[2] = lds_r16(QX + 2048); tk[4] = lds_r16(LB + C_K + 2048); }
            if constexpr (i == 1) { tk[5] = lds_r16(LB + C_K + 6144); tq[3] = lds_r16(QX + 3072); }
            if constexpr (i == 2) { tk[6] = lds_r16(LB + C_K + 3072); tk[7] = lds_r16(LB + C_K + 7168); }
            if constexpr (i == 3) { tv[0] = lds_r16(VO); tv[1] = lds_r16(VO + 1024); }
            if constexpr (i == 4) { oo = ln + (ln & 0x300u) + ((vsrc ^ 8192u) + (u32)((w & 1) * 256) + ((u32)s & OOB)); PIN(oo); }
            if constexpr (i == 5) { ksrc += NH * 16384; vsrc += NH * 16384; PINS(ksrc); PINS(vsrc); }
            if constexpr (i == 9) { asm volatile("s_waitcnt vmcnt(1)" : "+v"(fl) :: "memory"); kvok = ((u32)__builtin_amdgcn_ballot_w64(fl < (u32)s + 2) & 7u) == 0u; }
            if constexpr (i == 10) { kvo = l16 | (kvok ? 0u : OOB); PIN(kvo); }
            SCHED();
        SEND
        asm volatile("s_waitcnt lgkmcnt(0)\n\ts_barrier" : "+s"(clk) :: "memory");
        const v2u tvm = *(LAS v2u*)(VMX + (512 - (w & 1) * 1024));
        const v4u pv = lds_r16(ZP + 80);
        DMA32(dk, kvo, ksrc);
        SCHED(); asm volatile("" :: "v"(cm));
        float u[10], m, m2;
        {
            float NEG = -1000000000.0f / SCALE; PIN(NEG);
#define MSK(d, a, i) asm volatile("v_cndmask_b32 %0, %1, %2, %3" : "=v"(d) : "v"(NEG), "v"(a), "s"(vis[i]))
#pragma unroll
            for (int i = 0; i < 4; ++i) MSK(u[i], c0[i], i);
#pragma unroll
            for (int i = 0; i < 4; ++i) MSK(u[4 + i], c1[i], 4 + i);
            asm volatile("v_max3_f32 %0, %2, %3, %10\n\tv_max3_f32 %1, %4, %5, %11\n\tv_max3_f32 %0, %0, %6, %7\n\tv_max3_f32 %1, %1, %8, %9\n\t"
                         "v_max_f32 %0, %0, %1\n\tv_mov_b32 %1, %0\n\ts_nop 1\n\tv_permlane16_swap_b32 %0, %1\n\tv_max_f32 %0, %0, %1\n\tv_mov_b32 %1, %0\n\ts_nop 1\n\t"
                         "v_permlane32_swap_b32 %0, %1\n\tv_max_f32 %0, %0, %1\n\tv_mul_f32 %0, 0xbe0293ee, %0"
                         : "=&v"(m), "=&v"(m2) : "v"(u[0]), "v"(u[1]), "v"(u[2]), "v"(u[3]), "v"(u[4]), "v"(u[5]), "v"(u[6]), "v"(u[7]), "v"(cm[0]), "v"(cm[1]));
        }
        DMA32(dv, kvo, vsrc);
        {
            v4u px; v2u p0, p1;
#define EX2(d, a, b) asm volatile("v_fmamk_f32 %0, %2, 0x3e0293ee, %4\n\tv_fmamk_f32 %1, %3, 0x3e0293ee, %4\n\tv_exp_f32 %0, %0\n\tv_exp_f32 %1, %1" : "=&v"(d.x), "=&v"(d.y) : "v"(a), "v"(b), "v"(m))
            f32x2 ex[5];
            EX2(ex[0], u[0], u[1]); EX2(ex[1], u[2], u[3]); EX2(ex[2], u[4], u[5]); EX2(ex[3], u[6], u[7]); EX2(ex[4], cm[0], cm[1]);
            asm volatile("v_cvt_pk_bf16_f32 %0, %8, %9\n\tv_cvt_pk_bf16_f32 %1, %10, %11\n\tv_cvt_pk_bf16_f32 %2, %12, %13\n\tv_cvt_pk_bf16_f32 %3, %14, %15\n\t"
                         "v_mov_b32 %5, 0\n\tv_mov_b32 %6, 0\n\tv_cvt_pk_bf16_f32 %4, %16, %17\n\tv_cvt_pk_bf16_f32 %7, %16, %17"
                         : "=&v"(px.x), "=&v"(px.y), "=&v"(px.z), "=&v"(px.w), "=&v"(p0.x), "=&v"(p0.y), "=&v"(p1.x), "=&v"(p1.y)
                         : "v"(ex[0].x), "v"(ex[0].y), "v"(ex[1].x), "v"(ex[1].y), "v"(ex[2].x), "v"(ex[2].y), "v"(ex[3].x), "v"(ex[3].y), "v"(ex[4].x), "v"(ex[4].y));
            f32x4 o[4], sum;
            const v2u ones2 = ones.lo;
            const u32 now = (u32)clk;
            u32 oz;
            SCHED();
            MF0(sum, ones, px); { const u32 q = ksrc ^ 8192u; DMA32(dq, kvo, q); } SCHED();
            MF0(o[0], vx[0], px);
            MF0(o[1], vx[1], px);
            MF0(o[2], tv[0], px); oz = (oso & ~(3u << 17)) | (u32)((s + 3) & 3) << 17; PIN(oz); SCHED();
            MF0(o[3], tv[1], px); oso = (oso & ~(3u << 17)) | (u32)((s + 1) & 3) << 17; PIN(oso); SCHED();
            MF1H(sum, ones2, p0);
            MF1H(o[0], vm, p0);
            MF1H(o[1], vm, p1); st16(rs, oz, pv); SCHED();
            MF1H(o[2], tvm, p0);
            MF1H(o[3], tvm, p1);
            asm volatile("s_nop 2\n\tv_rcp_f32 %0, %1\n\ts_nop 0" : "=v"(inv) : "v"(sum[0]), "v"(sum));
            asm volatile("" : "+v"(o[0]), "+v"(o[1]));
            st16(rs, oo, (v4u){pk2(o[0].lo * inv), pk2(o[0].hi * inv), pk2(o[1].lo * inv), pk2(o[1].hi * inv)});
            asm volatile("" : "+v"(o[2]), "+v"(o[3]));
            st16(rs, oo ^ 2048u, (v4u){pk2(o[2].lo * inv), pk2(o[2].hi * inv), pk2(o[3].lo * inv), pk2(o[3].hi * inv)});
            SCHED();
            LS2(L == 0 && w == 0, h);
            st4s(rs, (h == 0 && tid == 192) ? hint + 128u : OOB, 0, (u32)s << 16 | min(missed, 1u) << 14 | min(now - tm, 0x3FFFu));
            tm = rdl(fl, 5);
            missed = missed == SPIN_CAP ? missed : 0u;
            if (__builtin_expect((int)(rdl(fl, 3) - now) < CH_VIS - 4 || s < 1 - L, 0)) {
                const bool first = s == 0 && L == 0;
                if (s < 0) { oso -= (ln & 256u) ? 0u : OOB; tm = L ? tm : 0u; st4s(rs, tid == 0 ? hint + 4096u + (u32)h * 128u : OOB, 0, 0u); }
#pragma unroll 1
                for (int n = 0; n < (s < 0 ? 0 : first ? 200 : 8); ++n) {
                    CFENCE();
                    const u32 hv = ld4(rs, fkh);
                    tm = rdl(hv, first ? 6 : 5);
                    if (first ? tm != PSENT : (int)(rdl(hv, 3) - now) >= CH_VIS - 4) break;
                }
            }
        }
        if (__builtin_expect(!kvok, 0) && s + 1 < NSEG) {
            st4s(rs, ln == 0 ? hint + 4096u + (u32)h * 128u : OOB, 0, (u32)s + 1u);
            keys_wait((u32)s + 2u);
        }
        ++s;
    }
    DRAIN(); BAR();
    st4s(rs, ofl, 0, NSEG);
}

#define WO_NL 3
#define WO_ROT 0
#define WO_AH 1
DEV void role_wo(const Params& p, lptr lds, int j, v2u ep) {
    const int tid = threadIdx.x, lane = tid & 63, w = uni(tid >> 6), g = lane >> 4, r = lane & 15;
    const int rg = j & 1, cg = j >> 1, rot = WO_ROT;
    const rsrc_t rs = mk_rsrc(p.ws, W_END);
    v8u wo[8][5];
    {
        const v4u so_ = mk_srd(p.Wo, DM * NQ * 2);
        u32 vo[5];
#pragma unroll
        for (int i = 0; i < 5; ++i) { const int n = 64 * i + lane; vo[i] = (u32)((n / 5) * DM + cg * 40 + (n % 5) * 8) * 2u; }
        v4u junk = {};
        const u32 av = (u32)(j * 5120 + lane * 16);
        auto ahead = [&](int b) {
#pragma unroll
            for (int i = 0; i < 5; ++i) AHEAD(so_, av, uu((8 * w + b) * (DH * DM * 2) + i * 1024));
        };
        auto fill = [&](int b) {
            if (b + WO_AH < 8) ahead(b + WO_AH);
            const u32 dst = uu(w * 40960 + (b & 3) * 10240), src = uu((8 * w + ((b + rot) & 7)) * (DH * DM * 2));
#pragma unroll
            for (int i = 0; i < 10; ++i) DMAIN(so_, dst + i * 1024, vo[i % 5], src + (i / 5) * (64 * DM * 2));
        };
#pragma unroll 1
        for (int b = 0; b < WO_AH; ++b) ahead(b);
#pragma unroll 1
        for (int b = 0; b < 4; ++b) fill(b);
        const int q4 = r >> 2;
        lptr tb = lds + w * 40960 + (32 * (g >> 1) + 16 * (q4 >> 1) + 8 * ((r >> 1) & 1) + 4 * (g & 1) + (q4 & 1)) * 80 + (r & 1) * 8;
        SFOR(b, 8)
            VMW((b < 5 ? 30 : 70 - 10 * b) + (b + 1 + WO_AH < 8 ? 5 : 0) + (b + 2 + WO_AH < 8 ? 5 : 0) + (b + 3 + WO_AH < 8 ? 5 : 0));
            if constexpr (b + WO_AH == 7) PIN(junk);
#pragma unroll
            for (int t = 0; t < 5; ++t) {
                lptr q = tb + (b & 3) * 10240 + t * 16;
                wo[b][t] = tr_frag(q, q + 160, q + 64 * 80, q + 64 * 80 + 160);
                if (b * 5 + t < 32) asm volatile("" : "+a"(wo[b][t])); else asm volatile("" : "+v"(wo[b][t]));
            }
            CFENCE();
            if constexpr (b < 4) fill(b + 4);
        SEND
    }
    if (j == 0) LST(265, rt32());
    LST(1100 + j * 4 + w, rt32());
    const u32 idx = (r & 2) ? 0xEEEEu : 0x4444u;
    const u32 pol0 = pol_wait(rs, ep);
    BAR();
    const u32 l16 = (u32)lane * 16u;
    u32 oo[8];
#pragma unroll
    for (int i = 0; i < 8; ++i) oo[i] = uu(W_ORING + ring_map((u32)(rg * 32768 + (8 * w + ((i + rot) & 7)) * 1024), pol0));
    const u32 hint = W_MRING + 4 * 32768u + pol0 * 8192u;
    u32 hvo[2];
#pragma unroll
    for (int l = 0; l < 2; ++l) hvo[l] = lane < 2 ? hint + (u32)(l ^ 1) * (MB / 4) + (u32)lane * 4u : lane == 2 ? hint + (u32)l * (MB / 4) + 128u : lane == 3 ? foff(F_ERR) :
                 (lane >= 32 && j == 0 && w == 3) ? hint + (u32)l * (MB / 4) + (u32)lane * 128u : OOB;
    lptr xw = r < 8 ? lds + w * 640 + (2 * (g & 1) + (g >> 1)) * 32 + r * 4 : lds + 8192 + lane * 4;
    const int G = ((5 * cg) >> 1) + w, tt = 2 * G + ((lane >> 3) & 1) - 5 * cg;
    const bool pub = w < 3 && lane < 32 && tt >= 0 && tt < 5;
    lptr xr = lds + (pub ? tt * 128 + (2 * (lane >> 4) + ((lane >> 2) & 1)) * 32 + (lane & 3) * 8 : 0);
    const u32 mpo = pub ? W_MRING + ring_map((u32)(G * 256 + rg * 128 + lane * 4), pol0) : OOB;
    constexpr u32 PER0 = CH_W0 + CH_MW + CH_H0 + CH_MH + 2 * CH_VIS, DW0 = CH_W0 + CH_VIS + CH_MW + CH_REC;
    const bool lead = j == 0 && w == 3;
    u32 hso = OOB;
    MK_SRD();
    u32 t = 0, tn = 0, tm0 = 0, dry = 2;
    int sn[2] = {0, 0};
    u32 hav = 4 * CH_H0, wav = 4 * CH_W0, mh = 8 * CH_MH, mw = 8 * CH_MW, ok[2] = {0u, 0u}, dw[2] = {DW0, DW0};
    u32 vo = l16 + 2 * 131072u, early = 1u, tp = 0, fin, left, cnt = 0;
    unsigned long long clk = 0;
    auto start = [&] {
        asm volatile("s_waitcnt lgkmcnt(0)" : "+s"(clk));
        if (w == 0) LST(1310 + j, rt32());
        if (lead) {
#pragma unroll 1
            for (int n = 0; n < SPIN_CAP; ++n) {
                CFENCE();
                const u32 a = ld4(rs, foff(F_QKV + lane)), b = ld4(rs, lane < 32 ? foff(F_QKV + 64 + lane) : hvo[0]);
                if (all_(a != 0u && b != (lane < 32 ? 0u : PSENT))) break;
                __builtin_amdgcn_s_sleep(1);
            }
        }
        if (lead) {
            t = rt32() + CH_T0; tn = t + PER0 / 2 + CH_REC;
            st16(rs, lane < 2 ? hint + (u32)lane * (MB / 4) : OOB, (v4u){lane == 0 ? t : tn, PER0, (lane == 0 ? t : tn) + DW0, t + DW0});
        } else {
#pragma unroll 1
            for (int n = 0; n < SPIN_CAP; ++n) {
                CFENCE();
                const u32 hv = ld4(rs, hvo[1]);
                t = rdl(hv, 0);
                if (rdl(hv, 1) < 400u) break;
                __builtin_amdgcn_s_sleep(1);
            }
        }
        hso = (lead && lane == 0) ? hint : OOB; tm0 = t + DW0; dw[0] = dw[1] = DW0; dry = 0; vo = l16; early = PSENT;
    };
    u32 zero = 0, hv = 0xFFFFu, pst = 1, pmp = OOB;
    const u32 psent = PSENT;
    lptr sw = lds + 2560 + w * 4;
    asm volatile("" : "+v"(zero), "+v"(sw));
    auto turn = [&](auto lc) __attribute__((always_inline)) {
        constexpr int L = decltype(lc)::value, M = L ^ 1;
        u32 done, rep, rd, lost, dh, dn, nt, za, pdur, mp; v4u hw;
        const int sc = sn[L];
        auto books = [&](auto ic) __attribute__((always_inline)) {
            constexpr int i = decltype(ic)::value;
            if constexpr (i == 0) { rep = rdl(hv, 2); PINS(rep); }
            if constexpr (i == 1) { lost = (u32)(__builtin_amdgcn_ballot_w64(hv == (u32)sn[M]) >> 32); PINS(lost); }
            if constexpr (i == 2) { lost |= (rep >> 16) ^ (u32)sn[M]; PINS(lost); }
            if constexpr (i == 3) { lost |= ok[M] ^ 2u; rd = rep & 0xFFFFu; PINS(lost); PINS(rd); }
            if constexpr (i == 4) { lost = lost == 0u ? CH_UP * 8u : 0u; PINS(lost); }
            if constexpr (i == 5) { dn = wav >> 2; PINS(dn); pdur = min((u32)clk - tp, dn + 8u); PINS(pdur); }
            if constexpr (i == 6) { pdur = (done & ok[M]) ? pdur : dn; PINS(pdur); }
            if constexpr (i == 7) { wav += pdur - dn; PINS(wav); }
            if constexpr (i == 8) { dh = hav >> 2; PINS(dh); nt = min(rd, dh + 8u); PINS(nt); }
            if constexpr (i == 9) { nt = rd < 0x3FFFu ? nt : dh; PINS(nt); }
            if constexpr (i == 10) { hav += nt - dh; PINS(hav); }
            if constexpr (i == 11) { mw = max(mw - 1u, CH_MW * 8u); PINS(mw); }
            if constexpr (i == 12) { mh = max(mh - 1u, CH_MH * 8u); PINS(mh); }
            if constexpr (i == 13) { rd = rd >> 14 == 1u ? lost : 0u; PINS(rd); }
            if constexpr (i == 14) { mw += rd; mh += lost - rd; PINS(mw); PINS(mh); }
            if constexpr (i == 15) { ok[M] = done ? 1u : (ok[M] & 1u) << 1; PINS(ok[M]); }
            if constexpr (i == 16) { dh = (2u * hav + mh) / 8u + (sn[1] < 3 ? CH_VIS + CH_PRE : CH_VIS); PINS(dh); }
            if constexpr (i == 17) { dn = (2u * wav + mw) / 8u + CH_VIS; PINS(dn); }
            if constexpr (i == 18) { nt = dw[L] + dh + (u32)max((int)((u32)clk + CH_WX - t), 0); PINS(nt); }
            if constexpr (i == 19) { dh += dn; dw[L] = dn; sn[M] += (int)done; PINS(dh); PINS(sn[M]); }
            if constexpr (i == 20) { rd = tn - t + (dh >> 1); PINS(rd); }
            if constexpr (i == 22) { nt = t + (u32)max((int)nt, (int)rd); PINS(nt); }
            if constexpr (i == 23) { hw.x = nt; hw.y = dh; PIN(hw.x); PIN(hw.y); }
            if constexpr (i == 24) { hw.z = nt + dn; hw.w = tm0; PIN(hw); }
            if constexpr (i == 25) st16(rs, hso + L * (MB / 4), hw);
        };
        {
#define WT(k) do {} while (0)
            WT(0);
            v4u d[8];
            {
                u32 tmp;
                asm volatile(WAIT_TXT("%1", "%2") "buffer_load_dwordx4 %0, %3, %4, %5 offen sc1\n\ts_waitcnt lgkmcnt(0)"
                             : "=&v"(d[0]), "=&s"(tmp) : "s"(uu(t - 4u)), "v"(vo), "s"(srd), "s"(oo[0]) : "vcc", "scc", "memory");
                PINS(clk);
            }
            SFOR(i, 7)
                asm volatile("buffer_load_dwordx4 %0, %1, %2, %3 offen sc1" : "=v"(d[i + 1]) : "v"(vo), "s"(srd), "s"(oo[i + 1]) : "memory");
                SCHED();
                if constexpr (i == 0) { done = uu(pst) ^ 1u; PINS(done); }
                static_for<3 * i, 3 * i + 3>(books);
                SCHED();
            SEND
            LD4A(hv, hvo[L], 0);
            SCHED();
            WT(1);
            static_for<22, 26>(books);
            za = done ? pmp : OOB;
            st4s(rs, za, 0, psent);
            *(LAS u32*)(sw + L * 4096) = zero;
            f32x4 acc[5] = {};
            mp = mpo + (L * (MB / 4) + (u32)((sc + 1) & 3) * 32768u); pmp = mpo + (L * (MB / 4) + (u32)(sc & 3) * 32768u);
            asm volatile("" : "+v"(acc[0]), "+v"(acc[1]), "+v"(acc[2]), "+v"(acc[3]), "+v"(acc[4]), "+v"(mp), "+v"(pmp));
            SCHED();
            float f0[5], f1[5]; unsigned long long bad;
#define WSW(o_) asm volatile("v_permlane32_swap_b32 %0, %1" : "+v"(f0[o_]), "+v"(f1[o_]))
#define WAD(o_) asm volatile("v_add_f32_dpp %0, %1, %0 row_ror:8 row_mask:0xf bank_mask:0xf" : "+v"(f0[o_]) : "v"(f1[o_]))
#define WFO(o_) do { VADD(f0[o_], acc[o_][0], acc[o_][2]); VADD(f1[o_], acc[o_][1], acc[o_][3]); } while (0)
#define WWR(o_) *(LAS float*)(xw + L * 4096 + (o_) * 128) = f0[o_]
            u32 th, per, so, erl = early;
            auto next = [&](auto ic_) __attribute__((always_inline)) {
                constexpr int i = decltype(ic_)::value;
                if constexpr (i == 0) { so = ((u32)sn[M] + dry) << 17; PINS(so); }
                if constexpr (i == 1) { so = (so & 0x60000u) + M * (MB / 2); PINS(so); }
                if constexpr (i == 2) { so = sn[M] + 1 < NSEG ? so : OOB; PINS(so); }
                if constexpr (i == 3) { early = sn[M] + 1 < NSEG ? PSENT + dry : 0u; PIN(early); }
                if constexpr (i == 4) { vo = l16 + so; PIN(vo); }
                if constexpr (i == 5 && L) { fin = (u32)(sn[0] + sn[1] + 2); PINS(fin); }
                if constexpr (i == 6 && L) { fin = (fin | uu(left)) >> 7; PIN(fin); }
                if constexpr (i == 6 && !L) { cnt += 1u; PIN(cnt); left = cnt >> 9; PIN(left); }
                if constexpr (i == 7) { PIN(hv); th = rdl(hv, 0); PINS(th); }
                if constexpr (i == 8) { per = rdl(hv, 1); PINS(per); so = th - t; PINS(so); }
                if constexpr (i == 9) { per = (int)so < 20 ? per : 0u; PINS(per); }
                if constexpr (i == 10) { th += per; PINS(th); if constexpr (L) { per = rdl(hv, 3); PINS(per); } }
                if constexpr (i == 11) { tp = t; t = lead ? tn : th; PINS(t); }
                if constexpr (i == 12) { if constexpr (L) { fin |= per; PIN(fin); } tn = nt; }
            };
            auto slot = [&](auto ic_) __attribute__((always_inline)) {
                constexpr int i = decltype(ic_)::value;
                constexpr int NL = WO_NL, I0 = 40 - 5 * NL, pc = i < I0 ? i / 5 : 8 - NL + (i - I0) % NL, oc = i < I0 ? i % 5 : (i - I0) / NL;
                if constexpr (oc == 0) asm volatile("s_waitcnt vmcnt(%1)" : "+v"(d[pc]) : "i"(10 - pc));
                if constexpr (pc * 5 + oc < 32) SMF_A(acc[oc], d[pc], wo[pc][oc]); else SMF_V(acc[oc], d[pc], wo[pc][oc]);
                SCHED();
                if constexpr (i == 0) WT(2);
                if constexpr (i >= 5 && i < 13 && i != 9) next(IC<i - 5 - (i > 9)>{});
                if constexpr (i == 20) WT(4);
                constexpr int e = i - (I0 + NL + 1), o = e < 0 ? 0 : e / 3;
                if constexpr (e >= 0 && e % 3 == 0) { CADD(f0[o], acc[o][0], acc[o][2]); if constexpr (o > 0) WAD(o - 1); }
                if constexpr (e >= 0 && e % 3 == 1) { if constexpr (o > 0) { WWR(o - 1); SCHED(); } CADD(f1[o], acc[o][1], acc[o][3]); }
                if constexpr (e >= 0 && e % 3 == 2) {
                    if constexpr (o == 0) asm volatile("v_cmp_u_f32 %0, %1, %2" : "=s"(bad) : "v"(f0[0]), "v"(f1[0]));
                    else LGK(15);
                    WSW(o);
                }
                SCHED();
            };
            asm volatile(".rept %0\n\ts_nop 0\n\t.endr" :: "i"(AL_W));
            static_for<0, 2>(slot);
            if (__builtin_expect(__builtin_amdgcn_ballot_w64(d[0].x == erl) != 0ull, 0)) {
                asm volatile("s_waitcnt vmcnt(3)" :: "v"(d[1]), "v"(d[2]), "v"(d[3]), "v"(d[4]), "v"(d[5]), "v"(d[6]), "v"(d[7]) : "memory");
                SCHED();
                *(LAS u32*)(sw + L * 4096) = 1u;
                static_for<0, 7>(next);
            } else {
            static_for<2, 40>(slot);
            WT(5);
            static_assert(WO_NL == 3, "tail");
            if (__builtin_expect(bad != 0ull, 0)) {
                bool miss = false;
#pragma unroll
                for (int i = 0; i < 8; ++i) miss |= d[i].x == PSENT;
                if (all_(!miss)) {
#pragma unroll
                    for (int i = 0; i < 8; ++i) miss |= d[i].y == PSENT || d[i].z == PSENT || d[i].w == PSENT;
                }
                *(LAS u32*)(sw + L * 4096) = all_(!miss) ? 0u : 1u;
            }
            asm volatile("v_permlane32_swap_b32 %0, %1\n\ts_nop 1\n\tv_add_f32_dpp %0, %1, %0 row_ror:8 row_mask:0xf bank_mask:0xf\n\tv_add_f32 %2, %4, %6\n\tv_add_f32 %3, %5, %7"
                         : "+v"(f0[3]), "+v"(f1[3]), "=&v"(f0[4]), "=&v"(f1[4]) : "v"(acc[4][0]), "v"(acc[4][1]), "v"(acc[4][2]), "v"(acc[4][3]));
            WWR(3); SCHED();
            asm volatile("s_nop 0\n\tv_permlane32_swap_b32 %0, %1\n\ts_nop 1\n\tv_add_f32_dpp %0, %1, %0 row_ror:8 row_mask:0xf bank_mask:0xf" : "+v"(f0[4]), "+v"(f1[4]));
            WWR(4);
            }
            WT(6);
            f32x2 a, b, c, e; u32 mq; v4u sv;
            asm volatile("s_waitcnt lgkmcnt(0)\n\ts_barrier\n\tds_read_b128 %4, %6 offset:%7+2560\n\t"
                         "ds_read_b64 %0, %5 offset:%7\n\tds_read_b64 %1, %5 offset:%7+640\n\tds_read_b64 %2, %5 offset:%7+1280\n\tds_read_b64 %3, %5 offset:%7+1920"
                         : "=&v"(a), "=&v"(b), "=&v"(c), "=&v"(e), "=&v"(sv) : "v"(xr), "v"(zero), "i"(L * 4096) : "memory");
            asm volatile("s_waitcnt lgkmcnt(4)\n\tv_or_b32 %4, %6, %7\n\tv_or3_b32 %4, %4, %8, %9\n\tv_lshl_or_b32 %5, %4, 31, %10\n\t"
                         "s_waitcnt lgkmcnt(2)\n\tv_pk_add_f32 %0, %0, %1\n\ts_waitcnt lgkmcnt(0)\n\tv_pk_add_f32 %2, %2, %3\n\ts_nop 0\n\tv_pk_add_f32 %0, %0, %2"
                         : "+v"(a), "+v"(b), "+v"(c), "+v"(e), "=&v"(pst), "=&v"(mq) : "v"(sv.x), "v"(sv.y), "v"(sv.z), "v"(sv.w), "v"(mp) : "memory");
            st4s(rs, mq, 0, pkv(a));
            SCHED();
            asm volatile("s_memrealtime %0" : "=s"(clk));
            VMW(3); SCHED();
            static_for<7, 13>(next);
            WT(7);
        }
    };
    do {
        turn(IC<0>{});
        turn(IC<1>{});
        if (__builtin_expect(dry != 0u, 0)) start();
    } while (uu(fin) == 0u);
    if (sn[0] + 1 < NSEG || sn[1] + 1 < NSEG) st4s(rs, tid == 0 ? foff(F_ERR) : OOB, 0, 1u);
    if (w == 0) LST(1900 + j, rt32());
}

__global__ void __launch_bounds__(256, 1) k_main(Params p) {
    lptr lds = (lptr)(u32)16 - 16;
    if (blockIdx.x == 0) LST(256, rt32());
    unsigned long long pc; int len;
    asm volatile("s_getpc_b64 %0\n.Lk_main_here:\n\ts_mov_b32 %1, .Lk_end_here-.Lk_main_here" : "=s"(pc), "=s"(len));
    const rsrc_t rs = mk_rsrc(p.ws, W_END);
    const v2u e0 = ld8s(rs, threadIdx.x == 0 ? foff(F_EPOCH) : OOB, 0);
    const v4u code = __builtin_amdgcn_raw_buffer_load_b128(mk_rsrc((const void*)(pc & ~15ull), (u32)max(len, 0)), (((blockIdx.x >> 3) & 31u) * 256u + threadIdx.x) * 16u, 0, 0);
    CFENCE();
    auto conv_id = [](int role) {
        const int x = role >> 5, i = role & 31, no = x < 5 ? 5 : 10, og = x < 5 ? x - 3 : 2 * (x - 4) + (i >= 5), r = (x - 3) * 5 + i - no, q = r / 3;
        return x < 3 || i >= no + 5 || r >= 24 ? NCV : i < no ? ((og & 1) ? (og >> 1) * 5 + i % 5 : NCV) : r % 3 == 0 ? 20 + (q & 1) * 20 + (q >> 1) : 16 + (r % 3) * 8 + q;
    };
    int role = (int)(((u32)__builtin_amdgcn_s_getreg(20 | (3 << 11)) & 7u) * 32u + (blockIdx.x >> 3));
    Early e;
    v4u rv;
#pragma unroll 1
    for (int n = 0;; ++n) {
        conv_early(p, rs, conv_id(role), e);
        if (n) break;
        CFENCE();
        if (threadIdx.x == 0) {
            GAS unsigned long long* slots = (GAS unsigned long long*)(p.ws + W_CTRL + F_SLOT * 128);
            const unsigned long long tag = (unsigned long long)e0.y << 32 | (e0.x + 1u);
            u32 mine = (u32)role;
#pragma unroll 1
            for (int k = 0; k < 256 && __hip_atomic_exchange(slots + mine * 16, tag, __ATOMIC_RELAXED, __HIP_MEMORY_SCOPE_AGENT) == tag; ++k) mine = (mine + 1u) & 255u;
            lds_w16(lds, (v4u){mine, e0.x, e0.y, 0u});
        }
        BAR();
        rv = lds_r16(lds);
        BAR();
        if (uni((int)rv.x) == role) break;
        role = uni((int)rv.x);
    }
    const int x = role >> 5, i = role & 31, w = uni(threadIdx.x >> 6), no = x < 5 ? 5 : 10, nr = x < 7 ? 5 : 4, j = conv_id(role);
    const v2u ep = {uu(rv.y), uu(rv.z)};
    asm volatile("" :: "v"(code));
    if (x < 2) { role_head(p, lds, i >> 4, x * 16 + (i & 15), ep); __builtin_amdgcn_endpgm(); }
    if (x == 2) { role_wo(p, lds, i, ep); __builtin_amdgcn_endpgm(); }
    if (i >= no + nr) { role_qkv(p, lds, (x < 5 ? (x - 3) * 22 : 44 + (x - 5) * 17) + i - no - nr, ep); __builtin_amdgcn_endpgm(); }
    const bool cv = j >= NPRE || (j >= 20 && j < 24);
    if (j < NCV) conv_stage(p, rs, lds, j, (j & 3) * 2 + (j >= NPRE), cv ? 40 : 0, ep, e);
    if (i >= no) {
        if (!cv) role_row(p, lds, (j - 24) * 4 + w, ep);
        __builtin_amdgcn_endpgm();
    }
    const int og = x < 5 ? x - 3 : 2 * (x - 4) + (i >= 5);
    role_out(p, lds, og & 1, og >> 1, i % 5, ep);
}
__global__ void k_end() { asm volatile(".Lk_end_here:"); }

extern "C" void kernel_launch(void* const* d_in, const int* in_sizes, int n_in, void* d_out, int out_size, void* d_ws, size_t ws_size,
                              hipStream_t stream) {
    Params p;
    p.x = (const u16*)d_in[0]; p.mem0 = (const u16*)d_in[1]; p.Wq = (const u16*)d_in[2]; p.Wk = (const u16*)d_in[3];
    p.Wv = (const u16*)d_in[4]; p.Wo = (const u16*)d_in[5];
    p.bk = (const u16*)d_in[8]; p.bv = (const u16*)d_in[9]; p.shift = (const u16*)d_in[10]; p.ssmk = (const u16*)d_in[11]; p.ssmD = (const u16*)d_in[12];
    p.out = (u16*)d_out;
    p.ws = (unsigned char*)(((size_t)d_ws + 2 * MB - 1) & ~(size_t)(2 * MB - 1));
    (void)hipFuncSetAttribute((const void*)k_main, hipFuncAttributeMaxDynamicSharedMemorySize, LDS_BYTES);
    hipLaunchKernelGGL(k_main, dim3(256), dim3(256), LDS_BYTES, stream, p);
}
```

```cpp
#include <hip/hip_runtime.h>
#include <hip/hip_bf16.h>

typedef unsigned short u16;
typedef unsigned int u32;
typedef u32 v2u __attribute__((ext_vector_type(2)));
typedef u32 v4u __attribute__((ext_vector_type(4)));
typedef u32 v8u __attribute__((ext_vector_type(8)));
typedef float f32x2 __attribute__((ext_vector_type(2)));
typedef float f32x4 __attribute__((ext_vector_type(4)));
typedef float f32x16 __attribute__((ext_vector_type(16)));
typedef __bf16 bf16x2 __attribute__((ext_vector_type(2)));
typedef __bf16 bf16x8 __attribute__((ext_vector_type(8)));
typedef short s16x4 __attribute__((ext_vector_type(4)));
#define LAS __attribute__((address_space(3)))
#define GAS __attribute__((address_space(1)))
typedef LAS unsigned char* lptr;
#define DEV __device__ __forceinline__

constexpr int SEQ = 2048, DM = 640, NH = 32, DH = 128, NSEG = 64, NQ = NH * DH;

constexpr u32 MB = 1u << 20;
constexpr u32 W_ORING = 0;
constexpr u32 W_MRING = MB;
constexpr u32 W_PROBE = 2 * MB - 65536;
constexpr u32 W_CTRL = 4 * MB;
constexpr u32 CTRL_BYTES = 128 * 1024;
constexpr u32 ACT = SEQ * DM * 2;
constexpr u32 W_H1 = 5 * MB, W_Y = W_H1 + ACT, W_H2 = W_Y + ACT, W_X2 = W_H2 + ACT;
constexpr u32 W_KQ = 16 * MB;
constexpr u32 W_VO = W_KQ + 64 * MB;
constexpr u32 W_P = W_VO + 64 * MB;
constexpr u32 P_ITEM = 32 * 4 * DM * 4;
constexpr u32 W_KVT = W_P + 2 * NSEG * P_ITEM;
constexpr u32 W_SP = W_KVT + ACT;
constexpr u32 W_END = W_KVT + 3 * MB;
static_assert((size_t)W_END + 2 * MB <= 256ull * MB, "workspace");
constexpr u32 F_ERR = 0, F_QKV = 16, F_O = F_QKV + 192, F_P = F_O + 64, F_H2 = F_P + 64, F_CLR = F_H2 + 32,
              F_EPOCH = F_CLR + 32, F_SLOT = F_EPOCH + 1, F_PRE = F_SLOT + 256, F_N = F_PRE + 192;
constexpr int NCV = 44, NPRE = 40;
static_assert(F_N * 128 <= CTRL_BYTES, "ctrl");
DEV u32 foff(u32 idx) { return W_CTRL + idx * 128u; }

constexpr u32 OOB = 0x80000000u;
constexpr u32 PSENT = 0x7F817F81u;
constexpr int SPIN_CAP = 40000;
constexpr int LDS_BYTES = 160 * 1024;

#define LS3(cond, slot, idx) do {} while (0)
#define LST(idx, v) do {} while (0)
#define LS2(cond, idx) do {} while (0)
struct Params {
    const u16 *x, *mem0, *Wq, *Wk, *Wv, *Wo, *bk, *bv, *shift, *ssmk, *ssmD;
    u16* out;
    unsigned char* ws;
};

typedef __amdgpu_buffer_rsrc_t rsrc_t;
DEV rsrc_t mk_rsrc(const void* p, u32 bytes) { return __builtin_amdgcn_make_buffer_rsrc((void*)p, 0, bytes, 0x00020000); }
DEV v4u ld16s(rsrc_t r, u32 off, u32 so) { return __builtin_amdgcn_raw_buffer_load_b128(r, off, so, 16); }
DEV v2u ld8s(rsrc_t r, u32 off, u32 so) { return __builtin_amdgcn_raw_buffer_load_b64(r, off, so, 16); }
DEV v4u ldin16(rsrc_t r, u32 off, u32 so) { return __builtin_amdgcn_raw_buffer_load_b128(r, off, so, 2); }
DEV v2u ldin8(rsrc_t r, u32 off, u32 so) { return __builtin_amdgcn_raw_buffer_load_b64(r, off, so, 2); }
DEV u32 ld4(rsrc_t r, u32 off) { return __builtin_amdgcn_raw_buffer_load_b32(r, off, 0, 16); }
DEV void st16(rsrc_t r, u32 off, v4u v) { __builtin_amdgcn_raw_buffer_store_b128(v, r, off, 0, 16); }
DEV void st8s(rsrc_t r, u32 off, u32 so, v2u v) { __builtin_amdgcn_raw_buffer_store_b64(v, r, off, so, 16); }
DEV void st4s(rsrc_t r, u32 off, u32 so, u32 v) { __builtin_amdgcn_raw_buffer_store_b32(v, r, off, so, 16); }
#define DRAIN() asm volatile("s_waitcnt vmcnt(0)" ::: "memory")
#define VMW(n) asm volatile("s_waitcnt vmcnt(%0)" :: "i"(n) : "memory")
#define BAR() asm volatile("s_waitcnt lgkmcnt(0)\n\ts_barrier" ::: "memory")
#define SCHED() __builtin_amdgcn_sched_barrier(0)
#define CFENCE() asm volatile("" ::: "memory")
#define NOPS(n) asm volatile("s_nop %0" :: "i"(n))
#define PIN(x) asm volatile("" : "+v"(x))
#define PINS(x) asm volatile("" : "+s"(x))

template <int V> struct IC { static constexpr int value = V; };
template <int I, int N, class F> DEV void static_for(F&& f) { if constexpr (I < N) { f(IC<I>{}); static_for<I + 1, N>(f); } }
#define SFOR(var, n) static_for<0, n>([&](auto ic_) __attribute__((always_inline)) { constexpr int var = decltype(ic_)::value;
#define SEND });

DEV u32 pk(float a, float b) { bf16x2 v = {(__bf16)a, (__bf16)b}; return __builtin_bit_cast(u32, v); }
DEV float lo_f(u32 u) { return __builtin_bit_cast(float, u << 16); }
DEV float hi_f(u32 u) { return __builtin_bit_cast(float, u & 0xffff0000u); }
DEV f32x2 up2(u32 u) { return (f32x2){lo_f(u), hi_f(u)}; }
DEV u32 pk2(f32x2 v) { return pk(v.x, v.y); }
DEV float rbf(float a) { return (float)(__bf16)a; }
DEV v4u lds_r16(lptr p) { return *(LAS v4u*)p; }
DEV void lds_w16(lptr p, v4u v) { *(LAS v4u*)p = v; }
DEV v2u lds_tr(lptr p) { return __builtin_bit_cast(v2u, __builtin_amdgcn_ds_read_tr16_b64_v4i16((LAS s16x4*)p)); }
DEV v4u tr2(lptr p0, lptr p1) { const v2u a = lds_tr(p0), b = lds_tr(p1); return (v4u){a.x, a.y, b.x, b.y}; }
DEV v8u cat8(v4u a, v4u b) { return __builtin_shufflevector(a, b, 0, 1, 2, 3, 4, 5, 6, 7); }
DEV f32x4 mfma16(v4u a, v4u b, f32x4 c) { return __builtin_amdgcn_mfma_f32_16x16x32_bf16(__builtin_bit_cast(bf16x8, a), __builtin_bit_cast(bf16x8, b), c, 0, 0, 0); }
DEV f32x16 mfma32(v4u a, v4u b, f32x16 c) { return __builtin_amdgcn_mfma_f32_32x32x16_bf16(__builtin_bit_cast(bf16x8, a), __builtin_bit_cast(bf16x8, b), c, 0, 0, 0); }

DEV float f_(u32 u) { return __builtin_bit_cast(float, u); }
DEV u32 u_(float f) { return __builtin_bit_cast(u32, f); }
template <int CTRL> DEV float dpp(float v) { return f_((u32)__builtin_amdgcn_update_dpp(0, (int)u_(v), CTRL, 0xf, 0xf, false)); }
DEV float max16(float v) { auto r = __builtin_amdgcn_permlane16_swap(u_(v), u_(v), false, false); return fmaxf(f_(r[0]), f_(r[1])); }
DEV float max32(float v) { auto r = __builtin_amdgcn_permlane32_swap(u_(v), u_(v), false, false); return fmaxf(f_(r[0]), f_(r[1])); }
DEV float add16(float v) { auto r = __builtin_amdgcn_permlane16_swap(u_(v), u_(v), false, false); return f_(r[0]) + f_(r[1]); }
DEV float add32(float v) { auto r = __builtin_amdgcn_permlane32_swap(u_(v), u_(v), false, false); return f_(r[0]) + f_(r[1]); }
DEV float wave_sum(float v) {
    v += dpp<0xB1>(v);
    v += dpp<0x4E>(v);
    v += dpp<0x124>(v);
    v += dpp<0x128>(v);
    return add32(add16(v));
}
DEV bool all_(bool c) { return __builtin_amdgcn_ballot_w64(!c) == 0ull; }
DEV int uni(int v) { return __builtin_amdgcn_readfirstlane(v); }
DEV u32 uu(u32 v) { return (u32)__builtin_amdgcn_readfirstlane((int)v); }
DEV u32 rdl(u32 v, int l) { return (u32)__builtin_amdgcn_readlane((int)v, l); }
DEV u32 rt32() { return (u32)__builtin_amdgcn_s_memrealtime(); }

#define WSL 2
DEV void wait_flags(rsrc_t rs, u32 off, u32 want, bool& dead) {
    if (dead) return;
#pragma unroll 1
    for (int n = 0;; ++n) {
        CFENCE();
        const u32 v = ld4(rs, off);
        if (all_(off >= OOB || v >= want)) break;
        __builtin_amdgcn_s_sleep(WSL);
        if ((n & 63) == 63) { if (uni((int)ld4(rs, foff(F_ERR))) != 0) { dead = true; break; } }
        if (n > SPIN_CAP) { st4s(rs, foff(F_ERR), 0, 1u); dead = true; break; }
    }
    CFENCE();
}

DEV void pre_post(rsrc_t rs, int idx, u32 stage, v2u ep) { st8s(rs, threadIdx.x == 0 ? foff(F_PRE + idx) : OOB, 0, (v2u){ep.x + stage, ep.y}); }
DEV void pre_poll(rsrc_t rs, int first, int n, u32 stage, v2u ep) {
    const int lane = threadIdx.x & 63;
#pragma unroll 1
    for (int it = 0; it < SPIN_CAP; ++it) {
        CFENCE();
        const v2u a = ld8s(rs, foff(F_PRE + first + min(lane, n - 1)), 0), b = ld8s(rs, foff(F_PRE + first + min(lane + 64, n - 1)), 0);
        if (all_(a.y == ep.y && b.y == ep.y && max(a.x - ep.x - stage, b.x - ep.x - stage) < 8u - stage)) break;
        __builtin_amdgcn_s_sleep(2);
    }
    CFENCE();
}
DEV void pre_wait(rsrc_t rs, int first, int n, u32 stage, v2u ep) {
    if (uni(threadIdx.x >> 6) == 0) pre_poll(rs, first, n, stage, ep);
    BAR();
}
DEV u32 pol_wait(rsrc_t rs, v2u ep) {
    const int lane = threadIdx.x & 63;
    v4u a;
#pragma unroll 1
    for (int it = 0; it < SPIN_CAP; ++it) {
        CFENCE();
        a = ld16s(rs, foff(F_PRE + (lane < NPRE || lane >= 48 ? lane : 0)), 0);
        if (all_(a.y == ep.y && a.x - ep.x - 1u < 7u)) break;
        __builtin_amdgcn_s_sleep(2);
    }
    CFENCE();
    return uu(wave_sum(lane >= 48 ? (float)(int)a.z : 0.f) >= 0.f ? 1u : 0u);
}
#define PROBE_T 1000
#define NPROBE 32
DEV u32 pol_probe(const Params& p, rsrc_t rs, int id) {
    const int lane = threadIdx.x & 63;
    const u32 off0 = W_PROBE + (u32)(id >> 2) * 16384u + (u32)(id & 3) * 128u;
    const u32 tend = rt32() + PROBE_T;
    st4s(rs, lane < 32 ? off0 + (u32)lane * 512 : OOB, 0, 0u); DRAIN();
    int calm = 0;
    int d[2] = {1 << 16, 1 << 16};
#pragma unroll 1
    for (int q = 0; q < NPROBE && ((q & 1) || (calm < 6 && (int)(rt32() - tend) < 0)); ++q) {
        const u32 half = (u32)(q ^ (q >> 1) ^ id) & 1u;
        const unsigned long long t0 = __builtin_amdgcn_s_memtime();
        const u32 v = ld4(rs, lane == 0 ? off0 + half * 8192 + (u32)(q >> 1) * 512 : OOB);
        asm volatile("s_waitcnt vmcnt(0)" :: "v"(v) : "memory");
        const int dt = (int)(__builtin_amdgcn_s_memtime() - t0);
        if (half) d[1] = min(d[1], dt); else d[0] = min(d[0], dt);
        calm += dt < 1100;
    }
    const u32 xcc = (u32)__builtin_amdgcn_s_getreg(20 | (3 << 11)) & 0xfu;
    return (u32)min(max((d[0] - d[1]) * ((xcc & 4u) ? -1 : 1), -250), 250);
}
DEV f32x2 ln_stats(const f32x2 (&x)[5]) {
    const float c = f_(uu(u_(x[0].x)));
    f32x2 d[5];
#pragma unroll
    for (int i = 0; i < 5; ++i) d[i] = x[i] - c;
    const f32x2 s2 = (d[0] + d[1]) + (d[2] + d[3]) + d[4], q2 = (d[0] * d[0] + d[1] * d[1]) + (d[2] * d[2] + d[3] * d[3] + d[4] * d[4]);
    float a = s2.x + s2.y, b = q2.x + q2.y, a2, b2;
#define LN_DPP(m) "v_add_f32_dpp %0, %0, %0 " m " row_mask:0xf bank_mask:0xf\n\tv_add_f32_dpp %1, %1, %1 " m " row_mask:0xf bank_mask:0xf\n\ts_nop 0\n\t"
    asm("s_nop 1\n\t" LN_DPP("quad_perm:[1,0,3,2]") LN_DPP("quad_perm:[2,3,0,1]") LN_DPP("row_ror:4")
        "v_add_f32_dpp %0, %0, %0 row_ror:8 row_mask:0xf bank_mask:0xf\n\tv_add_f32_dpp %1, %1, %1 row_ror:8 row_mask:0xf bank_mask:0xf\n\ts_nop 1\n\t"
        "v_permlane16_swap_b32 %0, %1\n\tv_add_f32 %0, %0, %1\n\tv_mov_b32 %1, %0\n\ts_nop 1\n\tv_permlane32_swap_b32 %0, %1\n\tv_add_f32 %0, %0, %1\n\ts_nop 0\n\t"
        "v_readlane_b32 %2, %0, 0\n\tv_readlane_b32 %3, %0, 16\n\ts_nop 1"
        : "+v"(a), "+v"(b), "=s"(a2), "=s"(b2));
    const float m = a2 * (1.0f / DM), var = fmaxf(b2 * (1.0f / DM) - m * m, 0.f);
    const float sd = rbf(__builtin_amdgcn_sqrtf(rbf(rbf(var) + 1e-5f)));
    return (f32x2){rbf(c + m), __builtin_amdgcn_rcpf(sd)};
}
DEV u32 pkv(f32x2 v) { u32 d; asm("v_cvt_pk_bf16_f32 %0, %1, %2" : "=v"(d) : "v"(v.x), "v"(v.y)); return d; }
DEV void store_h(rsrc_t rs, u32 base, int row, const u32 (&hp)[5]) {
    const u32 lane = threadIdx.x & 63;
    const u32 sw = (u32)(row & 15) * 16u, o8 = (lane >> 5) * 8192u + (((lane & 31u) * 8u) ^ sw), hb = base + (u32)(row >> 5) * (32 * DM * 2) + (u32)(row & 31) * 256u;
    st8s(rs, o8, hb, (v2u){hp[0], hp[1]});
    st8s(rs, o8, hb + 16384, (v2u){hp[2], hp[3]});
    st4s(rs, (lane * 4) ^ sw, hb + 32768, hp[4]);
}

#define DMA16(ldsb, vo, so) asm volatile("s_mov_b32 m0, %0\n\ts_nop 0\n\tbuffer_load_dwordx4 %1, %2, %3 offen sc1 lds" :: "s"(ldsb), "v"(vo), "s"(srd), "s"(so) : "memory")
#define DMA16I(ldsb, lo, vo, so, go) do { u32 so_; asm volatile("s_add_u32 m0, %1, %5\n\ts_add_u32 %0, %4, %6\n\tbuffer_load_dwordx4 %2, %3, %0 offen sc1 lds" \
                         : "=&s"(so_) : "s"(ldsb), "v"(vo), "s"(srd), "s"(so), "i"(lo), "i"(go) : "memory", "scc"); } while (0)
#define DMA32(ldsb, vo, so) asm volatile("s_mov_b32 m0, %0\n\ts_nop 0\n\tbuffer_load_dwordx4 %1, %2, %3 offen sc1 lds\n\tbuffer_load_dwordx4 %1, %2, %3 offen offset:1024 sc1 lds" \
                         :: "s"(ldsb), "v"(vo), "s"(srd), "s"(so) : "memory")
#define DMAIN(sr, ldsb, vo, so) asm volatile("s_mov_b32 m0, %0\n\ts_nop 0\n\tbuffer_load_dwordx4 %1, %2, %3 offen lds" :: "s"(ldsb), "v"(vo), "s"(sr), "s"(so) : "memory")
DEV v4u mk_srd(const void* q, u32 bytes) { return (v4u){uu((u32)(size_t)q), uu((u32)((size_t)q >> 32) & 0xffffu), bytes, 0x00020000u}; }
#define DMANT(sr, ldsb, vo, so) asm volatile("s_mov_b32 m0, %0\n\ts_nop 0\n\tbuffer_load_dwordx4 %1, %2, %3 offen nt lds" :: "s"(ldsb), "v"(vo), "s"(sr), "s"(so) : "memory")
#define AHEAD(sr, vo, so) asm volatile("buffer_load_dwordx4 %0, %1, %2, %3 offen" : "+v"(junk) : "v"(vo), "s"(sr), "s"(so) : "memory")
#define MK_SRD() const v4u srd = mk_srd(p.ws, W_END)

constexpr int CV_W = 40960;
template <bool B> struct CvL {
    static constexpr int NF = B ? 5 : 3, NK = B ? 4 : 2, KP = 544 + NK * 256, CH = NF * 1024 + 4 * KP;
};
constexpr int CV_Y = 4 * CvL<false>::CH, CV_YP = 272;
static_assert(CV_Y + 32 * CV_YP <= CV_W && 2 * CvL<true>::CH <= CV_Y, "conv lds");
DEV int cv_chan(int u, int i) { return 16 * (u % 40) + 4 * i + u / 40; }
template <bool B> DEV void cv_filt(const Params& p, u32 dst, int d, int i) {
    const u32 lane = threadIdx.x & 63;
    const v4u sf = mk_srd(p.ssmk + d * SEQ, SEQ * 2);
    if (i == 0) DMANT(sf, dst, lane < 4u ? OOB : lane * 16u - 64u, 0);
    else DMANT(sf, uu(dst + i * 1024), i == CvL<B>::NF - 1 && lane >= 4u ? OOB : lane * 16u, i * 1024 - 64);
}
DEV v4u cv_kvld(rsrc_t rs, int d, int i) { return ld16s(rs, (threadIdx.x & 63) * 16u, uu(W_KVT + (u32)d * (SEQ * 2) + i * 1024)); }
template <bool B> DEV void cv_kvput(lptr F, const v4u* k) {
    const int lane = threadIdx.x & 63;
#pragma unroll
    for (int i = 0; i < CvL<B>::NK; ++i) lds_w16(F + CvL<B>::NF * 1024 + (lane & 3) * CvL<B>::KP + 512 + i * 256 + (lane >> 2) * 16, k[i]);
}
template <bool B> DEV void cv_zero(lptr F) {
    const int lane = threadIdx.x & 63;
#pragma unroll
    for (int i = 0; i < 4; ++i) lds_w16(F + (lane >> 5) * CvL<B>::CH + CvL<B>::NF * 1024 + i * CvL<B>::KP + (lane & 31) * 16, (v4u){0u, 0u, 0u, 0u});
}
#define M32V(d, a, b) asm volatile("v_mfma_f32_32x32x16_bf16 %0, %1, %2, %0" : "+v"(d) : "v"(a), "v"(b))
template <bool B, class G> DEV void cv_run(const Params& p, rsrc_t rs, lptr F, int d, u32 D, f32x4 sp, float (&y)[16], G&& gap) {
    constexpr int KP = CvL<B>::KP, FO = B ? 2048 : 0;
    const int lane = threadIdx.x & 63, n = lane & 31, hh = lane >> 5, c = lane & 3;
    lptr fa = F + 64 + (32 * hh + 16 * (c & 1) + 8 * ((lane >> 4) & 1) + 4 * (c >> 1) - (lane & 12)) * 2;
    lptr kl = F + CvL<B>::NF * 1024 + (32 + n - hh) * 16;
    v4u dg;
#pragma unroll
    for (int q = 0; q < 4; ++q) { const int k = (lane & 35) ? -1 : (n & 4) + (n >> 3); dg[q] = k == 2 * q ? D : k == 2 * q + 1 ? D << 16 : 0u; }
    v4u l[4], h[4]; v2u fc[4], fh[4];
    f32x16 acc[4];
#pragma unroll
    for (int j = 0; j < 4; ++j) { l[j] = lds_r16(kl + j * KP); if constexpr (B) h[j] = lds_r16(kl + j * KP + 512); }
#pragma unroll
    for (int j = 0; j < 2; ++j) { fc[j] = lds_tr(fa + FO - 32 * j); if constexpr (B) fh[j] = lds_tr(fa - 32 * j); }
    asm volatile("s_nop 1" : "+v"(dg));
#pragma unroll
    for (int j = 0; j < 4; ++j) asm volatile("v_mfma_f32_32x32x16_bf16 %0, %1, %2, 0" : "=&v"(acc[j]) : "v"(dg), "v"(B ? h[j] : l[j]));
    constexpr int U = B ? 2 : 4;
    kl -= 32 * U; fa -= 32;
    auto body = [&](bool first) __attribute__((always_inline)) {
#pragma unroll
        for (int z = 0; z < U; ++z) {
            const int c = 2 * (z & 1);
            const v4u f = __builtin_shufflevector(fc[c], fc[c + 1], 0, 1, 2, 3), g = __builtin_shufflevector(fh[c], fh[c + 1], 0, 1, 2, 3);
#pragma unroll
            for (int j = 0; j < 4; ++j) {
                M32V(acc[j], f, l[j]); l[j] = lds_r16(kl + j * KP + 32 * (U - z - 1));
                if (j < 2) fc[(c ^ 2) + j] = lds_tr(fa + FO + (z + 1) * 128 + 32 - 32 * j);
                if (first && j > 0) gap(5 * z + j - 1);
                SCHED();
            }
            if constexpr (B) {
#pragma unroll
                for (int j = 0; j < 4; ++j) {
                    M32V(acc[j], g, h[j]); h[j] = lds_r16(kl + j * KP + 512 + 32 * (U - z - 1));
                    if (j < 2) fh[(c ^ 2) + j] = lds_tr(fa + (z + 1) * 128 + 32 - 32 * j);
                    if (first && j > 1) gap(5 * z + j + 1);
                    SCHED();
                }
            }
        }
        kl -= 32 * U; fa += 128 * U;
    };
    if constexpr (B) body(true);
#pragma unroll 1
    for (int m = B ? 2 * U : 0; m < 32; m += 2 * U) body(false);
    asm volatile("s_nop 11" : "+v"(acc[0]), "+v"(acc[1]), "+v"(acc[2]), "+v"(acc[3]));
    const float o[3] = {acc[1][15] + acc[2][14] + acc[3][13], acc[2][15] + acc[3][14], acc[3][15]};
    if constexpr (!B) st16(rs, lane == 63 ? W_SP + (u32)d * 16u : OOB, (v4u){u_(o[0]), u_(o[1]), u_(o[2]), 0u});
#pragma unroll
    for (int i = 0; i < 16; ++i) {
        float v = acc[0][i];
#pragma unroll
        for (int j = 1; j < 4; ++j) if (j <= i) v += acc[j][i - j];
        if (i < 3) { const float e = f_((u32)__builtin_amdgcn_ds_bpermute((hh ? lane - 32 : lane + 31) * 4, (int)u_(o[i]))); v += lane == 0 ? sp[i] : e; }
        y[i] = v;
    }
}
DEV void cv_put(lptr F, int pr, const float (&y0)[16], const float (&y1)[16]) {
    const int lane = threadIdx.x & 63;
#pragma unroll
    for (int i = 0; i < 16; ++i) *(LAS u32*)(F + CV_Y + (lane & 31) * CV_YP + (lane >> 5) * 128 + i * 8 + pr * 4) = pk(y0[i], y1[i]);
}
DEV void cv_out(rsrc_t rs, lptr F, int u, int blk0) {
    const int lane = threadIdx.x & 63;
    lptr q = F + CV_Y + ((lane >> 2) & 3) * CV_YP + ((lane >> 4) * 4 + (lane & 3)) * 8;
    const u32 yo = W_Y + (u32)(((lane >> 2) * 8 + blk0) * DM + 4 * u + (lane & 3)) * 16u;
#pragma unroll
    for (int i = 0; i < 8; ++i) { lptr a = q + (i >> 1) * (8 * CV_YP) + (i & 1) * 128; st16(rs, yo + (u32)((i & 1) * 128 + (i >> 1)) * (DM * 16), tr2(a, a + 4 * CV_YP)); }
}
struct KvIn { v2u k[19], v[16]; v4u s[2]; };
template <class F> DEV void kvt_load(const Params& p, int tile, KvIn& in, F&& mid) {
    const int lane = threadIdx.x & 63;
    const rsrc_t rk = mk_rsrc(p.bk, ACT), rv = mk_rsrc(p.bv, ACT), rh = mk_rsrc(p.shift, DM * 8);
    const int row = (tile / 10) * 64 + (lane >> 4) * 16, col = (tile % 10) * 64 + (lane & 15) * 4;
    const u32 vo = (u32)(row * DM + col) * 2u, vp = row > 0 ? vo - 3 * (DM * 2) : OOB;
#pragma unroll
    for (int i = 0; i < 2; ++i) in.s[i] = ldin16(rh, (u32)col * 8u + i * 16, 0);
#pragma unroll
    for (int i = 0; i < 19; ++i) { if (i == 7) mid(); in.k[i] = i < 3 ? ldin8(rk, vp, i * (DM * 2)) : ldin8(rk, vo, (i - 3) * (DM * 2)); if (i >= 3) in.v[i - 3] = ldin8(rv, vo, (i - 3) * (DM * 2)); }
}
DEV void kvt_make(rsrc_t rs, lptr xt, int tile, const KvIn& in) {
    const int lane = threadIdx.x & 63;
#pragma unroll
    for (int c = 0; c < 2; ++c) {
        const v4u sv = in.s[c];
        const f32x2 sh[4] = {{lo_f(sv.x), lo_f(sv.z)}, {hi_f(sv.x), hi_f(sv.z)}, {lo_f(sv.y), lo_f(sv.w)}, {hi_f(sv.y), hi_f(sv.w)}};
        f32x2 kf[19];
#pragma unroll
        for (int i = 0; i < 19; ++i) kf[i] = up2(in.k[i][c]);
        u32 kv[16];
#pragma unroll
        for (int i = 0; i < 16; ++i) kv[i] = pk2(((kf[i] * sh[3] + kf[i + 1] * sh[2]) + (kf[i + 2] * sh[1] + kf[i + 3] * sh[0])) * up2(in.v[i][c]));
#pragma unroll
        for (int q = 0; q < 8; ++q) {
            const u32 sel = (q & 1) ? 0x07060302u : 0x05040100u; const u32* z = kv + (q >> 1);
            *(LAS v2u*)(xt + ((2 * c + (q & 1)) * 16 + (lane & 15)) * 144 + (lane >> 5) * 64 + (q >> 1) * 16 + ((lane >> 4) & 1) * 8) =
                (v2u){__builtin_amdgcn_perm(z[4], z[0], sel), __builtin_amdgcn_perm(z[12], z[8], sel)};
        }
    }
    const u32 o = W_KVT + (u32)(((tile % 10) * 64 + 4 * (lane >> 3)) * SEQ + (tile / 10) * 64 + (lane & 7) * 8) * 2u;
#pragma unroll
    for (int i = 0; i < 8; ++i) st16(rs, o + (u32)(32 * (i & 1) + (i >> 1)) * (SEQ * 2), lds_r16(xt + (8 * i + (lane >> 3)) * 144 + (lane & 7) * 16));
}
struct RowX { v2u a, b; u32 c; };
DEV void ln_row(rsrc_t rs, int row, const RowX& r) {
    const f32x2 x[5] = {up2(r.a.x), up2(r.a.y), up2(r.b.x), up2(r.b.y), up2(r.c)};
    const f32x2 st = ln_stats(x);
    u32 hp[5];
#pragma unroll
    for (int q = 0; q < 5; ++q) hp[q] = pk2(up2(pkv(x[q] - st.x)) * st.y);
    store_h(rs, W_H1, row, hp);
}
DEV v4u mem0_load(const Params& p, int j) {
    const u32 gt = (u32)(j * 256 + threadIdx.x) * 16u, n = ((gt >> 14) << 9) + ((gt & 8191u) >> 4);
    return ldin16(mk_rsrc(p.mem0, 8 * DM * 2), n < 640u ? ((2 * ((n >> 2) & 3) + (n & 1)) * DM + (n >> 4) * 16 + (n & 2) * 4) * 2u : OOB, 0);
}
DEV void pre_rings(rsrc_t rs, int j, v4u m) {
    const u32 gi = (u32)(j * 256 + threadIdx.x), gt = gi * 16u, R = NPRE * 4096, n = ((gt >> 14) << 9) + ((gt & 8191u) >> 4);
#pragma unroll
    for (int i = 0; i < 4; ++i) m[i] = n >= 640u ? PSENT : m[i] == PSENT ? 0x7FC17FC1u : m[i];
    st4s(rs, gi - 36u * 256u < F_CLR ? foff(gi - 36u * 256u) : OOB, 0, 0u);
#pragma unroll 1
    for (u32 o = gt; o < MB; o += R) st16(rs, W_ORING + o, (v4u){PSENT, PSENT, PSENT, PSENT});
    const u32 mo = gt < 4 * 32768 + 16384 ? W_MRING + gt : OOB;
    st16(rs, mo, m);
    st16(rs, mo + MB / 4, m);
    static_assert(NPRE * 4096 >= 4 * 32768 + 16384, "memory ring");
}
constexpr int LN0 = NPRE * 4;
struct Early { RowX r; v4u m; u32 vote; };
DEV void conv_early(const Params& p, rsrc_t rs, int j, Early& e) {
    const int lane = threadIdx.x & 63, w = uni(threadIdx.x >> 6), u = j * 4 + w;
    if (j < NPRE) {
        const rsrc_t rx = mk_rsrc(p.x, ACT);
        e.r.a = __builtin_amdgcn_raw_buffer_load_b64(rx, lane * 8u, uu(u * (DM * 2)), 0); e.r.b = __builtin_amdgcn_raw_buffer_load_b64(rx, lane * 8u + 512u, uu(u * (DM * 2)), 0);
        e.r.c = __builtin_amdgcn_raw_buffer_load_b32(rx, lane * 4u + 1024u, uu(u * (DM * 2)), 0);
        e.m = mem0_load(p, j);
    } else if (j < NCV && w > 0) e.vote = pol_probe(p, rs, u - NPRE * 4);
}
DEV void conv_stage(const Params& p, rsrc_t rs, lptr lds, int j, int g0, int ge, v2u ep, Early& e) {
    const int tid = threadIdx.x, lane = tid & 63, w = uni(tid >> 6), u = j * 4 + w;
    lptr F = lds + w * CV_W;
    const u32 fb = uu(w * CV_W);
    const rsrc_t rd = mk_rsrc(p.ssmD, DM * 2);
    float y0[16], y1[16];
    KvIn in;
    if (j < NPRE) {
        constexpr int CH = CvL<false>::CH;
        if (w == 1) LST(1500 + j, rt32());
        ln_row(rs, u, e.r);
        pre_rings(rs, j, e.m);
        if (w == 1) LST(1550 + j, rt32());
        CFENCE();
        kvt_load(p, u, in, [&] { VMW(13); BAR(); pre_post(rs, j, 1u, ep); if (w == 0) LST(1450 + j, rt32()); });
        if (j == 20) LST(260, rt32());
        const u32 D = __builtin_amdgcn_raw_buffer_load_b16(rd, (u32)cv_chan(u, lane & 3) * 2u, 0, 0);
#pragma unroll 1
        for (int i = 0; i < 4; ++i) { const int d = cv_chan(u, i); cv_filt<false>(p, fb + i * CH, d, 0); cv_filt<false>(p, fb + i * CH, d, 1); cv_filt<false>(p, fb + i * CH, d, 2); }
        kvt_make(rs, F + CV_Y, u, in);
        cv_zero<false>(F); cv_zero<false>(F + 2 * CH);
        DRAIN(); BAR();
        pre_post(rs, j, 2u, ep);
        if (j == 20) LST(262, rt32());
        pre_wait(rs, 0, NPRE, 2u, ep);
        if (j == 20) LST(281, rt32());
        {
            v4u k[8];
#pragma unroll
            for (int i = 0; i < 8; ++i) k[i] = cv_kvld(rs, cv_chan(u, i >> 1), i & 1);
#pragma unroll
            for (int i = 0; i < 4; ++i) cv_kvput<false>(F + i * CH, k + 2 * i);
        }
        if (j == 20) LST(286, rt32());
#pragma unroll 1
        for (int i = 0; i < 4; i += 2) {
            cv_run<false>(p, rs, F + i * CH, cv_chan(u, i), rdl(D, i), f32x4{}, y0, [](int) {});
            cv_run<false>(p, rs, F + (i + 1) * CH, cv_chan(u, i + 1), rdl(D, i + 1), f32x4{}, y1, [](int) {});
            cv_put(F, i >> 1, y0, y1);
        }
        if (j == 20) LST(287, rt32());
        cv_out(rs, F, u, 0);
        if (j == 20) LST(263, rt32());
    } else {
        st16(rs, lane == 0 ? foff(F_PRE + 48 + u - NPRE * 4) : OOB, (v4u){ep.x + 1u, ep.y, w > 0 ? e.vote : 0u, 0u});
#pragma unroll 1
        for (int t = u + 128; t < 320; t += 16) { kvt_load(p, t, in, [] {}); kvt_make(rs, F, t, in); }
    }
    DRAIN(); BAR();
    if (g0 < ge) {
        constexpr int CH = CvL<true>::CH;
        pre_post(rs, j, 3u, ep);
        cv_zero<true>(F);
        if (w == 0) { pre_poll(rs, 64, 32, 3u, ep); pre_poll(rs, 0, NCV, 3u, ep); }
        BAR();
        v4u k[4];
        auto fetch = [&](u32 dst, int d, int i) { if (i < 5) cv_filt<true>(p, dst, d, i); else if (i < 9) k[i - 5] = cv_kvld(rs, d, i - 5); };
#pragma unroll
        for (int i = 0; i < 9; ++i) fetch(fb, cv_chan(g0 * 4 + w, 0), i);
        const u32 ch = (u32)cv_chan(min((g0 + 8 * (lane >> 2)) * 4 + w, 159), lane & 3);
        u32 D = __builtin_amdgcn_raw_buffer_load_b16(rd, ch * 2u, 0, 0);
        v4u sp = ld16s(rs, W_SP + ch * 16u, 0);
        asm volatile("" : "+v"(D), "+v"(sp));
#pragma unroll 1
        for (int g = g0, n = 0; g < ge; g += 8) {
            const int un = g * 4 + w;
            if (g == 16) { LST(282, rt32()); LST(288, __builtin_amdgcn_s_memtime()); }
#pragma unroll 1
            for (int i = 0; i < 4; i += 2, n += 2) {
                const int d1 = (int)rdl(ch, n + 1), d2 = (int)rdl(ch, n + 2);
                cv_kvput<true>(F, k);
                cv_run<true>(p, rs, F, 0, rdl(D, n), (f32x4){f_(rdl(sp.x, n)), f_(rdl(sp.y, n)), f_(rdl(sp.z, n)), 0.f}, y0, [&](int q) { fetch(fb + CH, d1, q); });
                cv_kvput<true>(F + CH, k);
                cv_run<true>(p, rs, F + CH, 326, rdl(D, n + 1), (f32x4){f_(rdl(sp.x, n + 1)), f_(rdl(sp.y, n + 1)), f_(rdl(sp.z, n + 1)), 0.f}, y1, [&](int q) { fetch(fb, d2, q); });
                cv_put(F, i >> 1, y0, y1);
            }
            cv_out(rs, F, un, 4);
            if (g == 16) { LST(283, rt32()); LST(289, __builtin_amdgcn_s_memtime()); }
        }
        DRAIN(); BAR();
    }
    pre_post(rs, j, 5u, ep);
    if (j == 20) LST(284, rt32());
    if (j == 3) LST(285, rt32());
}

#define M32A(first, d, a, b) do { if (first) asm volatile("v_mfma_f32_32x32x16_bf16 %0, %1, %2, 0" : "=&v"(d) : "a"(a), "v"(b)); \
                                  else asm volatile("v_mfma_f32_32x32x16_bf16 %0, %1, %2, %0" : "+v"(d) : "a"(a), "v"(b)); } while (0)
#define M32B(first, d, a, b) do { if (first) asm volatile("v_mfma_f32_32x32x16_bf16 %0, %1, %2, 0" : "=&v"(d) : "v"(a), "a"(b)); \
                                  else asm volatile("v_mfma_f32_32x32x16_bf16 %0, %1, %2, %0" : "+v"(d) : "v"(a), "a"(b)); } while (0)
#define VCVT(d, a, b) asm volatile("v_cvt_pk_bf16_f32 %0, %1, %2" : "=v"(d) : "v"(a), "v"(b))
#define VADD(d, a, b) asm volatile("v_add_f32 %0, %1, %2" : "=v"(d) : "v"(a), "v"(b))
#define LD4A(d, vo, so) asm volatile("buffer_load_dword %0, %1, %2, %3 offen sc1" : "=v"(d) : "v"(vo), "s"(srd), "s"(so) : "memory")
DEV u32 wst_src(int lane, int ld) { return (u32)((lane >> 4) * ld + 8 * ((lane & 15) ^ ((lane >> 4) << 2))) * 2u; }
DEV u32 wst_rd(int lane, int w) { const int q4 = (lane >> 2) & 3; return (u32)(q4 * 256 + ((w ^ q4) * 64) + ((lane >> 4) & 1) * 32 + (lane & 3) * 8); }

constexpr int QK_SEL = 81920;
#define QK_LEAD 12
#define QK_SMP 56
#define QK_W0 2
DEV void role_qkv(const Params& p, lptr lds, int c, v2u ep) {
    const int tid = threadIdx.x, lane = tid & 63, w = uni(tid >> 6), hh = lane >> 5, r32 = lane & 31;
    const int m = c >> 5, h = c & 31;
    const rsrc_t rs = mk_rsrc(p.ws, W_END);
    v4u wt[40];
    {
        const v4u sw = mk_srd(m == 0 ? p.Wq : m == 1 ? p.Wk : p.Wv, DM * NQ * 2), sx = mk_srd(p.x, ACT);
        const u32 vo = wst_src(lane, NQ) + (u32)h * (DH * 2), x8 = (u32)lane * 8u, x4 = (u32)lane * 4u;
        RowX xr[5];
        auto wfill = [&](int i0, int n) {
#pragma unroll 1
            for (int i = i0; i < i0 + n; ++i) DMAIN(sw, uu((4 * i + w) * 1024), vo, uu((16 * i + 4 * w) * (NQ * 2)));
        };
        wfill(0, QK_W0);
#pragma unroll
        for (int i = 0; i < 5; ++i)
            asm volatile("buffer_load_dwordx2 %0, %3, %5, %6 offen\n\tbuffer_load_dwordx2 %1, %3, %5, %6 offen offset:512\n\tbuffer_load_dword %2, %4, %5, %6 offen offset:1024"
                         : "=&v"(xr[i].a), "=&v"(xr[i].b), "=&v"(xr[i].c) : "v"(x8), "v"(x4), "s"(sx), "s"(uu((LN0 + 4 * c + w + 384 * i) * (DM * 2))) : "memory");
        wfill(QK_W0, 10 - QK_W0);
        if (c == 3) LST(275, rt32());
        VMW(10 - QK_W0);
        if (c == 3) LST(276, rt32());
#pragma unroll
        for (int i = 0; i < 5; ++i) {
            asm volatile("" : "+v"(xr[i].a), "+v"(xr[i].b), "+v"(xr[i].c));
            const int row = LN0 + 4 * c + w + 384 * i;
            if (i < 4 || row < SEQ) ln_row(rs, row, xr[i]);
            wfill(10 + 6 * i, 6);
        }
        if (c == 3) LST(264, rt32());
        lptr tb = lds + hh * 2048 + wst_rd(lane, w);
        SFOR(k, 40)
            if constexpr (k == 0) { VMW(15); BAR(); }
            if constexpr (k == 28) {
                if (w == 0) pre_poll(rs, 0, NPRE, 1u, ep);
                DRAIN(); BAR();
                pre_post(rs, 96 + c, 1u, ep);
            }
            lptr q = tb + (k >> 3) * 32768 + (k & 7) * 4096; wt[k] = tr2(q, q + 1024); asm volatile("" : "+a"(wt[k]));
        SEND
    }
    if (c == 3) LST(268, rt32());
    if (w == 0) LST(1350 + c, rt32());
    bool dead = false;
    int n0, n1;
    const u32 t16 = (u32)tid * 16u, wb = uu(w * 1024);
    u32 ar[16];
#pragma unroll
    for (int j = 0; j < 16; ++j) ar[j] = (u32)(r32 * 256 + ((2 * (j & 7) + hh) ^ (r32 & 15)) * 16 + (j >> 3) * 49152);
    MK_SRD();
#define XRD(y, ks) lds_r16(lds + ar[((ks) & 7) + ((ks) >= 24 ? 8 : 0)] + ((ks) >> 3) % 3 * 16384 + (y) * 8192)
#define QDMA(y, i, vo, so) DMA16I(wb, ((i) >> 1) * 16384 + ((i) & 1) * 4096 + (y) * 8192, vo, so, (i) * 4096)
    const u32 h2flag = (lane < 32 && w == 0) ? foff(F_H2 + lane) : OOB;
    const u32 ol = (m == 2 ? (u32)(2 * w + (r32 >> 4)) * 1024u : (u32)((r32 >> 4) * 4 + w) * 1024u) + (u32)(hh * 16 + (r32 & 15)) * 16u
                 + (m == 2 ? W_VO : m == 1 ? W_KQ : W_KQ + 8192u) + (u32)h * 16384u;
    const u32 fo = tid < 2 ? foff(F_QKV + tid * 96 + c) : OOB;
    lptr selw = lds + QK_SEL + (w == 0 ? 0 : 64), selr = lds + QK_SEL;
    auto late = [&](u32 fv) { const u32 need = n1 >= NSEG ? 0xFFFFFFFFu : dead ? 0u : (u32)n1 + 1u; return (u32)__builtin_amdgcn_ballot_w64(fv < need); };
    static_assert(W_H2 - W_H1 == 2 * 64 * (32 * DM * 2), "h");
    auto run = [&](auto isv) __attribute__((always_inline)) {
        constexpr bool V = decltype(isv)::value;
        f32x16 ax, ay = {};
        v4u x[8], pp, pq;
        u32 oy = OOB, lt = 1, fcnt = 0, fa = OOB;
        u32 nn = 0;
        PIN(lt); PIN(nn);
#pragma unroll
        for (int z = 0; z < 5; ++z) x[z] = XRD(1, 20 + z);
#define QPK(r_, a1, a_, q_) do { if constexpr (V) VCVT(r_[q_], a1[4 * (a_) + 8 * ((q_) >> 1) + 2 * ((q_) & 1)], a1[4 * (a_) + 8 * ((q_) >> 1) + 2 * ((q_) & 1) + 1]); \
                                 else VCVT(r_[q_], a1[4 * (a_) + (q_)], a1[4 * (a_) + (q_) + 8]); } while (0)
#define QPK4(r_, a1, a_) do { QPK(r_, a1, a_, 0); QPK(r_, a1, a_, 1); QPK(r_, a1, a_, 2); QPK(r_, a1, a_, 3); } while (0)
        for (;;) {
            u32 ls = uu(lt);
            n0 = uni((int)nn); n1 = n0 >> 8; n0 &= 255;
            if (ls != 0u && n0 >= min(NSEG, n1 + QK_LEAD)) {
                CFENCE();
                u32 fv = ld4(rs, h2flag);
                asm volatile("s_waitcnt vmcnt(0)" : "+v"(fv) :: "memory");
                *(LAS u32*)(selw + 16) = late(fv);
                BAR();
                st4s(rs, fa, 0, fcnt);
                fa = OOB;
                if (n1 >= NSEG) __builtin_amdgcn_endpgm();
                if (uu(*(LAS u32*)(selr + 16)) != 0u) {
                    if (w == 0) wait_flags(rs, h2flag, (u32)n1 + 1, dead);
                    BAR();
                }
                ls = 0u;
            }
            if (n0 == 4) pre_wait(rs, 96, 96, 1u, ep);
            int cur = n0;
            asm volatile("s_cmp_eq_u32 %3, 0\n\ts_cselect_b32 %0, %4, %0\n\ts_addc_u32 %2, %2, 0\n\ts_cmp_lg_u32 %3, 0\n\ts_addc_u32 %1, %1, 0"
                         : "+s"(cur), "+s"(n0), "+s"(n1) : "s"(ls), "s"(n1 | 64) : "scc");
            u32 xs = W_H1 + (u32)(cur + (cur & 64)) * (32 * DM * 2), ys, yvo, ox, fv;
            PINS(xs);
#define QT(k) do {} while (0)
            QT(0);
            SCHED();
            QDMA(0, 0, t16, xs); QDMA(0, 1, t16, xs);
            SFOR(z, 80)
                constexpr int ks = (z + 20) % 40, zn = (z + 5) % 80;
                if constexpr (z >= 20 && z < 60) { if constexpr (V) M32B(ks == 0, ax, x[z & 7], wt[ks]); else M32A(ks == 0, ax, wt[ks], x[z & 7]); }
                else { if constexpr (V) M32B(ks == 0, ay, x[z & 7], wt[ks]); else M32A(ks == 0, ay, wt[ks], x[z & 7]); }
                x[(z + 5) & 7] = XRD(zn < 20 || zn >= 60, (zn + 20) % 40);
                if constexpr (z == 77) lt = *(LAS u32*)selr;
                SCHED();
                if constexpr (z < 8) QDMA(0, z + 2, t16, xs);
                if constexpr (z == 9) { ys = W_H1 + (u32)n0 * (32 * DM * 2); yvo = t16 | (n0 < NSEG ? 0u : OOB); PINS(ys); PIN(yvo); }
                if constexpr (z == 11) { ox = ol + (u32)cur * (NH * 16384u); PIN(ox); }
                if constexpr (z == 14) { QT(1); asm volatile("s_waitcnt vmcnt(6) lgkmcnt(0)\n\ts_barrier" ::: "memory"); QT(2); }
                if constexpr (z == 15) st4s(rs, fa, 0, fcnt);
                if constexpr (z >= 16 && z < 26) QDMA(1, z - 16, yvo, ys);
                if constexpr (z == 26) QPK4(pp, ay, 0);
                if constexpr (z == 27) st16(rs, oy, pp);
                if constexpr (z == 28) QPK4(pq, ay, 1);
                if constexpr (z == 29) st16(rs, oy + 512, pq);
                if constexpr (z == 30) { QT(3); asm volatile("s_waitcnt vmcnt(13)\n\ts_barrier" ::: "memory"); QT(4); }
                if constexpr (z == 32) { fcnt = lane == 0 ? (u32)n0 : (u32)n1; fa = fo; asm volatile("" : "+v"(fcnt), "+v"(fa)); }
                if constexpr (z == 34) { oy = (ol + (u32)n0 * (NH * 16384u)) | (yvo & OOB); PIN(oy); }
                if constexpr (z == 35) { nn = (u32)(min(n0 + 1, NSEG) | n1 << 8); PIN(nn); }
                if constexpr (z == 54) { asm volatile("s_waitcnt vmcnt(2) lgkmcnt(0)\n\ts_barrier" ::: "memory"); QT(5); }
                if constexpr (z == QK_SMP) LD4A(fv, h2flag, 0);
                if constexpr (z == 62) QPK4(pp, ax, 0);
                if constexpr (z == 63) st16(rs, ox, pp);
                if constexpr (z == 64) QPK4(pq, ax, 1);
                if constexpr (z == 65) st16(rs, ox + 512, pq);
                if constexpr (z == 73) { asm volatile("s_waitcnt vmcnt(2)" : "+v"(fv) :: "memory"); QT(6); ls = late(fv); PINS(ls); }
                if constexpr (z == 74) { lt = ls; PIN(lt); *(LAS u32*)selw = lt; }
                if constexpr (z == 76) asm volatile("s_waitcnt lgkmcnt(2)\n\ts_barrier" ::: "memory");
                SCHED();
            SEND
        }
    };
    if (m == 2) run(IC<1>{}); else run(IC<0>{});
}

#define OU_SMP 44
DEV void role_out(const Params& p, lptr lds, int L, int kq, int ng, v2u ep) {
    const int tid = threadIdx.x, lane = tid & 63, w = uni(tid >> 6), hh = lane >> 5, r32 = lane & 31;
    const rsrc_t rs = mk_rsrc(p.ws, W_END);
    v4u wo[64];
    bool dead = false;
    {
        const v4u sw = mk_srd(p.Wo, DM * NQ * 2);
        const u32 vo = wst_src(lane, DM) + (u32)ng * 256u;
        auto fill = [&](int b) {
#pragma unroll 1
            for (int i = 0; i < 8; ++i) DMAIN(sw, uu((b % 5) * 32768 + (4 * i + w) * 1024), vo, uu((kq * 1024 + b * 128 + 16 * i + 4 * w) * (DM * 2)));
        };
#pragma unroll 1
        for (int b = 0; b < 5; ++b) fill(b);
        lptr tb = lds + hh * 1024 + wst_rd(lane, w);
        SFOR(b, 8)
            VMW(b < 4 ? 32 : 56 - 8 * b); BAR();
            lptr q = tb + (b % 5) * 32768;
#pragma unroll
            for (int k = 0; k < 8; ++k) { lptr a = q + (k >> 1) * 8192 + (k & 1) * 2048; wo[8 * b + k] = tr2(a, a + 4096); if (b < 7) asm volatile("" : "+a"(wo[8 * b + k])); else PIN(wo[8 * b + k]); }
            if constexpr (b < 3) { BAR(); fill(b + 5); }
        SEND
    }
    pre_wait(rs, 0, NPRE, 1u, ep);
    if (kq == 0 && ng == 0) LST(272 + L, rt32());
    MK_SRD();
    const u32 ofl = lane < 8 ? foff(F_O + L * 32 + kq * 8 + lane) : OOB, l16 = (u32)lane * 16u;
    const u32 fo = (tid == 0) ? foff(F_P + L * 32 + kq * 5 + ng) : OOB;
    const u32 src0 = uu(W_VO + 8192u + (u32)(L * NSEG * NH + kq * 8) * 16384u + w * 1024);
#define ODMA(i, dst, so) DMA16I(dst, (i) * 4096, l16, so, ((i) >> 1) * 16384 + ((i) & 1) * 4096)
    auto first = [&](int s) { const u32 dst = uu((s & 1) * 65536 + w * 1024), src = src0 + (u32)s * (NH * 16384u); SFOR(i, 10) ODMA(i, dst, src); SEND };
    u32 pa = W_P + (u32)(L * NSEG) * P_ITEM + (u32)(4 * hh * 4 + kq) * (DM * 4) + (u32)(ng * 128 + w * 32 + r32) * 4u, fv = 0;
    bool early = false;
    for (int s = 0; s < NSEG; ++s) {
        const u32 dst = uu((s & 1) * 65536 + w * 1024), src = src0 + (u32)s * (NH * 16384u);
        lptr A = lds + (s & 1) * 65536 + l16;
        if (!early) {
            if (!all_(ofl == OOB || fv >= (u32)s + 1)) wait_flags(rs, ofl, (u32)s + 1, dead);
            LS3(L == 0 && w == 1, 1, kq * 5 + ng);
            first(s);
        }
        asm volatile("s_waitcnt vmcnt(8)\n\ts_barrier" ::: "memory");
        v4u o[64];
#pragma unroll
        for (int k = 0; k < 3; ++k) o[k] = lds_r16(A + k * 1024);
        f32x16 acc;
        SCHED();
        SFOR(k, 64)
            if constexpr (k < 56) M32B(k == 0, acc, o[k], wo[k]); else M32V(acc, o[k], wo[k]);
            if constexpr (k < 61) o[k + 3] = lds_r16(A + (k + 3) * 1024);
            SCHED();
            if constexpr (k < 12 && (k & 1) == 0) ODMA(10 + k / 2, dst, src);
            if constexpr (k == 4) asm volatile("s_waitcnt vmcnt(7)\n\ts_barrier" ::: "memory");
            if constexpr (k == 20) asm volatile("s_waitcnt vmcnt(5)\n\ts_barrier" ::: "memory");
            if constexpr (k == 40) asm volatile("s_waitcnt vmcnt(0)\n\ts_barrier" ::: "memory");
            if constexpr (k == OU_SMP) LD4A(fv, ofl, 0);
            SCHED();
        SEND
        asm volatile("s_nop 9" : "+v"(acc), "+v"(pa));
#pragma unroll
        for (int i = 0; i < 16; ++i) st4s(rs, pa, (u32)((8 * (i >> 2) + (i & 3)) * (4 * DM * 4)), u_(acc[i]));
        asm volatile("s_waitcnt vmcnt(16)" : "+v"(fv) :: "memory");
        LS3(L == 0 && w == 1, 2, kq * 5 + ng);
        early = s + 1 < NSEG && all_(ofl == OOB || fv >= (u32)s + 2);
        if (early) { first(s + 1); VMW(10); }
        else { fv = ld4(rs, ofl); asm volatile("s_waitcnt vmcnt(0)" : "+v"(fv) :: "memory"); }
        BAR();
        st4s(rs, fo, 0, (u32)s + 1);
        pa += P_ITEM;
        LS2(L == 0 && w == 1, 160 + kq * 5 + ng);
    }
}

constexpr int RW_P = 20480, RW_X = RW_P + 10240;
DEV void role_row(const Params& p, lptr lds, int id, v2u ep) {
    const int lane = threadIdx.x & 63, w = uni(threadIdx.x >> 6);
    const int ri = id >> 1, L = id & 1;
    const rsrc_t rs = mk_rsrc(p.ws, W_END);
    bool dead = false, have = false;
    const u32 fp = lane < 20 ? foff(F_P + L * 32 + lane) : OOB, l16 = (u32)lane * 16u, lx = lane < 16 ? l16 + 1024u : OOB, fb = uu(w * CV_W);
    lptr F = lds + w * CV_W;
    MK_SRD();
    const v4u sx = mk_srd(p.x, ACT);
    auto yfill = [&](int b) {
#pragma unroll 1
        for (int i = 0; i < 5; ++i) DMA32(uu(fb + (b & 1) * 10240 + i * 2048), l16, uu(W_Y + (u32)(ri * 8 + b) * 10240u + i * 2048));
    };
    auto xfill = [&](int s) {
        const u32 d = uu(fb + RW_X + (s & 1) * 2048), o = uu((u32)(s * 32 + ri) * (DM * 2));
        if (L == 0) { DMAIN(sx, d, l16, o); DMAIN(sx, d + 1024, lx, o); } else { DMA16(d, l16, o + W_X2); DMA16(d + 1024, lx, o + W_X2); }
    };
    auto pfill = [&](int s) {
        const u32 o = uu(W_P + (u32)(L * NSEG + s) * P_ITEM + (u32)ri * (4 * DM * 4));
        SFOR(i, 10) DMA16I(fb, RW_P + i * 1024, l16, o, i * 1024); SEND
        if (L == 1) xfill(s);
    };
    if (L == 0) xfill(0);
    pre_poll(rs, 0, NPRE, 3u, ep);
    if (id == 0) LST(274, rt32());
    yfill(0); DRAIN();
    u32 fv = 0;
    for (int s = 0; s < NSEG; ++s) {
        if (!have) {
            if (!all_(fp == OOB || fv >= (u32)s + 1)) wait_flags(rs, fp, (u32)s + 1, dead);
            LS3(L == 0, 3, ri);
            pfill(s);
        }
        LS2(L == 0, 192 + ri);
        f32x2 y[5];
        {
            lptr ya = F + ((s >> 3) & 1) * 10240 + (s & 7) * 2 + lane * 16;
            auto yr = [&](lptr q) { return lo_f(*(LAS u16*)q); };
            y[0] = (f32x2){yr(ya), yr(ya + 2560)}; y[1] = (f32x2){yr(ya + 5120), yr(ya + 7680)};
            y[2] = (f32x2){yr(ya + 1024), yr(ya + 3584)}; y[3] = (f32x2){yr(ya + 6144), yr(ya + 8704)};
            lptr yc = ya - lane * 16 + 2048 + (lane >> 1) * 16 + (lane & 1) * 5120;
            y[4] = (f32x2){yr(yc), yr(yc + 2560)};
            asm volatile("" : "+v"(y[0]), "+v"(y[1]), "+v"(y[2]), "+v"(y[3]), "+v"(y[4]));
        }
        if (!have) DRAIN();
        LS3(L == 0, 4, ri);
        f32x2 x1[5];
        {
            f32x4 a[4], b[4]; f32x2 c[4];
            lptr q = F + RW_X + (s & 1) * 2048;
            v2u xa = *(LAS v2u*)(q + lane * 8), xb = *(LAS v2u*)(q + 512 + lane * 8); u32 xc = *(LAS u32*)(q + 1024 + lane * 4);
#pragma unroll
            for (int kq = 0; kq < 4; ++kq) { lptr r = F + RW_P + kq * (DM * 4); a[kq] = *(LAS f32x4*)(r + l16); b[kq] = *(LAS f32x4*)(r + 1024 + l16); c[kq] = *(LAS f32x2*)(r + 2048 + lane * 8); }
            SCHED();
            const f32x4 sa = (a[0] + a[1]) + (a[2] + a[3]), sb = (b[0] + b[1]) + (b[2] + b[3]);
            const f32x2 o[5] = {sa.lo, sa.hi, sb.lo, sb.hi, (c[0] + c[1]) + (c[2] + c[3])};
            const u32 xi[5] = {xa.x, xa.y, xb.x, xb.y, xc};
#pragma unroll
            for (int i = 0; i < 5; ++i) x1[i] = up2(xi[i]) + o[i];
            asm volatile("" : "+v"(x1[0]), "+v"(x1[1]), "+v"(x1[2]), "+v"(x1[3]), "+v"(x1[4]));
        }
        have = s + 1 < NSEG && all_(fp == OOB || fv >= (u32)s + 2);
        if (__builtin_expect(have, 0)) pfill(s + 1);
        if (L == 0) xfill(s + 1);
        const f32x2 st1 = ln_stats(x1);
        u32 xp[5];
#pragma unroll
        for (int i = 0; i < 5; ++i) xp[i] = pkv(x1[i] + (x1[i] - st1.x) * (y[i] * st1.y));
        const int row = s * 32 + ri;
        if (L == 0) {
            const u32 ro = (u32)row * (DM * 2);
            f32x2 x2[5];
#pragma unroll
            for (int i = 0; i < 5; ++i) x2[i] = up2(xp[i]);
            st8s(rs, (u32)lane * 8, W_X2 + ro, (v2u){xp[0], xp[1]});
            st8s(rs, (u32)lane * 8 + 512, W_X2 + ro, (v2u){xp[2], xp[3]});
            st4s(rs, (u32)lane * 4 + 1024, W_X2 + ro, xp[4]);
            const f32x2 st2 = ln_stats(x2);
            u32 hp[5];
#pragma unroll
            for (int i = 0; i < 5; ++i) hp[i] = pk2((x2[i] - st2.x) * st2.y);
            store_h(rs, W_H2, row, hp);
        } else {
            u16* o_ = p.out + (size_t)row * DM;
            *(v2u*)(o_ + 4 * lane) = (v2u){xp[0], xp[1]};
            *(v2u*)(o_ + 256 + 4 * lane) = (v2u){xp[2], xp[3]};
            *(u32*)(o_ + 512 + 2 * lane) = xp[4];
        }
        CFENCE();
        LS3(L == 0, 5, ri);
        fv = ld4(rs, fp);
        asm volatile("s_waitcnt vmcnt(0)" : "+v"(fv) :: "memory");
        st4s(rs, lane == 0 && L == 0 ? foff(F_H2 + ri) : OOB, 0, (u32)s + 1);
        if ((s & 7) == 6 && s < NSEG - 8) {
            if (s == 30) pre_poll(rs, 0, NCV, 5u, ep);
            if (s == 46) st8s(rs, (id == 1 && lane == 0) ? foff(F_EPOCH) : OOB, 0, (v2u){ep.x + 8u, ep.y});
            yfill((s >> 3) + 1);
        }
        if (ri == 0) LST(320 + L * 64 + s, rt32());
        LS2(L == 0, 224 + ri);
    }
}

constexpr float SCALE = 0.08838834764831845f, LOG2E = 1.4426950408889634f;
constexpr int C_K = 0, C_V = 8192, C_QX = 16384, C_MEM = 24576, C_Q = C_MEM + 10240, C_KM = C_Q + 2304, C_VM = C_KM + 2304, C_DUMP = C_VM + 2048,
              C_ZERO = C_DUMP + 1024, C_WL = C_ZERO + 128;
constexpr int NLF = 14;
static_assert(C_WL % 16 == 0 && C_WL + 4 * NLF * 2048 <= LDS_BYTES, "head lds");
#define CH_VIS 10
#define CH_H0 104
#define CH_W0 81
#define CH_MH 3
#define CH_MW 3
#define CH_REC 12
#define CH_PRE 9
#define CH_T0 190
#define CH_UP 2
#define CH_WX 6
#define WAIT_TXT(tmp, t4) "s_memrealtime vcc\n\t.p2align 5\n1:\n\ts_waitcnt lgkmcnt(0)\n\ts_sub_u32 " tmp ", " t4 ", vcc_lo\n\ts_memrealtime vcc\n\ts_cmpk_lt_u32 " tmp ", 400\n\ts_cbranch_scc1 1b\n\t" \
                          "s_add_u32 " tmp ", " tmp ", 4\n\ts_cselect_b32 " tmp ", " tmp ", 0\n2:\n\ts_sub_u32 " tmp ", " tmp ", 1\n\ts_cbranch_scc0 2b\n\t"
DEV void wait_until(u32 t) { u32 tmp; asm volatile(WAIT_TXT("%0", "%1") "s_waitcnt lgkmcnt(0)" : "=&s"(tmp) : "s"(uu(t - 4u)) : "vcc", "scc", "memory"); }
#define SMF_A(acc, a, b) asm volatile("v_smfmac_f32_16x16x64_bf16 %0, %1, %2, %3" : "+v"(acc) : "v"(a), "a"(b), "v"(idx))
#define SMF_V(acc, a, b) asm volatile("v_smfmac_f32_16x16x64_bf16 %0, %1, %2, %3" : "+v"(acc) : "v"(a), "v"(b), "v"(idx))
#define MF0(d, a, b) asm volatile("v_mfma_f32_16x16x32_bf16 %0, %1, %2, 0" : "=&v"(d) : "v"(a), "v"(b))
#define MF1(d, a, b) asm volatile("v_mfma_f32_16x16x32_bf16 %0, %1, %2, %0" : "+v"(d) : "v"(a), "v"(b))
#define MF0H(d, a, b) asm volatile("v_mfma_f32_16x16x16_bf16 %0, %1, %2, 0" : "=&v"(d) : "v"(a), "v"(b))
#define MF1H(d, a, b) asm volatile("v_mfma_f32_16x16x16_bf16 %0, %1, %2, %0" : "+v"(d) : "v"(a), "v"(b))
DEV v8u tr_frag(lptr p0, lptr p1, lptr p2, lptr p3) { return cat8(tr2(p0, p1), tr2(p2, p3)); }
constexpr int qslot(int ks) { return (ks & 1) && ks < 8 ? ks >> 1 : -1; }
constexpr int kslot(int ks) { return ks < 4 ? 4 + ks : -1; }
constexpr int vslot(int ks) { return ks >= 4 ? 4 + ks : -1; }
DEV u32 ring_map(u32 off, u32 half) { return ((off >> 13) << 14) + (half << 13) + (off & 8191u); }
#define ZRD(x, i) x = __builtin_bit_cast(f32x4, lds_r16(ZP + 16 * (i)))
#define VMAX(d, a, b) asm volatile("v_max_f32 %0, %1, %2" : "=v"(d) : "v"(a), "v"(b))
#define VMAX3(d, a, b, c_) asm volatile("v_max3_f32 %0, %1, %2, %3" : "=v"(d) : "v"(a), "v"(b), "v"(c_))
#define VEXP(d, a) asm volatile("v_exp_f32 %0, %1" : "=v"(d) : "v"(a))
#define VMULC(d, a) asm volatile("v_mul_f32 %0, 0x3e0293ee, %1" : "=v"(d) : "v"(a))
#define CADD(d, a, b) do { d = (a) + (b); PIN(d); } while (0)
#define CMULC(d, a) do { d = (a) * CEXP; PIN(d); } while (0)
#define CCVT(d, a, b) do { d = pk(a, b); PIN(d); } while (0)
#define LGK(n) __builtin_amdgcn_s_waitcnt(0xC07F | (n) << 8)
#define SEL(d, a, b) asm volatile("v_cndmask_b32 %0, %1, %2, %3" : "=v"(d) : "v"(a), "v"(b), "s"(himask))
#define QPERM(d, a, q) asm volatile("v_mov_b32_dpp %0, %1 quad_perm:" q " row_mask:0xf bank_mask:0xf" : "=v"(d) : "v"(a))

#define AL_H 3
#define AL_W 0
#define HD_SMP 56
DEV void role_head(const Params& p, lptr lds, int L, int h, v2u ep) {
    const int tid = threadIdx.x, lane = tid & 63, w = uni(tid >> 6), g = lane >> 4, r = lane & 15;
    const rsrc_t rs = mk_rsrc(p.ws, W_END);
    v8u wq0[10], wq1[10], wk0[10], wk1[10], wv0[10], wv1[10];
    lptr WL = lds + C_WL + w * (NLF * 2048) + lane * 16;
    {
        constexpr int GS = 1056, CS = 48 * GS;
        const v4u sq = mk_srd(p.Wq, DM * NQ * 2), sk = mk_srd(p.Wk, DM * NQ * 2), sv = mk_srd(p.Wv, DM * NQ * 2);
        const u32 vo = (u32)(g * NQ + h * DH + 8 * (r ^ ((g & 1) << 3))) * 2u;
        auto fill = [&](int ch) {
#pragma unroll 1
            for (int i = 0; i < 4; ++i) {
                const u32 dst = uu((ch % 3) * CS + (4 * i + w) * GS), so = uu((ch * 64 + 16 * i + 4 * w) * (NQ * 2));
                DMAIN(sq, dst, vo, so); DMAIN(sk, dst + 16 * GS, vo, so); DMAIN(sv, dst + 32 * GS, vo, so);
            }
        };
        fill(0); fill(1); fill(2);
        const int q4 = r >> 2;
        lptr tb = lds + (4 * (g >> 1) + 2 * (q4 >> 1) + (g & 1)) * GS + (q4 & 1) * 256 + ((w * 64 + (r & 3) * 8) ^ ((q4 & 1) << 7));
        SFOR(ch, 10)
            VMW(ch < 8 ? 24 : 108 - 12 * ch);
            BAR();
            auto frag = [&](int m, int t) { lptr b = tb + (ch % 3) * CS + m * 16 * GS + t * 32; return tr_frag(b, b + 512, b + 8 * GS, b + 8 * GS + 512); };
            wq0[ch] = frag(0, 0); wq1[ch] = frag(0, 1); wk0[ch] = frag(1, 0); wk1[ch] = frag(1, 1); wv0[ch] = frag(2, 0); wv1[ch] = frag(2, 1);
            asm volatile("" : "+a"(wq0[ch]), "+a"(wk0[ch]), "+v"(wv0[ch]), "+v"(wv1[ch]));
            if constexpr (qslot(ch) < 0) asm volatile("" : "+a"(wq1[ch])); else asm volatile("" : "+v"(wq1[ch]));
            if constexpr (kslot(ch) < 0) asm volatile("" : "+a"(wk1[ch])); else asm volatile("" : "+v"(wk1[ch]));
            BAR();
            if constexpr (ch < 7) fill(ch + 3);
        SEND
#pragma unroll
        for (int ch = 0; ch < 10; ++ch) {
            if (qslot(ch) >= 0) { lds_w16(WL + qslot(ch) * 2048, wq1[ch].lo); lds_w16(WL + qslot(ch) * 2048 + 1024, wq1[ch].hi); }
            if (kslot(ch) >= 0) { lds_w16(WL + kslot(ch) * 2048, wk1[ch].lo); lds_w16(WL + kslot(ch) * 2048 + 1024, wk1[ch].hi); }
            if (vslot(ch) >= 0) { lds_w16(WL + vslot(ch) * 2048, wv1[ch].lo); lds_w16(WL + vslot(ch) * 2048 + 1024, wv1[ch].hi); }
        }
        CFENCE();
    }
    const u32 idx = (r & 2) ? 0xEEEEu : 0x4444u;
    unsigned long long himask = 0xCCCCCCCCCCCCCCCCull;
    PINS(himask);
    bool dead = false;
    if (h == 3) LST(266 + L, rt32());
    if (w == 0) LST(1240 + L * 32 + h, rt32());
    if (L == 1) {
        KvIn in;
        kvt_load(p, LN0 + 4 * h + w, in, [] {}); kvt_make(rs, lds + w * 9216, LN0 + 4 * h + w, in);
        DRAIN(); BAR();
        pre_post(rs, 64 + h, 3u, ep);
    }
    const u32 pol0 = pol_wait(rs, ep);
    if (h == 3 && L == 0) LST(277, rt32());
    const u32 l16 = (u32)lane * 16u;
    const u32 mring = W_MRING + (u32)L * (MB / 4) + pol0 * 8192u, hint = mring + 4 * 32768u;
    const u32 fkh = lane < 3 ? foff(F_QKV + L * 96 + lane * 32 + h) : lane < 7 ? hint + (u32)(lane - 3) * 4u : OOB;
    u32 tm = 0;
    MK_SRD();
    u32 ksrc = uu(W_KQ + (u32)(L * (NSEG - 1) * NH + h) * 16384u + w * 2048), vsrc = ksrc + (W_VO - W_KQ);
    u32 dk = uu(C_K + w * 2048), dv = dk + (C_V - C_K), dq = dk + (C_QX - C_K), dm = uu(C_MEM + w * 2048), dm2 = uu(w < 2 ? C_MEM + 8192 + w * 1024 : C_DUMP);
    asm volatile("" : "+s"(dk), "+s"(dv), "+s"(dq), "+s"(dm), "+s"(dm2));
    auto keys_wait = [&](u32 need) {
#pragma unroll 1
        for (int n = 0; !dead; ++n) {
            const bool beat = need + 2 * L > 2u;
            if (beat) wait_until(tm);
            const u32 hv = ld4(rs, lane == 7 ? foff(F_ERR) : fkh);
            if (beat) { tm = rdl(hv, 5); wait_until(rdl(hv, 3) - 30u); }
            if (((u32)__builtin_amdgcn_ballot_w64(ld4(rs, fkh) < need) & 7u) == 0u) break;
            dead = n > SPIN_CAP || rdl(hv, 7) != 0u;
            st4s(rs, dead ? foff(F_ERR) : OOB, 0, 1u);
        }
        CFENCE();
        DMA32(dk, l16, ksrc); DMA32(dv, l16, vsrc); DMA32(dq, l16, ksrc ^ 8192u);
    };
    if (L == 0) { st4s(rs, tid == 0 ? hint + 4096u + (u32)h * 128u : OOB, 0, 0u); keys_wait(1u); }
    lptr qw = lds + C_Q + (2 * g) * 288 + (32 * w + 2 * r) * 2;
    lptr kw = qw + (C_KM - C_Q); PIN(kw);
    lptr qr = lds + C_Q + (2 * (r >> 2) + (r & 1)) * 288 + g * 16;
    lptr LB = lds + l16, VX = lds + C_V + w * 2048 + l16;
    PIN(VX);
    if (tid >= 64 && tid < 70) { const u32 z = tid == 68 ? 0x3F803F80u : tid == 69 ? PSENT : 0u; lds_w16(lds + C_ZERO + (tid - 64) * 16, (v4u){z, z, z, z}); }
    u32 zaddr = 0; PIN(zaddr);
    lptr ZP = lds + zaddr + C_ZERO;
    const float CEXP = SCALE * LOG2E;
#define LF_RD(slot, hf) lf[slot][hf] = lds_r16(WL + (slot) * 2048 + (hf) * 1024)
    BAR();
    const u32 ofl = tid == 0 ? foff(F_O + L * 32 + h) : OOB;
    u32 oso = (((g | L) & 1) ? OOB : 0u) + W_ORING + (u32)L * (MB / 2) + ring_map((u32)((r >> 3) * 32768 + h * 1024 + w * 256 + (g >> 1) * 128 + (r & 7) * 16), pol0);
    unsigned long long vis[8];
#pragma unroll
    for (int i = 0; i < 8; ++i) vis[i] = __builtin_amdgcn_ballot_w64(16 * (i >> 2) + 4 * g + (i & 3) <= 16 * (w & 1) + r);
    u32 missed = 0;
    for (int s = -L; s < NSEG;) {
        u32 sv = (u32)max(s, 0), ms = mring + (sv & 3u) * 32768u + w * 2048, ms2 = mring + (sv & 3u) * 32768u + 16384u + (w & 1) * 1024;
        asm volatile("" : "+s"(ms), "+s"(ms2)); PIN(sv);
        f32x4 aq0, aq1, ak0, ak1;
        v4u lf[NLF][2];
        ZRD(aq0, 0); ZRD(aq1, 1); ZRD(ak0, 2); ZRD(ak1, 3);
#pragma unroll
        for (int i = 0; i < 4; ++i) { LF_RD(i, 0); LF_RD(i, 1); }
        SCHED();
        v4u A[10];
#define HT(k) do {} while (0)
        v4u kx[8], qf[4], kmf[4], vx[2], ones, P;
        float fa, fb, fc, fd, inv, sx[4], t[4], tm0, tm1; u32 pa, pb, fl, e01, e23, pmm;
        f32x4 av0, av1, s0, s1, sm, oa, ob, rsum;
        v2u vm, pm;
#define LW(sl) cat8(lf[sl][0], lf[sl][1])
#define PQ(ks, tl) do { if constexpr (!(tl)) SMF_A(aq0, A[ks], wq0[ks]); else if constexpr (qslot(ks) < 0) SMF_A(aq1, A[ks], wq1[ks]); else { const v8u lw_ = LW(qslot(ks)); SMF_V(aq1, A[ks], lw_); } } while (0)
#define PK(ks, tl) do { if constexpr (!(tl)) SMF_A(ak0, A[ks], wk0[ks]); else if constexpr (kslot(ks) < 0) SMF_A(ak1, A[ks], wk1[ks]); else { const v8u lw_ = LW(kslot(ks)); SMF_V(ak1, A[ks], lw_); } } while (0)
#define PV(ks, tl) do { if constexpr (!(tl)) SMF_V(av0, A[ks], wv0[ks]); else if constexpr (vslot(ks) < 0) SMF_V(av1, A[ks], wv1[ks]); else { const v8u lw_ = LW(vslot(ks)); SMF_V(av1, A[ks], lw_); } } while (0)
#define KXR(i) kx[i] = lds_r16(LB + C_K + ((i) & 1) * 4096 + ((i) >> 1) * 1024)
#define XW(q_) do { *(LAS u32*)(q_) = pa; *(LAS u32*)((q_) + 288) = pb; } while (0)
        auto slot = [&](auto zc) __attribute__((always_inline)) {
            constexpr int z = decltype(zc)::value;
            if constexpr (z < 14) PQ(z >> 1, z & 1);
            else if constexpr (z < 17) PQ(z - 7, 0);
            else if constexpr (z < 20) PQ(z - 10, 1);
            else if constexpr (z < 34) PK((z - 20) >> 1, z & 1);
            else if constexpr (z < 37) PK(z - 27, 0);
            else if constexpr (z < 40) PK(z - 30, 1);
            else if constexpr (z < 42) { if constexpr (z == 40) MF0(s0, kx[0], qf[0]); else MF0(s1, kx[1], qf[0]); }
            else if constexpr (z < 48) { if constexpr (z & 1) MF1(s1, kx[z - 40], qf[(z - 40) >> 1]); else MF1(s0, kx[z - 40], qf[(z - 40) >> 1]); }
            else if constexpr (z < 54) PV(z - 48, 0);
            else if constexpr (z == 54) MF0(sm, kmf[0], qf[0]);
            else if constexpr (z < 58) MF1(sm, kmf[z - 54], qf[z - 54]);
            else if constexpr (z < 62) PV(z - 52, 0);
            else if constexpr (z < 64) PV(z - 62, 1);
            else if constexpr (z == 64) MF0(oa, vx[0], P);
            else if constexpr (z == 65) MF0(ob, vx[1], P);
            else if constexpr (z == 66) MF0(rsum, ones, P);
            else if constexpr (z < 75) PV(z < 70 ? z - 63 : z == 70 ? 2 : z == 71 ? 7 : z == 72 ? 3 : z - 65, 1);
            else { const v2u ones2 = ones.lo; MF1H(rsum, ones2, pm); }
            if constexpr (z < 6) A[4 + z] = lds_r16(LB + C_MEM + (4 + z) * 1024);
            if constexpr (z >= 6 && z < 10) LF_RD(4 + ((z - 6) >> 1), z & 1);
            if constexpr (z >= 14 && z < 18) LF_RD(6 + ((z - 14) >> 1), z & 1);
            if constexpr (z >= 22 && z < 28) KXR(z - 22);
            if constexpr (z == 28) XW(qw);
            if constexpr (z == 29 || z == 30) KXR(z - 23);
            if constexpr (z == 32 || z == 47) asm volatile("s_barrier" ::: "memory");
            if constexpr (z >= 33 && z < 37) qf[z - 33] = lds_r16(qr + (z - 33) * 64);
            if constexpr (z == 20) ZRD(av0, 0);
            if constexpr (z == 43) XW(kw);
            if constexpr (z == 44 || z == 45) vx[z - 44] = lds_r16(VX + (z - 44) * 1024);
            if constexpr (z >= 48 && z < 52) kmf[z - 48] = lds_r16(qr + (C_KM - C_Q) + (z - 48) * 64);
            if constexpr (z == 52) ZRD(av1, 1);
            if constexpr (z == 37) ones = lds_r16(ZP + 64);
            if constexpr (z >= 57 && z < 64) LF_RD(8 + ((z - 57) >> 1), (z - 57) & 1);
            if constexpr (z >= 65 && z < 70) LF_RD(8 + ((z - 58) >> 1), (z - 58) & 1);
            if constexpr (z == HD_SMP) LD4A(fl, fkh, 0);
            SCHED();
            if constexpr (z == 19) HT(2);
            if constexpr (z == 32) HT(3);
            if constexpr (z == 47) HT(4);
            if constexpr (z == 64) HT(5);
            if constexpr (z == 19) LGK(4);
            if constexpr (z == 21 || z == 41) LGK(1);
            if constexpr (z == 56 || z == 73) LGK(0);
            if constexpr (z == 31 || z == 46) LGK(2);
            if constexpr (z == 39) LGK(4);
            if constexpr (z >= 53 && z < 56) LGK(57 - z);
            if constexpr (z == 64) LGK(3);
            if constexpr (z == 68) LGK(5);
            if constexpr (z >= 70 && z < 73) LGK(74 - z);
            SCHED();
            if constexpr (z == 18) CADD(fa, aq0[0], aq0[2]);
            if constexpr (z == 19) CADD(fb, aq0[1], aq0[3]);
            if constexpr (z == 20) CMULC(fa, fa);
            if constexpr (z == 21) CADD(fc, aq1[0], aq1[2]);
            if constexpr (z == 22) CADD(fd, aq1[1], aq1[3]);
            if constexpr (z == 23) CMULC(fc, fc);
            if constexpr (z == 24) CMULC(fb, fb);
            if constexpr (z == 25) CMULC(fd, fd);
            if constexpr (z == 26) CCVT(pa, fa, fc);
            if constexpr (z == 27) CCVT(pb, fb, fd);
            if constexpr (z == 38) CADD(fa, ak0[0], ak0[2]);
            if constexpr (z == 39) CADD(fb, ak0[1], ak0[3]);
            if constexpr (z == 41) { CADD(fc, ak1[0], ak1[2]); CADD(fd, ak1[1], ak1[3]); }
            if constexpr (z == 42) { CCVT(pa, fa, fc); CCVT(pb, fb, fd); }
            if constexpr (z >= 49 && z < 53) SEL(sx[z - 49], s0[z - 49], s1[z - 49]);
            if constexpr (z >= 53 && z < 57) VEXP(t[z - 53], sx[z - 53]);
            if constexpr (z == 57) CCVT(e01, t[0], t[1]);
            if constexpr (z == 58) CCVT(e23, t[2], t[3]);
            if constexpr (z == 59) QPERM(P.x, e01, "[0,1,0,1]");
            if constexpr (z == 60) QPERM(P.z, e01, "[2,3,2,3]");
            if constexpr (z == 61) QPERM(P.y, e23, "[0,1,0,1]");
            if constexpr (z == 62) QPERM(P.w, e23, "[2,3,2,3]");
            if constexpr (z == 63) CADD(fa, av0[0], av0[2]);
            if constexpr (z == 64) { asm volatile("" :: "v"(sm)); VEXP(tm0, sm[0]); }
            if constexpr (z == 65) VEXP(tm1, sm[1]);
            if constexpr (z == 66) CADD(fb, av0[1], av0[3]);
            if constexpr (z == 67) CCVT(pmm, tm0, tm1);
            if constexpr (z >= 68 && z < 72) SEL(oa[z - 68], oa[z - 68], ob[z - 68]);
            if constexpr (z == 72) asm volatile("v_cndmask_b32 %0, %1, 0, %2" : "=v"(pm.x) : "v"(pmm), "s"(himask));
            if constexpr (z == 73) asm volatile("v_cndmask_b32 %0, 0, %1, %2" : "=v"(pm.y) : "v"(pmm), "s"(himask));
            if constexpr (z == 74) LGK(15);
            if constexpr (z == 75) CCVT(vm.x, fa, fb);
            SCHED();
        };
        {
            u32 tmp;
            asm volatile("s_mov_b32 m0, %2\n\t" WAIT_TXT("%0", "%1") "buffer_load_dwordx4 %4, %5, %6 offen sc1 lds\n\tbuffer_load_dwordx4 %4, %5, %6 offen offset:1024 sc1 lds\n\t"
                         "s_mov_b32 m0, %3\n\ts_nop 0\n\tbuffer_load_dwordx4 %4, %5, %7 offen sc1 lds\n\t.p2align 6\n\t.rept %8\n\ts_nop 0\n\t.endr\n\ts_waitcnt vmcnt(0) lgkmcnt(0)\n\ts_barrier"
                         : "=&s"(tmp) : "s"(uu(tm - 4u)), "s"(dm), "s"(dm2), "v"(l16), "s"(srd), "s"(ms), "s"(ms2), "i"(AL_H) : "vcc", "scc", "memory");
        }
        HT(1);
#pragma unroll
        for (int ks = 0; ks < 4; ++ks) A[ks] = lds_r16(LB + C_MEM + ks * 1024);
        SCHED();
        st4s(rs, ofl, 0, sv);
        SCHED();
        static_for<0, 76>(slot);
        asm volatile("s_nop 2\n\tv_add_f32 %1, %3, %5\n\tv_add_f32 %2, %4, %6\n\tv_cvt_pk_bf16_f32 %0, %1, %2"
                     : "=v"(vm.y), "=&v"(fc), "=&v"(fd) : "v"(av1[0]), "v"(av1[1]), "v"(av1[2]), "v"(av1[3]));
        __builtin_amdgcn_s_waitcnt(0xCF7F);
        asm volatile("s_nop 0\n\tv_mfma_f32_16x16x16_bf16 %0, %2, %3, %0\n\tv_rcp_f32 %1, %4" : "+v"(oa), "=&v"(inv) : "v"(vm), "v"(pm), "v"(rsum[0]), "v"(rsum));
        SCHED();
        if (__builtin_expect(__builtin_amdgcn_ballot_w64(u_(rsum[0]) - 0x21800000u > 0x3C000000u) != 0ull, 0)) {
            DRAIN(); SCHED();
            bool miss = false;
#pragma unroll 1
            for (int i = 0; i < 10; ++i) { const v4u a = lds_r16(LB + C_MEM + i * 1024); miss |= a.x == PSENT || a.y == PSENT || a.z == PSENT || a.w == PSENT; }
            if (missed < SPIN_CAP && !all_(!miss)) {
                *(LAS u32*)(lds + C_DUMP + (w == 0 ? 0 : 64)) = ld4(rs, foff(F_ERR));
                asm volatile("" : "+v"(fl) :: "memory");
                tm = rdl(fl, 5);
                BAR();
                missed = uu(*(LAS u32*)(lds + C_DUMP)) != 0u ? SPIN_CAP : missed + 1;
                st4s(rs, missed == SPIN_CAP ? foff(F_ERR) : OOB, 0, 1u);
                BAR();
                continue;
            }
            float mx = fmaxf(fmaxf(fmaxf(sx[0], sx[1]), fmaxf(sx[2], sx[3])), fmaxf(sm[0], sm[1]));
            mx = max32(max16(fmaxf(mx, dpp<0x4E>(mx))));
#pragma unroll
            for (int i = 0; i < 4; ++i) t[i] = __builtin_amdgcn_exp2f(sx[i] - mx);
            e01 = pk(t[0], t[1]); e23 = pk(t[2], t[3]);
            pmm = pk(__builtin_amdgcn_exp2f(sm[0] - mx), __builtin_amdgcn_exp2f(sm[1] - mx));
            asm volatile("v_cndmask_b32 %0, %2, 0, %3\n\tv_cndmask_b32 %1, 0, %2, %3" : "=&v"(pm.x), "=&v"(pm.y) : "v"(pmm), "s"(himask));
            asm volatile("s_nop 1" : "+v"(e01), "+v"(e23), "+v"(pm));
            QPERM(P.x, e01, "[0,1,0,1]"); QPERM(P.z, e01, "[2,3,2,3]"); QPERM(P.y, e23, "[0,1,0,1]"); QPERM(P.w, e23, "[2,3,2,3]");
            NOPS(1);
            MF0(rsum, ones, P); MF0(oa, vx[0], P); MF0(ob, vx[1], P);
            NOPS(7);
            { const v2u ones2 = ones.lo; MF1H(rsum, ones2, pm); }
#pragma unroll
            for (int i = 0; i < 4; ++i) SEL(oa[i], oa[i], ob[i]);
            NOPS(1);
            MF1H(oa, vm, pm);
            asm volatile("s_nop 7\n\tv_rcp_f32 %0, %1" : "=v"(inv) : "v"(rsum[0]), "v"(rsum));
        }
        {
            v4u oc; f32x2 u0 = oa.lo, u1 = oa.hi; const f32x2 i2 = {inv, inv};
            asm volatile("s_nop 0\n\tv_pk_mul_f32 %0, %0, %2\n\tv_pk_mul_f32 %1, %1, %2" : "+v"(u0), "+v"(u1) : "v"(i2));
            __builtin_amdgcn_s_waitcnt(0xCF7F);
            asm volatile("v_cvt_pk_bf16_f32 %0, %4, %5\n\tv_cvt_pk_bf16_f32 %2, %4, %5\n\tv_cvt_pk_bf16_f32 %1, %6, %7\n\tv_cvt_pk_bf16_f32 %3, %6, %7\n\t"
                         "v_permlane16_swap_b32 %0, %2\n\ts_nop 0\n\tv_permlane16_swap_b32 %1, %3"
                         : "=&v"(oc.x), "=&v"(oc.y), "=&v"(oc.z), "=&v"(oc.w) : "v"(u0.x), "v"(u0.y), "v"(u1.x), "v"(u1.y));
            st16(rs, oso, oc);
        }
        SCHED();
        unsigned long long clk;
        asm volatile("s_memrealtime %0" : "=s"(clk) :: "memory");
        HT(6);
        v4u tq[4], tk[8], tv[2];
        f32x4 c0, c1, cm;
        u32 oo, kvo; bool kvok;
        u32 ln = l16; PIN(ln);
        lptr QX = lds + C_QX + (w & 1) * 4096 + ln, VO = lds + C_V + (w ^ 1) * 2048 + ln, VMX = lds + C_VM + w * 512 + (ln >> 1);
#pragma unroll
        for (int i = 0; i < 2; ++i) { tq[i] = lds_r16(QX + i * 1024); kmf[i] = lds_r16(qr + (C_KM - C_Q) + i * 64); tk[2 * i] = lds_r16(LB + C_K + i * 1024); tk[2 * i + 1] = lds_r16(LB + C_K + 4096 + i * 1024); }
        *(LAS v2u*)VMX = vm;
        SCHED();
        SFOR(i, 12)
            if constexpr (i == 0) MF0(cm, kmf[0], tq[0]); else if constexpr (i == 1) MF0(c0, tk[0], tq[0]); else if constexpr (i == 2) MF0(c1, tk[1], tq[0]);
            else if constexpr (i % 3 == 0) MF1(cm, kmf[i / 3], tq[i / 3]); else if constexpr (i % 3 == 1) MF1(c0, tk[2 * (i / 3)], tq[i / 3]); else MF1(c1, tk[2 * (i / 3) + 1], tq[i / 3]);
            if constexpr (i < 2) kmf[2 + i] = lds_r16(qr + (C_KM - C_Q) + (2 + i) * 64);
            if constexpr (i == 0) { tq[2] = lds_r16(QX + 2048); tk[4] = lds_r16(LB + C_K + 2048); }
            if constexpr (i == 1) { tk[5] = lds_r16(LB + C_K + 6144); tq[3] = lds_r16(QX + 3072); }
            if constexpr (i == 2) { tk[6] = lds_r16(LB + C_K + 3072); tk[7] = lds_r16(LB + C_K + 7168); }
            if constexpr (i == 3) { tv[0] = lds_r16(VO); tv[1] = lds_r16(VO + 1024); }
            if constexpr (i == 4) { oo = ln + (ln & 0x300u) + ((vsrc ^ 8192u) + (u32)((w & 1) * 256) + ((u32)s & OOB)); PIN(oo); }
            if constexpr (i == 5) { ksrc += NH * 16384; vsrc += NH * 16384; PINS(ksrc); PINS(vsrc); }
            if constexpr (i == 9) { asm volatile("s_waitcnt vmcnt(1)" : "+v"(fl) :: "memory"); kvok = ((u32)__builtin_amdgcn_ballot_w64(fl < (u32)s + 2) & 7u) == 0u; }
            if constexpr (i == 10) { kvo = l16 | (kvok ? 0u : OOB); PIN(kvo); }
            SCHED();
        SEND
        asm volatile("s_waitcnt lgkmcnt(0)\n\ts_barrier" : "+s"(clk) :: "memory");
        const v2u tvm = *(LAS v2u*)(VMX + (512 - (w & 1) * 1024));
        const v4u pv = lds_r16(ZP + 80);
        DMA32(dk, kvo, ksrc);
        SCHED(); asm volatile("" :: "v"(cm));
        float u[10], m, m2;
        {
            float NEG = -1000000000.0f / SCALE; PIN(NEG);
#define MSK(d, a, i) asm volatile("v_cndmask_b32 %0, %1, %2, %3" : "=v"(d) : "v"(NEG), "v"(a), "s"(vis[i]))
#pragma unroll
            for (int i = 0; i < 4; ++i) MSK(u[i], c0[i], i);
#pragma unroll
            for (int i = 0; i < 4; ++i) MSK(u[4 + i], c1[i], 4 + i);
            asm volatile("v_max3_f32 %0, %2, %3, %10\n\tv_max3_f32 %1, %4, %5, %11\n\tv_max3_f32 %0, %0, %6, %7\n\tv_max3_f32 %1, %1, %8, %9\n\t"
                         "v_max_f32 %0, %0, %1\n\tv_mov_b32 %1, %0\n\ts_nop 1\n\tv_permlane16_swap_b32 %0, %1\n\tv_max_f32 %0, %0, %1\n\tv_mov_b32 %1, %0\n\ts_nop 1\n\t"
                         "v_permlane32_swap_b32 %0, %1\n\tv_max_f32 %0, %0, %1\n\tv_mul_f32 %0, 0xbe0293ee, %0"
                         : "=&v"(m), "=&v"(m2) : "v"(u[0]), "v"(u[1]), "v"(u[2]), "v"(u[3]), "v"(u[4]), "v"(u[5]), "v"(u[6]), "v"(u[7]), "v"(cm[0]), "v"(cm[1]));
        }
        DMA32(dv, kvo, vsrc);
        {
            v4u px; v2u p0, p1;
#define EX2(d, a, b) asm volatile("v_fmamk_f32 %0, %2, 0x3e0293ee, %4\n\tv_fmamk_f32 %1, %3, 0x3e0293ee, %4\n\tv_exp_f32 %0, %0\n\tv_exp_f32 %1, %1" : "=&v"(d.x), "=&v"(d.y) : "v"(a), "v"(b), "v"(m))
            f32x2 ex[5];
            EX2(ex[0], u[0], u[1]); EX2(ex[1], u[2], u[3]); EX2(ex[2], u[4], u[5]); EX2(ex[3], u[6], u[7]); EX2(ex[4], cm[0], cm[1]);
            asm volatile("v_cvt_pk_bf16_f32 %0, %8, %9\n\tv_cvt_pk_bf16_f32 %1, %10, %11\n\tv_cvt_pk_bf16_f32 %2, %12, %13\n\tv_cvt_pk_bf16_f32 %3, %14, %15\n\t"
                         "v_mov_b32 %5, 0\n\tv_mov_b32 %6, 0\n\tv_cvt_pk_bf16_f32 %4, %16, %17\n\tv_cvt_pk_bf16_f32 %7, %16, %17"
                         : "=&v"(px.x), "=&v"(px.y), "=&v"(px.z), "=&v"(px.w), "=&v"(p0.x), "=&v"(p0.y), "=&v"(p1.x), "=&v"(p1.y)
                         : "v"(ex[0].x), "v"(ex[0].y), "v"(ex[1].x), "v"(ex[1].y), "v"(ex[2].x), "v"(ex[2].y), "v"(ex[3].x), "v"(ex[3].y), "v"(ex[4].x), "v"(ex[4].y));
            f32x4 o[4], sum;
            const v2u ones2 = ones.lo;
            const u32 now = (u32)clk;
            u32 oz;
            SCHED();
            MF0(sum, ones, px); { const u32 q = ksrc ^ 8192u; DMA32(dq, kvo, q); } SCHED();
            MF0(o[0], vx[0], px);
            MF0(o[1], vx[1], px);
            MF0(o[2], tv[0], px); oz = (oso & ~(3u << 17)) | (u32)((s + 3) & 3) << 17; PIN(oz); SCHED();
            MF0(o[3], tv[1], px); oso = (oso & ~(3u << 17)) | (u32)((s + 1) & 3) << 17; PIN(oso); SCHED();
            MF1H(sum, ones2, p0);
            MF1H(o[0], vm, p0);
            MF1H(o[1], vm, p1); st16(rs, oz, pv); SCHED();
            MF1H(o[2], tvm, p0);
            MF1H(o[3], tvm, p1);
            asm volatile("s_nop 2\n\tv_rcp_f32 %0, %1\n\ts_nop 0" : "=v"(inv) : "v"(sum[0]), "v"(sum));
            asm volatile("" : "+v"(o[0]), "+v"(o[1]));
            st16(rs, oo, (v4u){pk2(o[0].lo * inv), pk2(o[0].hi * inv), pk2(o[1].lo * inv), pk2(o[1].hi * inv)});
            asm volatile("" : "+v"(o[2]), "+v"(o[3]));
            st16(rs, oo ^ 2048u, (v4u){pk2(o[2].lo * inv), pk2(o[2].hi * inv), pk2(o[3].lo * inv), pk2(o[3].hi * inv)});
            SCHED();
            LS2(L == 0 && w == 0, h);
            st4s(rs, (h == 0 && tid == 192) ? hint + 128u : OOB, 0, (u32)s << 16 | min(missed, 1u) << 14 | min(now - tm, 0x3FFFu));
            tm = rdl(fl, 5);
            missed = missed == SPIN_CAP ? missed : 0u;
            if (__builtin_expect((int)(rdl(fl, 3) - now) < CH_VIS - 4 || s < 1 - L, 0)) {
                const bool first = s == 0 && L == 0;
                if (s < 0) oso -= (ln & 256u) ? 0u : OOB;
#pragma unroll 1
                for (int n = 0; n < (s < 0 ? 0 : first ? 200 : 8); ++n) {
                    CFENCE();
                    const u32 hv = ld4(rs, fkh);
                    tm = rdl(hv, first ? 6 : 5);
                    if (first ? tm != PSENT : (int)(rdl(hv, 3) - now) >= CH_VIS - 4) break;
                }
            }
        }
        if (__builtin_expect(!kvok, 0) && s + 1 < NSEG) {
            st4s(rs, ln == 0 ? hint + 4096u + (u32)h * 128u : OOB, 0, (u32)s + 1u);
            keys_wait((u32)s + 2u);
        }
        ++s;
    }
    DRAIN(); BAR();
    st4s(rs, ofl, 0, NSEG);
}

#define WO_NL 3
#define WO_ROT 0
#define WO_AH 1
DEV void role_wo(const Params& p, lptr lds, int j, v2u ep) {
    const int tid = threadIdx.x, lane = tid & 63, w = uni(tid >> 6), g = lane >> 4, r = lane & 15;
    const int rg = j & 1, cg = j >> 1, rot = WO_ROT;
    const rsrc_t rs = mk_rsrc(p.ws, W_END);
    v8u wo[8][5];
    {
        const v4u so_ = mk_srd(p.Wo, DM * NQ * 2);
        u32 vo[5];
#pragma unroll
        for (int i = 0; i < 5; ++i) { const int n = 64 * i + lane; vo[i] = (u32)((n / 5) * DM + cg * 40 + (n % 5) * 8) * 2u; }
        v4u junk = {};
        const u32 av = (u32)(j * 5120 + lane * 16);
        auto ahead = [&](int b) {
#pragma unroll
            for (int i = 0; i < 5; ++i) AHEAD(so_, av, uu((8 * w + b) * (DH * DM * 2) + i * 1024));
        };
        auto fill = [&](int b) {
            if (b + WO_AH < 8) ahead(b + WO_AH);
            const u32 dst = uu(w * 40960 + (b & 3) * 10240), src = uu((8 * w + ((b + rot) & 7)) * (DH * DM * 2));
#pragma unroll
            for (int i = 0; i < 10; ++i) DMAIN(so_, dst + i * 1024, vo[i % 5], src + (i / 5) * (64 * DM * 2));
        };
#pragma unroll 1
        for (int b = 0; b < WO_AH; ++b) ahead(b);
#pragma unroll 1
        for (int b = 0; b < 4; ++b) fill(b);
        const int q4 = r >> 2;
        lptr tb = lds + w * 40960 + (32 * (g >> 1) + 16 * (q4 >> 1) + 8 * ((r >> 1) & 1) + 4 * (g & 1) + (q4 & 1)) * 80 + (r & 1) * 8;
        SFOR(b, 8)
            VMW((b < 5 ? 30 : 70 - 10 * b) + (b + 1 + WO_AH < 8 ? 5 : 0) + (b + 2 + WO_AH < 8 ? 5 : 0) + (b + 3 + WO_AH < 8 ? 5 : 0));
            if constexpr (b + WO_AH == 7) PIN(junk);
#pragma unroll
            for (int t = 0; t < 5; ++t) {
                lptr q = tb + (b & 3) * 10240 + t * 16;
                wo[b][t] = tr_frag(q, q + 160, q + 64 * 80, q + 64 * 80 + 160);
                if (b * 5 + t < 32) asm volatile("" : "+a"(wo[b][t])); else asm volatile("" : "+v"(wo[b][t]));
            }
            CFENCE();
            if constexpr (b < 4) fill(b + 4);
        SEND
    }
    if (j == 0) LST(265, rt32());
    LST(1100 + j * 4 + w, rt32());
    const u32 idx = (r & 2) ? 0xEEEEu : 0x4444u;
    const u32 pol0 = pol_wait(rs, ep);
    BAR();
    const u32 l16 = (u32)lane * 16u;
    u32 oo[8];
#pragma unroll
    for (int i = 0; i < 8; ++i) oo[i] = uu(W_ORING + ring_map((u32)(rg * 32768 + (8 * w + ((i + rot) & 7)) * 1024), pol0));
    const u32 hint = W_MRING + 4 * 32768u + pol0 * 8192u;
    u32 hvo[2];
#pragma unroll
    for (int l = 0; l < 2; ++l) hvo[l] = lane < 2 ? hint + (u32)(l ^ 1) * (MB / 4) + (u32)lane * 4u : lane == 2 ? hint + (u32)l * (MB / 4) + 128u : lane == 3 ? foff(F_ERR) :
                 (lane >= 32 && j == 0 && w == 3) ? hint + (u32)l * (MB / 4) + (u32)lane * 128u : OOB;
    lptr xw = r < 8 ? lds + w * 640 + (2 * (g & 1) + (g >> 1)) * 32 + r * 4 : lds + 8192 + lane * 4;
    const int G = ((5 * cg) >> 1) + w, tt = 2 * G + ((lane >> 3) & 1) - 5 * cg;
    const bool pub = w < 3 && lane < 32 && tt >= 0 && tt < 5;
    lptr xr = lds + (pub ? tt * 128 + (2 * (lane >> 4) + ((lane >> 2) & 1)) * 32 + (lane & 3) * 8 : 0);
    const u32 mpo = pub ? W_MRING + ring_map((u32)(G * 256 + rg * 128 + lane * 4), pol0) : OOB;
    constexpr u32 PER0 = CH_W0 + CH_MW + CH_H0 + CH_MH + 2 * CH_VIS, DW0 = CH_W0 + CH_VIS + CH_MW + CH_REC;
    const bool lead = j == 0 && w == 3;
    const u32 hso = (lead && lane == 0) ? hint : OOB;
    MK_SRD();
    u32 t = 0, tn = 0;
    int sn[2] = {0, 0};
    u32 hav = 4 * CH_H0, wav = 4 * CH_W0, mh = 8 * CH_MH, mw = 8 * CH_MW, ok[2] = {0u, 0u}, dw[2] = {DW0, DW0};
    u32 vo = l16, early = PSENT, tp = 0, fin, left, cnt = 0;
    unsigned long long clk = 0;
    {
        if (w == 0) LST(1310 + j, rt32());
        if (lead) {
#pragma unroll 1
            for (int n = 0; n < SPIN_CAP; ++n) {
                CFENCE();
                const u32 a = ld4(rs, foff(F_QKV + lane)), b = ld4(rs, lane < 32 ? foff(F_QKV + 64 + lane) : hvo[0]);
                if (all_(a != 0u && b != (lane < 32 ? 0u : PSENT))) break;
                __builtin_amdgcn_s_sleep(1);
            }
        }
        if (lead) {
            t = rt32() + CH_T0; tn = t + PER0 / 2 + CH_REC;
            st16(rs, lane < 2 ? hint + (u32)lane * (MB / 4) : OOB, (v4u){lane == 0 ? t : tn, PER0, (lane == 0 ? t : tn) + DW0, t + DW0});
        } else {
#pragma unroll 1
            for (int n = 0; n < SPIN_CAP; ++n) {
                CFENCE();
                const u32 hv = ld4(rs, hvo[1]);
                t = rdl(hv, 0);
                if (rdl(hv, 1) < 400u) break;
                __builtin_amdgcn_s_sleep(1);
            }
        }
        clk = t - 100u;
    }
    const u32 tm0 = t + DW0;
    u32 zero = 0, hv = 0xFFFFu, pst = 1, pmp = OOB;
    const u32 psent = PSENT;
    lptr sw = lds + 2560 + w * 4;
    asm volatile("" : "+v"(zero), "+v"(sw));
    auto turn = [&](auto lc) __attribute__((always_inline)) {
        constexpr int L = decltype(lc)::value, M = L ^ 1;
        u32 done, rep, rd, lost, dh, dn, nt, za, pdur, mp; v4u hw;
        const int sc = sn[L];
        auto books = [&](auto ic) __attribute__((always_inline)) {
            constexpr int i = decltype(ic)::value;
            if constexpr (i == 0) { rep = rdl(hv, 2); PINS(rep); }
            if constexpr (i == 1) { lost = (u32)(__builtin_amdgcn_ballot_w64(hv == (u32)sn[M]) >> 32); PINS(lost); }
            if constexpr (i == 2) { lost |= (rep >> 16) ^ (u32)sn[M]; PINS(lost); }
            if constexpr (i == 3) { lost |= ok[M] ^ 2u; rd = rep & 0xFFFFu; PINS(lost); PINS(rd); }
            if constexpr (i == 4) { lost = lost == 0u ? CH_UP * 8u : 0u; PINS(lost); }
            if constexpr (i == 5) { dn = wav >> 2; PINS(dn); pdur = min((u32)clk - tp, dn + 8u); PINS(pdur); }
            if constexpr (i == 6) { pdur = (done & ok[M]) ? pdur : dn; PINS(pdur); }
            if constexpr (i == 7) { wav += pdur - dn; PINS(wav); }
            if constexpr (i == 8) { dh = hav >> 2; PINS(dh); nt = min(rd, dh + 8u); PINS(nt); }
            if constexpr (i == 9) { nt = rd < 0x3FFFu ? nt : dh; PINS(nt); }
            if constexpr (i == 10) { hav += nt - dh; PINS(hav); }
            if constexpr (i == 11) { mw = max(mw - 1u, CH_MW * 8u); PINS(mw); }
            if constexpr (i == 12) { mh = max(mh - 1u, CH_MH * 8u); PINS(mh); }
            if constexpr (i == 13) { rd = rd >> 14 == 1u ? lost : 0u; PINS(rd); }
            if constexpr (i == 14) { mw += rd; mh += lost - rd; PINS(mw); PINS(mh); }
            if constexpr (i == 15) { ok[M] = done ? 1u : (ok[M] & 1u) << 1; PINS(ok[M]); }
            if constexpr (i == 16) { dh = (2u * hav + mh) / 8u + (sn[1] < 3 ? CH_VIS + CH_PRE : CH_VIS); PINS(dh); }
            if constexpr (i == 17) { dn = (2u * wav + mw) / 8u + CH_VIS; PINS(dn); }
            if constexpr (i == 18) { nt = dw[L] + dh + (u32)max((int)((u32)clk + CH_WX - t), 0); PINS(nt); }
            if constexpr (i == 19) { dh += dn; dw[L] = dn; sn[M] += (int)done; PINS(dh); PINS(sn[M]); }
            if constexpr (i == 20) { rd = tn - t + (dh >> 1); PINS(rd); }
            if constexpr (i == 22) { nt = t + (u32)max((int)nt, (int)rd); PINS(nt); }
            if constexpr (i == 23) { hw.x = nt; hw.y = dh; PIN(hw.x); PIN(hw.y); }
            if constexpr (i == 24) { hw.z = nt + dn; hw.w = tm0; PIN(hw); }
            if constexpr (i == 25) st16(rs, hso + L * (MB / 4), hw);
        };
        {
#define WT(k) do {} while (0)
            WT(0);
            v4u d[8];
            {
                u32 tmp;
                asm volatile(WAIT_TXT("%1", "%2") "buffer_load_dwordx4 %0, %3, %4, %5 offen sc1\n\ts_waitcnt lgkmcnt(0)"
                             : "=&v"(d[0]), "=&s"(tmp) : "s"(uu(t - 4u)), "v"(vo), "s"(srd), "s"(oo[0]) : "vcc", "scc", "memory");
                PINS(clk);
            }
            SFOR(i, 7)
                asm volatile("buffer_load_dwordx4 %0, %1, %2, %3 offen sc1" : "=v"(d[i + 1]) : "v"(vo), "s"(srd), "s"(oo[i + 1]) : "memory");
                SCHED();
                if constexpr (i == 0) { done = uu(pst) ^ 1u; PINS(done); }
                static_for<3 * i, 3 * i + 3>(books);
                SCHED();
            SEND
            LD4A(hv, hvo[L], 0);
            SCHED();
            WT(1);
            static_for<22, 26>(books);
            za = done ? pmp : OOB;
            st4s(rs, za, 0, psent);
            *(LAS u32*)(sw + L * 4096) = zero;
            f32x4 acc[5] = {};
            mp = mpo + (L * (MB / 4) + (u32)((sc + 1) & 3) * 32768u); pmp = mpo + (L * (MB / 4) + (u32)(sc & 3) * 32768u);
            asm volatile("" : "+v"(acc[0]), "+v"(acc[1]), "+v"(acc[2]), "+v"(acc[3]), "+v"(acc[4]), "+v"(mp), "+v"(pmp));
            SCHED();
            float f0[5], f1[5]; unsigned long long bad;
#define WSW(o_) asm volatile("v_permlane32_swap_b32 %0, %1" : "+v"(f0[o_]), "+v"(f1[o_]))
#define WAD(o_) asm volatile("v_add_f32_dpp %0, %1, %0 row_ror:8 row_mask:0xf bank_mask:0xf" : "+v"(f0[o_]) : "v"(f1[o_]))
#define WFO(o_) do { VADD(f0[o_], acc[o_][0], acc[o_][2]); VADD(f1[o_], acc[o_][1], acc[o_][3]); } while (0)
#define WWR(o_) *(LAS float*)(xw + L * 4096 + (o_) * 128) = f0[o_]
            u32 th, per, so, erl = early;
            auto next = [&](auto ic_) __attribute__((always_inline)) {
                constexpr int i = decltype(ic_)::value;
                if constexpr (i == 0) { so = (u32)sn[M] << 17; PINS(so); }
                if constexpr (i == 1) { so = (so & 0x60000u) + M * (MB / 2); PINS(so); }
                if constexpr (i == 2) { so = sn[M] + 1 < NSEG ? so : OOB; PINS(so); }
                if constexpr (i == 3) { early = sn[M] + 1 < NSEG ? PSENT : 0u; PIN(early); }
                if constexpr (i == 4) { vo = l16 + so; PIN(vo); }
                if constexpr (i == 5 && L) { fin = (u32)(sn[0] + sn[1] + 2); PINS(fin); }
                if constexpr (i == 6 && L) { fin = (fin | uu(left)) >> 7; PIN(fin); }
                if constexpr (i == 6 && !L) { cnt += 1u; PIN(cnt); left = cnt >> 9; PIN(left); }
                if constexpr (i == 7) { PIN(hv); th = rdl(hv, 0); PINS(th); }
                if constexpr (i == 8) { per = rdl(hv, 1); PINS(per); so = th - t; PINS(so); }
                if constexpr (i == 9) { per = (int)so < 20 ? per : 0u; PINS(per); }
                if constexpr (i == 10) { th += per; PINS(th); if constexpr (L) { per = rdl(hv, 3); PINS(per); } }
                if constexpr (i == 11) { tp = t; t = lead ? tn : th; PINS(t); }
                if constexpr (i == 12) { if constexpr (L) { fin |= per; PIN(fin); } tn = nt; }
            };
            auto slot = [&](auto ic_) __attribute__((always_inline)) {
                constexpr int i = decltype(ic_)::value;
                constexpr int NL = WO_NL, I0 = 40 - 5 * NL, pc = i < I0 ? i / 5 : 8 - NL + (i - I0) % NL, oc = i < I0 ? i % 5 : (i - I0) / NL;
                if constexpr (oc == 0) asm volatile("s_waitcnt vmcnt(%1)" : "+v"(d[pc]) : "i"(10 - pc));
                if constexpr (pc * 5 + oc < 32) SMF_A(acc[oc], d[pc], wo[pc][oc]); else SMF_V(acc[oc], d[pc], wo[pc][oc]);
                SCHED();
                if constexpr (i == 0) WT(2);
                if constexpr (i >= 5 && i < 13 && i != 9) next(IC<i - 5 - (i > 9)>{});
                if constexpr (i == 20) WT(4);
                constexpr int e = i - (I0 + NL + 1), o = e < 0 ? 0 : e / 3;
                if constexpr (e >= 0 && e % 3 == 0) { CADD(f0[o], acc[o][0], acc[o][2]); if constexpr (o > 0) WAD(o - 1); }
                if constexpr (e >= 0 && e % 3 == 1) { if constexpr (o > 0) { WWR(o - 1); SCHED(); } CADD(f1[o], acc[o][1], acc[o][3]); }
                if constexpr (e >= 0 && e % 3 == 2) {
                    if constexpr (o == 0) asm volatile("v_cmp_u_f32 %0, %1, %2" : "=s"(bad) : "v"(f0[0]), "v"(f1[0]));
                    else LGK(15);
                    WSW(o);
                }
                SCHED();
            };
            asm volatile(".rept %0\n\ts_nop 0\n\t.endr" :: "i"(AL_W));
            static_for<0, 2>(slot);
            if (__builtin_expect(__builtin_amdgcn_ballot_w64(d[0].x == erl) != 0ull, 0)) {
                asm volatile("s_waitcnt vmcnt(3)" :: "v"(d[1]), "v"(d[2]), "v"(d[3]), "v"(d[4]), "v"(d[5]), "v"(d[6]), "v"(d[7]) : "memory");
                SCHED();
                *(LAS u32*)(sw + L * 4096) = 1u;
                static_for<0, 7>(next);
            } else {
            static_for<2, 40>(slot);
            WT(5);
            static_assert(WO_NL == 3, "tail");
            if (__builtin_expect(bad != 0ull, 0)) {
                bool miss = false;
#pragma unroll
                for (int i = 0; i < 8; ++i) miss |= d[i].x == PSENT;
                if (all_(!miss)) {
#pragma unroll
                    for (int i = 0; i < 8; ++i) miss |= d[i].y == PSENT || d[i].z == PSENT || d[i].w == PSENT;
                }
                *(LAS u32*)(sw + L * 4096) = all_(!miss) ? 0u : 1u;
            }
            asm volatile("v_permlane32_swap_b32 %0, %1\n\ts_nop 1\n\tv_add_f32_dpp %0, %1, %0 row_ror:8 row_mask:0xf bank_mask:0xf\n\tv_add_f32 %2, %4, %6\n\tv_add_f32 %3, %5, %7"
                         : "+v"(f0[3]), "+v"(f1[3]), "=&v"(f0[4]), "=&v"(f1[4]) : "v"(acc[4][0]), "v"(acc[4][1]), "v"(acc[4][2]), "v"(acc[4][3]));
            WWR(3); SCHED();
            asm volatile("s_nop 0\n\tv_permlane32_swap_b32 %0, %1\n\ts_nop 1\n\tv_add_f32_dpp %0, %1, %0 row_ror:8 row_mask:0xf bank_mask:0xf" : "+v"(f0[4]), "+v"(f1[4]));
            WWR(4);
            }
            WT(6);
            f32x2 a, b, c, e; u32 mq; v4u sv;
            asm volatile("s_waitcnt lgkmcnt(0)\n\ts_barrier\n\tds_read_b128 %4, %6 offset:%7+2560\n\t"
                         "ds_read_b64 %0, %5 offset:%7\n\tds_read_b64 %1, %5 offset:%7+640\n\tds_read_b64 %2, %5 offset:%7+1280\n\tds_read_b64 %3, %5 offset:%7+1920"
                         : "=&v"(a), "=&v"(b), "=&v"(c), "=&v"(e), "=&v"(sv) : "v"(xr), "v"(zero), "i"(L * 4096) : "memory");
            asm volatile("s_waitcnt lgkmcnt(4)\n\tv_or_b32 %4, %6, %7\n\tv_or3_b32 %4, %4, %8, %9\n\tv_lshl_or_b32 %5, %4, 31, %10\n\t"
                         "s_waitcnt lgkmcnt(2)\n\tv_pk_add_f32 %0, %0, %1\n\ts_waitcnt lgkmcnt(0)\n\tv_pk_add_f32 %2, %2, %3\n\ts_nop 0\n\tv_pk_add_f32 %0, %0, %2"
                         : "+v"(a), "+v"(b), "+v"(c), "+v"(e), "=&v"(pst), "=&v"(mq) : "v"(sv.x), "v"(sv.y), "v"(sv.z), "v"(sv.w), "v"(mp) : "memory");
            st4s(rs, mq, 0, pkv(a));
            SCHED();
            asm volatile("s_memrealtime %0" : "=s"(clk));
            VMW(3); SCHED();
            static_for<7, 13>(next);
            WT(7);
        }
    };
    do {
        turn(IC<0>{});
        turn(IC<1>{});
    } while (uu(fin) == 0u);
    if (sn[0] + 1 < NSEG || sn[1] + 1 < NSEG) st4s(rs, tid == 0 ? foff(F_ERR) : OOB, 0, 1u);
    if (w == 0) LST(1900 + j, rt32());
}

__global__ void __launch_bounds__(256, 1) k_main(Params p) {
    lptr lds = (lptr)(u32)16 - 16;
    if (blockIdx.x == 0) LST(256, rt32());
    unsigned long long pc; int len;
    asm volatile("s_getpc_b64 %0\n.Lk_main_here:\n\ts_mov_b32 %1, .Lk_end_here-.Lk_main_here" : "=s"(pc), "=s"(len));
    const rsrc_t rs = mk_rsrc(p.ws, W_END);
    const v2u e0 = ld8s(rs, threadIdx.x == 0 ? foff(F_EPOCH) : OOB, 0);
    const v4u code = __builtin_amdgcn_raw_buffer_load_b128(mk_rsrc((const void*)(pc & ~15ull), (u32)max(len, 0)), (((blockIdx.x >> 3) & 31u) * 256u + threadIdx.x) * 16u, 0, 0);
    CFENCE();
    auto conv_id = [](int role) {
        const int x = role >> 5, i = role & 31, no = x < 5 ? 5 : 10, og = x < 5 ? x - 3 : 2 * (x - 4) + (i >= 5), r = (x - 3) * 5 + i - no, q = r / 3;
        return x < 3 || i >= no + 5 || r >= 24 ? NCV : i < no ? ((og & 1) ? (og >> 1) * 5 + i % 5 : NCV) : r % 3 == 0 ? 20 + (q & 1) * 20 + (q >> 1) : 16 + (r % 3) * 8 + q;
    };
    int role = (int)(((u32)__builtin_amdgcn_s_getreg(20 | (3 << 11)) & 7u) * 32u + (blockIdx.x >> 3));
    Early e;
    v4u rv;
#pragma unroll 1
    for (int n = 0;; ++n) {
        conv_early(p, rs, conv_id(role), e);
        if (n) break;
        CFENCE();
        if (threadIdx.x == 0) {
            GAS unsigned long long* slots = (GAS unsigned long long*)(p.ws + W_CTRL + F_SLOT * 128);
            const unsigned long long tag = (unsigned long long)e0.y << 32 | (e0.x + 1u);
            u32 mine = (u32)role;
#pragma unroll 1
            for (int k = 0; k < 256 && __hip_atomic_exchange(slots + mine * 16, tag, __ATOMIC_RELAXED, __HIP_MEMORY_SCOPE_AGENT) == tag; ++k) mine = (mine + 1u) & 255u;
            lds_w16(lds, (v4u){mine, e0.x, e0.y, 0u});
        }
        BAR();
        rv = lds_r16(lds);
        BAR();
        if (uni((int)rv.x) == role) break;
        role = uni((int)rv.x);
    }
    const int x = role >> 5, i = role & 31, w = uni(threadIdx.x >> 6), no = x < 5 ? 5 : 10, nr = x < 7 ? 5 : 4, j = conv_id(role);
    const v2u ep = {uu(rv.y), uu(rv.z)};
    asm volatile("" :: "v"(code));
    if (x < 2) { role_head(p, lds, i >> 4, x * 16 + (i & 15), ep); __builtin_amdgcn_endpgm(); }
    if (x == 2) { role_wo(p, lds, i, ep); __builtin_amdgcn_endpgm(); }
    if (i >= no + nr) { role_qkv(p, lds, (x < 5 ? (x - 3) * 22 : 44 + (x - 5) * 17) + i - no - nr, ep); __builtin_amdgcn_endpgm(); }
    const bool cv = j >= NPRE || (j >= 20 && j < 24);
    if (j < NCV) conv_stage(p, rs, lds, j, (j & 3) * 2 + (j >= NPRE), cv ? 40 : 0, ep, e);
    if (i >= no) {
        if (!cv) role_row(p, lds, (j - 24) * 4 + w, ep);
        __builtin_amdgcn_endpgm();
    }
    const int og = x < 5 ? x - 3 : 2 * (x - 4) + (i >= 5);
    role_out(p, lds, og & 1, og >> 1, i % 5, ep);
}
__global__ void k_end() { asm volatile(".Lk_end_here:"); }

extern "C" void kernel_launch(void* const* d_in, const int* in_sizes, int n_in, void* d_out, int out_size, void* d_ws, size_t ws_size,
                              hipStream_t stream) {
    Params p;
    p.x = (const u16*)d_in[0]; p.mem0 = (const u16*)d_in[1]; p.Wq = (const u16*)d_in[2]; p.Wk = (const u16*)d_in[3];
    p.Wv = (const u16*)d_in[4]; p.Wo = (const u16*)d_in[5];
    p.bk = (const u16*)d_in[8]; p.bv = (const u16*)d_in[9]; p.shift = (const u16*)d_in[10]; p.ssmk = (const u16*)d_in[11]; p.ssmD = (const u16*)d_in[12];
    p.out = (u16*)d_out;
    p.ws = (unsigned char*)(((size_t)d_ws + 2 * MB - 1) & ~(size_t)(2 * MB - 1));
    (void)hipFuncSetAttribute((const void*)k_main, hipFuncAttributeMaxDynamicSharedMemorySize, LDS_BYTES);
    hipLaunchKernelGGL(k_main, dim3(256), dim3(256), LDS_BYTES, stream, p);
}
```

```cpp
#include <hip/hip_runtime.h>
#include <hip/hip_bf16.h>

typedef unsigned short u16;
typedef unsigned int u32;
typedef u32 v2u __attribute__((ext_vector_type(2)));
typedef u32 v4u __attribute__((ext_vector_type(4)));
typedef u32 v8u __attribute__((ext_vector_type(8)));
typedef float f32x2 __attribute__((ext_vector_type(2)));
typedef float f32x4 __attribute__((ext_vector_type(4)));
typedef float f32x16 __attribute__((ext_vector_type(16)));
typedef __bf16 bf16x2 __attribute__((ext_vector_type(2)));
typedef __bf16 bf16x8 __attribute__((ext_vector_type(8)));
typedef short s16x4 __attribute__((ext_vector_type(4)));
#define LAS __attribute__((address_space(3)))
#define GAS __attribute__((address_space(1)))
typedef LAS unsigned char* lptr;
#define DEV __device__ __forceinline__

constexpr int SEQ = 2048, DM = 640, NH = 32, DH = 128, NSEG = 64, NQ = NH * DH;

constexpr u32 MB = 1u << 20;
constexpr u32 W_ORING = 0;
constexpr u32 W_MRING = MB;
constexpr u32 W_PROBE = 2 * MB - 65536;
constexpr u32 W_CTRL = 4 * MB;
constexpr u32 CTRL_BYTES = 128 * 1024;
constexpr u32 ACT = SEQ * DM * 2;
constexpr u32 W_H1 = 5 * MB, W_Y = W_H1 + ACT, W_H2 = W_Y + ACT, W_X2 = W_H2 + ACT;
constexpr u32 W_KQ = 16 * MB;
constexpr u32 W_VO = W_KQ + 64 * MB;
constexpr u32 W_P = W_VO + 64 * MB;
constexpr u32 P_ITEM = 32 * 4 * DM * 4;
constexpr u32 W_KVT = W_P + 2 * NSEG * P_ITEM;
constexpr u32 W_SP = W_KVT + ACT;
constexpr u32 W_END = W_KVT + 3 * MB;
static_assert((size_t)W_END + 2 * MB <= 256ull * MB, "workspace");
constexpr u32 F_ERR = 0, F_QKV = 16, F_O = F_QKV + 192, F_P = F_O + 64, F_H2 = F_P + 64, F_CLR = F_H2 + 32,
              F_EPOCH = F_CLR + 32, F_SLOT = F_EPOCH + 1, F_PRE = F_SLOT + 256, F_N = F_PRE + 192;
constexpr int NCV = 44, NPRE = 40;
static_assert(F_N * 128 <= CTRL_BYTES, "ctrl");
DEV u32 foff(u32 idx) { return W_CTRL + idx * 128u; }

constexpr u32 OOB = 0x80000000u;
constexpr u32 PSENT = 0x7F817F81u;
constexpr int SPIN_CAP = 40000;
constexpr int LDS_BYTES = 160 * 1024;

#define LS3(cond, slot, idx) do {} while (0)
#define LST(idx, v) do {} while (0)
#define LS2(cond, idx) do {} while (0)
struct Params {
    const u16 *x, *mem0, *Wq, *Wk, *Wv, *Wo, *bk, *bv, *shift, *ssmk, *ssmD;
    u16* out;
    unsigned char* ws;
};

typedef __amdgpu_buffer_rsrc_t rsrc_t;
DEV rsrc_t mk_rsrc(const void* p, u32 bytes) { return __builtin_amdgcn_make_buffer_rsrc((void*)p, 0, bytes, 0x00020000); }
DEV v4u ld16s(rsrc_t r, u32 off, u32 so) { return __builtin_amdgcn_raw_buffer_load_b128(r, off, so, 16); }
DEV v2u ld8s(rsrc_t r, u32 off, u32 so) { return __builtin_amdgcn_raw_buffer_load_b64(r, off, so, 16); }
DEV v4u ldin16(rsrc_t r, u32 off, u32 so) { return __builtin_amdgcn_raw_buffer_load_b128(r, off, so, 2); }
DEV v2u ldin8(rsrc_t r, u32 off, u32 so) { return __builtin_amdgcn_raw_buffer_load_b64(r, off, so, 2); }
DEV u32 ld4(rsrc_t r, u32 off) { return __builtin_amdgcn_raw_buffer_load_b32(r, off, 0, 16); }
DEV void st16(rsrc_t r, u32 off, v4u v) { __builtin_amdgcn_raw_buffer_store_b128(v, r, off, 0, 16); }
DEV void st8s(rsrc_t r, u32 off, u32 so, v2u v) { __builtin_amdgcn_raw_buffer_store_b64(v, r, off, so, 16); }
DEV void st4s(rsrc_t r, u32 off, u32 so, u32 v) { __builtin_amdgcn_raw_buffer_store_b32(v, r, off, so, 16); }
#define DRAIN() asm volatile("s_waitcnt vmcnt(0)" ::: "memory")
#define VMW(n) asm volatile("s_waitcnt vmcnt(%0)" :: "i"(n) : "memory")
#define BAR() asm volatile("s_waitcnt lgkmcnt(0)\n\ts_barrier" ::: "memory")
#define SCHED() __builtin_amdgcn_sched_barrier(0)
#define CFENCE() asm volatile("" ::: "memory")
#define NOPS(n) asm volatile("s_nop %0" :: "i"(n))
#define PIN(x) asm volatile("" : "+v"(x))
#define PINS(x) asm volatile("" : "+s"(x))

template <int V> struct IC { static constexpr int value = V; };
template <int I, int N, class F> DEV void static_for(F&& f) { if constexpr (I < N) { f(IC<I>{}); static_for<I + 1, N>(f); } }
#define SFOR(var, n) static_for<0, n>([&](auto ic_) __attribute__((always_inline)) { constexpr int var = decltype(ic_)::value;
#define SEND });

DEV u32 pk(float a, float b) { bf16x2 v = {(__bf16)a, (__bf16)b}; return __builtin_bit_cast(u32, v); }
DEV float lo_f(u32 u) { return __builtin_bit_cast(float, u << 16); }
DEV float hi_f(u32 u) { return __builtin_bit_cast(float, u & 0xffff0000u); }
DEV f32x2 up2(u32 u) { return (f32x2){lo_f(u), hi_f(u)}; }
DEV u32 pk2(f32x2 v) { return pk(v.x, v.y); }
DEV float rbf(float a) { return (float)(__bf16)a; }
DEV v4u lds_r16(lptr p) { return *(LAS v4u*)p; }
DEV void lds_w16(lptr p, v4u v) { *(LAS v4u*)p = v; }
DEV v2u lds_tr(lptr p) { return __builtin_bit_cast(v2u, __builtin_amdgcn_ds_read_tr16_b64_v4i16((LAS s16x4*)p)); }
DEV v4u tr2(lptr p0, lptr p1) { const v2u a = lds_tr(p0), b = lds_tr(p1); return (v4u){a.x, a.y, b.x, b.y}; }
DEV v8u cat8(v4u a, v4u b) { return __builtin_shufflevector(a, b, 0, 1, 2, 3, 4, 5, 6, 7); }
DEV f32x4 mfma16(v4u a, v4u b, f32x4 c) { return __builtin_amdgcn_mfma_f32_16x16x32_bf16(__builtin_bit_cast(bf16x8, a), __builtin_bit_cast(bf16x8, b), c, 0, 0, 0); }
DEV f32x16 mfma32(v4u a, v4u b, f32x16 c) { return __builtin_amdgcn_mfma_f32_32x32x16_bf16(__builtin_bit_cast(bf16x8, a), __builtin_bit_cast(bf16x8, b), c, 0, 0, 0); }

DEV float f_(u32 u) { return __builtin_bit_cast(float, u); }
DEV u32 u_(float f) { return __builtin_bit_cast(u32, f); }
template <int CTRL> DEV float dpp(float v) { return f_((u32)__builtin_amdgcn_update_dpp(0, (int)u_(v), CTRL, 0xf, 0xf, false)); }
DEV float max16(float v) { auto r = __builtin_amdgcn_permlane16_swap(u_(v), u_(v), false, false); return fmaxf(f_(r[0]), f_(r[1])); }
DEV float max32(float v) { auto r = __builtin_amdgcn_permlane32_swap(u_(v), u_(v), false, false); return fmaxf(f_(r[0]), f_(r[1])); }
DEV float add16(float v) { auto r = __builtin_amdgcn_permlane16_swap(u_(v), u_(v), false, false); return f_(r[0]) + f_(r[1]); }
DEV float add32(float v) { auto r = __builtin_amdgcn_permlane32_swap(u_(v), u_(v), false, false); return f_(r[0]) + f_(r[1]); }
DEV float wave_sum(float v) {
    v += dpp<0xB1>(v);
    v += dpp<0x4E>(v);
    v += dpp<0x124>(v);
    v += dpp<0x128>(v);
    return add32(add16(v));
}
DEV bool all_(bool c) { return __builtin_amdgcn_ballot_w64(!c) == 0ull; }
DEV int uni(int v) { return __builtin_amdgcn_readfirstlane(v); }
DEV u32 uu(u32 v) { return (u32)__builtin_amdgcn_readfirstlane((int)v); }
DEV u32 rdl(u32 v, int l) { return (u32)__builtin_amdgcn_readlane((int)v, l); }
DEV u32 rt32() { return (u32)__builtin_amdgcn_s_memrealtime(); }

#define WSL 2
DEV void wait_flags(rsrc_t rs, u32 off, u32 want, bool& dead) {
    if (dead) return;
#pragma unroll 1
    for (int n = 0;; ++n) {
        CFENCE();
        const u32 v = ld4(rs, off);
        if (all_(off >= OOB || v >= want)) break;
        __builtin_amdgcn_s_sleep(WSL);
        if ((n & 63) == 63) { if (uni((int)ld4(rs, foff(F_ERR))) != 0) { dead = true; break; } }
        if (n > SPIN_CAP) { st4s(rs, foff(F_ERR), 0, 1u); dead = true; break; }
    }
    CFENCE();
}

DEV void pre_post(rsrc_t rs, int idx, u32 stage, v2u ep) { st8s(rs, threadIdx.x == 0 ? foff(F_PRE + idx) : OOB, 0, (v2u){ep.x + stage, ep.y}); }
DEV void pre_poll(rsrc_t rs, int first, int n, u32 stage, v2u ep) {
    const int lane = threadIdx.x & 63;
#pragma unroll 1
    for (int it = 0; it < SPIN_CAP; ++it) {
        CFENCE();
        const v2u a = ld8s(rs, foff(F_PRE + first + min(lane, n - 1)), 0), b = ld8s(rs, foff(F_PRE + first + min(lane + 64, n - 1)), 0);
        if (all_(a.y == ep.y && b.y == ep.y && max(a.x - ep.x - stage, b.x - ep.x - stage) < 8u - stage)) break;
        __builtin_amdgcn_s_sleep(2);
    }
    CFENCE();
}
DEV void pre_wait(rsrc_t rs, int first, int n, u32 stage, v2u ep) {
    if (uni(threadIdx.x >> 6) == 0) pre_poll(rs, first, n, stage, ep);
    BAR();
}
DEV u32 pol_wait(rsrc_t rs, v2u ep) {
    const int lane = threadIdx.x & 63;
    v4u a;
#pragma unroll 1
    for (int it = 0; it < SPIN_CAP; ++it) {
        CFENCE();
        a = ld16s(rs, foff(F_PRE + (lane < NPRE || lane >= 48 ? lane : 0)), 0);
        if (all_(a.y == ep.y && a.x - ep.x - 1u < 7u)) break;
        __builtin_amdgcn_s_sleep(2);
    }
    CFENCE();
    return uu(wave_sum(lane >= 48 ? (float)(int)a.z : 0.f) >= 0.f ? 1u : 0u);
}
#define PROBE_T 1000
#define NPROBE 32
DEV u32 pol_probe(const Params& p, rsrc_t rs, int id) {
    const int lane = threadIdx.x & 63;
    const u32 off0 = W_PROBE + (u32)(id >> 2) * 16384u + (u32)(id & 3) * 128u;
    const u32 tend = rt32() + PROBE_T;
    st4s(rs, lane < 32 ? off0 + (u32)lane * 512 : OOB, 0, 0u); DRAIN();
    int calm = 0;
    int d[2] = {1 << 16, 1 << 16};
#pragma unroll 1
    for (int q = 0; q < NPROBE && ((q & 1) || (calm < 6 && (int)(rt32() - tend) < 0)); ++q) {
        const u32 half = (u32)(q ^ (q >> 1) ^ id) & 1u;
        const unsigned long long t0 = __builtin_amdgcn_s_memtime();
        const u32 v = ld4(rs, lane == 0 ? off0 + half * 8192 + (u32)(q >> 1) * 512 : OOB);
        asm volatile("s_waitcnt vmcnt(0)" :: "v"(v) : "memory");
        const int dt = (int)(__builtin_amdgcn_s_memtime() - t0);
        if (half) d[1] = min(d[1], dt); else d[0] = min(d[0], dt);
        calm += dt < 1100;
    }
    const u32 xcc = (u32)__builtin_amdgcn_s_getreg(20 | (3 << 11)) & 0xfu;
    return (u32)min(max((d[0] - d[1]) * ((xcc & 4u) ? -1 : 1), -250), 250);
}
DEV f32x2 ln_stats(const f32x2 (&x)[5]) {
    const float c = f_(uu(u_(x[0].x)));
    f32x2 d[5];
#pragma unroll
    for (int i = 0; i < 5; ++i) d[i] = x[i] - c;
    const f32x2 s2 = (d[0] + d[1]) + (d[2] + d[3]) + d[4], q2 = (d[0] * d[0] + d[1] * d[1]) + (d[2] * d[2] + d[3] * d[3] + d[4] * d[4]);
    float a = s2.x + s2.y, b = q2.x + q2.y, a2, b2;
#define LN_DPP(m) "v_add_f32_dpp %0, %0, %0 " m " row_mask:0xf bank_mask:0xf\n\tv_add_f32_dpp %1, %1, %1 " m " row_mask:0xf bank_mask:0xf\n\ts_nop 0\n\t"
    asm("s_nop 1\n\t" LN_DPP("quad_perm:[1,0,3,2]") LN_DPP("quad_perm:[2,3,0,1]") LN_DPP("row_ror:4")
        "v_add_f32_dpp %0, %0, %0 row_ror:8 row_mask:0xf bank_mask:0xf\n\tv_add_f32_dpp %1, %1, %1 row_ror:8 row_mask:0xf bank_mask:0xf\n\ts_nop 1\n\t"
        "v_permlane16_swap_b32 %0, %1\n\tv_add_f32 %0, %0, %1\n\tv_mov_b32 %1, %0\n\ts_nop 1\n\tv_permlane32_swap_b32 %0, %1\n\tv_add_f32 %0, %0, %1\n\ts_nop 0\n\t"
        "v_readlane_b32 %2, %0, 0\n\tv_readlane_b32 %3, %0, 16\n\ts_nop 1"
        : "+v"(a), "+v"(b), "=s"(a2), "=s"(b2));
    const float m = a2 * (1.0f / DM), var = fmaxf(b2 * (1.0f / DM) - m * m, 0.f);
    const float sd = rbf(__builtin_amdgcn_sqrtf(rbf(rbf(var) + 1e-5f)));
    return (f32x2){rbf(c + m), __builtin_amdgcn_rcpf(sd)};
}
DEV u32 pkv(f32x2 v) { u32 d; asm("v_cvt_pk_bf16_f32 %0, %1, %2" : "=v"(d) : "v"(v.x), "v"(v.y)); return d; }
DEV void store_h(rsrc_t rs, u32 base, int row, const u32 (&hp)[5]) {
    const u32 lane = threadIdx.x & 63;
    const u32 sw = (u32)(row & 15) * 16u, o8 = (lane >> 5) * 8192u + (((lane & 31u) * 8u) ^ sw), hb = base + (u32)(row >> 5) * (32 * DM * 2) + (u32)(row & 31) * 256u;
    st8s(rs, o8, hb, (v2u){hp[0], hp[1]});
    st8s(rs, o8, hb + 16384, (v2u){hp[2], hp[3]});
    st4s(rs, (lane * 4) ^ sw, hb + 32768, hp[4]);
}

#define DMA16(ldsb, vo, so) asm volatile("s_mov_b32 m0, %0\n\ts_nop 0\n\tbuffer_load_dwordx4 %1, %2, %3 offen sc1 lds" :: "s"(ldsb), "v"(vo), "s"(srd), "s"(so) : "memory")
#define DMA16I(ldsb, lo, vo, so, go) do { u32 so_; asm volatile("s_add_u32 m0, %1, %5\n\ts_add_u32 %0, %4, %6\n\tbuffer_load_dwordx4 %2, %3, %0 offen sc1 lds" \
                         : "=&s"(so_) : "s"(ldsb), "v"(vo), "s"(srd), "s"(so), "i"(lo), "i"(go) : "memory", "scc"); } while (0)
#define DMA32(ldsb, vo, so) asm volatile("s_mov_b32 m0, %0\n\ts_nop 0\n\tbuffer_load_dwordx4 %1, %2, %3 offen sc1 lds\n\tbuffer_load_dwordx4 %1, %2, %3 offen offset:1024 sc1 lds" \
                         :: "s"(ldsb), "v"(vo), "s"(srd), "s"(so) : "memory")
#define DMAIN(sr, ldsb, vo, so) asm volatile("s_mov_b32 m0, %0\n\ts_nop 0\n\tbuffer_load_dwordx4 %1, %2, %3 offen lds" :: "s"(ldsb), "v"(vo), "s"(sr), "s"(so) : "memory")
DEV v4u mk_srd(const void* q, u32 bytes) { return (v4u){uu((u32)(size_t)q), uu((u32)((size_t)q >> 32) & 0xffffu), bytes, 0x00020000u}; }
#define DMANT(sr, ldsb, vo, so) asm volatile("s_mov_b32 m0, %0\n\ts_nop 0\n\tbuffer_load_dwordx4 %1, %2, %3 offen nt lds" :: "s"(ldsb), "v"(vo), "s"(sr), "s"(so) : "memory")
#define AHEAD(sr, vo, so) asm volatile("buffer_load_dwordx4 %0, %1, %2, %3 offen" : "+v"(junk) : "v"(vo), "s"(sr), "s"(so) : "memory")
#define MK_SRD() const v4u srd = mk_srd(p.ws, W_END)

constexpr int CV_W = 40960;
template <bool B> struct CvL {
    static constexpr int NF = B ? 5 : 3, NK = B ? 4 : 2, KP = 544 + NK * 256, CH = NF * 1024 + 4 * KP;
};
constexpr int CV_Y = 4 * CvL<false>::CH, CV_YP = 272;
static_assert(CV_Y + 32 * CV_YP <= CV_W && 2 * CvL<true>::CH <= CV_Y, "conv lds");
DEV int cv_chan(int u, int i) { return 16 * (u % 40) + 4 * i + u / 40; }
template <bool B> DEV void cv_filt(const Params& p, u32 dst, int d, int i) {
    const u32 lane = threadIdx.x & 63;
    const v4u sf = mk_srd(p.ssmk + d * SEQ, SEQ * 2);
    if (i == 0) DMANT(sf, dst, lane < 4u ? OOB : lane * 16u - 64u, 0);
    else DMANT(sf, uu(dst + i * 1024), i == CvL<B>::NF - 1 && lane >= 4u ? OOB : lane * 16u, i * 1024 - 64);
}
DEV v4u cv_kvld(rsrc_t rs, int d, int i) { return ld16s(rs, (threadIdx.x & 63) * 16u, uu(W_KVT + (u32)d * (SEQ * 2) + i * 1024)); }
template <bool B> DEV void cv_kvput(lptr F, const v4u* k) {
    const int lane = threadIdx.x & 63;
#pragma unroll
    for (int i = 0; i < CvL<B>::NK; ++i) lds_w16(F + CvL<B>::NF * 1024 + (lane & 3) * CvL<B>::KP + 512 + i * 256 + (lane >> 2) * 16, k[i]);
}
template <bool B> DEV void cv_zero(lptr F) {
    const int lane = threadIdx.x & 63;
#pragma unroll
    for (int i = 0; i < 4; ++i) lds_w16(F + (lane >> 5) * CvL<B>::CH + CvL<B>::NF * 1024 + i * CvL<B>::KP + (lane & 31) * 16, (v4u){0u, 0u, 0u, 0u});
}
#define M32V(d, a, b) asm volatile("v_mfma_f32_32x32x16_bf16 %0, %1, %2, %0" : "+v"(d) : "v"(a), "v"(b))
template <bool B, class G> DEV void cv_run(const Params& p, rsrc_t rs, lptr F, int d, u32 D, f32x4 sp, float (&y)[16], G&& gap) {
    constexpr int KP = CvL<B>::KP, FO = B ? 2048 : 0;
    const int lane = threadIdx.x & 63, n = lane & 31, hh = lane >> 5, c = lane & 3;
    lptr fa = F + 64 + (32 * hh + 16 * (c & 1) + 8 * ((lane >> 4) & 1) + 4 * (c >> 1) - (lane & 12)) * 2;
    lptr kl = F + CvL<B>::NF * 1024 + (32 + n - hh) * 16;
    v4u dg;
#pragma unroll
    for (int q = 0; q < 4; ++q) { const int k = (lane & 35) ? -1 : (n & 4) + (n >> 3); dg[q] = k == 2 * q ? D : k == 2 * q + 1 ? D << 16 : 0u; }
    v4u l[4], h[4]; v2u fc[4], fh[4];
    f32x16 acc[4];
#pragma unroll
    for (int j = 0; j < 4; ++j) { l[j] = lds_r16(kl + j * KP); if constexpr (B) h[j] = lds_r16(kl + j * KP + 512); }
#pragma unroll
    for (int j = 0; j < 2; ++j) { fc[j] = lds_tr(fa + FO - 32 * j); if constexpr (B) fh[j] = lds_tr(fa - 32 * j); }
    asm volatile("s_nop 1" : "+v"(dg));
#pragma unroll
    for (int j = 0; j < 4; ++j) asm volatile("v_mfma_f32_32x32x16_bf16 %0, %1, %2, 0" : "=&v"(acc[j]) : "v"(dg), "v"(B ? h[j] : l[j]));
    constexpr int U = B ? 2 : 4;
    kl -= 32 * U; fa -= 32;
    auto body = [&](bool first) __attribute__((always_inline)) {
#pragma unroll
        for (int z = 0; z < U; ++z) {
            const int c = 2 * (z & 1);
            const v4u f = __builtin_shufflevector(fc[c], fc[c + 1], 0, 1, 2, 3), g = __builtin_shufflevector(fh[c], fh[c + 1], 0, 1, 2, 3);
#pragma unroll
            for (int j = 0; j < 4; ++j) {
                M32V(acc[j], f, l[j]); l[j] = lds_r16(kl + j * KP + 32 * (U - z - 1));
                if (j < 2) fc[(c ^ 2) + j] = lds_tr(fa + FO + (z + 1) * 128 + 32 - 32 * j);
                if (first && j > 0) gap(5 * z + j - 1);
                SCHED();
            }
            if constexpr (B) {
#pragma unroll
                for (int j = 0; j < 4; ++j) {
                    M32V(acc[j], g, h[j]); h[j] = lds_r16(kl + j * KP + 512 + 32 * (U - z - 1));
                    if (j < 2) fh[(c ^ 2) + j] = lds_tr(fa + (z + 1) * 128 + 32 - 32 * j);
                    if (first && j > 1) gap(5 * z + j + 1);
                    SCHED();
                }
            }
        }
        kl -= 32 * U; fa += 128 * U;
    };
    if constexpr (B) body(true);
#pragma unroll 1
    for (int m = B ? 2 * U : 0; m < 32; m += 2 * U) body(false);
    asm volatile("s_nop 11" : "+v"(acc[0]), "+v"(acc[1]), "+v"(acc[2]), "+v"(acc[3]));
    const float o[3] = {acc[1][15] + acc[2][14] + acc[3][13], acc[2][15] + acc[3][14], acc[3][15]};
    if constexpr (!B) st16(rs, lane == 63 ? W_SP + (u32)d * 16u : OOB, (v4u){u_(o[0]), u_(o[1]), u_(o[2]), 0u});
#pragma unroll
    for (int i = 0; i < 16; ++i) {
        float v = acc[0][i];
#pragma unroll
        for (int j = 1; j < 4; ++j) if (j <= i) v += acc[j][i - j];
        if (i < 3) { const float e = f_((u32)__builtin_amdgcn_ds_bpermute((hh ? lane - 32 : lane + 31) * 4, (int)u_(o[i]))); v += lane == 0 ? sp[i] : e; }
        y[i] = v;
    }
}
DEV void cv_put(lptr F, int pr, const float (&y0)[16], const float (&y1)[16]) {
    const int lane = threadIdx.x & 63;
#pragma unroll
    for (int i = 0; i < 16; ++i) *(LAS u32*)(F + CV_Y + (lane & 31) * CV_YP + (lane >> 5) * 128 + i * 8 + pr * 4) = pk(y0[i], y1[i]);
}
DEV void cv_out(rsrc_t rs, lptr F, int u, int blk0) {
    const int lane = threadIdx.x & 63;
    lptr q = F + CV_Y + ((lane >> 2) & 3) * CV_YP + ((lane >> 4) * 4 + (lane & 3)) * 8;
    const u32 yo = W_Y + (u32)(((lane >> 2) * 8 + blk0) * DM + 4 * u + (lane & 3)) * 16u;
#pragma unroll
    for (int i = 0; i < 8; ++i) { lptr a = q + (i >> 1) * (8 * CV_YP) + (i & 1) * 128; st16(rs, yo + (u32)((i & 1) * 128 + (i >> 1)) * (DM * 16), tr2(a, a + 4 * CV_YP)); }
}
struct KvIn { v2u k[19], v[16]; v4u s[2]; };
template <class F> DEV void kvt_load(const Params& p, int tile, KvIn& in, F&& mid) {
    const int lane = threadIdx.x & 63;
    const rsrc_t rk = mk_rsrc(p.bk, ACT), rv = mk_rsrc(p.bv, ACT), rh = mk_rsrc(p.shift, DM * 8);
    const int row = (tile / 10) * 64 + (lane >> 4) * 16, col = (tile % 10) * 64 + (lane & 15) * 4;
    const u32 vo = (u32)(row * DM + col) * 2u, vp = row > 0 ? vo - 3 * (DM * 2) : OOB;
#pragma unroll
    for (int i = 0; i < 2; ++i) in.s[i] = ldin16(rh, (u32)col * 8u + i * 16, 0);
#pragma unroll
    for (int i = 0; i < 19; ++i) { if (i == 7) mid(); in.k[i] = i < 3 ? ldin8(rk, vp, i * (DM * 2)) : ldin8(rk, vo, (i - 3) * (DM * 2)); if (i >= 3) in.v[i - 3] = ldin8(rv, vo, (i - 3) * (DM * 2)); }
}
DEV void kvt_make(rsrc_t rs, lptr xt, int tile, const KvIn& in) {
    const int lane = threadIdx.x & 63;
#pragma unroll
    for (int c = 0; c < 2; ++c) {
        const v4u sv = in.s[c];
        const f32x2 sh[4] = {{lo_f(sv.x), lo_f(sv.z)}, {hi_f(sv.x), hi_f(sv.z)}, {lo_f(sv.y), lo_f(sv.w)}, {hi_f(sv.y), hi_f(sv.w)}};
        f32x2 kf[19];
#pragma unroll
        for (int i = 0; i < 19; ++i) kf[i] = up2(in.k[i][c]);
        u32 kv[16];
#pragma unroll
        for (int i = 0; i < 16; ++i) kv[i] = pk2(((kf[i] * sh[3] + kf[i + 1] * sh[2]) + (kf[i + 2] * sh[1] + kf[i + 3] * sh[0])) * up2(in.v[i][c]));
#pragma unroll
        for (int q = 0; q < 8; ++q) {
            const u32 sel = (q & 1) ? 0x07060302u : 0x05040100u; const u32* z = kv + (q >> 1);
            *(LAS v2u*)(xt + ((2 * c + (q & 1)) * 16 + (lane & 15)) * 144 + (lane >> 5) * 64 + (q >> 1) * 16 + ((lane >> 4) & 1) * 8) =
                (v2u){__builtin_amdgcn_perm(z[4], z[0], sel), __builtin_amdgcn_perm(z[12], z[8], sel)};
        }
    }
    const u32 o = W_KVT + (u32)(((tile % 10) * 64 + 4 * (lane >> 3)) * SEQ + (tile / 10) * 64 + (lane & 7) * 8) * 2u;
#pragma unroll
    for (int i = 0; i < 8; ++i) st16(rs, o + (u32)(32 * (i & 1) + (i >> 1)) * (SEQ * 2), lds_r16(xt + (8 * i + (lane >> 3)) * 144 + (lane & 7) * 16));
}
struct RowX { v2u a, b; u32 c; };
DEV void ln_row(rsrc_t rs, int row, const RowX& r) {
    const f32x2 x[5] = {up2(r.a.x), up2(r.a.y), up2(r.b.x), up2(r.b.y), up2(r.c)};
    const f32x2 st = ln_stats(x);
    u32 hp[5];
#pragma unroll
    for (int q = 0; q < 5; ++q) hp[q] = pk2(up2(pkv(x[q] - st.x)) * st.y);
    store_h(rs, W_H1, row, hp);
}
DEV v4u mem0_load(const Params& p, int j) {
    const u32 gt = (u32)(j * 256 + threadIdx.x) * 16u, n = ((gt >> 14) << 9) + ((gt & 8191u) >> 4);
    return ldin16(mk_rsrc(p.mem0, 8 * DM * 2), n < 640u ? ((2 * ((n >> 2) & 3) + (n & 1)) * DM + (n >> 4) * 16 + (n & 2) * 4) * 2u : OOB, 0);
}
DEV void pre_rings(rsrc_t rs, int j, v4u m) {
    const u32 gi = (u32)(j * 256 + threadIdx.x), gt = gi * 16u, R = NPRE * 4096, n = ((gt >> 14) << 9) + ((gt & 8191u) >> 4);
#pragma unroll
    for (int i = 0; i < 4; ++i) m[i] = n >= 640u ? PSENT : m[i] == PSENT ? 0x7FC17FC1u : m[i];
    st4s(rs, gi - 36u * 256u < F_CLR ? foff(gi - 36u * 256u) : OOB, 0, 0u);
#pragma unroll 1
    for (u32 o = gt; o < MB; o += R) st16(rs, W_ORING + o, (v4u){PSENT, PSENT, PSENT, PSENT});
    const u32 mo = gt < 4 * 32768 + 16384 ? W_MRING + gt : OOB;
    st16(rs, mo, m);
    st16(rs, mo + MB / 4, m);
    static_assert(NPRE * 4096 >= 4 * 32768 + 16384, "memory ring");
}
constexpr int KT0 = NPRE * 4, LN0 = NPRE * 8;
struct Early { RowX r[2]; v4u m; u32 vote; };
DEV void conv_early(const Params& p, rsrc_t rs, int j, Early& e) {
    const int lane = threadIdx.x & 63, w = uni(threadIdx.x >> 6), u = j * 4 + w;
    if (j < NPRE) {
        const rsrc_t rx = mk_rsrc(p.x, ACT);
#pragma unroll
        for (int i = 0; i < 2; ++i) {
            const u32 so = uu((u + i * KT0) * (DM * 2));
            e.r[i].a = __builtin_amdgcn_raw_buffer_load_b64(rx, lane * 8u, so, 0); e.r[i].b = __builtin_amdgcn_raw_buffer_load_b64(rx, lane * 8u + 512u, so, 0);
            e.r[i].c = __builtin_amdgcn_raw_buffer_load_b32(rx, lane * 4u + 1024u, so, 0);
        }
        e.m = mem0_load(p, j);
    } else if (j < NCV && w > 0) e.vote = pol_probe(p, rs, u - NPRE * 4);
}
DEV void conv_stage(const Params& p, rsrc_t rs, lptr lds, int j, int g0, int ge, v2u ep, Early& e) {
    const int tid = threadIdx.x, lane = tid & 63, w = uni(tid >> 6), u = j * 4 + w;
    lptr F = lds + w * CV_W;
    const u32 fb = uu(w * CV_W);
    const rsrc_t rd = mk_rsrc(p.ssmD, DM * 2);
    float y0[16], y1[16];
    KvIn in;
    if (j < NPRE) {
        constexpr int CH = CvL<false>::CH;
        if (w == 1) LST(1500 + j, rt32());
        pre_rings(rs, j, e.m);
        ln_row(rs, u, e.r[0]); ln_row(rs, u + KT0, e.r[1]);
        if (w == 1) LST(1550 + j, rt32());
        CFENCE();
        kvt_load(p, u, in, [&] { VMW(13); BAR(); pre_post(rs, j, 1u, ep); if (w == 0) LST(1450 + j, rt32()); });
        if (j == 20) LST(260, rt32());
        const u32 D = __builtin_amdgcn_raw_buffer_load_b16(rd, (u32)cv_chan(u, lane & 3) * 2u, 0, 0);
#pragma unroll 1
        for (int i = 0; i < 4; ++i) { const int d = cv_chan(u, i); cv_filt<false>(p, fb + i * CH, d, 0); cv_filt<false>(p, fb + i * CH, d, 1); cv_filt<false>(p, fb + i * CH, d, 2); }
        kvt_make(rs, F + CV_Y, u, in);
        cv_zero<false>(F); cv_zero<false>(F + 2 * CH);
        DRAIN(); BAR();
        pre_post(rs, j, 2u, ep);
        if (j == 20) LST(262, rt32());
        pre_wait(rs, 0, NPRE, 2u, ep);
        if (j == 20) LST(281, rt32());
        {
            v4u k[8];
#pragma unroll
            for (int i = 0; i < 8; ++i) k[i] = cv_kvld(rs, cv_chan(u, i >> 1), i & 1);
#pragma unroll
            for (int i = 0; i < 4; ++i) cv_kvput<false>(F + i * CH, k + 2 * i);
        }
        if (j == 20) LST(286, rt32());
#pragma unroll 1
        for (int i = 0; i < 4; i += 2) {
            cv_run<false>(p, rs, F + i * CH, cv_chan(u, i), rdl(D, i), f32x4{}, y0, [](int) {});
            cv_run<false>(p, rs, F + (i + 1) * CH, cv_chan(u, i + 1), rdl(D, i + 1), f32x4{}, y1, [](int) {});
            cv_put(F, i >> 1, y0, y1);
        }
        if (j == 20) LST(287, rt32());
        cv_out(rs, F, u, 0);
        if (j == 20) LST(263, rt32());
    } else {
        st16(rs, lane == 0 ? foff(F_PRE + 48 + u - NPRE * 4) : OOB, (v4u){ep.x + 1u, ep.y, w > 0 ? e.vote : 0u, 0u});
#pragma unroll 1
        for (int t = u + 128; t < 320; t += 16) { kvt_load(p, t, in, [] {}); kvt_make(rs, F, t, in); }
    }
    DRAIN(); BAR();
    if (g0 < ge) {
        constexpr int CH = CvL<true>::CH;
        pre_post(rs, j, 3u, ep);
        cv_zero<true>(F);
        if (w == 0) { pre_poll(rs, 64, 32, 3u, ep); pre_poll(rs, 0, NCV, 3u, ep); }
        BAR();
        v4u k[4];
        auto fetch = [&](u32 dst, int d, int i) { if (i < 5) cv_filt<true>(p, dst, d, i); else if (i < 9) k[i - 5] = cv_kvld(rs, d, i - 5); };
#pragma unroll
        for (int i = 0; i < 9; ++i) fetch(fb, cv_chan(g0 * 4 + w, 0), i);
        const u32 ch = (u32)cv_chan(min((g0 + 8 * (lane >> 2)) * 4 + w, 159), lane & 3);
        u32 D = __builtin_amdgcn_raw_buffer_load_b16(rd, ch * 2u, 0, 0);
        v4u sp = ld16s(rs, W_SP + ch * 16u, 0);
        asm volatile("" : "+v"(D), "+v"(sp));
#pragma unroll 1
        for (int g = g0, n = 0; g < ge; g += 8) {
            const int un = g * 4 + w;
            if (g == 16) { LST(282, rt32()); LST(288, __builtin_amdgcn_s_memtime()); }
#pragma unroll 1
            for (int i = 0; i < 4; i += 2, n += 2) {
                const int d1 = (int)rdl(ch, n + 1), d2 = (int)rdl(ch, n + 2);
                cv_kvput<true>(F, k);
                cv_run<true>(p, rs, F, 0, rdl(D, n), (f32x4){f_(rdl(sp.x, n)), f_(rdl(sp.y, n)), f_(rdl(sp.z, n)), 0.f}, y0, [&](int q) { fetch(fb + CH, d1, q); });
                cv_kvput<true>(F + CH, k);
                cv_run<true>(p, rs, F + CH, 326, rdl(D, n + 1), (f32x4){f_(rdl(sp.x, n + 1)), f_(rdl(sp.y, n + 1)), f_(rdl(sp.z, n + 1)), 0.f}, y1, [&](int q) { fetch(fb, d2, q); });
                cv_put(F, i >> 1, y0, y1);
            }
            cv_out(rs, F, un, 4);
            if (g == 16) { LST(283, rt32()); LST(289, __builtin_amdgcn_s_memtime()); }
        }
        DRAIN(); BAR();
    }
    pre_post(rs, j, 5u, ep);
    if (j == 20) LST(284, rt32());
    if (j == 3) LST(285, rt32());
}

#define M32A(first, d, a, b) do { if (first) asm volatile("v_mfma_f32_32x32x16_bf16 %0, %1, %2, 0" : "=&v"(d) : "a"(a), "v"(b)); \
                                  else asm volatile("v_mfma_f32_32x32x16_bf16 %0, %1, %2, %0" : "+v"(d) : "a"(a), "v"(b)); } while (0)
#define M32B(first, d, a, b) do { if (first) asm volatile("v_mfma_f32_32x32x16_bf16 %0, %1, %2, 0" : "=&v"(d) : "v"(a), "a"(b)); \
                                  else asm volatile("v_mfma_f32_32x32x16_bf16 %0, %1, %2, %0" : "+v"(d) : "v"(a), "a"(b)); } while (0)
#define VCVT(d, a, b) asm volatile("v_cvt_pk_bf16_f32 %0, %1, %2" : "=v"(d) : "v"(a), "v"(b))
#define VADD(d, a, b) asm volatile("v_add_f32 %0, %1, %2" : "=v"(d) : "v"(a), "v"(b))
#define LD4A(d, vo, so) asm volatile("buffer_load_dword %0, %1, %2, %3 offen sc1" : "=v"(d) : "v"(vo), "s"(srd), "s"(so) : "memory")
DEV u32 wst_src(int lane, int ld) { return (u32)((lane >> 4) * ld + 8 * ((lane & 15) ^ ((lane >> 4) << 2))) * 2u; }
DEV u32 wst_rd(int lane, int w) { const int q4 = (lane >> 2) & 3; return (u32)(q4 * 256 + ((w ^ q4) * 64) + ((lane >> 4) & 1) * 32 + (lane & 3) * 8); }

constexpr int QK_SEL = 81920;
#define QK_LEAD 12
#define QK_SMP 35
#define QK_YB 22
#define QK_W0 2
DEV void role_qkv(const Params& p, lptr lds, int c, v2u ep) {
    const int tid = threadIdx.x, lane = tid & 63, w = uni(tid >> 6), hh = lane >> 5, r32 = lane & 31;
    const int m = c >> 5, h = c & 31;
    const rsrc_t rs = mk_rsrc(p.ws, W_END);
    v4u wt[40];
    {
        const v4u sw = mk_srd(m == 0 ? p.Wq : m == 1 ? p.Wk : p.Wv, DM * NQ * 2), sx = mk_srd(p.x, ACT);
        const u32 vo = wst_src(lane, NQ) + (u32)h * (DH * 2), x8 = (u32)lane * 8u, x4 = (u32)lane * 4u;
        RowX xr[5];
        auto wfill = [&](int i0, int n) {
#pragma unroll 1
            for (int i = i0; i < i0 + n; ++i) DMAIN(sw, uu((4 * i + w) * 1024), vo, uu((16 * i + 4 * w) * (NQ * 2)));
        };
        wfill(0, QK_W0);
#pragma unroll
        for (int i = 0; i < 5; ++i)
            asm volatile("buffer_load_dwordx2 %0, %3, %5, %6 offen\n\tbuffer_load_dwordx2 %1, %3, %5, %6 offen offset:512\n\tbuffer_load_dword %2, %4, %5, %6 offen offset:1024"
                         : "=&v"(xr[i].a), "=&v"(xr[i].b), "=&v"(xr[i].c) : "v"(x8), "v"(x4), "s"(sx), "s"(uu((LN0 + 4 * c + w + 384 * i) * (DM * 2))) : "memory");
        wfill(QK_W0, 10 - QK_W0);
        if (c == 3) LST(275, rt32());
        VMW(10 - QK_W0);
        if (c == 3) LST(276, rt32());
#pragma unroll
        for (int i = 0; i < 5; ++i) {
            asm volatile("" : "+v"(xr[i].a), "+v"(xr[i].b), "+v"(xr[i].c));
            const int row = LN0 + 4 * c + w + 384 * i;
            if (i < 4 || row < SEQ) ln_row(rs, row, xr[i]);
            wfill(10 + 6 * i, 6);
        }
        if (c == 3) LST(264, rt32());
        lptr tb = lds + hh * 2048 + wst_rd(lane, w);
        SFOR(k, 40)
            if constexpr (k == 0) { VMW(15); BAR(); }
            if constexpr (k == 28) {
                if (w == 0) pre_poll(rs, 0, NPRE, 1u, ep);
                DRAIN(); BAR();
                pre_post(rs, 96 + c, 1u, ep);
            }
            lptr q = tb + (k >> 3) * 32768 + (k & 7) * 4096; wt[k] = tr2(q, q + 1024); asm volatile("" : "+a"(wt[k]));
        SEND
    }
    if (c == 3) LST(268, rt32());
    if (w == 0) LST(1350 + c, rt32());
    bool dead = false;
    int n0, n1;
    const u32 t16 = (u32)tid * 16u, wb = uu(w * 1024);
    u32 ar[16];
#pragma unroll
    for (int j = 0; j < 16; ++j) ar[j] = (u32)(r32 * 256 + ((2 * (j & 7) + hh) ^ (r32 & 15)) * 16 + (j >> 3) * 49152);
    MK_SRD();
#define XRD(y, ks) lds_r16(lds + ar[((ks) & 7) + ((ks) >= 24 ? 8 : 0)] + ((ks) >> 3) % 3 * 16384 + (y) * 8192)
#define QDMA(y, i, vo, so) DMA16I(wb, ((i) >> 1) * 16384 + ((i) & 1) * 4096 + (y) * 8192, vo, so, (i) * 4096)
    const u32 h2flag = (lane < 32 && w == 0) ? foff(F_H2 + lane) : OOB;
    const u32 ol = (m == 2 ? (u32)(2 * w + (r32 >> 4)) * 1024u : (u32)((r32 >> 4) * 4 + w) * 1024u) + (u32)(hh * 16 + (r32 & 15)) * 16u
                 + (m == 2 ? W_VO : m == 1 ? W_KQ : W_KQ + 8192u) + (u32)h * 16384u;
    const u32 fo = tid < 2 ? foff(F_QKV + tid * 96 + c) : OOB;
    lptr selw = lds + QK_SEL + (w == 0 ? 0 : 64), selr = lds + QK_SEL;
    auto late = [&](u32 fv) { const u32 need = n1 >= NSEG ? 0xFFFFFFFFu : dead ? 0u : (u32)n1 + 1u; return (u32)__builtin_amdgcn_ballot_w64(fv < need); };
    static_assert(W_H2 - W_H1 == 2 * 64 * (32 * DM * 2), "h");
    auto run = [&](auto isv) __attribute__((always_inline)) {
        constexpr bool V = decltype(isv)::value;
        constexpr int YB = QK_YB;
        f32x16 ax, ay = {};
        v4u x[8], pp, pq;
        u32 oy = OOB, lt = 1, fcnt = 0, fa = OOB;
        u32 nn = 0;
        PIN(lt); PIN(nn);
#pragma unroll
        for (int z = 0; z < 5; ++z) x[z] = XRD(1, 40 - YB + z);
#define QPK(r_, a1, a_, q_) do { if constexpr (V) VCVT(r_[q_], a1[4 * (a_) + 8 * ((q_) >> 1) + 2 * ((q_) & 1)], a1[4 * (a_) + 8 * ((q_) >> 1) + 2 * ((q_) & 1) + 1]); \
                                 else VCVT(r_[q_], a1[4 * (a_) + (q_)], a1[4 * (a_) + (q_) + 8]); } while (0)
#define QPK4(r_, a1, a_) do { QPK(r_, a1, a_, 0); QPK(r_, a1, a_, 1); QPK(r_, a1, a_, 2); QPK(r_, a1, a_, 3); } while (0)
        for (;;) {
            u32 ls = uu(lt);
            nn = uu(nn); n0 = nn & 255; n1 = (nn >> 8) & 255;
            if (ls != 0u && n0 >= min(NSEG, n1 + QK_LEAD)) {
                CFENCE();
                u32 fv = ld4(rs, h2flag);
                asm volatile("s_waitcnt vmcnt(0)" : "+v"(fv) :: "memory");
                *(LAS u32*)(selw + 16) = late(fv);
                BAR();
                st4s(rs, fa, 0, fcnt);
                fa = OOB;
                if (n1 >= NSEG) __builtin_amdgcn_endpgm();
                if (uu(*(LAS u32*)(selr + 16)) != 0u) {
                    if (w == 0) wait_flags(rs, h2flag, (u32)n1 + 1, dead);
                    BAR();
                }
                ls = 0u;
            }
            if (n0 >= 8 && (nn >> 16) == 0u) pre_wait(rs, 96, 96, 1u, ep);
            int cur = n0;
            asm volatile("s_cmp_eq_u32 %3, 0\n\ts_cselect_b32 %0, %4, %0\n\ts_addc_u32 %2, %2, 0\n\ts_cmp_lg_u32 %3, 0\n\ts_addc_u32 %1, %1, 0"
                         : "+s"(cur), "+s"(n0), "+s"(n1) : "s"(ls), "s"(n1 | 64) : "scc");
            u32 xs = W_H1 + (u32)(cur + (cur & 64)) * (32 * DM * 2), ys, yvo, ox, fv;
            PINS(xs);
#define QT(k) do {} while (0)
            QT(0);
            SCHED();
            QDMA(0, 0, t16, xs); QDMA(0, 1, t16, xs);
            SFOR(z, 80)
                constexpr int ks = (z + 40 - YB) % 40, zn = (z + 5) % 80, zx = z - YB;
                if constexpr (zx >= 0 && zx < 40) { if constexpr (V) M32B(ks == 0, ax, x[z & 7], wt[ks]); else M32A(ks == 0, ax, wt[ks], x[z & 7]); }
                else { if constexpr (V) M32B(ks == 0, ay, x[z & 7], wt[ks]); else M32A(ks == 0, ay, wt[ks], x[z & 7]); }
                x[(z + 5) & 7] = XRD(zn < YB || zn >= YB + 40, (zn + 40 - YB) % 40);
                if constexpr (z == 77) lt = *(LAS u32*)selr;
                SCHED();
                if constexpr (z < 2) QDMA(0, z + 2, t16, xs);
                if constexpr (z >= 3 && z < 15 && (z & 1)) QDMA(0, (z + 5) >> 1, t16, xs);
                if constexpr (z == 8) { ys = W_H1 + (u32)n0 * (32 * DM * 2); yvo = t16 | (n0 < NSEG ? 0u : OOB); PINS(ys); PIN(yvo); }
                if constexpr (z == 10) { ox = ol + (u32)cur * (NH * 16384u); PIN(ox); }
                if constexpr (zx == -6) { QT(1); asm volatile("s_waitcnt vmcnt(6) lgkmcnt(0)\n\ts_barrier" ::: "memory"); QT(2); }
                if constexpr (zx == -5) st4s(rs, fa, 0, fcnt);
                if constexpr (zx == -3 || zx == -1 || zx == 1) QDMA(1, (zx + 3) >> 1, yvo, ys);
                if constexpr (zx == 2) QPK4(pp, ay, 0);
                if constexpr (zx == 3) st16(rs, oy, pp);
                if constexpr (zx == 4) QPK4(pq, ay, 1);
                if constexpr (zx == 5) st16(rs, oy + 512, pq);
                if constexpr (zx == 7 || zx == 9 || zx == 12) QDMA(1, zx == 12 ? 5 : (zx - 1) >> 1, yvo, ys);
                if constexpr (zx == 10) { QT(3); asm volatile("s_waitcnt vmcnt(8)\n\ts_barrier" ::: "memory"); QT(4); }
                if constexpr (zx >= 16 && zx < 32 && (zx & 3) == 0) QDMA(1, (zx + 8) >> 2, yvo, ys);
                if constexpr (zx == 14) { fcnt = lane == 0 ? (u32)n0 : (u32)n1; fa = fo; asm volatile("" : "+v"(fcnt), "+v"(fa)); }
                if constexpr (zx == 18) { oy = (ol + (u32)n0 * (NH * 16384u)) | (yvo & OOB); PIN(oy); }
                if constexpr (zx == 22) { nn = (u32)(min(n0 + 1, NSEG) | n1 << 8) | (nn & 0xF8u) << 16; PIN(nn); }
                if constexpr (zx == 34) { asm volatile("s_waitcnt vmcnt(4) lgkmcnt(0)\n\ts_barrier" ::: "memory"); QT(5); }
                if constexpr (zx == QK_SMP) LD4A(fv, h2flag, 0);
                if constexpr (zx == 42) QPK4(pp, ax, 0);
                if constexpr (zx == 43) st16(rs, ox, pp);
                if constexpr (zx == 44) QPK4(pq, ax, 1);
                if constexpr (zx == 45) st16(rs, ox + 512, pq);
                if constexpr (z == 73) { asm volatile("s_waitcnt vmcnt(2)" : "+v"(fv) :: "memory"); QT(6); ls = late(fv); PINS(ls); }
                if constexpr (z == 74) { lt = ls; PIN(lt); *(LAS u32*)selw = lt; }
                if constexpr (z == 76) asm volatile("s_waitcnt lgkmcnt(2)\n\ts_barrier" ::: "memory");
                SCHED();
            SEND
        }
    };
    if (m == 2) run(IC<1>{}); else run(IC<0>{});
}

#define OU_SMP 44
DEV void role_out(const Params& p, lptr lds, int L, int kq, int ng, v2u ep) {
    const int tid = threadIdx.x, lane = tid & 63, w = uni(tid >> 6), hh = lane >> 5, r32 = lane & 31;
    const rsrc_t rs = mk_rsrc(p.ws, W_END);
    v4u wo[64];
    bool dead = false;
    {
        const v4u sw = mk_srd(p.Wo, DM * NQ * 2);
        const u32 vo = wst_src(lane, DM) + (u32)ng * 256u;
        auto fill = [&](int b) {
#pragma unroll 1
            for (int i = 0; i < 8; ++i) DMAIN(sw, uu((b % 5) * 32768 + (4 * i + w) * 1024), vo, uu((kq * 1024 + b * 128 + 16 * i + 4 * w) * (DM * 2)));
        };
#pragma unroll 1
        for (int b = 0; b < 5; ++b) fill(b);
        lptr tb = lds + hh * 1024 + wst_rd(lane, w);
        SFOR(b, 8)
            VMW(b < 4 ? 32 : 56 - 8 * b); BAR();
            lptr q = tb + (b % 5) * 32768;
#pragma unroll
            for (int k = 0; k < 8; ++k) { lptr a = q + (k >> 1) * 8192 + (k & 1) * 2048; wo[8 * b + k] = tr2(a, a + 4096); if (b < 7) asm volatile("" : "+a"(wo[8 * b + k])); else PIN(wo[8 * b + k]); }
            if constexpr (b < 3) { BAR(); fill(b + 5); }
        SEND
    }
    pre_wait(rs, 0, NPRE, 1u, ep);
    if (kq == 0 && ng == 0) LST(272 + L, rt32());
    MK_SRD();
    const u32 ofl = lane < 8 ? foff(F_O + L * 32 + kq * 8 + lane) : OOB, l16 = (u32)lane * 16u;
    const u32 fo = (tid == 0) ? foff(F_P + L * 32 + kq * 5 + ng) : OOB;
    const u32 src0 = uu(W_VO + 8192u + (u32)(L * NSEG * NH + kq * 8) * 16384u + w * 1024);
#define ODMA(i, dst, so) DMA16I(dst, (i) * 4096, l16, so, ((i) >> 1) * 16384 + ((i) & 1) * 4096)
    auto first = [&](int s) { const u32 dst = uu((s & 1) * 65536 + w * 1024), src = src0 + (u32)s * (NH * 16384u); SFOR(i, 10) ODMA(i, dst, src); SEND };
    u32 pa = W_P + (u32)(L * NSEG) * P_ITEM + (u32)(4 * hh * 4 + kq) * (DM * 4) + (u32)(ng * 128 + w * 32 + r32) * 4u, fv = 0;
    bool early = false;
    for (int s = 0; s < NSEG; ++s) {
        const u32 dst = uu((s & 1) * 65536 + w * 1024), src = src0 + (u32)s * (NH * 16384u);
        lptr A = lds + (s & 1) * 65536 + l16;
        if (!early) {
            if (!all_(ofl == OOB || fv >= (u32)s + 1)) wait_flags(rs, ofl, (u32)s + 1, dead);
            LS3(L == 0 && w == 1, 1, kq * 5 + ng);
            first(s);
        }
        asm volatile("s_waitcnt vmcnt(8)\n\ts_barrier" ::: "memory");
        v4u o[64];
#pragma unroll
        for (int k = 0; k < 3; ++k) o[k] = lds_r16(A + k * 1024);
        f32x16 acc;
        SCHED();
        SFOR(k, 64)
            if constexpr (k < 56) M32B(k == 0, acc, o[k], wo[k]); else M32V(acc, o[k], wo[k]);
            if constexpr (k < 61) o[k + 3] = lds_r16(A + (k + 3) * 1024);
            SCHED();
            if constexpr (k < 12 && (k & 1) == 0) ODMA(10 + k / 2, dst, src);
            if constexpr (k == 4) asm volatile("s_waitcnt vmcnt(7)\n\ts_barrier" ::: "memory");
            if constexpr (k == 20) asm volatile("s_waitcnt vmcnt(5)\n\ts_barrier" ::: "memory");
            if constexpr (k == 40) asm volatile("s_waitcnt vmcnt(0)\n\ts_barrier" ::: "memory");
            if constexpr (k == OU_SMP) LD4A(fv, ofl, 0);
            SCHED();
        SEND
        asm volatile("s_nop 9" : "+v"(acc), "+v"(pa));
#pragma unroll
        for (int i = 0; i < 16; ++i) st4s(rs, pa, (u32)((8 * (i >> 2) + (i & 3)) * (4 * DM * 4)), u_(acc[i]));
        asm volatile("s_waitcnt vmcnt(16)" : "+v"(fv) :: "memory");
        LS3(L == 0 && w == 1, 2, kq * 5 + ng);
        early = s + 1 < NSEG && all_(ofl == OOB || fv >= (u32)s + 2);
        if (early) { first(s + 1); VMW(10); }
        else { fv = ld4(rs, ofl); asm volatile("s_waitcnt vmcnt(0)" : "+v"(fv) :: "memory"); }
        BAR();
        st4s(rs, fo, 0, (u32)s + 1);
        pa += P_ITEM;
        LS2(L == 0 && w == 1, 160 + kq * 5 + ng);
    }
}

constexpr int RW_P = 20480, RW_X = RW_P + 10240;
DEV void role_row(const Params& p, lptr lds, int id, v2u ep) {
    const int lane = threadIdx.x & 63, w = uni(threadIdx.x >> 6);
    const int ri = id >> 1, L = id & 1;
    const rsrc_t rs = mk_rsrc(p.ws, W_END);
    bool dead = false, have = false;
    const u32 fp = lane < 20 ? foff(F_P + L * 32 + lane) : OOB, l16 = (u32)lane * 16u, lx = lane < 16 ? l16 + 1024u : OOB, fb = uu(w * CV_W);
    lptr F = lds + w * CV_W;
    MK_SRD();
    const v4u sx = mk_srd(p.x, ACT);
    auto yfill = [&](int b) {
#pragma unroll 1
        for (int i = 0; i < 5; ++i) DMA32(uu(fb + (b & 1) * 10240 + i * 2048), l16, uu(W_Y + (u32)(ri * 8 + b) * 10240u + i * 2048));
    };
    auto xfill = [&](int s) {
        const u32 d = uu(fb + RW_X + (s & 1) * 2048), o = uu((u32)(s * 32 + ri) * (DM * 2));
        if (L == 0) { DMAIN(sx, d, l16, o); DMAIN(sx, d + 1024, lx, o); } else { DMA16(d, l16, o + W_X2); DMA16(d + 1024, lx, o + W_X2); }
    };
    auto pfill = [&](int s) {
        const u32 o = uu(W_P + (u32)(L * NSEG + s) * P_ITEM + (u32)ri * (4 * DM * 4));
        SFOR(i, 10) DMA16I(fb, RW_P + i * 1024, l16, o, i * 1024); SEND
        if (L == 1) xfill(s);
    };
    if (L == 0) xfill(0);
    pre_poll(rs, 0, NPRE, 3u, ep);
    if (id == 0) LST(274, rt32());
    yfill(0); DRAIN();
    u32 fv = 0;
    for (int s = 0; s < NSEG; ++s) {
        if (!have) {
            if (!all_(fp == OOB || fv >= (u32)s + 1)) wait_flags(rs, fp, (u32)s + 1, dead);
            LS3(L == 0, 3, ri);
            pfill(s);
        }
        LS2(L == 0, 192 + ri);
        f32x2 y[5];
        {
            lptr ya = F + ((s >> 3) & 1) * 10240 + (s & 7) * 2 + lane * 16;
            auto yr = [&](lptr q) { return lo_f(*(LAS u16*)q); };
            y[0] = (f32x2){yr(ya), yr(ya + 2560)}; y[1] = (f32x2){yr(ya + 5120), yr(ya + 7680)};
            y[2] = (f32x2){yr(ya + 1024), yr(ya + 3584)}; y[3] = (f32x2){yr(ya + 6144), yr(ya + 8704)};
            lptr yc = ya - lane * 16 + 2048 + (lane >> 1) * 16 + (lane & 1) * 5120;
            y[4] = (f32x2){yr(yc), yr(yc + 2560)};
            asm volatile("" : "+v"(y[0]), "+v"(y[1]), "+v"(y[2]), "+v"(y[3]), "+v"(y[4]));
        }
        if (!have) DRAIN();
        LS3(L == 0, 4, ri);
        f32x2 x1[5];
        {
            f32x4 a[4], b[4]; f32x2 c[4];
            lptr q = F + RW_X + (s & 1) * 2048;
            v2u xa = *(LAS v2u*)(q + lane * 8), xb = *(LAS v2u*)(q + 512 + lane * 8); u32 xc = *(LAS u32*)(q + 1024 + lane * 4);
#pragma unroll
            for (int kq = 0; kq < 4; ++kq) { lptr r = F + RW_P + kq * (DM * 4); a[kq] = *(LAS f32x4*)(r + l16); b[kq] = *(LAS f32x4*)(r + 1024 + l16); c[kq] = *(LAS f32x2*)(r + 2048 + lane * 8); }
            SCHED();
            const f32x4 sa = (a[0] + a[1]) + (a[2] + a[3]), sb = (b[0] + b[1]) + (b[2] + b[3]);
            const f32x2 o[5] = {sa.lo, sa.hi, sb.lo, sb.hi, (c[0] + c[1]) + (c[2] + c[3])};
            const u32 xi[5] = {xa.x, xa.y, xb.x, xb.y, xc};
#pragma unroll
            for (int i = 0; i < 5; ++i) x1[i] = up2(xi[i]) + o[i];
            asm volatile("" : "+v"(x1[0]), "+v"(x1[1]), "+v"(x1[2]), "+v"(x1[3]), "+v"(x1[4]));
        }
        have = s + 1 < NSEG && all_(fp == OOB || fv >= (u32)s + 2);
        if (__builtin_expect(have, 0)) pfill(s + 1);
        if (L == 0) xfill(s + 1);
        const f32x2 st1 = ln_stats(x1);
        u32 xp[5];
#pragma unroll
        for (int i = 0; i < 5; ++i) xp[i] = pkv(x1[i] + (x1[i] - st1.x) * (y[i] * st1.y));
        const int row = s * 32 + ri;
        if (L == 0) {
            const u32 ro = (u32)row * (DM * 2);
            f32x2 x2[5];
#pragma unroll
            for (int i = 0; i < 5; ++i) x2[i] = up2(xp[i]);
            st8s(rs, (u32)lane * 8, W_X2 + ro, (v2u){xp[0], xp[1]});
            st8s(rs, (u32)lane * 8 + 512, W_X2 + ro, (v2u){xp[2], xp[3]});
            st4s(rs, (u32)lane * 4 + 1024, W_X2 + ro, xp[4]);
            const f32x2 st2 = ln_stats(x2);
            u32 hp[5];
#pragma unroll
            for (int i = 0; i < 5; ++i) hp[i] = pk2((x2[i] - st2.x) * st2.y);
            store_h(rs, W_H2, row, hp);
        } else {
            u16* o_ = p.out + (size_t)row * DM;
            *(v2u*)(o_ + 4 * lane) = (v2u){xp[0], xp[1]};
            *(v2u*)(o_ + 256 + 4 * lane) = (v2u){xp[2], xp[3]};
            *(u32*)(o_ + 512 + 2 * lane) = xp[4];
        }
        CFENCE();
        LS3(L == 0, 5, ri);
        fv = ld4(rs, fp);
        asm volatile("s_waitcnt vmcnt(0)" : "+v"(fv) :: "memory");
        st4s(rs, lane == 0 && L == 0 ? foff(F_H2 + ri) : OOB, 0, (u32)s + 1);
        if ((s & 7) == 6 && s < NSEG - 8) {
            if (s == 30) pre_poll(rs, 0, NCV, 5u, ep);
            if (s == 46) st8s(rs, (id == 1 && lane == 0) ? foff(F_EPOCH) : OOB, 0, (v2u){ep.x + 8u, ep.y});
            yfill((s >> 3) + 1);
        }
        if (ri == 0) LST(320 + L * 64 + s, rt32());
        LS2(L == 0, 224 + ri);
    }
}

constexpr float SCALE = 0.08838834764831845f, LOG2E = 1.4426950408889634f;
constexpr int C_K = 0, C_V = 8192, C_QX = 16384, C_MEM = 24576, C_Q = C_MEM + 10240, C_KM = C_Q + 2304, C_VM = C_KM + 2304, C_DUMP = C_VM + 2048,
              C_ZERO = C_DUMP + 1024, C_WL = C_ZERO + 128;
constexpr int NLF = 14;
static_assert(C_WL % 16 == 0 && C_WL + 4 * NLF * 2048 <= LDS_BYTES, "head lds");
#define CH_VIS 10
#define CH_H0 104
#define CH_W0 81
#define CH_MH 3
#define CH_MW 3
#define CH_REC 12
#define CH_PRE 9
#define CH_T0 190
#define CH_UP 2
#define CH_WX 6
#define WAIT_TXT(tmp, t4) "s_memrealtime vcc\n\t.p2align 5\n1:\n\ts_waitcnt lgkmcnt(0)\n\ts_sub_u32 " tmp ", " t4 ", vcc_lo\n\ts_memrealtime vcc\n\ts_cmpk_lt_u32 " tmp ", 400\n\ts_cbranch_scc1 1b\n\t" \
                          "s_add_u32 " tmp ", " tmp ", 4\n\ts_cselect_b32 " tmp ", " tmp ", 0\n2:\n\ts_sub_u32 " tmp ", " tmp ", 1\n\ts_cbranch_scc0 2b\n\t"
DEV void wait_until(u32 t) { u32 tmp; asm volatile(WAIT_TXT("%0", "%1") "s_waitcnt lgkmcnt(0)" : "=&s"(tmp) : "s"(uu(t - 4u)) : "vcc", "scc", "memory"); }
#define SMF_A(acc, a, b) asm volatile("v_smfmac_f32_16x16x64_bf16 %0, %1, %2, %3" : "+v"(acc) : "v"(a), "a"(b), "v"(idx))
#define SMF_V(acc, a, b) asm volatile("v_smfmac_f32_16x16x64_bf16 %0, %1, %2, %3" : "+v"(acc) : "v"(a), "v"(b), "v"(idx))
#define MF0(d, a, b) asm volatile("v_mfma_f32_16x16x32_bf16 %0, %1, %2, 0" : "=&v"(d) : "v"(a), "v"(b))
#define MF1(d, a, b) asm volatile("v_mfma_f32_16x16x32_bf16 %0, %1, %2, %0" : "+v"(d) : "v"(a), "v"(b))
#define MF0H(d, a, b) asm volatile("v_mfma_f32_16x16x16_bf16 %0, %1, %2, 0" : "=&v"(d) : "v"(a), "v"(b))
#define MF1H(d, a, b) asm volatile("v_mfma_f32_16x16x16_bf16 %0, %1, %2, %0" : "+v"(d) : "v"(a), "v"(b))
DEV v8u tr_frag(lptr p0, lptr p1, lptr p2, lptr p3) { return cat8(tr2(p0, p1), tr2(p2, p3)); }
constexpr int qslot(int ks) { return (ks & 1) && ks < 8 ? ks >> 1 : -1; }
constexpr int kslot(int ks) { return ks < 4 ? 4 + ks : -1; }
constexpr int vslot(int ks) { return ks >= 4 ? 4 + ks : -1; }
DEV u32 ring_map(u32 off, u32 half) { return ((off >> 13) << 14) + (half << 13) + (off & 8191u); }
#define ZRD(x, i) x = __builtin_bit_cast(f32x4, lds_r16(ZP + 16 * (i)))
#define VMAX(d, a, b) asm volatile("v_max_f32 %0, %1, %2" : "=v"(d) : "v"(a), "v"(b))
#define VMAX3(d, a, b, c_) asm volatile("v_max3_f32 %0, %1, %2, %3" : "=v"(d) : "v"(a), "v"(b), "v"(c_))
#define VEXP(d, a) asm volatile("v_exp_f32 %0, %1" : "=v"(d) : "v"(a))
#define VMULC(d, a) asm volatile("v_mul_f32 %0, 0x3e0293ee, %1" : "=v"(d) : "v"(a))
#define CADD(d, a, b) do { d = (a) + (b); PIN(d); } while (0)
#define CMULC(d, a) do { d = (a) * CEXP; PIN(d); } while (0)
#define CCVT(d, a, b) do { d = pk(a, b); PIN(d); } while (0)
#define LGK(n) __builtin_amdgcn_s_waitcnt(0xC07F | (n) << 8)
#define SEL(d, a, b) asm volatile("v_cndmask_b32 %0, %1, %2, %3" : "=v"(d) : "v"(a), "v"(b), "s"(himask))
#define QPERM(d, a, q) asm volatile("v_mov_b32_dpp %0, %1 quad_perm:" q " row_mask:0xf bank_mask:0xf" : "=v"(d) : "v"(a))

#define AL_H 3
#define AL_W 0
#define HD_SMP 56
DEV void role_head(const Params& p, lptr lds, int L, int h, v2u ep) {
    const int tid = threadIdx.x, lane = tid & 63, w = uni(tid >> 6), g = lane >> 4, r = lane & 15;
    const rsrc_t rs = mk_rsrc(p.ws, W_END);
    v8u wq0[10], wq1[10], wk0[10], wk1[10], wv0[10], wv1[10];
    lptr WL = lds + C_WL + w * (NLF * 2048) + lane * 16;
    {
        constexpr int GS = 1056, CS = 48 * GS;
        const v4u sq = mk_srd(p.Wq, DM * NQ * 2), sk = mk_srd(p.Wk, DM * NQ * 2), sv = mk_srd(p.Wv, DM * NQ * 2);
        const u32 vo = (u32)(g * NQ + h * DH + 8 * (r ^ ((g & 1) << 3))) * 2u;
        auto fill = [&](int ch) {
#pragma unroll 1
            for (int i = 0; i < 4; ++i) {
                const u32 dst = uu((ch % 3) * CS + (4 * i + w) * GS), so = uu((ch * 64 + 16 * i + 4 * w) * (NQ * 2));
                DMAIN(sq, dst, vo, so); DMAIN(sk, dst + 16 * GS, vo, so); DMAIN(sv, dst + 32 * GS, vo, so);
            }
        };
        fill(0); fill(1); fill(2);
        const int q4 = r >> 2;
        lptr tb = lds + (4 * (g >> 1) + 2 * (q4 >> 1) + (g & 1)) * GS + (q4 & 1) * 256 + ((w * 64 + (r & 3) * 8) ^ ((q4 & 1) << 7));
        SFOR(ch, 10)
            VMW(ch < 8 ? 24 : 108 - 12 * ch);
            BAR();
            auto frag = [&](int m, int t) { lptr b = tb + (ch % 3) * CS + m * 16 * GS + t * 32; return tr_frag(b, b + 512, b + 8 * GS, b + 8 * GS + 512); };
            wq0[ch] = frag(0, 0); wq1[ch] = frag(0, 1); wk0[ch] = frag(1, 0); wk1[ch] = frag(1, 1); wv0[ch] = frag(2, 0); wv1[ch] = frag(2, 1);
            asm volatile("" : "+a"(wq0[ch]), "+a"(wk0[ch]), "+v"(wv0[ch]), "+v"(wv1[ch]));
            if constexpr (qslot(ch) < 0) asm volatile("" : "+a"(wq1[ch])); else asm volatile("" : "+v"(wq1[ch]));
            if constexpr (kslot(ch) < 0) asm volatile("" : "+a"(wk1[ch])); else asm volatile("" : "+v"(wk1[ch]));
            BAR();
            if constexpr (ch < 7) fill(ch + 3);
        SEND
#pragma unroll
        for (int ch = 0; ch < 10; ++ch) {
            if (qslot(ch) >= 0) { lds_w16(WL + qslot(ch) * 2048, wq1[ch].lo); lds_w16(WL + qslot(ch) * 2048 + 1024, wq1[ch].hi); }
            if (kslot(ch) >= 0) { lds_w16(WL + kslot(ch) * 2048, wk1[ch].lo); lds_w16(WL + kslot(ch) * 2048 + 1024, wk1[ch].hi); }
            if (vslot(ch) >= 0) { lds_w16(WL + vslot(ch) * 2048, wv1[ch].lo); lds_w16(WL + vslot(ch) * 2048 + 1024, wv1[ch].hi); }
        }
        CFENCE();
    }
    const u32 idx = (r & 2) ? 0xEEEEu : 0x4444u;
    unsigned long long himask = 0xCCCCCCCCCCCCCCCCull;
    PINS(himask);
    bool dead = false;
    if (h == 3) LST(266 + L, rt32());
    if (w == 0) LST(1240 + L * 32 + h, rt32());
    if (L == 1) {
        KvIn in;
        kvt_load(p, KT0 + 4 * h + w, in, [] {}); kvt_make(rs, lds + w * 9216, KT0 + 4 * h + w, in);
        DRAIN(); BAR();
        pre_post(rs, 64 + h, 3u, ep);
    }
    const u32 pol0 = pol_wait(rs, ep);
    if (h == 3 && L == 0) LST(277, rt32());
    const u32 l16 = (u32)lane * 16u;
    const u32 mring = W_MRING + (u32)L * (MB / 4) + pol0 * 8192u, hint = mring + 4 * 32768u;
    const u32 fkh = lane < 3 ? foff(F_QKV + L * 96 + lane * 32 + h) : lane < 7 ? hint + (u32)(lane - 3) * 4u : OOB;
    u32 tm = 0;
    MK_SRD();
    u32 ksrc = uu(W_KQ + (u32)(L * (NSEG - 1) * NH + h) * 16384u + w * 2048), vsrc = ksrc + (W_VO - W_KQ);
    u32 dk = uu(C_K + w * 2048), dv = dk + (C_V - C_K), dq = dk + (C_QX - C_K), dm = uu(C_MEM + w * 2048), dm2 = uu(w < 2 ? C_MEM + 8192 + w * 1024 : C_DUMP);
    asm volatile("" : "+s"(dk), "+s"(dv), "+s"(dq), "+s"(dm), "+s"(dm2));
    auto keys_wait = [&](u32 need) {
#pragma unroll 1
        for (int n = 0; !dead; ++n) {
            const bool beat = need + 2 * L > 2u;
            if (beat) wait_until(tm);
            const u32 hv = ld4(rs, lane == 7 ? foff(F_ERR) : fkh);
            if (beat) { tm = rdl(hv, 5); wait_until(rdl(hv, 3) - 30u); }
            if (((u32)__builtin_amdgcn_ballot_w64(ld4(rs, fkh) < need) & 7u) == 0u) break;
            dead = n > SPIN_CAP || rdl(hv, 7) != 0u;
            st4s(rs, dead ? foff(F_ERR) : OOB, 0, 1u);
        }
        CFENCE();
        DMA32(dk, l16, ksrc); DMA32(dv, l16, vsrc); DMA32(dq, l16, ksrc ^ 8192u);
    };
    if (L == 0) { st4s(rs, tid == 0 ? hint + 4096u + (u32)h * 128u : OOB, 0, 0u); keys_wait(1u); }
    lptr qw = lds + C_Q + (2 * g) * 288 + (32 * w + 2 * r) * 2;
    lptr kw = qw + (C_KM - C_Q); PIN(kw);
    lptr qr = lds + C_Q + (2 * (r >> 2) + (r & 1)) * 288 + g * 16;
    lptr LB = lds + l16, VX = lds + C_V + w * 2048 + l16;
    PIN(VX);
    if (tid >= 64 && tid < 70) { const u32 z = tid == 68 ? 0x3F803F80u : tid == 69 ? PSENT : 0u; lds_w16(lds + C_ZERO + (tid - 64) * 16, (v4u){z, z, z, z}); }
    u32 zaddr = 0; PIN(zaddr);
    lptr ZP = lds + zaddr + C_ZERO;
    const float CEXP = SCALE * LOG2E;
#define LF_RD(slot, hf) lf[slot][hf] = lds_r16(WL + (slot) * 2048 + (hf) * 1024)
    BAR();
    const u32 ofl = tid == 0 ? foff(F_O + L * 32 + h) : OOB;
    u32 oso = (((g | L) & 1) ? OOB : 0u) + W_ORING + (u32)L * (MB / 2) + ring_map((u32)((r >> 3) * 32768 + h * 1024 + w * 256 + (g >> 1) * 128 + (r & 7) * 16), pol0);
    unsigned long long vis[8];
#pragma unroll
    for (int i = 0; i < 8; ++i) vis[i] = __builtin_amdgcn_ballot_w64(16 * (i >> 2) + 4 * g + (i & 3) <= 16 * (w & 1) + r);
    u32 missed = 0;
    for (int s = -L; s < NSEG;) {
        u32 sv = (u32)max(s, 0), ms = mring + (sv & 3u) * 32768u + w * 2048, ms2 = mring + (sv & 3u) * 32768u + 16384u + (w & 1) * 1024;
        asm volatile("" : "+s"(ms), "+s"(ms2)); PIN(sv);
        f32x4 aq0, aq1, ak0, ak1;
        v4u lf[NLF][2];
        ZRD(aq0, 0); ZRD(aq1, 1); ZRD(ak0, 2); ZRD(ak1, 3);
#pragma unroll
        for (int i = 0; i < 4; ++i) { LF_RD(i, 0); LF_RD(i, 1); }
        SCHED();
        v4u A[10];
#define HT(k) do {} while (0)
        v4u kx[8], qf[4], kmf[4], vx[2], ones, P;
        float fa, fb, fc, fd, inv, sx[4], t[4], tm0, tm1; u32 pa, pb, fl, e01, e23, pmm;
        f32x4 av0, av1, s0, s1, sm, oa, ob, rsum;
        v2u vm, pm;
#define LW(sl) cat8(lf[sl][0], lf[sl][1])
#define PQ(ks, tl) do { if constexpr (!(tl)) SMF_A(aq0, A[ks], wq0[ks]); else if constexpr (qslot(ks) < 0) SMF_A(aq1, A[ks], wq1[ks]); else { const v8u lw_ = LW(qslot(ks)); SMF_V(aq1, A[ks], lw_); } } while (0)
#define PK(ks, tl) do { if constexpr (!(tl)) SMF_A(ak0, A[ks], wk0[ks]); else if constexpr (kslot(ks) < 0) SMF_A(ak1, A[ks], wk1[ks]); else { const v8u lw_ = LW(kslot(ks)); SMF_V(ak1, A[ks], lw_); } } while (0)
#define PV(ks, tl) do { if constexpr (!(tl)) SMF_V(av0, A[ks], wv0[ks]); else if constexpr (vslot(ks) < 0) SMF_V(av1, A[ks], wv1[ks]); else { const v8u lw_ = LW(vslot(ks)); SMF_V(av1, A[ks], lw_); } } while (0)
#define KXR(i) kx[i] = lds_r16(LB + C_K + ((i) & 1) * 4096 + ((i) >> 1) * 1024)
#define XW(q_) do { *(LAS u32*)(q_) = pa; *(LAS u32*)((q_) + 288) = pb; } while (0)
        auto slot = [&](auto zc) __attribute__((always_inline)) {
            constexpr int z = decltype(zc)::value;
            if constexpr (z < 14) PQ(z >> 1, z & 1);
            else if constexpr (z < 17) PQ(z - 7, 0);
            else if constexpr (z < 20) PQ(z - 10, 1);
            else if constexpr (z < 34) PK((z - 20) >> 1, z & 1);
            else if constexpr (z < 37) PK(z - 27, 0);
            else if constexpr (z < 40) PK(z - 30, 1);
            else if constexpr (z < 42) { if constexpr (z == 40) MF0(s0, kx[0], qf[0]); else MF0(s1, kx[1], qf[0]); }
            else if constexpr (z < 48) { if constexpr (z & 1) MF1(s1, kx[z - 40], qf[(z - 40) >> 1]); else MF1(s0, kx[z - 40], qf[(z - 40) >> 1]); }
            else if constexpr (z < 54) PV(z - 48, 0);
            else if constexpr (z == 54) MF0(sm, kmf[0], qf[0]);
            else if constexpr (z < 58) MF1(sm, kmf[z - 54], qf[z - 54]);
            else if constexpr (z < 62) PV(z - 52, 0);
            else if constexpr (z < 64) PV(z - 62, 1);
            else if constexpr (z == 64) MF0(oa, vx[0], P);
            else if constexpr (z == 65) MF0(ob, vx[1], P);
            else if constexpr (z == 66) MF0(rsum, ones, P);
            else if constexpr (z < 75) PV(z < 70 ? z - 63 : z == 70 ? 2 : z == 71 ? 7 : z == 72 ? 3 : z - 65, 1);
            else { const v2u ones2 = ones.lo; MF1H(rsum, ones2, pm); }
            if constexpr (z < 6) A[4 + z] = lds_r16(LB + C_MEM + (4 + z) * 1024);
            if constexpr (z >= 6 && z < 10) LF_RD(4 + ((z - 6) >> 1), z & 1);
            if constexpr (z >= 14 && z < 18) LF_RD(6 + ((z - 14) >> 1), z & 1);
            if constexpr (z >= 22 && z < 28) KXR(z - 22);
            if constexpr (z == 28) XW(qw);
            if constexpr (z == 29 || z == 30) KXR(z - 23);
            if constexpr (z == 32 || z == 47) asm volatile("s_barrier" ::: "memory");
            if constexpr (z >= 33 && z < 37) qf[z - 33] = lds_r16(qr + (z - 33) * 64);
            if constexpr (z == 20) ZRD(av0, 0);
            if constexpr (z == 43) XW(kw);
            if constexpr (z == 44 || z == 45) vx[z - 44] = lds_r16(VX + (z - 44) * 1024);
            if constexpr (z >= 48 && z < 52) kmf[z - 48] = lds_r16(qr + (C_KM - C_Q) + (z - 48) * 64);
            if constexpr (z == 52) ZRD(av1, 1);
            if constexpr (z == 37) ones = lds_r16(ZP + 64);
            if constexpr (z >= 57 && z < 64) LF_RD(8 + ((z - 57) >> 1), (z - 57) & 1);
            if constexpr (z >= 65 && z < 70) LF_RD(8 + ((z - 58) >> 1), (z - 58) & 1);
            if constexpr (z == HD_SMP) LD4A(fl, fkh, 0);
            SCHED();
            if constexpr (z == 19) HT(2);
            if constexpr (z == 32) HT(3);
            if constexpr (z == 47) HT(4);
            if constexpr (z == 64) HT(5);
            if constexpr (z == 19) LGK(4);
            if constexpr (z == 21 || z == 41) LGK(1);
            if constexpr (z == 56 || z == 73) LGK(0);
            if constexpr (z == 31 || z == 46) LGK(2);
            if constexpr (z == 39) LGK(4);
            if constexpr (z >= 53 && z < 56) LGK(57 - z);
            if constexpr (z == 64) LGK(3);
            if constexpr (z == 68) LGK(5);
            if constexpr (z >= 70 && z < 73) LGK(74 - z);
            SCHED();
            if constexpr (z == 18) CADD(fa, aq0[0], aq0[2]);
            if constexpr (z == 19) CADD(fb, aq0[1], aq0[3]);
            if constexpr (z == 20) CMULC(fa, fa);
            if constexpr (z == 21) CADD(fc, aq1[0], aq1[2]);
            if constexpr (z == 22) CADD(fd, aq1[1], aq1[3]);
            if constexpr (z == 23) CMULC(fc, fc);
            if constexpr (z == 24) CMULC(fb, fb);
            if constexpr (z == 25) CMULC(fd, fd);
            if constexpr (z == 26) CCVT(pa, fa, fc);
            if constexpr (z == 27) CCVT(pb, fb, fd);
            if constexpr (z == 38) CADD(fa, ak0[0], ak0[2]);
            if constexpr (z == 39) CADD(fb, ak0[1], ak0[3]);
            if constexpr (z == 41) { CADD(fc, ak1[0], ak1[2]); CADD(fd, ak1[1], ak1[3]); }
            if constexpr (z == 42) { CCVT(pa, fa, fc); CCVT(pb, fb, fd); }
            if constexpr (z >= 49 && z < 53) SEL(sx[z - 49], s0[z - 49], s1[z - 49]);
            if constexpr (z >= 53 && z < 57) VEXP(t[z - 53], sx[z - 53]);
            if constexpr (z == 57) CCVT(e01, t[0], t[1]);
            if constexpr (z == 58) CCVT(e23, t[2], t[3]);
            if constexpr (z == 59) QPERM(P.x, e01, "[0,1,0,1]");
            if constexpr (z == 60) QPERM(P.z, e01, "[2,3,2,3]");
            if constexpr (z == 61) QPERM(P.y, e23, "[0,1,0,1]");
            if constexpr (z == 62) QPERM(P.w, e23, "[2,3,2,3]");
            if constexpr (z == 63) CADD(fa, av0[0], av0[2]);
            if constexpr (z == 64) { asm volatile("" :: "v"(sm)); VEXP(tm0, sm[0]); }
            if constexpr (z == 65) VEXP(tm1, sm[1]);
            if constexpr (z == 66) CADD(fb, av0[1], av0[3]);
            if constexpr (z == 67) CCVT(pmm, tm0, tm1);
            if constexpr (z >= 68 && z < 72) SEL(oa[z - 68], oa[z - 68], ob[z - 68]);
            if constexpr (z == 72) asm volatile("v_cndmask_b32 %0, %1, 0, %2" : "=v"(pm.x) : "v"(pmm), "s"(himask));
            if constexpr (z == 73) asm volatile("v_cndmask_b32 %0, 0, %1, %2" : "=v"(pm.y) : "v"(pmm), "s"(himask));
            if constexpr (z == 74) LGK(15);
            if constexpr (z == 75) CCVT(vm.x, fa, fb);
            SCHED();
        };
        {
            u32 tmp;
            asm volatile("s_mov_b32 m0, %2\n\t" WAIT_TXT("%0", "%1") "buffer_load_dwordx4 %4, %5, %6 offen sc1 lds\n\tbuffer_load_dwordx4 %4, %5, %6 offen offset:1024 sc1 lds\n\t"
                         "s_mov_b32 m0, %3\n\ts_nop 0\n\tbuffer_load_dwordx4 %4, %5, %7 offen sc1 lds\n\t.p2align 6\n\t.rept %8\n\ts_nop 0\n\t.endr\n\ts_waitcnt vmcnt(0) lgkmcnt(0)\n\ts_barrier"
                         : "=&s"(tmp) : "s"(uu(tm - 4u)), "s"(dm), "s"(dm2), "v"(l16), "s"(srd), "s"(ms), "s"(ms2), "i"(AL_H) : "vcc", "scc", "memory");
        }
        HT(1);
#pragma unroll
        for (int ks = 0; ks < 4; ++ks) A[ks] = lds_r16(LB + C_MEM + ks * 1024);
        SCHED();
        st4s(rs, ofl, 0, sv);
        SCHED();
        static_for<0, 76>(slot);
        asm volatile("s_nop 2\n\tv_add_f32 %1, %3, %5\n\tv_add_f32 %2, %4, %6\n\tv_cvt_pk_bf16_f32 %0, %1, %2"
                     : "=v"(vm.y), "=&v"(fc), "=&v"(fd) : "v"(av1[0]), "v"(av1[1]), "v"(av1[2]), "v"(av1[3]));
        __builtin_amdgcn_s_waitcnt(0xCF7F);
        asm volatile("s_nop 0\n\tv_mfma_f32_16x16x16_bf16 %0, %2, %3, %0\n\tv_rcp_f32 %1, %4" : "+v"(oa), "=&v"(inv) : "v"(vm), "v"(pm), "v"(rsum[0]), "v"(rsum));
        SCHED();
        if (__builtin_expect(__builtin_amdgcn_ballot_w64(u_(rsum[0]) - 0x21800000u > 0x3C000000u) != 0ull, 0)) {
            DRAIN(); SCHED();
            bool miss = false;
#pragma unroll 1
            for (int i = 0; i < 10; ++i) { const v4u a = lds_r16(LB + C_MEM + i * 1024); miss |= a.x == PSENT || a.y == PSENT || a.z == PSENT || a.w == PSENT; }
            if (missed < SPIN_CAP && !all_(!miss)) {
                *(LAS u32*)(lds + C_DUMP + (w == 0 ? 0 : 64)) = ld4(rs, foff(F_ERR));
                asm volatile("" : "+v"(fl) :: "memory");
                tm = rdl(fl, 5);
                BAR();
                missed = uu(*(LAS u32*)(lds + C_DUMP)) != 0u ? SPIN_CAP : missed + 1;
                st4s(rs, missed == SPIN_CAP ? foff(F_ERR) : OOB, 0, 1u);
                BAR();
                continue;
            }
            float mx = fmaxf(fmaxf(fmaxf(sx[0], sx[1]), fmaxf(sx[2], sx[3])), fmaxf(sm[0], sm[1]));
            mx = max32(max16(fmaxf(mx, dpp<0x4E>(mx))));
#pragma unroll
            for (int i = 0; i < 4; ++i) t[i] = __builtin_amdgcn_exp2f(sx[i] - mx);
            e01 = pk(t[0], t[1]); e23 = pk(t[2], t[3]);
            pmm = pk(__builtin_amdgcn_exp2f(sm[0] - mx), __builtin_amdgcn_exp2f(sm[1] - mx));
            asm volatile("v_cndmask_b32 %0, %2, 0, %3\n\tv_cndmask_b32 %1, 0, %2, %3" : "=&v"(pm.x), "=&v"(pm.y) : "v"(pmm), "s"(himask));
            asm volatile("s_nop 1" : "+v"(e01), "+v"(e23), "+v"(pm));
            QPERM(P.x, e01, "[0,1,0,1]"); QPERM(P.z, e01, "[2,3,2,3]"); QPERM(P.y, e23, "[0,1,0,1]"); QPERM(P.w, e23, "[2,3,2,3]");
            NOPS(1);
            MF0(rsum, ones, P); MF0(oa, vx[0], P); MF0(ob, vx[1], P);
            NOPS(7);
            { const v2u ones2 = ones.lo; MF1H(rsum, ones2, pm); }
#pragma unroll
            for (int i = 0; i < 4; ++i) SEL(oa[i], oa[i], ob[i]);
            NOPS(1);
            MF1H(oa, vm, pm);
            asm volatile("s_nop 7\n\tv_rcp_f32 %0, %1" : "=v"(inv) : "v"(rsum[0]), "v"(rsum));
        }
        {
            v4u oc; f32x2 u0 = oa.lo, u1 = oa.hi; const f32x2 i2 = {inv, inv};
            asm volatile("s_nop 0\n\tv_pk_mul_f32 %0, %0, %2\n\tv_pk_mul_f32 %1, %1, %2" : "+v"(u0), "+v"(u1) : "v"(i2));
            __builtin_amdgcn_s_waitcnt(0xCF7F);
            asm volatile("v_cvt_pk_bf16_f32 %0, %4, %5\n\tv_cvt_pk_bf16_f32 %2, %4, %5\n\tv_cvt_pk_bf16_f32 %1, %6, %7\n\tv_cvt_pk_bf16_f32 %3, %6, %7\n\t"
                         "v_permlane16_swap_b32 %0, %2\n\ts_nop 0\n\tv_permlane16_swap_b32 %1, %3"
                         : "=&v"(oc.x), "=&v"(oc.y), "=&v"(oc.z), "=&v"(oc.w) : "v"(u0.x), "v"(u0.y), "v"(u1.x), "v"(u1.y));
            st16(rs, oso, oc);
        }
        SCHED();
        unsigned long long clk;
        asm volatile("s_memrealtime %0" : "=s"(clk) :: "memory");
        HT(6);
        v4u tq[4], tk[8], tv[2];
        f32x4 c0, c1, cm;
        u32 oo, kvo; bool kvok;
        u32 ln = l16; PIN(ln);
        lptr QX = lds + C_QX + (w & 1) * 4096 + ln, VO = lds + C_V + (w ^ 1) * 2048 + ln, VMX = lds + C_VM + w * 512 + (ln >> 1);
#pragma unroll
        for (int i = 0; i < 2; ++i) { tq[i] = lds_r16(QX + i * 1024); kmf[i] = lds_r16(qr + (C_KM - C_Q) + i * 64); tk[2 * i] = lds_r16(LB + C_K + i * 1024); tk[2 * i + 1] = lds_r16(LB + C_K + 4096 + i * 1024); }
        *(LAS v2u*)VMX = vm;
        SCHED();
        SFOR(i, 12)
            if constexpr (i == 0) MF0(cm, kmf[0], tq[0]); else if constexpr (i == 1) MF0(c0, tk[0], tq[0]); else if constexpr (i == 2) MF0(c1, tk[1], tq[0]);
            else if constexpr (i % 3 == 0) MF1(cm, kmf[i / 3], tq[i / 3]); else if constexpr (i % 3 == 1) MF1(c0, tk[2 * (i / 3)], tq[i / 3]); else MF1(c1, tk[2 * (i / 3) + 1], tq[i / 3]);
            if constexpr (i < 2) kmf[2 + i] = lds_r16(qr + (C_KM - C_Q) + (2 + i) * 64);
            if constexpr (i == 0) { tq[2] = lds_r16(QX + 2048); tk[4] = lds_r16(LB + C_K + 2048); }
            if constexpr (i == 1) { tk[5] = lds_r16(LB + C_K + 6144); tq[3] = lds_r16(QX + 3072); }
            if constexpr (i == 2) { tk[6] = lds_r16(LB + C_K + 3072); tk[7] = lds_r16(LB + C_K + 7168); }
            if constexpr (i == 3) { tv[0] = lds_r16(VO); tv[1] = lds_r16(VO + 1024); }
            if constexpr (i == 4) { oo = ln + (ln & 0x300u) + ((vsrc ^ 8192u) + (u32)((w & 1) * 256) + ((u32)s & OOB)); PIN(oo); }
            if constexpr (i == 5) { ksrc += NH * 16384; vsrc += NH * 16384; PINS(ksrc); PINS(vsrc); }
            if constexpr (i == 9) { asm volatile("s_waitcnt vmcnt(1)" : "+v"(fl) :: "memory"); kvok = ((u32)__builtin_amdgcn_ballot_w64(fl < (u32)s + 2) & 7u) == 0u; }
            if constexpr (i == 10) { kvo = l16 | (kvok ? 0u : OOB); PIN(kvo); }
            SCHED();
        SEND
        asm volatile("s_waitcnt lgkmcnt(0)\n\ts_barrier" : "+s"(clk) :: "memory");
        const v2u tvm = *(LAS v2u*)(VMX + (512 - (w & 1) * 1024));
        const v4u pv = lds_r16(ZP + 80);
        DMA32(dk, kvo, ksrc);
        SCHED(); asm volatile("" :: "v"(cm));
        float u[10], m, m2;
        {
            float NEG = -1000000000.0f / SCALE; PIN(NEG);
#define MSK(d, a, i) asm volatile("v_cndmask_b32 %0, %1, %2, %3" : "=v"(d) : "v"(NEG), "v"(a), "s"(vis[i]))
#pragma unroll
            for (int i = 0; i < 4; ++i) MSK(u[i], c0[i], i);
#pragma unroll
            for (int i = 0; i < 4; ++i) MSK(u[4 + i], c1[i], 4 + i);
            asm volatile("v_max3_f32 %0, %2, %3, %10\n\tv_max3_f32 %1, %4, %5, %11\n\tv_max3_f32 %0, %0, %6, %7\n\tv_max3_f32 %1, %1, %8, %9\n\t"
                         "v_max_f32 %0, %0, %1\n\tv_mov_b32 %1, %0\n\ts_nop 1\n\tv_permlane16_swap_b32 %0, %1\n\tv_max_f32 %0, %0, %1\n\tv_mov_b32 %1, %0\n\ts_nop 1\n\t"
                         "v_permlane32_swap_b32 %0, %1\n\tv_max_f32 %0, %0, %1\n\tv_mul_f32 %0, 0xbe0293ee, %0"
                         : "=&v"(m), "=&v"(m2) : "v"(u[0]), "v"(u[1]), "v"(u[2]), "v"(u[3]), "v"(u[4]), "v"(u[5]), "v"(u[6]), "v"(u[7]), "v"(cm[0]), "v"(cm[1]));
        }
        DMA32(dv, kvo, vsrc);
        {
            v4u px; v2u p0, p1;
#define EX2(d, a, b) asm volatile("v_fmamk_f32 %0, %2, 0x3e0293ee, %4\n\tv_fmamk_f32 %1, %3, 0x3e0293ee, %4\n\tv_exp_f32 %0, %0\n\tv_exp_f32 %1, %1" : "=&v"(d.x), "=&v"(d.y) : "v"(a), "v"(b), "v"(m))
            f32x2 ex[5];
            EX2(ex[0], u[0], u[1]); EX2(ex[1], u[2], u[3]); EX2(ex[2], u[4], u[5]); EX2(ex[3], u[6], u[7]); EX2(ex[4], cm[0], cm[1]);
            asm volatile("v_cvt_pk_bf16_f32 %0, %8, %9\n\tv_cvt_pk_bf16_f32 %1, %10, %11\n\tv_cvt_pk_bf16_f32 %2, %12, %13\n\tv_cvt_pk_bf16_f32 %3, %14, %15\n\t"
                         "v_mov_b32 %5, 0\n\tv_mov_b32 %6, 0\n\tv_cvt_pk_bf16_f32 %4, %16, %17\n\tv_cvt_pk_bf16_f32 %7, %16, %17"
                         : "=&v"(px.x), "=&v"(px.y), "=&v"(px.z), "=&v"(px.w), "=&v"(p0.x), "=&v"(p0.y), "=&v"(p1.x), "=&v"(p1.y)
                         : "v"(ex[0].x), "v"(ex[0].y), "v"(ex[1].x), "v"(ex[1].y), "v"(ex[2].x), "v"(ex[2].y), "v"(ex[3].x), "v"(ex[3].y), "v"(ex[4].x), "v"(ex[4].y));
            f32x4 o[4], sum;
            const v2u ones2 = ones.lo;
            const u32 now = (u32)clk;
            u32 oz;
            SCHED();
            MF0(sum, ones, px); { const u32 q = ksrc ^ 8192u; DMA32(dq, kvo, q); } SCHED();
            MF0(o[0], vx[0], px);
            MF0(o[1], vx[1], px);
            MF0(o[2], tv[0], px); oz = (oso & ~(3u << 17)) | (u32)((s + 3) & 3) << 17; PIN(oz); SCHED();
            MF0(o[3], tv[1], px); oso = (oso & ~(3u << 17)) | (u32)((s + 1) & 3) << 17; PIN(oso); SCHED();
            MF1H(sum, ones2, p0);
            MF1H(o[0], vm, p0);
            MF1H(o[1], vm, p1); st16(rs, oz, pv); SCHED();
            MF1H(o[2], tvm, p0);
            MF1H(o[3], tvm, p1);
            asm volatile("s_nop 2\n\tv_rcp_f32 %0, %1\n\ts_nop 0" : "=v"(inv) : "v"(sum[0]), "v"(sum));
            asm volatile("" : "+v"(o[0]), "+v"(o[1]));
            st16(rs, oo, (v4u){pk2(o[0].lo * inv), pk2(o[0].hi * inv), pk2(o[1].lo * inv), pk2(o[1].hi * inv)});
            asm volatile("" : "+v"(o[2]), "+v"(o[3]));
            st16(rs, oo ^ 2048u, (v4u){pk2(o[2].lo * inv), pk2(o[2].hi * inv), pk2(o[3].lo * inv), pk2(o[3].hi * inv)});
            SCHED();
            LS2(L == 0 && w == 0, h);
            st4s(rs, (h == 0 && tid == 192) ? hint + 128u : OOB, 0, (u32)s << 16 | min(missed, 1u) << 14 | min(now - tm, 0x3FFFu));
            tm = rdl(fl, 5);
            missed = missed == SPIN_CAP ? missed : 0u;
            if (__builtin_expect((int)(rdl(fl, 3) - now) < CH_VIS - 4 || s < 1 - L, 0)) {
                const bool first = s == 0 && L == 0;
                if (s < 0) oso -= (ln & 256u) ? 0u : OOB;
#pragma unroll 1
                for (int n = 0; n < (s < 0 ? 0 : first ? 200 : 8); ++n) {
                    CFENCE();
                    const u32 hv = ld4(rs, fkh);
                    tm = rdl(hv, first ? 6 : 5);
                    if (first ? tm != PSENT : (int)(rdl(hv, 3) - now) >= CH_VIS - 4) break;
                }
            }
        }
        if (__builtin_expect(!kvok, 0) && s + 1 < NSEG) {
            st4s(rs, ln == 0 ? hint + 4096u + (u32)h * 128u : OOB, 0, (u32)s + 1u);
            keys_wait((u32)s + 2u);
        }
        ++s;
    }
    DRAIN(); BAR();
    st4s(rs, ofl, 0, NSEG);
}

#define WO_NL 3
#define WO_ROT 0
#define WO_AH 1
DEV void role_wo(const Params& p, lptr lds, int j, v2u ep) {
    const int tid = threadIdx.x, lane = tid & 63, w = uni(tid >> 6), g = lane >> 4, r = lane & 15;
    const int rg = j & 1, cg = j >> 1, rot = WO_ROT;
    const rsrc_t rs = mk_rsrc(p.ws, W_END);
    v8u wo[8][5];
    {
        const v4u so_ = mk_srd(p.Wo, DM * NQ * 2);
        u32 vo[5];
#pragma unroll
        for (int i = 0; i < 5; ++i) { const int n = 64 * i + lane; vo[i] = (u32)((n / 5) * DM + cg * 40 + (n % 5) * 8) * 2u; }
        v4u junk = {};
        const u32 av = (u32)(j * 5120 + lane * 16);
        auto ahead = [&](int b) {
#pragma unroll
            for (int i = 0; i < 5; ++i) AHEAD(so_, av, uu((8 * w + b) * (DH * DM * 2) + i * 1024));
        };
        auto fill = [&](int b) {
            if (b + WO_AH < 8) ahead(b + WO_AH);
            const u32 dst = uu(w * 40960 + (b & 3) * 10240), src = uu((8 * w + ((b + rot) & 7)) * (DH * DM * 2));
#pragma unroll
            for (int i = 0; i < 10; ++i) DMAIN(so_, dst + i * 1024, vo[i % 5], src + (i / 5) * (64 * DM * 2));
        };
#pragma unroll 1
        for (int b = 0; b < WO_AH; ++b) ahead(b);
#pragma unroll 1
        for (int b = 0; b < 4; ++b) fill(b);
        const int q4 = r >> 2;
        lptr tb = lds + w * 40960 + (32 * (g >> 1) + 16 * (q4 >> 1) + 8 * ((r >> 1) & 1) + 4 * (g & 1) + (q4 & 1)) * 80 + (r & 1) * 8;
        SFOR(b, 8)
            VMW((b < 5 ? 30 : 70 - 10 * b) + (b + 1 + WO_AH < 8 ? 5 : 0) + (b + 2 + WO_AH < 8 ? 5 : 0) + (b + 3 + WO_AH < 8 ? 5 : 0));
            if constexpr (b + WO_AH == 7) PIN(junk);
#pragma unroll
            for (int t = 0; t < 5; ++t) {
                lptr q = tb + (b & 3) * 10240 + t * 16;
                wo[b][t] = tr_frag(q, q + 160, q + 64 * 80, q + 64 * 80 + 160);
                if (b * 5 + t < 32) asm volatile("" : "+a"(wo[b][t])); else asm volatile("" : "+v"(wo[b][t]));
            }
            CFENCE();
            if constexpr (b < 4) fill(b + 4);
        SEND
    }
    if (j == 0) LST(265, rt32());
    LST(1100 + j * 4 + w, rt32());
    const u32 idx = (r & 2) ? 0xEEEEu : 0x4444u;
    const u32 pol0 = pol_wait(rs, ep);
    BAR();
    const u32 l16 = (u32)lane * 16u;
    u32 oo[8];
#pragma unroll
    for (int i = 0; i < 8; ++i) oo[i] = uu(W_ORING + ring_map((u32)(rg * 32768 + (8 * w + ((i + rot) & 7)) * 1024), pol0));
    const u32 hint = W_MRING + 4 * 32768u + pol0 * 8192u;
    u32 hvo[2];
#pragma unroll
    for (int l = 0; l < 2; ++l) hvo[l] = lane < 2 ? hint + (u32)(l ^ 1) * (MB / 4) + (u32)lane * 4u : lane == 2 ? hint + (u32)l * (MB / 4) + 128u : lane == 3 ? foff(F_ERR) :
                 (lane >= 32 && j == 0 && w == 3) ? hint + (u32)l * (MB / 4) + (u32)lane * 128u : OOB;
    lptr xw = r < 8 ? lds + w * 640 + (2 * (g & 1) + (g >> 1)) * 32 + r * 4 : lds + 8192 + lane * 4;
    const int G = ((5 * cg) >> 1) + w, tt = 2 * G + ((lane >> 3) & 1) - 5 * cg;
    const bool pub = w < 3 && lane < 32 && tt >= 0 && tt < 5;
    lptr xr = lds + (pub ? tt * 128 + (2 * (lane >> 4) + ((lane >> 2) & 1)) * 32 + (lane & 3) * 8 : 0);
    const u32 mpo = pub ? W_MRING + ring_map((u32)(G * 256 + rg * 128 + lane * 4), pol0) : OOB;
    constexpr u32 PER0 = CH_W0 + CH_MW + CH_H0 + CH_MH + 2 * CH_VIS, DW0 = CH_W0 + CH_VIS + CH_MW + CH_REC;
    const bool lead = j == 0 && w == 3;
    const u32 hso = (lead && lane == 0) ? hint : OOB;
    MK_SRD();
    u32 t = 0, tn = 0;
    int sn[2] = {0, 0};
    u32 hav = 4 * CH_H0, wav = 4 * CH_W0, mh = 8 * CH_MH, mw = 8 * CH_MW, ok[2] = {0u, 0u}, dw[2] = {DW0, DW0};
    u32 vo = l16, early = PSENT, tp = 0, fin, left, cnt = 0;
    unsigned long long clk = 0;
    {
        if (w == 0) LST(1310 + j, rt32());
        if (lead) {
#pragma unroll 1
            for (int n = 0; n < SPIN_CAP; ++n) {
                CFENCE();
                const u32 a = ld4(rs, foff(F_QKV + lane)), b = ld4(rs, lane < 32 ? foff(F_QKV + 64 + lane) : hvo[0]);
                if (all_(a != 0u && b != (lane < 32 ? 0u : PSENT))) break;
                __builtin_amdgcn_s_sleep(1);
            }
        }
        if (lead) {
            t = rt32() + CH_T0; tn = t + PER0 / 2 + CH_REC;
            st16(rs, lane < 2 ? hint + (u32)lane * (MB / 4) : OOB, (v4u){lane == 0 ? t : tn, PER0, (lane == 0 ? t : tn) + DW0, t + DW0});
        } else {
#pragma unroll 1
            for (int n = 0; n < SPIN_CAP; ++n) {
                CFENCE();
                const u32 hv = ld4(rs, hvo[1]);
                t = rdl(hv, 0);
                if (rdl(hv, 1) < 400u) break;
                __builtin_amdgcn_s_sleep(1);
            }
        }
        clk = t - 100u;
    }
    const u32 tm0 = t + DW0;
    u32 zero = 0, hv = 0xFFFFu, pst = 1, pmp = OOB;
    const u32 psent = PSENT;
    lptr sw = lds + 2560 + w * 4;
    asm volatile("" : "+v"(zero), "+v"(sw));
    auto turn = [&](auto lc) __attribute__((always_inline)) {
        constexpr int L = decltype(lc)::value, M = L ^ 1;
        u32 done, rep, rd, lost, dh, dn, nt, za, pdur, mp; v4u hw;
        const int sc = sn[L];
        auto books = [&](auto ic) __attribute__((always_inline)) {
            constexpr int i = decltype(ic)::value;
            if constexpr (i == 0) { rep = rdl(hv, 2); PINS(rep); }
            if constexpr (i == 1) { lost = (u32)(__builtin_amdgcn_ballot_w64(hv == (u32)sn[M]) >> 32); PINS(lost); }
            if constexpr (i == 2) { lost |= (rep >> 16) ^ (u32)sn[M]; PINS(lost); }
            if constexpr (i == 3) { lost |= ok[M] ^ 2u; rd = rep & 0xFFFFu; PINS(lost); PINS(rd); }
            if constexpr (i == 4) { lost = lost == 0u ? CH_UP * 8u : 0u; PINS(lost); }
            if constexpr (i == 5) { dn = wav >> 2; PINS(dn); pdur = min((u32)clk - tp, dn + 8u); PINS(pdur); }
            if constexpr (i == 6) { pdur = (done & ok[M]) ? pdur : dn; PINS(pdur); }
            if constexpr (i == 7) { wav += pdur - dn; PINS(wav); }
            if constexpr (i == 8) { dh = hav >> 2; PINS(dh); nt = min(rd, dh + 8u); PINS(nt); }
            if constexpr (i == 9) { nt = rd < 0x3FFFu ? nt : dh; PINS(nt); }
            if constexpr (i == 10) { hav += nt - dh; PINS(hav); }
            if constexpr (i == 11) { mw = max(mw - 1u, CH_MW * 8u); PINS(mw); }
            if constexpr (i == 12) { mh = max(mh - 1u, CH_MH * 8u); PINS(mh); }
            if constexpr (i == 13) { rd = rd >> 14 == 1u ? lost : 0u; PINS(rd); }
            if constexpr (i == 14) { mw += rd; mh += lost - rd; PINS(mw); PINS(mh); }
            if constexpr (i == 15) { ok[M] = done ? 1u : (ok[M] & 1u) << 1; PINS(ok[M]); }
            if constexpr (i == 16) { dh = (2u * hav + mh) / 8u + (sn[1] < 3 ? CH_VIS + CH_PRE : CH_VIS); PINS(dh); }
            if constexpr (i == 17) { dn = (2u * wav + mw) / 8u + CH_VIS; PINS(dn); }
            if constexpr (i == 18) { nt = dw[L] + dh + (u32)max((int)((u32)clk + CH_WX - t), 0); PINS(nt); }
            if constexpr (i == 19) { dh += dn; dw[L] = dn; sn[M] += (int)done; PINS(dh); PINS(sn[M]); }
            if constexpr (i == 20) { rd = tn - t + (dh >> 1); PINS(rd); }
            if constexpr (i == 22) { nt = t + (u32)max((int)nt, (int)rd); PINS(nt); }
            if constexpr (i == 23) { hw.x = nt; hw.y = dh; PIN(hw.x); PIN(hw.y); }
            if constexpr (i == 24) { hw.z = nt + dn; hw.w = tm0; PIN(hw); }
            if constexpr (i == 25) st16(rs, hso + L * (MB / 4), hw);
        };
        {
#define WT(k) do {} while (0)
            WT(0);
            v4u d[8];
            {
                u32 tmp;
                asm volatile(WAIT_TXT("%1", "%2") "buffer_load_dwordx4 %0, %3, %4, %5 offen sc1\n\ts_waitcnt lgkmcnt(0)"
                             : "=&v"(d[0]), "=&s"(tmp) : "s"(uu(t - 4u)), "v"(vo), "s"(srd), "s"(oo[0]) : "vcc", "scc", "memory");
                PINS(clk);
            }
            SFOR(i, 7)
                asm volatile("buffer_load_dwordx4 %0, %1, %2, %3 offen sc1" : "=v"(d[i + 1]) : "v"(vo), "s"(srd), "s"(oo[i + 1]) : "memory");
                SCHED();
                if constexpr (i == 0) { done = uu(pst) ^ 1u; PINS(done); }
                static_for<3 * i, 3 * i + 3>(books);
                SCHED();
            SEND
            LD4A(hv, hvo[L], 0);
            SCHED();
            WT(1);
            static_for<22, 26>(books);
            za = done ? pmp : OOB;
            st4s(rs, za, 0, psent);
            *(LAS u32*)(sw + L * 4096) = zero;
            f32x4 acc[5] = {};
            mp = mpo + (L * (MB / 4) + (u32)((sc + 1) & 3) * 32768u); pmp = mpo + (L * (MB / 4) + (u32)(sc & 3) * 32768u);
            asm volatile("" : "+v"(acc[0]), "+v"(acc[1]), "+v"(acc[2]), "+v"(acc[3]), "+v"(acc[4]), "+v"(mp), "+v"(pmp));
            SCHED();
            float f0[5], f1[5]; unsigned long long bad;
#define WSW(o_) asm volatile("v_permlane32_swap_b32 %0, %1" : "+v"(f0[o_]), "+v"(f1[o_]))
#define WAD(o_) asm volatile("v_add_f32_dpp %0, %1, %0 row_ror:8 row_mask:0xf bank_mask:0xf" : "+v"(f0[o_]) : "v"(f1[o_]))
#define WFO(o_) do { VADD(f0[o_], acc[o_][0], acc[o_][2]); VADD(f1[o_], acc[o_][1], acc[o_][3]); } while (0)
#define WWR(o_) *(LAS float*)(xw + L * 4096 + (o_) * 128) = f0[o_]
            u32 th, per, so, erl = early;
            auto next = [&](auto ic_) __attribute__((always_inline)) {
                constexpr int i = decltype(ic_)::value;
                if constexpr (i == 0) { so = (u32)sn[M] << 17; PINS(so); }
                if constexpr (i == 1) { so = (so & 0x60000u) + M * (MB / 2); PINS(so); }
                if constexpr (i == 2) { so = sn[M] + 1 < NSEG ? so : OOB; PINS(so); }
                if constexpr (i == 3) { early = sn[M] + 1 < NSEG ? PSENT : 0u; PIN(early); }
                if constexpr (i == 4) { vo = l16 + so; PIN(vo); }
                if constexpr (i == 5 && L) { fin = (u32)(sn[0] + sn[1] + 2); PINS(fin); }
                if constexpr (i == 6 && L) { fin = (fin | uu(left)) >> 7; PIN(fin); }
                if constexpr (i == 6 && !L) { cnt += 1u; PIN(cnt); left = cnt >> 9; PIN(left); }
                if constexpr (i == 7) { PIN(hv); th = rdl(hv, 0); PINS(th); }
                if constexpr (i == 8) { per = rdl(hv, 1); PINS(per); so = th - t; PINS(so); }
                if constexpr (i == 9) { per = (int)so < 20 ? per : 0u; PINS(per); }
                if constexpr (i == 10) { th += per; PINS(th); if constexpr (L) { per = rdl(hv, 3); PINS(per); } }
                if constexpr (i == 11) { tp = t; t = lead ? tn : th; PINS(t); }
                if constexpr (i == 12) { if constexpr (L) { fin |= per; PIN(fin); } tn = nt; }
            };
            auto slot = [&](auto ic_) __attribute__((always_inline)) {
                constexpr int i = decltype(ic_)::value;
                constexpr int NL = WO_NL, I0 = 40 - 5 * NL, pc = i < I0 ? i / 5 : 8 - NL + (i - I0) % NL, oc = i < I0 ? i % 5 : (i - I0) / NL;
                if constexpr (oc == 0) asm volatile("s_waitcnt vmcnt(%1)" : "+v"(d[pc]) : "i"(10 - pc));
                if constexpr (pc * 5 + oc < 32) SMF_A(acc[oc], d[pc], wo[pc][oc]); else SMF_V(acc[oc], d[pc], wo[pc][oc]);
                SCHED();
                if constexpr (i == 0) WT(2);
                if constexpr (i >= 5 && i < 13 && i != 9) next(IC<i - 5 - (i > 9)>{});
                if constexpr (i == 20) WT(4);
                constexpr int e = i - (I0 + NL + 1), o = e < 0 ? 0 : e / 3;
                if constexpr (e >= 0 && e % 3 == 0) { CADD(f0[o], acc[o][0], acc[o][2]); if constexpr (o > 0) WAD(o - 1); }
                if constexpr (e >= 0 && e % 3 == 1) { if constexpr (o > 0) { WWR(o - 1); SCHED(); } CADD(f1[o], acc[o][1], acc[o][3]); }
                if constexpr (e >= 0 && e % 3 == 2) {
                    if constexpr (o == 0) asm volatile("v_cmp_u_f32 %0, %1, %2" : "=s"(bad) : "v"(f0[0]), "v"(f1[0]));
                    else LGK(15);
                    WSW(o);
                }
                SCHED();
            };
            asm volatile(".rept %0\n\ts_nop 0\n\t.endr" :: "i"(AL_W));
            static_for<0, 2>(slot);
            if (__builtin_expect(__builtin_amdgcn_ballot_w64(d[0].x == erl) != 0ull, 0)) {
                asm volatile("s_waitcnt vmcnt(3)" :: "v"(d[1]), "v"(d[2]), "v"(d[3]), "v"(d[4]), "v"(d[5]), "v"(d[6]), "v"(d[7]) : "memory");
                SCHED();
                *(LAS u32*)(sw + L * 4096) = 1u;
                static_for<0, 7>(next);
            } else {
            static_for<2, 40>(slot);
            WT(5);
            static_assert(WO_NL == 3, "tail");
            if (__builtin_expect(bad != 0ull, 0)) {
                bool miss = false;
#pragma unroll
                for (int i = 0; i < 8; ++i) miss |= d[i].x == PSENT;
                if (all_(!miss)) {
#pragma unroll
                    for (int i = 0; i < 8; ++i) miss |= d[i].y == PSENT || d[i].z == PSENT || d[i].w == PSENT;
                }
                *(LAS u32*)(sw + L * 4096) = all_(!miss) ? 0u : 1u;
            }
            asm volatile("v_permlane32_swap_b32 %0, %1\n\ts_nop 1\n\tv_add_f32_dpp %0, %1, %0 row_ror:8 row_mask:0xf bank_mask:0xf\n\tv_add_f32 %2, %4, %6\n\tv_add_f32 %3, %5, %7"
                         : "+v"(f0[3]), "+v"(f1[3]), "=&v"(f0[4]), "=&v"(f1[4]) : "v"(acc[4][0]), "v"(acc[4][1]), "v"(acc[4][2]), "v"(acc[4][3]));
            WWR(3); SCHED();
            asm volatile("s_nop 0\n\tv_permlane32_swap_b32 %0, %1\n\ts_nop 1\n\tv_add_f32_dpp %0, %1, %0 row_ror:8 row_mask:0xf bank_mask:0xf" : "+v"(f0[4]), "+v"(f1[4]));
            WWR(4);
            }
            WT(6);
            f32x2 a, b, c, e; u32 mq; v4u sv;
            asm volatile("s_waitcnt lgkmcnt(0)\n\ts_barrier\n\tds_read_b128 %4, %6 offset:%7+2560\n\t"
                         "ds_read_b64 %0, %5 offset:%7\n\tds_read_b64 %1, %5 offset:%7+640\n\tds_read_b64 %2, %5 offset:%7+1280\n\tds_read_b64 %3, %5 offset:%7+1920"
                         : "=&v"(a), "=&v"(b), "=&v"(c), "=&v"(e), "=&v"(sv) : "v"(xr), "v"(zero), "i"(L * 4096) : "memory");
            asm volatile("s_waitcnt lgkmcnt(4)\n\tv_or_b32 %4, %6, %7\n\tv_or3_b32 %4, %4, %8, %9\n\tv_lshl_or_b32 %5, %4, 31, %10\n\t"
                         "s_waitcnt lgkmcnt(2)\n\tv_pk_add_f32 %0, %0, %1\n\ts_waitcnt lgkmcnt(0)\n\tv_pk_add_f32 %2, %2, %3\n\ts_nop 0\n\tv_pk_add_f32 %0, %0, %2"
                         : "+v"(a), "+v"(b), "+v"(c), "+v"(e), "=&v"(pst), "=&v"(mq) : "v"(sv.x), "v"(sv.y), "v"(sv.z), "v"(sv.w), "v"(mp) : "memory");
            st4s(rs, mq, 0, pkv(a));
            SCHED();
            asm volatile("s_memrealtime %0" : "=s"(clk));
            VMW(3); SCHED();
            static_for<7, 13>(next);
            WT(7);
        }
    };
    do {
        turn(IC<0>{});
        turn(IC<1>{});
    } while (uu(fin) == 0u);
    if (sn[0] + 1 < NSEG || sn[1] + 1 < NSEG) st4s(rs, tid == 0 ? foff(F_ERR) : OOB, 0, 1u);
    if (w == 0) LST(1900 + j, rt32());
}

__global__ void __launch_bounds__(256, 1) k_main(Params p) {
    lptr lds = (lptr)(u32)16 - 16;
    if (blockIdx.x == 0) LST(256, rt32());
    unsigned long long pc; int len;
    asm volatile("s_getpc_b64 %0\n.Lk_main_here:\n\ts_mov_b32 %1, .Lk_end_here-.Lk_main_here" : "=s"(pc), "=s"(len));
    const rsrc_t rs = mk_rsrc(p.ws, W_END);
    const v2u e0 = ld8s(rs, threadIdx.x == 0 ? foff(F_EPOCH) : OOB, 0);
    const v4u code = __builtin_amdgcn_raw_buffer_load_b128(mk_rsrc((const void*)(pc & ~15ull), (u32)max(len, 0)), (((blockIdx.x >> 3) & 31u) * 256u + threadIdx.x) * 16u, 0, 0);
    CFENCE();
    auto conv_id = [](int role) {
        const int x = role >> 5, i = role & 31, no = x < 5 ? 5 : 10, og = x < 5 ? x - 3 : 2 * (x - 4) + (i >= 5), r = (x - 3) * 5 + i - no, q = r / 3;
        return x < 3 || i >= no + 5 || r >= 24 ? NCV : i < no ? ((og & 1) ? (og >> 1) * 5 + i % 5 : NCV) : r % 3 == 0 ? 20 + (q & 1) * 20 + (q >> 1) : 16 + (r % 3) * 8 + q;
    };
    int role = (int)(((u32)__builtin_amdgcn_s_getreg(20 | (3 << 11)) & 7u) * 32u + (blockIdx.x >> 3));
    Early e;
    v4u rv;
#pragma unroll 1
    for (int n = 0;; ++n) {
        conv_early(p, rs, conv_id(role), e);
        if (n) break;
        CFENCE();
        if (threadIdx.x == 0) {
            GAS unsigned long long* slots = (GAS unsigned long long*)(p.ws + W_CTRL + F_SLOT * 128);
            const unsigned long long tag = (unsigned long long)e0.y << 32 | (e0.x + 1u);
            u32 mine = (u32)role;
#pragma unroll 1
            for (int k = 0; k < 256 && __hip_atomic_exchange(slots + mine * 16, tag, __ATOMIC_RELAXED, __HIP_MEMORY_SCOPE_AGENT) == tag; ++k) mine = (mine + 1u) & 255u;
            lds_w16(lds, (v4u){mine, e0.x, e0.y, 0u});
        }
        BAR();
        rv = lds_r16(lds);
        BAR();
        if (uni((int)rv.x) == role) break;
        role = uni((int)rv.x);
    }
    const int x = role >> 5, i = role & 31, w = uni(threadIdx.x >> 6), no = x < 5 ? 5 : 10, nr = x < 7 ? 5 : 4, j = conv_id(role);
    const v2u ep = {uu(rv.y), uu(rv.z)};
    asm volatile("" :: "v"(code));
    if (x < 2) { role_head(p, lds, i >> 4, x * 16 + (i & 15), ep); __builtin_amdgcn_endpgm(); }
    if (x == 2) { role_wo(p, lds, i, ep); __builtin_amdgcn_endpgm(); }
    if (i >= no + nr) { role_qkv(p, lds, (x < 5 ? (x - 3) * 22 : 44 + (x - 5) * 17) + i - no - nr, ep); __builtin_amdgcn_endpgm(); }
    const bool cv = j >= NPRE || (j >= 20 && j < 24);
    if (j < NCV) conv_stage(p, rs, lds, j, (j & 3) * 2 + (j >= NPRE), cv ? 40 : 0, ep, e);
    if (i >= no) {
        if (!cv) role_row(p, lds, (j - 24) * 4 + w, ep);
        __builtin_amdgcn_endpgm();
    }
    const int og = x < 5 ? x - 3 : 2 * (x - 4) + (i >= 5);
    role_out(p, lds, og & 1, og >> 1, i % 5, ep);
}
__global__ void k_end() { asm volatile(".Lk_end_here:"); }

extern "C" void kernel_launch(void* const* d_in, const int* in_sizes, int n_in, void* d_out, int out_size, void* d_ws, size_t ws_size,
                              hipStream_t stream) {
    Params p;
    p.x = (const u16*)d_in[0]; p.mem0 = (const u16*)d_in[1]; p.Wq = (const u16*)d_in[2]; p.Wk = (const u16*)d_in[3];
    p.Wv = (const u16*)d_in[4]; p.Wo = (const u16*)d_in[5];
    p.bk = (const u16*)d_in[8]; p.bv = (const u16*)d_in[9]; p.shift = (const u16*)d_in[10]; p.ssmk = (const u16*)d_in[11]; p.ssmD = (const u16*)d_in[12];
    p.out = (u16*)d_out;
    p.ws = (unsigned char*)(((size_t)d_ws + 2 * MB - 1) & ~(size_t)(2 * MB - 1));
    (void)hipFuncSetAttribute((const void*)k_main, hipFuncAttributeMaxDynamicSharedMemorySize, LDS_BYTES);
    hipLaunchKernelGGL(k_main, dim3(256), dim3(256), LDS_BYTES, stream, p);
}
```

```cpp
#include <hip/hip_runtime.h>
#include <hip/hip_bf16.h>

typedef unsigned short u16;
typedef unsigned int u32;
typedef u32 v2u __attribute__((ext_vector_type(2)));
typedef u32 v4u __attribute__((ext_vector_type(4)));
typedef u32 v8u __attribute__((ext_vector_type(8)));
typedef float f32x2 __attribute__((ext_vector_type(2)));
typedef float f32x4 __attribute__((ext_vector_type(4)));
typedef float f32x16 __attribute__((ext_vector_type(16)));
typedef __bf16 bf16x2 __attribute__((ext_vector_type(2)));
typedef __bf16 bf16x8 __attribute__((ext_vector_type(8)));
typedef short s16x4 __attribute__((ext_vector_type(4)));
#define LAS __attribute__((address_space(3)))
#define GAS __attribute__((address_space(1)))
typedef LAS unsigned char* lptr;
#define DEV __device__ __forceinline__

constexpr int SEQ = 2048, DM = 640, NH = 32, DH = 128, NSEG = 64, NQ = NH * DH;

constexpr u32 MB = 1u << 20;
constexpr u32 W_ORING = 0;
constexpr u32 W_MRING = MB;
constexpr u32 W_PROBE = 2 * MB - 65536;
constexpr u32 W_CTRL = 4 * MB;
constexpr u32 CTRL_BYTES = 128 * 1024;
constexpr u32 ACT = SEQ * DM * 2;
constexpr u32 W_H1 = 5 * MB, W_Y = W_H1 + ACT, W_H2 = W_Y + ACT, W_X2 = W_H2 + ACT;
constexpr u32 W_KQ = 16 * MB;
constexpr u32 W_VO = W_KQ + 64 * MB;
constexpr u32 W_P = W_VO + 64 * MB;
constexpr u32 P_ITEM = 32 * 4 * DM * 4;
constexpr u32 W_KVT = W_P + 2 * NSEG * P_ITEM;
constexpr u32 W_SP = W_KVT + ACT;
constexpr u32 W_END = W_KVT + 3 * MB;
static_assert((size_t)W_END + 2 * MB <= 256ull * MB, "workspace");
constexpr u32 F_ERR = 0, F_QKV = 16, F_O = F_QKV + 192, F_P = F_O + 64, F_H2 = F_P + 64, F_CLR = F_H2 + 32,
              F_EPOCH = F_CLR + 32, F_SLOT = F_EPOCH + 1, F_PRE = F_SLOT + 256, F_N = F_PRE + 192;
constexpr int NCV = 44, NPRE = 40;
static_assert(F_N * 128 <= CTRL_BYTES, "ctrl");
DEV u32 foff(u32 idx) { return W_CTRL + idx * 128u; }

constexpr u32 OOB = 0x80000000u;
constexpr u32 PSENT = 0x7F817F81u;
constexpr int SPIN_CAP = 40000;
constexpr int LDS_BYTES = 160 * 1024;

#define LS3(cond, slot, idx) do {} while (0)
#define LST(idx, v) do {} while (0)
#define LS2(cond, idx) do {} while (0)
struct Params {
    const u16 *x, *mem0, *Wq, *Wk, *Wv, *Wo, *bk, *bv, *shift, *ssmk, *ssmD;
    u16* out;
    unsigned char* ws;
};

typedef __amdgpu_buffer_rsrc_t rsrc_t;
DEV rsrc_t mk_rsrc(const void* p, u32 bytes) { return __builtin_amdgcn_make_buffer_rsrc((void*)p, 0, bytes, 0x00020000); }
DEV v4u ld16s(rsrc_t r, u32 off, u32 so) { return __builtin_amdgcn_raw_buffer_load_b128(r, off, so, 16); }
DEV v2u ld8s(rsrc_t r, u32 off, u32 so) { return __builtin_amdgcn_raw_buffer_load_b64(r, off, so, 16); }
DEV v4u ldin16(rsrc_t r, u32 off, u32 so) { return __builtin_amdgcn_raw_buffer_load_b128(r, off, so, 2); }
DEV v2u ldin8(rsrc_t r, u32 off, u32 so) { return __builtin_amdgcn_raw_buffer_load_b64(r, off, so, 2); }
DEV u32 ld4(rsrc_t r, u32 off) { return __builtin_amdgcn_raw_buffer_load_b32(r, off, 0, 16); }
DEV void st16(rsrc_t r, u32 off, v4u v) { __builtin_amdgcn_raw_buffer_store_b128(v, r, off, 0, 16); }
DEV void st8s(rsrc_t r, u32 off, u32 so, v2u v) { __builtin_amdgcn_raw_buffer_store_b64(v, r, off, so, 16); }
DEV void st4s(rsrc_t r, u32 off, u32 so, u32 v) { __builtin_amdgcn_raw_buffer_store_b32(v, r, off, so, 16); }
#define DRAIN() asm volatile("s_waitcnt vmcnt(0)" ::: "memory")
#define VMW(n) asm volatile("s_waitcnt vmcnt(%0)" :: "i"(n) : "memory")
#define BAR() asm volatile("s_waitcnt lgkmcnt(0)\n\ts_barrier" ::: "memory")
#define SCHED() __builtin_amdgcn_sched_barrier(0)
#define CFENCE() asm volatile("" ::: "memory")
#define NOPS(n) asm volatile("s_nop %0" :: "i"(n))
#define PIN(x) asm volatile("" : "+v"(x))
#define PINS(x) asm volatile("" : "+s"(x))

template <int V> struct IC { static constexpr int value = V; };
template <int I, int N, class F> DEV void static_for(F&& f) { if constexpr (I < N) { f(IC<I>{}); static_for<I + 1, N>(f); } }
#define SFOR(var, n) static_for<0, n>([&](auto ic_) __attribute__((always_inline)) { constexpr int var = decltype(ic_)::value;
#define SEND });

DEV u32 pk(float a, float b) { bf16x2 v = {(__bf16)a, (__bf16)b}; return __builtin_bit_cast(u32, v); }
DEV float lo_f(u32 u) { return __builtin_bit_cast(float, u << 16); }
DEV float hi_f(u32 u) { return __builtin_bit_cast(float, u & 0xffff0000u); }
DEV f32x2 up2(u32 u) { return (f32x2){lo_f(u), hi_f(u)}; }
DEV u32 pk2(f32x2 v) { return pk(v.x, v.y); }
DEV float rbf(float a) { return (float)(__bf16)a; }
DEV v4u lds_r16(lptr p) { return *(LAS v4u*)p; }
DEV void lds_w16(lptr p, v4u v) { *(LAS v4u*)p = v; }
DEV v2u lds_tr(lptr p) { return __builtin_bit_cast(v2u, __builtin_amdgcn_ds_read_tr16_b64_v4i16((LAS s16x4*)p)); }
DEV v4u tr2(lptr p0, lptr p1) { const v2u a = lds_tr(p0), b = lds_tr(p1); return (v4u){a.x, a.y, b.x, b.y}; }
DEV v8u cat8(v4u a, v4u b) { return __builtin_shufflevector(a, b, 0, 1, 2, 3, 4, 5, 6, 7); }
DEV f32x4 mfma16(v4u a, v4u b, f32x4 c) { return __builtin_amdgcn_mfma_f32_16x16x32_bf16(__builtin_bit_cast(bf16x8, a), __builtin_bit_cast(bf16x8, b), c, 0, 0, 0); }
DEV f32x16 mfma32(v4u a, v4u b, f32x16 c) { return __builtin_amdgcn_mfma_f32_32x32x16_bf16(__builtin_bit_cast(bf16x8, a), __builtin_bit_cast(bf16x8, b), c, 0, 0, 0); }

DEV float f_(u32 u) { return __builtin_bit_cast(float, u); }
DEV u32 u_(float f) { return __builtin_bit_cast(u32, f); }
template <int CTRL> DEV float dpp(float v) { return f_((u32)__builtin_amdgcn_update_dpp(0, (int)u_(v), CTRL, 0xf, 0xf, false)); }
DEV float max16(float v) { auto r = __builtin_amdgcn_permlane16_swap(u_(v), u_(v), false, false); return fmaxf(f_(r[0]), f_(r[1])); }
DEV float max32(float v) { auto r = __builtin_amdgcn_permlane32_swap(u_(v), u_(v), false, false); return fmaxf(f_(r[0]), f_(r[1])); }
DEV float add16(float v) { auto r = __builtin_amdgcn_permlane16_swap(u_(v), u_(v), false, false); return f_(r[0]) + f_(r[1]); }
DEV float add32(float v) { auto r = __builtin_amdgcn_permlane32_swap(u_(v), u_(v), false, false); return f_(r[0]) + f_(r[1]); }
DEV float wave_sum(float v) {
    v += dpp<0xB1>(v);
    v += dpp<0x4E>(v);
    v += dpp<0x124>(v);
    v += dpp<0x128>(v);
    return add32(add16(v));
}
DEV bool all_(bool c) { return __builtin_amdgcn_ballot_w64(!c) == 0ull; }
DEV int uni(int v) { return __builtin_amdgcn_readfirstlane(v); }
DEV u32 uu(u32 v) { return (u32)__builtin_amdgcn_readfirstlane((int)v); }
DEV u32 rdl(u32 v, int l) { return (u32)__builtin_amdgcn_readlane((int)v, l); }
DEV u32 rt32() { return (u32)__builtin_amdgcn_s_memrealtime(); }

#define WSL 2
DEV void wait_flags(rsrc_t rs, u32 off, u32 want, bool& dead) {
    if (dead) return;
#pragma unroll 1
    for (int n = 0;; ++n) {
        CFENCE();
        const u32 v = ld4(rs, off);
        if (all_(off >= OOB || v >= want)) break;
        __builtin_amdgcn_s_sleep(WSL);
        if ((n & 63) == 63) { if (uni((int)ld4(rs, foff(F_ERR))) != 0) { dead = true; break; } }
        if (n > SPIN_CAP) { st4s(rs, foff(F_ERR), 0, 1u); dead = true; break; }
    }
    CFENCE();
}

DEV void pre_post(rsrc_t rs, int idx, u32 stage, v2u ep) { st8s(rs, threadIdx.x == 0 ? foff(F_PRE + idx) : OOB, 0, (v2u){ep.x + stage, ep.y}); }
DEV void pre_poll(rsrc_t rs, int first, int n, u32 stage, v2u ep) {
    const int lane = threadIdx.x & 63;
#pragma unroll 1
    for (int it = 0; it < SPIN_CAP; ++it) {
        CFENCE();
        const v2u a = ld8s(rs, foff(F_PRE + first + min(lane, n - 1)), 0), b = ld8s(rs, foff(F_PRE + first + min(lane + 64, n - 1)), 0);
        if (all_(a.y == ep.y && b.y == ep.y && max(a.x - ep.x - stage, b.x - ep.x - stage) < 8u - stage)) break;
        __builtin_amdgcn_s_sleep(2);
    }
    CFENCE();
}
DEV void pre_wait(rsrc_t rs, int first, int n, u32 stage, v2u ep) {
    if (uni(threadIdx.x >> 6) == 0) pre_poll(rs, first, n, stage, ep);
    BAR();
}
DEV u32 pol_wait(rsrc_t rs, v2u ep) {
    const int lane = threadIdx.x & 63;
    v4u a;
#pragma unroll 1
    for (int it = 0; it < SPIN_CAP; ++it) {
        CFENCE();
        a = ld16s(rs, foff(F_PRE + (lane < NPRE || lane >= 48 ? lane : 0)), 0);
        if (all_(a.y == ep.y && a.x - ep.x - 1u < 7u)) break;
        __builtin_amdgcn_s_sleep(2);
    }
    CFENCE();
    return uu(wave_sum(lane >= 48 ? (float)(int)a.z : 0.f) >= 0.f ? 1u : 0u);
}
#define PROBE_T 1000
#define NPROBE 32
DEV u32 pol_probe(const Params& p, rsrc_t rs, int id) {
    const int lane = threadIdx.x & 63;
    const u32 off0 = W_PROBE + (u32)(id >> 2) * 16384u + (u32)(id & 3) * 128u;
    const u32 tend = rt32() + PROBE_T;
    st4s(rs, lane < 32 ? off0 + (u32)lane * 512 : OOB, 0, 0u); DRAIN();
    int calm = 0;
    int d[2] = {1 << 16, 1 << 16};
#pragma unroll 1
    for (int q = 0; q < NPROBE && ((q & 1) || (calm < 6 && (int)(rt32() - tend) < 0)); ++q) {
        const u32 half = (u32)(q ^ (q >> 1) ^ id) & 1u;
        const unsigned long long t0 = __builtin_amdgcn_s_memtime();
        const u32 v = ld4(rs, lane == 0 ? off0 + half * 8192 + (u32)(q >> 1) * 512 : OOB);
        asm volatile("s_waitcnt vmcnt(0)" :: "v"(v) : "memory");
        const int dt = (int)(__builtin_amdgcn_s_memtime() - t0);
        if (half) d[1] = min(d[1], dt); else d[0] = min(d[0], dt);
        calm += dt < 1100;
    }
    const u32 xcc = (u32)__builtin_amdgcn_s_getreg(20 | (3 << 11)) & 0xfu;
    return (u32)min(max((d[0] - d[1]) * ((xcc & 4u) ? -1 : 1), -250), 250);
}
DEV f32x2 ln_stats(const f32x2 (&x)[5]) {
    const float c = f_(uu(u_(x[0].x)));
    f32x2 d[5];
#pragma unroll
    for (int i = 0; i < 5; ++i) d[i] = x[i] - c;
    const f32x2 s2 = (d[0] + d[1]) + (d[2] + d[3]) + d[4], q2 = (d[0] * d[0] + d[1] * d[1]) + (d[2] * d[2] + d[3] * d[3] + d[4] * d[4]);
    float a = s2.x + s2.y, b = q2.x + q2.y, a2, b2;
#define LN_DPP(m) "v_add_f32_dpp %0, %0, %0 " m " row_mask:0xf bank_mask:0xf\n\tv_add_f32_dpp %1, %1, %1 " m " row_mask:0xf bank_mask:0xf\n\ts_nop 0\n\t"
    asm("s_nop 1\n\t" LN_DPP("quad_perm:[1,0,3,2]") LN_DPP("quad_perm:[2,3,0,1]") LN_DPP("row_ror:4")
        "v_add_f32_dpp %0, %0, %0 row_ror:8 row_mask:0xf bank_mask:0xf\n\tv_add_f32_dpp %1, %1, %1 row_ror:8 row_mask:0xf bank_mask:0xf\n\ts_nop 1\n\t"
        "v_permlane16_swap_b32 %0, %1\n\tv_add_f32 %0, %0, %1\n\tv_mov_b32 %1, %0\n\ts_nop 1\n\tv_permlane32_swap_b32 %0, %1\n\tv_add_f32 %0, %0, %1\n\ts_nop 0\n\t"
        "v_readlane_b32 %2, %0, 0\n\tv_readlane_b32 %3, %0, 16\n\ts_nop 1"
        : "+v"(a), "+v"(b), "=s"(a2), "=s"(b2));
    const float m = a2 * (1.0f / DM), var = fmaxf(b2 * (1.0f / DM) - m * m, 0.f);
    const float sd = rbf(__builtin_amdgcn_sqrtf(rbf(rbf(var) + 1e-5f)));
    return (f32x2){rbf(c + m), __builtin_amdgcn_rcpf(sd)};
}
DEV u32 pkv(f32x2 v) { u32 d; asm("v_cvt_pk_bf16_f32 %0, %1, %2" : "=v"(d) : "v"(v.x), "v"(v.y)); return d; }
DEV void store_h(rsrc_t rs, u32 base, int row, const u32 (&hp)[5]) {
    const u32 lane = threadIdx.x & 63;
    const u32 sw = (u32)(row & 15) * 16u, o8 = (lane >> 5) * 8192u + (((lane & 31u) * 8u) ^ sw), hb = base + (u32)(row >> 5) * (32 * DM * 2) + (u32)(row & 31) * 256u;
    st8s(rs, o8, hb, (v2u){hp[0], hp[1]});
    st8s(rs, o8, hb + 16384, (v2u){hp[2], hp[3]});
    st4s(rs, (lane * 4) ^ sw, hb + 32768, hp[4]);
}

#define DMA16(ldsb, vo, so) asm volatile("s_mov_b32 m0, %0\n\ts_nop 0\n\tbuffer_load_dwordx4 %1, %2, %3 offen sc1 lds" :: "s"(ldsb), "v"(vo), "s"(srd), "s"(so) : "memory")
#define DMA16I(ldsb, lo, vo, so, go) do { u32 so_; asm volatile("s_add_u32 m0, %1, %5\n\ts_add_u32 %0, %4, %6\n\tbuffer_load_dwordx4 %2, %3, %0 offen sc1 lds" \
                         : "=&s"(so_) : "s"(ldsb), "v"(vo), "s"(srd), "s"(so), "i"(lo), "i"(go) : "memory", "scc"); } while (0)
#define DMA32(ldsb, vo, so) asm volatile("s_mov_b32 m0, %0\n\ts_nop 0\n\tbuffer_load_dwordx4 %1, %2, %3 offen sc1 lds\n\tbuffer_load_dwordx4 %1, %2, %3 offen offset:1024 sc1 lds" \
                         :: "s"(ldsb), "v"(vo), "s"(srd), "s"(so) : "memory")
#define DMAIN(sr, ldsb, vo, so) asm volatile("s_mov_b32 m0, %0\n\ts_nop 0\n\tbuffer_load_dwordx4 %1, %2, %3 offen lds" :: "s"(ldsb), "v"(vo), "s"(sr), "s"(so) : "memory")
DEV v4u mk_srd(const void* q, u32 bytes) { return (v4u){uu((u32)(size_t)q), uu((u32)((size_t)q >> 32) & 0xffffu), bytes, 0x00020000u}; }
#define DMANT(sr, ldsb, vo, so) asm volatile("s_mov_b32 m0, %0\n\ts_nop 0\n\tbuffer_load_dwordx4 %1, %2, %3 offen nt lds" :: "s"(ldsb), "v"(vo), "s"(sr), "s"(so) : "memory")
#define AHEAD(sr, vo, so) asm volatile("buffer_load_dwordx4 %0, %1, %2, %3 offen" : "+v"(junk) : "v"(vo), "s"(sr), "s"(so) : "memory")
#define MK_SRD() const v4u srd = mk_srd(p.ws, W_END)

constexpr int CV_W = 40960;
template <bool B> struct CvL {
    static constexpr int NF = B ? 5 : 3, NK = B ? 4 : 2, KP = 544 + NK * 256, CH = NF * 1024 + 4 * KP;
};
constexpr int CV_Y = 4 * CvL<false>::CH, CV_YP = 272;
static_assert(CV_Y + 32 * CV_YP <= CV_W && 2 * CvL<true>::CH <= CV_Y, "conv lds");
DEV int cv_chan(int u, int i) { return 16 * (u % 40) + 4 * i + u / 40; }
template <bool B> DEV void cv_filt(const Params& p, u32 dst, int d, int i) {
    const u32 lane = threadIdx.x & 63;
    const v4u sf = mk_srd(p.ssmk + d * SEQ, SEQ * 2);
    if (i == 0) DMANT(sf, dst, lane < 4u ? OOB : lane * 16u - 64u, 0);
    else DMANT(sf, uu(dst + i * 1024), i == CvL<B>::NF - 1 && lane >= 4u ? OOB : lane * 16u, i * 1024 - 64);
}
DEV v4u cv_kvld(rsrc_t rs, int d, int i) { return ld16s(rs, (threadIdx.x & 63) * 16u, uu(W_KVT + (u32)d * (SEQ * 2) + i * 1024)); }
template <bool B> DEV void cv_kvput(lptr F, const v4u* k) {
    const int lane = threadIdx.x & 63;
#pragma unroll
    for (int i = 0; i < CvL<B>::NK; ++i) lds_w16(F + CvL<B>::NF * 1024 + (lane & 3) * CvL<B>::KP + 512 + i * 256 + (lane >> 2) * 16, k[i]);
}
template <bool B> DEV void cv_zero(lptr F) {
    const int lane = threadIdx.x & 63;
#pragma unroll
    for (int i = 0; i < 4; ++i) lds_w16(F + (lane >> 5) * CvL<B>::CH + CvL<B>::NF * 1024 + i * CvL<B>::KP + (lane & 31) * 16, (v4u){0u, 0u, 0u, 0u});
}
#define M32V(d, a, b) asm volatile("v_mfma_f32_32x32x16_bf16 %0, %1, %2, %0" : "+v"(d) : "v"(a), "v"(b))
template <bool B, class G> DEV void cv_run(const Params& p, rsrc_t rs, lptr F, int d, u32 D, f32x4 sp, float (&y)[16], G&& gap) {
    constexpr int KP = CvL<B>::KP, FO = B ? 2048 : 0;
    const int lane = threadIdx.x & 63, n = lane & 31, hh = lane >> 5, c = lane & 3;
    lptr fa = F + 64 + (32 * hh + 16 * (c & 1) + 8 * ((lane >> 4) & 1) + 4 * (c >> 1) - (lane & 12)) * 2;
    lptr kl = F + CvL<B>::NF * 1024 + (32 + n - hh) * 16;
    v4u dg;
#pragma unroll
    for (int q = 0; q < 4; ++q) { const int k = (lane & 35) ? -1 : (n & 4) + (n >> 3); dg[q] = k == 2 * q ? D : k == 2 * q + 1 ? D << 16 : 0u; }
    v4u l[4], h[4]; v2u fc[4], fh[4];
    f32x16 acc[4];
#pragma unroll
    for (int j = 0; j < 4; ++j) { l[j] = lds_r16(kl + j * KP); if constexpr (B) h[j] = lds_r16(kl + j * KP + 512); }
#pragma unroll
    for (int j = 0; j < 2; ++j) { fc[j] = lds_tr(fa + FO - 32 * j); if constexpr (B) fh[j] = lds_tr(fa - 32 * j); }
    asm volatile("s_nop 1" : "+v"(dg));
#pragma unroll
    for (int j = 0; j < 4; ++j) asm volatile("v_mfma_f32_32x32x16_bf16 %0, %1, %2, 0" : "=&v"(acc[j]) : "v"(dg), "v"(B ? h[j] : l[j]));
    constexpr int U = B ? 2 : 4;
    kl -= 32 * U; fa -= 32;
    auto body = [&](bool first) __attribute__((always_inline)) {
#pragma unroll
        for (int z = 0; z < U; ++z) {
            const int c = 2 * (z & 1);
            const v4u f = __builtin_shufflevector(fc[c], fc[c + 1], 0, 1, 2, 3), g = __builtin_shufflevector(fh[c], fh[c + 1], 0, 1, 2, 3);
#pragma unroll
            for (int j = 0; j < 4; ++j) {
                M32V(acc[j], f, l[j]); l[j] = lds_r16(kl + j * KP + 32 * (U - z - 1));
                if (j < 2) fc[(c ^ 2) + j] = lds_tr(fa + FO + (z + 1) * 128 + 32 - 32 * j);
                if (first && j > 0) gap(5 * z + j - 1);
                SCHED();
            }
            if constexpr (B) {
#pragma unroll
                for (int j = 0; j < 4; ++j) {
                    M32V(acc[j], g, h[j]); h[j] = lds_r16(kl + j * KP + 512 + 32 * (U - z - 1));
                    if (j < 2) fh[(c ^ 2) + j] = lds_tr(fa + (z + 1) * 128 + 32 - 32 * j);
                    if (first && j > 1) gap(5 * z + j + 1);
                    SCHED();
                }
            }
        }
        kl -= 32 * U; fa += 128 * U;
    };
    if constexpr (B) body(true);
#pragma unroll 1
    for (int m = B ? 2 * U : 0; m < 32; m += 2 * U) body(false);
    asm volatile("s_nop 11" : "+v"(acc[0]), "+v"(acc[1]), "+v"(acc[2]), "+v"(acc[3]));
    const float o[3] = {acc[1][15] + acc[2][14] + acc[3][13], acc[2][15] + acc[3][14], acc[3][15]};
    if constexpr (!B) st16(rs, lane == 63 ? W_SP + (u32)d * 16u : OOB, (v4u){u_(o[0]), u_(o[1]), u_(o[2]), 0u});
#pragma unroll
    for (int i = 0; i < 16; ++i) {
        float v = acc[0][i];
#pragma unroll
        for (int j = 1; j < 4; ++j) if (j <= i) v += acc[j][i - j];
        if (i < 3) { const float e = f_((u32)__builtin_amdgcn_ds_bpermute((hh ? lane - 32 : lane + 31) * 4, (int)u_(o[i]))); v += lane == 0 ? sp[i] : e; }
        y[i] = v;
    }
}
DEV void cv_put(lptr F, int pr, const float (&y0)[16], const float (&y1)[16]) {
    const int lane = threadIdx.x & 63;
#pragma unroll
    for (int i = 0; i < 16; ++i) *(LAS u32*)(F + CV_Y + (lane & 31) * CV_YP + (lane >> 5) * 128 + i * 8 + pr * 4) = pk(y0[i], y1[i]);
}
DEV void cv_out(rsrc_t rs, lptr F, int u, int blk0) {
    const int lane = threadIdx.x & 63;
    lptr q = F + CV_Y + ((lane >> 2) & 3) * CV_YP + ((lane >> 4) * 4 + (lane & 3)) * 8;
    const u32 yo = W_Y + (u32)(((lane >> 2) * 8 + blk0) * DM + 4 * u + (lane & 3)) * 16u;
#pragma unroll
    for (int i = 0; i < 8; ++i) { lptr a = q + (i >> 1) * (8 * CV_YP) + (i & 1) * 128; st16(rs, yo + (u32)((i & 1) * 128 + (i >> 1)) * (DM * 16), tr2(a, a + 4 * CV_YP)); }
}
struct KvIn { v2u k[19], v[16]; v4u s[2]; };
template <class F> DEV void kvt_load(const Params& p, int tile, KvIn& in, F&& mid) {
    const int lane = threadIdx.x & 63;
    const rsrc_t rk = mk_rsrc(p.bk, ACT), rv = mk_rsrc(p.bv, ACT), rh = mk_rsrc(p.shift, DM * 8);
    const int row = (tile / 10) * 64 + (lane >> 4) * 16, col = (tile % 10) * 64 + (lane & 15) * 4;
    const u32 vo = (u32)(row * DM + col) * 2u, vp = row > 0 ? vo - 3 * (DM * 2) : OOB;
#pragma unroll
    for (int i = 0; i < 2; ++i) in.s[i] = ldin16(rh, (u32)col * 8u + i * 16, 0);
#pragma unroll
    for (int i = 0; i < 19; ++i) { if (i == 7) mid(); in.k[i] = i < 3 ? ldin8(rk, vp, i * (DM * 2)) : ldin8(rk, vo, (i - 3) * (DM * 2)); if (i >= 3) in.v[i - 3] = ldin8(rv, vo, (i - 3) * (DM * 2)); }
}
DEV void kvt_make(rsrc_t rs, lptr xt, int tile, const KvIn& in) {
    const int lane = threadIdx.x & 63;
#pragma unroll
    for (int c = 0; c < 2; ++c) {
        const v4u sv = in.s[c];
        const f32x2 sh[4] = {{lo_f(sv.x), lo_f(sv.z)}, {hi_f(sv.x), hi_f(sv.z)}, {lo_f(sv.y), lo_f(sv.w)}, {hi_f(sv.y), hi_f(sv.w)}};
        f32x2 kf[19];
#pragma unroll
        for (int i = 0; i < 19; ++i) kf[i] = up2(in.k[i][c]);
        u32 kv[16];
#pragma unroll
        for (int i = 0; i < 16; ++i) kv[i] = pk2(((kf[i] * sh[3] + kf[i + 1] * sh[2]) + (kf[i + 2] * sh[1] + kf[i + 3] * sh[0])) * up2(in.v[i][c]));
#pragma unroll
        for (int q = 0; q < 8; ++q) {
            const u32 sel = (q & 1) ? 0x07060302u : 0x05040100u; const u32* z = kv + (q >> 1);
            *(LAS v2u*)(xt + ((2 * c + (q & 1)) * 16 + (lane & 15)) * 144 + (lane >> 5) * 64 + (q >> 1) * 16 + ((lane >> 4) & 1) * 8) =
                (v2u){__builtin_amdgcn_perm(z[4], z[0], sel), __builtin_amdgcn_perm(z[12], z[8], sel)};
        }
    }
    const u32 o = W_KVT + (u32)(((tile % 10) * 64 + 4 * (lane >> 3)) * SEQ + (tile / 10) * 64 + (lane & 7) * 8) * 2u;
#pragma unroll
    for (int i = 0; i < 8; ++i) st16(rs, o + (u32)(32 * (i & 1) + (i >> 1)) * (SEQ * 2), lds_r16(xt + (8 * i + (lane >> 3)) * 144 + (lane & 7) * 16));
}
struct RowX { v2u a, b; u32 c; };
DEV void ln_row(rsrc_t rs, int row, const RowX& r) {
    const f32x2 x[5] = {up2(r.a.x), up2(r.a.y), up2(r.b.x), up2(r.b.y), up2(r.c)};
    const f32x2 st = ln_stats(x);
    u32 hp[5];
#pragma unroll
    for (int q = 0; q < 5; ++q) hp[q] = pk2(up2(pkv(x[q] - st.x)) * st.y);
    store_h(rs, W_H1, row, hp);
}
DEV v4u mem0_load(const Params& p, int j) {
    const u32 gt = (u32)(j * 256 + threadIdx.x) * 16u, n = ((gt >> 14) << 9) + ((gt & 8191u) >> 4);
    return ldin16(mk_rsrc(p.mem0, 8 * DM * 2), n < 640u ? ((2 * ((n >> 2) & 3) + (n & 1)) * DM + (n >> 4) * 16 + (n & 2) * 4) * 2u : OOB, 0);
}
DEV void pre_rings(rsrc_t rs, int j, v4u m) {
    const u32 gi = (u32)(j * 256 + threadIdx.x), gt = gi * 16u, R = NPRE * 4096, n = ((gt >> 14) << 9) + ((gt & 8191u) >> 4);
#pragma unroll
    for (int i = 0; i < 4; ++i) m[i] = n >= 640u ? PSENT : m[i] == PSENT ? 0x7FC17FC1u : m[i];
    st4s(rs, gi - 36u * 256u < F_CLR ? foff(gi - 36u * 256u) : OOB, 0, 0u);
#pragma unroll 1
    for (u32 o = gt; o < MB; o += R) st16(rs, W_ORING + o, (v4u){PSENT, PSENT, PSENT, PSENT});
    const u32 mo = gt < 4 * 32768 + 16384 ? W_MRING + gt : OOB;
    st16(rs, mo, m);
    st16(rs, mo + MB / 4, m);
    static_assert(NPRE * 4096 >= 4 * 32768 + 16384, "memory ring");
}
constexpr int KT0 = NPRE * 4, LN0 = NPRE * 8;
struct Early { RowX r[2]; v4u m; u32 vote; };
DEV void conv_early(const Params& p, rsrc_t rs, int j, Early& e) {
    const int lane = threadIdx.x & 63, w = uni(threadIdx.x >> 6), u = j * 4 + w;
    if (j < NPRE) {
        const rsrc_t rx = mk_rsrc(p.x, ACT);
#pragma unroll
        for (int i = 0; i < 2; ++i) {
            const u32 so = uu((u + i * KT0) * (DM * 2));
            e.r[i].a = __builtin_amdgcn_raw_buffer_load_b64(rx, lane * 8u, so, 0); e.r[i].b = __builtin_amdgcn_raw_buffer_load_b64(rx, lane * 8u + 512u, so, 0);
            e.r[i].c = __builtin_amdgcn_raw_buffer_load_b32(rx, lane * 4u + 1024u, so, 0);
        }
        e.m = mem0_load(p, j);
    } else if (j < NCV && w > 0) e.vote = pol_probe(p, rs, u - NPRE * 4);
}
DEV void conv_stage(const Params& p, rsrc_t rs, lptr lds, int j, int g0, int ge, v2u ep, Early& e) {
    const int tid = threadIdx.x, lane = tid & 63, w = uni(tid >> 6), u = j * 4 + w;
    lptr F = lds + w * CV_W;
    const u32 fb = uu(w * CV_W);
    const rsrc_t rd = mk_rsrc(p.ssmD, DM * 2);
    float y0[16], y1[16];
    KvIn in;
    if (j < NPRE) {
        constexpr int CH = CvL<false>::CH;
        if (w == 1) LST(1500 + j, rt32());
        pre_rings(rs, j, e.m);
        ln_row(rs, u, e.r[0]); ln_row(rs, u + KT0, e.r[1]);
        if (w == 1) LST(1550 + j, rt32());
        CFENCE();
        kvt_load(p, u, in, [&] { VMW(13); BAR(); pre_post(rs, j, 1u, ep); if (w == 0) LST(1450 + j, rt32()); });
        if (j == 20) LST(260, rt32());
        const u32 D = __builtin_amdgcn_raw_buffer_load_b16(rd, (u32)cv_chan(u, lane & 3) * 2u, 0, 0);
#pragma unroll 1
        for (int i = 0; i < 4; ++i) { const int d = cv_chan(u, i); cv_filt<false>(p, fb + i * CH, d, 0); cv_filt<false>(p, fb + i * CH, d, 1); cv_filt<false>(p, fb + i * CH, d, 2); }
        kvt_make(rs, F + CV_Y, u, in);
        cv_zero<false>(F); cv_zero<false>(F + 2 * CH);
        DRAIN(); BAR();
        pre_post(rs, j, 2u, ep);
        if (j == 20) LST(262, rt32());
        pre_wait(rs, 0, NPRE, 2u, ep);
        if (j == 20) LST(281, rt32());
        {
            v4u k[8];
#pragma unroll
            for (int i = 0; i < 8; ++i) k[i] = cv_kvld(rs, cv_chan(u, i >> 1), i & 1);
#pragma unroll
            for (int i = 0; i < 4; ++i) cv_kvput<false>(F + i * CH, k + 2 * i);
        }
        if (j == 20) LST(286, rt32());
#pragma unroll 1
        for (int i = 0; i < 4; i += 2) {
            cv_run<false>(p, rs, F + i * CH, cv_chan(u, i), rdl(D, i), f32x4{}, y0, [](int) {});
            cv_run<false>(p, rs, F + (i + 1) * CH, cv_chan(u, i + 1), rdl(D, i + 1), f32x4{}, y1, [](int) {});
            cv_put(F, i >> 1, y0, y1);
        }
        if (j == 20) LST(287, rt32());
        cv_out(rs, F, u, 0);
        if (j == 20) LST(263, rt32());
    } else {
        st16(rs, lane == 0 ? foff(F_PRE + 48 + u - NPRE * 4) : OOB, (v4u){ep.x + 1u, ep.y, w > 0 ? e.vote : 0u, 0u});
#pragma unroll 1
        for (int t = u + 128; t < 320; t += 16) { kvt_load(p, t, in, [] {}); kvt_make(rs, F, t, in); }
    }
    DRAIN(); BAR();
    if (g0 < ge) {
        constexpr int CH = CvL<true>::CH;
        pre_post(rs, j, 3u, ep);
        cv_zero<true>(F);
        if (w == 0) { pre_poll(rs, 64, 32, 3u, ep); pre_poll(rs, 0, NCV, 3u, ep); }
        BAR();
        v4u k[4];
        auto fetch = [&](u32 dst, int d, int i) { if (i < 5) cv_filt<true>(p, dst, d, i); else if (i < 9) k[i - 5] = cv_kvld(rs, d, i - 5); };
#pragma unroll
        for (int i = 0; i < 9; ++i) fetch(fb, cv_chan(g0 * 4 + w, 0), i);
        const u32 ch = (u32)cv_chan(min((g0 + 8 * (lane >> 2)) * 4 + w, 159), lane & 3);
        u32 D = __builtin_amdgcn_raw_buffer_load_b16(rd, ch * 2u, 0, 0);
        v4u sp = ld16s(rs, W_SP + ch * 16u, 0);
        asm volatile("" : "+v"(D), "+v"(sp));
#pragma unroll 1
        for (int g = g0, n = 0; g < ge; g += 8) {
            const int un = g * 4 + w;
            if (g == 16) { LST(282, rt32()); LST(288, __builtin_amdgcn_s_memtime()); }
#pragma unroll 1
            for (int i = 0; i < 4; i += 2, n += 2) {
                const int d1 = (int)rdl(ch, n + 1), d2 = (int)rdl(ch, n + 2);
                cv_kvput<true>(F, k);
                cv_run<true>(p, rs, F, 0, rdl(D, n), (f32x4){f_(rdl(sp.x, n)), f_(rdl(sp.y, n)), f_(rdl(sp.z, n)), 0.f}, y0, [&](int q) { fetch(fb + CH, d1, q); });
                cv_kvput<true>(F + CH, k);
                cv_run<true>(p, rs, F + CH, 326, rdl(D, n + 1), (f32x4){f_(rdl(sp.x, n + 1)), f_(rdl(sp.y, n + 1)), f_(rdl(sp.z, n + 1)), 0.f}, y1, [&](int q) { fetch(fb, d2, q); });
                cv_put(F, i >> 1, y0, y1);
            }
            cv_out(rs, F, un, 4);
            if (g == 16) { LST(283, rt32()); LST(289, __builtin_amdgcn_s_memtime()); }
        }
        DRAIN(); BAR();
    }
    pre_post(rs, j, 5u, ep);
    if (j == 20) LST(284, rt32());
    if (j == 3) LST(285, rt32());
}

#define M32A(first, d, a, b) do { if (first) asm volatile("v_mfma_f32_32x32x16_bf16 %0, %1, %2, 0" : "=&v"(d) : "a"(a), "v"(b)); \
                                  else asm volatile("v_mfma_f32_32x32x16_bf16 %0, %1, %2, %0" : "+v"(d) : "a"(a), "v"(b)); } while (0)
#define M32B(first, d, a, b) do { if (first) asm volatile("v_mfma_f32_32x32x16_bf16 %0, %1, %2, 0" : "=&v"(d) : "v"(a), "a"(b)); \
                                  else asm volatile("v_mfma_f32_32x32x16_bf16 %0, %1, %2, %0" : "+v"(d) : "v"(a), "a"(b)); } while (0)
#define VCVT(d, a, b) asm volatile("v_cvt_pk_bf16_f32 %0, %1, %2" : "=v"(d) : "v"(a), "v"(b))
#define VADD(d, a, b) asm volatile("v_add_f32 %0, %1, %2" : "=v"(d) : "v"(a), "v"(b))
#define LD4A(d, vo, so) asm volatile("buffer_load_dword %0, %1, %2, %3 offen sc1" : "=v"(d) : "v"(vo), "s"(srd), "s"(so) : "memory")
DEV u32 wst_src(int lane, int ld) { return (u32)((lane >> 4) * ld + 8 * ((lane & 15) ^ ((lane >> 4) << 2))) * 2u; }
DEV u32 wst_rd(int lane, int w) { const int q4 = (lane >> 2) & 3; return (u32)(q4 * 256 + ((w ^ q4) * 64) + ((lane >> 4) & 1) * 32 + (lane & 3) * 8); }

constexpr int QK_SEL = 81920;
#define QK_LEAD 12
#define QK_SMP 35
#define QK_YB 22
#define QK_W0 2
DEV void role_qkv(const Params& p, lptr lds, int c, v2u ep) {
    const int tid = threadIdx.x, lane = tid & 63, w = uni(tid >> 6), hh = lane >> 5, r32 = lane & 31;
    const int m = c >> 5, h = c & 31;
    const rsrc_t rs = mk_rsrc(p.ws, W_END);
    v4u wt[40];
    {
        const v4u sw = mk_srd(m == 0 ? p.Wq : m == 1 ? p.Wk : p.Wv, DM * NQ * 2), sx = mk_srd(p.x, ACT);
        const u32 vo = wst_src(lane, NQ) + (u32)h * (DH * 2), x8 = (u32)lane * 8u, x4 = (u32)lane * 4u;
        RowX xr[5];
        auto wfill = [&](int i0, int n) {
#pragma unroll 1
            for (int i = i0; i < i0 + n; ++i) DMAIN(sw, uu((4 * i + w) * 1024), vo, uu((16 * i + 4 * w) * (NQ * 2)));
        };
        wfill(0, QK_W0);
#pragma unroll
        for (int i = 0; i < 5; ++i)
            asm volatile("buffer_load_dwordx2 %0, %3, %5, %6 offen\n\tbuffer_load_dwordx2 %1, %3, %5, %6 offen offset:512\n\tbuffer_load_dword %2, %4, %5, %6 offen offset:1024"
                         : "=&v"(xr[i].a), "=&v"(xr[i].b), "=&v"(xr[i].c) : "v"(x8), "v"(x4), "s"(sx), "s"(uu((LN0 + 4 * c + w + 384 * i) * (DM * 2))) : "memory");
        wfill(QK_W0, 10 - QK_W0);
        if (c == 3) LST(275, rt32());
        VMW(10 - QK_W0);
        if (c == 3) LST(276, rt32());
#pragma unroll
        for (int i = 0; i < 5; ++i) {
            asm volatile("" : "+v"(xr[i].a), "+v"(xr[i].b), "+v"(xr[i].c));
            const int row = LN0 + 4 * c + w + 384 * i;
            if (i < 4 || row < SEQ) ln_row(rs, row, xr[i]);
            wfill(10 + 6 * i, 6);
        }
        if (c == 3) LST(264, rt32());
        lptr tb = lds + hh * 2048 + wst_rd(lane, w);
        SFOR(k, 40)
            if constexpr (k == 0) { VMW(15); BAR(); }
            if constexpr (k == 28) {
                if (w == 0) pre_poll(rs, 0, NPRE, 1u, ep);
                DRAIN(); BAR();
                pre_post(rs, 96 + c, 1u, ep);
            }
            lptr q = tb + (k >> 3) * 32768 + (k & 7) * 4096; wt[k] = tr2(q, q + 1024); asm volatile("" : "+a"(wt[k]));
        SEND
    }
    if (c == 3) LST(268, rt32());
    if (w == 0) LST(1350 + c, rt32());
    bool dead = false;
    int n0, n1;
    const u32 t16 = (u32)tid * 16u, wb = uu(w * 1024);
    u32 ar[16];
#pragma unroll
    for (int j = 0; j < 16; ++j) ar[j] = (u32)(r32 * 256 + ((2 * (j & 7) + hh) ^ (r32 & 15)) * 16 + (j >> 3) * 49152);
    MK_SRD();
#define XRD(y, ks) lds_r16(lds + ar[((ks) & 7) + ((ks) >= 24 ? 8 : 0)] + ((ks) >> 3) % 3 * 16384 + (y) * 8192)
#define QDMA(y, i, vo, so) DMA16I(wb, ((i) >> 1) * 16384 + ((i) & 1) * 4096 + (y) * 8192, vo, so, (i) * 4096)
    const u32 h2flag = (lane < 32 && w == 0) ? foff(F_H2 + lane) : OOB;
    const u32 ol = (m == 2 ? (u32)(2 * w + (r32 >> 4)) * 1024u : (u32)((r32 >> 4) * 4 + w) * 1024u) + (u32)(hh * 16 + (r32 & 15)) * 16u
                 + (m == 2 ? W_VO : m == 1 ? W_KQ : W_KQ + 8192u) + (u32)h * 16384u;
    const u32 fo = tid < 2 ? foff(F_QKV + tid * 96 + c) : OOB;
    lptr selw = lds + QK_SEL + (w == 0 ? 0 : 64), selr = lds + QK_SEL;
    auto late = [&](u32 fv) { const u32 need = n1 >= NSEG ? 0xFFFFFFFFu : dead ? 0u : (u32)n1 + 1u; return (u32)__builtin_amdgcn_ballot_w64(fv < need); };
    static_assert(W_H2 - W_H1 == 2 * 64 * (32 * DM * 2), "h");
    auto run = [&](auto isv) __attribute__((always_inline)) {
        constexpr bool V = decltype(isv)::value;
        constexpr int YB = QK_YB;
        f32x16 ax, ay = {};
        v4u x[8], pp, pq;
        u32 oy = OOB, lt = 1, fcnt = 0, fa = OOB;
        u32 nn = 0;
        PIN(lt); PIN(nn);
#pragma unroll
        for (int z = 0; z < 5; ++z) x[z] = XRD(1, 40 - YB + z);
#define QPK(r_, a1, a_, q_) do { if constexpr (V) VCVT(r_[q_], a1[4 * (a_) + 8 * ((q_) >> 1) + 2 * ((q_) & 1)], a1[4 * (a_) + 8 * ((q_) >> 1) + 2 * ((q_) & 1) + 1]); \
                                 else VCVT(r_[q_], a1[4 * (a_) + (q_)], a1[4 * (a_) + (q_) + 8]); } while (0)
#define QPK4(r_, a1, a_) do { QPK(r_, a1, a_, 0); QPK(r_, a1, a_, 1); QPK(r_, a1, a_, 2); QPK(r_, a1, a_, 3); } while (0)
        for (;;) {
            u32 ls = uu(lt);
            nn = uu(nn); n0 = nn & 255; n1 = (nn >> 8) & 255;
            if (ls != 0u && n0 >= min(NSEG, n1 + QK_LEAD)) {
                CFENCE();
                u32 fv = ld4(rs, h2flag);
                asm volatile("s_waitcnt vmcnt(0)" : "+v"(fv) :: "memory");
                *(LAS u32*)(selw + 16) = late(fv);
                BAR();
                st4s(rs, fa, 0, fcnt);
                fa = OOB;
                if (n1 >= NSEG) __builtin_amdgcn_endpgm();
                if (uu(*(LAS u32*)(selr + 16)) != 0u) {
                    if (w == 0) wait_flags(rs, h2flag, (u32)n1 + 1, dead);
                    BAR();
                }
                ls = 0u;
            }
            if (n0 >= 8 && (nn >> 16) == 0u) pre_wait(rs, 96, 96, 1u, ep);
            int cur = n0;
            asm volatile("s_cmp_eq_u32 %3, 0\n\ts_cselect_b32 %0, %4, %0\n\ts_addc_u32 %2, %2, 0\n\ts_cmp_lg_u32 %3, 0\n\ts_addc_u32 %1, %1, 0"
                         : "+s"(cur), "+s"(n0), "+s"(n1) : "s"(ls), "s"(n1 | 64) : "scc");
            u32 xs = W_H1 + (u32)(cur + (cur & 64)) * (32 * DM * 2), ys, yvo, ox, fv;
            PINS(xs);
#define QT(k) do {} while (0)
            QT(0);
            SCHED();
            QDMA(0, 0, t16, xs); QDMA(0, 1, t16, xs);
            SFOR(z, 80)
                constexpr int ks = (z + 40 - YB) % 40, zn = (z + 5) % 80, zx = z - YB;
                if constexpr (zx >= 0 && zx < 40) { if constexpr (V) M32B(ks == 0, ax, x[z & 7], wt[ks]); else M32A(ks == 0, ax, wt[ks], x[z & 7]); }
                else { if constexpr (V) M32B(ks == 0, ay, x[z & 7], wt[ks]); else M32A(ks == 0, ay, wt[ks], x[z & 7]); }
                x[(z + 5) & 7] = XRD(zn < YB || zn >= YB + 40, (zn + 40 - YB) % 40);
                if constexpr (z == 77) lt = *(LAS u32*)selr;
                SCHED();
                if constexpr (z < 2) QDMA(0, z + 2, t16, xs);
                if constexpr (z >= 3 && z < 15 && (z & 1)) QDMA(0, (z + 5) >> 1, t16, xs);
                if constexpr (z == 8) { ys = W_H1 + (u32)n0 * (32 * DM * 2); yvo = t16 | (n0 < NSEG ? 0u : OOB); PINS(ys); PIN(yvo); }
                if constexpr (z == 10) { ox = ol + (u32)cur * (NH * 16384u); PIN(ox); }
                if constexpr (zx == -6) { QT(1); asm volatile("s_waitcnt vmcnt(6) lgkmcnt(0)\n\ts_barrier" ::: "memory"); QT(2); }
                if constexpr (zx == -5) st4s(rs, fa, 0, fcnt);
                if constexpr (zx == -3 || zx == -1 || zx == 1) QDMA(1, (zx + 3) >> 1, yvo, ys);
                if constexpr (zx == 2) QPK4(pp, ay, 0);
                if constexpr (zx == 3) st16(rs, oy, pp);
                if constexpr (zx == 4) QPK4(pq, ay, 1);
                if constexpr (zx == 5) st16(rs, oy + 512, pq);
                if constexpr (zx == 7 || zx == 9 || zx == 12) QDMA(1, zx == 12 ? 5 : (zx - 1) >> 1, yvo, ys);
                if constexpr (zx == 10) { QT(3); asm volatile("s_waitcnt vmcnt(8)\n\ts_barrier" ::: "memory"); QT(4); }
                if constexpr (zx >= 16 && zx < 32 && (zx & 3) == 0) QDMA(1, (zx + 8) >> 2, yvo, ys);
                if constexpr (zx == 14) { fcnt = lane == 0 ? (u32)n0 : (u32)n1; fa = fo; asm volatile("" : "+v"(fcnt), "+v"(fa)); }
                if constexpr (zx == 18) { oy = (ol + (u32)n0 * (NH * 16384u)) | (yvo & OOB); PIN(oy); }
                if constexpr (zx == 22) { nn = (u32)(min(n0 + 1, NSEG) | n1 << 8) | (nn & 0xF8u) << 16; PIN(nn); }
                if constexpr (zx == 34) { asm volatile("s_waitcnt vmcnt(4) lgkmcnt(0)\n\ts_barrier" ::: "memory"); QT(5); }
                if constexpr (zx == QK_SMP) LD4A(fv, h2flag, 0);
                if constexpr (zx == 42) QPK4(pp, ax, 0);
                if constexpr (zx == 43) st16(rs, ox, pp);
                if constexpr (zx == 44) QPK4(pq, ax, 1);
                if constexpr (zx == 45) st16(rs, ox + 512, pq);
                if constexpr (z == 73) { asm volatile("s_waitcnt vmcnt(2)" : "+v"(fv) :: "memory"); QT(6); ls = late(fv); PINS(ls); }
                if constexpr (z == 74) { lt = ls; PIN(lt); *(LAS u32*)selw = lt; }
                if constexpr (z == 76) asm volatile("s_waitcnt lgkmcnt(2)\n\ts_barrier" ::: "memory");
                SCHED();
            SEND
        }
    };
    if (m == 2) run(IC<1>{}); else run(IC<0>{});
}

#define OU_SMP 44
DEV void role_out(const Params& p, lptr lds, int L, int kq, int ng, v2u ep) {
    const int tid = threadIdx.x, lane = tid & 63, w = uni(tid >> 6), hh = lane >> 5, r32 = lane & 31;
    const rsrc_t rs = mk_rsrc(p.ws, W_END);
    v4u wo[64];
    bool dead = false;
    {
        const v4u sw = mk_srd(p.Wo, DM * NQ * 2);
        const u32 vo = wst_src(lane, DM) + (u32)ng * 256u;
        auto fill = [&](int b) {
#pragma unroll 1
            for (int i = 0; i < 8; ++i) DMAIN(sw, uu((b % 5) * 32768 + (4 * i + w) * 1024), vo, uu((kq * 1024 + b * 128 + 16 * i + 4 * w) * (DM * 2)));
        };
#pragma unroll 1
        for (int b = 0; b < 5; ++b) fill(b);
        lptr tb = lds + hh * 1024 + wst_rd(lane, w);
        SFOR(b, 8)
            VMW(b < 4 ? 32 : 56 - 8 * b); BAR();
            lptr q = tb + (b % 5) * 32768;
#pragma unroll
            for (int k = 0; k < 8; ++k) { lptr a = q + (k >> 1) * 8192 + (k & 1) * 2048; wo[8 * b + k] = tr2(a, a + 4096); if (b < 7) asm volatile("" : "+a"(wo[8 * b + k])); else PIN(wo[8 * b + k]); }
            if constexpr (b < 3) { BAR(); fill(b + 5); }
        SEND
    }
    pre_wait(rs, 0, NPRE, 1u, ep);
    if (kq == 0 && ng == 0) LST(272 + L, rt32());
    MK_SRD();
    const u32 ofl = lane < 8 ? foff(F_O + L * 32 + kq * 8 + lane) : OOB, l16 = (u32)lane * 16u;
    const u32 fo = (tid == 0) ? foff(F_P + L * 32 + kq * 5 + ng) : OOB;
    const u32 src0 = uu(W_VO + 8192u + (u32)(L * NSEG * NH + kq * 8) * 16384u + w * 1024);
#define ODMA(i, dst, so) DMA16I(dst, (i) * 4096, l16, so, ((i) >> 1) * 16384 + ((i) & 1) * 4096)
    auto first = [&](int s) { const u32 dst = uu((s & 1) * 65536 + w * 1024), src = src0 + (u32)s * (NH * 16384u); SFOR(i, 10) ODMA(i, dst, src); SEND };
    u32 pa = W_P + (u32)(L * NSEG) * P_ITEM + (u32)(4 * hh * 4 + kq) * (DM * 4) + (u32)(ng * 128 + w * 32 + r32) * 4u, fv = 0;
    bool early = false;
    for (int s = 0; s < NSEG; ++s) {
        const u32 dst = uu((s & 1) * 65536 + w * 1024), src = src0 + (u32)s * (NH * 16384u);
        lptr A = lds + (s & 1) * 65536 + l16;
        if (!early) {
            if (!all_(ofl == OOB || fv >= (u32)s + 1)) wait_flags(rs, ofl, (u32)s + 1, dead);
            LS3(L == 0 && w == 1, 1, kq * 5 + ng);
            first(s);
        }
        asm volatile("s_waitcnt vmcnt(8)\n\ts_barrier" ::: "memory");
        v4u o[64];
#pragma unroll
        for (int k = 0; k < 3; ++k) o[k] = lds_r16(A + k * 1024);
        f32x16 acc;
        SCHED();
        SFOR(k, 64)
            if constexpr (k < 56) M32B(k == 0, acc, o[k], wo[k]); else M32V(acc, o[k], wo[k]);
            if constexpr (k < 61) o[k + 3] = lds_r16(A + (k + 3) * 1024);
            SCHED();
            if constexpr (k < 12 && (k & 1) == 0) ODMA(10 + k / 2, dst, src);
            if constexpr (k == 4) asm volatile("s_waitcnt vmcnt(7)\n\ts_barrier" ::: "memory");
            if constexpr (k == 20) asm volatile("s_waitcnt vmcnt(5)\n\ts_barrier" ::: "memory");
            if constexpr (k == 40) asm volatile("s_waitcnt vmcnt(0)\n\ts_barrier" ::: "memory");
            if constexpr (k == OU_SMP) LD4A(fv, ofl, 0);
            SCHED();
        SEND
        asm volatile("s_nop 9" : "+v"(acc), "+v"(pa));
#pragma unroll
        for (int i = 0; i < 16; ++i) st4s(rs, pa, (u32)((8 * (i >> 2) + (i & 3)) * (4 * DM * 4)), u_(acc[i]));
        asm volatile("s_waitcnt vmcnt(16)" : "+v"(fv) :: "memory");
        LS3(L == 0 && w == 1, 2, kq * 5 + ng);
        early = s + 1 < NSEG && all_(ofl == OOB || fv >= (u32)s + 2);
        if (early) { first(s + 1); VMW(10); }
        else { fv = ld4(rs, ofl); asm volatile("s_waitcnt vmcnt(0)" : "+v"(fv) :: "memory"); }
        BAR();
        st4s(rs, fo, 0, (u32)s + 1);
        pa += P_ITEM;
        LS2(L == 0 && w == 1, 160 + kq * 5 + ng);
    }
}

constexpr int RW_P = 20480, RW_X = RW_P + 10240;
DEV void role_row(const Params& p, lptr lds, int id, v2u ep) {
    const int lane = threadIdx.x & 63, w = uni(threadIdx.x >> 6);
    const int ri = id >> 1, L = id & 1;
    const rsrc_t rs = mk_rsrc(p.ws, W_END);
    bool dead = false, have = false;
    const u32 fp = lane < 20 ? foff(F_P + L * 32 + lane) : OOB, l16 = (u32)lane * 16u, lx = lane < 16 ? l16 + 1024u : OOB, fb = uu(w * CV_W);
    lptr F = lds + w * CV_W;
    MK_SRD();
    const v4u sx = mk_srd(p.x, ACT);
    auto yfill = [&](int b) {
#pragma unroll 1
        for (int i = 0; i < 5; ++i) DMA32(uu(fb + (b & 1) * 10240 + i * 2048), l16, uu(W_Y + (u32)(ri * 8 + b) * 10240u + i * 2048));
    };
    auto xfill = [&](int s) {
        const u32 d = uu(fb + RW_X + (s & 1) * 2048), o = uu((u32)(s * 32 + ri) * (DM * 2));
        if (L == 0) { DMAIN(sx, d, l16, o); DMAIN(sx, d + 1024, lx, o); } else { DMA16(d, l16, o + W_X2); DMA16(d + 1024, lx, o + W_X2); }
    };
    auto pfill = [&](int s) {
        const u32 o = uu(W_P + (u32)(L * NSEG + s) * P_ITEM + (u32)ri * (4 * DM * 4));
        SFOR(i, 10) DMA16I(fb, RW_P + i * 1024, l16, o, i * 1024); SEND
        if (L == 1) xfill(s);
    };
    if (L == 0) xfill(0);
    pre_poll(rs, 0, NPRE, 3u, ep);
    if (id == 0) LST(274, rt32());
    yfill(0); DRAIN();
    u32 fv = 0;
    for (int s = 0; s < NSEG; ++s) {
        if (!have) {
            if (!all_(fp == OOB || fv >= (u32)s + 1)) wait_flags(rs, fp, (u32)s + 1, dead);
            LS3(L == 0, 3, ri);
            pfill(s);
        }
        LS2(L == 0, 192 + ri);
        f32x2 y[5];
        {
            lptr ya = F + ((s >> 3) & 1) * 10240 + (s & 7) * 2 + lane * 16;
            auto yr = [&](lptr q) { return lo_f(*(LAS u16*)q); };
            y[0] = (f32x2){yr(ya), yr(ya + 2560)}; y[1] = (f32x2){yr(ya + 5120), yr(ya + 7680)};
            y[2] = (f32x2){yr(ya + 1024), yr(ya + 3584)}; y[3] = (f32x2){yr(ya + 6144), yr(ya + 8704)};
            lptr yc = ya - lane * 16 + 2048 + (lane >> 1) * 16 + (lane & 1) * 5120;
            y[4] = (f32x2){yr(yc), yr(yc + 2560)};
            asm volatile("" : "+v"(y[0]), "+v"(y[1]), "+v"(y[2]), "+v"(y[3]), "+v"(y[4]));
        }
        if (!have) DRAIN();
        LS3(L == 0, 4, ri);
        f32x2 x1[5];
        {
            f32x4 a[4], b[4]; f32x2 c[4];
            lptr q = F + RW_X + (s & 1) * 2048;
            v2u xa = *(LAS v2u*)(q + lane * 8), xb = *(LAS v2u*)(q + 512 + lane * 8); u32 xc = *(LAS u32*)(q + 1024 + lane * 4);
#pragma unroll
            for (int kq = 0; kq < 4; ++kq) { lptr r = F + RW_P + kq * (DM * 4); a[kq] = *(LAS f32x4*)(r + l16); b[kq] = *(LAS f32x4*)(r + 1024 + l16); c[kq] = *(LAS f32x2*)(r + 2048 + lane * 8); }
            SCHED();
            const f32x4 sa = (a[0] + a[1]) + (a[2] + a[3]), sb = (b[0] + b[1]) + (b[2] + b[3]);
            const f32x2 o[5] = {sa.lo, sa.hi, sb.lo, sb.hi, (c[0] + c[1]) + (c[2] + c[3])};
            const u32 xi[5] = {xa.x, xa.y, xb.x, xb.y, xc};
#pragma unroll
            for (int i = 0; i < 5; ++i) x1[i] = up2(xi[i]) + o[i];
            asm volatile("" : "+v"(x1[0]), "+v"(x1[1]), "+v"(x1[2]), "+v"(x1[3]), "+v"(x1[4]));
        }
        have = s + 1 < NSEG && all_(fp == OOB || fv >= (u32)s + 2);
        if (__builtin_expect(have, 0)) pfill(s + 1);
        if (L == 0) xfill(s + 1);
        const f32x2 st1 = ln_stats(x1);
        u32 xp[5];
#pragma unroll
        for (int i = 0; i < 5; ++i) xp[i] = pkv(x1[i] + (x1[i] - st1.x) * (y[i] * st1.y));
        const int row = s * 32 + ri;
        if (L == 0) {
            const u32 ro = (u32)row * (DM * 2);
            f32x2 x2[5];
#pragma unroll
            for (int i = 0; i < 5; ++i) x2[i] = up2(xp[i]);
            st8s(rs, (u32)lane * 8, W_X2 + ro, (v2u){xp[0], xp[1]});
            st8s(rs, (u32)lane * 8 + 512, W_X2 + ro, (v2u){xp[2], xp[3]});
            st4s(rs, (u32)lane * 4 + 1024, W_X2 + ro, xp[4]);
            const f32x2 st2 = ln_stats(x2);
            u32 hp[5];
#pragma unroll
            for (int i = 0; i < 5; ++i) hp[i] = pk2((x2[i] - st2.x) * st2.y);
            store_h(rs, W_H2, row, hp);
        } else {
            u16* o_ = p.out + (size_t)row * DM;
            *(v2u*)(o_ + 4 * lane) = (v2u){xp[0], xp[1]};
            *(v2u*)(o_ + 256 + 4 * lane) = (v2u){xp[2], xp[3]};
            *(u32*)(o_ + 512 + 2 * lane) = xp[4];
        }
        CFENCE();
        LS3(L == 0, 5, ri);
        fv = ld4(rs, fp);
        asm volatile("s_waitcnt vmcnt(0)" : "+v"(fv) :: "memory");
        st4s(rs, lane == 0 && L == 0 ? foff(F_H2 + ri) : OOB, 0, (u32)s + 1);
        if ((s & 7) == 6 && s < NSEG - 8) {
            if (s == 30) pre_poll(rs, 0, NCV, 5u, ep);
            if (s == 46) st8s(rs, (id == 1 && lane == 0) ? foff(F_EPOCH) : OOB, 0, (v2u){ep.x + 8u, ep.y});
            yfill((s >> 3) + 1);
        }
        if (ri == 0) LST(320 + L * 64 + s, rt32());
        LS2(L == 0, 224 + ri);
    }
}

constexpr float SCALE = 0.08838834764831845f, LOG2E = 1.4426950408889634f;
constexpr int C_K = 0, C_V = 8192, C_QX = 16384, C_MEM = 24576, C_Q = C_MEM + 10240, C_KM = C_Q + 2304, C_VM = C_KM + 2304, C_DUMP = C_VM + 2048,
              C_ZERO = C_DUMP + 1024, C_WL = C_ZERO + 128;
constexpr int NLF = 14;
static_assert(C_WL % 16 == 0 && C_WL + 4 * NLF * 2048 <= LDS_BYTES, "head lds");
#define CH_VIS 10
#define CH_H0 104
#define CH_W0 81
#define CH_MH 3
#define CH_MW 2
#define CH_REC 12
#define CH_RISE 12u
#define CH_FALL 3u
#define CH_PRE 9
#define CH_T0 190
#define CH_UP 2
#define CH_WX 6
#define WAIT_TXT(tmp, t4) "s_memrealtime vcc\n\t.p2align 5\n1:\n\ts_waitcnt lgkmcnt(0)\n\ts_sub_u32 " tmp ", " t4 ", vcc_lo\n\ts_memrealtime vcc\n\ts_cmpk_lt_u32 " tmp ", 400\n\ts_cbranch_scc1 1b\n\t" \
                          "s_add_u32 " tmp ", " tmp ", 4\n\ts_cselect_b32 " tmp ", " tmp ", 0\n2:\n\ts_sub_u32 " tmp ", " tmp ", 1\n\ts_cbranch_scc0 2b\n\t"
DEV void wait_until(u32 t) { u32 tmp; asm volatile(WAIT_TXT("%0", "%1") "s_waitcnt lgkmcnt(0)" : "=&s"(tmp) : "s"(uu(t - 4u)) : "vcc", "scc", "memory"); }
#define SMF_A(acc, a, b) asm volatile("v_smfmac_f32_16x16x64_bf16 %0, %1, %2, %3" : "+v"(acc) : "v"(a), "a"(b), "v"(idx))
#define SMF_V(acc, a, b) asm volatile("v_smfmac_f32_16x16x64_bf16 %0, %1, %2, %3" : "+v"(acc) : "v"(a), "v"(b), "v"(idx))
#define MF0(d, a, b) asm volatile("v_mfma_f32_16x16x32_bf16 %0, %1, %2, 0" : "=&v"(d) : "v"(a), "v"(b))
#define MF1(d, a, b) asm volatile("v_mfma_f32_16x16x32_bf16 %0, %1, %2, %0" : "+v"(d) : "v"(a), "v"(b))
#define MF0H(d, a, b) asm volatile("v_mfma_f32_16x16x16_bf16 %0, %1, %2, 0" : "=&v"(d) : "v"(a), "v"(b))
#define MF1H(d, a, b) asm volatile("v_mfma_f32_16x16x16_bf16 %0, %1, %2, %0" : "+v"(d) : "v"(a), "v"(b))
DEV v8u tr_frag(lptr p0, lptr p1, lptr p2, lptr p3) { return cat8(tr2(p0, p1), tr2(p2, p3)); }
constexpr int qslot(int ks) { return (ks & 1) && ks < 8 ? ks >> 1 : -1; }
constexpr int kslot(int ks) { return ks < 4 ? 4 + ks : -1; }
constexpr int vslot(int ks) { return ks >= 4 ? 4 + ks : -1; }
DEV u32 ring_map(u32 off, u32 half) { return ((off >> 13) << 14) + (half << 13) + (off & 8191u); }
#define ZRD(x, i) x = __builtin_bit_cast(f32x4, lds_r16(ZP + 16 * (i)))
#define VMAX(d, a, b) asm volatile("v_max_f32 %0, %1, %2" : "=v"(d) : "v"(a), "v"(b))
#define VMAX3(d, a, b, c_) asm volatile("v_max3_f32 %0, %1, %2, %3" : "=v"(d) : "v"(a), "v"(b), "v"(c_))
#define VEXP(d, a) asm volatile("v_exp_f32 %0, %1" : "=v"(d) : "v"(a))
#define VMULC(d, a) asm volatile("v_mul_f32 %0, 0x3e0293ee, %1" : "=v"(d) : "v"(a))
#define CADD(d, a, b) do { d = (a) + (b); PIN(d); } while (0)
#define CMULC(d, a) do { d = (a) * CEXP; PIN(d); } while (0)
#define CCVT(d, a, b) do { d = pk(a, b); PIN(d); } while (0)
#define LGK(n) __builtin_amdgcn_s_waitcnt(0xC07F | (n) << 8)
#define SEL(d, a, b) asm volatile("v_cndmask_b32 %0, %1, %2, %3" : "=v"(d) : "v"(a), "v"(b), "s"(himask))
#define QPERM(d, a, q) asm volatile("v_mov_b32_dpp %0, %1 quad_perm:" q " row_mask:0xf bank_mask:0xf" : "=v"(d) : "v"(a))

#define AL_H 3
#define AL_W 0
#define HD_SMP 56
DEV void role_head(const Params& p, lptr lds, int L, int h, v2u ep) {
    const int tid = threadIdx.x, lane = tid & 63, w = uni(tid >> 6), g = lane >> 4, r = lane & 15;
    const rsrc_t rs = mk_rsrc(p.ws, W_END);
    v8u wq0[10], wq1[10], wk0[10], wk1[10], wv0[10], wv1[10];
    lptr WL = lds + C_WL + w * (NLF * 2048) + lane * 16;
    {
        constexpr int GS = 1056, CS = 48 * GS;
        const v4u sq = mk_srd(p.Wq, DM * NQ * 2), sk = mk_srd(p.Wk, DM * NQ * 2), sv = mk_srd(p.Wv, DM * NQ * 2);
        const u32 vo = (u32)(g * NQ + h * DH + 8 * (r ^ ((g & 1) << 3))) * 2u;
        auto fill = [&](int ch) {
#pragma unroll 1
            for (int i = 0; i < 4; ++i) {
                const u32 dst = uu((ch % 3) * CS + (4 * i + w) * GS), so = uu((ch * 64 + 16 * i + 4 * w) * (NQ * 2));
                DMAIN(sq, dst, vo, so); DMAIN(sk, dst + 16 * GS, vo, so); DMAIN(sv, dst + 32 * GS, vo, so);
            }
        };
        fill(0); fill(1); fill(2);
        const int q4 = r >> 2;
        lptr tb = lds + (4 * (g >> 1) + 2 * (q4 >> 1) + (g & 1)) * GS + (q4 & 1) * 256 + ((w * 64 + (r & 3) * 8) ^ ((q4 & 1) << 7));
        SFOR(ch, 10)
            VMW(ch < 8 ? 24 : 108 - 12 * ch);
            BAR();
            auto frag = [&](int m, int t) { lptr b = tb + (ch % 3) * CS + m * 16 * GS + t * 32; return tr_frag(b, b + 512, b + 8 * GS, b + 8 * GS + 512); };
            wq0[ch] = frag(0, 0); wq1[ch] = frag(0, 1); wk0[ch] = frag(1, 0); wk1[ch] = frag(1, 1); wv0[ch] = frag(2, 0); wv1[ch] = frag(2, 1);
            asm volatile("" : "+a"(wq0[ch]), "+a"(wk0[ch]), "+v"(wv0[ch]), "+v"(wv1[ch]));
            if constexpr (qslot(ch) < 0) asm volatile("" : "+a"(wq1[ch])); else asm volatile("" : "+v"(wq1[ch]));
            if constexpr (kslot(ch) < 0) asm volatile("" : "+a"(wk1[ch])); else asm volatile("" : "+v"(wk1[ch]));
            BAR();
            if constexpr (ch < 7) fill(ch + 3);
        SEND
#pragma unroll
        for (int ch = 0; ch < 10; ++ch) {
            if (qslot(ch) >= 0) { lds_w16(WL + qslot(ch) * 2048, wq1[ch].lo); lds_w16(WL + qslot(ch) * 2048 + 1024, wq1[ch].hi); }
            if (kslot(ch) >= 0) { lds_w16(WL + kslot(ch) * 2048, wk1[ch].lo); lds_w16(WL + kslot(ch) * 2048 + 1024, wk1[ch].hi); }
            if (vslot(ch) >= 0) { lds_w16(WL + vslot(ch) * 2048, wv1[ch].lo); lds_w16(WL + vslot(ch) * 2048 + 1024, wv1[ch].hi); }
        }
        CFENCE();
    }
    const u32 idx = (r & 2) ? 0xEEEEu : 0x4444u;
    unsigned long long himask = 0xCCCCCCCCCCCCCCCCull;
    PINS(himask);
    bool dead = false;
    if (h == 3) LST(266 + L, rt32());
    if (w == 0) LST(1240 + L * 32 + h, rt32());
    if (L == 1) {
        KvIn in;
        kvt_load(p, KT0 + 4 * h + w, in, [] {}); kvt_make(rs, lds + w * 9216, KT0 + 4 * h + w, in);
        DRAIN(); BAR();
        pre_post(rs, 64 + h, 3u, ep);
    }
    const u32 pol0 = pol_wait(rs, ep);
    if (h == 3 && L == 0) LST(277, rt32());
    const u32 l16 = (u32)lane * 16u;
    const u32 mring = W_MRING + (u32)L * (MB / 4) + pol0 * 8192u, hint = mring + 4 * 32768u;
    const u32 fkh = lane < 3 ? foff(F_QKV + L * 96 + lane * 32 + h) : lane < 7 ? hint + (u32)(lane - 3) * 4u : OOB;
    u32 tm = 0;
    MK_SRD();
    u32 ksrc = uu(W_KQ + (u32)(L * (NSEG - 1) * NH + h) * 16384u + w * 2048), vsrc = ksrc + (W_VO - W_KQ);
    u32 dk = uu(C_K + w * 2048), dv = dk + (C_V - C_K), dq = dk + (C_QX - C_K), dm = uu(C_MEM + w * 2048), dm2 = uu(w < 2 ? C_MEM + 8192 + w * 1024 : C_DUMP);
    asm volatile("" : "+s"(dk), "+s"(dv), "+s"(dq), "+s"(dm), "+s"(dm2));
    auto keys_wait = [&](u32 need) {
#pragma unroll 1
        for (int n = 0; !dead; ++n) {
            const bool beat = need + 2 * L > 2u;
            if (beat) wait_until(tm);
            const u32 hv = ld4(rs, lane == 7 ? foff(F_ERR) : fkh);
            if (beat) { tm = rdl(hv, 5); wait_until(rdl(hv, 3) - 30u); }
            if (((u32)__builtin_amdgcn_ballot_w64(ld4(rs, fkh) < need) & 7u) == 0u) break;
            dead = n > SPIN_CAP || rdl(hv, 7) != 0u;
            st4s(rs, dead ? foff(F_ERR) : OOB, 0, 1u);
        }
        CFENCE();
        DMA32(dk, l16, ksrc); DMA32(dv, l16, vsrc); DMA32(dq, l16, ksrc ^ 8192u);
    };
    if (L == 0) { st4s(rs, tid == 0 ? hint + 4096u + (u32)h * 128u : OOB, 0, 0u); keys_wait(1u); }
    lptr qw = lds + C_Q + (2 * g) * 288 + (32 * w + 2 * r) * 2;
    lptr kw = qw + (C_KM - C_Q); PIN(kw);
    lptr qr = lds + C_Q + (2 * (r >> 2) + (r & 1)) * 288 + g * 16;
    lptr LB = lds + l16, VX = lds + C_V + w * 2048 + l16;
    PIN(VX);
    if (tid >= 64 && tid < 70) { const u32 z = tid == 68 ? 0x3F803F80u : tid == 69 ? PSENT : 0u; lds_w16(lds + C_ZERO + (tid - 64) * 16, (v4u){z, z, z, z}); }
    u32 zaddr = 0; PIN(zaddr);
    lptr ZP = lds + zaddr + C_ZERO;
    const float CEXP = SCALE * LOG2E;
#define LF_RD(slot, hf) lf[slot][hf] = lds_r16(WL + (slot) * 2048 + (hf) * 1024)
    BAR();
    const u32 ofl = tid == 0 ? foff(F_O + L * 32 + h) : OOB;
    u32 oso = (((g | L) & 1) ? OOB : 0u) + W_ORING + (u32)L * (MB / 2) + ring_map((u32)((r >> 3) * 32768 + h * 1024 + w * 256 + (g >> 1) * 128 + (r & 7) * 16), pol0);
    unsigned long long vis[8];
#pragma unroll
    for (int i = 0; i < 8; ++i) vis[i] = __builtin_amdgcn_ballot_w64(16 * (i >> 2) + 4 * g + (i & 3) <= 16 * (w & 1) + r);
    u32 missed = 0;
    for (int s = -L; s < NSEG;) {
        u32 sv = (u32)max(s, 0), ms = mring + (sv & 3u) * 32768u + w * 2048, ms2 = mring + (sv & 3u) * 32768u + 16384u + (w & 1) * 1024;
        asm volatile("" : "+s"(ms), "+s"(ms2)); PIN(sv);
        f32x4 aq0, aq1, ak0, ak1;
        v4u lf[NLF][2];
        ZRD(aq0, 0); ZRD(aq1, 1); ZRD(ak0, 2); ZRD(ak1, 3);
#pragma unroll
        for (int i = 0; i < 4; ++i) { LF_RD(i, 0); LF_RD(i, 1); }
        SCHED();
        v4u A[10];
#define HT(k) do {} while (0)
        v4u kx[8], qf[4], kmf[4], vx[2], ones, P;
        float fa, fb, fc, fd, inv, sx[4], t[4], tm0, tm1; u32 pa, pb, fl, e01, e23, pmm;
        f32x4 av0, av1, s0, s1, sm, oa, ob, rsum;
        v2u vm, pm;
#define LW(sl) cat8(lf[sl][0], lf[sl][1])
#define PQ(ks, tl) do { if constexpr (!(tl)) SMF_A(aq0, A[ks], wq0[ks]); else if constexpr (qslot(ks) < 0) SMF_A(aq1, A[ks], wq1[ks]); else { const v8u lw_ = LW(qslot(ks)); SMF_V(aq1, A[ks], lw_); } } while (0)
#define PK(ks, tl) do { if constexpr (!(tl)) SMF_A(ak0, A[ks], wk0[ks]); else if constexpr (kslot(ks) < 0) SMF_A(ak1, A[ks], wk1[ks]); else { const v8u lw_ = LW(kslot(ks)); SMF_V(ak1, A[ks], lw_); } } while (0)
#define PV(ks, tl) do { if constexpr (!(tl)) SMF_V(av0, A[ks], wv0[ks]); else if constexpr (vslot(ks) < 0) SMF_V(av1, A[ks], wv1[ks]); else { const v8u lw_ = LW(vslot(ks)); SMF_V(av1, A[ks], lw_); } } while (0)
#define KXR(i) kx[i] = lds_r16(LB + C_K + ((i) & 1) * 4096 + ((i) >> 1) * 1024)
#define XW(q_) do { *(LAS u32*)(q_) = pa; *(LAS u32*)((q_) + 288) = pb; } while (0)
        auto slot = [&](auto zc) __attribute__((always_inline)) {
            constexpr int z = decltype(zc)::value;
            if constexpr (z < 14) PQ(z >> 1, z & 1);
            else if constexpr (z < 17) PQ(z - 7, 0);
            else if constexpr (z < 20) PQ(z - 10, 1);
            else if constexpr (z < 34) PK((z - 20) >> 1, z & 1);
            else if constexpr (z < 37) PK(z - 27, 0);
            else if constexpr (z < 40) PK(z - 30, 1);
            else if constexpr (z < 42) { if constexpr (z == 40) MF0(s0, kx[0], qf[0]); else MF0(s1, kx[1], qf[0]); }
            else if constexpr (z < 48) { if constexpr (z & 1) MF1(s1, kx[z - 40], qf[(z - 40) >> 1]); else MF1(s0, kx[z - 40], qf[(z - 40) >> 1]); }
            else if constexpr (z < 54) PV(z - 48, 0);
            else if constexpr (z == 54) MF0(sm, kmf[0], qf[0]);
            else if constexpr (z < 58) MF1(sm, kmf[z - 54], qf[z - 54]);
            else if constexpr (z < 62) PV(z - 52, 0);
            else if constexpr (z < 64) PV(z - 62, 1);
            else if constexpr (z == 64) MF0(oa, vx[0], P);
            else if constexpr (z == 65) MF0(ob, vx[1], P);
            else if constexpr (z == 66) MF0(rsum, ones, P);
            else if constexpr (z < 75) PV(z < 70 ? z - 63 : z == 70 ? 2 : z == 71 ? 7 : z == 72 ? 3 : z - 65, 1);
            else { const v2u ones2 = ones.lo; MF1H(rsum, ones2, pm); }
            if constexpr (z < 6) A[4 + z] = lds_r16(LB + C_MEM + (4 + z) * 1024);
            if constexpr (z >= 6 && z < 10) LF_RD(4 + ((z - 6) >> 1), z & 1);
            if constexpr (z >= 14 && z < 18) LF_RD(6 + ((z - 14) >> 1), z & 1);
            if constexpr (z >= 22 && z < 28) KXR(z - 22);
            if constexpr (z == 28) XW(qw);
            if constexpr (z == 29 || z == 30) KXR(z - 23);
            if constexpr (z == 32 || z == 47) asm volatile("s_barrier" ::: "memory");
            if constexpr (z >= 33 && z < 37) qf[z - 33] = lds_r16(qr + (z - 33) * 64);
            if constexpr (z == 20) ZRD(av0, 0);
            if constexpr (z == 43) XW(kw);
            if constexpr (z == 44 || z == 45) vx[z - 44] = lds_r16(VX + (z - 44) * 1024);
            if constexpr (z >= 48 && z < 52) kmf[z - 48] = lds_r16(qr + (C_KM - C_Q) + (z - 48) * 64);
            if constexpr (z == 52) ZRD(av1, 1);
            if constexpr (z == 37) ones = lds_r16(ZP + 64);
            if constexpr (z >= 57 && z < 64) LF_RD(8 + ((z - 57) >> 1), (z - 57) & 1);
            if constexpr (z >= 65 && z < 70) LF_RD(8 + ((z - 58) >> 1), (z - 58) & 1);
            if constexpr (z == HD_SMP) LD4A(fl, fkh, 0);
            SCHED();
            if constexpr (z == 19) HT(2);
            if constexpr (z == 32) HT(3);
            if constexpr (z == 47) HT(4);
            if constexpr (z == 64) HT(5);
            if constexpr (z == 19) LGK(4);
            if constexpr (z == 21 || z == 41) LGK(1);
            if constexpr (z == 56 || z == 73) LGK(0);
            if constexpr (z == 31 || z == 46) LGK(2);
            if constexpr (z == 39) LGK(4);
            if constexpr (z >= 53 && z < 56) LGK(57 - z);
            if constexpr (z == 64) LGK(3);
            if constexpr (z == 68) LGK(5);
            if constexpr (z >= 70 && z < 73) LGK(74 - z);
            SCHED();
            if constexpr (z == 18) CADD(fa, aq0[0], aq0[2]);
            if constexpr (z == 19) CADD(fb, aq0[1], aq0[3]);
            if constexpr (z == 20) CMULC(fa, fa);
            if constexpr (z == 21) CADD(fc, aq1[0], aq1[2]);
            if constexpr (z == 22) CADD(fd, aq1[1], aq1[3]);
            if constexpr (z == 23) CMULC(fc, fc);
            if constexpr (z == 24) CMULC(fb, fb);
            if constexpr (z == 25) CMULC(fd, fd);
            if constexpr (z == 26) CCVT(pa, fa, fc);
            if constexpr (z == 27) CCVT(pb, fb, fd);
            if constexpr (z == 38) CADD(fa, ak0[0], ak0[2]);
            if constexpr (z == 39) CADD(fb, ak0[1], ak0[3]);
            if constexpr (z == 41) { CADD(fc, ak1[0], ak1[2]); CADD(fd, ak1[1], ak1[3]); }
            if constexpr (z == 42) { CCVT(pa, fa, fc); CCVT(pb, fb, fd); }
            if constexpr (z >= 49 && z < 53) SEL(sx[z - 49], s0[z - 49], s1[z - 49]);
            if constexpr (z >= 53 && z < 57) VEXP(t[z - 53], sx[z - 53]);
            if constexpr (z == 57) CCVT(e01, t[0], t[1]);
            if constexpr (z == 58) CCVT(e23, t[2], t[3]);
            if constexpr (z == 59) QPERM(P.x, e01, "[0,1,0,1]");
            if constexpr (z == 60) QPERM(P.z, e01, "[2,3,2,3]");
            if constexpr (z == 61) QPERM(P.y, e23, "[0,1,0,1]");
            if constexpr (z == 62) QPERM(P.w, e23, "[2,3,2,3]");
            if constexpr (z == 63) CADD(fa, av0[0], av0[2]);
            if constexpr (z == 64) { asm volatile("" :: "v"(sm)); VEXP(tm0, sm[0]); }
            if constexpr (z == 65) VEXP(tm1, sm[1]);
            if constexpr (z == 66) CADD(fb, av0[1], av0[3]);
            if constexpr (z == 67) CCVT(pmm, tm0, tm1);
            if constexpr (z >= 68 && z < 72) SEL(oa[z - 68], oa[z - 68], ob[z - 68]);
            if constexpr (z == 72) asm volatile("v_cndmask_b32 %0, %1, 0, %2" : "=v"(pm.x) : "v"(pmm), "s"(himask));
            if constexpr (z == 73) asm volatile("v_cndmask_b32 %0, 0, %1, %2" : "=v"(pm.y) : "v"(pmm), "s"(himask));
            if constexpr (z == 74) LGK(15);
            if constexpr (z == 75) CCVT(vm.x, fa, fb);
            SCHED();
        };
        {
            u32 tmp;
            asm volatile("s_mov_b32 m0, %2\n\t" WAIT_TXT("%0", "%1") "buffer_load_dwordx4 %4, %5, %6 offen sc1 lds\n\tbuffer_load_dwordx4 %4, %5, %6 offen offset:1024 sc1 lds\n\t"
                         "s_mov_b32 m0, %3\n\ts_nop 0\n\tbuffer_load_dwordx4 %4, %5, %7 offen sc1 lds\n\t.p2align 6\n\t.rept %8\n\ts_nop 0\n\t.endr\n\ts_waitcnt vmcnt(0) lgkmcnt(0)\n\ts_barrier"
                         : "=&s"(tmp) : "s"(uu(tm - 4u)), "s"(dm), "s"(dm2), "v"(l16), "s"(srd), "s"(ms), "s"(ms2), "i"(AL_H) : "vcc", "scc", "memory");
        }
        HT(1);
#pragma unroll
        for (int ks = 0; ks < 4; ++ks) A[ks] = lds_r16(LB + C_MEM + ks * 1024);
        SCHED();
        st4s(rs, ofl, 0, sv);
        SCHED();
        static_for<0, 76>(slot);
        asm volatile("s_nop 2\n\tv_add_f32 %1, %3, %5\n\tv_add_f32 %2, %4, %6\n\tv_cvt_pk_bf16_f32 %0, %1, %2"
                     : "=v"(vm.y), "=&v"(fc), "=&v"(fd) : "v"(av1[0]), "v"(av1[1]), "v"(av1[2]), "v"(av1[3]));
        __builtin_amdgcn_s_waitcnt(0xCF7F);
        asm volatile("s_nop 0\n\tv_mfma_f32_16x16x16_bf16 %0, %2, %3, %0\n\tv_rcp_f32 %1, %4" : "+v"(oa), "=&v"(inv) : "v"(vm), "v"(pm), "v"(rsum[0]), "v"(rsum));
        SCHED();
        if (__builtin_expect(__builtin_amdgcn_ballot_w64(u_(rsum[0]) - 0x21800000u > 0x3C000000u) != 0ull, 0)) {
            DRAIN(); SCHED();
            bool miss = false;
#pragma unroll 1
            for (int i = 0; i < 10; ++i) { const v4u a = lds_r16(LB + C_MEM + i * 1024); miss |= a.x == PSENT || a.y == PSENT || a.z == PSENT || a.w == PSENT; }
            if (missed < SPIN_CAP && !all_(!miss)) {
                *(LAS u32*)(lds + C_DUMP + (w == 0 ? 0 : 64)) = ld4(rs, foff(F_ERR));
                asm volatile("" : "+v"(fl) :: "memory");
                tm = rdl(fl, 5);
                BAR();
                missed = uu(*(LAS u32*)(lds + C_DUMP)) != 0u ? SPIN_CAP : missed + 1;
                st4s(rs, missed == SPIN_CAP ? foff(F_ERR) : OOB, 0, 1u);
                BAR();
                continue;
            }
            float mx = fmaxf(fmaxf(fmaxf(sx[0], sx[1]), fmaxf(sx[2], sx[3])), fmaxf(sm[0], sm[1]));
            mx = max32(max16(fmaxf(mx, dpp<0x4E>(mx))));
#pragma unroll
            for (int i = 0; i < 4; ++i) t[i] = __builtin_amdgcn_exp2f(sx[i] - mx);
            e01 = pk(t[0], t[1]); e23 = pk(t[2], t[3]);
            pmm = pk(__builtin_amdgcn_exp2f(sm[0] - mx), __builtin_amdgcn_exp2f(sm[1] - mx));
            asm volatile("v_cndmask_b32 %0, %2, 0, %3\n\tv_cndmask_b32 %1, 0, %2, %3" : "=&v"(pm.x), "=&v"(pm.y) : "v"(pmm), "s"(himask));
            asm volatile("s_nop 1" : "+v"(e01), "+v"(e23), "+v"(pm));
            QPERM(P.x, e01, "[0,1,0,1]"); QPERM(P.z, e01, "[2,3,2,3]"); QPERM(P.y, e23, "[0,1,0,1]"); QPERM(P.w, e23, "[2,3,2,3]");
            NOPS(1);
            MF0(rsum, ones, P); MF0(oa, vx[0], P); MF0(ob, vx[1], P);
            NOPS(7);
            { const v2u ones2 = ones.lo; MF1H(rsum, ones2, pm); }
#pragma unroll
            for (int i = 0; i < 4; ++i) SEL(oa[i], oa[i], ob[i]);
            NOPS(1);
            MF1H(oa, vm, pm);
            asm volatile("s_nop 7\n\tv_rcp_f32 %0, %1" : "=v"(inv) : "v"(rsum[0]), "v"(rsum));
        }
        {
            v4u oc; f32x2 u0 = oa.lo, u1 = oa.hi; const f32x2 i2 = {inv, inv};
            asm volatile("s_nop 0\n\tv_pk_mul_f32 %0, %0, %2\n\tv_pk_mul_f32 %1, %1, %2" : "+v"(u0), "+v"(u1) : "v"(i2));
            __builtin_amdgcn_s_waitcnt(0xCF7F);
            asm volatile("v_cvt_pk_bf16_f32 %0, %4, %5\n\tv_cvt_pk_bf16_f32 %2, %4, %5\n\tv_cvt_pk_bf16_f32 %1, %6, %7\n\tv_cvt_pk_bf16_f32 %3, %6, %7\n\t"
                         "v_permlane16_swap_b32 %0, %2\n\ts_nop 0\n\tv_permlane16_swap_b32 %1, %3"
                         : "=&v"(oc.x), "=&v"(oc.y), "=&v"(oc.z), "=&v"(oc.w) : "v"(u0.x), "v"(u0.y), "v"(u1.x), "v"(u1.y));
            st16(rs, oso, oc);
        }
        SCHED();
        unsigned long long clk;
        asm volatile("s_memrealtime %0" : "=s"(clk) :: "memory");
        HT(6);
        v4u tq[4], tk[8], tv[2];
        f32x4 c0, c1, cm;
        u32 oo, kvo; bool kvok;
        u32 ln = l16; PIN(ln);
        lptr QX = lds + C_QX + (w & 1) * 4096 + ln, VO = lds + C_V + (w ^ 1) * 2048 + ln, VMX = lds + C_VM + w * 512 + (ln >> 1);
#pragma unroll
        for (int i = 0; i < 2; ++i) { tq[i] = lds_r16(QX + i * 1024); kmf[i] = lds_r16(qr + (C_KM - C_Q) + i * 64); tk[2 * i] = lds_r16(LB + C_K + i * 1024); tk[2 * i + 1] = lds_r16(LB + C_K + 4096 + i * 1024); }
        *(LAS v2u*)VMX = vm;
        SCHED();
        SFOR(i, 12)
            if constexpr (i == 0) MF0(cm, kmf[0], tq[0]); else if constexpr (i == 1) MF0(c0, tk[0], tq[0]); else if constexpr (i == 2) MF0(c1, tk[1], tq[0]);
            else if constexpr (i % 3 == 0) MF1(cm, kmf[i / 3], tq[i / 3]); else if constexpr (i % 3 == 1) MF1(c0, tk[2 * (i / 3)], tq[i / 3]); else MF1(c1, tk[2 * (i / 3) + 1], tq[i / 3]);
            if constexpr (i < 2) kmf[2 + i] = lds_r16(qr + (C_KM - C_Q) + (2 + i) * 64);
            if constexpr (i == 0) { tq[2] = lds_r16(QX + 2048); tk[4] = lds_r16(LB + C_K + 2048); }
            if constexpr (i == 1) { tk[5] = lds_r16(LB + C_K + 6144); tq[3] = lds_r16(QX + 3072); }
            if constexpr (i == 2) { tk[6] = lds_r16(LB + C_K + 3072); tk[7] = lds_r16(LB + C_K + 7168); }
            if constexpr (i == 3) { tv[0] = lds_r16(VO); tv[1] = lds_r16(VO + 1024); }
            if constexpr (i == 4) { oo = ln + (ln & 0x300u) + ((vsrc ^ 8192u) + (u32)((w & 1) * 256) + ((u32)s & OOB)); PIN(oo); }
            if constexpr (i == 5) { ksrc += NH * 16384; vsrc += NH * 16384; PINS(ksrc); PINS(vsrc); }
            if constexpr (i == 9) { asm volatile("s_waitcnt vmcnt(1)" : "+v"(fl) :: "memory"); kvok = ((u32)__builtin_amdgcn_ballot_w64(fl < (u32)s + 2) & 7u) == 0u; }
            if constexpr (i == 10) { kvo = l16 | (kvok ? 0u : OOB); PIN(kvo); }
            SCHED();
        SEND
        asm volatile("s_waitcnt lgkmcnt(0)\n\ts_barrier" : "+s"(clk) :: "memory");
        const v2u tvm = *(LAS v2u*)(VMX + (512 - (w & 1) * 1024));
        const v4u pv = lds_r16(ZP + 80);
        DMA32(dk, kvo, ksrc);
        SCHED(); asm volatile("" :: "v"(cm));
        float u[10], m, m2;
        {
            float NEG = -1000000000.0f / SCALE; PIN(NEG);
#define MSK(d, a, i) asm volatile("v_cndmask_b32 %0, %1, %2, %3" : "=v"(d) : "v"(NEG), "v"(a), "s"(vis[i]))
#pragma unroll
            for (int i = 0; i < 4; ++i) MSK(u[i], c0[i], i);
#pragma unroll
            for (int i = 0; i < 4; ++i) MSK(u[4 + i], c1[i], 4 + i);
            asm volatile("v_max3_f32 %0, %2, %3, %10\n\tv_max3_f32 %1, %4, %5, %11\n\tv_max3_f32 %0, %0, %6, %7\n\tv_max3_f32 %1, %1, %8, %9\n\t"
                         "v_max_f32 %0, %0, %1\n\tv_mov_b32 %1, %0\n\ts_nop 1\n\tv_permlane16_swap_b32 %0, %1\n\tv_max_f32 %0, %0, %1\n\tv_mov_b32 %1, %0\n\ts_nop 1\n\t"
                         "v_permlane32_swap_b32 %0, %1\n\tv_max_f32 %0, %0, %1\n\tv_mul_f32 %0, 0xbe0293ee, %0"
                         : "=&v"(m), "=&v"(m2) : "v"(u[0]), "v"(u[1]), "v"(u[2]), "v"(u[3]), "v"(u[4]), "v"(u[5]), "v"(u[6]), "v"(u[7]), "v"(cm[0]), "v"(cm[1]));
        }
        DMA32(dv, kvo, vsrc);
        {
            v4u px; v2u p0, p1;
#define EX2(d, a, b) asm volatile("v_fmamk_f32 %0, %2, 0x3e0293ee, %4\n\tv_fmamk_f32 %1, %3, 0x3e0293ee, %4\n\tv_exp_f32 %0, %0\n\tv_exp_f32 %1, %1" : "=&v"(d.x), "=&v"(d.y) : "v"(a), "v"(b), "v"(m))
            f32x2 ex[5];
            EX2(ex[0], u[0], u[1]); EX2(ex[1], u[2], u[3]); EX2(ex[2], u[4], u[5]); EX2(ex[3], u[6], u[7]); EX2(ex[4], cm[0], cm[1]);
            asm volatile("v_cvt_pk_bf16_f32 %0, %8, %9\n\tv_cvt_pk_bf16_f32 %1, %10, %11\n\tv_cvt_pk_bf16_f32 %2, %12, %13\n\tv_cvt_pk_bf16_f32 %3, %14, %15\n\t"
                         "v_mov_b32 %5, 0\n\tv_mov_b32 %6, 0\n\tv_cvt_pk_bf16_f32 %4, %16, %17\n\tv_cvt_pk_bf16_f32 %7, %16, %17"
                         : "=&v"(px.x), "=&v"(px.y), "=&v"(px.z), "=&v"(px.w), "=&v"(p0.x), "=&v"(p0.y), "=&v"(p1.x), "=&v"(p1.y)
                         : "v"(ex[0].x), "v"(ex[0].y), "v"(ex[1].x), "v"(ex[1].y), "v"(ex[2].x), "v"(ex[2].y), "v"(ex[3].x), "v"(ex[3].y), "v"(ex[4].x), "v"(ex[4].y));
            f32x4 o[4], sum;
            const v2u ones2 = ones.lo;
            const u32 now = (u32)clk;
            u32 oz;
            SCHED();
            MF0(sum, ones, px); { const u32 q = ksrc ^ 8192u; DMA32(dq, kvo, q); } SCHED();
            MF0(o[0], vx[0], px);
            MF0(o[1], vx[1], px);
            MF0(o[2], tv[0], px); oz = (oso & ~(3u << 17)) | (u32)((s + 3) & 3) << 17; PIN(oz); SCHED();
            MF0(o[3], tv[1], px); oso = (oso & ~(3u << 17)) | (u32)((s + 1) & 3) << 17; PIN(oso); SCHED();
            MF1H(sum, ones2, p0);
            MF1H(o[0], vm, p0);
            MF1H(o[1], vm, p1); st16(rs, oz, pv); SCHED();
            MF1H(o[2], tvm, p0);
            MF1H(o[3], tvm, p1);
            asm volatile("s_nop 2\n\tv_rcp_f32 %0, %1\n\ts_nop 0" : "=v"(inv) : "v"(sum[0]), "v"(sum));
            asm volatile("" : "+v"(o[0]), "+v"(o[1]));
            st16(rs, oo, (v4u){pk2(o[0].lo * inv), pk2(o[0].hi * inv), pk2(o[1].lo * inv), pk2(o[1].hi * inv)});
            asm volatile("" : "+v"(o[2]), "+v"(o[3]));
            st16(rs, oo ^ 2048u, (v4u){pk2(o[2].lo * inv), pk2(o[2].hi * inv), pk2(o[3].lo * inv), pk2(o[3].hi * inv)});
            SCHED();
            LS2(L == 0 && w == 0, h);
            st4s(rs, ((h & 15) == 0 && tid == 192) ? hint + 128u + (u32)h * 8u : OOB, 0, (u32)s << 16 | min(missed, 1u) << 14 | min(now - tm, 0x3FFFu));
            tm = rdl(fl, 5);
            missed = missed == SPIN_CAP ? missed : 0u;
            if (__builtin_expect((int)(rdl(fl, 3) - now) < CH_VIS - 4 || s < 1 - L, 0)) {
                const bool first = s == 0 && L == 0;
                if (s < 0) oso -= (ln & 256u) ? 0u : OOB;
#pragma unroll 1
                for (int n = 0; n < (s < 0 ? 0 : first ? 200 : 8); ++n) {
                    CFENCE();
                    const u32 hv = ld4(rs, fkh);
                    tm = rdl(hv, first ? 6 : 5);
                    if (first ? tm != PSENT : (int)(rdl(hv, 3) - now) >= CH_VIS - 4) break;
                }
            }
        }
        if (__builtin_expect(!kvok, 0) && s + 1 < NSEG) {
            st4s(rs, ln == 0 ? hint + 4096u + (u32)h * 128u : OOB, 0, (u32)s + 1u);
            keys_wait((u32)s + 2u);
        }
        ++s;
    }
    DRAIN(); BAR();
    st4s(rs, ofl, 0, NSEG);
}

#define WO_NL 3
#define WO_ROT 0
#define WO_AH 1
DEV void role_wo(const Params& p, lptr lds, int j, v2u ep) {
    const int tid = threadIdx.x, lane = tid & 63, w = uni(tid >> 6), g = lane >> 4, r = lane & 15;
    const int rg = j & 1, cg = j >> 1, rot = WO_ROT;
    const rsrc_t rs = mk_rsrc(p.ws, W_END);
    v8u wo[8][5];
    {
        const v4u so_ = mk_srd(p.Wo, DM * NQ * 2);
        u32 vo[5];
#pragma unroll
        for (int i = 0; i < 5; ++i) { const int n = 64 * i + lane; vo[i] = (u32)((n / 5) * DM + cg * 40 + (n % 5) * 8) * 2u; }
        v4u junk = {};
        const u32 av = (u32)(j * 5120 + lane * 16);
        auto ahead = [&](int b) {
#pragma unroll
            for (int i = 0; i < 5; ++i) AHEAD(so_, av, uu((8 * w + b) * (DH * DM * 2) + i * 1024));
        };
        auto fill = [&](int b) {
            if (b + WO_AH < 8) ahead(b + WO_AH);
            const u32 dst = uu(w * 40960 + (b & 3) * 10240), src = uu((8 * w + ((b + rot) & 7)) * (DH * DM * 2));
#pragma unroll
            for (int i = 0; i < 10; ++i) DMAIN(so_, dst + i * 1024, vo[i % 5], src + (i / 5) * (64 * DM * 2));
        };
#pragma unroll 1
        for (int b = 0; b < WO_AH; ++b) ahead(b);
#pragma unroll 1
        for (int b = 0; b < 4; ++b) fill(b);
        const int q4 = r >> 2;
        lptr tb = lds + w * 40960 + (32 * (g >> 1) + 16 * (q4 >> 1) + 8 * ((r >> 1) & 1) + 4 * (g & 1) + (q4 & 1)) * 80 + (r & 1) * 8;
        SFOR(b, 8)
            VMW((b < 5 ? 30 : 70 - 10 * b) + (b + 1 + WO_AH < 8 ? 5 : 0) + (b + 2 + WO_AH < 8 ? 5 : 0) + (b + 3 + WO_AH < 8 ? 5 : 0));
            if constexpr (b + WO_AH == 7) PIN(junk);
#pragma unroll
            for (int t = 0; t < 5; ++t) {
                lptr q = tb + (b & 3) * 10240 + t * 16;
                wo[b][t] = tr_frag(q, q + 160, q + 64 * 80, q + 64 * 80 + 160);
                if (b * 5 + t < 32) asm volatile("" : "+a"(wo[b][t])); else asm volatile("" : "+v"(wo[b][t]));
            }
            CFENCE();
            if constexpr (b < 4) fill(b + 4);
        SEND
    }
    if (j == 0) LST(265, rt32());
    LST(1100 + j * 4 + w, rt32());
    const u32 idx = (r & 2) ? 0xEEEEu : 0x4444u;
    const u32 pol0 = pol_wait(rs, ep);
    BAR();
    const u32 l16 = (u32)lane * 16u;
    u32 oo[8];
#pragma unroll
    for (int i = 0; i < 8; ++i) oo[i] = uu(W_ORING + ring_map((u32)(rg * 32768 + (8 * w + ((i + rot) & 7)) * 1024), pol0));
    const u32 hint = W_MRING + 4 * 32768u + pol0 * 8192u;
    u32 hvo[2];
#pragma unroll
    for (int l = 0; l < 2; ++l) hvo[l] = lane < 2 ? hint + (u32)(l ^ 1) * (MB / 4) + (u32)lane * 4u : lane == 2 ? hint + (u32)l * (MB / 4) + 128u : lane == 3 ? foff(F_ERR) : lane == 4 ? hint + (u32)l * (MB / 4) + 256u :
                 (lane >= 32 && j == 0 && w == 3) ? hint + (u32)l * (MB / 4) + (u32)lane * 128u : OOB;
    lptr xw = r < 8 ? lds + w * 640 + (2 * (g & 1) + (g >> 1)) * 32 + r * 4 : lds + 8192 + lane * 4;
    const int G = ((5 * cg) >> 1) + w, tt = 2 * G + ((lane >> 3) & 1) - 5 * cg;
    const bool pub = w < 3 && lane < 32 && tt >= 0 && tt < 5;
    lptr xr = lds + (pub ? tt * 128 + (2 * (lane >> 4) + ((lane >> 2) & 1)) * 32 + (lane & 3) * 8 : 0);
    const u32 mpo = pub ? W_MRING + ring_map((u32)(G * 256 + rg * 128 + lane * 4), pol0) : OOB;
    constexpr u32 PER0 = CH_W0 + CH_MW + CH_H0 + CH_MH + 2 * CH_VIS, DW0 = CH_W0 + CH_VIS + CH_MW + CH_REC;
    const bool lead = j == 0 && w == 3;
    const u32 hso = (lead && lane == 0) ? hint : OOB;
    MK_SRD();
    u32 t = 0, tn = 0;
    int sn[2] = {0, 0};
    u32 hav = 8 * CH_H0, wav = 8 * CH_W0, mh = 8 * CH_MH, mw = 8 * CH_MW, ok[2] = {0u, 0u}, dw[2] = {DW0, DW0};
    u32 vo = l16, early = PSENT, tp = 0, fin, left, cnt = 0;
    unsigned long long clk = 0;
    {
        if (w == 0) LST(1310 + j, rt32());
        if (lead) {
#pragma unroll 1
            for (int n = 0; n < SPIN_CAP; ++n) {
                CFENCE();
                const u32 a = ld4(rs, foff(F_QKV + lane)), b = ld4(rs, lane < 32 ? foff(F_QKV + 64 + lane) : hvo[0]);
                if (all_(a != 0u && b != (lane < 32 ? 0u : PSENT))) break;
                __builtin_amdgcn_s_sleep(1);
            }
        }
        if (lead) {
            t = rt32() + CH_T0; tn = t + PER0 / 2 + CH_REC;
            st16(rs, lane < 2 ? hint + (u32)lane * (MB / 4) : OOB, (v4u){lane == 0 ? t : tn, PER0, (lane == 0 ? t : tn) + DW0, t + DW0});
        } else {
#pragma unroll 1
            for (int n = 0; n < SPIN_CAP; ++n) {
                CFENCE();
                const u32 hv = ld4(rs, hvo[1]);
                t = rdl(hv, 0);
                if (rdl(hv, 1) < 400u) break;
                __builtin_amdgcn_s_sleep(1);
            }
        }
        clk = t - 100u;
    }
    const u32 tm0 = t + DW0;
    u32 zero = 0, hv = 0xFFFFu, pst = 1, pmp = OOB;
    const u32 psent = PSENT;
    lptr sw = lds + 2560 + w * 4;
    asm volatile("" : "+v"(zero), "+v"(sw));
    auto turn = [&](auto lc) __attribute__((always_inline)) {
        constexpr int L = decltype(lc)::value, M = L ^ 1;
        u32 done, rep, rd, lost, dh, dn, nt, za, pdur, mp; v4u hw;
        const int sc = sn[L];
        auto books = [&](auto ic) __attribute__((always_inline)) {
            constexpr int i = decltype(ic)::value;
            if constexpr (i == 0) { rep = max(rdl(hv, 2), rdl(hv, 4)); PINS(rep); }
            if constexpr (i == 1) { lost = (u32)(__builtin_amdgcn_ballot_w64(hv == (u32)sn[M]) >> 32); PINS(lost); }
            if constexpr (i == 2) { lost |= (rep >> 16) ^ (u32)sn[M]; PINS(lost); }
            if constexpr (i == 3) { lost |= ok[M] ^ 2u; rd = rep & 0xFFFFu; PINS(lost); PINS(rd); }
            if constexpr (i == 4) { lost = lost == 0u ? CH_UP * 8u : 0u; PINS(lost); }
            if constexpr (i == 5) { pdur = ((u32)clk - tp) * 8u; PINS(pdur); pdur = min(pdur, wav + CH_RISE); PINS(pdur); }
            if constexpr (i == 6) { pdur = (done & ok[M]) ? pdur : wav; PINS(pdur); }
            if constexpr (i == 7) { wav = max(pdur, wav - CH_FALL); PINS(wav); }
            if constexpr (i == 8) { nt = min(rd * 8u, hav + CH_RISE); PINS(nt); }
            if constexpr (i == 9) { nt = rd < 0x3FFFu ? nt : hav; PINS(nt); }
            if constexpr (i == 10) { hav = max(nt, hav - CH_FALL); PINS(hav); }
            if constexpr (i == 11) { mw = max(mw - 1u, CH_MW * 8u); PINS(mw); }
            if constexpr (i == 12) { mh = max(mh - 1u, CH_MH * 8u); PINS(mh); }
            if constexpr (i == 13) { rd = rd >> 14 == 1u ? lost : 0u; PINS(rd); }
            if constexpr (i == 14) { mw += rd; mh += lost - rd; PINS(mw); PINS(mh); }
            if constexpr (i == 15) { ok[M] = done ? 1u : (ok[M] & 1u) << 1; PINS(ok[M]); }
            if constexpr (i == 16) { dh = (hav + mh) / 8u + (sn[1] < 3 ? CH_VIS + CH_PRE : CH_VIS); PINS(dh); }
            if constexpr (i == 17) { dn = (wav + mw) / 8u + CH_VIS; PINS(dn); }
            if constexpr (i == 18) { nt = dw[L] + dh + (u32)max((int)((u32)clk + CH_WX - t), 0); PINS(nt); }
            if constexpr (i == 19) { dh += dn; dw[L] = dn; sn[M] += (int)done; PINS(dh); PINS(sn[M]); }
            if constexpr (i == 20) { rd = tn - t + (dh >> 1); PINS(rd); }
            if constexpr (i == 22) { nt = t + (u32)max((int)nt, (int)rd); PINS(nt); }
            if constexpr (i == 23) { hw.x = nt; hw.y = dh; PIN(hw.x); PIN(hw.y); }
            if constexpr (i == 24) { hw.z = nt + dn; hw.w = tm0; PIN(hw); }
            if constexpr (i == 25) st16(rs, hso + L * (MB / 4), hw);
        };
        {
#define WT(k) do {} while (0)
            WT(0);
            v4u d[8];
            {
                u32 tmp;
                asm volatile(WAIT_TXT("%1", "%2") "buffer_load_dwordx4 %0, %3, %4, %5 offen sc1\n\ts_waitcnt lgkmcnt(0)"
                             : "=&v"(d[0]), "=&s"(tmp) : "s"(uu(t - 4u)), "v"(vo), "s"(srd), "s"(oo[0]) : "vcc", "scc", "memory");
                PINS(clk);
            }
            SFOR(i, 7)
                asm volatile("buffer_load_dwordx4 %0, %1, %2, %3 offen sc1" : "=v"(d[i + 1]) : "v"(vo), "s"(srd), "s"(oo[i + 1]) : "memory");
                SCHED();
                if constexpr (i == 0) { done = uu(pst) ^ 1u; PINS(done); }
                static_for<3 * i, 3 * i + 3>(books);
                SCHED();
            SEND
            LD4A(hv, hvo[L], 0);
            SCHED();
            WT(1);
            static_for<22, 26>(books);
            za = done ? pmp : OOB;
            st4s(rs, za, 0, psent);
            *(LAS u32*)(sw + L * 4096) = zero;
            f32x4 acc[5] = {};
            mp = mpo + (L * (MB / 4) + (u32)((sc + 1) & 3) * 32768u); pmp = mpo + (L * (MB / 4) + (u32)(sc & 3) * 32768u);
            asm volatile("" : "+v"(acc[0]), "+v"(acc[1]), "+v"(acc[2]), "+v"(acc[3]), "+v"(acc[4]), "+v"(mp), "+v"(pmp));
            SCHED();
            float f0[5], f1[5]; unsigned long long bad;
#define WSW(o_) asm volatile("v_permlane32_swap_b32 %0, %1" : "+v"(f0[o_]), "+v"(f1[o_]))
#define WAD(o_) asm volatile("v_add_f32_dpp %0, %1, %0 row_ror:8 row_mask:0xf bank_mask:0xf" : "+v"(f0[o_]) : "v"(f1[o_]))
#define WFO(o_) do { VADD(f0[o_], acc[o_][0], acc[o_][2]); VADD(f1[o_], acc[o_][1], acc[o_][3]); } while (0)
#define WWR(o_) *(LAS float*)(xw + L * 4096 + (o_) * 128) = f0[o_]
            u32 th, per, so, erl = early;
            auto next = [&](auto ic_) __attribute__((always_inline)) {
                constexpr int i = decltype(ic_)::value;
                if constexpr (i == 0) { so = (u32)sn[M] << 17; PINS(so); }
                if constexpr (i == 1) { so = (so & 0x60000u) + M * (MB / 2); PINS(so); }
                if constexpr (i == 2) { so = sn[M] + 1 < NSEG ? so : OOB; PINS(so); }
                if constexpr (i == 3) { early = sn[M] + 1 < NSEG ? PSENT : 0u; PIN(early); }
                if constexpr (i == 4) { vo = l16 + so; PIN(vo); }
                if constexpr (i == 5 && L) { fin = (u32)(sn[0] + sn[1] + 2); PINS(fin); }
                if constexpr (i == 6 && L) { fin = (fin | uu(left)) >> 7; PIN(fin); }
                if constexpr (i == 6 && !L) { cnt += 1u; PIN(cnt); left = cnt >> 9; PIN(left); }
                if constexpr (i == 7) { PIN(hv); th = rdl(hv, 0); PINS(th); }
                if constexpr (i == 8) { per = rdl(hv, 1); PINS(per); so = th - t; PINS(so); }
                if constexpr (i == 9) { per = (int)so < 20 ? per : 0u; PINS(per); }
                if constexpr (i == 10) { th += per; PINS(th); if constexpr (L) { per = rdl(hv, 3); PINS(per); } }
                if constexpr (i == 11) { tp = t; t = lead ? tn : th; PINS(t); }
                if constexpr (i == 12) { if constexpr (L) { fin |= per; PIN(fin); } tn = nt; }
            };
            auto slot = [&](auto ic_) __attribute__((always_inline)) {
                constexpr int i = decltype(ic_)::value;
                constexpr int NL = WO_NL, I0 = 40 - 5 * NL, pc = i < I0 ? i / 5 : 8 - NL + (i - I0) % NL, oc = i < I0 ? i % 5 : (i - I0) / NL;
                if constexpr (oc == 0) asm volatile("s_waitcnt vmcnt(%1)" : "+v"(d[pc]) : "i"(10 - pc));
                if constexpr (pc * 5 + oc < 32) SMF_A(acc[oc], d[pc], wo[pc][oc]); else SMF_V(acc[oc], d[pc], wo[pc][oc]);
                SCHED();
                if constexpr (i == 0) WT(2);
                if constexpr (i >= 5 && i < 13 && i != 9) next(IC<i - 5 - (i > 9)>{});
                if constexpr (i == 20) WT(4);
                constexpr int e = i - (I0 + NL + 1), o = e < 0 ? 0 : e / 3;
                if constexpr (e >= 0 && e % 3 == 0) { CADD(f0[o], acc[o][0], acc[o][2]); if constexpr (o > 0) WAD(o - 1); }
                if constexpr (e >= 0 && e % 3 == 1) { if constexpr (o > 0) { WWR(o - 1); SCHED(); } CADD(f1[o], acc[o][1], acc[o][3]); }
                if constexpr (e >= 0 && e % 3 == 2) {
                    if constexpr (o == 0) asm volatile("v_cmp_u_f32 %0, %1, %2" : "=s"(bad) : "v"(f0[0]), "v"(f1[0]));
                    else LGK(15);
                    WSW(o);
                }
                SCHED();
            };
            asm volatile(".rept %0\n\ts_nop 0\n\t.endr" :: "i"(AL_W));
            static_for<0, 2>(slot);
            if (__builtin_expect(__builtin_amdgcn_ballot_w64(d[0].x == erl) != 0ull, 0)) {
                asm volatile("s_waitcnt vmcnt(3)" :: "v"(d[1]), "v"(d[2]), "v"(d[3]), "v"(d[4]), "v"(d[5]), "v"(d[6]), "v"(d[7]) : "memory");
                SCHED();
                *(LAS u32*)(sw + L * 4096) = 1u;
                static_for<0, 7>(next);
            } else {
            static_for<2, 40>(slot);
            WT(5);
            static_assert(WO_NL == 3, "tail");
            if (__builtin_expect(bad != 0ull, 0)) {
                bool miss = false;
#pragma unroll
                for (int i = 0; i < 8; ++i) miss |= d[i].x == PSENT;
                if (all_(!miss)) {
#pragma unroll
                    for (int i = 0; i < 8; ++i) miss |= d[i].y == PSENT || d[i].z == PSENT || d[i].w == PSENT;
                }
                *(LAS u32*)(sw + L * 4096) = all_(!miss) ? 0u : 1u;
            }
            asm volatile("v_permlane32_swap_b32 %0, %1\n\ts_nop 1\n\tv_add_f32_dpp %0, %1, %0 row_ror:8 row_mask:0xf bank_mask:0xf\n\tv_add_f32 %2, %4, %6\n\tv_add_f32 %3, %5, %7"
                         : "+v"(f0[3]), "+v"(f1[3]), "=&v"(f0[4]), "=&v"(f1[4]) : "v"(acc[4][0]), "v"(acc[4][1]), "v"(acc[4][2]), "v"(acc[4][3]));
            WWR(3); SCHED();
            asm volatile("s_nop 0\n\tv_permlane32_swap_b32 %0, %1\n\ts_nop 1\n\tv_add_f32_dpp %0, %1, %0 row_ror:8 row_mask:0xf bank_mask:0xf" : "+v"(f0[4]), "+v"(f1[4]));
            WWR(4);
            }
            WT(6);
            f32x2 a, b, c, e; u32 mq; v4u sv;
            asm volatile("s_waitcnt lgkmcnt(0)\n\ts_barrier\n\tds_read_b128 %4, %6 offset:%7+2560\n\t"
                         "ds_read_b64 %0, %5 offset:%7\n\tds_read_b64 %1, %5 offset:%7+640\n\tds_read_b64 %2, %5 offset:%7+1280\n\tds_read_b64 %3, %5 offset:%7+1920"
                         : "=&v"(a), "=&v"(b), "=&v"(c), "=&v"(e), "=&v"(sv) : "v"(xr), "v"(zero), "i"(L * 4096) : "memory");
            asm volatile("s_waitcnt lgkmcnt(4)\n\tv_or_b32 %4, %6, %7\n\tv_or3_b32 %4, %4, %8, %9\n\tv_lshl_or_b32 %5, %4, 31, %10\n\t"
                         "s_waitcnt lgkmcnt(2)\n\tv_pk_add_f32 %0, %0, %1\n\ts_waitcnt lgkmcnt(0)\n\tv_pk_add_f32 %2, %2, %3\n\ts_nop 0\n\tv_pk_add_f32 %0, %0, %2"
                         : "+v"(a), "+v"(b), "+v"(c), "+v"(e), "=&v"(pst), "=&v"(mq) : "v"(sv.x), "v"(sv.y), "v"(sv.z), "v"(sv.w), "v"(mp) : "memory");
            st4s(rs, mq, 0, pkv(a));
            SCHED();
            asm volatile("s_memrealtime %0" : "=s"(clk));
            VMW(3); SCHED();
            static_for<7, 13>(next);
            WT(7);
        }
    };
    do {
        turn(IC<0>{});
        turn(IC<1>{});
    } while (uu(fin) == 0u);
    if (sn[0] + 1 < NSEG || sn[1] + 1 < NSEG) st4s(rs, tid == 0 ? foff(F_ERR) : OOB, 0, 1u);
    if (w == 0) LST(1900 + j, rt32());
}

__global__ void __launch_bounds__(256, 1) k_main(Params p) {
    lptr lds = (lptr)(u32)16 - 16;
    if (blockIdx.x == 0) LST(256, rt32());
    unsigned long long pc; int len;
    asm volatile("s_getpc_b64 %0\n.Lk_main_here:\n\ts_mov_b32 %1, .Lk_end_here-.Lk_main_here" : "=s"(pc), "=s"(len));
    const rsrc_t rs = mk_rsrc(p.ws, W_END);
    const v2u e0 = ld8s(rs, threadIdx.x == 0 ? foff(F_EPOCH) : OOB, 0);
    const v4u code = __builtin_amdgcn_raw_buffer_load_b128(mk_rsrc((const void*)(pc & ~15ull), (u32)max(len, 0)), (((blockIdx.x >> 3) & 31u) * 256u + threadIdx.x) * 16u, 0, 0);
    CFENCE();
    auto conv_id = [](int role) {
        const int x = role >> 5, i = role & 31, no = x < 5 ? 5 : 10, og = x < 5 ? x - 3 : 2 * (x - 4) + (i >= 5), r = (x - 3) * 5 + i - no, q = r / 3;
        return x < 3 || i >= no + 5 || r >= 24 ? NCV : i < no ? ((og & 1) ? (og >> 1) * 5 + i % 5 : NCV) : r % 3 == 0 ? 20 + (q & 1) * 20 + (q >> 1) : 16 + (r % 3) * 8 + q;
    };
    int role = (int)(((u32)__builtin_amdgcn_s_getreg(20 | (3 << 11)) & 7u) * 32u + (blockIdx.x >> 3));
    Early e;
    v4u rv;
#pragma unroll 1
    for (int n = 0;; ++n) {
        conv_early(p, rs, conv_id(role), e);
        if (n) break;
        CFENCE();
        if (threadIdx.x == 0) {
            GAS unsigned long long* slots = (GAS unsigned long long*)(p.ws + W_CTRL + F_SLOT * 128);
            const unsigned long long tag = (unsigned long long)e0.y << 32 | (e0.x + 1u);
            u32 mine = (u32)role;
#pragma unroll 1
            for (int k = 0; k < 256 && __hip_atomic_exchange(slots + mine * 16, tag, __ATOMIC_RELAXED, __HIP_MEMORY_SCOPE_AGENT) == tag; ++k) mine = (mine + 1u) & 255u;
            lds_w16(lds, (v4u){mine, e0.x, e0.y, 0u});
        }
        BAR();
        rv = lds_r16(lds);
        BAR();
        if (uni((int)rv.x) == role) break;
        role = uni((int)rv.x);
    }
    const int x = role >> 5, i = role & 31, w = uni(threadIdx.x >> 6), no = x < 5 ? 5 : 10, nr = x < 7 ? 5 : 4, j = conv_id(role);
    const v2u ep = {uu(rv.y), uu(rv.z)};
    asm volatile("" :: "v"(code));
    if (x < 2) { role_head(p, lds, i >> 4, x * 16 + (i & 15), ep); __builtin_amdgcn_endpgm(); }
    if (x == 2) { role_wo(p, lds, i, ep); __builtin_amdgcn_endpgm(); }
    if (i >= no + nr) { role_qkv(p, lds, (x < 5 ? (x - 3) * 22 : 44 + (x - 5) * 17) + i - no - nr, ep); __builtin_amdgcn_endpgm(); }
    const bool cv = j >= NPRE || (j >= 20 && j < 24);
    if (j < NCV) conv_stage(p, rs, lds, j, (j & 3) * 2 + (j >= NPRE), cv ? 40 : 0, ep, e);
    if (i >= no) {
        if (!cv) role_row(p, lds, (j - 24) * 4 + w, ep);
        __builtin_amdgcn_endpgm();
    }
    const int og = x < 5 ? x - 3 : 2 * (x - 4) + (i >= 5);
    role_out(p, lds, og & 1, og >> 1, i % 5, ep);
}
__global__ void k_end() { asm volatile(".Lk_end_here:"); }

extern "C" void kernel_launch(void* const* d_in, const int* in_sizes, int n_in, void* d_out, int out_size, void* d_ws, size_t ws_size,
                              hipStream_t stream) {
    Params p;
    p.x = (const u16*)d_in[0]; p.mem0 = (const u16*)d_in[1]; p.Wq = (const u16*)d_in[2]; p.Wk = (const u16*)d_in[3];
    p.Wv = (const u16*)d_in[4]; p.Wo = (const u16*)d_in[5];
    p.bk = (const u16*)d_in[8]; p.bv = (const u16*)d_in[9]; p.shift = (const u16*)d_in[10]; p.ssmk = (const u16*)d_in[11]; p.ssmD = (const u16*)d_in[12];
    p.out = (u16*)d_out;
    p.ws = (unsigned char*)(((size_t)d_ws + 2 * MB - 1) & ~(size_t)(2 * MB - 1));
    (void)hipFuncSetAttribute((const void*)k_main, hipFuncAttributeMaxDynamicSharedMemorySize, LDS_BYTES);
    hipLaunchKernelGGL(k_main, dim3(256), dim3(256), LDS_BYTES, stream, p);
}
```

```cpp
#include <hip/hip_runtime.h>
#include <hip/hip_bf16.h>

typedef unsigned short u16;
typedef unsigned int u32;
typedef u32 v2u __attribute__((ext_vector_type(2)));
typedef u32 v4u __attribute__((ext_vector_type(4)));
typedef u32 v8u __attribute__((ext_vector_type(8)));
typedef float f32x2 __attribute__((ext_vector_type(2)));
typedef float f32x4 __attribute__((ext_vector_type(4)));
typedef float f32x16 __attribute__((ext_vector_type(16)));
typedef __bf16 bf16x2 __attribute__((ext_vector_type(2)));
typedef __bf16 bf16x8 __attribute__((ext_vector_type(8)));
typedef short s16x4 __attribute__((ext_vector_type(4)));
#define LAS __attribute__((address_space(3)))
#define GAS __attribute__((address_space(1)))
typedef LAS unsigned char* lptr;
#define DEV __device__ __forceinline__

constexpr int SEQ = 2048, DM = 640, NH = 32, DH = 128, NSEG = 64, NQ = NH * DH;

constexpr u32 MB = 1u << 20;
constexpr u32 W_ORING = 0;
constexpr u32 W_MRING = MB;
constexpr u32 W_PROBE = 2 * MB - 65536;
constexpr u32 W_CTRL = 4 * MB;
constexpr u32 CTRL_BYTES = 128 * 1024;
constexpr u32 ACT = SEQ * DM * 2;
constexpr u32 W_H1 = 5 * MB, W_Y = W_H1 + ACT, W_H2 = W_Y + ACT, W_X2 = W_H2 + ACT;
constexpr u32 W_KQ = 16 * MB;
constexpr u32 W_VO = W_KQ + 64 * MB;
constexpr u32 W_P = W_VO + 64 * MB;
constexpr u32 P_ITEM = 32 * 4 * DM * 4;
constexpr u32 W_KVT = W_P + 2 * NSEG * P_ITEM;
constexpr u32 W_SP = W_KVT + ACT;
constexpr u32 W_END = W_KVT + 3 * MB;
static_assert((size_t)W_END + 2 * MB <= 256ull * MB, "workspace");
constexpr u32 F_ERR = 0, F_QKV = 16, F_O = F_QKV + 192, F_P = F_O + 64, F_H2 = F_P + 64, F_CLR = F_H2 + 32,
              F_EPOCH = F_CLR + 32, F_SLOT = F_EPOCH + 1, F_PRE = F_SLOT + 256, F_N = F_PRE + 192;
constexpr int NCV = 44, NPRE = 40;
static_assert(F_N * 128 <= CTRL_BYTES, "ctrl");
DEV u32 foff(u32 idx) { return W_CTRL + idx * 128u; }

constexpr u32 OOB = 0x80000000u;
constexpr u32 PSENT = 0x7F817F81u;
constexpr int SPIN_CAP = 40000;
constexpr int LDS_BYTES = 160 * 1024;

#define LAD(cond, k, s_) do {} while (0)
#define LMX(cond, k, s_, v) do {} while (0)
#define LS3(cond, slot, idx) do {} while (0)
#define LST(idx, v) do {} while (0)
#define LS2(cond, idx) do {} while (0)
struct Params {
    const u16 *x, *mem0, *Wq, *Wk, *Wv, *Wo, *bk, *bv, *shift, *ssmk, *ssmD;
    u16* out;
    unsigned char* ws;
};

typedef __amdgpu_buffer_rsrc_t rsrc_t;
DEV rsrc_t mk_rsrc(const void* p, u32 bytes) { return __builtin_amdgcn_make_buffer_rsrc((void*)p, 0, bytes, 0x00020000); }
DEV v4u ld16s(rsrc_t r, u32 off, u32 so) { return __builtin_amdgcn_raw_buffer_load_b128(r, off, so, 16); }
DEV v2u ld8s(rsrc_t r, u32 off, u32 so) { return __builtin_amdgcn_raw_buffer_load_b64(r, off, so, 16); }
DEV v4u ldin16(rsrc_t r, u32 off, u32 so) { return __builtin_amdgcn_raw_buffer_load_b128(r, off, so, 2); }
DEV v2u ldin8(rsrc_t r, u32 off, u32 so) { return __builtin_amdgcn_raw_buffer_load_b64(r, off, so, 2); }
DEV u32 ld4(rsrc_t r, u32 off) { return __builtin_amdgcn_raw_buffer_load_b32(r, off, 0, 16); }
DEV void st16(rsrc_t r, u32 off, v4u v) { __builtin_amdgcn_raw_buffer_store_b128(v, r, off, 0, 16); }
DEV void st8s(rsrc_t r, u32 off, u32 so, v2u v) { __builtin_amdgcn_raw_buffer_store_b64(v, r, off, so, 16); }
DEV void st4s(rsrc_t r, u32 off, u32 so, u32 v) { __builtin_amdgcn_raw_buffer_store_b32(v, r, off, so, 16); }
#define DRAIN() asm volatile("s_waitcnt vmcnt(0)" ::: "memory")
#define VMW(n) asm volatile("s_waitcnt vmcnt(%0)" :: "i"(n) : "memory")
#define BAR() asm volatile("s_waitcnt lgkmcnt(0)\n\ts_barrier" ::: "memory")
#define SCHED() __builtin_amdgcn_sched_barrier(0)
#define CFENCE() asm volatile("" ::: "memory")
#define NOPS(n) asm volatile("s_nop %0" :: "i"(n))
#define PIN(x) asm volatile("" : "+v"(x))
#define PINS(x) asm volatile("" : "+s"(x))

template <int V> struct IC { static constexpr int value = V; };
template <int I, int N, class F> DEV void static_for(F&& f) { if constexpr (I < N) { f(IC<I>{}); static_for<I + 1, N>(f); } }
#define SFOR(var, n) static_for<0, n>([&](auto ic_) __attribute__((always_inline)) { constexpr int var = decltype(ic_)::value;
#define SEND });

DEV u32 pk(float a, float b) { bf16x2 v = {(__bf16)a, (__bf16)b}; return __builtin_bit_cast(u32, v); }
DEV float lo_f(u32 u) { return __builtin_bit_cast(float, u << 16); }
DEV float hi_f(u32 u) { return __builtin_bit_cast(float, u & 0xffff0000u); }
DEV f32x2 up2(u32 u) { return (f32x2){lo_f(u), hi_f(u)}; }
DEV u32 pk2(f32x2 v) { return pk(v.x, v.y); }
DEV float rbf(float a) { return (float)(__bf16)a; }
DEV v4u lds_r16(lptr p) { return *(LAS v4u*)p; }
DEV void lds_w16(lptr p, v4u v) { *(LAS v4u*)p = v; }
DEV v2u lds_tr(lptr p) { return __builtin_bit_cast(v2u, __builtin_amdgcn_ds_read_tr16_b64_v4i16((LAS s16x4*)p)); }
DEV v4u tr2(lptr p0, lptr p1) { const v2u a = lds_tr(p0), b = lds_tr(p1); return (v4u){a.x, a.y, b.x, b.y}; }
DEV v8u cat8(v4u a, v4u b) { return __builtin_shufflevector(a, b, 0, 1, 2, 3, 4, 5, 6, 7); }
DEV f32x4 mfma16(v4u a, v4u b, f32x4 c) { return __builtin_amdgcn_mfma_f32_16x16x32_bf16(__builtin_bit_cast(bf16x8, a), __builtin_bit_cast(bf16x8, b), c, 0, 0, 0); }
DEV f32x16 mfma32(v4u a, v4u b, f32x16 c) { return __builtin_amdgcn_mfma_f32_32x32x16_bf16(__builtin_bit_cast(bf16x8, a), __builtin_bit_cast(bf16x8, b), c, 0, 0, 0); }

DEV float f_(u32 u) { return __builtin_bit_cast(float, u); }
DEV u32 u_(float f) { return __builtin_bit_cast(u32, f); }
template <int CTRL> DEV float dpp(float v) { return f_((u32)__builtin_amdgcn_update_dpp(0, (int)u_(v), CTRL, 0xf, 0xf, false)); }
DEV float max16(float v) { auto r = __builtin_amdgcn_permlane16_swap(u_(v), u_(v), false, false); return fmaxf(f_(r[0]), f_(r[1])); }
DEV float max32(float v) { auto r = __builtin_amdgcn_permlane32_swap(u_(v), u_(v), false, false); return fmaxf(f_(r[0]), f_(r[1])); }
DEV float add16(float v) { auto r = __builtin_amdgcn_permlane16_swap(u_(v), u_(v), false, false); return f_(r[0]) + f_(r[1]); }
DEV float add32(float v) { auto r = __builtin_amdgcn_permlane32_swap(u_(v), u_(v), false, false); return f_(r[0]) + f_(r[1]); }
DEV float wave_sum(float v) {
    v += dpp<0xB1>(v);
    v += dpp<0x4E>(v);
    v += dpp<0x124>(v);
    v += dpp<0x128>(v);
    return add32(add16(v));
}
DEV bool all_(bool c) { return __builtin_amdgcn_ballot_w64(!c) == 0ull; }
DEV int uni(int v) { return __builtin_amdgcn_readfirstlane(v); }
DEV u32 uu(u32 v) { return (u32)__builtin_amdgcn_readfirstlane((int)v); }
DEV u32 rdl(u32 v, int l) { return (u32)__builtin_amdgcn_readlane((int)v, l); }
DEV u32 rt32() { return (u32)__builtin_amdgcn_s_memrealtime(); }

#define WSL 2
DEV void wait_flags(rsrc_t rs, u32 off, u32 want, bool& dead) {
    if (dead) return;
#pragma unroll 1
    for (int n = 0;; ++n) {
        CFENCE();
        const u32 v = ld4(rs, off);
        if (all_(off >= OOB || v >= want)) break;
        __builtin_amdgcn_s_sleep(WSL);
        if ((n & 63) == 63) { if (uni((int)ld4(rs, foff(F_ERR))) != 0) { dead = true; break; } }
        if (n > SPIN_CAP) { st4s(rs, foff(F_ERR), 0, 1u); dead = true; break; }
    }
    CFENCE();
}

DEV void pre_post(rsrc_t rs, int idx, u32 stage, v2u ep) { st8s(rs, threadIdx.x == 0 ? foff(F_PRE + idx) : OOB, 0, (v2u){ep.x + stage, ep.y}); }
DEV void pre_poll(rsrc_t rs, int first, int n, u32 stage, v2u ep) {
    const int lane = threadIdx.x & 63;
#pragma unroll 1
    for (int it = 0; it < SPIN_CAP; ++it) {
        CFENCE();
        const v2u a = ld8s(rs, foff(F_PRE + first + min(lane, n - 1)), 0), b = ld8s(rs, foff(F_PRE + first + min(lane + 64, n - 1)), 0);
        if (all_(a.y == ep.y && b.y == ep.y && max(a.x - ep.x - stage, b.x - ep.x - stage) < 8u - stage)) break;
        __builtin_amdgcn_s_sleep(2);
    }
    CFENCE();
}
DEV void pre_wait(rsrc_t rs, int first, int n, u32 stage, v2u ep) {
    if (uni(threadIdx.x >> 6) == 0) pre_poll(rs, first, n, stage, ep);
    BAR();
}
DEV u32 pol_wait(rsrc_t rs, v2u ep) {
    const int lane = threadIdx.x & 63;
    v4u a;
#pragma unroll 1
    for (int it = 0; it < SPIN_CAP; ++it) {
        CFENCE();
        a = ld16s(rs, foff(F_PRE + (lane < NPRE || lane >= 48 ? lane : 0)), 0);
        if (all_(a.y == ep.y && a.x - ep.x - 1u < 7u)) break;
        __builtin_amdgcn_s_sleep(2);
    }
    CFENCE();
    return uu(wave_sum(lane >= 48 ? (float)(int)a.z : 0.f) >= 0.f ? 1u : 0u);
}
#define PROBE_T 1000
#define NPROBE 32
DEV u32 pol_probe(const Params& p, rsrc_t rs, int id) {
    const int lane = threadIdx.x & 63;
    const u32 off0 = W_PROBE + (u32)(id >> 2) * 16384u + (u32)(id & 3) * 128u;
    const u32 tend = rt32() + PROBE_T;
    st4s(rs, lane < 32 ? off0 + (u32)lane * 512 : OOB, 0, 0u); DRAIN();
    int calm = 0;
    int d[2] = {1 << 16, 1 << 16};
#pragma unroll 1
    for (int q = 0; q < NPROBE && ((q & 1) || (calm < 6 && (int)(rt32() - tend) < 0)); ++q) {
        const u32 half = (u32)(q ^ (q >> 1) ^ id) & 1u;
        const unsigned long long t0 = __builtin_amdgcn_s_memtime();
        const u32 v = ld4(rs, lane == 0 ? off0 + half * 8192 + (u32)(q >> 1) * 512 : OOB);
        asm volatile("s_waitcnt vmcnt(0)" :: "v"(v) : "memory");
        const int dt = (int)(__builtin_amdgcn_s_memtime() - t0);
        if (half) d[1] = min(d[1], dt); else d[0] = min(d[0], dt);
        calm += dt < 1100;
    }
    const u32 xcc = (u32)__builtin_amdgcn_s_getreg(20 | (3 << 11)) & 0xfu;
    return (u32)min(max((d[0] - d[1]) * ((xcc & 4u) ? -1 : 1), -250), 250);
}
DEV f32x2 ln_stats(const f32x2 (&x)[5]) {
    const float c = f_(uu(u_(x[0].x)));
    f32x2 d[5];
#pragma unroll
    for (int i = 0; i < 5; ++i) d[i] = x[i] - c;
    const f32x2 s2 = (d[0] + d[1]) + (d[2] + d[3]) + d[4], q2 = (d[0] * d[0] + d[1] * d[1]) + (d[2] * d[2] + d[3] * d[3] + d[4] * d[4]);
    float a = s2.x + s2.y, b = q2.x + q2.y, a2, b2;
#define LN_DPP(m) "v_add_f32_dpp %0, %0, %0 " m " row_mask:0xf bank_mask:0xf\n\tv_add_f32_dpp %1, %1, %1 " m " row_mask:0xf bank_mask:0xf\n\ts_nop 0\n\t"
    asm("s_nop 1\n\t" LN_DPP("quad_perm:[1,0,3,2]") LN_DPP("quad_perm:[2,3,0,1]") LN_DPP("row_ror:4")
        "v_add_f32_dpp %0, %0, %0 row_ror:8 row_mask:0xf bank_mask:0xf\n\tv_add_f32_dpp %1, %1, %1 row_ror:8 row_mask:0xf bank_mask:0xf\n\ts_nop 1\n\t"
        "v_permlane16_swap_b32 %0, %1\n\tv_add_f32 %0, %0, %1\n\tv_mov_b32 %1, %0\n\ts_nop 1\n\tv_permlane32_swap_b32 %0, %1\n\tv_add_f32 %0, %0, %1\n\ts_nop 0\n\t"
        "v_readlane_b32 %2, %0, 0\n\tv_readlane_b32 %3, %0, 16\n\ts_nop 1"
        : "+v"(a), "+v"(b), "=s"(a2), "=s"(b2));
    const float m = a2 * (1.0f / DM), var = fmaxf(b2 * (1.0f / DM) - m * m, 0.f);
    const float sd = rbf(__builtin_amdgcn_sqrtf(rbf(rbf(var) + 1e-5f)));
    return (f32x2){rbf(c + m), __builtin_amdgcn_rcpf(sd)};
}
DEV u32 pkv(f32x2 v) { u32 d; asm("v_cvt_pk_bf16_f32 %0, %1, %2" : "=v"(d) : "v"(v.x), "v"(v.y)); return d; }
DEV void store_h(rsrc_t rs, u32 base, int row, const u32 (&hp)[5]) {
    const u32 lane = threadIdx.x & 63;
    const u32 sw = (u32)(row & 15) * 16u, o8 = (lane >> 5) * 8192u + (((lane & 31u) * 8u) ^ sw), hb = base + (u32)(row >> 5) * (32 * DM * 2) + (u32)(row & 31) * 256u;
    st8s(rs, o8, hb, (v2u){hp[0], hp[1]});
    st8s(rs, o8, hb + 16384, (v2u){hp[2], hp[3]});
    st4s(rs, (lane * 4) ^ sw, hb + 32768, hp[4]);
}

#define DMA16(ldsb, vo, so) asm volatile("s_mov_b32 m0, %0\n\ts_nop 0\n\tbuffer_load_dwordx4 %1, %2, %3 offen sc1 lds" :: "s"(ldsb), "v"(vo), "s"(srd), "s"(so) : "memory")
#define DMA16I(ldsb, lo, vo, so, go) do { u32 so_; asm volatile("s_add_u32 m0, %1, %5\n\ts_add_u32 %0, %4, %6\n\tbuffer_load_dwordx4 %2, %3, %0 offen sc1 lds" \
                         : "=&s"(so_) : "s"(ldsb), "v"(vo), "s"(srd), "s"(so), "i"(lo), "i"(go) : "memory", "scc"); } while (0)
#define DMA32(ldsb, vo, so) asm volatile("s_mov_b32 m0, %0\n\ts_nop 0\n\tbuffer_load_dwordx4 %1, %2, %3 offen sc1 lds\n\tbuffer_load_dwordx4 %1, %2, %3 offen offset:1024 sc1 lds" \
                         :: "s"(ldsb), "v"(vo), "s"(srd), "s"(so) : "memory")
#define DMAIN(sr, ldsb, vo, so) asm volatile("s_mov_b32 m0, %0\n\ts_nop 0\n\tbuffer_load_dwordx4 %1, %2, %3 offen lds" :: "s"(ldsb), "v"(vo), "s"(sr), "s"(so) : "memory")
DEV v4u mk_srd(const void* q, u32 bytes) { return (v4u){uu((u32)(size_t)q), uu((u32)((size_t)q >> 32) & 0xffffu), bytes, 0x00020000u}; }
#define DMANT(sr, ldsb, vo, so) asm volatile("s_mov_b32 m0, %0\n\ts_nop 0\n\tbuffer_load_dwordx4 %1, %2, %3 offen nt lds" :: "s"(ldsb), "v"(vo), "s"(sr), "s"(so) : "memory")
#define AHEAD(sr, vo, so) asm volatile("buffer_load_dwordx4 %0, %1, %2, %3 offen" : "+v"(junk) : "v"(vo), "s"(sr), "s"(so) : "memory")
#define MK_SRD() const v4u srd = mk_srd(p.ws, W_END)

constexpr int CV_W = 40960;
template <bool B> struct CvL {
    static constexpr int NF = B ? 5 : 3, NK = B ? 4 : 2, KP = 544 + NK * 256, CH = NF * 1024 + 4 * KP;
};
constexpr int CV_Y = 4 * CvL<false>::CH, CV_YP = 272;
static_assert(CV_Y + 32 * CV_YP <= CV_W && 2 * CvL<true>::CH <= CV_Y, "conv lds");
DEV int cv_chan(int u, int i) { return 16 * (u % 40) + 4 * i + u / 40; }
template <bool B> DEV void cv_filt(const Params& p, u32 dst, int d, int i) {
    const u32 lane = threadIdx.x & 63;
    const v4u sf = mk_srd(p.ssmk + d * SEQ, SEQ * 2);
    if (i == 0) DMANT(sf, dst, lane < 4u ? OOB : lane * 16u - 64u, 0);
    else DMANT(sf, uu(dst + i * 1024), i == CvL<B>::NF - 1 && lane >= 4u ? OOB : lane * 16u, i * 1024 - 64);
}
DEV v4u cv_kvld(rsrc_t rs, int d, int i) { return ld16s(rs, (threadIdx.x & 63) * 16u, uu(W_KVT + (u32)d * (SEQ * 2) + i * 1024)); }
template <bool B> DEV void cv_kvput(lptr F, const v4u* k) {
    const int lane = threadIdx.x & 63;
#pragma unroll
    for (int i = 0; i < CvL<B>::NK; ++i) lds_w16(F + CvL<B>::NF * 1024 + (lane & 3) * CvL<B>::KP + 512 + i * 256 + (lane >> 2) * 16, k[i]);
}
template <bool B> DEV void cv_zero(lptr F) {
    const int lane = threadIdx.x & 63;
#pragma unroll
    for (int i = 0; i < 4; ++i) lds_w16(F + (lane >> 5) * CvL<B>::CH + CvL<B>::NF * 1024 + i * CvL<B>::KP + (lane & 31) * 16, (v4u){0u, 0u, 0u, 0u});
}
#define M32V(d, a, b) asm volatile("v_mfma_f32_32x32x16_bf16 %0, %1, %2, %0" : "+v"(d) : "v"(a), "v"(b))
template <bool B, class G> DEV void cv_run(const Params& p, rsrc_t rs, lptr F, int d, u32 D, f32x4 sp, float (&y)[16], G&& gap) {
    constexpr int KP = CvL<B>::KP, FO = B ? 2048 : 0;
    const int lane = threadIdx.x & 63, n = lane & 31, hh = lane >> 5, c = lane & 3;
    lptr fa = F + 64 + (32 * hh + 16 * (c & 1) + 8 * ((lane >> 4) & 1) + 4 * (c >> 1) - (lane & 12)) * 2;
    lptr kl = F + CvL<B>::NF * 1024 + (32 + n - hh) * 16;
    v4u dg;
#pragma unroll
    for (int q = 0; q < 4; ++q) { const int k = (lane & 35) ? -1 : (n & 4) + (n >> 3); dg[q] = k == 2 * q ? D : k == 2 * q + 1 ? D << 16 : 0u; }
    v4u l[4], h[4]; v2u fc[4], fh[4];
    f32x16 acc[4];
#pragma unroll
    for (int j = 0; j < 4; ++j) { l[j] = lds_r16(kl + j * KP); if constexpr (B) h[j] = lds_r16(kl + j * KP + 512); }
#pragma unroll
    for (int j = 0; j < 2; ++j) { fc[j] = lds_tr(fa + FO - 32 * j); if constexpr (B) fh[j] = lds_tr(fa - 32 * j); }
    asm volatile("s_nop 1" : "+v"(dg));
#pragma unroll
    for (int j = 0; j < 4; ++j) asm volatile("v_mfma_f32_32x32x16_bf16 %0, %1, %2, 0" : "=&v"(acc[j]) : "v"(dg), "v"(B ? h[j] : l[j]));
    constexpr int U = B ? 2 : 4;
    kl -= 32 * U; fa -= 32;
    auto body = [&](bool first) __attribute__((always_inline)) {
#pragma unroll
        for (int z = 0; z < U; ++z) {
            const int c = 2 * (z & 1);
            const v4u f = __builtin_shufflevector(fc[c], fc[c + 1], 0, 1, 2, 3), g = __builtin_shufflevector(fh[c], fh[c + 1], 0, 1, 2, 3);
#pragma unroll
            for (int j = 0; j < 4; ++j) {
                M32V(acc[j], f, l[j]); l[j] = lds_r16(kl + j * KP + 32 * (U - z - 1));
                if (j < 2) fc[(c ^ 2) + j] = lds_tr(fa + FO + (z + 1) * 128 + 32 - 32 * j);
                if (first && j > 0) gap(5 * z + j - 1);
                SCHED();
            }
            if constexpr (B) {
#pragma unroll
                for (int j = 0; j < 4; ++j) {
                    M32V(acc[j], g, h[j]); h[j] = lds_r16(kl + j * KP + 512 + 32 * (U - z - 1));
                    if (j < 2) fh[(c ^ 2) + j] = lds_tr(fa + (z + 1) * 128 + 32 - 32 * j);
                    if (first && j > 1) gap(5 * z + j + 1);
                    SCHED();
                }
            }
        }
        kl -= 32 * U; fa += 128 * U;
    };
    if constexpr (B) body(true);
#pragma unroll 1
    for (int m = B ? 2 * U : 0; m < 32; m += 2 * U) body(false);
    asm volatile("s_nop 11" : "+v"(acc[0]), "+v"(acc[1]), "+v"(acc[2]), "+v"(acc[3]));
    const float o[3] = {acc[1][15] + acc[2][14] + acc[3][13], acc[2][15] + acc[3][14], acc[3][15]};
    if constexpr (!B) st16(rs, lane == 63 ? W_SP + (u32)d * 16u : OOB, (v4u){u_(o[0]), u_(o[1]), u_(o[2]), 0u});
#pragma unroll
    for (int i = 0; i < 16; ++i) {
        float v = acc[0][i];
#pragma unroll
        for (int j = 1; j < 4; ++j) if (j <= i) v += acc[j][i - j];
        if (i < 3) { const float e = f_((u32)__builtin_amdgcn_ds_bpermute((hh ? lane - 32 : lane + 31) * 4, (int)u_(o[i]))); v += lane == 0 ? sp[i] : e; }
        y[i] = v;
    }
}
DEV void cv_put(lptr F, int pr, const float (&y0)[16], const float (&y1)[16]) {
    const int lane = threadIdx.x & 63;
#pragma unroll
    for (int i = 0; i < 16; ++i) *(LAS u32*)(F + CV_Y + (lane & 31) * CV_YP + (lane >> 5) * 128 + i * 8 + pr * 4) = pk(y0[i], y1[i]);
}
DEV void cv_out(rsrc_t rs, lptr F, int u, int blk0) {
    const int lane = threadIdx.x & 63;
    lptr q = F + CV_Y + ((lane >> 2) & 3) * CV_YP + ((lane >> 4) * 4 + (lane & 3)) * 8;
    const u32 yo = W_Y + (u32)(((lane >> 2) * 8 + blk0) * DM + 4 * u + (lane & 3)) * 16u;
#pragma unroll
    for (int i = 0; i < 8; ++i) { lptr a = q + (i >> 1) * (8 * CV_YP) + (i & 1) * 128; st16(rs, yo + (u32)((i & 1) * 128 + (i >> 1)) * (DM * 16), tr2(a, a + 4 * CV_YP)); }
}
struct KvIn { v2u k[19], v[16]; v4u s[2]; };
template <class F> DEV void kvt_load(const Params& p, int tile, KvIn& in, F&& mid) {
    const int lane = threadIdx.x & 63;
    const rsrc_t rk = mk_rsrc(p.bk, ACT), rv = mk_rsrc(p.bv, ACT), rh = mk_rsrc(p.shift, DM * 8);
    const int row = (tile / 10) * 64 + (lane >> 4) * 16, col = (tile % 10) * 64 + (lane & 15) * 4;
    const u32 vo = (u32)(row * DM + col) * 2u, vp = row > 0 ? vo - 3 * (DM * 2) : OOB;
#pragma unroll
    for (int i = 0; i < 2; ++i) in.s[i] = ldin16(rh, (u32)col * 8u + i * 16, 0);
#pragma unroll
    for (int i = 0; i < 19; ++i) { if (i == 7) mid(); in.k[i] = i < 3 ? ldin8(rk, vp, i * (DM * 2)) : ldin8(rk, vo, (i - 3) * (DM * 2)); if (i >= 3) in.v[i - 3] = ldin8(rv, vo, (i - 3) * (DM * 2)); }
}
DEV void kvt_make(rsrc_t rs, lptr xt, int tile, const KvIn& in) {
    const int lane = threadIdx.x & 63;
#pragma unroll
    for (int c = 0; c < 2; ++c) {
        const v4u sv = in.s[c];
        const f32x2 sh[4] = {{lo_f(sv.x), lo_f(sv.z)}, {hi_f(sv.x), hi_f(sv.z)}, {lo_f(sv.y), lo_f(sv.w)}, {hi_f(sv.y), hi_f(sv.w)}};
        f32x2 kf[19];
#pragma unroll
        for (int i = 0; i < 19; ++i) kf[i] = up2(in.k[i][c]);
        u32 kv[16];
#pragma unroll
        for (int i = 0; i < 16; ++i) kv[i] = pk2(((kf[i] * sh[3] + kf[i + 1] * sh[2]) + (kf[i + 2] * sh[1] + kf[i + 3] * sh[0])) * up2(in.v[i][c]));
#pragma unroll
        for (int q = 0; q < 8; ++q) {
            const u32 sel = (q & 1) ? 0x07060302u : 0x05040100u; const u32* z = kv + (q >> 1);
            *(LAS v2u*)(xt + ((2 * c + (q & 1)) * 16 + (lane & 15)) * 144 + (lane >> 5) * 64 + (q >> 1) * 16 + ((lane >> 4) & 1) * 8) =
                (v2u){__builtin_amdgcn_perm(z[4], z[0], sel), __builtin_amdgcn_perm(z[12], z[8], sel)};
        }
    }
    const u32 o = W_KVT + (u32)(((tile % 10) * 64 + 4 * (lane >> 3)) * SEQ + (tile / 10) * 64 + (lane & 7) * 8) * 2u;
#pragma unroll
    for (int i = 0; i < 8; ++i) st16(rs, o + (u32)(32 * (i & 1) + (i >> 1)) * (SEQ * 2), lds_r16(xt + (8 * i + (lane >> 3)) * 144 + (lane & 7) * 16));
}
struct RowX { v2u a, b; u32 c; };
DEV void ln_row(rsrc_t rs, int row, const RowX& r) {
    const f32x2 x[5] = {up2(r.a.x), up2(r.a.y), up2(r.b.x), up2(r.b.y), up2(r.c)};
    const f32x2 st = ln_stats(x);
    u32 hp[5];
#pragma unroll
    for (int q = 0; q < 5; ++q) hp[q] = pk2(up2(pkv(x[q] - st.x)) * st.y);
    store_h(rs, W_H1, row, hp);
}
DEV v4u mem0_load(const Params& p, int j) {
    const u32 gt = (u32)(j * 256 + threadIdx.x) * 16u, n = ((gt >> 14) << 9) + ((gt & 8191u) >> 4);
    return ldin16(mk_rsrc(p.mem0, 8 * DM * 2), n < 640u ? ((2 * ((n >> 2) & 3) + (n & 1)) * DM + (n >> 4) * 16 + (n & 2) * 4) * 2u : OOB, 0);
}
DEV void pre_rings(rsrc_t rs, int j, v4u m) {
    const u32 gi = (u32)(j * 256 + threadIdx.x), gt = gi * 16u, R = NPRE * 4096, n = ((gt >> 14) << 9) + ((gt & 8191u) >> 4);
#pragma unroll
    for (int i = 0; i < 4; ++i) m[i] = n >= 640u ? PSENT : m[i] == PSENT ? 0x7FC17FC1u : m[i];
    st4s(rs, gi - 36u * 256u < F_CLR ? foff(gi - 36u * 256u) : OOB, 0, 0u);
#pragma unroll 1
    for (u32 o = gt; o < MB; o += R) st16(rs, W_ORING + o, (v4u){PSENT, PSENT, PSENT, PSENT});
    const u32 mo = gt < 4 * 32768 + 16384 ? W_MRING + gt : OOB;
    st16(rs, mo, m);
    st16(rs, mo + MB / 4, m);
    static_assert(NPRE * 4096 >= 4 * 32768 + 16384, "memory ring");
}
constexpr int KT0 = NPRE * 4, LN0 = NPRE * 8;
struct Early { RowX r[2]; v4u m; u32 vote; };
DEV void conv_early(const Params& p, rsrc_t rs, int j, Early& e) {
    const int lane = threadIdx.x & 63, w = uni(threadIdx.x >> 6), u = j * 4 + w;
    if (j < NPRE) {
        const rsrc_t rx = mk_rsrc(p.x, ACT);
#pragma unroll
        for (int i = 0; i < 2; ++i) {
            const u32 so = uu((u + i * KT0) * (DM * 2));
            e.r[i].a = __builtin_amdgcn_raw_buffer_load_b64(rx, lane * 8u, so, 0); e.r[i].b = __builtin_amdgcn_raw_buffer_load_b64(rx, lane * 8u + 512u, so, 0);
            e.r[i].c = __builtin_amdgcn_raw_buffer_load_b32(rx, lane * 4u + 1024u, so, 0);
        }
        e.m = mem0_load(p, j);
    } else if (j < NCV && w > 0) e.vote = pol_probe(p, rs, u - NPRE * 4);
}
DEV void conv_stage(const Params& p, rsrc_t rs, lptr lds, int j, int g0, int ge, v2u ep, Early& e) {
    const int tid = threadIdx.x, lane = tid & 63, w = uni(tid >> 6), u = j * 4 + w;
    lptr F = lds + w * CV_W;
    const u32 fb = uu(w * CV_W);
    const rsrc_t rd = mk_rsrc(p.ssmD, DM * 2);
    float y0[16], y1[16];
    KvIn in;
    if (j < NPRE) {
        constexpr int CH = CvL<false>::CH;
        if (w == 1) LST(1500 + j, rt32());
        pre_rings(rs, j, e.m);
        ln_row(rs, u, e.r[0]); ln_row(rs, u + KT0, e.r[1]);
        if (w == 1) LST(1550 + j, rt32());
        CFENCE();
        kvt_load(p, u, in, [&] { VMW(13); BAR(); pre_post(rs, j, 1u, ep); if (w == 0) LST(1450 + j, rt32()); });
        if (j == 20) LST(260, rt32());
        const u32 D = __builtin_amdgcn_raw_buffer_load_b16(rd, (u32)cv_chan(u, lane & 3) * 2u, 0, 0);
#pragma unroll 1
        for (int i = 0; i < 4; ++i) { const int d = cv_chan(u, i); cv_filt<false>(p, fb + i * CH, d, 0); cv_filt<false>(p, fb + i * CH, d, 1); cv_filt<false>(p, fb + i * CH, d, 2); }
        kvt_make(rs, F + CV_Y, u, in);
        cv_zero<false>(F); cv_zero<false>(F + 2 * CH);
        DRAIN(); BAR();
        pre_post(rs, j, 2u, ep);
        if (j == 20) LST(262, rt32());
        pre_wait(rs, 0, NPRE, 2u, ep);
        if (j == 20) LST(281, rt32());
        {
            v4u k[8];
#pragma unroll
            for (int i = 0; i < 8; ++i) k[i] = cv_kvld(rs, cv_chan(u, i >> 1), i & 1);
#pragma unroll
            for (int i = 0; i < 4; ++i) cv_kvput<false>(F + i * CH, k + 2 * i);
        }
        if (j == 20) LST(286, rt32());
#pragma unroll 1
        for (int i = 0; i < 4; i += 2) {
            cv_run<false>(p, rs, F + i * CH, cv_chan(u, i), rdl(D, i), f32x4{}, y0, [](int) {});
            cv_run<false>(p, rs, F + (i + 1) * CH, cv_chan(u, i + 1), rdl(D, i + 1), f32x4{}, y1, [](int) {});
            cv_put(F, i >> 1, y0, y1);
        }
        if (j == 20) LST(287, rt32());
        cv_out(rs, F, u, 0);
        if (j == 20) LST(263, rt32());
    } else {
        st16(rs, lane == 0 ? foff(F_PRE + 48 + u - NPRE * 4) : OOB, (v4u){ep.x + 1u, ep.y, w > 0 ? e.vote : 0u, 0u});
#pragma unroll 1
        for (int t = u + 128; t < 320; t += 16) { kvt_load(p, t, in, [] {}); kvt_make(rs, F, t, in); }
    }
    DRAIN(); BAR();
    if (g0 < ge) {
        constexpr int CH = CvL<true>::CH;
        pre_post(rs, j, 3u, ep);
        cv_zero<true>(F);
        if (w == 0) { pre_poll(rs, 64, 32, 3u, ep); pre_poll(rs, 0, NCV, 3u, ep); }
        BAR();
        v4u k[4];
        auto fetch = [&](u32 dst, int d, int i) { if (i < 5) cv_filt<true>(p, dst, d, i); else if (i < 9) k[i - 5] = cv_kvld(rs, d, i - 5); };
#pragma unroll
        for (int i = 0; i < 9; ++i) fetch(fb, cv_chan(g0 * 4 + w, 0), i);
        const u32 ch = (u32)cv_chan(min((g0 + 8 * (lane >> 2)) * 4 + w, 159), lane & 3);
        u32 D = __builtin_amdgcn_raw_buffer_load_b16(rd, ch * 2u, 0, 0);
        v4u sp = ld16s(rs, W_SP + ch * 16u, 0);
        asm volatile("" : "+v"(D), "+v"(sp));
#pragma unroll 1
        for (int g = g0, n = 0; g < ge; g += 8) {
            const int un = g * 4 + w;
            if (g == 16) { LST(282, rt32()); LST(288, __builtin_amdgcn_s_memtime()); }
#pragma unroll 1
            for (int i = 0; i < 4; i += 2, n += 2) {
                const int d1 = (int)rdl(ch, n + 1), d2 = (int)rdl(ch, n + 2);
                cv_kvput<true>(F, k);
                cv_run<true>(p, rs, F, 0, rdl(D, n), (f32x4){f_(rdl(sp.x, n)), f_(rdl(sp.y, n)), f_(rdl(sp.z, n)), 0.f}, y0, [&](int q) { fetch(fb + CH, d1, q); });
                cv_kvput<true>(F + CH, k);
                cv_run<true>(p, rs, F + CH, 326, rdl(D, n + 1), (f32x4){f_(rdl(sp.x, n + 1)), f_(rdl(sp.y, n + 1)), f_(rdl(sp.z, n + 1)), 0.f}, y1, [&](int q) { fetch(fb, d2, q); });
                cv_put(F, i >> 1, y0, y1);
            }
            cv_out(rs, F, un, 4);
            if (g == 16) { LST(283, rt32()); LST(289, __builtin_amdgcn_s_memtime()); }
        }
        DRAIN(); BAR();
    }
    pre_post(rs, j, 5u, ep);
    if (j == 20) LST(284, rt32());
    if (j == 3) LST(285, rt32());
}

#define M32A(first, d, a, b) do { if (first) asm volatile("v_mfma_f32_32x32x16_bf16 %0, %1, %2, 0" : "=&v"(d) : "a"(a), "v"(b)); \
                                  else asm volatile("v_mfma_f32_32x32x16_bf16 %0, %1, %2, %0" : "+v"(d) : "a"(a), "v"(b)); } while (0)
#define M32B(first, d, a, b) do { if (first) asm volatile("v_mfma_f32_32x32x16_bf16 %0, %1, %2, 0" : "=&v"(d) : "v"(a), "a"(b)); \
                                  else asm volatile("v_mfma_f32_32x32x16_bf16 %0, %1, %2, %0" : "+v"(d) : "v"(a), "a"(b)); } while (0)
#define VCVT(d, a, b) asm volatile("v_cvt_pk_bf16_f32 %0, %1, %2" : "=v"(d) : "v"(a), "v"(b))
#define VADD(d, a, b) asm volatile("v_add_f32 %0, %1, %2" : "=v"(d) : "v"(a), "v"(b))
#define LD4A(d, vo, so) asm volatile("buffer_load_dword %0, %1, %2, %3 offen sc1" : "=v"(d) : "v"(vo), "s"(srd), "s"(so) : "memory")
DEV u32 wst_src(int lane, int ld) { return (u32)((lane >> 4) * ld + 8 * ((lane & 15) ^ ((lane >> 4) << 2))) * 2u; }
DEV u32 wst_rd(int lane, int w) { const int q4 = (lane >> 2) & 3; return (u32)(q4 * 256 + ((w ^ q4) * 64) + ((lane >> 4) & 1) * 32 + (lane & 3) * 8); }

constexpr int QK_SEL = 81920;
#define QK_LEAD 12
#define QK_SMP 35
#define QK_YB 22
#define QK_W0 2
DEV void role_qkv(const Params& p, lptr lds, int c, v2u ep) {
    const int tid = threadIdx.x, lane = tid & 63, w = uni(tid >> 6), hh = lane >> 5, r32 = lane & 31;
    const int m = c >> 5, h = c & 31;
    const rsrc_t rs = mk_rsrc(p.ws, W_END);
    v4u wt[40];
    {
        const v4u sw = mk_srd(m == 0 ? p.Wq : m == 1 ? p.Wk : p.Wv, DM * NQ * 2), sx = mk_srd(p.x, ACT);
        const u32 vo = wst_src(lane, NQ) + (u32)h * (DH * 2), x8 = (u32)lane * 8u, x4 = (u32)lane * 4u;
        RowX xr[5];
        auto wfill = [&](int i0, int n) {
#pragma unroll 1
            for (int i = i0; i < i0 + n; ++i) DMAIN(sw, uu((4 * i + w) * 1024), vo, uu((16 * i + 4 * w) * (NQ * 2)));
        };
        wfill(0, QK_W0);
#pragma unroll
        for (int i = 0; i < 5; ++i)
            asm volatile("buffer_load_dwordx2 %0, %3, %5, %6 offen\n\tbuffer_load_dwordx2 %1, %3, %5, %6 offen offset:512\n\tbuffer_load_dword %2, %4, %5, %6 offen offset:1024"
                         : "=&v"(xr[i].a), "=&v"(xr[i].b), "=&v"(xr[i].c) : "v"(x8), "v"(x4), "s"(sx), "s"(uu((LN0 + 4 * c + w + 384 * i) * (DM * 2))) : "memory");
        wfill(QK_W0, 10 - QK_W0);
        if (c == 3) LST(275, rt32());
        VMW(10 - QK_W0);
        if (c == 3) LST(276, rt32());
#pragma unroll
        for (int i = 0; i < 5; ++i) {
            asm volatile("" : "+v"(xr[i].a), "+v"(xr[i].b), "+v"(xr[i].c));
            const int row = LN0 + 4 * c + w + 384 * i;
            if (i < 4 || row < SEQ) ln_row(rs, row, xr[i]);
            wfill(10 + 6 * i, 6);
        }
        if (c == 3) LST(264, rt32());
        lptr tb = lds + hh * 2048 + wst_rd(lane, w);
        SFOR(k, 40)
            if constexpr (k == 0) { VMW(15); BAR(); }
            if constexpr (k == 28) {
                if (w == 0) pre_poll(rs, 0, NPRE, 1u, ep);
                DRAIN(); BAR();
                pre_post(rs, 96 + c, 1u, ep);
            }
            lptr q = tb + (k >> 3) * 32768 + (k & 7) * 4096; wt[k] = tr2(q, q + 1024); asm volatile("" : "+a"(wt[k]));
        SEND
    }
    if (c == 3) LST(268, rt32());
    if (w == 0) LST(1350 + c, rt32());
    bool dead = false;
    int n0, n1;
    const u32 t16 = (u32)(w * 2048 + lane * 16), wb = uu(w * 2048);
    u32 ar[16];
#pragma unroll
    for (int j = 0; j < 16; ++j) ar[j] = (u32)(r32 * 256 + ((2 * (j & 7) + hh) ^ (r32 & 15)) * 16 + (j >> 3) * 49152);
    MK_SRD();
#define XRD(y, ks) lds_r16(lds + ar[((ks) & 7) + ((ks) >= 24 ? 8 : 0)] + ((ks) >> 3) % 3 * 16384 + (y) * 8192)
#define QM0(y, b) asm volatile("s_add_u32 m0, %0, %1" :: "s"(wb), "i"((b) * 16384 + (y) * 8192) : "scc", "memory")
#define QDMA(i, vo) asm volatile("buffer_load_dwordx4 %0, %1, %2 offen offset:%3 sc1 lds" :: "v"(vo), "s"(srd), "s"(((i) >> 1) * 8192), "i"(((i) & 1) * 1024) : "memory")
    const u32 h2flag = (lane < 32 && w == 0) ? foff(F_H2 + lane) : OOB;
    const u32 ol = (m == 2 ? (u32)(2 * w + (r32 >> 4)) * 1024u : (u32)((r32 >> 4) * 4 + w) * 1024u) + (u32)(hh * 16 + (r32 & 15)) * 16u
                 + (m == 2 ? W_VO : m == 1 ? W_KQ : W_KQ + 8192u) + (u32)h * 16384u;
    const u32 fo = tid < 2 ? foff(F_QKV + tid * 96 + c) : OOB;
    lptr selw = lds + QK_SEL + (w == 0 ? 0 : 64), selr = lds + QK_SEL;
    PIN(selw);
    static_assert(QK_LEAD > 8 && QK_LEAD % 2 == 0 && LN0 == 10 * 32, "X = 8 comes up, before any item of layer 1");
    static_assert(W_H2 - W_H1 == 2 * 64 * (32 * DM * 2), "h");
    auto run = [&](auto isv) __attribute__((always_inline)) {
        constexpr bool V = decltype(isv)::value;
        constexpr int YB = QK_YB;
        f32x16 ax, ay = {};
        v4u x[8], pp, pq;
        u32 xv = t16 + W_H1, ox = ol, oy = OOB, nn = 1, lt = 0, fcnt = 0, fa = OOB;
        asm volatile("" : "+v"(lt), "+v"(nn));
#pragma unroll
        for (int z = 0; z < 5; ++z) x[z] = XRD(1, 40 - YB + z);
#define QPK(r_, a1, a_, q_) do { if constexpr (V) VCVT(r_[q_], a1[4 * (a_) + 8 * ((q_) >> 1) + 2 * ((q_) & 1)], a1[4 * (a_) + 8 * ((q_) >> 1) + 2 * ((q_) & 1) + 1]); \
                                 else VCVT(r_[q_], a1[4 * (a_) + (q_)], a1[4 * (a_) + (q_) + 8]); } while (0)
#define QPK4(r_, a1, a_) do { QPK(r_, a1, a_, 0); QPK(r_, a1, a_, 1); QPK(r_, a1, a_, 2); QPK(r_, a1, a_, 3); } while (0)
        for (;;) {
            u32 ls = uu(lt), yv, fv, nx, need;
            if (ls != 0u && uu(nn) < 256u) pre_wait(rs, 96, 96, 1u, ep);
            else if (ls != 0u) {
                n1 = (int)(uu(nn) >> 8);
                CFENCE();
                u32 f2 = ld4(rs, h2flag);
                asm volatile("s_waitcnt vmcnt(0)" : "+v"(f2) :: "memory");
                *(LAS u32*)(selw + 16) = (u32)__builtin_amdgcn_ballot_w64(f2 < (u32)n1);
                BAR();
                st4s(rs, fa, 0, fcnt);
                fa = OOB;
                if (n1 > NSEG) __builtin_amdgcn_endpgm();
                if (uu(*(LAS u32*)(selr + 16)) != 0u) {
                    if (w == 0) wait_flags(rs, h2flag, (u32)n1, dead);
                    BAR();
                }
            }
#define QT(k) do {} while (0)
            QT(0);
            SCHED();
            QM0(0, 0); NOPS(0); QDMA(0, xv); QDMA(1, xv); QM0(0, 1);
            SFOR(z, 80)
                constexpr int ks = (z + 40 - YB) % 40, zx = z - YB;
                constexpr auto upto = [](int q) { const int d = (q - YB + 49) % 40; return q + 6 + (d == 0 || d == 2 ? 1 : d == 1 ? 2 : 0); };
                if constexpr (zx >= 0 && zx < 40) { if constexpr (V) M32B(ks == 0, ax, x[z & 7], wt[ks]); else M32A(ks == 0, ax, wt[ks], x[z & 7]); }
                else { if constexpr (V) M32B(ks == 0, ay, x[z & 7], wt[ks]); else M32A(ks == 0, ay, wt[ks], x[z & 7]); }
                if constexpr (z == 78) lt = *(LAS u32*)selr;
#pragma unroll
                for (int q = upto(z - 1); q < upto(z); ++q) x[q & 7] = XRD(q % 80 < YB || q % 80 >= YB + 40, (q + 40 - YB) % 40);
                SCHED();
                (void)&srd; (void)&wb;
                if constexpr (z < 2) QDMA(z + 2, xv);
                if constexpr (z >= 2 && z < 14 && (z & 3) == 2) QM0(0, (z + 6) >> 2);
                if constexpr (z >= 3 && z < 15 && (z & 1)) QDMA((z + 5) >> 1, xv);
                if constexpr (z == 4) { n0 = uni((int)nn); n1 = n0 >> 8; n0 &= 255; PINS(n0); PINS(n1); }
                if constexpr (z == 8) { nx = n0 < NSEG ? W_H1 + (u32)n0 * (32 * DM * 2) : OOB; PINS(nx); }
                if constexpr (z == 12) { yv = t16 + nx; PIN(yv); }
                if constexpr (zx == -6) { QT(1); asm volatile("s_waitcnt vmcnt(6) lgkmcnt(0)\n\ts_barrier" ::: "memory"); QT(2); }
                if constexpr (zx == -5) st4s(rs, fa, 0, fcnt);
                if constexpr (zx == -4 || zx == 0 || zx == 4) QM0(1, (zx + 4) >> 2);
                if constexpr (zx >= -3 && zx < 8 && (zx & 1)) QDMA((zx + 3) >> 1, yv);
                if constexpr (zx == 2) QPK4(pp, ay, 0);
                if constexpr (zx == 6) QPK4(pq, ay, 1);
                if constexpr (zx == 8) st16(rs, oy, pp);
                if constexpr (zx == 9) st16(rs, oy + 512, pq);
                if constexpr (zx == 10) { QT(3); asm volatile("s_waitcnt vmcnt(9)\n\ts_barrier" ::: "memory"); QT(4); }
                if constexpr (zx == 11 || zx == 15) QM0(1, (zx + 1) >> 2);
                if constexpr (zx >= 12 && zx < 20 && (zx & 1) == 0) QDMA(zx >> 1, yv);
                if constexpr (zx == 21) { fcnt = lane == 0 ? (u32)n0 : (u32)n1; fa = fo; asm volatile("" : "+v"(fcnt), "+v"(fa)); }
                if constexpr (zx == 22) { oy = (ol + (u32)n0 * (NH * 16384u)) | (yv & OOB); PIN(oy); }
                if constexpr (zx == 23) { n0 = min(n0 + 1, NSEG); PINS(n0); nx = (u32)min(NSEG, n1 + QK_LEAD); PINS(nx); }
                if constexpr (zx == 24)
                    asm volatile("s_cmp_ge_u32 %1, %0\n\ts_cselect_b32 %0, %4, %1\n\ts_addc_u32 %2, %2, 0\n\ts_cmp_lt_u32 %0, 64\n\ts_cselect_b32 %3, 0, %2"
                                 : "+s"(nx), "+s"(n0), "+s"(n1), "=&s"(need) : "s"(n1 + 64) : "scc");
                if constexpr (zx == 25) { n0 += 1 - (int)min(nx >> 6, 1u); PINS(n0); }
                if constexpr (zx == 26) { nn = (u32)(n0 | n1 << 8); PIN(nn); }
                if constexpr (zx == 27) { ls = W_H1 + (nx + (nx & 64u)) * (32 * DM * 2); PINS(ls); }
                if constexpr (zx == 28) { xv = t16 + ls; PIN(xv); need = nx == 8u ? 255u : need; PINS(need); }
                if constexpr (zx == 34) { asm volatile("s_waitcnt vmcnt(6) lgkmcnt(0)\n\ts_barrier" ::: "memory"); QT(5); }
                if constexpr (zx == QK_SMP) LD4A(fv, h2flag, 0);
                if constexpr (zx == 42) QPK4(pp, ax, 0);
                if constexpr (zx == 43) st16(rs, ox, pp);
                if constexpr (zx == 44) QPK4(pq, ax, 1);
                if constexpr (zx == 45) st16(rs, ox + 512, pq);
                if constexpr (zx == 47) { ox = ol + nx * (NH * 16384u); PIN(ox); }
                if constexpr (z == 74) { asm volatile("s_waitcnt vmcnt(2)" : "+v"(fv) :: "memory"); QT(6); ls = (u32)__builtin_amdgcn_ballot_w64(fv < need); PINS(ls); }
                if constexpr (z == 75) { lt = ls; PIN(lt); *(LAS u32*)selw = lt; }
                if constexpr (z == 77) asm volatile("s_waitcnt lgkmcnt(2)\n\ts_barrier" ::: "memory");
                SCHED();
            SEND
        }
    };
    if (m == 2) run(IC<1>{}); else run(IC<0>{});
}

#define OU_SMP 44
DEV void role_out(const Params& p, lptr lds, int L, int kq, int ng, v2u ep) {
    const int tid = threadIdx.x, lane = tid & 63, w = uni(tid >> 6), hh = lane >> 5, r32 = lane & 31;
    const rsrc_t rs = mk_rsrc(p.ws, W_END);
    v4u wo[64];
    bool dead = false;
    {
        const v4u sw = mk_srd(p.Wo, DM * NQ * 2);
        const u32 vo = wst_src(lane, DM) + (u32)ng * 256u;
        auto fill = [&](int b) {
#pragma unroll 1
            for (int i = 0; i < 8; ++i) DMAIN(sw, uu((b % 5) * 32768 + (4 * i + w) * 1024), vo, uu((kq * 1024 + b * 128 + 16 * i + 4 * w) * (DM * 2)));
        };
#pragma unroll 1
        for (int b = 0; b < 5; ++b) fill(b);
        lptr tb = lds + hh * 1024 + wst_rd(lane, w);
        SFOR(b, 8)
            VMW(b < 4 ? 32 : 56 - 8 * b); BAR();
            lptr q = tb + (b % 5) * 32768;
#pragma unroll
            for (int k = 0; k < 8; ++k) { lptr a = q + (k >> 1) * 8192 + (k & 1) * 2048; wo[8 * b + k] = tr2(a, a + 4096); if (b < 7) asm volatile("" : "+a"(wo[8 * b + k])); else PIN(wo[8 * b + k]); }
            if constexpr (b < 3) { BAR(); fill(b + 5); }
        SEND
    }
    pre_wait(rs, 0, NPRE, 1u, ep);
    if (kq == 0 && ng == 0) LST(272 + L, rt32());
    MK_SRD();
    const u32 ofl = lane < 8 ? foff(F_O + L * 32 + kq * 8 + lane) : OOB, l16 = (u32)lane * 16u;
    const u32 fo = (tid == 0) ? foff(F_P + L * 32 + kq * 5 + ng) : OOB;
    const u32 src0 = uu(W_VO + 8192u + (u32)(L * NSEG * NH + kq * 8) * 16384u + w * 1024);
#define ODMA(i, dst, so) DMA16I(dst, (i) * 4096, l16, so, ((i) >> 1) * 16384 + ((i) & 1) * 4096)
    auto first = [&](int s) { const u32 dst = uu((s & 1) * 65536 + w * 1024), src = src0 + (u32)s * (NH * 16384u); SFOR(i, 10) ODMA(i, dst, src); SEND };
    u32 pa = W_P + (u32)(L * NSEG) * P_ITEM + (u32)(4 * hh * 4 + kq) * (DM * 4) + (u32)(ng * 128 + w * 32 + r32) * 4u, fv = 0;
    bool early = false;
    for (int s = 0; s < NSEG; ++s) {
        const u32 dst = uu((s & 1) * 65536 + w * 1024), src = src0 + (u32)s * (NH * 16384u);
        lptr A = lds + (s & 1) * 65536 + l16;
        if (!early) {
            if (!all_(ofl == OOB || fv >= (u32)s + 1)) wait_flags(rs, ofl, (u32)s + 1, dead);
            LS3(L == 0 && w == 1, 1, kq * 5 + ng);
            first(s);
        }
        asm volatile("s_waitcnt vmcnt(8)\n\ts_barrier" ::: "memory");
        v4u o[64];
#pragma unroll
        for (int k = 0; k < 3; ++k) o[k] = lds_r16(A + k * 1024);
        f32x16 acc;
        SCHED();
        SFOR(k, 64)
            if constexpr (k < 56) M32B(k == 0, acc, o[k], wo[k]); else M32V(acc, o[k], wo[k]);
            if constexpr (k < 61) o[k + 3] = lds_r16(A + (k + 3) * 1024);
            SCHED();
            if constexpr (k < 12 && (k & 1) == 0) ODMA(10 + k / 2, dst, src);
            if constexpr (k == 4) asm volatile("s_waitcnt vmcnt(7)\n\ts_barrier" ::: "memory");
            if constexpr (k == 20) asm volatile("s_waitcnt vmcnt(5)\n\ts_barrier" ::: "memory");
            if constexpr (k == 40) asm volatile("s_waitcnt vmcnt(0)\n\ts_barrier" ::: "memory");
            if constexpr (k == OU_SMP) LD4A(fv, ofl, 0);
            SCHED();
        SEND
        asm volatile("s_nop 9" : "+v"(acc), "+v"(pa));
#pragma unroll
        for (int i = 0; i < 16; ++i) st4s(rs, pa, (u32)((8 * (i >> 2) + (i & 3)) * (4 * DM * 4)), u_(acc[i]));
        asm volatile("s_waitcnt vmcnt(16)" : "+v"(fv) :: "memory");
        LS3(L == 0 && w == 1, 2, kq * 5 + ng);
        early = s + 1 < NSEG && all_(ofl == OOB || fv >= (u32)s + 2);
        if (early) { first(s + 1); VMW(10); }
        else { fv = ld4(rs, ofl); asm volatile("s_waitcnt vmcnt(0)" : "+v"(fv) :: "memory"); }
        BAR();
        st4s(rs, fo, 0, (u32)s + 1);
        pa += P_ITEM;
        LS2(L == 0 && w == 1, 160 + kq * 5 + ng);
    }
}

constexpr int RW_P = 20480, RW_X = RW_P + 10240;
DEV void role_row(const Params& p, lptr lds, int id, v2u ep) {
    const int lane = threadIdx.x & 63, w = uni(threadIdx.x >> 6);
    const int ri = id >> 1, L = id & 1;
    const rsrc_t rs = mk_rsrc(p.ws, W_END);
    bool dead = false, have = false;
    const u32 fp = lane < 20 ? foff(F_P + L * 32 + lane) : OOB, l16 = (u32)lane * 16u, lx = lane < 16 ? l16 + 1024u : OOB, fb = uu(w * CV_W);
    lptr F = lds + w * CV_W;
    MK_SRD();
    const v4u sx = mk_srd(p.x, ACT);
    auto yfill = [&](int b) {
#pragma unroll 1
        for (int i = 0; i < 5; ++i) DMA32(uu(fb + (b & 1) * 10240 + i * 2048), l16, uu(W_Y + (u32)(ri * 8 + b) * 10240u + i * 2048));
    };
    auto xfill = [&](int s) {
        const u32 d = uu(fb + RW_X + (s & 1) * 2048), o = uu((u32)(s * 32 + ri) * (DM * 2));
        if (L == 0) { DMAIN(sx, d, l16, o); DMAIN(sx, d + 1024, lx, o); } else { DMA16(d, l16, o + W_X2); DMA16(d + 1024, lx, o + W_X2); }
    };
    auto pfill = [&](int s) {
        const u32 o = uu(W_P + (u32)(L * NSEG + s) * P_ITEM + (u32)ri * (4 * DM * 4));
        SFOR(i, 10) DMA16I(fb, RW_P + i * 1024, l16, o, i * 1024); SEND
        if (L == 1) xfill(s);
    };
    if (L == 0) xfill(0);
    pre_poll(rs, 0, NPRE, 3u, ep);
    if (id == 0) LST(274, rt32());
    yfill(0); DRAIN();
    u32 fv = 0;
    for (int s = 0; s < NSEG; ++s) {
        if (!have) {
            if (!all_(fp == OOB || fv >= (u32)s + 1)) wait_flags(rs, fp, (u32)s + 1, dead);
            LS3(L == 0, 3, ri);
            pfill(s);
        }
        LS2(L == 0, 192 + ri);
        f32x2 y[5];
        {
            lptr ya = F + ((s >> 3) & 1) * 10240 + (s & 7) * 2 + lane * 16;
            auto yr = [&](lptr q) { return lo_f(*(LAS u16*)q); };
            y[0] = (f32x2){yr(ya), yr(ya + 2560)}; y[1] = (f32x2){yr(ya + 5120), yr(ya + 7680)};
            y[2] = (f32x2){yr(ya + 1024), yr(ya + 3584)}; y[3] = (f32x2){yr(ya + 6144), yr(ya + 8704)};
            lptr yc = ya - lane * 16 + 2048 + (lane >> 1) * 16 + (lane & 1) * 5120;
            y[4] = (f32x2){yr(yc), yr(yc + 2560)};
            asm volatile("" : "+v"(y[0]), "+v"(y[1]), "+v"(y[2]), "+v"(y[3]), "+v"(y[4]));
        }
        if (!have) DRAIN();
        LS3(L == 0, 4, ri);
        f32x2 x1[5];
        {
            f32x4 a[4], b[4]; f32x2 c[4];
            lptr q = F + RW_X + (s & 1) * 2048;
            v2u xa = *(LAS v2u*)(q + lane * 8), xb = *(LAS v2u*)(q + 512 + lane * 8); u32 xc = *(LAS u32*)(q + 1024 + lane * 4);
#pragma unroll
            for (int kq = 0; kq < 4; ++kq) { lptr r = F + RW_P + kq * (DM * 4); a[kq] = *(LAS f32x4*)(r + l16); b[kq] = *(LAS f32x4*)(r + 1024 + l16); c[kq] = *(LAS f32x2*)(r + 2048 + lane * 8); }
            SCHED();
            const f32x4 sa = (a[0] + a[1]) + (a[2] + a[3]), sb = (b[0] + b[1]) + (b[2] + b[3]);
            const f32x2 o[5] = {sa.lo, sa.hi, sb.lo, sb.hi, (c[0] + c[1]) + (c[2] + c[3])};
            const u32 xi[5] = {xa.x, xa.y, xb.x, xb.y, xc};
#pragma unroll
            for (int i = 0; i < 5; ++i) x1[i] = up2(xi[i]) + o[i];
            asm volatile("" : "+v"(x1[0]), "+v"(x1[1]), "+v"(x1[2]), "+v"(x1[3]), "+v"(x1[4]));
        }
        have = s + 1 < NSEG && all_(fp == OOB || fv >= (u32)s + 2);
        if (__builtin_expect(have, 0)) pfill(s + 1);
        if (L == 0) xfill(s + 1);
        const f32x2 st1 = ln_stats(x1);
        u32 xp[5];
#pragma unroll
        for (int i = 0; i < 5; ++i) xp[i] = pkv(x1[i] + (x1[i] - st1.x) * (y[i] * st1.y));
        const int row = s * 32 + ri;
        if (L == 0) {
            const u32 ro = (u32)row * (DM * 2);
            f32x2 x2[5];
#pragma unroll
            for (int i = 0; i < 5; ++i) x2[i] = up2(xp[i]);
            st8s(rs, (u32)lane * 8, W_X2 + ro, (v2u){xp[0], xp[1]});
            st8s(rs, (u32)lane * 8 + 512, W_X2 + ro, (v2u){xp[2], xp[3]});
            st4s(rs, (u32)lane * 4 + 1024, W_X2 + ro, xp[4]);
            const f32x2 st2 = ln_stats(x2);
            u32 hp[5];
#pragma unroll
            for (int i = 0; i < 5; ++i) hp[i] = pk2((x2[i] - st2.x) * st2.y);
            store_h(rs, W_H2, row, hp);
        } else {
            u16* o_ = p.out + (size_t)row * DM;
            *(v2u*)(o_ + 4 * lane) = (v2u){xp[0], xp[1]};
            *(v2u*)(o_ + 256 + 4 * lane) = (v2u){xp[2], xp[3]};
            *(u32*)(o_ + 512 + 2 * lane) = xp[4];
        }
        CFENCE();
        LS3(L == 0, 5, ri);
        fv = ld4(rs, fp);
        asm volatile("s_waitcnt vmcnt(0)" : "+v"(fv) :: "memory");
        st4s(rs, lane == 0 && L == 0 ? foff(F_H2 + ri) : OOB, 0, (u32)s + 1);
        if ((s & 7) == 6 && s < NSEG - 8) {
            if (s == 30) pre_poll(rs, 0, NCV, 5u, ep);
            if (s == 46) st8s(rs, (id == 1 && lane == 0) ? foff(F_EPOCH) : OOB, 0, (v2u){ep.x + 8u, ep.y});
            yfill((s >> 3) + 1);
        }
        if (ri == 0) LST(320 + L * 64 + s, rt32());
        LS2(L == 0, 224 + ri);
        LMX(L == 0, 2, s, rt32());
    }
}

constexpr float SCALE = 0.08838834764831845f, LOG2E = 1.4426950408889634f;
constexpr int C_K = 0, C_V = 8192, C_QX = 16384, C_MEM = 24576, C_Q = C_MEM + 10240, C_KM = C_Q + 2304, C_VM = C_KM + 2304, C_DUMP = C_VM + 2048,
              C_ZERO = C_DUMP + 1024, C_WL = C_ZERO + 128;
constexpr int NLF = 14;
static_assert(C_WL % 16 == 0 && C_WL + 4 * NLF * 2048 <= LDS_BYTES, "head lds");
#define CH_VIS 10
#define CH_H0 104
#define CH_W0 81
#define CH_MH 2
#define CH_MW 4
#define CH_REC 34
#define CH_LATE (-10)
#define CH_RISE 12u
#define CH_FALL 3u
#define CH_PRE 9
#define CH_T0 190
#define CH_UP 2
#define CH_WX 6
#define WAIT_TXT(tmp, t4) "s_memrealtime vcc\n\t.p2align 5\n1:\n\ts_waitcnt lgkmcnt(0)\n\ts_sub_u32 " tmp ", " t4 ", vcc_lo\n\ts_memrealtime vcc\n\ts_cmpk_lt_u32 " tmp ", 400\n\ts_cbranch_scc1 1b\n\t" \
                          "s_add_u32 " tmp ", " tmp ", 4\n\ts_cselect_b32 " tmp ", " tmp ", 0\n2:\n\ts_sub_u32 " tmp ", " tmp ", 1\n\ts_cbranch_scc0 2b\n\t"
DEV void wait_until(u32 t) { u32 tmp; asm volatile(WAIT_TXT("%0", "%1") "s_waitcnt lgkmcnt(0)" : "=&s"(tmp) : "s"(uu(t - 4u)) : "vcc", "scc", "memory"); }
#define SMF_A(acc, a, b) asm volatile("v_smfmac_f32_16x16x64_bf16 %0, %1, %2, %3" : "+v"(acc) : "v"(a), "a"(b), "v"(idx))
#define SMF_V(acc, a, b) asm volatile("v_smfmac_f32_16x16x64_bf16 %0, %1, %2, %3" : "+v"(acc) : "v"(a), "v"(b), "v"(idx))
#define MF0(d, a, b) asm volatile("v_mfma_f32_16x16x32_bf16 %0, %1, %2, 0" : "=&v"(d) : "v"(a), "v"(b))
#define MF1(d, a, b) asm volatile("v_mfma_f32_16x16x32_bf16 %0, %1, %2, %0" : "+v"(d) : "v"(a), "v"(b))
#define MF0H(d, a, b) asm volatile("v_mfma_f32_16x16x16_bf16 %0, %1, %2, 0" : "=&v"(d) : "v"(a), "v"(b))
#define MF1H(d, a, b) asm volatile("v_mfma_f32_16x16x16_bf16 %0, %1, %2, %0" : "+v"(d) : "v"(a), "v"(b))
DEV v8u tr_frag(lptr p0, lptr p1, lptr p2, lptr p3) { return cat8(tr2(p0, p1), tr2(p2, p3)); }
constexpr int qslot(int ks) { return (ks & 1) && ks < 8 ? ks >> 1 : -1; }
constexpr int kslot(int ks) { return ks < 4 ? 4 + ks : -1; }
constexpr int vslot(int ks) { return ks >= 4 ? 4 + ks : -1; }
DEV u32 ring_map(u32 off, u32 half) { return ((off >> 13) << 14) + (half << 13) + (off & 8191u); }
#define ZRD(x, i) x = __builtin_bit_cast(f32x4, lds_r16(ZP + 16 * (i)))
#define VMAX(d, a, b) asm volatile("v_max_f32 %0, %1, %2" : "=v"(d) : "v"(a), "v"(b))
#define VMAX3(d, a, b, c_) asm volatile("v_max3_f32 %0, %1, %2, %3" : "=v"(d) : "v"(a), "v"(b), "v"(c_))
#define VEXP(d, a) asm volatile("v_exp_f32 %0, %1" : "=v"(d) : "v"(a))
#define VMULC(d, a) asm volatile("v_mul_f32 %0, 0x3e0293ee, %1" : "=v"(d) : "v"(a))
#define CADD(d, a, b) do { d = (a) + (b); PIN(d); } while (0)
#define CMULC(d, a) do { d = (a) * CEXP; PIN(d); } while (0)
#define CCVT(d, a, b) do { d = pk(a, b); PIN(d); } while (0)
#define LGK(n) __builtin_amdgcn_s_waitcnt(0xC07F | (n) << 8)
#define SEL(d, a, b) asm volatile("v_cndmask_b32 %0, %1, %2, %3" : "=v"(d) : "v"(a), "v"(b), "s"(himask))
#define QPERM(d, a, q) asm volatile("v_mov_b32_dpp %0, %1 quad_perm:" q " row_mask:0xf bank_mask:0xf" : "=v"(d) : "v"(a))

#define AL_H 3
#define AL_W 0
#define HD_SMP 56
DEV void role_head(const Params& p, lptr lds, int L, int h, v2u ep) {
    const int tid = threadIdx.x, lane = tid & 63, w = uni(tid >> 6), g = lane >> 4, r = lane & 15;
    const rsrc_t rs = mk_rsrc(p.ws, W_END);
    v8u wq0[10], wq1[10], wk0[10], wk1[10], wv0[10], wv1[10];
    lptr WL = lds + C_WL + w * (NLF * 2048) + lane * 16;
    {
        constexpr int GS = 1056, CS = 48 * GS;
        const v4u sq = mk_srd(p.Wq, DM * NQ * 2), sk = mk_srd(p.Wk, DM * NQ * 2), sv = mk_srd(p.Wv, DM * NQ * 2);
        const u32 vo = (u32)(g * NQ + h * DH + 8 * (r ^ ((g & 1) << 3))) * 2u;
        auto fill = [&](int ch) {
#pragma unroll 1
            for (int i = 0; i < 4; ++i) {
                const u32 dst = uu((ch % 3) * CS + (4 * i + w) * GS), so = uu((ch * 64 + 16 * i + 4 * w) * (NQ * 2));
                DMAIN(sq, dst, vo, so); DMAIN(sk, dst + 16 * GS, vo, so); DMAIN(sv, dst + 32 * GS, vo, so);
            }
        };
        fill(0); fill(1); fill(2);
        const int q4 = r >> 2;
        lptr tb = lds + (4 * (g >> 1) + 2 * (q4 >> 1) + (g & 1)) * GS + (q4 & 1) * 256 + ((w * 64 + (r & 3) * 8) ^ ((q4 & 1) << 7));
        SFOR(ch, 10)
            VMW(ch < 8 ? 24 : 108 - 12 * ch);
            BAR();
            auto frag = [&](int m, int t) { lptr b = tb + (ch % 3) * CS + m * 16 * GS + t * 32; return tr_frag(b, b + 512, b + 8 * GS, b + 8 * GS + 512); };
            wq0[ch] = frag(0, 0); wq1[ch] = frag(0, 1); wk0[ch] = frag(1, 0); wk1[ch] = frag(1, 1); wv0[ch] = frag(2, 0); wv1[ch] = frag(2, 1);
            asm volatile("" : "+a"(wq0[ch]), "+a"(wk0[ch]), "+v"(wv0[ch]), "+v"(wv1[ch]));
            if constexpr (qslot(ch) < 0) asm volatile("" : "+a"(wq1[ch])); else asm volatile("" : "+v"(wq1[ch]));
            if constexpr (kslot(ch) < 0) asm volatile("" : "+a"(wk1[ch])); else asm volatile("" : "+v"(wk1[ch]));
            BAR();
            if constexpr (ch < 7) fill(ch + 3);
        SEND
#pragma unroll
        for (int ch = 0; ch < 10; ++ch) {
            if (qslot(ch) >= 0) { lds_w16(WL + qslot(ch) * 2048, wq1[ch].lo); lds_w16(WL + qslot(ch) * 2048 + 1024, wq1[ch].hi); }
            if (kslot(ch) >= 0) { lds_w16(WL + kslot(ch) * 2048, wk1[ch].lo); lds_w16(WL + kslot(ch) * 2048 + 1024, wk1[ch].hi); }
            if (vslot(ch) >= 0) { lds_w16(WL + vslot(ch) * 2048, wv1[ch].lo); lds_w16(WL + vslot(ch) * 2048 + 1024, wv1[ch].hi); }
        }
        CFENCE();
    }
    const u32 idx = (r & 2) ? 0xEEEEu : 0x4444u;
    unsigned long long himask = 0xCCCCCCCCCCCCCCCCull;
    PINS(himask);
    bool dead = false;
    if (h == 3) LST(266 + L, rt32());
    if (w == 0) LST(1240 + L * 32 + h, rt32());
    if (L == 1) {
        KvIn in;
        kvt_load(p, KT0 + 4 * h + w, in, [] {}); kvt_make(rs, lds + w * 9216, KT0 + 4 * h + w, in);
        DRAIN(); BAR();
        pre_post(rs, 64 + h, 3u, ep);
    }
    const u32 pol0 = pol_wait(rs, ep);
    if (h == 3 && L == 0) LST(277, rt32());
    const u32 l16 = (u32)lane * 16u;
    const u32 mring = W_MRING + (u32)L * (MB / 4) + pol0 * 8192u, hint = mring + 4 * 32768u;
    const u32 fkh = lane < 3 ? foff(F_QKV + L * 96 + lane * 32 + h) : lane < 7 ? hint + (u32)(lane - 3) * 4u : OOB;
    u32 tm = 0;
    MK_SRD();
    u32 ksrc = uu(W_KQ + (u32)(L * (NSEG - 1) * NH + h) * 16384u + w * 2048), vsrc = ksrc + (W_VO - W_KQ);
    u32 dk = uu(C_K + w * 2048), dv = dk + (C_V - C_K), dq = dk + (C_QX - C_K), dm = uu(C_MEM + w * 2048), dm2 = uu(w < 2 ? C_MEM + 8192 + w * 1024 : C_DUMP);
    asm volatile("" : "+s"(dk), "+s"(dv), "+s"(dq), "+s"(dm), "+s"(dm2));
    auto keys_wait = [&](u32 need) {
#pragma unroll 1
        for (int n = 0; !dead; ++n) {
            const bool beat = need + 2 * L > 2u;
            if (beat) wait_until(tm);
            const u32 hv = ld4(rs, lane == 7 ? foff(F_ERR) : fkh);
            if (beat) { tm = rdl(hv, 5); wait_until(rdl(hv, 3) - 30u); }
            if (((u32)__builtin_amdgcn_ballot_w64(ld4(rs, fkh) < need) & 7u) == 0u) break;
            dead = n > SPIN_CAP || rdl(hv, 7) != 0u;
            st4s(rs, dead ? foff(F_ERR) : OOB, 0, 1u);
        }
        CFENCE();
        DMA32(dk, l16, ksrc); DMA32(dv, l16, vsrc); DMA32(dq, l16, ksrc ^ 8192u);
    };
    if (L == 0) { st4s(rs, tid == 0 ? hint + 4096u + (u32)h * 128u : OOB, 0, 0u); keys_wait(1u); }
    lptr qw = lds + C_Q + (2 * g) * 288 + (32 * w + 2 * r) * 2;
    lptr kw = qw + (C_KM - C_Q); PIN(kw);
    lptr qr = lds + C_Q + (2 * (r >> 2) + (r & 1)) * 288 + g * 16;
    lptr LB = lds + l16, VX = lds + C_V + w * 2048 + l16;
    PIN(VX);
    if (tid >= 64 && tid < 70) { const u32 z = tid == 68 ? 0x3F803F80u : tid == 69 ? PSENT : 0u; lds_w16(lds + C_ZERO + (tid - 64) * 16, (v4u){z, z, z, z}); }
    u32 zaddr = 0; PIN(zaddr);
    lptr ZP = lds + zaddr + C_ZERO;
    const float CEXP = SCALE * LOG2E;
#define LF_RD(slot, hf) lf[slot][hf] = lds_r16(WL + (slot) * 2048 + (hf) * 1024)
    BAR();
    const u32 ofl = tid == 0 ? foff(F_O + L * 32 + h) : OOB;
    u32 oso = (((g | L) & 1) ? OOB : 0u) + W_ORING + (u32)L * (MB / 2) + ring_map((u32)((r >> 3) * 32768 + h * 1024 + w * 256 + (g >> 1) * 128 + (r & 7) * 16), pol0);
    unsigned long long vis[8];
#pragma unroll
    for (int i = 0; i < 8; ++i) vis[i] = __builtin_amdgcn_ballot_w64(16 * (i >> 2) + 4 * g + (i & 3) <= 16 * (w & 1) + r);
    u32 missed = 0;
    for (int s = -L; s < NSEG;) {
        u32 sv = (u32)max(s, 0), ms = mring + (sv & 3u) * 32768u + w * 2048, ms2 = mring + (sv & 3u) * 32768u + 16384u + (w & 1) * 1024;
        asm volatile("" : "+s"(ms), "+s"(ms2)); PIN(sv);
        f32x4 aq0, aq1, ak0, ak1;
        v4u lf[NLF][2];
        ZRD(aq0, 0); ZRD(aq1, 1); ZRD(ak0, 2); ZRD(ak1, 3);
#pragma unroll
        for (int i = 0; i < 4; ++i) { LF_RD(i, 0); LF_RD(i, 1); }
        SCHED();
        v4u A[10];
#define HT(k) do {} while (0)
        v4u kx[8], qf[4], kmf[4], vx[2], ones, P;
        float fa, fb, fc, fd, inv, sx[4], t[4], tm0, tm1; u32 pa, pb, fl, e01, e23, pmm;
        f32x4 av0, av1, s0, s1, sm, oa, ob, rsum;
        v2u vm, pm;
#define LW(sl) cat8(lf[sl][0], lf[sl][1])
#define PQ(ks, tl) do { if constexpr (!(tl)) SMF_A(aq0, A[ks], wq0[ks]); else if constexpr (qslot(ks) < 0) SMF_A(aq1, A[ks], wq1[ks]); else { const v8u lw_ = LW(qslot(ks)); SMF_V(aq1, A[ks], lw_); } } while (0)
#define PK(ks, tl) do { if constexpr (!(tl)) SMF_A(ak0, A[ks], wk0[ks]); else if constexpr (kslot(ks) < 0) SMF_A(ak1, A[ks], wk1[ks]); else { const v8u lw_ = LW(kslot(ks)); SMF_V(ak1, A[ks], lw_); } } while (0)
#define PV(ks, tl) do { if constexpr (!(tl)) SMF_V(av0, A[ks], wv0[ks]); else if constexpr (vslot(ks) < 0) SMF_V(av1, A[ks], wv1[ks]); else { const v8u lw_ = LW(vslot(ks)); SMF_V(av1, A[ks], lw_); } } while (0)
#define KXR(i) kx[i] = lds_r16(LB + C_K + ((i) & 1) * 4096 + ((i) >> 1) * 1024)
#define XW(q_) do { *(LAS u32*)(q_) = pa; *(LAS u32*)((q_) + 288) = pb; } while (0)
        auto slot = [&](auto zc) __attribute__((always_inline)) {
            constexpr int z = decltype(zc)::value;
            if constexpr (z < 14) PQ(z >> 1, z & 1);
            else if constexpr (z < 17) PQ(z - 7, 0);
            else if constexpr (z < 20) PQ(z - 10, 1);
            else if constexpr (z < 34) PK((z - 20) >> 1, z & 1);
            else if constexpr (z < 37) PK(z - 27, 0);
            else if constexpr (z < 40) PK(z - 30, 1);
            else if constexpr (z < 42) { if constexpr (z == 40) MF0(s0, kx[0], qf[0]); else MF0(s1, kx[1], qf[0]); }
            else if constexpr (z < 48) { if constexpr (z & 1) MF1(s1, kx[z - 40], qf[(z - 40) >> 1]); else MF1(s0, kx[z - 40], qf[(z - 40) >> 1]); }
            else if constexpr (z < 54) PV(z - 48, 0);
            else if constexpr (z == 54) MF0(sm, kmf[0], qf[0]);
            else if constexpr (z < 58) MF1(sm, kmf[z - 54], qf[z - 54]);
            else if constexpr (z < 62) PV(z - 52, 0);
            else if constexpr (z < 64) PV(z - 62, 1);
            else if constexpr (z == 64) MF0(oa, vx[0], P);
            else if constexpr (z == 65) MF0(ob, vx[1], P);
            else if constexpr (z == 66) MF0(rsum, ones, P);
            else if constexpr (z < 75) PV(z < 70 ? z - 63 : z == 70 ? 2 : z == 71 ? 7 : z == 72 ? 3 : z - 65, 1);
            else { const v2u ones2 = ones.lo; MF1H(rsum, ones2, pm); }
            if constexpr (z < 6) A[4 + z] = lds_r16(LB + C_MEM + (4 + z) * 1024);
            if constexpr (z >= 6 && z < 10) LF_RD(4 + ((z - 6) >> 1), z & 1);
            if constexpr (z >= 14 && z < 18) LF_RD(6 + ((z - 14) >> 1), z & 1);
            if constexpr (z >= 22 && z < 28) KXR(z - 22);
            if constexpr (z == 28) XW(qw);
            if constexpr (z == 29 || z == 30) KXR(z - 23);
            if constexpr (z == 32 || z == 47) asm volatile("s_barrier" ::: "memory");
            if constexpr (z >= 33 && z < 37) qf[z - 33] = lds_r16(qr + (z - 33) * 64);
            if constexpr (z == 20) ZRD(av0, 0);
            if constexpr (z == 43) XW(kw);
            if constexpr (z == 44 || z == 45) vx[z - 44] = lds_r16(VX + (z - 44) * 1024);
            if constexpr (z >= 48 && z < 52) kmf[z - 48] = lds_r16(qr + (C_KM - C_Q) + (z - 48) * 64);
            if constexpr (z == 52) ZRD(av1, 1);
            if constexpr (z == 37) ones = lds_r16(ZP + 64);
            if constexpr (z >= 57 && z < 64) LF_RD(8 + ((z - 57) >> 1), (z - 57) & 1);
            if constexpr (z >= 65 && z < 70) LF_RD(8 + ((z - 58) >> 1), (z - 58) & 1);
            if constexpr (z == HD_SMP) LD4A(fl, fkh, 0);
            SCHED();
            if constexpr (z == 19) HT(2);
            if constexpr (z == 32) HT(3);
            if constexpr (z == 47) HT(4);
            if constexpr (z == 64) HT(5);
            if constexpr (z == 19) LGK(4);
            if constexpr (z == 21 || z == 41) LGK(1);
            if constexpr (z == 56 || z == 73) LGK(0);
            if constexpr (z == 31 || z == 46) LGK(2);
            if constexpr (z == 39) LGK(4);
            if constexpr (z >= 53 && z < 56) LGK(57 - z);
            if constexpr (z == 64) LGK(3);
            if constexpr (z == 68) LGK(5);
            if constexpr (z >= 70 && z < 73) LGK(74 - z);
            SCHED();
            if constexpr (z == 18) CADD(fa, aq0[0], aq0[2]);
            if constexpr (z == 19) CADD(fb, aq0[1], aq0[3]);
            if constexpr (z == 20) CMULC(fa, fa);
            if constexpr (z == 21) CADD(fc, aq1[0], aq1[2]);
            if constexpr (z == 22) CADD(fd, aq1[1], aq1[3]);
            if constexpr (z == 23) CMULC(fc, fc);
            if constexpr (z == 24) CMULC(fb, fb);
            if constexpr (z == 25) CMULC(fd, fd);
            if constexpr (z == 26) CCVT(pa, fa, fc);
            if constexpr (z == 27) CCVT(pb, fb, fd);
            if constexpr (z == 38) CADD(fa, ak0[0], ak0[2]);
            if constexpr (z == 39) CADD(fb, ak0[1], ak0[3]);
            if constexpr (z == 41) { CADD(fc, ak1[0], ak1[2]); CADD(fd, ak1[1], ak1[3]); }
            if constexpr (z == 42) { CCVT(pa, fa, fc); CCVT(pb, fb, fd); }
            if constexpr (z >= 49 && z < 53) SEL(sx[z - 49], s0[z - 49], s1[z - 49]);
            if constexpr (z >= 53 && z < 57) VEXP(t[z - 53], sx[z - 53]);
            if constexpr (z == 57) CCVT(e01, t[0], t[1]);
            if constexpr (z == 58) CCVT(e23, t[2], t[3]);
            if constexpr (z == 59) QPERM(P.x, e01, "[0,1,0,1]");
            if constexpr (z == 60) QPERM(P.z, e01, "[2,3,2,3]");
            if constexpr (z == 61) QPERM(P.y, e23, "[0,1,0,1]");
            if constexpr (z == 62) QPERM(P.w, e23, "[2,3,2,3]");
            if constexpr (z == 63) CADD(fa, av0[0], av0[2]);
            if constexpr (z == 64) { asm volatile("" :: "v"(sm)); VEXP(tm0, sm[0]); }
            if constexpr (z == 65) VEXP(tm1, sm[1]);
            if constexpr (z == 66) CADD(fb, av0[1], av0[3]);
            if constexpr (z == 67) CCVT(pmm, tm0, tm1);
            if constexpr (z >= 68 && z < 72) SEL(oa[z - 68], oa[z - 68], ob[z - 68]);
            if constexpr (z == 72) asm volatile("v_cndmask_b32 %0, %1, 0, %2" : "=v"(pm.x) : "v"(pmm), "s"(himask));
            if constexpr (z == 73) asm volatile("v_cndmask_b32 %0, 0, %1, %2" : "=v"(pm.y) : "v"(pmm), "s"(himask));
            if constexpr (z == 74) LGK(15);
            if constexpr (z == 75) CCVT(vm.x, fa, fb);
            SCHED();
        };
        {
            u32 tmp;
            asm volatile("s_mov_b32 m0, %2\n\t" WAIT_TXT("%0", "%1") "buffer_load_dwordx4 %4, %5, %6 offen sc1 lds\n\tbuffer_load_dwordx4 %4, %5, %6 offen offset:1024 sc1 lds\n\t"
                         "s_mov_b32 m0, %3\n\ts_nop 0\n\tbuffer_load_dwordx4 %4, %5, %7 offen sc1 lds\n\t.p2align 6\n\t.rept %8\n\ts_nop 0\n\t.endr\n\ts_waitcnt vmcnt(0) lgkmcnt(0)\n\ts_barrier"
                         : "=&s"(tmp) : "s"(uu(tm - 4u)), "s"(dm), "s"(dm2), "v"(l16), "s"(srd), "s"(ms), "s"(ms2), "i"(AL_H) : "vcc", "scc", "memory");
        }
        HT(1);
#pragma unroll
        for (int ks = 0; ks < 4; ++ks) A[ks] = lds_r16(LB + C_MEM + ks * 1024);
        SCHED();
        st4s(rs, ofl, 0, sv);
        SCHED();
        static_for<0, 76>(slot);
        asm volatile("s_nop 2\n\tv_add_f32 %1, %3, %5\n\tv_add_f32 %2, %4, %6\n\tv_cvt_pk_bf16_f32 %0, %1, %2"
                     : "=v"(vm.y), "=&v"(fc), "=&v"(fd) : "v"(av1[0]), "v"(av1[1]), "v"(av1[2]), "v"(av1[3]));
        __builtin_amdgcn_s_waitcnt(0xCF7F);
        asm volatile("s_nop 0\n\tv_mfma_f32_16x16x16_bf16 %0, %2, %3, %0\n\tv_rcp_f32 %1, %4" : "+v"(oa), "=&v"(inv) : "v"(vm), "v"(pm), "v"(rsum[0]), "v"(rsum));
        SCHED();
        if (__builtin_expect(__builtin_amdgcn_ballot_w64(u_(rsum[0]) - 0x21800000u > 0x3C000000u) != 0ull, 0)) {
            DRAIN(); SCHED();
            bool miss = false;
#pragma unroll 1
            for (int i = 0; i < 10; ++i) { const v4u a = lds_r16(LB + C_MEM + i * 1024); miss |= a.x == PSENT || a.y == PSENT || a.z == PSENT || a.w == PSENT; }
            if (missed < SPIN_CAP && !all_(!miss)) {
                *(LAS u32*)(lds + C_DUMP + (w == 0 ? 0 : 64)) = ld4(rs, foff(F_ERR));
                asm volatile("" : "+v"(fl) :: "memory");
                tm = rdl(fl, 5);
                LAD(w == 0, 1, L * 64 + s);
                BAR();
                missed = uu(*(LAS u32*)(lds + C_DUMP)) != 0u ? SPIN_CAP : missed + 1;
                st4s(rs, missed == SPIN_CAP ? foff(F_ERR) : OOB, 0, 1u);
                BAR();
                continue;
            }
            float mx = fmaxf(fmaxf(fmaxf(sx[0], sx[1]), fmaxf(sx[2], sx[3])), fmaxf(sm[0], sm[1]));
            mx = max32(max16(fmaxf(mx, dpp<0x4E>(mx))));
#pragma unroll
            for (int i = 0; i < 4; ++i) t[i] = __builtin_amdgcn_exp2f(sx[i] - mx);
            e01 = pk(t[0], t[1]); e23 = pk(t[2], t[3]);
            pmm = pk(__builtin_amdgcn_exp2f(sm[0] - mx), __builtin_amdgcn_exp2f(sm[1] - mx));
            asm volatile("v_cndmask_b32 %0, %2, 0, %3\n\tv_cndmask_b32 %1, 0, %2, %3" : "=&v"(pm.x), "=&v"(pm.y) : "v"(pmm), "s"(himask));
            asm volatile("s_nop 1" : "+v"(e01), "+v"(e23), "+v"(pm));
            QPERM(P.x, e01, "[0,1,0,1]"); QPERM(P.z, e01, "[2,3,2,3]"); QPERM(P.y, e23, "[0,1,0,1]"); QPERM(P.w, e23, "[2,3,2,3]");
            NOPS(1);
            MF0(rsum, ones, P); MF0(oa, vx[0], P); MF0(ob, vx[1], P);
            NOPS(7);
            { const v2u ones2 = ones.lo; MF1H(rsum, ones2, pm); }
#pragma unroll
            for (int i = 0; i < 4; ++i) SEL(oa[i], oa[i], ob[i]);
            NOPS(1);
            MF1H(oa, vm, pm);
            asm volatile("s_nop 7\n\tv_rcp_f32 %0, %1" : "=v"(inv) : "v"(rsum[0]), "v"(rsum));
        }
        {
            v4u oc; f32x2 u0 = oa.lo, u1 = oa.hi; const f32x2 i2 = {inv, inv};
            asm volatile("s_nop 0\n\tv_pk_mul_f32 %0, %0, %2\n\tv_pk_mul_f32 %1, %1, %2" : "+v"(u0), "+v"(u1) : "v"(i2));
            __builtin_amdgcn_s_waitcnt(0xCF7F);
            asm volatile("v_cvt_pk_bf16_f32 %0, %4, %5\n\tv_cvt_pk_bf16_f32 %2, %4, %5\n\tv_cvt_pk_bf16_f32 %1, %6, %7\n\tv_cvt_pk_bf16_f32 %3, %6, %7\n\t"
                         "v_permlane16_swap_b32 %0, %2\n\ts_nop 0\n\tv_permlane16_swap_b32 %1, %3"
                         : "=&v"(oc.x), "=&v"(oc.y), "=&v"(oc.z), "=&v"(oc.w) : "v"(u0.x), "v"(u0.y), "v"(u1.x), "v"(u1.y));
            st16(rs, oso, oc);
        }
        SCHED();
        unsigned long long clk;
        asm volatile("s_memrealtime %0" : "=s"(clk) :: "memory");
        HT(6);
        v4u tq[4], tk[8], tv[2];
        f32x4 c0, c1, cm;
        u32 oo, kvo; bool kvok;
        u32 ln = l16; PIN(ln);
        lptr QX = lds + C_QX + (w & 1) * 4096 + ln, VO = lds + C_V + (w ^ 1) * 2048 + ln, VMX = lds + C_VM + w * 512 + (ln >> 1);
#pragma unroll
        for (int i = 0; i < 2; ++i) { tq[i] = lds_r16(QX + i * 1024); kmf[i] = lds_r16(qr + (C_KM - C_Q) + i * 64); tk[2 * i] = lds_r16(LB + C_K + i * 1024); tk[2 * i + 1] = lds_r16(LB + C_K + 4096 + i * 1024); }
        *(LAS v2u*)VMX = vm;
        SCHED();
        SFOR(i, 12)
            if constexpr (i == 0) MF0(cm, kmf[0], tq[0]); else if constexpr (i == 1) MF0(c0, tk[0], tq[0]); else if constexpr (i == 2) MF0(c1, tk[1], tq[0]);
            else if constexpr (i % 3 == 0) MF1(cm, kmf[i / 3], tq[i / 3]); else if constexpr (i % 3 == 1) MF1(c0, tk[2 * (i / 3)], tq[i / 3]); else MF1(c1, tk[2 * (i / 3) + 1], tq[i / 3]);
            if constexpr (i < 2) kmf[2 + i] = lds_r16(qr + (C_KM - C_Q) + (2 + i) * 64);
            if constexpr (i == 0) { tq[2] = lds_r16(QX + 2048); tk[4] = lds_r16(LB + C_K + 2048); }
            if constexpr (i == 1) { tk[5] = lds_r16(LB + C_K + 6144); tq[3] = lds_r16(QX + 3072); }
            if constexpr (i == 2) { tk[6] = lds_r16(LB + C_K + 3072); tk[7] = lds_r16(LB + C_K + 7168); }
            if constexpr (i == 3) { tv[0] = lds_r16(VO); tv[1] = lds_r16(VO + 1024); }
            if constexpr (i == 4) { oo = ln + (ln & 0x300u) + ((vsrc ^ 8192u) + (u32)((w & 1) * 256) + ((u32)s & OOB)); PIN(oo); }
            if constexpr (i == 5) { ksrc += NH * 16384; vsrc += NH * 16384; PINS(ksrc); PINS(vsrc); }
            if constexpr (i == 9) { asm volatile("s_waitcnt vmcnt(1)" : "+v"(fl) :: "memory"); kvok = ((u32)__builtin_amdgcn_ballot_w64(fl < (u32)s + 2) & 7u) == 0u; }
            if constexpr (i == 10) { kvo = l16 | (kvok ? 0u : OOB); PIN(kvo); }
            SCHED();
        SEND
        asm volatile("s_waitcnt lgkmcnt(0)\n\ts_barrier" : "+s"(clk) :: "memory");
        const v2u tvm = *(LAS v2u*)(VMX + (512 - (w & 1) * 1024));
        const v4u pv = lds_r16(ZP + 80);
        DMA32(dk, kvo, ksrc);
        SCHED(); asm volatile("" :: "v"(cm));
        float u[10], m, m2;
        {
            float NEG = -1000000000.0f / SCALE; PIN(NEG);
#define MSK(d, a, i) asm volatile("v_cndmask_b32 %0, %1, %2, %3" : "=v"(d) : "v"(NEG), "v"(a), "s"(vis[i]))
#pragma unroll
            for (int i = 0; i < 4; ++i) MSK(u[i], c0[i], i);
#pragma unroll
            for (int i = 0; i < 4; ++i) MSK(u[4 + i], c1[i], 4 + i);
            asm volatile("v_max3_f32 %0, %2, %3, %10\n\tv_max3_f32 %1, %4, %5, %11\n\tv_max3_f32 %0, %0, %6, %7\n\tv_max3_f32 %1, %1, %8, %9\n\t"
                         "v_max_f32 %0, %0, %1\n\tv_mov_b32 %1, %0\n\ts_nop 1\n\tv_permlane16_swap_b32 %0, %1\n\tv_max_f32 %0, %0, %1\n\tv_mov_b32 %1, %0\n\ts_nop 1\n\t"
                         "v_permlane32_swap_b32 %0, %1\n\tv_max_f32 %0, %0, %1\n\tv_mul_f32 %0, 0xbe0293ee, %0"
                         : "=&v"(m), "=&v"(m2) : "v"(u[0]), "v"(u[1]), "v"(u[2]), "v"(u[3]), "v"(u[4]), "v"(u[5]), "v"(u[6]), "v"(u[7]), "v"(cm[0]), "v"(cm[1]));
        }
        DMA32(dv, kvo, vsrc);
        {
            v4u px; v2u p0, p1;
#define EX2(d, a, b) asm volatile("v_fmamk_f32 %0, %2, 0x3e0293ee, %4\n\tv_fmamk_f32 %1, %3, 0x3e0293ee, %4\n\tv_exp_f32 %0, %0\n\tv_exp_f32 %1, %1" : "=&v"(d.x), "=&v"(d.y) : "v"(a), "v"(b), "v"(m))
            f32x2 ex[5];
            EX2(ex[0], u[0], u[1]); EX2(ex[1], u[2], u[3]); EX2(ex[2], u[4], u[5]); EX2(ex[3], u[6], u[7]); EX2(ex[4], cm[0], cm[1]);
            asm volatile("v_cvt_pk_bf16_f32 %0, %8, %9\n\tv_cvt_pk_bf16_f32 %1, %10, %11\n\tv_cvt_pk_bf16_f32 %2, %12, %13\n\tv_cvt_pk_bf16_f32 %3, %14, %15\n\t"
                         "v_mov_b32 %5, 0\n\tv_mov_b32 %6, 0\n\tv_cvt_pk_bf16_f32 %4, %16, %17\n\tv_cvt_pk_bf16_f32 %7, %16, %17"
                         : "=&v"(px.x), "=&v"(px.y), "=&v"(px.z), "=&v"(px.w), "=&v"(p0.x), "=&v"(p0.y), "=&v"(p1.x), "=&v"(p1.y)
                         : "v"(ex[0].x), "v"(ex[0].y), "v"(ex[1].x), "v"(ex[1].y), "v"(ex[2].x), "v"(ex[2].y), "v"(ex[3].x), "v"(ex[3].y), "v"(ex[4].x), "v"(ex[4].y));
            f32x4 o[4], sum;
            const v2u ones2 = ones.lo;
            const u32 now = (u32)clk;
            u32 oz;
            SCHED();
            MF0(sum, ones, px); { const u32 q = ksrc ^ 8192u; DMA32(dq, kvo, q); } SCHED();
            MF0(o[0], vx[0], px);
            MF0(o[1], vx[1], px);
            MF0(o[2], tv[0], px); oz = (oso & ~(3u << 17)) | (u32)((s + 3) & 3) << 17; PIN(oz); SCHED();
            MF0(o[3], tv[1], px); oso = (oso & ~(3u << 17)) | (u32)((s + 1) & 3) << 17; PIN(oso); SCHED();
            MF1H(sum, ones2, p0);
            MF1H(o[0], vm, p0);
            MF1H(o[1], vm, p1); st16(rs, oz, pv); SCHED();
            MF1H(o[2], tvm, p0);
            MF1H(o[3], tvm, p1);
            asm volatile("s_nop 2\n\tv_rcp_f32 %0, %1\n\ts_nop 0" : "=v"(inv) : "v"(sum[0]), "v"(sum));
            asm volatile("" : "+v"(o[0]), "+v"(o[1]));
            st16(rs, oo, (v4u){pk2(o[0].lo * inv), pk2(o[0].hi * inv), pk2(o[1].lo * inv), pk2(o[1].hi * inv)});
            asm volatile("" : "+v"(o[2]), "+v"(o[3]));
            st16(rs, oo ^ 2048u, (v4u){pk2(o[2].lo * inv), pk2(o[2].hi * inv), pk2(o[3].lo * inv), pk2(o[3].hi * inv)});
            SCHED();
            LS2(L == 0 && w == 0, h);
            st4s(rs, ((h & 15) == 0 && tid == 192) ? hint + 128u + (u32)h * 8u : OOB, 0, (u32)s << 16 | min(missed, 1u) << 14 | min(now - tm, 0x3FFFu));
            tm = rdl(fl, 5);
            missed = missed == SPIN_CAP ? missed : 0u;
            if (__builtin_expect((int)(rdl(fl, 3) - now) < CH_LATE || s < 1 - L, 0)) {
                const bool first = s == 0 && L == 0;
                LAD(w == 0 && s > 0, 3, L * 64 + s);
                if (s < 0) oso -= (ln & 256u) ? 0u : OOB;
#pragma unroll 1
                for (int n = 0; n < (s < 0 ? 0 : first ? 200 : 8); ++n) {
                    CFENCE();
                    const u32 hv = ld4(rs, fkh);
                    tm = rdl(hv, first ? 6 : 5);
                    if (first ? tm != PSENT : (int)(rdl(hv, 3) - now) >= CH_LATE) break;
                }
            }
        }
        if (__builtin_expect(!kvok, 0) && s + 1 < NSEG) {
            st4s(rs, ln == 0 ? hint + 4096u + (u32)h * 128u : OOB, 0, (u32)s + 1u);
            LAD(true, 0, L * 64 + s + 1);
            keys_wait((u32)s + 2u);
        }
        ++s;
    }
    DRAIN(); BAR();
    st4s(rs, ofl, 0, NSEG);
}

#define WO_NL 3
#define WO_ROT 0
#define WO_AH 1
DEV void role_wo(const Params& p, lptr lds, int j, v2u ep) {
    const int tid = threadIdx.x, lane = tid & 63, w = uni(tid >> 6), g = lane >> 4, r = lane & 15;
    const int rg = j & 1, cg = j >> 1, rot = WO_ROT;
    const rsrc_t rs = mk_rsrc(p.ws, W_END);
    v8u wo[8][5];
    {
        const v4u so_ = mk_srd(p.Wo, DM * NQ * 2);
        u32 vo[5];
#pragma unroll
        for (int i = 0; i < 5; ++i) { const int n = 64 * i + lane; vo[i] = (u32)((n / 5) * DM + cg * 40 + (n % 5) * 8) * 2u; }
        v4u junk = {};
        const u32 av = (u32)(j * 5120 + lane * 16);
        auto ahead = [&](int b) {
#pragma unroll
            for (int i = 0; i < 5; ++i) AHEAD(so_, av, uu((8 * w + b) * (DH * DM * 2) + i * 1024));
        };
        auto fill = [&](int b) {
            if (b + WO_AH < 8) ahead(b + WO_AH);
            const u32 dst = uu(w * 40960 + (b & 3) * 10240), src = uu((8 * w + ((b + rot) & 7)) * (DH * DM * 2));
#pragma unroll
            for (int i = 0; i < 10; ++i) DMAIN(so_, dst + i * 1024, vo[i % 5], src + (i / 5) * (64 * DM * 2));
        };
#pragma unroll 1
        for (int b = 0; b < WO_AH; ++b) ahead(b);
#pragma unroll 1
        for (int b = 0; b < 4; ++b) fill(b);
        const int q4 = r >> 2;
        lptr tb = lds + w * 40960 + (32 * (g >> 1) + 16 * (q4 >> 1) + 8 * ((r >> 1) & 1) + 4 * (g & 1) + (q4 & 1)) * 80 + (r & 1) * 8;
        SFOR(b, 8)
            VMW((b < 5 ? 30 : 70 - 10 * b) + (b + 1 + WO_AH < 8 ? 5 : 0) + (b + 2 + WO_AH < 8 ? 5 : 0) + (b + 3 + WO_AH < 8 ? 5 : 0));
            if constexpr (b + WO_AH == 7) PIN(junk);
#pragma unroll
            for (int t = 0; t < 5; ++t) {
                lptr q = tb + (b & 3) * 10240 + t * 16;
                wo[b][t] = tr_frag(q, q + 160, q + 64 * 80, q + 64 * 80 + 160);
                if (b * 5 + t < 32) asm volatile("" : "+a"(wo[b][t])); else asm volatile("" : "+v"(wo[b][t]));
            }
            CFENCE();
            if constexpr (b < 4) fill(b + 4);
        SEND
    }
    if (j == 0) LST(265, rt32());
    LST(1100 + j * 4 + w, rt32());
    const u32 idx = (r & 2) ? 0xEEEEu : 0x4444u;
    const u32 pol0 = pol_wait(rs, ep);
    BAR();
    const u32 l16 = (u32)lane * 16u;
    u32 oo[8];
#pragma unroll
    for (int i = 0; i < 8; ++i) oo[i] = uu(W_ORING + ring_map((u32)(rg * 32768 + (8 * w + ((i + rot) & 7)) * 1024), pol0));
    const u32 hint = W_MRING + 4 * 32768u + pol0 * 8192u;
    u32 hvo[2];
#pragma unroll
    for (int l = 0; l < 2; ++l) hvo[l] = lane < 2 ? hint + (u32)(l ^ 1) * (MB / 4) + (u32)lane * 4u : lane == 2 ? hint + (u32)l * (MB / 4) + 128u : lane == 3 ? foff(F_ERR) : lane == 4 ? hint + (u32)l * (MB / 4) + 256u :
                 (lane >= 32 && j == 0 && w == 3) ? hint + (u32)l * (MB / 4) + (u32)lane * 128u : OOB;
    lptr xw = r < 8 ? lds + w * 640 + (2 * (g & 1) + (g >> 1)) * 32 + r * 4 : lds + 8192 + lane * 4;
    const int G = ((5 * cg) >> 1) + w, tt = 2 * G + ((lane >> 3) & 1) - 5 * cg;
    const bool pub = w < 3 && lane < 32 && tt >= 0 && tt < 5;
    lptr xr = lds + (pub ? tt * 128 + (2 * (lane >> 4) + ((lane >> 2) & 1)) * 32 + (lane & 3) * 8 : 0);
    const u32 mpo = pub ? W_MRING + ring_map((u32)(G * 256 + rg * 128 + lane * 4), pol0) : OOB;
    constexpr u32 PER0 = CH_W0 + CH_MW + CH_H0 + CH_MH + 2 * CH_VIS, DW0 = CH_W0 + CH_VIS + CH_MW + CH_REC;
    const bool lead = j == 0 && w == 3;
    const u32 hso = (lead && lane == 0) ? hint : OOB;
    MK_SRD();
    u32 t = 0, tn = 0;
    int sn[2] = {0, 0};
    u32 hav = 8 * CH_H0, wav = 8 * CH_W0, mh = 8 * CH_MH, mw = 8 * CH_MW, ok[2] = {0u, 0u}, dw[2] = {DW0, DW0};
    u32 vo = l16, early = PSENT, tp = 0, fin, left, cnt = 0;
    unsigned long long clk = 0;
    {
        if (w == 0) LST(1310 + j, rt32());
        if (lead) {
#pragma unroll 1
            for (int n = 0; n < SPIN_CAP; ++n) {
                CFENCE();
                const u32 a = ld4(rs, foff(F_QKV + lane)), b = ld4(rs, lane < 32 ? foff(F_QKV + 64 + lane) : hvo[0]);
                if (all_(a != 0u && b != (lane < 32 ? 0u : PSENT))) break;
                __builtin_amdgcn_s_sleep(1);
            }
        }
        if (lead) {
            t = rt32() + CH_T0; tn = t + PER0 / 2 + CH_REC;
            st16(rs, lane < 2 ? hint + (u32)lane * (MB / 4) : OOB, (v4u){lane == 0 ? t : tn, PER0, (lane == 0 ? t : tn) + DW0, t + DW0});
        } else {
#pragma unroll 1
            for (int n = 0; n < SPIN_CAP; ++n) {
                CFENCE();
                const u32 hv = ld4(rs, hvo[1]);
                t = rdl(hv, 0);
                if (rdl(hv, 1) < 400u) break;
                __builtin_amdgcn_s_sleep(1);
            }
        }
        clk = t - 100u;
    }
    const u32 tm0 = t + DW0;
    u32 zero = 0, hv = 0xFFFFu, pst = 1, pmp = OOB;
    const u32 psent = PSENT;
    lptr sw = lds + 2560 + w * 4;
    asm volatile("" : "+v"(zero), "+v"(sw));
    auto turn = [&](auto lc) __attribute__((always_inline)) {
        constexpr int L = decltype(lc)::value, M = L ^ 1;
        u32 done, rep, rd, lost, dh, dn, nt, za, pdur, mp; v4u hw;
        const int sc = sn[L];
        auto books = [&](auto ic) __attribute__((always_inline)) {
            constexpr int i = decltype(ic)::value;
            if constexpr (i == 0) { rep = max(rdl(hv, 2), rdl(hv, 4)); PINS(rep); }
            if constexpr (i == 1) { lost = (u32)(__builtin_amdgcn_ballot_w64(hv == (u32)sn[M]) >> 32); PINS(lost); }
            if constexpr (i == 2) { lost |= (rep >> 16) ^ (u32)sn[M]; PINS(lost); }
            if constexpr (i == 3) { lost |= ok[M] ^ 2u; rd = rep & 0xFFFFu; PINS(lost); PINS(rd); }
            if constexpr (i == 4) { lost = lost == 0u ? CH_UP * 8u : 0u; PINS(lost); }
            if constexpr (i == 5) { pdur = ((u32)clk - tp) * 8u; PINS(pdur); pdur = min(pdur, wav + CH_RISE); PINS(pdur); }
            if constexpr (i == 6) { pdur = (done & ok[M]) ? pdur : wav; PINS(pdur); }
            if constexpr (i == 7) { wav = max(pdur, wav - CH_FALL); PINS(wav); }
            if constexpr (i == 8) { nt = min(rd * 8u, hav + CH_RISE); PINS(nt); }
            if constexpr (i == 9) { nt = rd < 0x3FFFu ? nt : hav; PINS(nt); }
            if constexpr (i == 10) { hav = max(nt, hav - CH_FALL); PINS(hav); }
            if constexpr (i == 11) { mw = max(mw - 1u, CH_MW * 8u); PINS(mw); }
            if constexpr (i == 12) { mh = max(mh - 1u, CH_MH * 8u); PINS(mh); }
            if constexpr (i == 13) { rd = rd >> 14 == 1u ? lost : 0u; PINS(rd); }
            if constexpr (i == 14) { mw += rd; mh += lost - rd; PINS(mw); PINS(mh); }
            if constexpr (i == 15) { ok[M] = done ? 1u : (ok[M] & 1u) << 1; PINS(ok[M]); }
            if constexpr (i == 16) { dh = (hav + mh) / 8u + (sn[1] < 3 ? CH_VIS + CH_PRE : CH_VIS); PINS(dh); }
            if constexpr (i == 17) { dn = (wav + mw) / 8u + CH_VIS; PINS(dn); }
            if constexpr (i == 18) { nt = dw[L] + dh + (u32)max((int)((u32)clk + CH_WX - t), 0); PINS(nt); }
            if constexpr (i == 19) { dh += dn; dw[L] = dn; sn[M] += (int)done; PINS(dh); PINS(sn[M]); }
            if constexpr (i == 20) { rd = tn - t + (dh >> 1); PINS(rd); }
            if constexpr (i == 22) { nt = t + (u32)max((int)nt, (int)rd); PINS(nt); }
            if constexpr (i == 23) { hw.x = nt; hw.y = dh; PIN(hw.x); PIN(hw.y); }
            if constexpr (i == 24) { hw.z = nt + dn; hw.w = tm0; PIN(hw); }
            if constexpr (i == 25) st16(rs, hso + L * (MB / 4), hw);
        };
        {
#define WT(k) do {} while (0)
            WT(0);
            v4u d[8];
            {
                u32 tmp;
                asm volatile(WAIT_TXT("%1", "%2") "buffer_load_dwordx4 %0, %3, %4, %5 offen sc1\n\ts_waitcnt lgkmcnt(0)"
                             : "=&v"(d[0]), "=&s"(tmp) : "s"(uu(t - 4u)), "v"(vo), "s"(srd), "s"(oo[0]) : "vcc", "scc", "memory");
                PINS(clk);
            }
            SFOR(i, 7)
                asm volatile("buffer_load_dwordx4 %0, %1, %2, %3 offen sc1" : "=v"(d[i + 1]) : "v"(vo), "s"(srd), "s"(oo[i + 1]) : "memory");
                SCHED();
                if constexpr (i == 0) { done = uu(pst) ^ 1u; PINS(done); }
                static_for<3 * i, 3 * i + 3>(books);
                SCHED();
            SEND
            LD4A(hv, hvo[L], 0);
            SCHED();
            WT(1);
            static_for<22, 26>(books);
            za = done ? pmp : OOB;
            st4s(rs, za, 0, psent);
            *(LAS u32*)(sw + L * 4096) = zero;
            f32x4 acc[5] = {};
            mp = mpo + (L * (MB / 4) + (u32)((sc + 1) & 3) * 32768u); pmp = mpo + (L * (MB / 4) + (u32)(sc & 3) * 32768u);
            asm volatile("" : "+v"(acc[0]), "+v"(acc[1]), "+v"(acc[2]), "+v"(acc[3]), "+v"(acc[4]), "+v"(mp), "+v"(pmp));
            SCHED();
            float f0[5], f1[5]; unsigned long long bad;
#define WSW(o_) asm volatile("v_permlane32_swap_b32 %0, %1" : "+v"(f0[o_]), "+v"(f1[o_]))
#define WAD(o_) asm volatile("v_add_f32_dpp %0, %1, %0 row_ror:8 row_mask:0xf bank_mask:0xf" : "+v"(f0[o_]) : "v"(f1[o_]))
#define WFO(o_) do { VADD(f0[o_], acc[o_][0], acc[o_][2]); VADD(f1[o_], acc[o_][1], acc[o_][3]); } while (0)
#define WWR(o_) *(LAS float*)(xw + L * 4096 + (o_) * 128) = f0[o_]
            u32 th, per, so, erl = early;
            auto next = [&](auto ic_) __attribute__((always_inline)) {
                constexpr int i = decltype(ic_)::value;
                if constexpr (i == 0) { so = (u32)sn[M] << 17; PINS(so); }
                if constexpr (i == 1) { so = (so & 0x60000u) + M * (MB / 2); PINS(so); }
                if constexpr (i == 2) { so = sn[M] + 1 < NSEG ? so : OOB; PINS(so); }
                if constexpr (i == 3) { early = sn[M] + 1 < NSEG ? PSENT : 0u; PIN(early); }
                if constexpr (i == 4) { vo = l16 + so; PIN(vo); }
                if constexpr (i == 5 && L) { fin = (u32)(sn[0] + sn[1] + 2); PINS(fin); }
                if constexpr (i == 6 && L) { fin = (fin | uu(left)) >> 7; PIN(fin); }
                if constexpr (i == 6 && !L) { cnt += 1u; PIN(cnt); left = cnt >> 9; PIN(left); }
                if constexpr (i == 7) { PIN(hv); th = rdl(hv, 0); PINS(th); }
                if constexpr (i == 8) { per = rdl(hv, 1); PINS(per); so = th - t; PINS(so); }
                if constexpr (i == 9) { per = (int)so < 20 ? per : 0u; PINS(per); }
                if constexpr (i == 10) { th += per; PINS(th); if constexpr (L) { per = rdl(hv, 3); PINS(per); } }
                if constexpr (i == 11) { tp = t; t = lead ? tn : th; PINS(t); }
                if constexpr (i == 12) { if constexpr (L) { fin |= per; PIN(fin); } tn = nt; }
            };
            auto slot = [&](auto ic_) __attribute__((always_inline)) {
                constexpr int i = decltype(ic_)::value;
                constexpr int NL = WO_NL, I0 = 40 - 5 * NL, pc = i < I0 ? i / 5 : 8 - NL + (i - I0) % NL, oc = i < I0 ? i % 5 : (i - I0) / NL;
                if constexpr (oc == 0) asm volatile("s_waitcnt vmcnt(%1)" : "+v"(d[pc]) : "i"(10 - pc));
                if constexpr (pc * 5 + oc < 32) SMF_A(acc[oc], d[pc], wo[pc][oc]); else SMF_V(acc[oc], d[pc], wo[pc][oc]);
                SCHED();
                if constexpr (i == 0) WT(2);
                if constexpr (i >= 5 && i < 13 && i != 9) next(IC<i - 5 - (i > 9)>{});
                if constexpr (i == 20) WT(4);
                constexpr int e = i - (I0 + NL + 1), o = e < 0 ? 0 : e / 3;
                if constexpr (e >= 0 && e % 3 == 0) { CADD(f0[o], acc[o][0], acc[o][2]); if constexpr (o > 0) WAD(o - 1); }
                if constexpr (e >= 0 && e % 3 == 1) { if constexpr (o > 0) { WWR(o - 1); SCHED(); } CADD(f1[o], acc[o][1], acc[o][3]); }
                if constexpr (e >= 0 && e % 3 == 2) {
                    if constexpr (o == 0) asm volatile("v_cmp_u_f32 %0, %1, %2" : "=s"(bad) : "v"(f0[0]), "v"(f1[0]));
                    else LGK(15);
                    WSW(o);
                }
                SCHED();
            };
            asm volatile(".rept %0\n\ts_nop 0\n\t.endr" :: "i"(AL_W));
            static_for<0, 2>(slot);
            if (__builtin_expect(__builtin_amdgcn_ballot_w64(d[0].x == erl) != 0ull, 0)) {
                asm volatile("s_waitcnt vmcnt(3)" :: "v"(d[1]), "v"(d[2]), "v"(d[3]), "v"(d[4]), "v"(d[5]), "v"(d[6]), "v"(d[7]) : "memory");
                SCHED();
                *(LAS u32*)(sw + L * 4096) = 1u;
                static_for<0, 7>(next);
            } else {
            static_for<2, 40>(slot);
            WT(5);
            static_assert(WO_NL == 3, "tail");
            if (__builtin_expect(bad != 0ull, 0)) {
                bool miss = false;
#pragma unroll
                for (int i = 0; i < 8; ++i) miss |= d[i].x == PSENT;
                if (all_(!miss)) {
#pragma unroll
                    for (int i = 0; i < 8; ++i) miss |= d[i].y == PSENT || d[i].z == PSENT || d[i].w == PSENT;
                }
                *(LAS u32*)(sw + L * 4096) = all_(!miss) ? 0u : 1u;
            }
            asm volatile("v_permlane32_swap_b32 %0, %1\n\ts_nop 1\n\tv_add_f32_dpp %0, %1, %0 row_ror:8 row_mask:0xf bank_mask:0xf\n\tv_add_f32 %2, %4, %6\n\tv_add_f32 %3, %5, %7"
                         : "+v"(f0[3]), "+v"(f1[3]), "=&v"(f0[4]), "=&v"(f1[4]) : "v"(acc[4][0]), "v"(acc[4][1]), "v"(acc[4][2]), "v"(acc[4][3]));
            WWR(3); SCHED();
            asm volatile("s_nop 0\n\tv_permlane32_swap_b32 %0, %1\n\ts_nop 1\n\tv_add_f32_dpp %0, %1, %0 row_ror:8 row_mask:0xf bank_mask:0xf" : "+v"(f0[4]), "+v"(f1[4]));
            WWR(4);
            }
            WT(6);
            f32x2 a, b, c, e; u32 mq; v4u sv;
            asm volatile("s_waitcnt lgkmcnt(0)\n\ts_barrier\n\tds_read_b128 %4, %6 offset:%7+2560\n\t"
                         "ds_read_b64 %0, %5 offset:%7\n\tds_read_b64 %1, %5 offset:%7+640\n\tds_read_b64 %2, %5 offset:%7+1280\n\tds_read_b64 %3, %5 offset:%7+1920"
                         : "=&v"(a), "=&v"(b), "=&v"(c), "=&v"(e), "=&v"(sv) : "v"(xr), "v"(zero), "i"(L * 4096) : "memory");
            asm volatile("s_waitcnt lgkmcnt(4)\n\tv_or_b32 %4, %6, %7\n\tv_or3_b32 %4, %4, %8, %9\n\tv_lshl_or_b32 %5, %4, 31, %10\n\t"
                         "s_waitcnt lgkmcnt(2)\n\tv_pk_add_f32 %0, %0, %1\n\ts_waitcnt lgkmcnt(0)\n\tv_pk_add_f32 %2, %2, %3\n\ts_nop 0\n\tv_pk_add_f32 %0, %0, %2"
                         : "+v"(a), "+v"(b), "+v"(c), "+v"(e), "=&v"(pst), "=&v"(mq) : "v"(sv.x), "v"(sv.y), "v"(sv.z), "v"(sv.w), "v"(mp) : "memory");
            st4s(rs, mq, 0, pkv(a));
            SCHED();
            asm volatile("s_memrealtime %0" : "=s"(clk));
            VMW(3); SCHED();
            static_for<7, 13>(next);
            WT(7);
        }
    };
    do {
        turn(IC<0>{});
        turn(IC<1>{});
    } while (uu(fin) == 0u);
    if (sn[0] + 1 < NSEG || sn[1] + 1 < NSEG) st4s(rs, tid == 0 ? foff(F_ERR) : OOB, 0, 1u);
    if (w == 0) LST(1900 + j, rt32());
}

__global__ void __launch_bounds__(256, 1) k_main(Params p) {
    lptr lds = (lptr)(u32)16 - 16;
    if (blockIdx.x == 0) LST(256, rt32());
    unsigned long long pc; int len;
    asm volatile("s_getpc_b64 %0\n.Lk_main_here:\n\ts_mov_b32 %1, .Lk_end_here-.Lk_main_here" : "=s"(pc), "=s"(len));
    const rsrc_t rs = mk_rsrc(p.ws, W_END);
    const v2u e0 = ld8s(rs, threadIdx.x == 0 ? foff(F_EPOCH) : OOB, 0);
    const v4u code = __builtin_amdgcn_raw_buffer_load_b128(mk_rsrc((const void*)(pc & ~15ull), (u32)max(len, 0)), (((blockIdx.x >> 3) & 31u) * 256u + threadIdx.x) * 16u, 0, 0);
    CFENCE();
    auto conv_id = [](int role) {
        const int x = role >> 5, i = role & 31, no = x < 5 ? 5 : 10, og = x < 5 ? x - 3 : 2 * (x - 4) + (i >= 5), r = (x - 3) * 5 + i - no, q = r / 3;
        return x < 3 || i >= no + 5 || r >= 24 ? NCV : i < no ? ((og & 1) ? (og >> 1) * 5 + i % 5 : NCV) : r % 3 == 0 ? 20 + (q & 1) * 20 + (q >> 1) : 16 + (r % 3) * 8 + q;
    };
    int role = (int)(((u32)__builtin_amdgcn_s_getreg(20 | (3 << 11)) & 7u) * 32u + (blockIdx.x >> 3));
    Early e;
    v4u rv;
#pragma unroll 1
    for (int n = 0;; ++n) {
        conv_early(p, rs, conv_id(role), e);
        if (n) break;
        CFENCE();
        if (threadIdx.x == 0) {
            GAS unsigned long long* slots = (GAS unsigned long long*)(p.ws + W_CTRL + F_SLOT * 128);
            const unsigned long long tag = (unsigned long long)e0.y << 32 | (e0.x + 1u);
            u32 mine = (u32)role;
#pragma unroll 1
            for (int k = 0; k < 256 && __hip_atomic_exchange(slots + mine * 16, tag, __ATOMIC_RELAXED, __HIP_MEMORY_SCOPE_AGENT) == tag; ++k) mine = (mine + 1u) & 255u;
            lds_w16(lds, (v4u){mine, e0.x, e0.y, 0u});
        }
        BAR();
        rv = lds_r16(lds);
        BAR();
        if (uni((int)rv.x) == role) break;
        role = uni((int)rv.x);
    }
    const int x = role >> 5, i = role & 31, w = uni(threadIdx.x >> 6), no = x < 5 ? 5 : 10, nr = x < 7 ? 5 : 4, j = conv_id(role);
    const v2u ep = {uu(rv.y), uu(rv.z)};
    asm volatile("" :: "v"(code));
    if (x < 2) { role_head(p, lds, i >> 4, x * 16 + (i & 15), ep); __builtin_amdgcn_endpgm(); }
    if (x == 2) { role_wo(p, lds, i, ep); __builtin_amdgcn_endpgm(); }
    if (i >= no + nr) { role_qkv(p, lds, (x < 5 ? (x - 3) * 22 : 44 + (x - 5) * 17) + i - no - nr, ep); __builtin_amdgcn_endpgm(); }
    const bool cv = j >= NPRE || (j >= 20 && j < 24);
    if (j < NCV) conv_stage(p, rs, lds, j, (j & 3) * 2 + (j >= NPRE), cv ? 40 : 0, ep, e);
    if (i >= no) {
        if (!cv) role_row(p, lds, (j - 24) * 4 + w, ep);
        __builtin_amdgcn_endpgm();
    }
    const int og = x < 5 ? x - 3 : 2 * (x - 4) + (i >= 5);
    role_out(p, lds, og & 1, og >> 1, i % 5, ep);
}
__global__ void k_end() { asm volatile(".Lk_end_here:"); }

extern "C" void kernel_launch(void* const* d_in, const int* in_sizes, int n_in, void* d_out, int out_size, void* d_ws, size_t ws_size,
                              hipStream_t stream) {
    Params p;
    p.x = (const u16*)d_in[0]; p.mem0 = (const u16*)d_in[1]; p.Wq = (const u16*)d_in[2]; p.Wk = (const u16*)d_in[3];
    p.Wv = (const u16*)d_in[4]; p.Wo = (const u16*)d_in[5];
    p.bk = (const u16*)d_in[8]; p.bv = (const u16*)d_in[9]; p.shift = (const u16*)d_in[10]; p.ssmk = (const u16*)d_in[11]; p.ssmD = (const u16*)d_in[12];
    p.out = (u16*)d_out;
    p.ws = (unsigned char*)(((size_t)d_ws + 2 * MB - 1) & ~(size_t)(2 * MB - 1));
    (void)hipFuncSetAttribute((const void*)k_main, hipFuncAttributeMaxDynamicSharedMemorySize, LDS_BYTES);
    hipLaunchKernelGGL(k_main, dim3(256), dim3(256), LDS_BYTES, stream, p);
}
```

```cpp
#include <hip/hip_runtime.h>
#include <hip/hip_bf16.h>

typedef unsigned short u16;
typedef unsigned int u32;
typedef u32 v2u __attribute__((ext_vector_type(2)));
typedef u32 v4u __attribute__((ext_vector_type(4)));
typedef u32 v8u __attribute__((ext_vector_type(8)));
typedef float f32x2 __attribute__((ext_vector_type(2)));
typedef float f32x4 __attribute__((ext_vector_type(4)));
typedef float f32x16 __attribute__((ext_vector_type(16)));
typedef __bf16 bf16x2 __attribute__((ext_vector_type(2)));
typedef __bf16 bf16x8 __attribute__((ext_vector_type(8)));
typedef short s16x4 __attribute__((ext_vector_type(4)));
#define LAS __attribute__((address_space(3)))
#define GAS __attribute__((address_space(1)))
typedef LAS unsigned char* lptr;
#define DEV __device__ __forceinline__

constexpr int SEQ = 2048, DM = 640, NH = 32, DH = 128, NSEG = 64, NQ = NH * DH;

constexpr u32 MB = 1u << 20;
constexpr u32 W_ORING = 0;
constexpr u32 W_MRING = MB;
constexpr u32 W_PROBE = 2 * MB - 65536;
constexpr u32 W_CTRL = 4 * MB;
constexpr u32 CTRL_BYTES = 128 * 1024;
constexpr u32 ACT = SEQ * DM * 2;
constexpr u32 W_H1 = 5 * MB, W_Y = W_H1 + ACT, W_H2 = W_Y + ACT, W_X2 = W_H2 + ACT;
constexpr u32 W_KQ = 16 * MB;
constexpr u32 W_VO = W_KQ + 64 * MB;
constexpr u32 W_P = W_VO + 64 * MB;
constexpr u32 P_ITEM = 16 * 4 * DM * 4;
constexpr u32 W_KVT = W_P + 4 * NSEG * P_ITEM;
constexpr u32 W_SP = W_KVT + ACT;
constexpr u32 W_END = W_KVT + 3 * MB;
static_assert((size_t)W_END + 2 * MB <= 256ull * MB, "workspace");
constexpr u32 F_ERR = 0, F_QKV = 16, F_O = F_QKV + 192, F_P = F_O + 64, F_H2 = F_P + 64, F_CLR = F_H2 + 32,
              F_EPOCH = F_CLR + 32, F_SLOT = F_EPOCH + 1, F_PRE = F_SLOT + 256, F_N = F_PRE + 192;
constexpr int NCV = 44, NPRE = 40;
static_assert(F_N * 128 <= CTRL_BYTES, "ctrl");
DEV u32 foff(u32 idx) { return W_CTRL + idx * 128u; }

constexpr u32 OOB = 0x80000000u;
constexpr u32 PSENT = 0x7F817F81u;
constexpr int SPIN_CAP = 40000;
constexpr int LDS_BYTES = 160 * 1024;

#define LDUMP(cond, id) do {} while (0)
#define LAD(cond, k, s_) do {} while (0)
#define LMX(cond, k, s_, v) do {} while (0)
#define LS3(cond, slot, idx) do {} while (0)
#define LST(idx, v) do {} while (0)
#define LS2(cond, idx) do {} while (0)
struct Params {
    const u16 *x, *mem0, *Wq, *Wk, *Wv, *Wo, *bk, *bv, *shift, *ssmk, *ssmD;
    u16* out;
    unsigned char* ws;
};

typedef __amdgpu_buffer_rsrc_t rsrc_t;
DEV rsrc_t mk_rsrc(const void* p, u32 bytes) { return __builtin_amdgcn_make_buffer_rsrc((void*)p, 0, bytes, 0x00020000); }
DEV v4u ld16s(rsrc_t r, u32 off, u32 so) { return __builtin_amdgcn_raw_buffer_load_b128(r, off, so, 16); }
DEV v2u ld8s(rsrc_t r, u32 off, u32 so) { return __builtin_amdgcn_raw_buffer_load_b64(r, off, so, 16); }
DEV v4u ldin16(rsrc_t r, u32 off, u32 so) { return __builtin_amdgcn_raw_buffer_load_b128(r, off, so, 2); }
DEV v2u ldin8(rsrc_t r, u32 off, u32 so) { return __builtin_amdgcn_raw_buffer_load_b64(r, off, so, 2); }
DEV u32 ld4(rsrc_t r, u32 off) { return __builtin_amdgcn_raw_buffer_load_b32(r, off, 0, 16); }
DEV void st16(rsrc_t r, u32 off, v4u v) { __builtin_amdgcn_raw_buffer_store_b128(v, r, off, 0, 16); }
DEV void st8s(rsrc_t r, u32 off, u32 so, v2u v) { __builtin_amdgcn_raw_buffer_store_b64(v, r, off, so, 16); }
DEV void st4s(rsrc_t r, u32 off, u32 so, u32 v) { __builtin_amdgcn_raw_buffer_store_b32(v, r, off, so, 16); }
#define DRAIN() asm volatile("s_waitcnt vmcnt(0)" ::: "memory")
#define VMW(n) asm volatile("s_waitcnt vmcnt(%0)" :: "i"(n) : "memory")
#define BAR() asm volatile("s_waitcnt lgkmcnt(0)\n\ts_barrier" ::: "memory")
#define SCHED() __builtin_amdgcn_sched_barrier(0)
#define CFENCE() asm volatile("" ::: "memory")
#define NOPS(n) asm volatile("s_nop %0" :: "i"(n))
#define PIN(x) asm volatile("" : "+v"(x))
#define PINS(x) asm volatile("" : "+s"(x))

template <int V> struct IC { static constexpr int value = V; };
template <int I, int N, class F> DEV void static_for(F&& f) { if constexpr (I < N) { f(IC<I>{}); static_for<I + 1, N>(f); } }
#define SFOR(var, n) static_for<0, n>([&](auto ic_) __attribute__((always_inline)) { constexpr int var = decltype(ic_)::value;
#define SEND });

DEV u32 pk(float a, float b) { bf16x2 v = {(__bf16)a, (__bf16)b}; return __builtin_bit_cast(u32, v); }
DEV float lo_f(u32 u) { return __builtin_bit_cast(float, u << 16); }
DEV float hi_f(u32 u) { return __builtin_bit_cast(float, u & 0xffff0000u); }
DEV f32x2 up2(u32 u) { return (f32x2){lo_f(u), hi_f(u)}; }
DEV u32 pk2(f32x2 v) { return pk(v.x, v.y); }
DEV float rbf(float a) { return (float)(__bf16)a; }
DEV v4u lds_r16(lptr p) { return *(LAS v4u*)p; }
DEV void lds_w16(lptr p, v4u v) { *(LAS v4u*)p = v; }
DEV v2u lds_tr(lptr p) { return __builtin_bit_cast(v2u, __builtin_amdgcn_ds_read_tr16_b64_v4i16((LAS s16x4*)p)); }
DEV v4u tr2(lptr p0, lptr p1) { const v2u a = lds_tr(p0), b = lds_tr(p1); return (v4u){a.x, a.y, b.x, b.y}; }
DEV v8u cat8(v4u a, v4u b) { return __builtin_shufflevector(a, b, 0, 1, 2, 3, 4, 5, 6, 7); }
DEV f32x4 mfma16(v4u a, v4u b, f32x4 c) { return __builtin_amdgcn_mfma_f32_16x16x32_bf16(__builtin_bit_cast(bf16x8, a), __builtin_bit_cast(bf16x8, b), c, 0, 0, 0); }
DEV f32x16 mfma32(v4u a, v4u b, f32x16 c) { return __builtin_amdgcn_mfma_f32_32x32x16_bf16(__builtin_bit_cast(bf16x8, a), __builtin_bit_cast(bf16x8, b), c, 0, 0, 0); }

DEV float f_(u32 u) { return __builtin_bit_cast(float, u); }
DEV u32 u_(float f) { return __builtin_bit_cast(u32, f); }
template <int CTRL> DEV float dpp(float v) { return f_((u32)__builtin_amdgcn_update_dpp(0, (int)u_(v), CTRL, 0xf, 0xf, false)); }
DEV float max16(float v) { auto r = __builtin_amdgcn_permlane16_swap(u_(v), u_(v), false, false); return fmaxf(f_(r[0]), f_(r[1])); }
DEV float max32(float v) { auto r = __builtin_amdgcn_permlane32_swap(u_(v), u_(v), false, false); return fmaxf(f_(r[0]), f_(r[1])); }
DEV float add16(float v) { auto r = __builtin_amdgcn_permlane16_swap(u_(v), u_(v), false, false); return f_(r[0]) + f_(r[1]); }
DEV float add32(float v) { auto r = __builtin_amdgcn_permlane32_swap(u_(v), u_(v), false, false); return f_(r[0]) + f_(r[1]); }
DEV float wave_sum(float v) {
    v += dpp<0xB1>(v);
    v += dpp<0x4E>(v);
    v += dpp<0x124>(v);
    v += dpp<0x128>(v);
    return add32(add16(v));
}
DEV bool all_(bool c) { return __builtin_amdgcn_ballot_w64(!c) == 0ull; }
DEV int uni(int v) { return __builtin_amdgcn_readfirstlane(v); }
DEV u32 uu(u32 v) { return (u32)__builtin_amdgcn_readfirstlane((int)v); }
DEV u32 rdl(u32 v, int l) { return (u32)__builtin_amdgcn_readlane((int)v, l); }
DEV u32 rt32() { return (u32)__builtin_amdgcn_s_memrealtime(); }

#define WSL 2
DEV void wait_flags(rsrc_t rs, u32 off, u32 want, bool& dead) {
    if (dead) return;
#pragma unroll 1
    for (int n = 0;; ++n) {
        CFENCE();
        const u32 v = ld4(rs, off);
        if (all_(off >= OOB || v >= want)) break;
        __builtin_amdgcn_s_sleep(WSL);
        if ((n & 63) == 63) { if (uni((int)ld4(rs, foff(F_ERR))) != 0) { dead = true; break; } }
        if (n > SPIN_CAP) { st4s(rs, foff(F_ERR), 0, 1u); dead = true; break; }
    }
    CFENCE();
}

DEV void pre_post(rsrc_t rs, int idx, u32 stage, v2u ep) { st8s(rs, threadIdx.x == 0 ? foff(F_PRE + idx) : OOB, 0, (v2u){ep.x + stage, ep.y}); }
DEV void pre_poll(rsrc_t rs, int first, int n, u32 stage, v2u ep) {
    const int lane = threadIdx.x & 63;
#pragma unroll 1
    for (int it = 0; it < SPIN_CAP; ++it) {
        CFENCE();
        const v2u a = ld8s(rs, foff(F_PRE + first + min(lane, n - 1)), 0), b = ld8s(rs, foff(F_PRE + first + min(lane + 64, n - 1)), 0);
        if (all_(a.y == ep.y && b.y == ep.y && max(a.x - ep.x - stage, b.x - ep.x - stage) < 8u - stage)) break;
        __builtin_amdgcn_s_sleep(2);
    }
    CFENCE();
}
DEV void pre_wait(rsrc_t rs, int first, int n, u32 stage, v2u ep) {
    if (uni(threadIdx.x >> 6) == 0) pre_poll(rs, first, n, stage, ep);
    BAR();
}
DEV u32 pol_wait(rsrc_t rs, v2u ep) {
    const int lane = threadIdx.x & 63;
    v4u a;
#pragma unroll 1
    for (int it = 0; it < SPIN_CAP; ++it) {
        CFENCE();
        a = ld16s(rs, foff(F_PRE + (lane < NPRE || lane >= 48 ? lane : 0)), 0);
        if (all_(a.y == ep.y && a.x - ep.x - 1u < 7u)) break;
        __builtin_amdgcn_s_sleep(2);
    }
    CFENCE();
    return uu(wave_sum(lane >= 48 ? (float)(int)a.z : 0.f) >= 0.f ? 1u : 0u);
}
#define PROBE_T 1000
#define NPROBE 32
DEV u32 pol_probe(const Params& p, rsrc_t rs, int id) {
    const int lane = threadIdx.x & 63;
    const u32 off0 = W_PROBE + (u32)(id >> 2) * 16384u + (u32)(id & 3) * 128u;
    const u32 tend = rt32() + PROBE_T;
    st4s(rs, lane < 32 ? off0 + (u32)lane * 512 : OOB, 0, 0u); DRAIN();
    int calm = 0;
    int d[2] = {1 << 16, 1 << 16};
#pragma unroll 1
    for (int q = 0; q < NPROBE && ((q & 1) || (calm < 6 && (int)(rt32() - tend) < 0)); ++q) {
        const u32 half = (u32)(q ^ (q >> 1) ^ id) & 1u;
        const unsigned long long t0 = __builtin_amdgcn_s_memtime();
        const u32 v = ld4(rs, lane == 0 ? off0 + half * 8192 + (u32)(q >> 1) * 512 : OOB);
        asm volatile("s_waitcnt vmcnt(0)" :: "v"(v) : "memory");
        const int dt = (int)(__builtin_amdgcn_s_memtime() - t0);
        if (half) d[1] = min(d[1], dt); else d[0] = min(d[0], dt);
        calm += dt < 1100;
    }
    const u32 xcc = (u32)__builtin_amdgcn_s_getreg(20 | (3 << 11)) & 0xfu;
    return (u32)min(max((d[0] - d[1]) * ((xcc & 4u) ? -1 : 1), -250), 250);
}
DEV f32x2 ln_stats(const f32x2 (&x)[5]) {
    const float c = f_(uu(u_(x[0].x)));
    f32x2 d[5];
#pragma unroll
    for (int i = 0; i < 5; ++i) d[i] = x[i] - c;
    const f32x2 s2 = (d[0] + d[1]) + (d[2] + d[3]) + d[4], q2 = (d[0] * d[0] + d[1] * d[1]) + (d[2] * d[2] + d[3] * d[3] + d[4] * d[4]);
    float a = s2.x + s2.y, b = q2.x + q2.y, a2, b2;
#define LN_DPP(m) "v_add_f32_dpp %0, %0, %0 " m " row_mask:0xf bank_mask:0xf\n\tv_add_f32_dpp %1, %1, %1 " m " row_mask:0xf bank_mask:0xf\n\ts_nop 0\n\t"
    asm("s_nop 1\n\t" LN_DPP("quad_perm:[1,0,3,2]") LN_DPP("quad_perm:[2,3,0,1]") LN_DPP("row_ror:4")
        "v_add_f32_dpp %0, %0, %0 row_ror:8 row_mask:0xf bank_mask:0xf\n\tv_add_f32_dpp %1, %1, %1 row_ror:8 row_mask:0xf bank_mask:0xf\n\ts_nop 1\n\t"
        "v_permlane16_swap_b32 %0, %1\n\tv_add_f32 %0, %0, %1\n\tv_mov_b32 %1, %0\n\ts_nop 1\n\tv_permlane32_swap_b32 %0, %1\n\tv_add_f32 %0, %0, %1\n\ts_nop 0\n\t"
        "v_readlane_b32 %2, %0, 0\n\tv_readlane_b32 %3, %0, 16\n\ts_nop 1"
        : "+v"(a), "+v"(b), "=s"(a2), "=s"(b2));
    const float m = a2 * (1.0f / DM), var = fmaxf(b2 * (1.0f / DM) - m * m, 0.f);
    const float sd = rbf(__builtin_amdgcn_sqrtf(rbf(rbf(var) + 1e-5f)));
    return (f32x2){rbf(c + m), __builtin_amdgcn_rcpf(sd)};
}
DEV u32 pkv(f32x2 v) { u32 d; asm("v_cvt_pk_bf16_f32 %0, %1, %2" : "=v"(d) : "v"(v.x), "v"(v.y)); return d; }
DEV void store_h(rsrc_t rs, u32 base, int row, const u32 (&hp)[5]) {
    const u32 lane = threadIdx.x & 63;
    const u32 sw = (u32)(row & 15) * 16u, o8 = (lane >> 5) * 8192u + (((lane & 31u) * 8u) ^ sw), hb = base + (u32)(row >> 5) * (32 * DM * 2) + (u32)(row & 31) * 256u;
    st8s(rs, o8, hb, (v2u){hp[0], hp[1]});
    st8s(rs, o8, hb + 16384, (v2u){hp[2], hp[3]});
    st4s(rs, (lane * 4) ^ sw, hb + 32768, hp[4]);
}

#define DMA16(ldsb, vo, so) asm volatile("s_mov_b32 m0, %0\n\ts_nop 0\n\tbuffer_load_dwordx4 %1, %2, %3 offen sc1 lds" :: "s"(ldsb), "v"(vo), "s"(srd), "s"(so) : "memory")
#define DMA16I(ldsb, lo, vo, so, go) do { u32 so_; asm volatile("s_add_u32 m0, %1, %5\n\ts_add_u32 %0, %4, %6\n\tbuffer_load_dwordx4 %2, %3, %0 offen sc1 lds" \
                         : "=&s"(so_) : "s"(ldsb), "v"(vo), "s"(srd), "s"(so), "i"(lo), "i"(go) : "memory", "scc"); } while (0)
#define DMA32(ldsb, vo, so) asm volatile("s_mov_b32 m0, %0\n\ts_nop 0\n\tbuffer_load_dwordx4 %1, %2, %3 offen sc1 lds\n\tbuffer_load_dwordx4 %1, %2, %3 offen offset:1024 sc1 lds" \
                         :: "s"(ldsb), "v"(vo), "s"(srd), "s"(so) : "memory")
#define DMAIN(sr, ldsb, vo, so) asm volatile("s_mov_b32 m0, %0\n\ts_nop 0\n\tbuffer_load_dwordx4 %1, %2, %3 offen lds" :: "s"(ldsb), "v"(vo), "s"(sr), "s"(so) : "memory")
DEV v4u mk_srd(const void* q, u32 bytes) { return (v4u){uu((u32)(size_t)q), uu((u32)((size_t)q >> 32) & 0xffffu), bytes, 0x00020000u}; }
#define DMANT(sr, ldsb, vo, so) asm volatile("s_mov_b32 m0, %0\n\ts_nop 0\n\tbuffer_load_dwordx4 %1, %2, %3 offen nt lds" :: "s"(ldsb), "v"(vo), "s"(sr), "s"(so) : "memory")
#define AHEAD(sr, vo, so) asm volatile("buffer_load_dwordx4 %0, %1, %2, %3 offen" : "+v"(junk) : "v"(vo), "s"(sr), "s"(so) : "memory")
#define MK_SRD() const v4u srd = mk_srd(p.ws, W_END)

constexpr int CV_W = 40960;
template <bool B> struct CvL {
    static constexpr int NF = B ? 5 : 3, NK = B ? 4 : 2, KP = 544 + NK * 256, CH = NF * 1024 + 4 * KP;
};
constexpr int CV_Y = 4 * CvL<false>::CH, CV_YP = 272;
static_assert(CV_Y + 32 * CV_YP <= CV_W && 2 * CvL<true>::CH <= CV_Y, "conv lds");
DEV int cv_chan(int u, int i) { return 16 * (u % 40) + 4 * i + u / 40; }
template <bool B> DEV void cv_filt(const Params& p, u32 dst, int d, int i) {
    const u32 lane = threadIdx.x & 63;
    const v4u sf = mk_srd(p.ssmk + d * SEQ, SEQ * 2);
    if (i == 0) DMANT(sf, dst, lane < 4u ? OOB : lane * 16u - 64u, 0);
    else DMANT(sf, uu(dst + i * 1024), i == CvL<B>::NF - 1 && lane >= 4u ? OOB : lane * 16u, i * 1024 - 64);
}
DEV v4u cv_kvld(rsrc_t rs, int d, int i) { return ld16s(rs, (threadIdx.x & 63) * 16u, uu(W_KVT + (u32)d * (SEQ * 2) + i * 1024)); }
template <bool B> DEV void cv_kvput(lptr F, const v4u* k) {
    const int lane = threadIdx.x & 63;
#pragma unroll
    for (int i = 0; i < CvL<B>::NK; ++i) lds_w16(F + CvL<B>::NF * 1024 + (lane & 3) * CvL<B>::KP + 512 + i * 256 + (lane >> 2) * 16, k[i]);
}
template <bool B> DEV void cv_zero(lptr F) {
    const int lane = threadIdx.x & 63;
#pragma unroll
    for (int i = 0; i < 4; ++i) lds_w16(F + (lane >> 5) * CvL<B>::CH + CvL<B>::NF * 1024 + i * CvL<B>::KP + (lane & 31) * 16, (v4u){0u, 0u, 0u, 0u});
}
#define M32V(d, a, b) asm volatile("v_mfma_f32_32x32x16_bf16 %0, %1, %2, %0" : "+v"(d) : "v"(a), "v"(b))
template <bool B, class G> DEV void cv_run(const Params& p, rsrc_t rs, lptr F, int d, u32 D, f32x4 sp, float (&y)[16], G&& gap) {
    constexpr int KP = CvL<B>::KP, FO = B ? 2048 : 0;
    const int lane = threadIdx.x & 63, n = lane & 31, hh = lane >> 5, c = lane & 3;
    lptr fa = F + 64 + (32 * hh + 16 * (c & 1) + 8 * ((lane >> 4) & 1) + 4 * (c >> 1) - (lane & 12)) * 2;
    lptr kl = F + CvL<B>::NF * 1024 + (32 + n - hh) * 16;
    v4u dg;
#pragma unroll
    for (int q = 0; q < 4; ++q) { const int k = (lane & 35) ? -1 : (n & 4) + (n >> 3); dg[q] = k == 2 * q ? D : k == 2 * q + 1 ? D << 16 : 0u; }
    v4u l[4], h[4]; v2u fc[4], fh[4];
    f32x16 acc[4];
#pragma unroll
    for (int j = 0; j < 4; ++j) { l[j] = lds_r16(kl + j * KP); if constexpr (B) h[j] = lds_r16(kl + j * KP + 512); }
#pragma unroll
    for (int j = 0; j < 2; ++j) { fc[j] = lds_tr(fa + FO - 32 * j); if constexpr (B) fh[j] = lds_tr(fa - 32 * j); }
    asm volatile("s_nop 1" : "+v"(dg));
#pragma unroll
    for (int j = 0; j < 4; ++j) asm volatile("v_mfma_f32_32x32x16_bf16 %0, %1, %2, 0" : "=&v"(acc[j]) : "v"(dg), "v"(B ? h[j] : l[j]));
    constexpr int U = B ? 2 : 4;
    kl -= 32 * U; fa -= 32;
    auto body = [&](bool first) __attribute__((always_inline)) {
#pragma unroll
        for (int z = 0; z < U; ++z) {
            const int c = 2 * (z & 1);
            const v4u f = __builtin_shufflevector(fc[c], fc[c + 1], 0, 1, 2, 3), g = __builtin_shufflevector(fh[c], fh[c + 1], 0, 1, 2, 3);
#pragma unroll
            for (int j = 0; j < 4; ++j) {
                M32V(acc[j], f, l[j]); l[j] = lds_r16(kl + j * KP + 32 * (U - z - 1));
                if (j < 2) fc[(c ^ 2) + j] = lds_tr(fa + FO + (z + 1) * 128 + 32 - 32 * j);
                if (first && j > 0) gap(5 * z + j - 1);
                SCHED();
            }
            if constexpr (B) {
#pragma unroll
                for (int j = 0; j < 4; ++j) {
                    M32V(acc[j], g, h[j]); h[j] = lds_r16(kl + j * KP + 512 + 32 * (U - z - 1));
                    if (j < 2) fh[(c ^ 2) + j] = lds_tr(fa + (z + 1) * 128 + 32 - 32 * j);
                    if (first && j > 1) gap(5 * z + j + 1);
                    SCHED();
                }
            }
        }
        kl -= 32 * U; fa += 128 * U;
    };
    if constexpr (B) body(true);
#pragma unroll 1
    for (int m = B ? 2 * U : 0; m < 32; m += 2 * U) body(false);
    asm volatile("s_nop 11" : "+v"(acc[0]), "+v"(acc[1]), "+v"(acc[2]), "+v"(acc[3]));
    const float o[3] = {acc[1][15] + acc[2][14] + acc[3][13], acc[2][15] + acc[3][14], acc[3][15]};
    if constexpr (!B) st16(rs, lane == 63 ? W_SP + (u32)d * 16u : OOB, (v4u){u_(o[0]), u_(o[1]), u_(o[2]), 0u});
#pragma unroll
    for (int i = 0; i < 16; ++i) {
        float v = acc[0][i];
#pragma unroll
        for (int j = 1; j < 4; ++j) if (j <= i) v += acc[j][i - j];
        if (i < 3) { const float e = f_((u32)__builtin_amdgcn_ds_bpermute((hh ? lane - 32 : lane + 31) * 4, (int)u_(o[i]))); v += lane == 0 ? sp[i] : e; }
        y[i] = v;
    }
}
DEV void cv_put(lptr F, int pr, const float (&y0)[16], const float (&y1)[16]) {
    const int lane = threadIdx.x & 63;
#pragma unroll
    for (int i = 0; i < 16; ++i) *(LAS u32*)(F + CV_Y + (lane & 31) * CV_YP + (lane >> 5) * 128 + i * 8 + pr * 4) = pk(y0[i], y1[i]);
}
DEV void cv_out(rsrc_t rs, lptr F, int u, int blk0) {
    const int lane = threadIdx.x & 63;
    lptr q = F + CV_Y + ((lane >> 2) & 3) * CV_YP + ((lane >> 4) * 4 + (lane & 3)) * 8;
    const u32 yo = W_Y + (u32)(((lane >> 2) * 8 + blk0) * DM + 4 * u + (lane & 3)) * 16u;
#pragma unroll
    for (int i = 0; i < 8; ++i) { lptr a = q + (i >> 1) * (8 * CV_YP) + (i & 1) * 128; st16(rs, yo + (u32)((i & 1) * 128 + (i >> 1)) * (DM * 16), tr2(a, a + 4 * CV_YP)); }
}
struct KvIn { v2u k[19], v[16]; v4u s[2]; };
template <class F> DEV void kvt_load(const Params& p, int tile, KvIn& in, F&& mid) {
    const int lane = threadIdx.x & 63;
    const rsrc_t rk = mk_rsrc(p.bk, ACT), rv = mk_rsrc(p.bv, ACT), rh = mk_rsrc(p.shift, DM * 8);
    const int row = (tile / 10) * 64 + (lane >> 4) * 16, col = (tile % 10) * 64 + (lane & 15) * 4;
    const u32 vo = (u32)(row * DM + col) * 2u, vp = row > 0 ? vo - 3 * (DM * 2) : OOB;
#pragma unroll
    for (int i = 0; i < 2; ++i) in.s[i] = ldin16(rh, (u32)col * 8u + i * 16, 0);
#pragma unroll
    for (int i = 0; i < 19; ++i) { if (i == 7) mid(); in.k[i] = i < 3 ? ldin8(rk, vp, i * (DM * 2)) : ldin8(rk, vo, (i - 3) * (DM * 2)); if (i >= 3) in.v[i - 3] = ldin8(rv, vo, (i - 3) * (DM * 2)); }
}
DEV void kvt_make(rsrc_t rs, lptr xt, int tile, const KvIn& in) {
    const int lane = threadIdx.x & 63;
#pragma unroll
    for (int c = 0; c < 2; ++c) {
        const v4u sv = in.s[c];
        const f32x2 sh[4] = {{lo_f(sv.x), lo_f(sv.z)}, {hi_f(sv.x), hi_f(sv.z)}, {lo_f(sv.y), lo_f(sv.w)}, {hi_f(sv.y), hi_f(sv.w)}};
        f32x2 kf[19];
#pragma unroll
        for (int i = 0; i < 19; ++i) kf[i] = up2(in.k[i][c]);
        u32 kv[16];
#pragma unroll
        for (int i = 0; i < 16; ++i) kv[i] = pk2(((kf[i] * sh[3] + kf[i + 1] * sh[2]) + (kf[i + 2] * sh[1] + kf[i + 3] * sh[0])) * up2(in.v[i][c]));
#pragma unroll
        for (int q = 0; q < 8; ++q) {
            const u32 sel = (q & 1) ? 0x07060302u : 0x05040100u; const u32* z = kv + (q >> 1);
            *(LAS v2u*)(xt + ((2 * c + (q & 1)) * 16 + (lane & 15)) * 144 + (lane >> 5) * 64 + (q >> 1) * 16 + ((lane >> 4) & 1) * 8) =
                (v2u){__builtin_amdgcn_perm(z[4], z[0], sel), __builtin_amdgcn_perm(z[12], z[8], sel)};
        }
    }
    const u32 o = W_KVT + (u32)(((tile % 10) * 64 + 4 * (lane >> 3)) * SEQ + (tile / 10) * 64 + (lane & 7) * 8) * 2u;
#pragma unroll
    for (int i = 0; i < 8; ++i) st16(rs, o + (u32)(32 * (i & 1) + (i >> 1)) * (SEQ * 2), lds_r16(xt + (8 * i + (lane >> 3)) * 144 + (lane & 7) * 16));
}
struct RowX { v2u a, b; u32 c; };
DEV void ln_row(rsrc_t rs, int row, const RowX& r) {
    const f32x2 x[5] = {up2(r.a.x), up2(r.a.y), up2(r.b.x), up2(r.b.y), up2(r.c)};
    const f32x2 st = ln_stats(x);
    u32 hp[5];
#pragma unroll
    for (int q = 0; q < 5; ++q) hp[q] = pk2(up2(pkv(x[q] - st.x)) * st.y);
    store_h(rs, W_H1, row, hp);
}
DEV v4u mem0_load(const Params& p, int j) {
    const u32 gt = (u32)(j * 256 + threadIdx.x) * 16u, n = ((gt >> 14) << 9) + ((gt & 8191u) >> 4);
    return ldin16(mk_rsrc(p.mem0, 8 * DM * 2), n < 640u ? ((2 * ((n >> 2) & 3) + (n & 1)) * DM + (n >> 4) * 16 + (n & 2) * 4) * 2u : OOB, 0);
}
DEV void pre_rings(rsrc_t rs, int j, v4u m) {
    const u32 gi = (u32)(j * 256 + threadIdx.x), gt = gi * 16u, R = NPRE * 4096, n = ((gt >> 14) << 9) + ((gt & 8191u) >> 4);
#pragma unroll
    for (int i = 0; i < 4; ++i) m[i] = n >= 640u ? PSENT : m[i] == PSENT ? 0x7FC17FC1u : m[i];
    st4s(rs, gi - 36u * 256u < F_CLR ? foff(gi - 36u * 256u) : OOB, 0, 0u);
#pragma unroll 1
    for (u32 o = gt; o < MB; o += R) st16(rs, W_ORING + o, (v4u){PSENT, PSENT, PSENT, PSENT});
    const u32 mo = gt < 4 * 32768 + 16384 ? W_MRING + gt : OOB;
    st16(rs, mo, m);
    st16(rs, mo + MB / 4, m);
    static_assert(NPRE * 4096 >= 4 * 32768 + 16384, "memory ring");
}
constexpr int KT0 = NPRE * 4, LN0 = NPRE * 8;
struct Early { RowX r[2]; v4u m; u32 vote; };
DEV void conv_early(const Params& p, rsrc_t rs, int j, Early& e) {
    const int lane = threadIdx.x & 63, w = uni(threadIdx.x >> 6), u = j * 4 + w;
    if (j < NPRE) {
        const rsrc_t rx = mk_rsrc(p.x, ACT);
#pragma unroll
        for (int i = 0; i < 2; ++i) {
            const u32 so = uu((u + i * KT0) * (DM * 2));
            e.r[i].a = __builtin_amdgcn_raw_buffer_load_b64(rx, lane * 8u, so, 0); e.r[i].b = __builtin_amdgcn_raw_buffer_load_b64(rx, lane * 8u + 512u, so, 0);
            e.r[i].c = __builtin_amdgcn_raw_buffer_load_b32(rx, lane * 4u + 1024u, so, 0);
        }
        e.m = mem0_load(p, j);
    } else if (j < NCV && w > 0) e.vote = pol_probe(p, rs, u - NPRE * 4);
}
DEV void conv_stage(const Params& p, rsrc_t rs, lptr lds, int j, int g0, int ge, v2u ep, Early& e) {
    const int tid = threadIdx.x, lane = tid & 63, w = uni(tid >> 6), u = j * 4 + w;
    lptr F = lds + w * CV_W;
    const u32 fb = uu(w * CV_W);
    const rsrc_t rd = mk_rsrc(p.ssmD, DM * 2);
    float y0[16], y1[16];
    KvIn in;
    if (j < NPRE) {
        constexpr int CH = CvL<false>::CH;
        if (w == 1) LST(1500 + j, rt32());
        pre_rings(rs, j, e.m);
        ln_row(rs, u, e.r[0]); ln_row(rs, u + KT0, e.r[1]);
        if (w == 1) LST(1550 + j, rt32());
        CFENCE();
        kvt_load(p, u, in, [&] { VMW(13); BAR(); pre_post(rs, j, 1u, ep); if (w == 0) LST(1450 + j, rt32()); });
        if (j == 20) LST(260, rt32());
        const u32 D = __builtin_amdgcn_raw_buffer_load_b16(rd, (u32)cv_chan(u, lane & 3) * 2u, 0, 0);
#pragma unroll 1
        for (int i = 0; i < 4; ++i) { const int d = cv_chan(u, i); cv_filt<false>(p, fb + i * CH, d, 0); cv_filt<false>(p, fb + i * CH, d, 1); cv_filt<false>(p, fb + i * CH, d, 2); }
        kvt_make(rs, F + CV_Y, u, in);
        cv_zero<false>(F); cv_zero<false>(F + 2 * CH);
        DRAIN(); BAR();
        pre_post(rs, j, 2u, ep);
        if (j == 20) LST(262, rt32());
        pre_wait(rs, 0, NPRE, 2u, ep);
        if (j == 20) LST(281, rt32());
        {
            v4u k[8];
#pragma unroll
            for (int i = 0; i < 8; ++i) k[i] = cv_kvld(rs, cv_chan(u, i >> 1), i & 1);
#pragma unroll
            for (int i = 0; i < 4; ++i) cv_kvput<false>(F + i * CH, k + 2 * i);
        }
        if (j == 20) LST(286, rt32());
#pragma unroll 1
        for (int i = 0; i < 4; i += 2) {
            cv_run<false>(p, rs, F + i * CH, cv_chan(u, i), rdl(D, i), f32x4{}, y0, [](int) {});
            cv_run<false>(p, rs, F + (i + 1) * CH, cv_chan(u, i + 1), rdl(D, i + 1), f32x4{}, y1, [](int) {});
            cv_put(F, i >> 1, y0, y1);
        }
        if (j == 20) LST(287, rt32());
        cv_out(rs, F, u, 0);
        if (j == 20) LST(263, rt32());
    } else {
        st16(rs, lane == 0 ? foff(F_PRE + 48 + u - NPRE * 4) : OOB, (v4u){ep.x + 1u, ep.y, w > 0 ? e.vote : 0u, 0u});
#pragma unroll 1
        for (int t = u + 128; t < 320; t += 16) { kvt_load(p, t, in, [] {}); kvt_make(rs, F, t, in); }
    }
    DRAIN(); BAR();
    if (g0 < ge) {
        constexpr int CH = CvL<true>::CH;
        pre_post(rs, j, 3u, ep);
        cv_zero<true>(F);
        if (w == 0) { pre_poll(rs, 64, 32, 3u, ep); pre_poll(rs, 0, NCV, 3u, ep); }
        BAR();
        v4u k[4];
        auto fetch = [&](u32 dst, int d, int i) { if (i < 5) cv_filt<true>(p, dst, d, i); else if (i < 9) k[i - 5] = cv_kvld(rs, d, i - 5); };
#pragma unroll
        for (int i = 0; i < 9; ++i) fetch(fb, cv_chan(g0 * 4 + w, 0), i);
        const u32 ch = (u32)cv_chan(min((g0 + 8 * (lane >> 2)) * 4 + w, 159), lane & 3);
        u32 D = __builtin_amdgcn_raw_buffer_load_b16(rd, ch * 2u, 0, 0);
        v4u sp = ld16s(rs, W_SP + ch * 16u, 0);
        asm volatile("" : "+v"(D), "+v"(sp));
#pragma unroll 1
        for (int g = g0, n = 0; g < ge; g += 8) {
            const int un = g * 4 + w;
            if (g == 16) { LST(282, rt32()); LST(288, __builtin_amdgcn_s_memtime()); }
#pragma unroll 1
            for (int i = 0; i < 4; i += 2, n += 2) {
                const int d1 = (int)rdl(ch, n + 1), d2 = (int)rdl(ch, n + 2);
                cv_kvput<true>(F, k);
                cv_run<true>(p, rs, F, 0, rdl(D, n), (f32x4){f_(rdl(sp.x, n)), f_(rdl(sp.y, n)), f_(rdl(sp.z, n)), 0.f}, y0, [&](int q) { fetch(fb + CH, d1, q); });
                cv_kvput<true>(F + CH, k);
                cv_run<true>(p, rs, F + CH, 326, rdl(D, n + 1), (f32x4){f_(rdl(sp.x, n + 1)), f_(rdl(sp.y, n + 1)), f_(rdl(sp.z, n + 1)), 0.f}, y1, [&](int q) { fetch(fb, d2, q); });
                cv_put(F, i >> 1, y0, y1);
            }
            cv_out(rs, F, un, 4);
            if (g == 16) { LST(283, rt32()); LST(289, __builtin_amdgcn_s_memtime()); }
        }
        DRAIN(); BAR();
    }
    pre_post(rs, j, 5u, ep);
    if (j == 20) LST(284, rt32());
    if (j == 3) LST(285, rt32());
}

#define M32A(first, d, a, b) do { if (first) asm volatile("v_mfma_f32_32x32x16_bf16 %0, %1, %2, 0" : "=&v"(d) : "a"(a), "v"(b)); \
                                  else asm volatile("v_mfma_f32_32x32x16_bf16 %0, %1, %2, %0" : "+v"(d) : "a"(a), "v"(b)); } while (0)
#define M32B(first, d, a, b) do { if (first) asm volatile("v_mfma_f32_32x32x16_bf16 %0, %1, %2, 0" : "=&v"(d) : "v"(a), "a"(b)); \
                                  else asm volatile("v_mfma_f32_32x32x16_bf16 %0, %1, %2, %0" : "+v"(d) : "v"(a), "a"(b)); } while (0)
#define VCVT(d, a, b) asm volatile("v_cvt_pk_bf16_f32 %0, %1, %2" : "=v"(d) : "v"(a), "v"(b))
#define VADD(d, a, b) asm volatile("v_add_f32 %0, %1, %2" : "=v"(d) : "v"(a), "v"(b))
#define LD4A(d, vo, so) asm volatile("buffer_load_dword %0, %1, %2, %3 offen sc1" : "=v"(d) : "v"(vo), "s"(srd), "s"(so) : "memory")
DEV u32 wst_src(int lane, int ld) { return (u32)((lane >> 4) * ld + 8 * ((lane & 15) ^ ((lane >> 4) << 2))) * 2u; }
DEV u32 wst_rd(int lane, int w) { const int q4 = (lane >> 2) & 3; return (u32)(q4 * 256 + ((w ^ q4) * 64) + ((lane >> 4) & 1) * 32 + (lane & 3) * 8); }

constexpr int QK_SEL = 81920;
#define QK_LEAD 12
#define QK_SMP 26
#define QK_YB 26
#define QK_W0 2
DEV void role_qkv(const Params& p, lptr lds, int c, v2u ep) {
    const int tid = threadIdx.x, lane = tid & 63, w = uni(tid >> 6), hh = lane >> 5, r32 = lane & 31;
    const int m = c >> 5, h = c & 31;
    const rsrc_t rs = mk_rsrc(p.ws, W_END);
    v4u wt[40];
    {
        const v4u sw = mk_srd(m == 0 ? p.Wq : m == 1 ? p.Wk : p.Wv, DM * NQ * 2), sx = mk_srd(p.x, ACT);
        const u32 vo = wst_src(lane, NQ) + (u32)h * (DH * 2), x8 = (u32)lane * 8u, x4 = (u32)lane * 4u;
        RowX xr[5];
        auto wfill = [&](int i0, int n) {
#pragma unroll 1
            for (int i = i0; i < i0 + n; ++i) DMAIN(sw, uu((4 * i + w) * 1024), vo, uu((16 * i + 4 * w) * (NQ * 2)));
        };
        wfill(0, QK_W0);
#pragma unroll
        for (int i = 0; i < 5; ++i)
            asm volatile("buffer_load_dwordx2 %0, %3, %5, %6 offen\n\tbuffer_load_dwordx2 %1, %3, %5, %6 offen offset:512\n\tbuffer_load_dword %2, %4, %5, %6 offen offset:1024"
                         : "=&v"(xr[i].a), "=&v"(xr[i].b), "=&v"(xr[i].c) : "v"(x8), "v"(x4), "s"(sx), "s"(uu((LN0 + 4 * c + w + 384 * i) * (DM * 2))) : "memory");
        wfill(QK_W0, 10 - QK_W0);
        if (c == 3) LST(275, rt32());
        VMW(10 - QK_W0);
        if (c == 3) LST(276, rt32());
#pragma unroll
        for (int i = 0; i < 5; ++i) {
            asm volatile("" : "+v"(xr[i].a), "+v"(xr[i].b), "+v"(xr[i].c));
            const int row = LN0 + 4 * c + w + 384 * i;
            if (i < 4 || row < SEQ) ln_row(rs, row, xr[i]);
            wfill(10 + 6 * i, 6);
        }
        if (c == 3) LST(264, rt32());
        lptr tb = lds + hh * 2048 + wst_rd(lane, w);
        SFOR(k, 40)
            if constexpr (k == 0) { VMW(15); BAR(); }
            if constexpr (k == 28) {
                if (w == 0) pre_poll(rs, 0, NPRE, 1u, ep);
                DRAIN(); BAR();
                pre_post(rs, 96 + c, 1u, ep);
            }
            lptr q = tb + (k >> 3) * 32768 + (k & 7) * 4096; wt[k] = tr2(q, q + 1024); asm volatile("" : "+a"(wt[k]));
        SEND
    }
    if (c == 3) LST(268, rt32());
    if (w == 0) LST(1350 + c, rt32());
    bool dead = false;
    int n0, n1;
    const u32 t16 = (u32)(w * 2048 + lane * 16), wb = uu(w * 2048);
    u32 ar[16];
#pragma unroll
    for (int j = 0; j < 16; ++j) ar[j] = (u32)(r32 * 256 + ((2 * (j & 7) + hh) ^ (r32 & 15)) * 16 + (j >> 3) * 49152);
    MK_SRD();
#define XRD(y, ks) lds_r16(lds + ar[((ks) & 7) + ((ks) >= 24 ? 8 : 0)] + ((ks) >> 3) % 3 * 16384 + (y) * 8192)
#define QM0(y, b) asm volatile("s_add_u32 m0, %0, %1" :: "s"(wb), "i"((b) * 16384 + (y) * 8192) : "scc", "memory")
#define QDMA(i, vo) asm volatile("buffer_load_dwordx4 %0, %1, %2 offen offset:%3 sc1 lds" :: "v"(vo), "s"(srd), "s"(((i) >> 1) * 8192), "i"(((i) & 1) * 1024) : "memory")
    const u32 h2flag = (lane < 32 && w == 0) ? foff(F_H2 + lane) : OOB;
    const u32 ol = (m == 2 ? (u32)(2 * w + (r32 >> 4)) * 1024u : (u32)((r32 >> 4) * 4 + w) * 1024u) + (u32)(hh * 16 + (r32 & 15)) * 16u
                 + (m == 2 ? W_VO : m == 1 ? W_KQ : W_KQ + 8192u) + (u32)h * 16384u;
    const u32 fo = tid < 2 ? foff(F_QKV + tid * 96 + c) : OOB;
    lptr selw = lds + QK_SEL + (w == 0 ? 0 : 64), selr = lds + QK_SEL;
    PIN(selw);
    static_assert(QK_LEAD > 8 && QK_LEAD % 2 == 0 && LN0 == 10 * 32, "X = 8 comes up, before any item of layer 1");
    static_assert(W_H2 - W_H1 == 2 * 64 * (32 * DM * 2), "h");
    auto run = [&](auto isv) __attribute__((always_inline)) {
        constexpr bool V = decltype(isv)::value;
        constexpr int YB = QK_YB;
        f32x16 ax, ay = {};
        v4u x[8], pp, pq;
        u32 xv = t16 + W_H1, ox = ol, oy = OOB, nn = 1, lt = 0, fcnt = 0, fa = OOB;
        asm volatile("" : "+v"(lt), "+v"(nn));
#pragma unroll
        for (int z = 0; z < 5; ++z) x[z] = XRD(1, 40 - YB + z);
#define QPK(r_, a1, a_, q_) do { if constexpr (V) VCVT(r_[q_], a1[4 * (a_) + 8 * ((q_) >> 1) + 2 * ((q_) & 1)], a1[4 * (a_) + 8 * ((q_) >> 1) + 2 * ((q_) & 1) + 1]); \
                                 else VCVT(r_[q_], a1[4 * (a_) + (q_)], a1[4 * (a_) + (q_) + 8]); } while (0)
#define QPK4(r_, a1, a_) do { QPK(r_, a1, a_, 0); QPK(r_, a1, a_, 1); QPK(r_, a1, a_, 2); QPK(r_, a1, a_, 3); } while (0)
        for (;;) {
            u32 ls = uu(lt), yv, fv, nx, need;
            if (ls != 0u && uu(nn) < 256u) pre_wait(rs, 96, 96, 1u, ep);
            else if (ls != 0u) {
                n1 = (int)(uu(nn) >> 8);
                CFENCE();
                u32 f2 = ld4(rs, h2flag);
                asm volatile("s_waitcnt vmcnt(0)" : "+v"(f2) :: "memory");
                *(LAS u32*)(selw + 16) = (u32)__builtin_amdgcn_ballot_w64(f2 < (u32)n1);
                BAR();
                st4s(rs, fa, 0, fcnt);
                fa = OOB;
                if (n1 > NSEG) { LDUMP(w == 0, 64 + c); __builtin_amdgcn_endpgm(); }
                if (uu(*(LAS u32*)(selr + 16)) != 0u) {
                    if (w == 0) wait_flags(rs, h2flag, (u32)n1, dead);
                    BAR();
                }
            }
#define QT(k) do {} while (0)
            QT(0);
            SCHED();
            QM0(0, 0); NOPS(0); QDMA(0, xv); QDMA(1, xv); QM0(0, 1);
            SFOR(z, 80)
                constexpr int ks = (z + 40 - YB) % 40, zx = z - YB;
                constexpr auto upto = [](int q) { const int d = (q - YB + 49) % 40; return q + 6 + (d == 0 || d == 2 ? 1 : d == 1 ? 2 : 0); };
                if constexpr (zx >= 0 && zx < 40) { if constexpr (V) M32B(ks == 0, ax, x[z & 7], wt[ks]); else M32A(ks == 0, ax, wt[ks], x[z & 7]); }
                else { if constexpr (V) M32B(ks == 0, ay, x[z & 7], wt[ks]); else M32A(ks == 0, ay, wt[ks], x[z & 7]); }
                if constexpr (z == 78) lt = *(LAS u32*)selr;
#pragma unroll
                for (int q = upto(z - 1); q < upto(z); ++q) x[q & 7] = XRD(q % 80 < YB || q % 80 >= YB + 40, (q + 40 - YB) % 40);
                SCHED();
                (void)&srd; (void)&wb;
                if constexpr (z < 2) QDMA(z + 2, xv);
                if constexpr (z >= 2 && z < 14 && (z & 3) == 2) QM0(0, (z + 6) >> 2);
                if constexpr (z >= 3 && z < 15 && (z & 1)) QDMA((z + 5) >> 1, xv);
                if constexpr (z == 4) { n0 = uni((int)nn); n1 = n0 >> 8; n0 &= 255; PINS(n0); PINS(n1); }
                if constexpr (z == 8) { nx = n0 < NSEG ? W_H1 + (u32)n0 * (32 * DM * 2) : OOB; PINS(nx); }
                if constexpr (z == 12) { yv = t16 + nx; PIN(yv); }
                if constexpr (zx == -6) { QT(1); asm volatile("s_waitcnt vmcnt(6) lgkmcnt(0)\n\ts_barrier" ::: "memory"); QT(2); }
                if constexpr (zx == -5) st4s(rs, fa, 0, fcnt);
                if constexpr (zx == -4 || zx == 0 || zx == 4) QM0(1, (zx + 4) >> 2);
                if constexpr (zx >= -3 && zx < 8 && (zx & 1)) QDMA((zx + 3) >> 1, yv);
                if constexpr (zx == 2) QPK4(pp, ay, 0);
                if constexpr (zx == 6) QPK4(pq, ay, 1);
                if constexpr (zx == 8) st16(rs, oy, pp);
                if constexpr (zx == 9) st16(rs, oy + 512, pq);
                if constexpr (zx == 10) { QT(3); asm volatile("s_waitcnt vmcnt(9)\n\ts_barrier" ::: "memory"); QT(4); }
                if constexpr (zx == 11 || zx == 15) QM0(1, (zx + 1) >> 2);
                if constexpr (zx >= 12 && zx < 20 && (zx & 1) == 0) QDMA(zx >> 1, yv);
                if constexpr (zx == 21) { fcnt = lane == 0 ? (u32)n0 : (u32)n1; fa = fo; asm volatile("" : "+v"(fcnt), "+v"(fa)); }
                if constexpr (zx == 22) { oy = (ol + (u32)n0 * (NH * 16384u)) | (yv & OOB); PIN(oy); }
                if constexpr (zx == 23) { n0 = min(n0 + 1, NSEG); PINS(n0); nx = (u32)min(NSEG, n1 + QK_LEAD); PINS(nx); }
                if constexpr (zx == 24)
                    asm volatile("s_cmp_ge_u32 %1, %0\n\ts_cselect_b32 %0, %4, %1\n\ts_addc_u32 %2, %2, 0\n\ts_cmp_lt_u32 %0, 64\n\ts_cselect_b32 %3, 0, %2"
                                 : "+s"(nx), "+s"(n0), "+s"(n1), "=&s"(need) : "s"(n1 + 64) : "scc");
                if constexpr (zx == 25) { n0 += 1 - (int)min(nx >> 6, 1u); PINS(n0); }
                if constexpr (zx == 26) { nn = (u32)(n0 | n1 << 8); PIN(nn); }
                if constexpr (zx == 27) { ls = W_H1 + (nx + (nx & 64u)) * (32 * DM * 2); PINS(ls); }
                if constexpr (zx == 28) { xv = t16 + ls; PIN(xv); need = nx == 8u ? 255u : need; PINS(need); }
                if constexpr (zx == 34) { asm volatile("s_waitcnt vmcnt(%0) lgkmcnt(0)\n\ts_barrier" :: "i"(QK_SMP < 34 ? 7 : 6) : "memory"); QT(5); }
                if constexpr (zx == QK_SMP) LD4A(fv, h2flag, 0);
                if constexpr (zx == 42) QPK4(pp, ax, 0);
                if constexpr (zx == 43) st16(rs, ox, pp);
                if constexpr (zx == 44) QPK4(pq, ax, 1);
                if constexpr (zx == 45) st16(rs, ox + 512, pq);
                if constexpr (zx == 47) { ox = ol + nx * (NH * 16384u); PIN(ox); }
                if constexpr (z == 74) { asm volatile("s_waitcnt vmcnt(2)" : "+v"(fv) :: "memory"); QT(6); ls = (u32)__builtin_amdgcn_ballot_w64(fv < need); PINS(ls); }
                if constexpr (z == 75) { lt = ls; PIN(lt); *(LAS u32*)selw = lt; }
                if constexpr (z == 77) asm volatile("s_waitcnt lgkmcnt(2)\n\ts_barrier" ::: "memory");
                SCHED();
            SEND
        }
    };
    if (m == 2) run(IC<1>{}); else run(IC<0>{});
}

DEV void role_out(const Params& p, lptr lds, int L, int kq, int ng, v2u ep) {
    const int tid = threadIdx.x, lane = tid & 63, w = uni(tid >> 6), hh = lane >> 5, r32 = lane & 31;
    const rsrc_t rs = mk_rsrc(p.ws, W_END);
    v4u wo[64];
    bool dead = false;
    {
        const v4u sw = mk_srd(p.Wo, DM * NQ * 2);
        const u32 vo = wst_src(lane, DM) + (u32)ng * 256u;
        auto fill = [&](int b) {
#pragma unroll 1
            for (int i = 0; i < 8; ++i) DMAIN(sw, uu((b % 5) * 32768 + (4 * i + w) * 1024), vo, uu((kq * 1024 + b * 128 + 16 * i + 4 * w) * (DM * 2)));
        };
#pragma unroll 1
        for (int b = 0; b < 5; ++b) fill(b);
        lptr tb = lds + hh * 1024 + wst_rd(lane, w);
        SFOR(b, 8)
            VMW(b < 4 ? 32 : 56 - 8 * b); BAR();
            lptr q = tb + (b % 5) * 32768;
#pragma unroll
            for (int k = 0; k < 8; ++k) { lptr a = q + (k >> 1) * 8192 + (k & 1) * 2048; wo[8 * b + k] = tr2(a, a + 4096); if (b < 7) asm volatile("" : "+a"(wo[8 * b + k])); else PIN(wo[8 * b + k]); }
            if constexpr (b < 3) { BAR(); fill(b + 5); }
        SEND
    }
    pre_wait(rs, 0, NPRE, 1u, ep);
    if (kq == 0 && ng == 0) LST(272 + L, rt32());
    MK_SRD();
    const u32 ofl = lane < 8 ? foff(F_O + L * 32 + kq * 8 + lane) : OOB, l16 = (u32)lane * 16u;
    const u32 fo = (tid == 0) ? foff(F_P + L * 32 + kq * 5 + ng) : OOB;
#define OM0(dst, b) asm volatile("s_add_u32 m0, %0, %1" :: "s"(dst), "i"((b) * 8192) : "scc", "memory")
#define ODMA(i, vo) asm volatile("buffer_load_dwordx4 %0, %1, %2 offen offset:%3 sc1 lds" :: "v"(vo), "s"(srd), "s"(((i) >> 1) * 16384), "i"(((i) & 1) * 1024) : "memory")
    auto first = [&](u32 dst, u32 vo) {
#pragma unroll
        for (int b = 0; b < 5; ++b) { OM0(dst, b); NOPS(0); ODMA(2 * b, vo); ODMA(2 * b + 1, vo); }
    };
    u32 pa = W_P + (u32)(L * NSEG) * P_ITEM + (u32)(2 * hh * 4 + kq) * (DM * 4) + (u32)(ng * 128 + w * 32 + r32) * 4u, fv = 0;
    u32 ov = W_VO + 8192u + (u32)(L * NSEG * NH + kq * 8) * 16384u + (u32)w * 2048u + l16;
    bool early = false;
    for (int s = 0; s < NSEG; ++s) {
        const u32 dst = uu((s & 1) * 65536 + w * 2048), ndst = dst ^ 65536u, need = s + 1 < NSEG ? (u32)s + 2u : 255u;
        lptr A = lds + (s & 1) * 65536 + l16;
        if (!early) {
            if (!all_(ofl == OOB || fv >= (u32)s + 1)) wait_flags(rs, ofl, (u32)s + 1, dead);
            LS3(L == 0 && w == 1, 1, kq * 5 + ng);
            first(dst, ov);
        }
#define OT(k) do {} while (0)
        OM0(dst, 5);
        asm volatile("s_waitcnt vmcnt(8)\n\ts_barrier" ::: "memory");
        OT(0);
        v4u o[64];
#pragma unroll
        for (int k = 0; k < 4; ++k) o[k] = lds_r16(A + k * 1024);
        f32x16 acc;
        u32 far, nv;
        SCHED();
        SFOR(k, 64)
            if constexpr (k < 56) M32B(k == 0, acc, o[k], wo[k]); else M32V(acc, o[k], wo[k]);
            if constexpr (k < 60) o[k + 4] = lds_r16(A + (k + 4) * 1024);
            SCHED();
            (void)&srd; (void)&dst; (void)&ndst;
            if constexpr (k < 12 && (k & 1) == 0) ODMA(10 + k / 2, ov);
            if constexpr (k == 3) asm volatile("s_waitcnt vmcnt(6)\n\ts_barrier" ::: "memory");
            if constexpr (k == 3 || k == 7) OM0(dst, (k + 21) >> 2);
            if constexpr (k == 12) { ov += NH * 16384u; PIN(ov); }
            if constexpr (k == 19) asm volatile("s_waitcnt vmcnt(6)\n\ts_barrier" ::: "memory");
            if constexpr (k == 20) LD4A(fv, ofl, 0);
            if constexpr (k == 35) asm volatile("s_waitcnt vmcnt(1)\n\ts_barrier" ::: "memory");
            if constexpr (k == 40) { asm volatile("s_waitcnt vmcnt(0)" : "+v"(fv) :: "memory"); far = (u32)__builtin_amdgcn_ballot_w64(fv < need) & 255u; PINS(far); }
            if constexpr (k == 41) { nv = far ? OOB : ov; PIN(nv); }
            if constexpr (k >= 42 && k < 62 && (k & 3) == 2) OM0(ndst, (k - 42) >> 2);
            if constexpr (k >= 43 && k < 63 && (k & 1)) ODMA((k - 43) >> 1, nv);
            if constexpr (k == 48) LD4A(fv, ofl, 0);
            if constexpr (k == 4) OT(1);
            if constexpr (k == 20) OT(2);
            if constexpr (k == 36) OT(3);
            if constexpr (k == 41) OT(4);
            SCHED();
        SEND
        OT(5);
        asm volatile("s_nop 11" : "+v"(acc), "+v"(pa));
#pragma unroll
        for (int i = 0; i < 8; ++i) st4s(rs, pa, (u32)((4 * (i >> 1) + (i & 1)) * (4 * DM * 4)), pk(acc[2 * i], acc[2 * i + 1]));
        asm volatile("s_waitcnt vmcnt(15)" : "+v"(fv) :: "memory");
        OT(6);
        LS3(L == 0 && w == 1, 2, kq * 5 + ng);
        early = far == 0u || all_(ofl == OOB || fv >= need);
        if (far != 0u && early) { first(ndst, ov); VMW(10); }
        else { fv = ld4(rs, ofl); asm volatile("s_waitcnt vmcnt(0)" : "+v"(fv) :: "memory"); }
        BAR();
        st4s(rs, fo, 0, (u32)s + 1);
        pa += P_ITEM;
        LS2(L == 0 && w == 1, 160 + kq * 5 + ng);
    }
    LDUMP(L == 0 && w == 1, kq * 5 + ng);
}

constexpr int RW_P = 20480, RW_X = RW_P + 10240;
DEV void role_row(const Params& p, lptr lds, int id, v2u ep) {
    const int lane = threadIdx.x & 63, w = uni(threadIdx.x >> 6);
    const int ri = id >> 1, L = id & 1;
    const rsrc_t rs = mk_rsrc(p.ws, W_END);
    bool dead = false, have = false;
    const u32 fp = lane < 20 ? foff(F_P + L * 32 + lane) : OOB, l16 = (u32)lane * 16u, lx = lane < 16 ? l16 + 1024u : OOB, fb = uu(w * CV_W);
    lptr F = lds + w * CV_W;
    MK_SRD();
    const v4u sx = mk_srd(p.x, ACT);
    const u32 pick = 0x3F80u << (16 * (ri & 1));
    auto yfill = [&](int b) {
#pragma unroll 1
        for (int i = 0; i < 5; ++i) DMA32(uu(fb + (b & 1) * 10240 + i * 2048), l16, uu(W_Y + (u32)(ri * 8 + b) * 10240u + i * 2048));
    };
    auto xfill = [&](int s) {
        const u32 d = uu(fb + RW_X + (s & 1) * 2048), o = uu((u32)(s * 32 + ri) * (DM * 2));
        if (L == 0) { DMAIN(sx, d, l16, o); DMAIN(sx, d + 1024, lx, o); } else { DMA16(d, l16, o + W_X2); DMA16(d + 1024, lx, o + W_X2); }
    };
    auto pfill = [&](int s) {
        const u32 o = uu(W_P + (u32)(L * NSEG + s) * P_ITEM + (u32)(ri >> 1) * (4 * DM * 4));
        SFOR(i, 10) DMA16I(fb, RW_P + i * 1024, l16, o, i * 1024); SEND
        if (L == 1) xfill(s);
    };
    if (L == 0) xfill(0);
    pre_poll(rs, 0, NPRE, 3u, ep);
    if (id == 0) LST(274, rt32());
    yfill(0); DRAIN();
    u32 fv = 0;
    for (int s = 0; s < NSEG; ++s) {
        if (!have) {
            if (!all_(fp == OOB || fv >= (u32)s + 1)) wait_flags(rs, fp, (u32)s + 1, dead);
            LS3(L == 0, 3, ri);
            LMX(L == 0, 0, s, rt32());
            pfill(s);
        }
        LS2(L == 0, 192 + ri);
        f32x2 y[5];
        {
            lptr ya = F + ((s >> 3) & 1) * 10240 + (s & 7) * 2 + lane * 16;
            auto yr = [&](lptr q) { return lo_f(*(LAS u16*)q); };
            y[0] = (f32x2){yr(ya), yr(ya + 2560)}; y[1] = (f32x2){yr(ya + 5120), yr(ya + 7680)};
            y[2] = (f32x2){yr(ya + 1024), yr(ya + 3584)}; y[3] = (f32x2){yr(ya + 6144), yr(ya + 8704)};
            lptr yc = ya - lane * 16 + 2048 + (lane >> 1) * 16 + (lane & 1) * 5120;
            y[4] = (f32x2){yr(yc), yr(yc + 2560)};
            asm volatile("" : "+v"(y[0]), "+v"(y[1]), "+v"(y[2]), "+v"(y[3]), "+v"(y[4]));
        }
        if (!have) DRAIN();
        LS3(L == 0, 4, ri);
        f32x2 x1[5];
        {
            v4u a[4], b[4]; v2u c[4];
            lptr q = F + RW_X + (s & 1) * 2048;
            v2u xa = *(LAS v2u*)(q + lane * 8), xb = *(LAS v2u*)(q + 512 + lane * 8); u32 xc = *(LAS u32*)(q + 1024 + lane * 4);
#pragma unroll
            for (int kq = 0; kq < 4; ++kq) { lptr r = F + RW_P + kq * (DM * 4); a[kq] = lds_r16(r + l16); b[kq] = lds_r16(r + 1024 + l16); c[kq] = *(LAS v2u*)(r + 2048 + lane * 8); }
            SCHED();
            auto sum4 = [&](u32 p0, u32 p1, u32 p2, u32 p3) {
                float t;
                asm("v_dot2_f32_bf16 %0, %1, %5, 0\n\tv_dot2_f32_bf16 %0, %2, %5, %0\n\tv_dot2_f32_bf16 %0, %3, %5, %0\n\tv_dot2_f32_bf16 %0, %4, %5, %0"
                    : "=&v"(t) : "v"(p0), "v"(p1), "v"(p2), "v"(p3), "s"(pick));
                return t;
            };
            f32x2 o[5];
#pragma unroll
            for (int i = 0; i < 4; ++i) { o[i >> 1][i & 1] = sum4(a[0][i], a[1][i], a[2][i], a[3][i]); o[2 + (i >> 1)][i & 1] = sum4(b[0][i], b[1][i], b[2][i], b[3][i]); }
#pragma unroll
            for (int i = 0; i < 2; ++i) o[4][i] = sum4(c[0][i], c[1][i], c[2][i], c[3][i]);
            asm volatile("s_nop 2" : "+v"(o[0]), "+v"(o[1]), "+v"(o[2]), "+v"(o[3]), "+v"(o[4]));
            const u32 xi[5] = {xa.x, xa.y, xb.x, xb.y, xc};
#pragma unroll
            for (int i = 0; i < 5; ++i) x1[i] = up2(xi[i]) + o[i];
            asm volatile("" : "+v"(x1[0]), "+v"(x1[1]), "+v"(x1[2]), "+v"(x1[3]), "+v"(x1[4]));
        }
        have = s + 1 < NSEG && all_(fp == OOB || fv >= (u32)s + 2);
        if (__builtin_expect(have, 0)) pfill(s + 1);
        if (L == 0) xfill(s + 1);
        const f32x2 st1 = ln_stats(x1);
        u32 xp[5];
#pragma unroll
        for (int i = 0; i < 5; ++i) xp[i] = pkv(x1[i] + (x1[i] - st1.x) * (y[i] * st1.y));
        const int row = s * 32 + ri;
        if (L == 0) {
            const u32 ro = (u32)row * (DM * 2);
            f32x2 x2[5];
#pragma unroll
            for (int i = 0; i < 5; ++i) x2[i] = up2(xp[i]);
            st8s(rs, (u32)lane * 8, W_X2 + ro, (v2u){xp[0], xp[1]});
            st8s(rs, (u32)lane * 8 + 512, W_X2 + ro, (v2u){xp[2], xp[3]});
            st4s(rs, (u32)lane * 4 + 1024, W_X2 + ro, xp[4]);
            const f32x2 st2 = ln_stats(x2);
            u32 hp[5];
#pragma unroll
            for (int i = 0; i < 5; ++i) hp[i] = pk2((x2[i] - st2.x) * st2.y);
            store_h(rs, W_H2, row, hp);
        } else {
            u16* o_ = p.out + (size_t)row * DM;
            *(v2u*)(o_ + 4 * lane) = (v2u){xp[0], xp[1]};
            *(v2u*)(o_ + 256 + 4 * lane) = (v2u){xp[2], xp[3]};
            *(u32*)(o_ + 512 + 2 * lane) = xp[4];
        }
        CFENCE();
        LS3(L == 0, 5, ri);
        fv = ld4(rs, fp);
        asm volatile("s_waitcnt vmcnt(0)" : "+v"(fv) :: "memory");
        st4s(rs, lane == 0 && L == 0 ? foff(F_H2 + ri) : OOB, 0, (u32)s + 1);
        if ((s & 7) == 6 && s < NSEG - 8) {
            if (s == 30) pre_poll(rs, 0, NCV, 5u, ep);
            if (s == 46) st8s(rs, (id == 1 && lane == 0) ? foff(F_EPOCH) : OOB, 0, (v2u){ep.x + 8u, ep.y});
            yfill((s >> 3) + 1);
        }
        if (ri == 0) LST(320 + L * 64 + s, rt32());
        LS2(L == 0, 224 + ri);
        LMX(L == 0, 1, s, rt32());
    }
    LDUMP(L == 0, 32 + ri);
}

constexpr float SCALE = 0.08838834764831845f, LOG2E = 1.4426950408889634f;
constexpr int C_K = 0, C_V = 8192, C_QX = 16384, C_MEM = 24576, C_Q = C_MEM + 10240, C_KM = C_Q + 2304, C_VM = C_KM + 2304, C_DUMP = C_VM + 2048,
              C_ZERO = C_DUMP + 1024, C_WL = C_ZERO + 128;
constexpr int NLF = 14;
static_assert(C_WL % 16 == 0 && C_WL + 4 * NLF * 2048 <= LDS_BYTES, "head lds");
#define CH_VIS 10
#define CH_H0 104
#define CH_W0 81
#define CH_MH 2
#define CH_MW 4
#define CH_REC 34
#define CH_LATE (-10)
#define CH_RISE 12u
#define CH_FALL 3u
#define CH_PRE 9
#define CH_T0 190
#define CH_UP 2
#define CH_WX 6
#define WAIT_TXT(tmp, t4) "s_memrealtime vcc\n\t.p2align 5\n1:\n\ts_waitcnt lgkmcnt(0)\n\ts_sub_u32 " tmp ", " t4 ", vcc_lo\n\ts_memrealtime vcc\n\ts_cmpk_lt_u32 " tmp ", 400\n\ts_cbranch_scc1 1b\n\t" \
                          "s_add_u32 " tmp ", " tmp ", 4\n\ts_cselect_b32 " tmp ", " tmp ", 0\n2:\n\ts_sub_u32 " tmp ", " tmp ", 1\n\ts_cbranch_scc0 2b\n\t"
DEV void wait_until(u32 t) { u32 tmp; asm volatile(WAIT_TXT("%0", "%1") "s_waitcnt lgkmcnt(0)" : "=&s"(tmp) : "s"(uu(t - 4u)) : "vcc", "scc", "memory"); }
#define SMF_A(acc, a, b) asm volatile("v_smfmac_f32_16x16x64_bf16 %0, %1, %2, %3" : "+v"(acc) : "v"(a), "a"(b), "v"(idx))
#define SMF_V(acc, a, b) asm volatile("v_smfmac_f32_16x16x64_bf16 %0, %1, %2, %3" : "+v"(acc) : "v"(a), "v"(b), "v"(idx))
#define MF0(d, a, b) asm volatile("v_mfma_f32_16x16x32_bf16 %0, %1, %2, 0" : "=&v"(d) : "v"(a), "v"(b))
#define MF1(d, a, b) asm volatile("v_mfma_f32_16x16x32_bf16 %0, %1, %2, %0" : "+v"(d) : "v"(a), "v"(b))
#define MF0H(d, a, b) asm volatile("v_mfma_f32_16x16x16_bf16 %0, %1, %2, 0" : "=&v"(d) : "v"(a), "v"(b))
#define MF1H(d, a, b) asm volatile("v_mfma_f32_16x16x16_bf16 %0, %1, %2, %0" : "+v"(d) : "v"(a), "v"(b))
DEV v8u tr_frag(lptr p0, lptr p1, lptr p2, lptr p3) { return cat8(tr2(p0, p1), tr2(p2, p3)); }
constexpr int qslot(int ks) { return (ks & 1) && ks < 8 ? ks >> 1 : -1; }
constexpr int kslot(int ks) { return ks < 4 ? 4 + ks : -1; }
constexpr int vslot(int ks) { return ks >= 4 ? 4 + ks : -1; }
DEV u32 ring_map(u32 off, u32 half) { return ((off >> 13) << 14) + (half << 13) + (off & 8191u); }
#define ZRD(x, i) x = __builtin_bit_cast(f32x4, lds_r16(ZP + 16 * (i)))
#define VMAX(d, a, b) asm volatile("v_max_f32 %0, %1, %2" : "=v"(d) : "v"(a), "v"(b))
#define VMAX3(d, a, b, c_) asm volatile("v_max3_f32 %0, %1, %2, %3" : "=v"(d) : "v"(a), "v"(b), "v"(c_))
#define VEXP(d, a) asm volatile("v_exp_f32 %0, %1" : "=v"(d) : "v"(a))
#define VMULC(d, a) asm volatile("v_mul_f32 %0, 0x3e0293ee, %1" : "=v"(d) : "v"(a))
#define CADD(d, a, b) do { d = (a) + (b); PIN(d); } while (0)
#define CMULC(d, a) do { d = (a) * CEXP; PIN(d); } while (0)
#define CCVT(d, a, b) do { d = pk(a, b); PIN(d); } while (0)
#define LGK(n) __builtin_amdgcn_s_waitcnt(0xC07F | (n) << 8)
#define SEL(d, a, b) asm volatile("v_cndmask_b32 %0, %1, %2, %3" : "=v"(d) : "v"(a), "v"(b), "s"(himask))
#define QPERM(d, a, q) asm volatile("v_mov_b32_dpp %0, %1 quad_perm:" q " row_mask:0xf bank_mask:0xf" : "=v"(d) : "v"(a))

#define AL_H 3
#define AL_W 0
#define HD_SMP 56
DEV void role_head(const Params& p, lptr lds, int L, int h, v2u ep) {
    const int tid = threadIdx.x, lane = tid & 63, w = uni(tid >> 6), g = lane >> 4, r = lane & 15;
    const rsrc_t rs = mk_rsrc(p.ws, W_END);
    v8u wq0[10], wq1[10], wk0[10], wk1[10], wv0[10], wv1[10];
    lptr WL = lds + C_WL + w * (NLF * 2048) + lane * 16;
    {
        constexpr int GS = 1056, CS = 48 * GS;
        const v4u sq = mk_srd(p.Wq, DM * NQ * 2), sk = mk_srd(p.Wk, DM * NQ * 2), sv = mk_srd(p.Wv, DM * NQ * 2);
        const u32 vo = (u32)(g * NQ + h * DH + 8 * (r ^ ((g & 1) << 3))) * 2u;
        auto fill = [&](int ch) {
#pragma unroll 1
            for (int i = 0; i < 4; ++i) {
                const u32 dst = uu((ch % 3) * CS + (4 * i + w) * GS), so = uu((ch * 64 + 16 * i + 4 * w) * (NQ * 2));
                DMAIN(sq, dst, vo, so); DMAIN(sk, dst + 16 * GS, vo, so); DMAIN(sv, dst + 32 * GS, vo, so);
            }
        };
        fill(0); fill(1); fill(2);
        const int q4 = r >> 2;
        lptr tb = lds + (4 * (g >> 1) + 2 * (q4 >> 1) + (g & 1)) * GS + (q4 & 1) * 256 + ((w * 64 + (r & 3) * 8) ^ ((q4 & 1) << 7));
        SFOR(ch, 10)
            VMW(ch < 8 ? 24 : 108 - 12 * ch);
            BAR();
            auto frag = [&](int m, int t) { lptr b = tb + (ch % 3) * CS + m * 16 * GS + t * 32; return tr_frag(b, b + 512, b + 8 * GS, b + 8 * GS + 512); };
            wq0[ch] = frag(0, 0); wq1[ch] = frag(0, 1); wk0[ch] = frag(1, 0); wk1[ch] = frag(1, 1); wv0[ch] = frag(2, 0); wv1[ch] = frag(2, 1);
            asm volatile("" : "+a"(wq0[ch]), "+a"(wk0[ch]), "+v"(wv0[ch]), "+v"(wv1[ch]));
            if constexpr (qslot(ch) < 0) asm volatile("" : "+a"(wq1[ch])); else asm volatile("" : "+v"(wq1[ch]));
            if constexpr (kslot(ch) < 0) asm volatile("" : "+a"(wk1[ch])); else asm volatile("" : "+v"(wk1[ch]));
            BAR();
            if constexpr (ch < 7) fill(ch + 3);
        SEND
#pragma unroll
        for (int ch = 0; ch < 10; ++ch) {
            if (qslot(ch) >= 0) { lds_w16(WL + qslot(ch) * 2048, wq1[ch].lo); lds_w16(WL + qslot(ch) * 2048 + 1024, wq1[ch].hi); }
            if (kslot(ch) >= 0) { lds_w16(WL + kslot(ch) * 2048, wk1[ch].lo); lds_w16(WL + kslot(ch) * 2048 + 1024, wk1[ch].hi); }
            if (vslot(ch) >= 0) { lds_w16(WL + vslot(ch) * 2048, wv1[ch].lo); lds_w16(WL + vslot(ch) * 2048 + 1024, wv1[ch].hi); }
        }
        CFENCE();
    }
    const u32 idx = (r & 2) ? 0xEEEEu : 0x4444u;
    unsigned long long himask = 0xCCCCCCCCCCCCCCCCull;
    PINS(himask);
    bool dead = false;
    if (h == 3) LST(266 + L, rt32());
    if (w == 0) LST(1240 + L * 32 + h, rt32());
    if (L == 1) {
        KvIn in;
        kvt_load(p, KT0 + 4 * h + w, in, [] {}); kvt_make(rs, lds + w * 9216, KT0 + 4 * h + w, in);
        DRAIN(); BAR();
        pre_post(rs, 64 + h, 3u, ep);
    }
    const u32 pol0 = pol_wait(rs, ep);
    if (h == 3 && L == 0) LST(277, rt32());
    const u32 l16 = (u32)lane * 16u;
    const u32 mring = W_MRING + (u32)L * (MB / 4) + pol0 * 8192u, hint = mring + 4 * 32768u;
    const u32 fkh = lane < 3 ? foff(F_QKV + L * 96 + lane * 32 + h) : lane < 7 ? hint + (u32)(lane - 3) * 4u : OOB;
    u32 tm = 0;
    MK_SRD();
    u32 ksrc = uu(W_KQ + (u32)(L * (NSEG - 1) * NH + h) * 16384u + w * 2048), vsrc = ksrc + (W_VO - W_KQ);
    u32 dk = uu(C_K + w * 2048), dv = dk + (C_V - C_K), dq = dk + (C_QX - C_K), dm = uu(C_MEM + w * 2048), dm2 = uu(w < 2 ? C_MEM + 8192 + w * 1024 : C_DUMP);
    asm volatile("" : "+s"(dk), "+s"(dv), "+s"(dq), "+s"(dm), "+s"(dm2));
    auto keys_wait = [&](u32 need) {
#pragma unroll 1
        for (int n = 0; !dead; ++n) {
            const bool beat = need + 2 * L > 2u;
            if (beat) wait_until(tm);
            const u32 hv = ld4(rs, lane == 7 ? foff(F_ERR) : fkh);
            if (beat) { tm = rdl(hv, 5); wait_until(rdl(hv, 3) - 30u); }
            if (((u32)__builtin_amdgcn_ballot_w64(ld4(rs, fkh) < need) & 7u) == 0u) break;
            dead = n > SPIN_CAP || rdl(hv, 7) != 0u;
            st4s(rs, dead ? foff(F_ERR) : OOB, 0, 1u);
        }
        CFENCE();
        DMA32(dk, l16, ksrc); DMA32(dv, l16, vsrc); DMA32(dq, l16, ksrc ^ 8192u);
    };
    if (L == 0) { st4s(rs, tid == 0 ? hint + 4096u + (u32)h * 128u : OOB, 0, 0u); keys_wait(1u); }
    lptr qw = lds + C_Q + (2 * g) * 288 + (32 * w + 2 * r) * 2;
    lptr kw = qw + (C_KM - C_Q); PIN(kw);
    lptr qr = lds + C_Q + (2 * (r >> 2) + (r & 1)) * 288 + g * 16;
    lptr LB = lds + l16, VX = lds + C_V + w * 2048 + l16;
    PIN(VX);
    if (tid >= 64 && tid < 70) { const u32 z = tid == 68 ? 0x3F803F80u : tid == 69 ? PSENT : 0u; lds_w16(lds + C_ZERO + (tid - 64) * 16, (v4u){z, z, z, z}); }
    u32 zaddr = 0; PIN(zaddr);
    lptr ZP = lds + zaddr + C_ZERO;
    const float CEXP = SCALE * LOG2E;
#define LF_RD(slot, hf) lf[slot][hf] = lds_r16(WL + (slot) * 2048 + (hf) * 1024)
    BAR();
    const u32 ofl = tid == 0 ? foff(F_O + L * 32 + h) : OOB;
    u32 oso = (((g | L) & 1) ? OOB : 0u) + W_ORING + (u32)L * (MB / 2) + ring_map((u32)((r >> 3) * 32768 + h * 1024 + w * 256 + (g >> 1) * 128 + (r & 7) * 16), pol0);
    unsigned long long vis[8];
#pragma unroll
    for (int i = 0; i < 8; ++i) vis[i] = __builtin_amdgcn_ballot_w64(16 * (i >> 2) + 4 * g + (i & 3) <= 16 * (w & 1) + r);
    u32 missed = 0;
    for (int s = -L; s < NSEG;) {
        u32 sv = (u32)max(s, 0), ms = mring + (sv & 3u) * 32768u + w * 2048, ms2 = mring + (sv & 3u) * 32768u + 16384u + (w & 1) * 1024;
        asm volatile("" : "+s"(ms), "+s"(ms2)); PIN(sv);
        f32x4 aq0, aq1, ak0, ak1;
        v4u lf[NLF][2];
        ZRD(aq0, 0); ZRD(aq1, 1); ZRD(ak0, 2); ZRD(ak1, 3);
#pragma unroll
        for (int i = 0; i < 4; ++i) { LF_RD(i, 0); LF_RD(i, 1); }
        SCHED();
        v4u A[10];
#define HT(k) do {} while (0)
        v4u kx[8], qf[4], kmf[4], vx[2], ones, P;
        float fa, fb, fc, fd, inv, sx[4], t[4], tm0, tm1; u32 pa, pb, fl, e01, e23, pmm;
        f32x4 av0, av1, s0, s1, sm, oa, ob, rsum;
        v2u vm, pm;
#define LW(sl) cat8(lf[sl][0], lf[sl][1])
#define PQ(ks, tl) do { if constexpr (!(tl)) SMF_A(aq0, A[ks], wq0[ks]); else if constexpr (qslot(ks) < 0) SMF_A(aq1, A[ks], wq1[ks]); else { const v8u lw_ = LW(qslot(ks)); SMF_V(aq1, A[ks], lw_); } } while (0)
#define PK(ks, tl) do { if constexpr (!(tl)) SMF_A(ak0, A[ks], wk0[ks]); else if constexpr (kslot(ks) < 0) SMF_A(ak1, A[ks], wk1[ks]); else { const v8u lw_ = LW(kslot(ks)); SMF_V(ak1, A[ks], lw_); } } while (0)
#define PV(ks, tl) do { if constexpr (!(tl)) SMF_V(av0, A[ks], wv0[ks]); else if constexpr (vslot(ks) < 0) SMF_V(av1, A[ks], wv1[ks]); else { const v8u lw_ = LW(vslot(ks)); SMF_V(av1, A[ks], lw_); } } while (0)
#define KXR(i) kx[i] = lds_r16(LB + C_K + ((i) & 1) * 4096 + ((i) >> 1) * 1024)
#define XW(q_) do { *(LAS u32*)(q_) = pa; *(LAS u32*)((q_) + 288) = pb; } while (0)
        auto slot = [&](auto zc) __attribute__((always_inline)) {
            constexpr int z = decltype(zc)::value;
            if constexpr (z < 14) PQ(z >> 1, z & 1);
            else if constexpr (z < 17) PQ(z - 7, 0);
            else if constexpr (z < 20) PQ(z - 10, 1);
            else if constexpr (z < 34) PK((z - 20) >> 1, z & 1);
            else if constexpr (z < 37) PK(z - 27, 0);
            else if constexpr (z < 40) PK(z - 30, 1);
            else if constexpr (z < 42) { if constexpr (z == 40) MF0(s0, kx[0], qf[0]); else MF0(s1, kx[1], qf[0]); }
            else if constexpr (z < 48) { if constexpr (z & 1) MF1(s1, kx[z - 40], qf[(z - 40) >> 1]); else MF1(s0, kx[z - 40], qf[(z - 40) >> 1]); }
            else if constexpr (z < 54) PV(z - 48, 0);
            else if constexpr (z == 54) MF0(sm, kmf[0], qf[0]);
            else if constexpr (z < 58) MF1(sm, kmf[z - 54], qf[z - 54]);
            else if constexpr (z < 62) PV(z - 52, 0);
            else if constexpr (z < 64) PV(z - 62, 1);
            else if constexpr (z == 64) MF0(oa, vx[0], P);
            else if constexpr (z == 65) MF0(ob, vx[1], P);
            else if constexpr (z == 66) MF0(rsum, ones, P);
            else if constexpr (z < 75) PV(z < 70 ? z - 63 : z == 70 ? 2 : z == 71 ? 7 : z == 72 ? 3 : z - 65, 1);
            else { const v2u ones2 = ones.lo; MF1H(rsum, ones2, pm); }
            if constexpr (z < 6) A[4 + z] = lds_r16(LB + C_MEM + (4 + z) * 1024);
            if constexpr (z >= 6 && z < 10) LF_RD(4 + ((z - 6) >> 1), z & 1);
            if constexpr (z >= 14 && z < 18) LF_RD(6 + ((z - 14) >> 1), z & 1);
            if constexpr (z >= 22 && z < 28) KXR(z - 22);
            if constexpr (z == 28) XW(qw);
            if constexpr (z == 29 || z == 30) KXR(z - 23);
            if constexpr (z == 32 || z == 47) asm volatile("s_barrier" ::: "memory");
            if constexpr (z >= 33 && z < 37) qf[z - 33] = lds_r16(qr + (z - 33) * 64);
            if constexpr (z == 20) ZRD(av0, 0);
            if constexpr (z == 43) XW(kw);
            if constexpr (z == 44 || z == 45) vx[z - 44] = lds_r16(VX + (z - 44) * 1024);
            if constexpr (z >= 48 && z < 52) kmf[z - 48] = lds_r16(qr + (C_KM - C_Q) + (z - 48) * 64);
            if constexpr (z == 52) ZRD(av1, 1);
            if constexpr (z == 37) ones = lds_r16(ZP + 64);
            if constexpr (z >= 57 && z < 64) LF_RD(8 + ((z - 57) >> 1), (z - 57) & 1);
            if constexpr (z >= 65 && z < 70) LF_RD(8 + ((z - 58) >> 1), (z - 58) & 1);
            if constexpr (z == HD_SMP) LD4A(fl, fkh, 0);
            SCHED();
            if constexpr (z == 19) HT(2);
            if constexpr (z == 32) HT(3);
            if constexpr (z == 47) HT(4);
            if constexpr (z == 64) HT(5);
            if constexpr (z == 19) LGK(4);
            if constexpr (z == 21 || z == 41) LGK(1);
            if constexpr (z == 56 || z == 73) LGK(0);
            if constexpr (z == 31 || z == 46) LGK(2);
            if constexpr (z == 39) LGK(4);
            if constexpr (z >= 53 && z < 56) LGK(57 - z);
            if constexpr (z == 64) LGK(3);
            if constexpr (z == 68) LGK(5);
            if constexpr (z >= 70 && z < 73) LGK(74 - z);
            SCHED();
            if constexpr (z == 18) CADD(fa, aq0[0], aq0[2]);
            if constexpr (z == 19) CADD(fb, aq0[1], aq0[3]);
            if constexpr (z == 20) CMULC(fa, fa);
            if constexpr (z == 21) CADD(fc, aq1[0], aq1[2]);
            if constexpr (z == 22) CADD(fd, aq1[1], aq1[3]);
            if constexpr (z == 23) CMULC(fc, fc);
            if constexpr (z == 24) CMULC(fb, fb);
            if constexpr (z == 25) CMULC(fd, fd);
            if constexpr (z == 26) CCVT(pa, fa, fc);
            if constexpr (z == 27) CCVT(pb, fb, fd);
            if constexpr (z == 38) CADD(fa, ak0[0], ak0[2]);
            if constexpr (z == 39) CADD(fb, ak0[1], ak0[3]);
            if constexpr (z == 41) { CADD(fc, ak1[0], ak1[2]); CADD(fd, ak1[1], ak1[3]); }
            if constexpr (z == 42) { CCVT(pa, fa, fc); CCVT(pb, fb, fd); }
            if constexpr (z >= 49 && z < 53) SEL(sx[z - 49], s0[z - 49], s1[z - 49]);
            if constexpr (z >= 53 && z < 57) VEXP(t[z - 53], sx[z - 53]);
            if constexpr (z == 57) CCVT(e01, t[0], t[1]);
            if constexpr (z == 58) CCVT(e23, t[2], t[3]);
            if constexpr (z == 59) QPERM(P.x, e01, "[0,1,0,1]");
            if constexpr (z == 60) QPERM(P.z, e01, "[2,3,2,3]");
            if constexpr (z == 61) QPERM(P.y, e23, "[0,1,0,1]");
            if constexpr (z == 62) QPERM(P.w, e23, "[2,3,2,3]");
            if constexpr (z == 63) CADD(fa, av0[0], av0[2]);
            if constexpr (z == 64) { asm volatile("" :: "v"(sm)); VEXP(tm0, sm[0]); }
            if constexpr (z == 65) VEXP(tm1, sm[1]);
            if constexpr (z == 66) CADD(fb, av0[1], av0[3]);
            if constexpr (z == 67) CCVT(pmm, tm0, tm1);
            if constexpr (z >= 68 && z < 72) SEL(oa[z - 68], oa[z - 68], ob[z - 68]);
            if constexpr (z == 72) asm volatile("v_cndmask_b32 %0, %1, 0, %2" : "=v"(pm.x) : "v"(pmm), "s"(himask));
            if constexpr (z == 73) asm volatile("v_cndmask_b32 %0, 0, %1, %2" : "=v"(pm.y) : "v"(pmm), "s"(himask));
            if constexpr (z == 74) LGK(15);
            if constexpr (z == 75) CCVT(vm.x, fa, fb);
            SCHED();
        };
        {
            u32 tmp;
            asm volatile("s_mov_b32 m0, %2\n\t" WAIT_TXT("%0", "%1") "buffer_load_dwordx4 %4, %5, %6 offen sc1 lds\n\tbuffer_load_dwordx4 %4, %5, %6 offen offset:1024 sc1 lds\n\t"
                         "s_mov_b32 m0, %3\n\ts_nop 0\n\tbuffer_load_dwordx4 %4, %5, %7 offen sc1 lds\n\t.p2align 6\n\t.rept %8\n\ts_nop 0\n\t.endr\n\ts_waitcnt vmcnt(0) lgkmcnt(0)\n\ts_barrier"
                         : "=&s"(tmp) : "s"(uu(tm - 4u)), "s"(dm), "s"(dm2), "v"(l16), "s"(srd), "s"(ms), "s"(ms2), "i"(AL_H) : "vcc", "scc", "memory");
        }
        HT(1);
#pragma unroll
        for (int ks = 0; ks < 4; ++ks) A[ks] = lds_r16(LB + C_MEM + ks * 1024);
        SCHED();
        st4s(rs, ofl, 0, sv);
        SCHED();
        static_for<0, 76>(slot);
        asm volatile("s_nop 2\n\tv_add_f32 %1, %3, %5\n\tv_add_f32 %2, %4, %6\n\tv_cvt_pk_bf16_f32 %0, %1, %2"
                     : "=v"(vm.y), "=&v"(fc), "=&v"(fd) : "v"(av1[0]), "v"(av1[1]), "v"(av1[2]), "v"(av1[3]));
        __builtin_amdgcn_s_waitcnt(0xCF7F);
        asm volatile("s_nop 0\n\tv_mfma_f32_16x16x16_bf16 %0, %2, %3, %0\n\tv_rcp_f32 %1, %4" : "+v"(oa), "=&v"(inv) : "v"(vm), "v"(pm), "v"(rsum[0]), "v"(rsum));
        SCHED();
        if (__builtin_expect(__builtin_amdgcn_ballot_w64(u_(rsum[0]) - 0x21800000u > 0x3C000000u) != 0ull, 0)) {
            DRAIN(); SCHED();
            bool miss = false;
#pragma unroll 1
            for (int i = 0; i < 10; ++i) { const v4u a = lds_r16(LB + C_MEM + i * 1024); miss |= a.x == PSENT || a.y == PSENT || a.z == PSENT || a.w == PSENT; }
            if (missed < SPIN_CAP && !all_(!miss)) {
                *(LAS u32*)(lds + C_DUMP + (w == 0 ? 0 : 64)) = ld4(rs, foff(F_ERR));
                asm volatile("" : "+v"(fl) :: "memory");
                tm = rdl(fl, 5);
                LAD(w == 0, 1, L * 64 + s);
                BAR();
                missed = uu(*(LAS u32*)(lds + C_DUMP)) != 0u ? SPIN_CAP : missed + 1;
                st4s(rs, missed == SPIN_CAP ? foff(F_ERR) : OOB, 0, 1u);
                BAR();
                continue;
            }
            float mx = fmaxf(fmaxf(fmaxf(sx[0], sx[1]), fmaxf(sx[2], sx[3])), fmaxf(sm[0], sm[1]));
            mx = max32(max16(fmaxf(mx, dpp<0x4E>(mx))));
#pragma unroll
            for (int i = 0; i < 4; ++i) t[i] = __builtin_amdgcn_exp2f(sx[i] - mx);
            e01 = pk(t[0], t[1]); e23 = pk(t[2], t[3]);
            pmm = pk(__builtin_amdgcn_exp2f(sm[0] - mx), __builtin_amdgcn_exp2f(sm[1] - mx));
            asm volatile("v_cndmask_b32 %0, %2, 0, %3\n\tv_cndmask_b32 %1, 0, %2, %3" : "=&v"(pm.x), "=&v"(pm.y) : "v"(pmm), "s"(himask));
            asm volatile("s_nop 1" : "+v"(e01), "+v"(e23), "+v"(pm));
            QPERM(P.x, e01, "[0,1,0,1]"); QPERM(P.z, e01, "[2,3,2,3]"); QPERM(P.y, e23, "[0,1,0,1]"); QPERM(P.w, e23, "[2,3,2,3]");
            NOPS(1);
            MF0(rsum, ones, P); MF0(oa, vx[0], P); MF0(ob, vx[1], P);
            NOPS(7);
            { const v2u ones2 = ones.lo; MF1H(rsum, ones2, pm); }
#pragma unroll
            for (int i = 0; i < 4; ++i) SEL(oa[i], oa[i], ob[i]);
            NOPS(1);
            MF1H(oa, vm, pm);
            asm volatile("s_nop 7\n\tv_rcp_f32 %0, %1" : "=v"(inv) : "v"(rsum[0]), "v"(rsum));
        }
        {
            v4u oc; f32x2 u0 = oa.lo, u1 = oa.hi; const f32x2 i2 = {inv, inv};
            asm volatile("s_nop 0\n\tv_pk_mul_f32 %0, %0, %2\n\tv_pk_mul_f32 %1, %1, %2" : "+v"(u0), "+v"(u1) : "v"(i2));
            __builtin_amdgcn_s_waitcnt(0xCF7F);
            asm volatile("v_cvt_pk_bf16_f32 %0, %4, %5\n\tv_cvt_pk_bf16_f32 %2, %4, %5\n\tv_cvt_pk_bf16_f32 %1, %6, %7\n\tv_cvt_pk_bf16_f32 %3, %6, %7\n\t"
                         "v_permlane16_swap_b32 %0, %2\n\ts_nop 0\n\tv_permlane16_swap_b32 %1, %3"
                         : "=&v"(oc.x), "=&v"(oc.y), "=&v"(oc.z), "=&v"(oc.w) : "v"(u0.x), "v"(u0.y), "v"(u1.x), "v"(u1.y));
            st16(rs, oso, oc);
        }
        SCHED();
        unsigned long long clk;
        asm volatile("s_memrealtime %0" : "=s"(clk) :: "memory");
        HT(6);
        v4u tq[4], tk[8], tv[2];
        f32x4 c0, c1, cm;
        u32 oo, kvo; bool kvok;
        u32 ln = l16; PIN(ln);
        lptr QX = lds + C_QX + (w & 1) * 4096 + ln, VO = lds + C_V + (w ^ 1) * 2048 + ln, VMX = lds + C_VM + w * 512 + (ln >> 1);
#pragma unroll
        for (int i = 0; i < 2; ++i) { tq[i] = lds_r16(QX + i * 1024); kmf[i] = lds_r16(qr + (C_KM - C_Q) + i * 64); tk[2 * i] = lds_r16(LB + C_K + i * 1024); tk[2 * i + 1] = lds_r16(LB + C_K + 4096 + i * 1024); }
        *(LAS v2u*)VMX = vm;
        SCHED();
        SFOR(i, 12)
            if constexpr (i == 0) MF0(cm, kmf[0], tq[0]); else if constexpr (i == 1) MF0(c0, tk[0], tq[0]); else if constexpr (i == 2) MF0(c1, tk[1], tq[0]);
            else if constexpr (i % 3 == 0) MF1(cm, kmf[i / 3], tq[i / 3]); else if constexpr (i % 3 == 1) MF1(c0, tk[2 * (i / 3)], tq[i / 3]); else MF1(c1, tk[2 * (i / 3) + 1], tq[i / 3]);
            if constexpr (i < 2) kmf[2 + i] = lds_r16(qr + (C_KM - C_Q) + (2 + i) * 64);
            if constexpr (i == 0) { tq[2] = lds_r16(QX + 2048); tk[4] = lds_r16(LB + C_K + 2048); }
            if constexpr (i == 1) { tk[5] = lds_r16(LB + C_K + 6144); tq[3] = lds_r16(QX + 3072); }
            if constexpr (i == 2) { tk[6] = lds_r16(LB + C_K + 3072); tk[7] = lds_r16(LB + C_K + 7168); }
            if constexpr (i == 3) { tv[0] = lds_r16(VO); tv[1] = lds_r16(VO + 1024); }
            if constexpr (i == 4) { oo = ln + (ln & 0x300u) + ((vsrc ^ 8192u) + (u32)((w & 1) * 256) + ((u32)s & OOB)); PIN(oo); }
            if constexpr (i == 5) { ksrc += NH * 16384; vsrc += NH * 16384; PINS(ksrc); PINS(vsrc); }
            if constexpr (i == 9) { asm volatile("s_waitcnt vmcnt(1)" : "+v"(fl) :: "memory"); kvok = ((u32)__builtin_amdgcn_ballot_w64(fl < (u32)s + 2) & 7u) == 0u; }
            if constexpr (i == 10) { kvo = l16 | (kvok ? 0u : OOB); PIN(kvo); }
            SCHED();
        SEND
        asm volatile("s_waitcnt lgkmcnt(0)\n\ts_barrier" : "+s"(clk) :: "memory");
        const v2u tvm = *(LAS v2u*)(VMX + (512 - (w & 1) * 1024));
        const v4u pv = lds_r16(ZP + 80);
        DMA32(dk, kvo, ksrc);
        SCHED(); asm volatile("" :: "v"(cm));
        float u[10], m, m2;
        {
            float NEG = -1000000000.0f / SCALE; PIN(NEG);
#define MSK(d, a, i) asm volatile("v_cndmask_b32 %0, %1, %2, %3" : "=v"(d) : "v"(NEG), "v"(a), "s"(vis[i]))
#pragma unroll
            for (int i = 0; i < 4; ++i) MSK(u[i], c0[i], i);
#pragma unroll
            for (int i = 0; i < 4; ++i) MSK(u[4 + i], c1[i], 4 + i);
            asm volatile("v_max3_f32 %0, %2, %3, %10\n\tv_max3_f32 %1, %4, %5, %11\n\tv_max3_f32 %0, %0, %6, %7\n\tv_max3_f32 %1, %1, %8, %9\n\t"
                         "v_max_f32 %0, %0, %1\n\tv_mov_b32 %1, %0\n\ts_nop 1\n\tv_permlane16_swap_b32 %0, %1\n\tv_max_f32 %0, %0, %1\n\tv_mov_b32 %1, %0\n\ts_nop 1\n\t"
                         "v_permlane32_swap_b32 %0, %1\n\tv_max_f32 %0, %0, %1\n\tv_mul_f32 %0, 0xbe0293ee, %0"
                         : "=&v"(m), "=&v"(m2) : "v"(u[0]), "v"(u[1]), "v"(u[2]), "v"(u[3]), "v"(u[4]), "v"(u[5]), "v"(u[6]), "v"(u[7]), "v"(cm[0]), "v"(cm[1]));
        }
        DMA32(dv, kvo, vsrc);
        {
            v4u px; v2u p0, p1;
#define EX2(d, a, b) asm volatile("v_fmamk_f32 %0, %2, 0x3e0293ee, %4\n\tv_fmamk_f32 %1, %3, 0x3e0293ee, %4\n\tv_exp_f32 %0, %0\n\tv_exp_f32 %1, %1" : "=&v"(d.x), "=&v"(d.y) : "v"(a), "v"(b), "v"(m))
            f32x2 ex[5];
            EX2(ex[0], u[0], u[1]); EX2(ex[1], u[2], u[3]); EX2(ex[2], u[4], u[5]); EX2(ex[3], u[6], u[7]); EX2(ex[4], cm[0], cm[1]);
            asm volatile("v_cvt_pk_bf16_f32 %0, %8, %9\n\tv_cvt_pk_bf16_f32 %1, %10, %11\n\tv_cvt_pk_bf16_f32 %2, %12, %13\n\tv_cvt_pk_bf16_f32 %3, %14, %15\n\t"
                         "v_mov_b32 %5, 0\n\tv_mov_b32 %6, 0\n\tv_cvt_pk_bf16_f32 %4, %16, %17\n\tv_cvt_pk_bf16_f32 %7, %16, %17"
                         : "=&v"(px.x), "=&v"(px.y), "=&v"(px.z), "=&v"(px.w), "=&v"(p0.x), "=&v"(p0.y), "=&v"(p1.x), "=&v"(p1.y)
                         : "v"(ex[0].x), "v"(ex[0].y), "v"(ex[1].x), "v"(ex[1].y), "v"(ex[2].x), "v"(ex[2].y), "v"(ex[3].x), "v"(ex[3].y), "v"(ex[4].x), "v"(ex[4].y));
            f32x4 o[4], sum;
            const v2u ones2 = ones.lo;
            const u32 now = (u32)clk;
            u32 oz;
            SCHED();
            MF0(sum, ones, px); { const u32 q = ksrc ^ 8192u; DMA32(dq, kvo, q); } SCHED();
            MF0(o[0], vx[0], px);
            MF0(o[1], vx[1], px);
            MF0(o[2], tv[0], px); oz = (oso & ~(3u << 17)) | (u32)((s + 3) & 3) << 17; PIN(oz); SCHED();
            MF0(o[3], tv[1], px); oso = (oso & ~(3u << 17)) | (u32)((s + 1) & 3) << 17; PIN(oso); SCHED();
            MF1H(sum, ones2, p0);
            MF1H(o[0], vm, p0);
            MF1H(o[1], vm, p1); st16(rs, oz, pv); SCHED();
            MF1H(o[2], tvm, p0);
            MF1H(o[3], tvm, p1);
            asm volatile("s_nop 2\n\tv_rcp_f32 %0, %1\n\ts_nop 0" : "=v"(inv) : "v"(sum[0]), "v"(sum));
            asm volatile("" : "+v"(o[0]), "+v"(o[1]));
            st16(rs, oo, (v4u){pk2(o[0].lo * inv), pk2(o[0].hi * inv), pk2(o[1].lo * inv), pk2(o[1].hi * inv)});
            asm volatile("" : "+v"(o[2]), "+v"(o[3]));
            st16(rs, oo ^ 2048u, (v4u){pk2(o[2].lo * inv), pk2(o[2].hi * inv), pk2(o[3].lo * inv), pk2(o[3].hi * inv)});
            SCHED();
            LS2(L == 0 && w == 0, h);
            st4s(rs, ((h & 15) == 0 && tid == 192) ? hint + 128u + (u32)h * 8u : OOB, 0, (u32)s << 16 | min(missed, 1u) << 14 | min(now - tm, 0x3FFFu));
            tm = rdl(fl, 5);
            missed = missed == SPIN_CAP ? missed : 0u;
            if (__builtin_expect((int)(rdl(fl, 3) - now) < CH_LATE || s < 1 - L, 0)) {
                const bool first = s == 0 && L == 0;
                LAD(w == 0 && s > 0, 3, L * 64 + s);
                if (s < 0) oso -= (ln & 256u) ? 0u : OOB;
#pragma unroll 1
                for (int n = 0; n < (s < 0 ? 0 : first ? 200 : 8); ++n) {
                    CFENCE();
                    const u32 hv = ld4(rs, fkh);
                    tm = rdl(hv, first ? 6 : 5);
                    if (first ? tm != PSENT : (int)(rdl(hv, 3) - now) >= CH_LATE) break;
                }
            }
        }
        if (__builtin_expect(!kvok, 0) && s + 1 < NSEG) {
            st4s(rs, ln == 0 ? hint + 4096u + (u32)h * 128u : OOB, 0, (u32)s + 1u);
            LAD(true, 0, L * 64 + s + 1);
            keys_wait((u32)s + 2u);
        }
        ++s;
    }
    DRAIN(); BAR();
    st4s(rs, ofl, 0, NSEG);
}

#define WO_NL 3
#define WO_ROT 0
#define WO_AH 1
DEV void role_wo(const Params& p, lptr lds, int j, v2u ep) {
    const int tid = threadIdx.x, lane = tid & 63, w = uni(tid >> 6), g = lane >> 4, r = lane & 15;
    const int rg = j & 1, cg = j >> 1, rot = WO_ROT;
    const rsrc_t rs = mk_rsrc(p.ws, W_END);
    v8u wo[8][5];
    {
        const v4u so_ = mk_srd(p.Wo, DM * NQ * 2);
        u32 vo[5];
#pragma unroll
        for (int i = 0; i < 5; ++i) { const int n = 64 * i + lane; vo[i] = (u32)((n / 5) * DM + cg * 40 + (n % 5) * 8) * 2u; }
        v4u junk = {};
        const u32 av = (u32)(j * 5120 + lane * 16);
        auto ahead = [&](int b) {
#pragma unroll
            for (int i = 0; i < 5; ++i) AHEAD(so_, av, uu((8 * w + b) * (DH * DM * 2) + i * 1024));
        };
        auto fill = [&](int b) {
            if (b + WO_AH < 8) ahead(b + WO_AH);
            const u32 dst = uu(w * 40960 + (b & 3) * 10240), src = uu((8 * w + ((b + rot) & 7)) * (DH * DM * 2));
#pragma unroll
            for (int i = 0; i < 10; ++i) DMAIN(so_, dst + i * 1024, vo[i % 5], src + (i / 5) * (64 * DM * 2));
        };
#pragma unroll 1
        for (int b = 0; b < WO_AH; ++b) ahead(b);
#pragma unroll 1
        for (int b = 0; b < 4; ++b) fill(b);
        const int q4 = r >> 2;
        lptr tb = lds + w * 40960 + (32 * (g >> 1) + 16 * (q4 >> 1) + 8 * ((r >> 1) & 1) + 4 * (g & 1) + (q4 & 1)) * 80 + (r & 1) * 8;
        SFOR(b, 8)
            VMW((b < 5 ? 30 : 70 - 10 * b) + (b + 1 + WO_AH < 8 ? 5 : 0) + (b + 2 + WO_AH < 8 ? 5 : 0) + (b + 3 + WO_AH < 8 ? 5 : 0));
            if constexpr (b + WO_AH == 7) PIN(junk);
#pragma unroll
            for (int t = 0; t < 5; ++t) {
                lptr q = tb + (b & 3) * 10240 + t * 16;
                wo[b][t] = tr_frag(q, q + 160, q + 64 * 80, q + 64 * 80 + 160);
                if (b * 5 + t < 32) asm volatile("" : "+a"(wo[b][t])); else asm volatile("" : "+v"(wo[b][t]));
            }
            CFENCE();
            if constexpr (b < 4) fill(b + 4);
        SEND
    }
    if (j == 0) LST(265, rt32());
    LST(1100 + j * 4 + w, rt32());
    const u32 idx = (r & 2) ? 0xEEEEu : 0x4444u;
    const u32 pol0 = pol_wait(rs, ep);
    BAR();
    const u32 l16 = (u32)lane * 16u;
    u32 oo[8];
#pragma unroll
    for (int i = 0; i < 8; ++i) oo[i] = uu(W_ORING + ring_map((u32)(rg * 32768 + (8 * w + ((i + rot) & 7)) * 1024), pol0));
    const u32 hint = W_MRING + 4 * 32768u + pol0 * 8192u;
    u32 hvo[2];
#pragma unroll
    for (int l = 0; l < 2; ++l) hvo[l] = lane < 2 ? hint + (u32)(l ^ 1) * (MB / 4) + (u32)lane * 4u : lane == 2 ? hint + (u32)l * (MB / 4) + 128u : lane == 3 ? foff(F_ERR) : lane == 4 ? hint + (u32)l * (MB / 4) + 256u :
                 (lane >= 32 && j == 0 && w == 3) ? hint + (u32)l * (MB / 4) + (u32)lane * 128u : OOB;
    lptr xw = r < 8 ? lds + w * 640 + (2 * (g & 1) + (g >> 1)) * 32 + r * 4 : lds + 8192 + lane * 4;
    const int G = ((5 * cg) >> 1) + w, tt = 2 * G + ((lane >> 3) & 1) - 5 * cg;
    const bool pub = w < 3 && lane < 32 && tt >= 0 && tt < 5;
    lptr xr = lds + (pub ? tt * 128 + (2 * (lane >> 4) + ((lane >> 2) & 1)) * 32 + (lane & 3) * 8 : 0);
    const u32 mpo = pub ? W_MRING + ring_map((u32)(G * 256 + rg * 128 + lane * 4), pol0) : OOB;
    constexpr u32 PER0 = CH_W0 + CH_MW + CH_H0 + CH_MH + 2 * CH_VIS, DW0 = CH_W0 + CH_VIS + CH_MW + CH_REC;
    const bool lead = j == 0 && w == 3;
    const u32 hso = (lead && lane == 0) ? hint : OOB;
    MK_SRD();
    u32 t = 0, tn = 0;
    int sn[2] = {0, 0};
    u32 hav = 8 * CH_H0, wav = 8 * CH_W0, mh = 8 * CH_MH, mw = 8 * CH_MW, ok[2] = {0u, 0u}, dw[2] = {DW0, DW0};
    u32 vo = l16, early = PSENT, tp = 0, fin, left, cnt = 0;
    unsigned long long clk = 0;
    {
        if (w == 0) LST(1310 + j, rt32());
        if (lead) {
#pragma unroll 1
            for (int n = 0; n < SPIN_CAP; ++n) {
                CFENCE();
                const u32 a = ld4(rs, foff(F_QKV + lane)), b = ld4(rs, lane < 32 ? foff(F_QKV + 64 + lane) : hvo[0]);
                if (all_(a != 0u && b != (lane < 32 ? 0u : PSENT))) break;
                __builtin_amdgcn_s_sleep(1);
            }
        }
        if (lead) {
            t = rt32() + CH_T0; tn = t + PER0 / 2 + CH_REC;
            st16(rs, lane < 2 ? hint + (u32)lane * (MB / 4) : OOB, (v4u){lane == 0 ? t : tn, PER0, (lane == 0 ? t : tn) + DW0, t + DW0});
        } else {
#pragma unroll 1
            for (int n = 0; n < SPIN_CAP; ++n) {
                CFENCE();
                const u32 hv = ld4(rs, hvo[1]);
                t = rdl(hv, 0);
                if (rdl(hv, 1) < 400u) break;
                __builtin_amdgcn_s_sleep(1);
            }
        }
        clk = t - 100u;
    }
    const u32 tm0 = t + DW0;
    u32 zero = 0, hv = 0xFFFFu, pst = 1, pmp = OOB;
    const u32 psent = PSENT;
    lptr sw = lds + 2560 + w * 4;
    asm volatile("" : "+v"(zero), "+v"(sw));
    auto turn = [&](auto lc) __attribute__((always_inline)) {
        constexpr int L = decltype(lc)::value, M = L ^ 1;
        u32 done, rep, rd, lost, dh, dn, nt, za, pdur, mp; v4u hw;
        const int sc = sn[L];
        auto books = [&](auto ic) __attribute__((always_inline)) {
            constexpr int i = decltype(ic)::value;
            if constexpr (i == 0) { rep = max(rdl(hv, 2), rdl(hv, 4)); PINS(rep); }
            if constexpr (i == 1) { lost = (u32)(__builtin_amdgcn_ballot_w64(hv == (u32)sn[M]) >> 32); PINS(lost); }
            if constexpr (i == 2) { lost |= (rep >> 16) ^ (u32)sn[M]; PINS(lost); }
            if constexpr (i == 3) { lost |= ok[M] ^ 2u; rd = rep & 0xFFFFu; PINS(lost); PINS(rd); }
            if constexpr (i == 4) { lost = lost == 0u ? CH_UP * 8u : 0u; PINS(lost); }
            if constexpr (i == 5) { pdur = ((u32)clk - tp) * 8u; PINS(pdur); pdur = min(pdur, wav + CH_RISE); PINS(pdur); }
            if constexpr (i == 6) { pdur = (done & ok[M]) ? pdur : wav; PINS(pdur); }
            if constexpr (i == 7) { wav = max(pdur, wav - CH_FALL); PINS(wav); }
            if constexpr (i == 8) { nt = min(rd * 8u, hav + CH_RISE); PINS(nt); }
            if constexpr (i == 9) { nt = rd < 0x3FFFu ? nt : hav; PINS(nt); }
            if constexpr (i == 10) { hav = max(nt, hav - CH_FALL); PINS(hav); }
            if constexpr (i == 11) { mw = max(mw - 1u, CH_MW * 8u); PINS(mw); }
            if constexpr (i == 12) { mh = max(mh - 1u, CH_MH * 8u); PINS(mh); }
            if constexpr (i == 13) { rd = rd >> 14 == 1u ? lost : 0u; PINS(rd); }
            if constexpr (i == 14) { mw += rd; mh += lost - rd; PINS(mw); PINS(mh); }
            if constexpr (i == 15) { ok[M] = done ? 1u : (ok[M] & 1u) << 1; PINS(ok[M]); }
            if constexpr (i == 16) { dh = (hav + mh) / 8u + (sn[1] < 3 ? CH_VIS + CH_PRE : CH_VIS); PINS(dh); }
            if constexpr (i == 17) { dn = (wav + mw) / 8u + CH_VIS; PINS(dn); }
            if constexpr (i == 18) { nt = dw[L] + dh + (u32)max((int)((u32)clk + CH_WX - t), 0); PINS(nt); }
            if constexpr (i == 19) { dh += dn; dw[L] = dn; sn[M] += (int)done; PINS(dh); PINS(sn[M]); }
            if constexpr (i == 20) { rd = tn - t + (dh >> 1); PINS(rd); }
            if constexpr (i == 22) { nt = t + (u32)max((int)nt, (int)rd); PINS(nt); }
            if constexpr (i == 23) { hw.x = nt; hw.y = dh; PIN(hw.x); PIN(hw.y); }
            if constexpr (i == 24) { hw.z = nt + dn; hw.w = tm0; PIN(hw); }
            if constexpr (i == 25) st16(rs, hso + L * (MB / 4), hw);
        };
        {
#define WT(k) do {} while (0)
            WT(0);
            v4u d[8];
            {
                u32 tmp;
                asm volatile(WAIT_TXT("%1", "%2") "buffer_load_dwordx4 %0, %3, %4, %5 offen sc1\n\ts_waitcnt lgkmcnt(0)"
                             : "=&v"(d[0]), "=&s"(tmp) : "s"(uu(t - 4u)), "v"(vo), "s"(srd), "s"(oo[0]) : "vcc", "scc", "memory");
                PINS(clk);
            }
            SFOR(i, 7)
                asm volatile("buffer_load_dwordx4 %0, %1, %2, %3 offen sc1" : "=v"(d[i + 1]) : "v"(vo), "s"(srd), "s"(oo[i + 1]) : "memory");
                SCHED();
                if constexpr (i == 0) { done = uu(pst) ^ 1u; PINS(done); }
                static_for<3 * i, 3 * i + 3>(books);
                SCHED();
            SEND
            LD4A(hv, hvo[L], 0);
            SCHED();
            WT(1);
            static_for<22, 26>(books);
            za = done ? pmp : OOB;
            st4s(rs, za, 0, psent);
            *(LAS u32*)(sw + L * 4096) = zero;
            f32x4 acc[5] = {};
            mp = mpo + (L * (MB / 4) + (u32)((sc + 1) & 3) * 32768u); pmp = mpo + (L * (MB / 4) + (u32)(sc & 3) * 32768u);
            asm volatile("" : "+v"(acc[0]), "+v"(acc[1]), "+v"(acc[2]), "+v"(acc[3]), "+v"(acc[4]), "+v"(mp), "+v"(pmp));
            SCHED();
            float f0[5], f1[5]; unsigned long long bad;
#define WSW(o_) asm volatile("v_permlane32_swap_b32 %0, %1" : "+v"(f0[o_]), "+v"(f1[o_]))
#define WAD(o_) asm volatile("v_add_f32_dpp %0, %1, %0 row_ror:8 row_mask:0xf bank_mask:0xf" : "+v"(f0[o_]) : "v"(f1[o_]))
#define WFO(o_) do { VADD(f0[o_], acc[o_][0], acc[o_][2]); VADD(f1[o_], acc[o_][1], acc[o_][3]); } while (0)
#define WWR(o_) *(LAS float*)(xw + L * 4096 + (o_) * 128) = f0[o_]
            u32 th, per, so, erl = early;
            auto next = [&](auto ic_) __attribute__((always_inline)) {
                constexpr int i = decltype(ic_)::value;
                if constexpr (i == 0) { so = (u32)sn[M] << 17; PINS(so); }
                if constexpr (i == 1) { so = (so & 0x60000u) + M * (MB / 2); PINS(so); }
                if constexpr (i == 2) { so = sn[M] + 1 < NSEG ? so : OOB; PINS(so); }
                if constexpr (i == 3) { early = sn[M] + 1 < NSEG ? PSENT : 0u; PIN(early); }
                if constexpr (i == 4) { vo = l16 + so; PIN(vo); }
                if constexpr (i == 5 && L) { fin = (u32)(sn[0] + sn[1] + 2); PINS(fin); }
                if constexpr (i == 6 && L) { fin = (fin | uu(left)) >> 7; PIN(fin); }
                if constexpr (i == 6 && !L) { cnt += 1u; PIN(cnt); left = cnt >> 9; PIN(left); }
                if constexpr (i == 7) { PIN(hv); th = rdl(hv, 0); PINS(th); }
                if constexpr (i == 8) { per = rdl(hv, 1); PINS(per); so = th - t; PINS(so); }
                if constexpr (i == 9) { per = (int)so < 20 ? per : 0u; PINS(per); }
                if constexpr (i == 10) { th += per; PINS(th); if constexpr (L) { per = rdl(hv, 3); PINS(per); } }
                if constexpr (i == 11) { tp = t; t = lead ? tn : th; PINS(t); }
                if constexpr (i == 12) { if constexpr (L) { fin |= per; PIN(fin); } tn = nt; }
            };
            auto slot = [&](auto ic_) __attribute__((always_inline)) {
                constexpr int i = decltype(ic_)::value;
                constexpr int NL = WO_NL, I0 = 40 - 5 * NL, pc = i < I0 ? i / 5 : 8 - NL + (i - I0) % NL, oc = i < I0 ? i % 5 : (i - I0) / NL;
                if constexpr (oc == 0) asm volatile("s_waitcnt vmcnt(%1)" : "+v"(d[pc]) : "i"(10 - pc));
                if constexpr (pc * 5 + oc < 32) SMF_A(acc[oc], d[pc], wo[pc][oc]); else SMF_V(acc[oc], d[pc], wo[pc][oc]);
                SCHED();
                if constexpr (i == 0) WT(2);
                if constexpr (i >= 5 && i < 13 && i != 9) next(IC<i - 5 - (i > 9)>{});
                if constexpr (i == 20) WT(4);
                constexpr int e = i - (I0 + NL + 1), o = e < 0 ? 0 : e / 3;
                if constexpr (e >= 0 && e % 3 == 0) { CADD(f0[o], acc[o][0], acc[o][2]); if constexpr (o > 0) WAD(o - 1); }
                if constexpr (e >= 0 && e % 3 == 1) { if constexpr (o > 0) { WWR(o - 1); SCHED(); } CADD(f1[o], acc[o][1], acc[o][3]); }
                if constexpr (e >= 0 && e % 3 == 2) {
                    if constexpr (o == 0) asm volatile("v_cmp_u_f32 %0, %1, %2" : "=s"(bad) : "v"(f0[0]), "v"(f1[0]));
                    else LGK(15);
                    WSW(o);
                }
                SCHED();
            };
            asm volatile(".rept %0\n\ts_nop 0\n\t.endr" :: "i"(AL_W));
            static_for<0, 2>(slot);
            if (__builtin_expect(__builtin_amdgcn_ballot_w64(d[0].x == erl) != 0ull, 0)) {
                asm volatile("s_waitcnt vmcnt(3)" :: "v"(d[1]), "v"(d[2]), "v"(d[3]), "v"(d[4]), "v"(d[5]), "v"(d[6]), "v"(d[7]) : "memory");
                SCHED();
                *(LAS u32*)(sw + L * 4096) = 1u;
                static_for<0, 7>(next);
            } else {
            static_for<2, 40>(slot);
            WT(5);
            static_assert(WO_NL == 3, "tail");
            if (__builtin_expect(bad != 0ull, 0)) {
                bool miss = false;
#pragma unroll
                for (int i = 0; i < 8; ++i) miss |= d[i].x == PSENT;
                if (all_(!miss)) {
#pragma unroll
                    for (int i = 0; i < 8; ++i) miss |= d[i].y == PSENT || d[i].z == PSENT || d[i].w == PSENT;
                }
                *(LAS u32*)(sw + L * 4096) = all_(!miss) ? 0u : 1u;
            }
            asm volatile("v_permlane32_swap_b32 %0, %1\n\ts_nop 1\n\tv_add_f32_dpp %0, %1, %0 row_ror:8 row_mask:0xf bank_mask:0xf\n\tv_add_f32 %2, %4, %6\n\tv_add_f32 %3, %5, %7"
                         : "+v"(f0[3]), "+v"(f1[3]), "=&v"(f0[4]), "=&v"(f1[4]) : "v"(acc[4][0]), "v"(acc[4][1]), "v"(acc[4][2]), "v"(acc[4][3]));
            WWR(3); SCHED();
            asm volatile("s_nop 0\n\tv_permlane32_swap_b32 %0, %1\n\ts_nop 1\n\tv_add_f32_dpp %0, %1, %0 row_ror:8 row_mask:0xf bank_mask:0xf" : "+v"(f0[4]), "+v"(f1[4]));
            WWR(4);
            }
            WT(6);
            f32x2 a, b, c, e; u32 mq; v4u sv;
            asm volatile("s_waitcnt lgkmcnt(0)\n\ts_barrier\n\tds_read_b128 %4, %6 offset:%7+2560\n\t"
                         "ds_read_b64 %0, %5 offset:%7\n\tds_read_b64 %1, %5 offset:%7+640\n\tds_read_b64 %2, %5 offset:%7+1280\n\tds_read_b64 %3, %5 offset:%7+1920"
                         : "=&v"(a), "=&v"(b), "=&v"(c), "=&v"(e), "=&v"(sv) : "v"(xr), "v"(zero), "i"(L * 4096) : "memory");
            asm volatile("s_waitcnt lgkmcnt(4)\n\tv_or_b32 %4, %6, %7\n\tv_or3_b32 %4, %4, %8, %9\n\tv_lshl_or_b32 %5, %4, 31, %10\n\t"
                         "s_waitcnt lgkmcnt(2)\n\tv_pk_add_f32 %0, %0, %1\n\ts_waitcnt lgkmcnt(0)\n\tv_pk_add_f32 %2, %2, %3\n\ts_nop 0\n\tv_pk_add_f32 %0, %0, %2"
                         : "+v"(a), "+v"(b), "+v"(c), "+v"(e), "=&v"(pst), "=&v"(mq) : "v"(sv.x), "v"(sv.y), "v"(sv.z), "v"(sv.w), "v"(mp) : "memory");
            st4s(rs, mq, 0, pkv(a));
            SCHED();
            asm volatile("s_memrealtime %0" : "=s"(clk));
            VMW(3); SCHED();
            static_for<7, 13>(next);
            WT(7);
        }
    };
    do {
        turn(IC<0>{});
        turn(IC<1>{});
    } while (uu(fin) == 0u);
    if (sn[0] + 1 < NSEG || sn[1] + 1 < NSEG) st4s(rs, tid == 0 ? foff(F_ERR) : OOB, 0, 1u);
    if (w == 0) LST(1900 + j, rt32());
}

__global__ void __launch_bounds__(256, 1) k_main(Params p) {
    lptr lds = (lptr)(u32)16 - 16;
    if (blockIdx.x == 0) LST(256, rt32());
    unsigned long long pc; int len;
    asm volatile("s_getpc_b64 %0\n.Lk_main_here:\n\ts_mov_b32 %1, .Lk_end_here-.Lk_main_here" : "=s"(pc), "=s"(len));
    const rsrc_t rs = mk_rsrc(p.ws, W_END);
    const v2u e0 = ld8s(rs, threadIdx.x == 0 ? foff(F_EPOCH) : OOB, 0);
    const v4u code = __builtin_amdgcn_raw_buffer_load_b128(mk_rsrc((const void*)(pc & ~15ull), (u32)max(len, 0)), (((blockIdx.x >> 3) & 31u) * 256u + threadIdx.x) * 16u, 0, 0);
    CFENCE();
    auto conv_id = [](int role) {
        const int x = role >> 5, i = role & 31, no = x < 5 ? 5 : 10, og = x < 5 ? x - 3 : 2 * (x - 4) + (i >= 5), r = (x - 3) * 5 + i - no, q = r / 3;
        return x < 3 || i >= no + 5 || r >= 24 ? NCV : i < no ? ((og & 1) ? (og >> 1) * 5 + i % 5 : NCV) : r % 3 == 0 ? 20 + (q & 1) * 20 + (q >> 1) : 16 + (r % 3) * 8 + q;
    };
    int role = (int)(((u32)__builtin_amdgcn_s_getreg(20 | (3 << 11)) & 7u) * 32u + (blockIdx.x >> 3));
    Early e;
    v4u rv;
#pragma unroll 1
    for (int n = 0;; ++n) {
        conv_early(p, rs, conv_id(role), e);
        if (n) break;
        CFENCE();
        if (threadIdx.x == 0) {
            GAS unsigned long long* slots = (GAS unsigned long long*)(p.ws + W_CTRL + F_SLOT * 128);
            const unsigned long long tag = (unsigned long long)e0.y << 32 | (e0.x + 1u);
            u32 mine = (u32)role;
#pragma unroll 1
            for (int k = 0; k < 256 && __hip_atomic_exchange(slots + mine * 16, tag, __ATOMIC_RELAXED, __HIP_MEMORY_SCOPE_AGENT) == tag; ++k) mine = (mine + 1u) & 255u;
            lds_w16(lds, (v4u){mine, e0.x, e0.y, 0u});
        }
        BAR();
        rv = lds_r16(lds);
        BAR();
        if (uni((int)rv.x) == role) break;
        role = uni((int)rv.x);
    }
    const int x = role >> 5, i = role & 31, w = uni(threadIdx.x >> 6), no = x < 5 ? 5 : 10, nr = x < 7 ? 5 : 4, j = conv_id(role);
    const v2u ep = {uu(rv.y), uu(rv.z)};
    asm volatile("" :: "v"(code));
    if (x < 2) { role_head(p, lds, i >> 4, x * 16 + (i & 15), ep); __builtin_amdgcn_endpgm(); }
    if (x == 2) { role_wo(p, lds, i, ep); __builtin_amdgcn_endpgm(); }
    if (i >= no + nr) { role_qkv(p, lds, (x < 5 ? (x - 3) * 22 : 44 + (x - 5) * 17) + i - no - nr, ep); __builtin_amdgcn_endpgm(); }
    const bool cv = j >= NPRE || (j >= 20 && j < 24);
    if (j < NCV) conv_stage(p, rs, lds, j, (j & 3) * 2 + (j >= NPRE), cv ? 40 : 0, ep, e);
    if (i >= no) {
        if (!cv) role_row(p, lds, (j - 24) * 4 + w, ep);
        __builtin_amdgcn_endpgm();
    }
    const int og = x < 5 ? x - 3 : 2 * (x - 4) + (i >= 5);
    role_out(p, lds, og & 1, og >> 1, i % 5, ep);
}
__global__ void k_end() { asm volatile(".Lk_end_here:"); }

extern "C" void kernel_launch(void* const* d_in, const int* in_sizes, int n_in, void* d_out, int out_size, void* d_ws, size_t ws_size,
                              hipStream_t stream) {
    Params p;
    p.x = (const u16*)d_in[0]; p.mem0 = (const u16*)d_in[1]; p.Wq = (const u16*)d_in[2]; p.Wk = (const u16*)d_in[3];
    p.Wv = (const u16*)d_in[4]; p.Wo = (const u16*)d_in[5];
    p.bk = (const u16*)d_in[8]; p.bv = (const u16*)d_in[9]; p.shift = (const u16*)d_in[10]; p.ssmk = (const u16*)d_in[11]; p.ssmD = (const u16*)d_in[12];
    p.out = (u16*)d_out;
    p.ws = (unsigned char*)(((size_t)d_ws + 2 * MB - 1) & ~(size_t)(2 * MB - 1));
    (void)hipFuncSetAttribute((const void*)k_main, hipFuncAttributeMaxDynamicSharedMemorySize, LDS_BYTES);
    hipLaunchKernelGGL(k_main, dim3(256), dim3(256), LDS_BYTES, stream, p);
}
```

```cpp
#include <hip/hip_runtime.h>
#include <hip/hip_bf16.h>

typedef unsigned short u16;
typedef unsigned int u32;
typedef u32 v2u __attribute__((ext_vector_type(2)));
typedef u32 v4u __attribute__((ext_vector_type(4)));
typedef u32 v8u __attribute__((ext_vector_type(8)));
typedef float f32x2 __attribute__((ext_vector_type(2)));
typedef float f32x4 __attribute__((ext_vector_type(4)));
typedef float f32x16 __attribute__((ext_vector_type(16)));
typedef __bf16 bf16x2 __attribute__((ext_vector_type(2)));
typedef __bf16 bf16x8 __attribute__((ext_vector_type(8)));
typedef short s16x4 __attribute__((ext_vector_type(4)));
#define LAS __attribute__((address_space(3)))
#define GAS __attribute__((address_space(1)))
typedef LAS unsigned char* lptr;
#define DEV __device__ __forceinline__

constexpr int SEQ = 2048, DM = 640, NH = 32, DH = 128, NSEG = 64, NQ = NH * DH;

constexpr u32 MB = 1u << 20;
constexpr u32 W_ORING = 0;
constexpr u32 W_MRING = MB;
constexpr u32 W_PROBE = 2 * MB - 65536;
constexpr u32 W_CTRL = 4 * MB;
constexpr u32 CTRL_BYTES = 128 * 1024;
constexpr u32 ACT = SEQ * DM * 2;
constexpr u32 W_H1 = 5 * MB, W_Y = W_H1 + ACT, W_H2 = W_Y + ACT, W_X2 = W_H2 + ACT;
constexpr u32 W_KQ = 16 * MB;
constexpr u32 W_VO = W_KQ + 64 * MB;
constexpr u32 W_P = W_VO + 64 * MB;
constexpr u32 P_ITEM = 16 * 4 * DM * 4;
constexpr u32 W_KVT = W_P + 4 * NSEG * P_ITEM;
constexpr u32 W_SP = W_KVT + ACT;
constexpr u32 W_END = W_KVT + 3 * MB;
static_assert((size_t)W_END + 2 * MB <= 256ull * MB, "workspace");
constexpr u32 F_ERR = 0, F_QKV = 16, F_O = F_QKV + 192, F_P = F_O + 64, F_H2 = F_P + 64, F_CLR = F_H2 + 32,
              F_EPOCH = F_CLR + 32, F_SLOT = F_EPOCH + 1, F_PRE = F_SLOT + 256, F_N = F_PRE + 192;
constexpr int NCV = 44, NPRE = 40;
static_assert(F_N * 128 <= CTRL_BYTES, "ctrl");
DEV u32 foff(u32 idx) { return W_CTRL + idx * 128u; }

constexpr u32 OOB = 0x80000000u;
constexpr u32 PSENT = 0x7F817F81u;
constexpr int SPIN_CAP = 40000;
constexpr int LDS_BYTES = 160 * 1024;

#define LDUMP(cond, id) do {} while (0)
#define LAD(cond, k, s_) do {} while (0)
#define LMX(cond, k, s_, v) do {} while (0)
#define LS3(cond, slot, idx) do {} while (0)
#define LST(idx, v) do {} while (0)
#define LS2(cond, idx) do {} while (0)
struct Params {
    const u16 *x, *mem0, *Wq, *Wk, *Wv, *Wo, *bk, *bv, *shift, *ssmk, *ssmD;
    u16* out;
    unsigned char* ws;
};

typedef __amdgpu_buffer_rsrc_t rsrc_t;
DEV rsrc_t mk_rsrc(const void* p, u32 bytes) { return __builtin_amdgcn_make_buffer_rsrc((void*)p, 0, bytes, 0x00020000); }
DEV v4u ld16s(rsrc_t r, u32 off, u32 so) { return __builtin_amdgcn_raw_buffer_load_b128(r, off, so, 16); }
DEV v2u ld8s(rsrc_t r, u32 off, u32 so) { return __builtin_amdgcn_raw_buffer_load_b64(r, off, so, 16); }
DEV v4u ldin16(rsrc_t r, u32 off, u32 so) { return __builtin_amdgcn_raw_buffer_load_b128(r, off, so, 2); }
DEV v2u ldin8(rsrc_t r, u32 off, u32 so) { return __builtin_amdgcn_raw_buffer_load_b64(r, off, so, 2); }
DEV u32 ld4(rsrc_t r, u32 off) { return __builtin_amdgcn_raw_buffer_load_b32(r, off, 0, 16); }
DEV void st16(rsrc_t r, u32 off, v4u v) { __builtin_amdgcn_raw_buffer_store_b128(v, r, off, 0, 16); }
DEV void st8s(rsrc_t r, u32 off, u32 so, v2u v) { __builtin_amdgcn_raw_buffer_store_b64(v, r, off, so, 16); }
DEV void st4s(rsrc_t r, u32 off, u32 so, u32 v) { __builtin_amdgcn_raw_buffer_store_b32(v, r, off, so, 16); }
#define DRAIN() asm volatile("s_waitcnt vmcnt(0)" ::: "memory")
#define VMW(n) asm volatile("s_waitcnt vmcnt(%0)" :: "i"(n) : "memory")
#define BAR() asm volatile("s_waitcnt lgkmcnt(0)\n\ts_barrier" ::: "memory")
#define SCHED() __builtin_amdgcn_sched_barrier(0)
#define CFENCE() asm volatile("" ::: "memory")
#define NOPS(n) asm volatile("s_nop %0" :: "i"(n))
#define PIN(x) asm volatile("" : "+v"(x))
#define PINS(x) asm volatile("" : "+s"(x))

template <int V> struct IC { static constexpr int value = V; };
template <int I, int N, class F> DEV void static_for(F&& f) { if constexpr (I < N) { f(IC<I>{}); static_for<I + 1, N>(f); } }
#define SFOR(var, n) static_for<0, n>([&](auto ic_) __attribute__((always_inline)) { constexpr int var = decltype(ic_)::value;
#define SEND });

DEV u32 pk(float a, float b) { bf16x2 v = {(__bf16)a, (__bf16)b}; return __builtin_bit_cast(u32, v); }
DEV float lo_f(u32 u) { return __builtin_bit_cast(float, u << 16); }
DEV float hi_f(u32 u) { return __builtin_bit_cast(float, u & 0xffff0000u); }
DEV f32x2 up2(u32 u) { return (f32x2){lo_f(u), hi_f(u)}; }
DEV u32 pk2(f32x2 v) { return pk(v.x, v.y); }
DEV float rbf(float a) { return (float)(__bf16)a; }
DEV v4u lds_r16(lptr p) { return *(LAS v4u*)p; }
DEV void lds_w16(lptr p, v4u v) { *(LAS v4u*)p = v; }
DEV v2u lds_tr(lptr p) { return __builtin_bit_cast(v2u, __builtin_amdgcn_ds_read_tr16_b64_v4i16((LAS s16x4*)p)); }
DEV v4u tr2(lptr p0, lptr p1) { const v2u a = lds_tr(p0), b = lds_tr(p1); return (v4u){a.x, a.y, b.x, b.y}; }
DEV v8u cat8(v4u a, v4u b) { return __builtin_shufflevector(a, b, 0, 1, 2, 3, 4, 5, 6, 7); }
DEV f32x4 mfma16(v4u a, v4u b, f32x4 c) { return __builtin_amdgcn_mfma_f32_16x16x32_bf16(__builtin_bit_cast(bf16x8, a), __builtin_bit_cast(bf16x8, b), c, 0, 0, 0); }
DEV f32x16 mfma32(v4u a, v4u b, f32x16 c) { return __builtin_amdgcn_mfma_f32_32x32x16_bf16(__builtin_bit_cast(bf16x8, a), __builtin_bit_cast(bf16x8, b), c, 0, 0, 0); }

DEV float f_(u32 u) { return __builtin_bit_cast(float, u); }
DEV u32 u_(float f) { return __builtin_bit_cast(u32, f); }
template <int CTRL> DEV float dpp(float v) { return f_((u32)__builtin_amdgcn_update_dpp(0, (int)u_(v), CTRL, 0xf, 0xf, false)); }
DEV float max16(float v) { auto r = __builtin_amdgcn_permlane16_swap(u_(v), u_(v), false, false); return fmaxf(f_(r[0]), f_(r[1])); }
DEV float max32(float v) { auto r = __builtin_amdgcn_permlane32_swap(u_(v), u_(v), false, false); return fmaxf(f_(r[0]), f_(r[1])); }
DEV float add16(float v) { auto r = __builtin_amdgcn_permlane16_swap(u_(v), u_(v), false, false); return f_(r[0]) + f_(r[1]); }
DEV float add32(float v) { auto r = __builtin_amdgcn_permlane32_swap(u_(v), u_(v), false, false); return f_(r[0]) + f_(r[1]); }
DEV float wave_sum(float v) {
    v += dpp<0xB1>(v);
    v += dpp<0x4E>(v);
    v += dpp<0x124>(v);
    v += dpp<0x128>(v);
    return add32(add16(v));
}
DEV bool all_(bool c) { return __builtin_amdgcn_ballot_w64(!c) == 0ull; }
DEV int uni(int v) { return __builtin_amdgcn_readfirstlane(v); }
DEV u32 uu(u32 v) { return (u32)__builtin_amdgcn_readfirstlane((int)v); }
DEV u32 rdl(u32 v, int l) { return (u32)__builtin_amdgcn_readlane((int)v, l); }
DEV u32 rt32() { return (u32)__builtin_amdgcn_s_memrealtime(); }

#define WSL 2
DEV void wait_flags(rsrc_t rs, u32 off, u32 want, bool& dead) {
    if (dead) return;
#pragma unroll 1
    for (int n = 0;; ++n) {
        CFENCE();
        const u32 v = ld4(rs, off);
        if (all_(off >= OOB || v >= want)) break;
        __builtin_amdgcn_s_sleep(WSL);
        if ((n & 63) == 63) { if (uni((int)ld4(rs, foff(F_ERR))) != 0) { dead = true; break; } }
        if (n > SPIN_CAP) { st4s(rs, foff(F_ERR), 0, 1u); dead = true; break; }
    }
    CFENCE();
}

DEV void pre_post(rsrc_t rs, int idx, u32 stage, v2u ep) { st8s(rs, threadIdx.x == 0 ? foff(F_PRE + idx) : OOB, 0, (v2u){ep.x + stage, ep.y}); }
DEV void pre_poll(rsrc_t rs, int first, int n, u32 stage, v2u ep) {
    const int lane = threadIdx.x & 63;
#pragma unroll 1
    for (int it = 0; it < SPIN_CAP; ++it) {
        CFENCE();
        const v2u a = ld8s(rs, foff(F_PRE + first + min(lane, n - 1)), 0), b = ld8s(rs, foff(F_PRE + first + min(lane + 64, n - 1)), 0);
        if (all_(a.y == ep.y && b.y == ep.y && max(a.x - ep.x - stage, b.x - ep.x - stage) < 8u - stage)) break;
        __builtin_amdgcn_s_sleep(2);
    }
    CFENCE();
}
DEV void pre_wait(rsrc_t rs, int first, int n, u32 stage, v2u ep) {
    if (uni(threadIdx.x >> 6) == 0) pre_poll(rs, first, n, stage, ep);
    BAR();
}
DEV u32 pol_wait(rsrc_t rs, v2u ep) {
    const int lane = threadIdx.x & 63;
    v4u a;
#pragma unroll 1
    for (int it = 0; it < SPIN_CAP; ++it) {
        CFENCE();
        a = ld16s(rs, foff(F_PRE + (lane < NPRE || lane >= 48 ? lane : 0)), 0);
        if (all_(a.y == ep.y && a.x - ep.x - 1u < 7u)) break;
        __builtin_amdgcn_s_sleep(2);
    }
    CFENCE();
    return uu(wave_sum(lane >= 48 ? (float)(int)a.z : 0.f) >= 0.f ? 1u : 0u);
}
#define PROBE_T 1000
#define NPROBE 32
DEV u32 pol_probe(const Params& p, rsrc_t rs, int id) {
    const int lane = threadIdx.x & 63;
    const u32 off0 = W_PROBE + (u32)(id >> 2) * 16384u + (u32)(id & 3) * 128u;
    const u32 tend = rt32() + PROBE_T;
    st4s(rs, lane < 32 ? off0 + (u32)lane * 512 : OOB, 0, 0u); DRAIN();
    int calm = 0;
    int d[2] = {1 << 16, 1 << 16};
#pragma unroll 1
    for (int q = 0; q < NPROBE && ((q & 1) || (calm < 6 && (int)(rt32() - tend) < 0)); ++q) {
        const u32 half = (u32)(q ^ (q >> 1) ^ id) & 1u;
        const unsigned long long t0 = __builtin_amdgcn_s_memtime();
        const u32 v = ld4(rs, lane == 0 ? off0 + half * 8192 + (u32)(q >> 1) * 512 : OOB);
        asm volatile("s_waitcnt vmcnt(0)" :: "v"(v) : "memory");
        const int dt = (int)(__builtin_amdgcn_s_memtime() - t0);
        if (half) d[1] = min(d[1], dt); else d[0] = min(d[0], dt);
        calm += dt < 1100;
    }
    const u32 xcc = (u32)__builtin_amdgcn_s_getreg(20 | (3 << 11)) & 0xfu;
    return (u32)min(max((d[0] - d[1]) * ((xcc & 4u) ? -1 : 1), -250), 250);
}
DEV f32x2 ln_stats(const f32x2 (&x)[5]) {
    const float c = f_(uu(u_(x[0].x)));
    f32x2 d[5];
#pragma unroll
    for (int i = 0; i < 5; ++i) d[i] = x[i] - c;
    const f32x2 s2 = (d[0] + d[1]) + (d[2] + d[3]) + d[4], q2 = (d[0] * d[0] + d[1] * d[1]) + (d[2] * d[2] + d[3] * d[3] + d[4] * d[4]);
    float a = s2.x + s2.y, b = q2.x + q2.y, a2, b2;
#define LN_DPP(m) "v_add_f32_dpp %0, %0, %0 " m " row_mask:0xf bank_mask:0xf\n\tv_add_f32_dpp %1, %1, %1 " m " row_mask:0xf bank_mask:0xf\n\ts_nop 0\n\t"
    asm("s_nop 1\n\t" LN_DPP("quad_perm:[1,0,3,2]") LN_DPP("quad_perm:[2,3,0,1]") LN_DPP("row_ror:4")
        "v_add_f32_dpp %0, %0, %0 row_ror:8 row_mask:0xf bank_mask:0xf\n\tv_add_f32_dpp %1, %1, %1 row_ror:8 row_mask:0xf bank_mask:0xf\n\ts_nop 1\n\t"
        "v_permlane16_swap_b32 %0, %1\n\tv_add_f32 %0, %0, %1\n\tv_mov_b32 %1, %0\n\ts_nop 1\n\tv_permlane32_swap_b32 %0, %1\n\tv_add_f32 %0, %0, %1\n\ts_nop 0\n\t"
        "v_readlane_b32 %2, %0, 0\n\tv_readlane_b32 %3, %0, 16\n\ts_nop 1"
        : "+v"(a), "+v"(b), "=s"(a2), "=s"(b2));
    const float m = a2 * (1.0f / DM), var = fmaxf(b2 * (1.0f / DM) - m * m, 0.f);
    const float sd = rbf(__builtin_amdgcn_sqrtf(rbf(rbf(var) + 1e-5f)));
    return (f32x2){rbf(c + m), __builtin_amdgcn_rcpf(sd)};
}
DEV u32 pkv(f32x2 v) { u32 d; asm("v_cvt_pk_bf16_f32 %0, %1, %2" : "=v"(d) : "v"(v.x), "v"(v.y)); return d; }
DEV void store_h(rsrc_t rs, u32 base, int row, const u32 (&hp)[5]) {
    const u32 lane = threadIdx.x & 63;
    const u32 sw = (u32)(row & 15) * 16u, o8 = (lane >> 5) * 8192u + (((lane & 31u) * 8u) ^ sw), hb = base + (u32)(row >> 5) * (32 * DM * 2) + (u32)(row & 31) * 256u;
    st8s(rs, o8, hb, (v2u){hp[0], hp[1]});
    st8s(rs, o8, hb + 16384, (v2u){hp[2], hp[3]});
    st4s(rs, (lane * 4) ^ sw, hb + 32768, hp[4]);
}

#define DMA16(ldsb, vo, so) asm volatile("s_mov_b32 m0, %0\n\ts_nop 0\n\tbuffer_load_dwordx4 %1, %2, %3 offen sc1 lds" :: "s"(ldsb), "v"(vo), "s"(srd), "s"(so) : "memory")
#define DMA16I(ldsb, lo, vo, so, go) do { u32 so_; asm volatile("s_add_u32 m0, %1, %5\n\ts_add_u32 %0, %4, %6\n\tbuffer_load_dwordx4 %2, %3, %0 offen sc1 lds" \
                         : "=&s"(so_) : "s"(ldsb), "v"(vo), "s"(srd), "s"(so), "i"(lo), "i"(go) : "memory", "scc"); } while (0)
#define DMA32(ldsb, vo, so) asm volatile("s_mov_b32 m0, %0\n\ts_nop 0\n\tbuffer_load_dwordx4 %1, %2, %3 offen sc1 lds\n\tbuffer_load_dwordx4 %1, %2, %3 offen offset:1024 sc1 lds" \
                         :: "s"(ldsb), "v"(vo), "s"(srd), "s"(so) : "memory")
#define DMAIN(sr, ldsb, vo, so) asm volatile("s_mov_b32 m0, %0\n\ts_nop 0\n\tbuffer_load_dwordx4 %1, %2, %3 offen lds" :: "s"(ldsb), "v"(vo), "s"(sr), "s"(so) : "memory")
DEV v4u mk_srd(const void* q, u32 bytes) { return (v4u){uu((u32)(size_t)q), uu((u32)((size_t)q >> 32) & 0xffffu), bytes, 0x00020000u}; }
#define DMANT(sr, ldsb, vo, so) asm volatile("s_mov_b32 m0, %0\n\ts_nop 0\n\tbuffer_load_dwordx4 %1, %2, %3 offen nt lds" :: "s"(ldsb), "v"(vo), "s"(sr), "s"(so) : "memory")
#define AHEAD(sr, vo, so) asm volatile("buffer_load_dwordx4 %0, %1, %2, %3 offen" : "+v"(junk) : "v"(vo), "s"(sr), "s"(so) : "memory")
#define MK_SRD() const v4u srd = mk_srd(p.ws, W_END)

constexpr int CV_W = 40960;
template <bool B> struct CvL {
    static constexpr int NF = B ? 5 : 3, NK = B ? 4 : 2, KP = 544 + NK * 256, CH = NF * 1024 + 4 * KP;
};
constexpr int CV_Y = 4 * CvL<false>::CH, CV_YP = 272;
static_assert(CV_Y + 32 * CV_YP <= CV_W && 2 * CvL<true>::CH <= CV_Y, "conv lds");
DEV int cv_chan(int u, int i) { return 16 * (u % 40) + 4 * i + u / 40; }
template <bool B> DEV void cv_filt(const Params& p, u32 dst, int d, int i) {
    const u32 lane = threadIdx.x & 63;
    const v4u sf = mk_srd(p.ssmk + d * SEQ, SEQ * 2);
    if (i == 0) DMANT(sf, dst, lane < 4u ? OOB : lane * 16u - 64u, 0);
    else DMANT(sf, uu(dst + i * 1024), i == CvL<B>::NF - 1 && lane >= 4u ? OOB : lane * 16u, i * 1024 - 64);
}
DEV v4u cv_kvld(rsrc_t rs, int d, int i) { return ld16s(rs, (threadIdx.x & 63) * 16u, uu(W_KVT + (u32)d * (SEQ * 2) + i * 1024)); }
template <bool B> DEV void cv_kvput(lptr F, const v4u* k) {
    const int lane = threadIdx.x & 63;
#pragma unroll
    for (int i = 0; i < CvL<B>::NK; ++i) lds_w16(F + CvL<B>::NF * 1024 + (lane & 3) * CvL<B>::KP + 512 + i * 256 + (lane >> 2) * 16, k[i]);
}
template <bool B> DEV void cv_zero(lptr F) {
    const int lane = threadIdx.x & 63;
#pragma unroll
    for (int i = 0; i < 4; ++i) lds_w16(F + (lane >> 5) * CvL<B>::CH + CvL<B>::NF * 1024 + i * CvL<B>::KP + (lane & 31) * 16, (v4u){0u, 0u, 0u, 0u});
}
#define M32V(d, a, b) asm volatile("v_mfma_f32_32x32x16_bf16 %0, %1, %2, %0" : "+v"(d) : "v"(a), "v"(b))
template <bool B, class G> DEV void cv_run(const Params& p, rsrc_t rs, lptr F, int d, u32 D, f32x4 sp, float (&y)[16], G&& gap) {
    constexpr int KP = CvL<B>::KP, FO = B ? 2048 : 0;
    const int lane = threadIdx.x & 63, n = lane & 31, hh = lane >> 5, c = lane & 3;
    lptr fa = F + 64 + (32 * hh + 16 * (c & 1) + 8 * ((lane >> 4) & 1) + 4 * (c >> 1) - (lane & 12)) * 2;
    lptr kl = F + CvL<B>::NF * 1024 + (32 + n - hh) * 16;
    v4u dg;
#pragma unroll
    for (int q = 0; q < 4; ++q) { const int k = (lane & 35) ? -1 : (n & 4) + (n >> 3); dg[q] = k == 2 * q ? D : k == 2 * q + 1 ? D << 16 : 0u; }
    v4u l[4], h[4]; v2u fc[4], fh[4];
    f32x16 acc[4];
#pragma unroll
    for (int j = 0; j < 4; ++j) { l[j] = lds_r16(kl + j * KP); if constexpr (B) h[j] = lds_r16(kl + j * KP + 512); }
#pragma unroll
    for (int j = 0; j < 2; ++j) { fc[j] = lds_tr(fa + FO - 32 * j); if constexpr (B) fh[j] = lds_tr(fa - 32 * j); }
    asm volatile("s_nop 1" : "+v"(dg));
#pragma unroll
    for (int j = 0; j < 4; ++j) asm volatile("v_mfma_f32_32x32x16_bf16 %0, %1, %2, 0" : "=&v"(acc[j]) : "v"(dg), "v"(B ? h[j] : l[j]));
    constexpr int U = B ? 2 : 4;
    kl -= 32 * U; fa -= 32;
    auto body = [&](bool first) __attribute__((always_inline)) {
#pragma unroll
        for (int z = 0; z < U; ++z) {
            const int c = 2 * (z & 1);
            const v4u f = __builtin_shufflevector(fc[c], fc[c + 1], 0, 1, 2, 3), g = __builtin_shufflevector(fh[c], fh[c + 1], 0, 1, 2, 3);
#pragma unroll
            for (int j = 0; j < 4; ++j) {
                M32V(acc[j], f, l[j]); l[j] = lds_r16(kl + j * KP + 32 * (U - z - 1));
                if (j < 2) fc[(c ^ 2) + j] = lds_tr(fa + FO + (z + 1) * 128 + 32 - 32 * j);
                if (first && j > 0) gap(5 * z + j - 1);
                SCHED();
            }
            if constexpr (B) {
#pragma unroll
                for (int j = 0; j < 4; ++j) {
                    M32V(acc[j], g, h[j]); h[j] = lds_r16(kl + j * KP + 512 + 32 * (U - z - 1));
                    if (j < 2) fh[(c ^ 2) + j] = lds_tr(fa + (z + 1) * 128 + 32 - 32 * j);
                    if (first && j > 1) gap(5 * z + j + 1);
                    SCHED();
                }
            }
        }
        kl -= 32 * U; fa += 128 * U;
    };
    if constexpr (B) body(true);
#pragma unroll 1
    for (int m = B ? 2 * U : 0; m < 32; m += 2 * U) body(false);
    asm volatile("s_nop 11" : "+v"(acc[0]), "+v"(acc[1]), "+v"(acc[2]), "+v"(acc[3]));
    const float o[3] = {acc[1][15] + acc[2][14] + acc[3][13], acc[2][15] + acc[3][14], acc[3][15]};
    if constexpr (!B) st16(rs, lane == 63 ? W_SP + (u32)d * 16u : OOB, (v4u){u_(o[0]), u_(o[1]), u_(o[2]), 0u});
#pragma unroll
    for (int i = 0; i < 16; ++i) {
        float v = acc[0][i];
#pragma unroll
        for (int j = 1; j < 4; ++j) if (j <= i) v += acc[j][i - j];
        if (i < 3) { const float e = f_((u32)__builtin_amdgcn_ds_bpermute((hh ? lane - 32 : lane + 31) * 4, (int)u_(o[i]))); v += lane == 0 ? sp[i] : e; }
        y[i] = v;
    }
}
DEV void cv_put(lptr F, int pr, const float (&y0)[16], const float (&y1)[16]) {
    const int lane = threadIdx.x & 63;
#pragma unroll
    for (int i = 0; i < 16; ++i) *(LAS u32*)(F + CV_Y + (lane & 31) * CV_YP + (lane >> 5) * 128 + i * 8 + pr * 4) = pk(y0[i], y1[i]);
}
DEV void cv_out(rsrc_t rs, lptr F, int u, int blk0) {
    const int lane = threadIdx.x & 63;
    lptr q = F + CV_Y + ((lane >> 2) & 3) * CV_YP + ((lane >> 4) * 4 + (lane & 3)) * 8;
    const u32 yo = W_Y + (u32)(((lane >> 2) * 8 + blk0) * DM + 4 * u + (lane & 3)) * 16u;
#pragma unroll
    for (int i = 0; i < 8; ++i) { lptr a = q + (i >> 1) * (8 * CV_YP) + (i & 1) * 128; st16(rs, yo + (u32)((i & 1) * 128 + (i >> 1)) * (DM * 16), tr2(a, a + 4 * CV_YP)); }
}
struct KvIn { v2u k[19], v[16]; v4u s[2]; };
template <class F> DEV void kvt_load(const Params& p, int tile, KvIn& in, F&& mid) {
    const int lane = threadIdx.x & 63;
    const rsrc_t rk = mk_rsrc(p.bk, ACT), rv = mk_rsrc(p.bv, ACT), rh = mk_rsrc(p.shift, DM * 8);
    const int row = (tile / 10) * 64 + (lane >> 4) * 16, col = (tile % 10) * 64 + (lane & 15) * 4;
    const u32 vo = (u32)(row * DM + col) * 2u, vp = row > 0 ? vo - 3 * (DM * 2) : OOB;
#pragma unroll
    for (int i = 0; i < 2; ++i) in.s[i] = ldin16(rh, (u32)col * 8u + i * 16, 0);
#pragma unroll
    for (int i = 0; i < 19; ++i) { if (i == 7) mid(); in.k[i] = i < 3 ? ldin8(rk, vp, i * (DM * 2)) : ldin8(rk, vo, (i - 3) * (DM * 2)); if (i >= 3) in.v[i - 3] = ldin8(rv, vo, (i - 3) * (DM * 2)); }
}
DEV void kvt_make(rsrc_t rs, lptr xt, int tile, const KvIn& in) {
    const int lane = threadIdx.x & 63;
#pragma unroll
    for (int c = 0; c < 2; ++c) {
        const v4u sv = in.s[c];
        const f32x2 sh[4] = {{lo_f(sv.x), lo_f(sv.z)}, {hi_f(sv.x), hi_f(sv.z)}, {lo_f(sv.y), lo_f(sv.w)}, {hi_f(sv.y), hi_f(sv.w)}};
        f32x2 kf[19];
#pragma unroll
        for (int i = 0; i < 19; ++i) kf[i] = up2(in.k[i][c]);
        u32 kv[16];
#pragma unroll
        for (int i = 0; i < 16; ++i) kv[i] = pk2(((kf[i] * sh[3] + kf[i + 1] * sh[2]) + (kf[i + 2] * sh[1] + kf[i + 3] * sh[0])) * up2(in.v[i][c]));
#pragma unroll
        for (int q = 0; q < 8; ++q) {
            const u32 sel = (q & 1) ? 0x07060302u : 0x05040100u; const u32* z = kv + (q >> 1);
            *(LAS v2u*)(xt + ((2 * c + (q & 1)) * 16 + (lane & 15)) * 144 + (lane >> 5) * 64 + (q >> 1) * 16 + ((lane >> 4) & 1) * 8) =
                (v2u){__builtin_amdgcn_perm(z[4], z[0], sel), __builtin_amdgcn_perm(z[12], z[8], sel)};
        }
    }
    const u32 o = W_KVT + (u32)(((tile % 10) * 64 + 4 * (lane >> 3)) * SEQ + (tile / 10) * 64 + (lane & 7) * 8) * 2u;
#pragma unroll
    for (int i = 0; i < 8; ++i) st16(rs, o + (u32)(32 * (i & 1) + (i >> 1)) * (SEQ * 2), lds_r16(xt + (8 * i + (lane >> 3)) * 144 + (lane & 7) * 16));
}
struct RowX { v2u a, b; u32 c; };
DEV void ln_row(rsrc_t rs, int row, const RowX& r) {
    const f32x2 x[5] = {up2(r.a.x), up2(r.a.y), up2(r.b.x), up2(r.b.y), up2(r.c)};
    const f32x2 st = ln_stats(x);
    u32 hp[5];
#pragma unroll
    for (int q = 0; q < 5; ++q) hp[q] = pk2(up2(pkv(x[q] - st.x)) * st.y);
    store_h(rs, W_H1, row, hp);
}
DEV v4u mem0_load(const Params& p, int j) {
    const u32 gt = (u32)(j * 256 + threadIdx.x) * 16u, n = ((gt >> 14) << 9) + ((gt & 8191u) >> 4);
    return ldin16(mk_rsrc(p.mem0, 8 * DM * 2), n < 640u ? ((2 * ((n >> 2) & 3) + (n & 1)) * DM + (n >> 4) * 16 + (n & 2) * 4) * 2u : OOB, 0);
}
DEV void pre_rings(rsrc_t rs, int j, v4u m) {
    const u32 gi = (u32)(j * 256 + threadIdx.x), gt = gi * 16u, R = NPRE * 4096, n = ((gt >> 14) << 9) + ((gt & 8191u) >> 4);
#pragma unroll
    for (int i = 0; i < 4; ++i) m[i] = n >= 640u ? PSENT : m[i] == PSENT ? 0x7FC17FC1u : m[i];
    st4s(rs, gi - 36u * 256u < F_CLR ? foff(gi - 36u * 256u) : OOB, 0, 0u);
#pragma unroll 1
    for (u32 o = gt; o < MB; o += R) st16(rs, W_ORING + o, (v4u){PSENT, PSENT, PSENT, PSENT});
    const u32 mo = gt < 4 * 32768 + 16384 ? W_MRING + gt : OOB;
    st16(rs, mo, m);
    st16(rs, mo + MB / 4, m);
    static_assert(NPRE * 4096 >= 4 * 32768 + 16384, "memory ring");
}
constexpr int KT0 = NPRE * 4, LN0 = NPRE * 8;
struct Early { RowX r[2]; v4u m; u32 vote; };
DEV void conv_early(const Params& p, rsrc_t rs, int j, Early& e) {
    const int lane = threadIdx.x & 63, w = uni(threadIdx.x >> 6), u = j * 4 + w;
    if (j < NPRE) {
        const rsrc_t rx = mk_rsrc(p.x, ACT);
#pragma unroll
        for (int i = 0; i < 2; ++i) {
            const u32 so = uu((u + i * KT0) * (DM * 2));
            e.r[i].a = __builtin_amdgcn_raw_buffer_load_b64(rx, lane * 8u, so, 0); e.r[i].b = __builtin_amdgcn_raw_buffer_load_b64(rx, lane * 8u + 512u, so, 0);
            e.r[i].c = __builtin_amdgcn_raw_buffer_load_b32(rx, lane * 4u + 1024u, so, 0);
        }
        e.m = mem0_load(p, j);
    } else if (j < NCV && w > 0) e.vote = pol_probe(p, rs, u - NPRE * 4);
}
DEV void conv_stage(const Params& p, rsrc_t rs, lptr lds, int j, int g0, int ge, v2u ep, Early& e) {
    const int tid = threadIdx.x, lane = tid & 63, w = uni(tid >> 6), u = j * 4 + w;
    lptr F = lds + w * CV_W;
    const u32 fb = uu(w * CV_W);
    const rsrc_t rd = mk_rsrc(p.ssmD, DM * 2);
    float y0[16], y1[16];
    KvIn in;
    if (j < NPRE) {
        constexpr int CH = CvL<false>::CH;
        if (w == 1) LST(1500 + j, rt32());
        pre_rings(rs, j, e.m);
        ln_row(rs, u, e.r[0]); ln_row(rs, u + KT0, e.r[1]);
        if (w == 1) LST(1550 + j, rt32());
        CFENCE();
        kvt_load(p, u, in, [&] { VMW(13); BAR(); pre_post(rs, j, 1u, ep); if (w == 0) LST(1450 + j, rt32()); });
        if (j == 20) LST(260, rt32());
        const u32 D = __builtin_amdgcn_raw_buffer_load_b16(rd, (u32)cv_chan(u, lane & 3) * 2u, 0, 0);
#pragma unroll 1
        for (int i = 0; i < 4; ++i) { const int d = cv_chan(u, i); cv_filt<false>(p, fb + i * CH, d, 0); cv_filt<false>(p, fb + i * CH, d, 1); cv_filt<false>(p, fb + i * CH, d, 2); }
        kvt_make(rs, F + CV_Y, u, in);
        cv_zero<false>(F); cv_zero<false>(F + 2 * CH);
        DRAIN(); BAR();
        pre_post(rs, j, 2u, ep);
        if (j == 20) LST(262, rt32());
        pre_wait(rs, 0, NPRE, 2u, ep);
        if (j == 20) LST(281, rt32());
        {
            v4u k[8];
#pragma unroll
            for (int i = 0; i < 8; ++i) k[i] = cv_kvld(rs, cv_chan(u, i >> 1), i & 1);
#pragma unroll
            for (int i = 0; i < 4; ++i) cv_kvput<false>(F + i * CH, k + 2 * i);
        }
        if (j == 20) LST(286, rt32());
#pragma unroll 1
        for (int i = 0; i < 4; i += 2) {
            cv_run<false>(p, rs, F + i * CH, cv_chan(u, i), rdl(D, i), f32x4{}, y0, [](int) {});
            cv_run<false>(p, rs, F + (i + 1) * CH, cv_chan(u, i + 1), rdl(D, i + 1), f32x4{}, y1, [](int) {});
            cv_put(F, i >> 1, y0, y1);
        }
        if (j == 20) LST(287, rt32());
        cv_out(rs, F, u, 0);
        if (j == 20) LST(263, rt32());
    } else {
        st16(rs, lane == 0 ? foff(F_PRE + 48 + u - NPRE * 4) : OOB, (v4u){ep.x + 1u, ep.y, w > 0 ? e.vote : 0u, 0u});
#pragma unroll 1
        for (int t = u + 128; t < 320; t += 16) { kvt_load(p, t, in, [] {}); kvt_make(rs, F, t, in); }
    }
    DRAIN(); BAR();
    if (g0 < ge) {
        constexpr int CH = CvL<true>::CH;
        pre_post(rs, j, 3u, ep);
        cv_zero<true>(F);
        if (w == 0) { pre_poll(rs, 64, 32, 3u, ep); pre_poll(rs, 0, NCV, 3u, ep); }
        BAR();
        v4u k[4];
        auto fetch = [&](u32 dst, int d, int i) { if (i < 5) cv_filt<true>(p, dst, d, i); else if (i < 9) k[i - 5] = cv_kvld(rs, d, i - 5); };
#pragma unroll
        for (int i = 0; i < 9; ++i) fetch(fb, cv_chan(g0 * 4 + w, 0), i);
        const u32 ch = (u32)cv_chan(min((g0 + 8 * (lane >> 2)) * 4 + w, 159), lane & 3);
        u32 D = __builtin_amdgcn_raw_buffer_load_b16(rd, ch * 2u, 0, 0);
        v4u sp = ld16s(rs, W_SP + ch * 16u, 0);
        asm volatile("" : "+v"(D), "+v"(sp));
#pragma unroll 1
        for (int g = g0, n = 0; g < ge; g += 8) {
            const int un = g * 4 + w;
            if (g == 16) { LST(282, rt32()); LST(288, __builtin_amdgcn_s_memtime()); }
#pragma unroll 1
            for (int i = 0; i < 4; i += 2, n += 2) {
                const int d1 = (int)rdl(ch, n + 1), d2 = (int)rdl(ch, n + 2);
                cv_kvput<true>(F, k);
                cv_run<true>(p, rs, F, 0, rdl(D, n), (f32x4){f_(rdl(sp.x, n)), f_(rdl(sp.y, n)), f_(rdl(sp.z, n)), 0.f}, y0, [&](int q) { fetch(fb + CH, d1, q); });
                cv_kvput<true>(F + CH, k);
                cv_run<true>(p, rs, F + CH, 326, rdl(D, n + 1), (f32x4){f_(rdl(sp.x, n + 1)), f_(rdl(sp.y, n + 1)), f_(rdl(sp.z, n + 1)), 0.f}, y1, [&](int q) { fetch(fb, d2, q); });
                cv_put(F, i >> 1, y0, y1);
            }
            cv_out(rs, F, un, 4);
            if (g == 16) { LST(283, rt32()); LST(289, __builtin_amdgcn_s_memtime()); }
        }
        DRAIN(); BAR();
    }
    pre_post(rs, j, 5u, ep);
    if (j == 20) LST(284, rt32());
    if (j == 3) LST(285, rt32());
}

#define M32A(first, d, a, b) do { if (first) asm volatile("v_mfma_f32_32x32x16_bf16 %0, %1, %2, 0" : "=&v"(d) : "a"(a), "v"(b)); \
                                  else asm volatile("v_mfma_f32_32x32x16_bf16 %0, %1, %2, %0" : "+v"(d) : "a"(a), "v"(b)); } while (0)
#define M32B(first, d, a, b) do { if (first) asm volatile("v_mfma_f32_32x32x16_bf16 %0, %1, %2, 0" : "=&v"(d) : "v"(a), "a"(b)); \
                                  else asm volatile("v_mfma_f32_32x32x16_bf16 %0, %1, %2, %0" : "+v"(d) : "v"(a), "a"(b)); } while (0)
#define VCVT(d, a, b) asm volatile("v_cvt_pk_bf16_f32 %0, %1, %2" : "=v"(d) : "v"(a), "v"(b))
#define VADD(d, a, b) asm volatile("v_add_f32 %0, %1, %2" : "=v"(d) : "v"(a), "v"(b))
#define LD4A(d, vo, so) asm volatile("buffer_load_dword %0, %1, %2, %3 offen sc1" : "=v"(d) : "v"(vo), "s"(srd), "s"(so) : "memory")
DEV u32 wst_src(int lane, int ld) { return (u32)((lane >> 4) * ld + 8 * ((lane & 15) ^ ((lane >> 4) << 2))) * 2u; }
DEV u32 wst_rd(int lane, int w) { const int q4 = (lane >> 2) & 3; return (u32)(q4 * 256 + ((w ^ q4) * 64) + ((lane >> 4) & 1) * 32 + (lane & 3) * 8); }

constexpr int QK_SEL = 81920;
#define QK_LEAD 12
#define QK_SMP 29
#define QK_YB 26
#define QK_W0 2
DEV void role_qkv(const Params& p, lptr lds, int c, v2u ep) {
    const int tid = threadIdx.x, lane = tid & 63, w = uni(tid >> 6), hh = lane >> 5, r32 = lane & 31;
    const int m = c >> 5, h = c & 31;
    const rsrc_t rs = mk_rsrc(p.ws, W_END);
    v4u wt[40];
    {
        const v4u sw = mk_srd(m == 0 ? p.Wq : m == 1 ? p.Wk : p.Wv, DM * NQ * 2), sx = mk_srd(p.x, ACT);
        const u32 vo = wst_src(lane, NQ) + (u32)h * (DH * 2), x8 = (u32)lane * 8u, x4 = (u32)lane * 4u;
        RowX xr[5];
        auto wfill = [&](int i0, int n) {
#pragma unroll 1
            for (int i = i0; i < i0 + n; ++i) DMAIN(sw, uu((4 * i + w) * 1024), vo, uu((16 * i + 4 * w) * (NQ * 2)));
        };
        wfill(0, QK_W0);
#pragma unroll
        for (int i = 0; i < 5; ++i)
            asm volatile("buffer_load_dwordx2 %0, %3, %5, %6 offen\n\tbuffer_load_dwordx2 %1, %3, %5, %6 offen offset:512\n\tbuffer_load_dword %2, %4, %5, %6 offen offset:1024"
                         : "=&v"(xr[i].a), "=&v"(xr[i].b), "=&v"(xr[i].c) : "v"(x8), "v"(x4), "s"(sx), "s"(uu((LN0 + 4 * c + w + 384 * i) * (DM * 2))) : "memory");
        wfill(QK_W0, 10 - QK_W0);
        if (c == 3) LST(275, rt32());
        VMW(10 - QK_W0);
        if (c == 3) LST(276, rt32());
#pragma unroll
        for (int i = 0; i < 5; ++i) {
            asm volatile("" : "+v"(xr[i].a), "+v"(xr[i].b), "+v"(xr[i].c));
            const int row = LN0 + 4 * c + w + 384 * i;
            if (i < 4 || row < SEQ) ln_row(rs, row, xr[i]);
            wfill(10 + 6 * i, 6);
        }
        if (c == 3) LST(264, rt32());
        lptr tb = lds + hh * 2048 + wst_rd(lane, w);
        SFOR(k, 40)
            if constexpr (k == 0) { VMW(15); BAR(); }
            if constexpr (k == 28) {
                if (w == 0) pre_poll(rs, 0, NPRE, 1u, ep);
                DRAIN(); BAR();
                pre_post(rs, 96 + c, 1u, ep);
            }
            lptr q = tb + (k >> 3) * 32768 + (k & 7) * 4096; wt[k] = tr2(q, q + 1024); asm volatile("" : "+a"(wt[k]));
        SEND
    }
    if (c == 3) LST(268, rt32());
    if (w == 0) LST(1350 + c, rt32());
    bool dead = false;
    int n0, n1;
    const u32 t16 = (u32)(w * 2048 + lane * 16), wb = uu(w * 2048);
    u32 ar[16];
#pragma unroll
    for (int j = 0; j < 16; ++j) ar[j] = (u32)(r32 * 256 + ((2 * (j & 7) + hh) ^ (r32 & 15)) * 16 + (j >> 3) * 49152);
    MK_SRD();
#define XRD(y, ks) lds_r16(lds + ar[((ks) & 7) + ((ks) >= 24 ? 8 : 0)] + ((ks) >> 3) % 3 * 16384 + (y) * 8192)
#define QM0(y, b) asm volatile("s_add_u32 m0, %0, %1" :: "s"(wb), "i"((b) * 16384 + (y) * 8192) : "scc", "memory")
#define QDMA(i, vo) asm volatile("buffer_load_dwordx4 %0, %1, %2 offen offset:%3 sc1 lds" :: "v"(vo), "s"(srd), "s"(((i) >> 1) * 8192), "i"(((i) & 1) * 1024) : "memory")
    const u32 h2flag = (lane < 16 && w == 0) ? foff(F_H2 + lane) : OOB;
    const u32 ol = (m == 2 ? (u32)(2 * w + (r32 >> 4)) * 1024u : (u32)((r32 >> 4) * 4 + w) * 1024u) + (u32)(hh * 16 + (r32 & 15)) * 16u
                 + (m == 2 ? W_VO : m == 1 ? W_KQ : W_KQ + 8192u) + (u32)h * 16384u;
    const u32 fo = tid < 2 ? foff(F_QKV + tid * 96 + c) : OOB;
    lptr selw = lds + QK_SEL + (w == 0 ? 0 : 64), selr = lds + QK_SEL;
    PIN(selw);
    static_assert(QK_LEAD > 8 && QK_LEAD % 2 == 0 && LN0 == 10 * 32, "X = 8 comes up, before any item of layer 1");
    static_assert(W_H2 - W_H1 == 2 * 64 * (32 * DM * 2), "h");
    auto run = [&](auto isv) __attribute__((always_inline)) {
        constexpr bool V = decltype(isv)::value;
        constexpr int YB = QK_YB;
        f32x16 ax, ay = {};
        v4u x[8], pp, pq;
        u32 xv = t16 + W_H1, ox = ol, oy = OOB, nn = 1, lt = 0, fcnt = 0, fa = OOB;
        asm volatile("" : "+v"(lt), "+v"(nn));
#pragma unroll
        for (int z = 0; z < 5; ++z) x[z] = XRD(1, 40 - YB + z);
#define QPK(r_, a1, a_, q_) do { if constexpr (V) VCVT(r_[q_], a1[4 * (a_) + 8 * ((q_) >> 1) + 2 * ((q_) & 1)], a1[4 * (a_) + 8 * ((q_) >> 1) + 2 * ((q_) & 1) + 1]); \
                                 else VCVT(r_[q_], a1[4 * (a_) + (q_)], a1[4 * (a_) + (q_) + 8]); } while (0)
#define QPK4(r_, a1, a_) do { QPK(r_, a1, a_, 0); QPK(r_, a1, a_, 1); QPK(r_, a1, a_, 2); QPK(r_, a1, a_, 3); } while (0)
        for (;;) {
            u32 ls = uu(lt), yv, fv, nx, need;
            if (ls != 0u && uu(nn) < 256u) pre_wait(rs, 96, 96, 1u, ep);
            else if (ls != 0u) {
                n1 = (int)(uu(nn) >> 8);
                CFENCE();
                const u32 hf = h2flag + (u32)(~n1 & 1) * 2048u;
                u32 f2 = ld4(rs, hf);
                asm volatile("s_waitcnt vmcnt(0)" : "+v"(f2) :: "memory");
                *(LAS u32*)(selw + 16) = (u32)__builtin_amdgcn_ballot_w64(f2 < (u32)n1) & 0xFFFFu;
                BAR();
                st4s(rs, fa, 0, fcnt);
                fa = OOB;
                if (n1 > NSEG) { LDUMP(w == 0, 64 + c); __builtin_amdgcn_endpgm(); }
                if (uu(*(LAS u32*)(selr + 16)) != 0u) {
                    if (w == 0) wait_flags(rs, hf, (u32)n1, dead);
                    BAR();
                }
            }
#define QT(k) do {} while (0)
            QT(0);
            SCHED();
            QM0(0, 0); NOPS(0); QDMA(0, xv); QDMA(1, xv); QM0(0, 1);
            SFOR(z, 80)
                constexpr int ks = (z + 40 - YB) % 40, zx = z - YB;
                constexpr auto upto = [](int q) { const int d = (q - YB + 49) % 40; return q + 6 + (d == 0 || d == 2 ? 1 : d == 1 ? 2 : 0); };
                if constexpr (zx >= 0 && zx < 40) { if constexpr (V) M32B(ks == 0, ax, x[z & 7], wt[ks]); else M32A(ks == 0, ax, wt[ks], x[z & 7]); }
                else { if constexpr (V) M32B(ks == 0, ay, x[z & 7], wt[ks]); else M32A(ks == 0, ay, wt[ks], x[z & 7]); }
                if constexpr (z == 78) lt = *(LAS u32*)selr;
#pragma unroll
                for (int q = upto(z - 1); q < upto(z); ++q) x[q & 7] = XRD(q % 80 < YB || q % 80 >= YB + 40, (q + 40 - YB) % 40);
                SCHED();
                (void)&srd; (void)&wb;
                if constexpr (z < 2) QDMA(z + 2, xv);
                if constexpr (z >= 2 && z < 14 && (z & 3) == 2) QM0(0, (z + 6) >> 2);
                if constexpr (z >= 3 && z < 15 && (z & 1)) QDMA((z + 5) >> 1, xv);
                if constexpr (z == 4) { n0 = uni((int)nn); n1 = n0 >> 8; n0 &= 255; PINS(n0); PINS(n1); }
                if constexpr (z == 8) { nx = n0 < NSEG ? W_H1 + (u32)n0 * (32 * DM * 2) : OOB; PINS(nx); }
                if constexpr (z == 12) { yv = t16 + nx; PIN(yv); }
                if constexpr (zx == -6) { QT(1); asm volatile("s_waitcnt vmcnt(6) lgkmcnt(0)\n\ts_barrier" ::: "memory"); QT(2); }
                if constexpr (zx == -5) st4s(rs, fa, 0, fcnt);
                if constexpr (zx == -4 || zx == 0 || zx == 4) QM0(1, (zx + 4) >> 2);
                if constexpr (zx >= -3 && zx < 8 && (zx & 1)) QDMA((zx + 3) >> 1, yv);
                if constexpr (zx == 2) QPK4(pp, ay, 0);
                if constexpr (zx == 6) QPK4(pq, ay, 1);
                if constexpr (zx == 8) st16(rs, oy, pp);
                if constexpr (zx == 9) st16(rs, oy + 512, pq);
                if constexpr (zx == 10) { QT(3); asm volatile("s_waitcnt vmcnt(9)\n\ts_barrier" ::: "memory"); QT(4); }
                if constexpr (zx == 11 || zx == 15) QM0(1, (zx + 1) >> 2);
                if constexpr (zx >= 12 && zx < 20 && (zx & 1) == 0) QDMA(zx >> 1, yv);
                if constexpr (zx == 21) { fcnt = lane == 0 ? (u32)n0 : (u32)n1; fa = fo; asm volatile("" : "+v"(fcnt), "+v"(fa)); }
                if constexpr (zx == 22) { oy = (ol + (u32)n0 * (NH * 16384u)) | (yv & OOB); PIN(oy); }
                if constexpr (zx == 23) { n0 = min(n0 + 1, NSEG); PINS(n0); nx = (u32)min(NSEG, n1 + QK_LEAD); PINS(nx); }
                if constexpr (zx == 24)
                    asm volatile("s_cmp_ge_u32 %1, %0\n\ts_cselect_b32 %0, %4, %1\n\ts_addc_u32 %2, %2, 0\n\ts_cmp_lt_u32 %0, 64\n\ts_cselect_b32 %3, 0, %2"
                                 : "+s"(nx), "+s"(n0), "+s"(n1), "=&s"(need) : "s"(n1 + 64) : "scc");
                if constexpr (zx == 25) { n0 += 1 - (int)min(nx >> 6, 1u); PINS(n0); }
                if constexpr (zx == 26) { nn = (u32)(n0 | n1 << 8); PIN(nn); }
                if constexpr (zx == 27) { ls = W_H1 + (nx + (nx & 64u)) * (32 * DM * 2); PINS(ls); }
                if constexpr (zx == 28) { xv = t16 + ls; PIN(xv); need = nx == 8u ? 255u : need; PINS(need); }
                if constexpr (zx == 34) { asm volatile("s_waitcnt vmcnt(%0) lgkmcnt(0)\n\ts_barrier" :: "i"(QK_SMP < 34 ? 7 : 6) : "memory"); QT(5); }
                if constexpr (zx == QK_SMP) { const u32 po = (nx & 1u) * 2048u; LD4A(fv, h2flag, po); }
                if constexpr (zx == 42) QPK4(pp, ax, 0);
                if constexpr (zx == 43) st16(rs, ox, pp);
                if constexpr (zx == 44) QPK4(pq, ax, 1);
                if constexpr (zx == 45) st16(rs, ox + 512, pq);
                if constexpr (zx == 47) { ox = ol + nx * (NH * 16384u); PIN(ox); }
                if constexpr (z == 74) { asm volatile("s_waitcnt vmcnt(2)" : "+v"(fv) :: "memory"); QT(6); ls = (u32)__builtin_amdgcn_ballot_w64(fv < need) & 0xFFFFu; PINS(ls); }
                if constexpr (z == 75) { lt = ls; PIN(lt); *(LAS u32*)selw = lt; }
                if constexpr (z == 77) asm volatile("s_waitcnt lgkmcnt(2)\n\ts_barrier" ::: "memory");
                SCHED();
            SEND
        }
    };
    if (m == 2) run(IC<1>{}); else run(IC<0>{});
}

DEV void role_out(const Params& p, lptr lds, int L, int kq, int ng, v2u ep) {
    const int tid = threadIdx.x, lane = tid & 63, w = uni(tid >> 6), hh = lane >> 5, r32 = lane & 31;
    const rsrc_t rs = mk_rsrc(p.ws, W_END);
    v4u wo[64];
    bool dead = false;
    {
        const v4u sw = mk_srd(p.Wo, DM * NQ * 2);
        const u32 vo = wst_src(lane, DM) + (u32)ng * 256u;
        auto fill = [&](int b) {
#pragma unroll 1
            for (int i = 0; i < 8; ++i) DMAIN(sw, uu((b % 5) * 32768 + (4 * i + w) * 1024), vo, uu((kq * 1024 + b * 128 + 16 * i + 4 * w) * (DM * 2)));
        };
#pragma unroll 1
        for (int b = 0; b < 5; ++b) fill(b);
        lptr tb = lds + hh * 1024 + wst_rd(lane, w);
        SFOR(b, 8)
            VMW(b < 4 ? 32 : 56 - 8 * b); BAR();
            lptr q = tb + (b % 5) * 32768;
#pragma unroll
            for (int k = 0; k < 8; ++k) { lptr a = q + (k >> 1) * 8192 + (k & 1) * 2048; wo[8 * b + k] = tr2(a, a + 4096); if (b < 7) asm volatile("" : "+a"(wo[8 * b + k])); else PIN(wo[8 * b + k]); }
            if constexpr (b < 3) { BAR(); fill(b + 5); }
        SEND
    }
    pre_wait(rs, 0, NPRE, 1u, ep);
    if (kq == 0 && ng == 0) LST(272 + L, rt32());
    MK_SRD();
    const u32 ofl = lane < 8 ? foff(F_O + L * 32 + kq * 8 + lane) : OOB, l16 = (u32)lane * 16u;
    const u32 fo = (tid == 0) ? foff(F_P + L * 32 + kq * 5 + ng) : OOB;
#define OM0(dst, b) asm volatile("s_add_u32 m0, %0, %1" :: "s"(dst), "i"((b) * 8192) : "scc", "memory")
#define ODMA(i, vo) asm volatile("buffer_load_dwordx4 %0, %1, %2 offen offset:%3 sc1 lds" :: "v"(vo), "s"(srd), "s"(((i) >> 1) * 16384), "i"(((i) & 1) * 1024) : "memory")
    auto first = [&](u32 dst, u32 vo) {
#pragma unroll
        for (int b = 0; b < 5; ++b) { OM0(dst, b); NOPS(0); ODMA(2 * b, vo); ODMA(2 * b + 1, vo); }
    };
    u32 pa = W_P + (u32)(L * NSEG) * P_ITEM + (u32)(2 * hh * 4 + kq) * (DM * 4) + (u32)(ng * 128 + w * 32 + r32) * 4u, fv = 0;
    u32 ov = W_VO + 8192u + (u32)(L * NSEG * NH + kq * 8) * 16384u + (u32)w * 2048u + l16;
    bool early = false;
    for (int s = 0; s < NSEG; ++s) {
        const u32 dst = uu((s & 1) * 65536 + w * 2048), ndst = dst ^ 65536u, need = s + 1 < NSEG ? (u32)s + 2u : 255u;
        lptr A = lds + (s & 1) * 65536 + l16;
        if (!early) {
            if (!all_(ofl == OOB || fv >= (u32)s + 1)) wait_flags(rs, ofl, (u32)s + 1, dead);
            LS3(L == 0 && w == 1, 1, kq * 5 + ng);
            first(dst, ov);
        }
#define OT(k) do {} while (0)
        OM0(dst, 5);
        asm volatile("s_waitcnt vmcnt(8)\n\ts_barrier" ::: "memory");
        OT(0);
        v4u o[64];
#pragma unroll
        for (int k = 0; k < 4; ++k) o[k] = lds_r16(A + k * 1024);
        f32x16 acc;
        u32 far, nv;
        SCHED();
        SFOR(k, 64)
            if constexpr (k < 56) M32B(k == 0, acc, o[k], wo[k]); else M32V(acc, o[k], wo[k]);
            if constexpr (k < 60) o[k + 4] = lds_r16(A + (k + 4) * 1024);
            SCHED();
            (void)&srd; (void)&dst; (void)&ndst;
            if constexpr (k < 12 && (k & 1) == 0) ODMA(10 + k / 2, ov);
            if constexpr (k == 3) asm volatile("s_waitcnt vmcnt(6)\n\ts_barrier" ::: "memory");
            if constexpr (k == 3 || k == 7) OM0(dst, (k + 21) >> 2);
            if constexpr (k == 12) { ov += NH * 16384u; PIN(ov); }
            if constexpr (k == 19) asm volatile("s_waitcnt vmcnt(6)\n\ts_barrier" ::: "memory");
            if constexpr (k == 20) LD4A(fv, ofl, 0);
            if constexpr (k == 35) asm volatile("s_waitcnt vmcnt(1)\n\ts_barrier" ::: "memory");
            if constexpr (k == 40) { asm volatile("s_waitcnt vmcnt(0)" : "+v"(fv) :: "memory"); far = (u32)__builtin_amdgcn_ballot_w64(fv < need) & 255u; PINS(far); }
            if constexpr (k == 41) { nv = far ? OOB : ov; PIN(nv); }
            if constexpr (k >= 42 && k < 62 && (k & 3) == 2) OM0(ndst, (k - 42) >> 2);
            if constexpr (k >= 43 && k < 63 && (k & 1)) ODMA((k - 43) >> 1, nv);
            if constexpr (k == 48) LD4A(fv, ofl, 0);
            if constexpr (k == 4) OT(1);
            if constexpr (k == 20) OT(2);
            if constexpr (k == 36) OT(3);
            if constexpr (k == 41) OT(4);
            SCHED();
        SEND
        OT(5);
        asm volatile("s_nop 11" : "+v"(acc), "+v"(pa));
#pragma unroll
        for (int i = 0; i < 8; ++i) st4s(rs, pa, (u32)((4 * (i >> 1) + (i & 1)) * (4 * DM * 4)), pk(acc[2 * i], acc[2 * i + 1]));
        asm volatile("s_waitcnt vmcnt(15)" : "+v"(fv) :: "memory");
        OT(6);
        LS3(L == 0 && w == 1, 2, kq * 5 + ng);
        early = far == 0u || all_(ofl == OOB || fv >= need);
        if (far != 0u && early) { first(ndst, ov); VMW(10); }
        else { fv = ld4(rs, ofl); asm volatile("s_waitcnt vmcnt(0)" : "+v"(fv) :: "memory"); }
        BAR();
        st4s(rs, fo, 0, (u32)s + 1);
        pa += P_ITEM;
        LS2(L == 0 && w == 1, 160 + kq * 5 + ng);
    }
    LDUMP(L == 0 && w == 1, kq * 5 + ng);
}

DEV void ln_stats2(const f32x2 (&x)[10], f32x2& mean, f32x2& rstd) {
    const f32x2 c = {f_(uu(u_(x[0].x))), f_(uu(u_(x[0].y)))};
    f32x2 d[10];
#pragma unroll
    for (int i = 0; i < 10; ++i) d[i] = x[i] - c;
    f32x2 a = ((d[0] + d[1]) + (d[2] + d[3])) + ((d[4] + d[5]) + (d[6] + d[7])) + (d[8] + d[9]);
    f32x2 b = (d[0] * d[0] + d[2] * d[2] + d[4] * d[4] + d[6] * d[6] + d[8] * d[8]) + (d[1] * d[1] + d[3] * d[3] + d[5] * d[5] + d[7] * d[7] + d[9] * d[9]);
    float a0, a1, b0, b1;
#define LN_DP4(m) "v_add_f32_dpp %0, %0, %0 " m " row_mask:0xf bank_mask:0xf\n\tv_add_f32_dpp %1, %1, %1 " m " row_mask:0xf bank_mask:0xf\n\t" \
                  "v_add_f32_dpp %2, %2, %2 " m " row_mask:0xf bank_mask:0xf\n\tv_add_f32_dpp %3, %3, %3 " m " row_mask:0xf bank_mask:0xf\n\t"
    asm("s_nop 1\n\t" LN_DP4("quad_perm:[1,0,3,2]") LN_DP4("quad_perm:[2,3,0,1]") LN_DP4("row_ror:4") LN_DP4("row_ror:8")
        "v_permlane16_swap_b32 %0, %1\n\tv_add_f32 %0, %0, %1\n\tv_permlane16_swap_b32 %2, %3\n\tv_add_f32 %2, %2, %3\n\ts_nop 1\n\t"
        "v_permlane32_swap_b32 %0, %2\n\tv_add_f32 %0, %0, %2\n\ts_nop 0\n\t"
        "v_readlane_b32 %4, %0, 0\n\tv_readlane_b32 %5, %0, 16\n\tv_readlane_b32 %6, %0, 32\n\tv_readlane_b32 %7, %0, 48\n\ts_nop 1"
        : "+v"(a.x), "+v"(b.x), "+v"(a.y), "+v"(b.y), "=s"(a0), "=s"(b0), "=s"(a1), "=s"(b1));
    const f32x2 m = (f32x2){a0, a1} * (1.0f / DM), v = (f32x2){b0, b1} * (1.0f / DM) - m * m;
    const f32x2 q = up2(pkv(up2(pkv((f32x2){fmaxf(v.x, 0.f), fmaxf(v.y, 0.f)})) + 1e-5f));
    const f32x2 sd = up2(pk(__builtin_amdgcn_sqrtf(q.x), __builtin_amdgcn_sqrtf(q.y)));
    mean = up2(pkv(c + m)); rstd = (f32x2){__builtin_amdgcn_rcpf(sd.x), __builtin_amdgcn_rcpf(sd.y)};
}
DEV void role_row(const Params& p, lptr lds, int id, v2u ep) {
    const int lane = threadIdx.x & 63, w = uni(threadIdx.x >> 6);
    const int pr = id >> 2, L = id & 1, par = (id >> 1) & 1;
    const rsrc_t rs = mk_rsrc(p.ws, W_END), rx = mk_rsrc(p.x, ACT);
    bool dead = false;
    const u32 fp = lane < 20 ? foff(F_P + L * 32 + lane) : OOB, l16 = (u32)lane * 16u, l8 = (u32)lane * 8u, l4 = (u32)lane * 4u, fb = uu(w * CV_W);
    lptr F = lds + w * CV_W;
    MK_SRD();
    for (int s = par; s < NSEG; s += 2) {
        const u32 ro = (u32)(s * 32 + 2 * pr) * (DM * 2);
        if ((s & 6) == 0) {
            if ((s & 30) == 0) pre_poll(rs, 0, s < 32 ? NPRE : NCV, s < 32 ? 3u : 5u, ep);
            if (id == 0 && s == 0) LST(274, rt32());
            if (s == 48) st8s(rs, (id == 1 && lane == 0) ? foff(F_EPOCH) : OOB, 0, (v2u){ep.x + 8u, ep.y});
#pragma unroll 1
            for (int i = 0; i < 5; ++i) {
                const u32 d = fb + i * 2048, o = W_Y + (u32)(16 * pr + (s >> 3)) * 10240u + i * 2048;
                DMA32(d, l16, o); DMA32(d + 10240u, l16, o + 81920u);
            }
            DRAIN();
        }
        RowX xr[2];
        auto xload = [&](rsrc_t r, u32 o, auto aux) {
#pragma unroll
            for (int i = 0; i < 2; ++i) {
                xr[i].a = __builtin_amdgcn_raw_buffer_load_b64(r, l8, o + i * (DM * 2), aux.value); xr[i].b = __builtin_amdgcn_raw_buffer_load_b64(r, l8 + 512u, o + i * (DM * 2), aux.value);
                xr[i].c = __builtin_amdgcn_raw_buffer_load_b32(r, l4 + 1024u, o + i * (DM * 2), aux.value);
            }
        };
        if (L == 0) xload(rx, ro, IC<0>{});
        f32x2 y[10], x[10];
        {
            lptr ya = F + (s & 7) * 2 + l16, yc = F + (s & 7) * 2 + 2048 + (lane >> 1) * 16 + (lane & 1) * 5120;
#pragma unroll
            for (int i = 0; i < 10; ++i) {
                lptr q = i < 8 ? ya + (i & 3) * 2560 + (i >> 2) * 1024 : yc + (i & 1) * 2560;
                y[i] = (f32x2){lo_f(*(LAS u16*)q), lo_f(*(LAS u16*)(q + 10240))}; PIN(y[i]);
            }
        }
        wait_flags(rs, fp, (u32)s + 1, dead);
        LS3(L == 0, 3, ri);
        LMX(L == 0, 0, s, rt32());
        {
            const u32 o = uu(W_P + (u32)(L * NSEG + s) * P_ITEM + (u32)pr * (4 * DM * 4));
            if (L == 1) xload(rs, W_X2 + ro, IC<16>{});
            v4u a[4], b[4]; v2u c[4];
#pragma unroll
            for (int kq = 0; kq < 4; ++kq) { a[kq] = ld16s(rs, l16, o + kq * (DM * 4)); SCHED(); b[kq] = ld16s(rs, l16 + 1024u, o + kq * (DM * 4)); SCHED(); c[kq] = ld8s(rs, l8 + 2048u, o + kq * (DM * 4)); SCHED(); }
            const u32 u0[5] = {xr[0].a.x, xr[0].a.y, xr[0].b.x, xr[0].b.y, xr[0].c}, u1[5] = {xr[1].a.x, xr[1].a.y, xr[1].b.x, xr[1].b.y, xr[1].c};
#pragma unroll
            for (int i = 0; i < 5; ++i) { x[2 * i] = (f32x2){lo_f(u0[i]), lo_f(u1[i])}; x[2 * i + 1] = (f32x2){hi_f(u0[i]), hi_f(u1[i])}; }
#pragma unroll
            for (int kq = 0; kq < 4; ++kq)
#pragma unroll
                for (int i = 0; i < 10; ++i)
                    asm volatile("v_dot2_f32_bf16 %0, %2, %3, %0\n\tv_dot2_f32_bf16 %1, %2, %4, %1" : "+v"(x[i].x), "+v"(x[i].y)
                                 : "v"(i < 4 ? a[kq][i & 3] : i < 8 ? b[kq][i & 3] : c[kq][i & 1]), "s"(0x3F80u), "s"(0x3F800000u));
            asm volatile("s_nop 2" : "+v"(x[8]), "+v"(x[9]));
        }
        LS3(L == 0, 4, ri);
        f32x2 mean, rstd;
        ln_stats2(x, mean, rstd);
#pragma unroll
        for (int i = 0; i < 10; ++i) x[i] += (x[i] - mean) * (y[i] * rstd);
        u32 xp[2][5];
#pragma unroll
        for (int i = 0; i < 5; ++i) { xp[0][i] = pkv((f32x2){x[2 * i].x, x[2 * i + 1].x}); xp[1][i] = pkv((f32x2){x[2 * i].y, x[2 * i + 1].y}); }
        if (L == 0) {
#pragma unroll
            for (int i = 0; i < 5; ++i) { x[2 * i] = (f32x2){lo_f(xp[0][i]), lo_f(xp[1][i])}; x[2 * i + 1] = (f32x2){hi_f(xp[0][i]), hi_f(xp[1][i])}; }
            ln_stats2(x, mean, rstd);
#pragma unroll
            for (int i = 0; i < 10; ++i) x[i] = (x[i] - mean) * rstd;
#pragma unroll
            for (int r = 0; r < 2; ++r) {
                u32 hp[5];
#pragma unroll
                for (int i = 0; i < 5; ++i) hp[i] = pkv((f32x2){x[2 * i][r], x[2 * i + 1][r]});
                store_h(rs, W_H2, s * 32 + 2 * pr + r, hp);
            }
#pragma unroll
            for (int r = 0; r < 2; ++r) {
                st8s(rs, l8, W_X2 + ro + r * (DM * 2), (v2u){xp[r][0], xp[r][1]});
                st8s(rs, l8 + 512u, W_X2 + ro + r * (DM * 2), (v2u){xp[r][2], xp[r][3]});
                st4s(rs, l4 + 1024u, W_X2 + ro + r * (DM * 2), xp[r][4]);
            }
            LS3(L == 0, 5, ri);
            DRAIN();
            st4s(rs, lane == 0 ? foff(F_H2 + par * 16 + pr) : OOB, 0, (u32)s + 1);
        } else {
#pragma unroll
            for (int r = 0; r < 2; ++r) {
                u16* o_ = p.out + (size_t)(s * 32 + 2 * pr + r) * DM;
                *(v2u*)(o_ + 4 * lane) = (v2u){xp[r][0], xp[r][1]};
                *(v2u*)(o_ + 256 + 4 * lane) = (v2u){xp[r][2], xp[r][3]};
                *(u32*)(o_ + 512 + 2 * lane) = xp[r][4];
            }
        }
        if (pr == 0) LST(320 + L * 64 + s, rt32());
        LS2(L == 0, 224 + ri);
        LMX(L == 0, 1, s, rt32());
    }
    LDUMP(L == 0, 32 + ri);
}

constexpr float SCALE = 0.08838834764831845f, LOG2E = 1.4426950408889634f;
constexpr int C_K = 0, C_V = 8192, C_QX = 16384, C_MEM = 24576, C_Q = C_MEM + 10240, C_KM = C_Q + 2304, C_VM = C_KM + 2304, C_DUMP = C_VM + 2048,
              C_ZERO = C_DUMP + 1024, C_WL = C_ZERO + 128;
constexpr int NLF = 14;
static_assert(C_WL % 16 == 0 && C_WL + 4 * NLF * 2048 <= LDS_BYTES, "head lds");
#define CH_VIS 10
#define CH_H0 104
#define CH_W0 81
#define CH_MH 2
#define CH_MW 4
#define CH_REC 34
#define CH_LATE (-10)
#define CH_RISE 12u
#define CH_FALL 3u
#define CH_PRE 9
#define CH_T0 190
#define CH_UP 2
#define CH_WX 6
#define WAIT_TXT(tmp, t4) "s_memrealtime vcc\n\t.p2align 5\n1:\n\ts_waitcnt lgkmcnt(0)\n\ts_sub_u32 " tmp ", " t4 ", vcc_lo\n\ts_memrealtime vcc\n\ts_cmpk_lt_u32 " tmp ", 400\n\ts_cbranch_scc1 1b\n\t" \
                          "s_add_u32 " tmp ", " tmp ", 4\n\ts_cselect_b32 " tmp ", " tmp ", 0\n2:\n\ts_sub_u32 " tmp ", " tmp ", 1\n\ts_cbranch_scc0 2b\n\t"
DEV void wait_until(u32 t) { u32 tmp; asm volatile(WAIT_TXT("%0", "%1") "s_waitcnt lgkmcnt(0)" : "=&s"(tmp) : "s"(uu(t - 4u)) : "vcc", "scc", "memory"); }
#define SMF_A(acc, a, b) asm volatile("v_smfmac_f32_16x16x64_bf16 %0, %1, %2, %3" : "+v"(acc) : "v"(a), "a"(b), "v"(idx))
#define SMF_V(acc, a, b) asm volatile("v_smfmac_f32_16x16x64_bf16 %0, %1, %2, %3" : "+v"(acc) : "v"(a), "v"(b), "v"(idx))
#define MF0(d, a, b) asm volatile("v_mfma_f32_16x16x32_bf16 %0, %1, %2, 0" : "=&v"(d) : "v"(a), "v"(b))
#define MF1(d, a, b) asm volatile("v_mfma_f32_16x16x32_bf16 %0, %1, %2, %0" : "+v"(d) : "v"(a), "v"(b))
#define MF0H(d, a, b) asm volatile("v_mfma_f32_16x16x16_bf16 %0, %1, %2, 0" : "=&v"(d) : "v"(a), "v"(b))
#define MF1H(d, a, b) asm volatile("v_mfma_f32_16x16x16_bf16 %0, %1, %2, %0" : "+v"(d) : "v"(a), "v"(b))
DEV v8u tr_frag(lptr p0, lptr p1, lptr p2, lptr p3) { return cat8(tr2(p0, p1), tr2(p2, p3)); }
constexpr int qslot(int ks) { return (ks & 1) && ks < 8 ? ks >> 1 : -1; }
constexpr int kslot(int ks) { return ks < 4 ? 4 + ks : -1; }
constexpr int vslot(int ks) { return ks >= 4 ? 4 + ks : -1; }
DEV u32 ring_map(u32 off, u32 half) { return ((off >> 13) << 14) + (half << 13) + (off & 8191u); }
#define ZRD(x, i) x = __builtin_bit_cast(f32x4, lds_r16(ZP + 16 * (i)))
#define VMAX(d, a, b) asm volatile("v_max_f32 %0, %1, %2" : "=v"(d) : "v"(a), "v"(b))
#define VMAX3(d, a, b, c_) asm volatile("v_max3_f32 %0, %1, %2, %3" : "=v"(d) : "v"(a), "v"(b), "v"(c_))
#define VEXP(d, a) asm volatile("v_exp_f32 %0, %1" : "=v"(d) : "v"(a))
#define VMULC(d, a) asm volatile("v_mul_f32 %0, 0x3e0293ee, %1" : "=v"(d) : "v"(a))
#define CADD(d, a, b) do { d = (a) + (b); PIN(d); } while (0)
#define CMULC(d, a) do { d = (a) * CEXP; PIN(d); } while (0)
#define CCVT(d, a, b) do { d = pk(a, b); PIN(d); } while (0)
#define LGK(n) __builtin_amdgcn_s_waitcnt(0xC07F | (n) << 8)
#define SEL(d, a, b) asm volatile("v_cndmask_b32 %0, %1, %2, %3" : "=v"(d) : "v"(a), "v"(b), "s"(himask))
#define QPERM(d, a, q) asm volatile("v_mov_b32_dpp %0, %1 quad_perm:" q " row_mask:0xf bank_mask:0xf" : "=v"(d) : "v"(a))

#define AL_H 3
#define AL_W 0
#define HD_SMP 56
DEV void role_head(const Params& p, lptr lds, int L, int h, v2u ep) {
    const int tid = threadIdx.x, lane = tid & 63, w = uni(tid >> 6), g = lane >> 4, r = lane & 15;
    const rsrc_t rs = mk_rsrc(p.ws, W_END);
    v8u wq0[10], wq1[10], wk0[10], wk1[10], wv0[10], wv1[10];
    lptr WL = lds + C_WL + w * (NLF * 2048) + lane * 16;
    {
        constexpr int GS = 1056, CS = 48 * GS;
        const v4u sq = mk_srd(p.Wq, DM * NQ * 2), sk = mk_srd(p.Wk, DM * NQ * 2), sv = mk_srd(p.Wv, DM * NQ * 2);
        const u32 vo = (u32)(g * NQ + h * DH + 8 * (r ^ ((g & 1) << 3))) * 2u;
        auto fill = [&](int ch) {
#pragma unroll 1
            for (int i = 0; i < 4; ++i) {
                const u32 dst = uu((ch % 3) * CS + (4 * i + w) * GS), so = uu((ch * 64 + 16 * i + 4 * w) * (NQ * 2));
                DMAIN(sq, dst, vo, so); DMAIN(sk, dst + 16 * GS, vo, so); DMAIN(sv, dst + 32 * GS, vo, so);
            }
        };
        fill(0); fill(1); fill(2);
        const int q4 = r >> 2;
        lptr tb = lds + (4 * (g >> 1) + 2 * (q4 >> 1) + (g & 1)) * GS + (q4 & 1) * 256 + ((w * 64 + (r & 3) * 8) ^ ((q4 & 1) << 7));
        SFOR(ch, 10)
            VMW(ch < 8 ? 24 : 108 - 12 * ch);
            BAR();
            auto frag = [&](int m, int t) { lptr b = tb + (ch % 3) * CS + m * 16 * GS + t * 32; return tr_frag(b, b + 512, b + 8 * GS, b + 8 * GS + 512); };
            wq0[ch] = frag(0, 0); wq1[ch] = frag(0, 1); wk0[ch] = frag(1, 0); wk1[ch] = frag(1, 1); wv0[ch] = frag(2, 0); wv1[ch] = frag(2, 1);
            asm volatile("" : "+a"(wq0[ch]), "+a"(wk0[ch]), "+v"(wv0[ch]), "+v"(wv1[ch]));
            if constexpr (qslot(ch) < 0) asm volatile("" : "+a"(wq1[ch])); else asm volatile("" : "+v"(wq1[ch]));
            if constexpr (kslot(ch) < 0) asm volatile("" : "+a"(wk1[ch])); else asm volatile("" : "+v"(wk1[ch]));
            BAR();
            if constexpr (ch < 7) fill(ch + 3);
        SEND
#pragma unroll
        for (int ch = 0; ch < 10; ++ch) {
            if (qslot(ch) >= 0) { lds_w16(WL + qslot(ch) * 2048, wq1[ch].lo); lds_w16(WL + qslot(ch) * 2048 + 1024, wq1[ch].hi); }
            if (kslot(ch) >= 0) { lds_w16(WL + kslot(ch) * 2048, wk1[ch].lo); lds_w16(WL + kslot(ch) * 2048 + 1024, wk1[ch].hi); }
            if (vslot(ch) >= 0) { lds_w16(WL + vslot(ch) * 2048, wv1[ch].lo); lds_w16(WL + vslot(ch) * 2048 + 1024, wv1[ch].hi); }
        }
        CFENCE();
    }
    const u32 idx = (r & 2) ? 0xEEEEu : 0x4444u;
    unsigned long long himask = 0xCCCCCCCCCCCCCCCCull;
    PINS(himask);
    bool dead = false;
    if (h == 3) LST(266 + L, rt32());
    if (w == 0) LST(1240 + L * 32 + h, rt32());
    if (L == 1) {
        KvIn in;
        kvt_load(p, KT0 + 4 * h + w, in, [] {}); kvt_make(rs, lds + w * 9216, KT0 + 4 * h + w, in);
        DRAIN(); BAR();
        pre_post(rs, 64 + h, 3u, ep);
    }
    const u32 pol0 = pol_wait(rs, ep);
    if (h == 3 && L == 0) LST(277, rt32());
    const u32 l16 = (u32)lane * 16u;
    const u32 mring = W_MRING + (u32)L * (MB / 4) + pol0 * 8192u, hint = mring + 4 * 32768u;
    const u32 fkh = lane < 3 ? foff(F_QKV + L * 96 + lane * 32 + h) : lane < 7 ? hint + (u32)(lane - 3) * 4u : OOB;
    u32 tm = 0;
    MK_SRD();
    u32 ksrc = uu(W_KQ + (u32)(L * (NSEG - 1) * NH + h) * 16384u + w * 2048), vsrc = ksrc + (W_VO - W_KQ);
    u32 dk = uu(C_K + w * 2048), dv = dk + (C_V - C_K), dq = dk + (C_QX - C_K), dm = uu(C_MEM + w * 2048), dm2 = uu(w < 2 ? C_MEM + 8192 + w * 1024 : C_DUMP);
    asm volatile("" : "+s"(dk), "+s"(dv), "+s"(dq), "+s"(dm), "+s"(dm2));
    auto keys_wait = [&](u32 need) {
#pragma unroll 1
        for (int n = 0; !dead; ++n) {
            const bool beat = need + 2 * L > 2u;
            if (beat) wait_until(tm);
            const u32 hv = ld4(rs, lane == 7 ? foff(F_ERR) : fkh);
            if (beat) { tm = rdl(hv, 5); wait_until(rdl(hv, 3) - 30u); }
            if (((u32)__builtin_amdgcn_ballot_w64(ld4(rs, fkh) < need) & 7u) == 0u) break;
            dead = n > SPIN_CAP || rdl(hv, 7) != 0u;
            st4s(rs, dead ? foff(F_ERR) : OOB, 0, 1u);
        }
        CFENCE();
        DMA32(dk, l16, ksrc); DMA32(dv, l16, vsrc); DMA32(dq, l16, ksrc ^ 8192u);
    };
    if (L == 0) { st4s(rs, tid == 0 ? hint + 4096u + (u32)h * 128u : OOB, 0, 0u); keys_wait(1u); }
    lptr qw = lds + C_Q + (2 * g) * 288 + (32 * w + 2 * r) * 2;
    lptr kw = qw + (C_KM - C_Q); PIN(kw);
    lptr qr = lds + C_Q + (2 * (r >> 2) + (r & 1)) * 288 + g * 16;
    lptr LB = lds + l16, VX = lds + C_V + w * 2048 + l16;
    PIN(VX);
    if (tid >= 64 && tid < 70) { const u32 z = tid == 68 ? 0x3F803F80u : tid == 69 ? PSENT : 0u; lds_w16(lds + C_ZERO + (tid - 64) * 16, (v4u){z, z, z, z}); }
    u32 zaddr = 0; PIN(zaddr);
    lptr ZP = lds + zaddr + C_ZERO;
    const float CEXP = SCALE * LOG2E;
#define LF_RD(slot, hf) lf[slot][hf] = lds_r16(WL + (slot) * 2048 + (hf) * 1024)
    BAR();
    const u32 ofl = tid == 0 ? foff(F_O + L * 32 + h) : OOB;
    u32 oso = (((g | L) & 1) ? OOB : 0u) + W_ORING + (u32)L * (MB / 2) + ring_map((u32)((r >> 3) * 32768 + h * 1024 + w * 256 + (g >> 1) * 128 + (r & 7) * 16), pol0);
    unsigned long long vis[8];
#pragma unroll
    for (int i = 0; i < 8; ++i) vis[i] = __builtin_amdgcn_ballot_w64(16 * (i >> 2) + 4 * g + (i & 3) <= 16 * (w & 1) + r);
    u32 missed = 0;
    for (int s = -L; s < NSEG;) {
        u32 sv = (u32)max(s, 0), ms = mring + (sv & 3u) * 32768u + w * 2048, ms2 = mring + (sv & 3u) * 32768u + 16384u + (w & 1) * 1024;
        asm volatile("" : "+s"(ms), "+s"(ms2)); PIN(sv);
        f32x4 aq0, aq1, ak0, ak1;
        v4u lf[NLF][2];
        ZRD(aq0, 0); ZRD(aq1, 1); ZRD(ak0, 2); ZRD(ak1, 3);
#pragma unroll
        for (int i = 0; i < 4; ++i) { LF_RD(i, 0); LF_RD(i, 1); }
        SCHED();
        v4u A[10];
#define HT(k) do {} while (0)
        v4u kx[8], qf[4], kmf[4], vx[2], ones, P;
        float fa, fb, fc, fd, inv, sx[4], t[4], tm0, tm1; u32 pa, pb, fl, e01, e23, pmm;
        f32x4 av0, av1, s0, s1, sm, oa, ob, rsum;
        v2u vm, pm;
#define LW(sl) cat8(lf[sl][0], lf[sl][1])
#define PQ(ks, tl) do { if constexpr (!(tl)) SMF_A(aq0, A[ks], wq0[ks]); else if constexpr (qslot(ks) < 0) SMF_A(aq1, A[ks], wq1[ks]); else { const v8u lw_ = LW(qslot(ks)); SMF_V(aq1, A[ks], lw_); } } while (0)
#define PK(ks, tl) do { if constexpr (!(tl)) SMF_A(ak0, A[ks], wk0[ks]); else if constexpr (kslot(ks) < 0) SMF_A(ak1, A[ks], wk1[ks]); else { const v8u lw_ = LW(kslot(ks)); SMF_V(ak1, A[ks], lw_); } } while (0)
#define PV(ks, tl) do { if constexpr (!(tl)) SMF_V(av0, A[ks], wv0[ks]); else if constexpr (vslot(ks) < 0) SMF_V(av1, A[ks], wv1[ks]); else { const v8u lw_ = LW(vslot(ks)); SMF_V(av1, A[ks], lw_); } } while (0)
#define KXR(i) kx[i] = lds_r16(LB + C_K + ((i) & 1) * 4096 + ((i) >> 1) * 1024)
#define XW(q_) do { *(LAS u32*)(q_) = pa; *(LAS u32*)((q_) + 288) = pb; } while (0)
        auto slot = [&](auto zc) __attribute__((always_inline)) {
            constexpr int z = decltype(zc)::value;
            if constexpr (z < 14) PQ(z >> 1, z & 1);
            else if constexpr (z < 17) PQ(z - 7, 0);
            else if constexpr (z < 20) PQ(z - 10, 1);
            else if constexpr (z < 34) PK((z - 20) >> 1, z & 1);
            else if constexpr (z < 37) PK(z - 27, 0);
            else if constexpr (z < 40) PK(z - 30, 1);
            else if constexpr (z < 42) { if constexpr (z == 40) MF0(s0, kx[0], qf[0]); else MF0(s1, kx[1], qf[0]); }
            else if constexpr (z < 48) { if constexpr (z & 1) MF1(s1, kx[z - 40], qf[(z - 40) >> 1]); else MF1(s0, kx[z - 40], qf[(z - 40) >> 1]); }
            else if constexpr (z < 54) PV(z - 48, 0);
            else if constexpr (z == 54) MF0(sm, kmf[0], qf[0]);
            else if constexpr (z < 58) MF1(sm, kmf[z - 54], qf[z - 54]);
            else if constexpr (z < 62) PV(z - 52, 0);
            else if constexpr (z < 64) PV(z - 62, 1);
            else if constexpr (z == 64) MF0(oa, vx[0], P);
            else if constexpr (z == 65) MF0(ob, vx[1], P);
            else if constexpr (z == 66) MF0(rsum, ones, P);
            else if constexpr (z < 75) PV(z < 70 ? z - 63 : z == 70 ? 2 : z == 71 ? 7 : z == 72 ? 3 : z - 65, 1);
            else { const v2u ones2 = ones.lo; MF1H(rsum, ones2, pm); }
            if constexpr (z < 6) A[4 + z] = lds_r16(LB + C_MEM + (4 + z) * 1024);
            if constexpr (z >= 6 && z < 10) LF_RD(4 + ((z - 6) >> 1), z & 1);
            if constexpr (z >= 14 && z < 18) LF_RD(6 + ((z - 14) >> 1), z & 1);
            if constexpr (z >= 22 && z < 28) KXR(z - 22);
            if constexpr (z == 28) XW(qw);
            if constexpr (z == 29 || z == 30) KXR(z - 23);
            if constexpr (z == 32 || z == 47) asm volatile("s_barrier" ::: "memory");
            if constexpr (z >= 33 && z < 37) qf[z - 33] = lds_r16(qr + (z - 33) * 64);
            if constexpr (z == 20) ZRD(av0, 0);
            if constexpr (z == 43) XW(kw);
            if constexpr (z == 44 || z == 45) vx[z - 44] = lds_r16(VX + (z - 44) * 1024);
            if constexpr (z >= 48 && z < 52) kmf[z - 48] = lds_r16(qr + (C_KM - C_Q) + (z - 48) * 64);
            if constexpr (z == 52) ZRD(av1, 1);
            if constexpr (z == 37) ones = lds_r16(ZP + 64);
            if constexpr (z >= 57 && z < 64) LF_RD(8 + ((z - 57) >> 1), (z - 57) & 1);
            if constexpr (z >= 65 && z < 70) LF_RD(8 + ((z - 58) >> 1), (z - 58) & 1);
            if constexpr (z == HD_SMP) LD4A(fl, fkh, 0);
            SCHED();
            if constexpr (z == 19) HT(2);
            if constexpr (z == 32) HT(3);
            if constexpr (z == 47) HT(4);
            if constexpr (z == 64) HT(5);
            if constexpr (z == 19) LGK(4);
            if constexpr (z == 21 || z == 41) LGK(1);
            if constexpr (z == 56 || z == 73) LGK(0);
            if constexpr (z == 31 || z == 46) LGK(2);
            if constexpr (z == 39) LGK(4);
            if constexpr (z >= 53 && z < 56) LGK(57 - z);
            if constexpr (z == 64) LGK(3);
            if constexpr (z == 68) LGK(5);
            if constexpr (z >= 70 && z < 73) LGK(74 - z);
            SCHED();
            if constexpr (z == 18) CADD(fa, aq0[0], aq0[2]);
            if constexpr (z == 19) CADD(fb, aq0[1], aq0[3]);
            if constexpr (z == 20) CMULC(fa, fa);
            if constexpr (z == 21) CADD(fc, aq1[0], aq1[2]);
            if constexpr (z == 22) CADD(fd, aq1[1], aq1[3]);
            if constexpr (z == 23) CMULC(fc, fc);
            if constexpr (z == 24) CMULC(fb, fb);
            if constexpr (z == 25) CMULC(fd, fd);
            if constexpr (z == 26) CCVT(pa, fa, fc);
            if constexpr (z == 27) CCVT(pb, fb, fd);
            if constexpr (z == 38) CADD(fa, ak0[0], ak0[2]);
            if constexpr (z == 39) CADD(fb, ak0[1], ak0[3]);
            if constexpr (z == 41) { CADD(fc, ak1[0], ak1[2]); CADD(fd, ak1[1], ak1[3]); }
            if constexpr (z == 42) { CCVT(pa, fa, fc); CCVT(pb, fb, fd); }
            if constexpr (z >= 49 && z < 53) SEL(sx[z - 49], s0[z - 49], s1[z - 49]);
            if constexpr (z >= 53 && z < 57) VEXP(t[z - 53], sx[z - 53]);
            if constexpr (z == 57) CCVT(e01, t[0], t[1]);
            if constexpr (z == 58) CCVT(e23, t[2], t[3]);
            if constexpr (z == 59) QPERM(P.x, e01, "[0,1,0,1]");
            if constexpr (z == 60) QPERM(P.z, e01, "[2,3,2,3]");
            if constexpr (z == 61) QPERM(P.y, e23, "[0,1,0,1]");
            if constexpr (z == 62) QPERM(P.w, e23, "[2,3,2,3]");
            if constexpr (z == 63) CADD(fa, av0[0], av0[2]);
            if constexpr (z == 64) { asm volatile("" :: "v"(sm)); VEXP(tm0, sm[0]); }
            if constexpr (z == 65) VEXP(tm1, sm[1]);
            if constexpr (z == 66) CADD(fb, av0[1], av0[3]);
            if constexpr (z == 67) CCVT(pmm, tm0, tm1);
            if constexpr (z >= 68 && z < 72) SEL(oa[z - 68], oa[z - 68], ob[z - 68]);
            if constexpr (z == 72) asm volatile("v_cndmask_b32 %0, %1, 0, %2" : "=v"(pm.x) : "v"(pmm), "s"(himask));
            if constexpr (z == 73) asm volatile("v_cndmask_b32 %0, 0, %1, %2" : "=v"(pm.y) : "v"(pmm), "s"(himask));
            if constexpr (z == 74) LGK(15);
            if constexpr (z == 75) CCVT(vm.x, fa, fb);
            SCHED();
        };
        {
            u32 tmp;
            asm volatile("s_mov_b32 m0, %2\n\t" WAIT_TXT("%0", "%1") "buffer_load_dwordx4 %4, %5, %6 offen sc1 lds\n\tbuffer_load_dwordx4 %4, %5, %6 offen offset:1024 sc1 lds\n\t"
                         "s_mov_b32 m0, %3\n\ts_nop 0\n\tbuffer_load_dwordx4 %4, %5, %7 offen sc1 lds\n\t.p2align 6\n\t.rept %8\n\ts_nop 0\n\t.endr\n\ts_waitcnt vmcnt(0) lgkmcnt(0)\n\ts_barrier"
                         : "=&s"(tmp) : "s"(uu(tm - 4u)), "s"(dm), "s"(dm2), "v"(l16), "s"(srd), "s"(ms), "s"(ms2), "i"(AL_H) : "vcc", "scc", "memory");
        }
        HT(1);
#pragma unroll
        for (int ks = 0; ks < 4; ++ks) A[ks] = lds_r16(LB + C_MEM + ks * 1024);
        SCHED();
        st4s(rs, ofl, 0, sv);
        SCHED();
        static_for<0, 76>(slot);
        asm volatile("s_nop 2\n\tv_add_f32 %1, %3, %5\n\tv_add_f32 %2, %4, %6\n\tv_cvt_pk_bf16_f32 %0, %1, %2"
                     : "=v"(vm.y), "=&v"(fc), "=&v"(fd) : "v"(av1[0]), "v"(av1[1]), "v"(av1[2]), "v"(av1[3]));
        __builtin_amdgcn_s_waitcnt(0xCF7F);
        asm volatile("s_nop 0\n\tv_mfma_f32_16x16x16_bf16 %0, %2, %3, %0\n\tv_rcp_f32 %1, %4" : "+v"(oa), "=&v"(inv) : "v"(vm), "v"(pm), "v"(rsum[0]), "v"(rsum));
        SCHED();
        if (__builtin_expect(__builtin_amdgcn_ballot_w64(u_(rsum[0]) - 0x21800000u > 0x3C000000u) != 0ull, 0)) {
            DRAIN(); SCHED();
            bool miss = false;
#pragma unroll 1
            for (int i = 0; i < 10; ++i) { const v4u a = lds_r16(LB + C_MEM + i * 1024); miss |= a.x == PSENT || a.y == PSENT || a.z == PSENT || a.w == PSENT; }
            if (missed < SPIN_CAP && !all_(!miss)) {
                *(LAS u32*)(lds + C_DUMP + (w == 0 ? 0 : 64)) = ld4(rs, foff(F_ERR));
                asm volatile("" : "+v"(fl) :: "memory");
                tm = rdl(fl, 5);
                LAD(w == 0, 1, L * 64 + s);
                BAR();
                missed = uu(*(LAS u32*)(lds + C_DUMP)) != 0u ? SPIN_CAP : missed + 1;
                st4s(rs, missed == SPIN_CAP ? foff(F_ERR) : OOB, 0, 1u);
                BAR();
                continue;
            }
            float mx = fmaxf(fmaxf(fmaxf(sx[0], sx[1]), fmaxf(sx[2], sx[3])), fmaxf(sm[0], sm[1]));
            mx = max32(max16(fmaxf(mx, dpp<0x4E>(mx))));
#pragma unroll
            for (int i = 0; i < 4; ++i) t[i] = __builtin_amdgcn_exp2f(sx[i] - mx);
            e01 = pk(t[0], t[1]); e23 = pk(t[2], t[3]);
            pmm = pk(__builtin_amdgcn_exp2f(sm[0] - mx), __builtin_amdgcn_exp2f(sm[1] - mx));
            asm volatile("v_cndmask_b32 %0, %2, 0, %3\n\tv_cndmask_b32 %1, 0, %2, %3" : "=&v"(pm.x), "=&v"(pm.y) : "v"(pmm), "s"(himask));
            asm volatile("s_nop 1" : "+v"(e01), "+v"(e23), "+v"(pm));
            QPERM(P.x, e01, "[0,1,0,1]"); QPERM(P.z, e01, "[2,3,2,3]"); QPERM(P.y, e23, "[0,1,0,1]"); QPERM(P.w, e23, "[2,3,2,3]");
            NOPS(1);
            MF0(rsum, ones, P); MF0(oa, vx[0], P); MF0(ob, vx[1], P);
            NOPS(7);
            { const v2u ones2 = ones.lo; MF1H(rsum, ones2, pm); }
#pragma unroll
            for (int i = 0; i < 4; ++i) SEL(oa[i], oa[i], ob[i]);
            NOPS(1);
            MF1H(oa, vm, pm);
            asm volatile("s_nop 7\n\tv_rcp_f32 %0, %1" : "=v"(inv) : "v"(rsum[0]), "v"(rsum));
        }
        {
            v4u oc; f32x2 u0 = oa.lo, u1 = oa.hi; const f32x2 i2 = {inv, inv};
            asm volatile("s_nop 0\n\tv_pk_mul_f32 %0, %0, %2\n\tv_pk_mul_f32 %1, %1, %2" : "+v"(u0), "+v"(u1) : "v"(i2));
            __builtin_amdgcn_s_waitcnt(0xCF7F);
            asm volatile("v_cvt_pk_bf16_f32 %0, %4, %5\n\tv_cvt_pk_bf16_f32 %2, %4, %5\n\tv_cvt_pk_bf16_f32 %1, %6, %7\n\tv_cvt_pk_bf16_f32 %3, %6, %7\n\t"
                         "v_permlane16_swap_b32 %0, %2\n\ts_nop 0\n\tv_permlane16_swap_b32 %1, %3"
                         : "=&v"(oc.x), "=&v"(oc.y), "=&v"(oc.z), "=&v"(oc.w) : "v"(u0.x), "v"(u0.y), "v"(u1.x), "v"(u1.y));
            st16(rs, oso, oc);
        }
        SCHED();
        unsigned long long clk;
        asm volatile("s_memrealtime %0" : "=s"(clk) :: "memory");
        HT(6);
        v4u tq[4], tk[8], tv[2];
        f32x4 c0, c1, cm;
        u32 oo, kvo; bool kvok;
        u32 ln = l16; PIN(ln);
        lptr QX = lds + C_QX + (w & 1) * 4096 + ln, VO = lds + C_V + (w ^ 1) * 2048 + ln, VMX = lds + C_VM + w * 512 + (ln >> 1);
#pragma unroll
        for (int i = 0; i < 2; ++i) { tq[i] = lds_r16(QX + i * 1024); kmf[i] = lds_r16(qr + (C_KM - C_Q) + i * 64); tk[2 * i] = lds_r16(LB + C_K + i * 1024); tk[2 * i + 1] = lds_r16(LB + C_K + 4096 + i * 1024); }
        *(LAS v2u*)VMX = vm;
        SCHED();
        SFOR(i, 12)
            if constexpr (i == 0) MF0(cm, kmf[0], tq[0]); else if constexpr (i == 1) MF0(c0, tk[0], tq[0]); else if constexpr (i == 2) MF0(c1, tk[1], tq[0]);
            else if constexpr (i % 3 == 0) MF1(cm, kmf[i / 3], tq[i / 3]); else if constexpr (i % 3 == 1) MF1(c0, tk[2 * (i / 3)], tq[i / 3]); else MF1(c1, tk[2 * (i / 3) + 1], tq[i / 3]);
            if constexpr (i < 2) kmf[2 + i] = lds_r16(qr + (C_KM - C_Q) + (2 + i) * 64);
            if constexpr (i == 0) { tq[2] = lds_r16(QX + 2048); tk[4] = lds_r16(LB + C_K + 2048); }
            if constexpr (i == 1) { tk[5] = lds_r16(LB + C_K + 6144); tq[3] = lds_r16(QX + 3072); }
            if constexpr (i == 2) { tk[6] = lds_r16(LB + C_K + 3072); tk[7] = lds_r16(LB + C_K + 7168); }
            if constexpr (i == 3) { tv[0] = lds_r16(VO); tv[1] = lds_r16(VO + 1024); }
            if constexpr (i == 4) { oo = ln + (ln & 0x300u) + ((vsrc ^ 8192u) + (u32)((w & 1) * 256) + ((u32)s & OOB)); PIN(oo); }
            if constexpr (i == 5) { ksrc += NH * 16384; vsrc += NH * 16384; PINS(ksrc); PINS(vsrc); }
            if constexpr (i == 9) { asm volatile("s_waitcnt vmcnt(1)" : "+v"(fl) :: "memory"); kvok = ((u32)__builtin_amdgcn_ballot_w64(fl < (u32)s + 2) & 7u) == 0u; }
            if constexpr (i == 10) { kvo = l16 | (kvok ? 0u : OOB); PIN(kvo); }
            SCHED();
        SEND
        asm volatile("s_waitcnt lgkmcnt(0)\n\ts_barrier" : "+s"(clk) :: "memory");
        const v2u tvm = *(LAS v2u*)(VMX + (512 - (w & 1) * 1024));
        const v4u pv = lds_r16(ZP + 80);
        DMA32(dk, kvo, ksrc);
        SCHED(); asm volatile("" :: "v"(cm));
        float u[10], m, m2;
        {
            float NEG = -1000000000.0f / SCALE; PIN(NEG);
#define MSK(d, a, i) asm volatile("v_cndmask_b32 %0, %1, %2, %3" : "=v"(d) : "v"(NEG), "v"(a), "s"(vis[i]))
#pragma unroll
            for (int i = 0; i < 4; ++i) MSK(u[i], c0[i], i);
#pragma unroll
            for (int i = 0; i < 4; ++i) MSK(u[4 + i], c1[i], 4 + i);
            asm volatile("v_max3_f32 %0, %2, %3, %10\n\tv_max3_f32 %1, %4, %5, %11\n\tv_max3_f32 %0, %0, %6, %7\n\tv_max3_f32 %1, %1, %8, %9\n\t"
                         "v_max_f32 %0, %0, %1\n\tv_mov_b32 %1, %0\n\ts_nop 1\n\tv_permlane16_swap_b32 %0, %1\n\tv_max_f32 %0, %0, %1\n\tv_mov_b32 %1, %0\n\ts_nop 1\n\t"
                         "v_permlane32_swap_b32 %0, %1\n\tv_max_f32 %0, %0, %1\n\tv_mul_f32 %0, 0xbe0293ee, %0"
                         : "=&v"(m), "=&v"(m2) : "v"(u[0]), "v"(u[1]), "v"(u[2]), "v"(u[3]), "v"(u[4]), "v"(u[5]), "v"(u[6]), "v"(u[7]), "v"(cm[0]), "v"(cm[1]));
        }
        DMA32(dv, kvo, vsrc);
        {
            v4u px; v2u p0, p1;
#define EX2(d, a, b) asm volatile("v_fmamk_f32 %0, %2, 0x3e0293ee, %4\n\tv_fmamk_f32 %1, %3, 0x3e0293ee, %4\n\tv_exp_f32 %0, %0\n\tv_exp_f32 %1, %1" : "=&v"(d.x), "=&v"(d.y) : "v"(a), "v"(b), "v"(m))
            f32x2 ex[5];
            EX2(ex[0], u[0], u[1]); EX2(ex[1], u[2], u[3]); EX2(ex[2], u[4], u[5]); EX2(ex[3], u[6], u[7]); EX2(ex[4], cm[0], cm[1]);
            asm volatile("v_cvt_pk_bf16_f32 %0, %8, %9\n\tv_cvt_pk_bf16_f32 %1, %10, %11\n\tv_cvt_pk_bf16_f32 %2, %12, %13\n\tv_cvt_pk_bf16_f32 %3, %14, %15\n\t"
                         "v_mov_b32 %5, 0\n\tv_mov_b32 %6, 0\n\tv_cvt_pk_bf16_f32 %4, %16, %17\n\tv_cvt_pk_bf16_f32 %7, %16, %17"
                         : "=&v"(px.x), "=&v"(px.y), "=&v"(px.z), "=&v"(px.w), "=&v"(p0.x), "=&v"(p0.y), "=&v"(p1.x), "=&v"(p1.y)
                         : "v"(ex[0].x), "v"(ex[0].y), "v"(ex[1].x), "v"(ex[1].y), "v"(ex[2].x), "v"(ex[2].y), "v"(ex[3].x), "v"(ex[3].y), "v"(ex[4].x), "v"(ex[4].y));
            f32x4 o[4], sum;
            const v2u ones2 = ones.lo;
            const u32 now = (u32)clk;
            u32 oz;
            SCHED();
            MF0(sum, ones, px); { const u32 q = ksrc ^ 8192u; DMA32(dq, kvo, q); } SCHED();
            MF0(o[0], vx[0], px);
            MF0(o[1], vx[1], px);
            MF0(o[2], tv[0], px); oz = (oso & ~(3u << 17)) | (u32)((s + 3) & 3) << 17; PIN(oz); SCHED();
            MF0(o[3], tv[1], px); oso = (oso & ~(3u << 17)) | (u32)((s + 1) & 3) << 17; PIN(oso); SCHED();
            MF1H(sum, ones2, p0);
            MF1H(o[0], vm, p0);
            MF1H(o[1], vm, p1); st16(rs, oz, pv); SCHED();
            MF1H(o[2], tvm, p0);
            MF1H(o[3], tvm, p1);
            asm volatile("s_nop 2\n\tv_rcp_f32 %0, %1\n\ts_nop 0" : "=v"(inv) : "v"(sum[0]), "v"(sum));
            asm volatile("" : "+v"(o[0]), "+v"(o[1]));
            st16(rs, oo, (v4u){pk2(o[0].lo * inv), pk2(o[0].hi * inv), pk2(o[1].lo * inv), pk2(o[1].hi * inv)});
            asm volatile("" : "+v"(o[2]), "+v"(o[3]));
            st16(rs, oo ^ 2048u, (v4u){pk2(o[2].lo * inv), pk2(o[2].hi * inv), pk2(o[3].lo * inv), pk2(o[3].hi * inv)});
            SCHED();
            LS2(L == 0 && w == 0, h);
            st4s(rs, ((h & 15) == 0 && tid == 192) ? hint + 128u + (u32)h * 8u : OOB, 0, (u32)s << 16 | min(missed, 1u) << 14 | min(now - tm, 0x3FFFu));
            tm = rdl(fl, 5);
            missed = missed == SPIN_CAP ? missed : 0u;
            if (__builtin_expect((int)(rdl(fl, 3) - now) < CH_LATE || s < 1 - L, 0)) {
                const bool first = s == 0 && L == 0;
                LAD(w == 0 && s > 0, 3, L * 64 + s);
                if (s < 0) oso -= (ln & 256u) ? 0u : OOB;
#pragma unroll 1
                for (int n = 0; n < (s < 0 ? 0 : first ? 200 : 8); ++n) {
                    CFENCE();
                    const u32 hv = ld4(rs, fkh);
                    tm = rdl(hv, first ? 6 : 5);
                    if (first ? tm != PSENT : (int)(rdl(hv, 3) - now) >= CH_LATE) break;
                }
            }
        }
        if (__builtin_expect(!kvok, 0) && s + 1 < NSEG) {
            st4s(rs, ln == 0 ? hint + 4096u + (u32)h * 128u : OOB, 0, (u32)s + 1u);
            LAD(true, 0, L * 64 + s + 1);
            keys_wait((u32)s + 2u);
        }
        ++s;
    }
    DRAIN(); BAR();
    st4s(rs, ofl, 0, NSEG);
}

#define WO_NL 3
#define WO_ROT 0
#define WO_AH 1
DEV void role_wo(const Params& p, lptr lds, int j, v2u ep) {
    const int tid = threadIdx.x, lane = tid & 63, w = uni(tid >> 6), g = lane >> 4, r = lane & 15;
    const int rg = j & 1, cg = j >> 1, rot = WO_ROT;
    const rsrc_t rs = mk_rsrc(p.ws, W_END);
    v8u wo[8][5];
    {
        const v4u so_ = mk_srd(p.Wo, DM * NQ * 2);
        u32 vo[5];
#pragma unroll
        for (int i = 0; i < 5; ++i) { const int n = 64 * i + lane; vo[i] = (u32)((n / 5) * DM + cg * 40 + (n % 5) * 8) * 2u; }
        v4u junk = {};
        const u32 av = (u32)(j * 5120 + lane * 16);
        auto ahead = [&](int b) {
#pragma unroll
            for (int i = 0; i < 5; ++i) AHEAD(so_, av, uu((8 * w + b) * (DH * DM * 2) + i * 1024));
        };
        auto fill = [&](int b) {
            if (b + WO_AH < 8) ahead(b + WO_AH);
            const u32 dst = uu(w * 40960 + (b & 3) * 10240), src = uu((8 * w + ((b + rot) & 7)) * (DH * DM * 2));
#pragma unroll
            for (int i = 0; i < 10; ++i) DMAIN(so_, dst + i * 1024, vo[i % 5], src + (i / 5) * (64 * DM * 2));
        };
#pragma unroll 1
        for (int b = 0; b < WO_AH; ++b) ahead(b);
#pragma unroll 1
        for (int b = 0; b < 4; ++b) fill(b);
        const int q4 = r >> 2;
        lptr tb = lds + w * 40960 + (32 * (g >> 1) + 16 * (q4 >> 1) + 8 * ((r >> 1) & 1) + 4 * (g & 1) + (q4 & 1)) * 80 + (r & 1) * 8;
        SFOR(b, 8)
            VMW((b < 5 ? 30 : 70 - 10 * b) + (b + 1 + WO_AH < 8 ? 5 : 0) + (b + 2 + WO_AH < 8 ? 5 : 0) + (b + 3 + WO_AH < 8 ? 5 : 0));
            if constexpr (b + WO_AH == 7) PIN(junk);
#pragma unroll
            for (int t = 0; t < 5; ++t) {
                lptr q = tb + (b & 3) * 10240 + t * 16;
                wo[b][t] = tr_frag(q, q + 160, q + 64 * 80, q + 64 * 80 + 160);
                if (b * 5 + t < 32) asm volatile("" : "+a"(wo[b][t])); else asm volatile("" : "+v"(wo[b][t]));
            }
            CFENCE();
            if constexpr (b < 4) fill(b + 4);
        SEND
    }
    if (j == 0) LST(265, rt32());
    LST(1100 + j * 4 + w, rt32());
    const u32 idx = (r & 2) ? 0xEEEEu : 0x4444u;
    const u32 pol0 = pol_wait(rs, ep);
    BAR();
    const u32 l16 = (u32)lane * 16u;
    u32 oo[8];
#pragma unroll
    for (int i = 0; i < 8; ++i) oo[i] = uu(W_ORING + ring_map((u32)(rg * 32768 + (8 * w + ((i + rot) & 7)) * 1024), pol0));
    const u32 hint = W_MRING + 4 * 32768u + pol0 * 8192u;
    u32 hvo[2];
#pragma unroll
    for (int l = 0; l < 2; ++l) hvo[l] = lane < 2 ? hint + (u32)(l ^ 1) * (MB / 4) + (u32)lane * 4u : lane == 2 ? hint + (u32)l * (MB / 4) + 128u : lane == 3 ? foff(F_ERR) : lane == 4 ? hint + (u32)l * (MB / 4) + 256u :
                 (lane >= 32 && j == 0 && w == 3) ? hint + (u32)l * (MB / 4) + (u32)lane * 128u : OOB;
    lptr xw = r < 8 ? lds + w * 640 + (2 * (g & 1) + (g >> 1)) * 32 + r * 4 : lds + 8192 + lane * 4;
    const int G = ((5 * cg) >> 1) + w, tt = 2 * G + ((lane >> 3) & 1) - 5 * cg;
    const bool pub = w < 3 && lane < 32 && tt >= 0 && tt < 5;
    lptr xr = lds + (pub ? tt * 128 + (2 * (lane >> 4) + ((lane >> 2) & 1)) * 32 + (lane & 3) * 8 : 0);
    const u32 mpo = pub ? W_MRING + ring_map((u32)(G * 256 + rg * 128 + lane * 4), pol0) : OOB;
    constexpr u32 PER0 = CH_W0 + CH_MW + CH_H0 + CH_MH + 2 * CH_VIS, DW0 = CH_W0 + CH_VIS + CH_MW + CH_REC;
    const bool lead = j == 0 && w == 3;
    const u32 hso = (lead && lane == 0) ? hint : OOB;
    MK_SRD();
    u32 t = 0, tn = 0;
    int sn[2] = {0, 0};
    u32 hav = 8 * CH_H0, wav = 8 * CH_W0, mh = 8 * CH_MH, mw = 8 * CH_MW, ok[2] = {0u, 0u}, dw[2] = {DW0, DW0};
    u32 vo = l16, early = PSENT, tp = 0, fin, left, cnt = 0;
    unsigned long long clk = 0;
    {
        if (w == 0) LST(1310 + j, rt32());
        if (lead) {
#pragma unroll 1
            for (int n = 0; n < SPIN_CAP; ++n) {
                CFENCE();
                const u32 a = ld4(rs, foff(F_QKV + lane)), b = ld4(rs, lane < 32 ? foff(F_QKV + 64 + lane) : hvo[0]);
                if (all_(a != 0u && b != (lane < 32 ? 0u : PSENT))) break;
                __builtin_amdgcn_s_sleep(1);
            }
        }
        if (lead) {
            t = rt32() + CH_T0; tn = t + PER0 / 2 + CH_REC;
            st16(rs, lane < 2 ? hint + (u32)lane * (MB / 4) : OOB, (v4u){lane == 0 ? t : tn, PER0, (lane == 0 ? t : tn) + DW0, t + DW0});
        } else {
#pragma unroll 1
            for (int n = 0; n < SPIN_CAP; ++n) {
                CFENCE();
                const u32 hv = ld4(rs, hvo[1]);
                t = rdl(hv, 0);
                if (rdl(hv, 1) < 400u) break;
                __builtin_amdgcn_s_sleep(1);
            }
        }
        clk = t - 100u;
    }
    const u32 tm0 = t + DW0;
    u32 zero = 0, hv = 0xFFFFu, pst = 1, pmp = OOB;
    const u32 psent = PSENT;
    lptr sw = lds + 2560 + w * 4;
    asm volatile("" : "+v"(zero), "+v"(sw));
    auto turn = [&](auto lc) __attribute__((always_inline)) {
        constexpr int L = decltype(lc)::value, M = L ^ 1;
        u32 done, rep, rd, lost, dh, dn, nt, za, pdur, mp; v4u hw;
        const int sc = sn[L];
        auto books = [&](auto ic) __attribute__((always_inline)) {
            constexpr int i = decltype(ic)::value;
            if constexpr (i == 0) { rep = max(rdl(hv, 2), rdl(hv, 4)); PINS(rep); }
            if constexpr (i == 1) { lost = (u32)(__builtin_amdgcn_ballot_w64(hv == (u32)sn[M]) >> 32); PINS(lost); }
            if constexpr (i == 2) { lost |= (rep >> 16) ^ (u32)sn[M]; PINS(lost); }
            if constexpr (i == 3) { lost |= ok[M] ^ 2u; rd = rep & 0xFFFFu; PINS(lost); PINS(rd); }
            if constexpr (i == 4) { lost = lost == 0u ? CH_UP * 8u : 0u; PINS(lost); }
            if constexpr (i == 5) { pdur = ((u32)clk - tp) * 8u; PINS(pdur); pdur = min(pdur, wav + CH_RISE); PINS(pdur); }
            if constexpr (i == 6) { pdur = (done & ok[M]) ? pdur : wav; PINS(pdur); }
            if constexpr (i == 7) { wav = max(pdur, wav - CH_FALL); PINS(wav); }
            if constexpr (i == 8) { nt = min(rd * 8u, hav + CH_RISE); PINS(nt); }
            if constexpr (i == 9) { nt = rd < 0x3FFFu ? nt : hav; PINS(nt); }
            if constexpr (i == 10) { hav = max(nt, hav - CH_FALL); PINS(hav); }
            if constexpr (i == 11) { mw = max(mw - 1u, CH_MW * 8u); PINS(mw); }
            if constexpr (i == 12) { mh = max(mh - 1u, CH_MH * 8u); PINS(mh); }
            if constexpr (i == 13) { rd = rd >> 14 == 1u ? lost : 0u; PINS(rd); }
            if constexpr (i == 14) { mw += rd; mh += lost - rd; PINS(mw); PINS(mh); }
            if constexpr (i == 15) { ok[M] = done ? 1u : (ok[M] & 1u) << 1; PINS(ok[M]); }
            if constexpr (i == 16) { dh = (hav + mh) / 8u + (sn[1] < 3 ? CH_VIS + CH_PRE : CH_VIS); PINS(dh); }
            if constexpr (i == 17) { dn = (wav + mw) / 8u + CH_VIS; PINS(dn); }
            if constexpr (i == 18) { nt = dw[L] + dh + (u32)max((int)((u32)clk + CH_WX - t), 0); PINS(nt); }
            if constexpr (i == 19) { dh += dn; dw[L] = dn; sn[M] += (int)done; PINS(dh); PINS(sn[M]); }
            if constexpr (i == 20) { rd = tn - t + (dh >> 1); PINS(rd); }
            if constexpr (i == 22) { nt = t + (u32)max((int)nt, (int)rd); PINS(nt); }
            if constexpr (i == 23) { hw.x = nt; hw.y = dh; PIN(hw.x); PIN(hw.y); }
            if constexpr (i == 24) { hw.z = nt + dn; hw.w = tm0; PIN(hw); }
            if constexpr (i == 25) st16(rs, hso + L * (MB / 4), hw);
        };
        {
#define WT(k) do {} while (0)
            WT(0);
            v4u d[8];
            {
                u32 tmp;
                asm volatile(WAIT_TXT("%1", "%2") "buffer_load_dwordx4 %0, %3, %4, %5 offen sc1\n\ts_waitcnt lgkmcnt(0)"
                             : "=&v"(d[0]), "=&s"(tmp) : "s"(uu(t - 4u)), "v"(vo), "s"(srd), "s"(oo[0]) : "vcc", "scc", "memory");
                PINS(clk);
            }
            SFOR(i, 7)
                asm volatile("buffer_load_dwordx4 %0, %1, %2, %3 offen sc1" : "=v"(d[i + 1]) : "v"(vo), "s"(srd), "s"(oo[i + 1]) : "memory");
                SCHED();
                if constexpr (i == 0) { done = uu(pst) ^ 1u; PINS(done); }
                static_for<3 * i, 3 * i + 3>(books);
                SCHED();
            SEND
            LD4A(hv, hvo[L], 0);
            SCHED();
            WT(1);
            static_for<22, 26>(books);
            za = done ? pmp : OOB;
            st4s(rs, za, 0, psent);
            *(LAS u32*)(sw + L * 4096) = zero;
            f32x4 acc[5] = {};
            mp = mpo + (L * (MB / 4) + (u32)((sc + 1) & 3) * 32768u); pmp = mpo + (L * (MB / 4) + (u32)(sc & 3) * 32768u);
            asm volatile("" : "+v"(acc[0]), "+v"(acc[1]), "+v"(acc[2]), "+v"(acc[3]), "+v"(acc[4]), "+v"(mp), "+v"(pmp));
            SCHED();
            float f0[5], f1[5]; unsigned long long bad;
#define WSW(o_) asm volatile("v_permlane32_swap_b32 %0, %1" : "+v"(f0[o_]), "+v"(f1[o_]))
#define WAD(o_) asm volatile("v_add_f32_dpp %0, %1, %0 row_ror:8 row_mask:0xf bank_mask:0xf" : "+v"(f0[o_]) : "v"(f1[o_]))
#define WFO(o_) do { VADD(f0[o_], acc[o_][0], acc[o_][2]); VADD(f1[o_], acc[o_][1], acc[o_][3]); } while (0)
#define WWR(o_) *(LAS float*)(xw + L * 4096 + (o_) * 128) = f0[o_]
            u32 th, per, so, erl = early;
            auto next = [&](auto ic_) __attribute__((always_inline)) {
                constexpr int i = decltype(ic_)::value;
                if constexpr (i == 0) { so = (u32)sn[M] << 17; PINS(so); }
                if constexpr (i == 1) { so = (so & 0x60000u) + M * (MB / 2); PINS(so); }
                if constexpr (i == 2) { so = sn[M] + 1 < NSEG ? so : OOB; PINS(so); }
                if constexpr (i == 3) { early = sn[M] + 1 < NSEG ? PSENT : 0u; PIN(early); }
                if constexpr (i == 4) { vo = l16 + so; PIN(vo); }
                if constexpr (i == 5 && L) { fin = (u32)(sn[0] + sn[1] + 2); PINS(fin); }
                if constexpr (i == 6 && L) { fin = (fin | uu(left)) >> 7; PIN(fin); }
                if constexpr (i == 6 && !L) { cnt += 1u; PIN(cnt); left = cnt >> 9; PIN(left); }
                if constexpr (i == 7) { PIN(hv); th = rdl(hv, 0); PINS(th); }
                if constexpr (i == 8) { per = rdl(hv, 1); PINS(per); so = th - t; PINS(so); }
                if constexpr (i == 9) { per = (int)so < 20 ? per : 0u; PINS(per); }
                if constexpr (i == 10) { th += per; PINS(th); if constexpr (L) { per = rdl(hv, 3); PINS(per); } }
                if constexpr (i == 11) { tp = t; t = lead ? tn : th; PINS(t); }
                if constexpr (i == 12) { if constexpr (L) { fin |= per; PIN(fin); } tn = nt; }
            };
            auto slot = [&](auto ic_) __attribute__((always_inline)) {
                constexpr int i = decltype(ic_)::value;
                constexpr int NL = WO_NL, I0 = 40 - 5 * NL, pc = i < I0 ? i / 5 : 8 - NL + (i - I0) % NL, oc = i < I0 ? i % 5 : (i - I0) / NL;
                if constexpr (oc == 0) asm volatile("s_waitcnt vmcnt(%1)" : "+v"(d[pc]) : "i"(10 - pc));
                if constexpr (pc * 5 + oc < 32) SMF_A(acc[oc], d[pc], wo[pc][oc]); else SMF_V(acc[oc], d[pc], wo[pc][oc]);
                SCHED();
                if constexpr (i == 0) WT(2);
                if constexpr (i >= 5 && i < 13 && i != 9) next(IC<i - 5 - (i > 9)>{});
                if constexpr (i == 20) WT(4);
                constexpr int e = i - (I0 + NL + 1), o = e < 0 ? 0 : e / 3;
                if constexpr (e >= 0 && e % 3 == 0) { CADD(f0[o], acc[o][0], acc[o][2]); if constexpr (o > 0) WAD(o - 1); }
                if constexpr (e >= 0 && e % 3 == 1) { if constexpr (o > 0) { WWR(o - 1); SCHED(); } CADD(f1[o], acc[o][1], acc[o][3]); }
                if constexpr (e >= 0 && e % 3 == 2) {
                    if constexpr (o == 0) asm volatile("v_cmp_u_f32 %0, %1, %2" : "=s"(bad) : "v"(f0[0]), "v"(f1[0]));
                    else LGK(15);
                    WSW(o);
                }
                SCHED();
            };
            asm volatile(".rept %0\n\ts_nop 0\n\t.endr" :: "i"(AL_W));
            static_for<0, 2>(slot);
            if (__builtin_expect(__builtin_amdgcn_ballot_w64(d[0].x == erl) != 0ull, 0)) {
                asm volatile("s_waitcnt vmcnt(3)" :: "v"(d[1]), "v"(d[2]), "v"(d[3]), "v"(d[4]), "v"(d[5]), "v"(d[6]), "v"(d[7]) : "memory");
                SCHED();
                *(LAS u32*)(sw + L * 4096) = 1u;
                static_for<0, 7>(next);
            } else {
            static_for<2, 40>(slot);
            WT(5);
            static_assert(WO_NL == 3, "tail");
            if (__builtin_expect(bad != 0ull, 0)) {
                bool miss = false;
#pragma unroll
                for (int i = 0; i < 8; ++i) miss |= d[i].x == PSENT;
                if (all_(!miss)) {
#pragma unroll
                    for (int i = 0; i < 8; ++i) miss |= d[i].y == PSENT || d[i].z == PSENT || d[i].w == PSENT;
                }
                *(LAS u32*)(sw + L * 4096) = all_(!miss) ? 0u : 1u;
            }
            asm volatile("v_permlane32_swap_b32 %0, %1\n\ts_nop 1\n\tv_add_f32_dpp %0, %1, %0 row_ror:8 row_mask:0xf bank_mask:0xf\n\tv_add_f32 %2, %4, %6\n\tv_add_f32 %3, %5, %7"
                         : "+v"(f0[3]), "+v"(f1[3]), "=&v"(f0[4]), "=&v"(f1[4]) : "v"(acc[4][0]), "v"(acc[4][1]), "v"(acc[4][2]), "v"(acc[4][3]));
            WWR(3); SCHED();
            asm volatile("s_nop 0\n\tv_permlane32_swap_b32 %0, %1\n\ts_nop 1\n\tv_add_f32_dpp %0, %1, %0 row_ror:8 row_mask:0xf bank_mask:0xf" : "+v"(f0[4]), "+v"(f1[4]));
            WWR(4);
            }
            WT(6);
            f32x2 a, b, c, e; u32 mq; v4u sv;
            asm volatile("s_waitcnt lgkmcnt(0)\n\ts_barrier\n\tds_read_b128 %4, %6 offset:%7+2560\n\t"
                         "ds_read_b64 %0, %5 offset:%7\n\tds_read_b64 %1, %5 offset:%7+640\n\tds_read_b64 %2, %5 offset:%7+1280\n\tds_read_b64 %3, %5 offset:%7+1920"
                         : "=&v"(a), "=&v"(b), "=&v"(c), "=&v"(e), "=&v"(sv) : "v"(xr), "v"(zero), "i"(L * 4096) : "memory");
            asm volatile("s_waitcnt lgkmcnt(4)\n\tv_or_b32 %4, %6, %7\n\tv_or3_b32 %4, %4, %8, %9\n\tv_lshl_or_b32 %5, %4, 31, %10\n\t"
                         "s_waitcnt lgkmcnt(2)\n\tv_pk_add_f32 %0, %0, %1\n\ts_waitcnt lgkmcnt(0)\n\tv_pk_add_f32 %2, %2, %3\n\ts_nop 0\n\tv_pk_add_f32 %0, %0, %2"
                         : "+v"(a), "+v"(b), "+v"(c), "+v"(e), "=&v"(pst), "=&v"(mq) : "v"(sv.x), "v"(sv.y), "v"(sv.z), "v"(sv.w), "v"(mp) : "memory");
            st4s(rs, mq, 0, pkv(a));
            SCHED();
            asm volatile("s_memrealtime %0" : "=s"(clk));
            VMW(3); SCHED();
            static_for<7, 13>(next);
            WT(7);
        }
    };
    do {
        turn(IC<0>{});
        turn(IC<1>{});
    } while (uu(fin) == 0u);
    if (sn[0] + 1 < NSEG || sn[1] + 1 < NSEG) st4s(rs, tid == 0 ? foff(F_ERR) : OOB, 0, 1u);
    if (w == 0) LST(1900 + j, rt32());
}

__global__ void __launch_bounds__(256, 1) k_main(Params p) {
    lptr lds = (lptr)(u32)16 - 16;
    if (blockIdx.x == 0) LST(256, rt32());
    unsigned long long pc; int len;
    asm volatile("s_getpc_b64 %0\n.Lk_main_here:\n\ts_mov_b32 %1, .Lk_end_here-.Lk_main_here" : "=s"(pc), "=s"(len));
    const rsrc_t rs = mk_rsrc(p.ws, W_END);
    const v2u e0 = ld8s(rs, threadIdx.x == 0 ? foff(F_EPOCH) : OOB, 0);
    const v4u code = __builtin_amdgcn_raw_buffer_load_b128(mk_rsrc((const void*)(pc & ~15ull), (u32)max(len, 0)), (((blockIdx.x >> 3) & 31u) * 256u + threadIdx.x) * 16u, 0, 0);
    CFENCE();
    auto conv_id = [](int role) {
        const int x = role >> 5, i = role & 31, no = x < 5 ? 5 : 10, og = x < 5 ? x - 3 : 2 * (x - 4) + (i >= 5), r = (x - 3) * 5 + i - no, q = r / 3;
        return x < 3 || i >= no + 5 || r >= 24 ? NCV : i < no ? ((og & 1) ? (og >> 1) * 5 + i % 5 : NCV) : r % 3 == 0 ? 20 + (q & 1) * 20 + (q >> 1) : 16 + (r % 3) * 8 + q;
    };
    int role = (int)(((u32)__builtin_amdgcn_s_getreg(20 | (3 << 11)) & 7u) * 32u + (blockIdx.x >> 3));
    Early e;
    v4u rv;
#pragma unroll 1
    for (int n = 0;; ++n) {
        conv_early(p, rs, conv_id(role), e);
        if (n) break;
        CFENCE();
        if (threadIdx.x == 0) {
            GAS unsigned long long* slots = (GAS unsigned long long*)(p.ws + W_CTRL + F_SLOT * 128);
            const unsigned long long tag = (unsigned long long)e0.y << 32 | (e0.x + 1u);
            u32 mine = (u32)role;
#pragma unroll 1
            for (int k = 0; k < 256 && __hip_atomic_exchange(slots + mine * 16, tag, __ATOMIC_RELAXED, __HIP_MEMORY_SCOPE_AGENT) == tag; ++k) mine = (mine + 1u) & 255u;
            lds_w16(lds, (v4u){mine, e0.x, e0.y, 0u});
        }
        BAR();
        rv = lds_r16(lds);
        BAR();
        if (uni((int)rv.x) == role) break;
        role = uni((int)rv.x);
    }
    const int x = role >> 5, i = role & 31, w = uni(threadIdx.x >> 6), no = x < 5 ? 5 : 10, nr = x < 7 ? 5 : 4, j = conv_id(role);
    const v2u ep = {uu(rv.y), uu(rv.z)};
    asm volatile("" :: "v"(code));
    if (x < 2) { role_head(p, lds, i >> 4, x * 16 + (i & 15), ep); __builtin_amdgcn_endpgm(); }
    if (x == 2) { role_wo(p, lds, i, ep); __builtin_amdgcn_endpgm(); }
    if (i >= no + nr) { role_qkv(p, lds, (x < 5 ? (x - 3) * 22 : 44 + (x - 5) * 17) + i - no - nr, ep); __builtin_amdgcn_endpgm(); }
    const bool cv = j >= NPRE || (j >= 20 && j < 24);
    if (j < NCV) conv_stage(p, rs, lds, j, (j & 3) * 2 + (j >= NPRE), cv ? 40 : 0, ep, e);
    if (i >= no) {
        if (!cv) role_row(p, lds, (j - 24) * 4 + w, ep);
        __builtin_amdgcn_endpgm();
    }
    const int og = x < 5 ? x - 3 : 2 * (x - 4) + (i >= 5);
    role_out(p, lds, og & 1, og >> 1, i % 5, ep);
}
__global__ void k_end() { asm volatile(".Lk_end_here:"); }

extern "C" void kernel_launch(void* const* d_in, const int* in_sizes, int n_in, void* d_out, int out_size, void* d_ws, size_t ws_size,
                              hipStream_t stream) {
    Params p;
    p.x = (const u16*)d_in[0]; p.mem0 = (const u16*)d_in[1]; p.Wq = (const u16*)d_in[2]; p.Wk = (const u16*)d_in[3];
    p.Wv = (const u16*)d_in[4]; p.Wo = (const u16*)d_in[5];
    p.bk = (const u16*)d_in[8]; p.bv = (const u16*)d_in[9]; p.shift = (const u16*)d_in[10]; p.ssmk = (const u16*)d_in[11]; p.ssmD = (const u16*)d_in[12];
    p.out = (u16*)d_out;
    p.ws = (unsigned char*)(((size_t)d_ws + 2 * MB - 1) & ~(size_t)(2 * MB - 1));
    (void)hipFuncSetAttribute((const void*)k_main, hipFuncAttributeMaxDynamicSharedMemorySize, LDS_BYTES);
    hipLaunchKernelGGL(k_main, dim3(256), dim3(256), LDS_BYTES, stream, p);
}
```

```cpp
#include <hip/hip_runtime.h>
#include <hip/hip_bf16.h>

typedef unsigned short u16;
typedef unsigned int u32;
typedef u32 v2u __attribute__((ext_vector_type(2)));
typedef u32 v4u __attribute__((ext_vector_type(4)));
typedef u32 v8u __attribute__((ext_vector_type(8)));
typedef float f32x2 __attribute__((ext_vector_type(2)));
typedef float f32x4 __attribute__((ext_vector_type(4)));
typedef float f32x16 __attribute__((ext_vector_type(16)));
typedef __bf16 bf16x2 __attribute__((ext_vector_type(2)));
typedef __bf16 bf16x8 __attribute__((ext_vector_type(8)));
typedef short s16x4 __attribute__((ext_vector_type(4)));
#define LAS __attribute__((address_space(3)))
#define GAS __attribute__((address_space(1)))
typedef LAS unsigned char* lptr;
#define DEV __device__ __forceinline__

constexpr int SEQ = 2048, DM = 640, NH = 32, DH = 128, NSEG = 64, NQ = NH * DH;

constexpr u32 MB = 1u << 20;
constexpr u32 W_ORING = 0;
constexpr u32 W_MRING = MB;
constexpr u32 W_PROBE = 2 * MB - 65536;
constexpr u32 W_CTRL = 4 * MB;
constexpr u32 CTRL_BYTES = 128 * 1024;
constexpr u32 ACT = SEQ * DM * 2;
constexpr u32 W_H1 = 5 * MB, W_Y = W_H1 + ACT, W_H2 = W_Y + ACT, W_X2 = W_H2 + ACT;
constexpr u32 W_KQ = 16 * MB;
constexpr u32 W_VO = W_KQ + 64 * MB;
constexpr u32 W_P = W_VO + 64 * MB;
constexpr u32 P_ITEM = 16 * 4 * DM * 4;
constexpr u32 W_KVT = W_P + 4 * NSEG * P_ITEM;
constexpr u32 W_SP = W_KVT + ACT;
constexpr u32 W_END = W_KVT + 3 * MB;
static_assert((size_t)W_END + 2 * MB <= 256ull * MB, "workspace");
constexpr u32 F_ERR = 0, F_QKV = 16, F_O = F_QKV + 192, F_P = F_O + 64, F_H2 = F_P + 64, F_CLR = F_H2 + 32,
              F_EPOCH = F_CLR + 32, F_SLOT = F_EPOCH + 1, F_PRE = F_SLOT + 256, F_N = F_PRE + 192;
constexpr int NCV = 44, NPRE = 40;
static_assert(F_N * 128 <= CTRL_BYTES, "ctrl");
DEV u32 foff(u32 idx) { return W_CTRL + idx * 128u; }

constexpr u32 OOB = 0x80000000u;
constexpr u32 PSENT = 0x7F817F81u;
constexpr int SPIN_CAP = 40000;
constexpr int LDS_BYTES = 160 * 1024;

#define LDUMP(cond, id) do {} while (0)
#define LMX(cond, k, s_, v) do {} while (0)
#define LS3(cond, slot, idx) do {} while (0)
#define LST(idx, v) do {} while (0)
#define LS2(cond, idx) do {} while (0)
struct Params {
    const u16 *x, *mem0, *Wq, *Wk, *Wv, *Wo, *bk, *bv, *shift, *ssmk, *ssmD;
    u16* out;
    unsigned char* ws;
};

typedef __amdgpu_buffer_rsrc_t rsrc_t;
DEV rsrc_t mk_rsrc(const void* p, u32 bytes) { return __builtin_amdgcn_make_buffer_rsrc((void*)p, 0, bytes, 0x00020000); }
DEV v4u ld16s(rsrc_t r, u32 off, u32 so) { return __builtin_amdgcn_raw_buffer_load_b128(r, off, so, 16); }
DEV v2u ld8s(rsrc_t r, u32 off, u32 so) { return __builtin_amdgcn_raw_buffer_load_b64(r, off, so, 16); }
DEV v4u ldin16(rsrc_t r, u32 off, u32 so) { return __builtin_amdgcn_raw_buffer_load_b128(r, off, so, 2); }
DEV v2u ldin8(rsrc_t r, u32 off, u32 so) { return __builtin_amdgcn_raw_buffer_load_b64(r, off, so, 2); }
DEV u32 ld4(rsrc_t r, u32 off) { return __builtin_amdgcn_raw_buffer_load_b32(r, off, 0, 16); }
DEV void st16(rsrc_t r, u32 off, v4u v) { __builtin_amdgcn_raw_buffer_store_b128(v, r, off, 0, 16); }
DEV void st8s(rsrc_t r, u32 off, u32 so, v2u v) { __builtin_amdgcn_raw_buffer_store_b64(v, r, off, so, 16); }
DEV void st4s(rsrc_t r, u32 off, u32 so, u32 v) { __builtin_amdgcn_raw_buffer_store_b32(v, r, off, so, 16); }
#define DRAIN() asm volatile("s_waitcnt vmcnt(0)" ::: "memory")
#define VMW(n) asm volatile("s_waitcnt vmcnt(%0)" :: "i"(n) : "memory")
#define BAR() asm volatile("s_waitcnt lgkmcnt(0)\n\ts_barrier" ::: "memory")
#define SCHED() __builtin_amdgcn_sched_barrier(0)
#define CFENCE() asm volatile("" ::: "memory")
#define NOPS(n) asm volatile("s_nop %0" :: "i"(n))
#define PIN(x) asm volatile("" : "+v"(x))
#define PINS(x) asm volatile("" : "+s"(x))

template <int V> struct IC { static constexpr int value = V; };
template <int I, int N, class F> DEV void static_for(F&& f) { if constexpr (I < N) { f(IC<I>{}); static_for<I + 1, N>(f); } }
#define SFOR(var, n) static_for<0, n>([&](auto ic_) __attribute__((always_inline)) { constexpr int var = decltype(ic_)::value;
#define SEND });

DEV u32 pk(float a, float b) { bf16x2 v = {(__bf16)a, (__bf16)b}; return __builtin_bit_cast(u32, v); }
DEV float lo_f(u32 u) { return __builtin_bit_cast(float, u << 16); }
DEV float hi_f(u32 u) { return __builtin_bit_cast(float, u & 0xffff0000u); }
DEV f32x2 up2(u32 u) { return (f32x2){lo_f(u), hi_f(u)}; }
DEV u32 pk2(f32x2 v) { return pk(v.x, v.y); }
DEV float rbf(float a) { return (float)(__bf16)a; }
DEV v4u lds_r16(lptr p) { return *(LAS v4u*)p; }
DEV void lds_w16(lptr p, v4u v) { *(LAS v4u*)p = v; }
DEV v2u lds_tr(lptr p) { return __builtin_bit_cast(v2u, __builtin_amdgcn_ds_read_tr16_b64_v4i16((LAS s16x4*)p)); }
DEV v4u tr2(lptr p0, lptr p1) { const v2u a = lds_tr(p0), b = lds_tr(p1); return (v4u){a.x, a.y, b.x, b.y}; }
DEV v8u cat8(v4u a, v4u b) { return __builtin_shufflevector(a, b, 0, 1, 2, 3, 4, 5, 6, 7); }
DEV f32x4 mfma16(v4u a, v4u b, f32x4 c) { return __builtin_amdgcn_mfma_f32_16x16x32_bf16(__builtin_bit_cast(bf16x8, a), __builtin_bit_cast(bf16x8, b), c, 0, 0, 0); }
DEV f32x16 mfma32(v4u a, v4u b, f32x16 c) { return __builtin_amdgcn_mfma_f32_32x32x16_bf16(__builtin_bit_cast(bf16x8, a), __builtin_bit_cast(bf16x8, b), c, 0, 0, 0); }

DEV float f_(u32 u) { return __builtin_bit_cast(float, u); }
DEV u32 u_(float f) { return __builtin_bit_cast(u32, f); }
template <int CTRL> DEV float dpp(float v) { return f_((u32)__builtin_amdgcn_update_dpp(0, (int)u_(v), CTRL, 0xf, 0xf, false)); }
DEV float max16(float v) { auto r = __builtin_amdgcn_permlane16_swap(u_(v), u_(v), false, false); return fmaxf(f_(r[0]), f_(r[1])); }
DEV float max32(float v) { auto r = __builtin_amdgcn_permlane32_swap(u_(v), u_(v), false, false); return fmaxf(f_(r[0]), f_(r[1])); }
DEV float add16(float v) { auto r = __builtin_amdgcn_permlane16_swap(u_(v), u_(v), false, false); return f_(r[0]) + f_(r[1]); }
DEV float add32(float v) { auto r = __builtin_amdgcn_permlane32_swap(u_(v), u_(v), false, false); return f_(r[0]) + f_(r[1]); }
DEV float wave_sum(float v) {
    v += dpp<0xB1>(v);
    v += dpp<0x4E>(v);
    v += dpp<0x124>(v);
    v += dpp<0x128>(v);
    return add32(add16(v));
}
DEV bool all_(bool c) { return __builtin_amdgcn_ballot_w64(!c) == 0ull; }
DEV int uni(int v) { return __builtin_amdgcn_readfirstlane(v); }
DEV u32 uu(u32 v) { return (u32)__builtin_amdgcn_readfirstlane((int)v); }
DEV u32 rdl(u32 v, int l) { return (u32)__builtin_amdgcn_readlane((int)v, l); }
DEV u32 rt32() { return (u32)__builtin_amdgcn_s_memrealtime(); }

#define WSL 2
DEV void wait_flags(rsrc_t rs, u32 off, u32 want, bool& dead) {
    if (dead) return;
#pragma unroll 1
    for (int n = 0;; ++n) {
        CFENCE();
        const u32 v = ld4(rs, off);
        if (all_(off >= OOB || v >= want)) break;
        __builtin_amdgcn_s_sleep(WSL);
        if ((n & 63) == 63) { if (uni((int)ld4(rs, foff(F_ERR))) != 0) { dead = true; break; } }
        if (n > SPIN_CAP) { st4s(rs, foff(F_ERR), 0, 1u); dead = true; break; }
    }
    CFENCE();
}

DEV void pre_post(rsrc_t rs, int idx, u32 stage, v2u ep) { st8s(rs, threadIdx.x == 0 ? foff(F_PRE + idx) : OOB, 0, (v2u){ep.x + stage, ep.y}); }
DEV void pre_poll(rsrc_t rs, int first, int n, u32 stage, v2u ep) {
    const int lane = threadIdx.x & 63;
#pragma unroll 1
    for (int it = 0; it < SPIN_CAP; ++it) {
        CFENCE();
        const v2u a = ld8s(rs, foff(F_PRE + first + min(lane, n - 1)), 0), b = ld8s(rs, foff(F_PRE + first + min(lane + 64, n - 1)), 0);
        if (all_(a.y == ep.y && b.y == ep.y && max(a.x - ep.x - stage, b.x - ep.x - stage) < 8u - stage)) break;
        __builtin_amdgcn_s_sleep(2);
    }
    CFENCE();
}
DEV void pre_wait(rsrc_t rs, int first, int n, u32 stage, v2u ep) {
    if (uni(threadIdx.x >> 6) == 0) pre_poll(rs, first, n, stage, ep);
    BAR();
}
DEV u32 pol_wait(rsrc_t rs, v2u ep) {
    const int lane = threadIdx.x & 63;
    v4u a;
#pragma unroll 1
    for (int it = 0; it < SPIN_CAP; ++it) {
        CFENCE();
        a = ld16s(rs, foff(F_PRE + (lane < NPRE || lane >= 48 ? lane : 0)), 0);
        if (all_(a.y == ep.y && a.x - ep.x - 1u < 7u)) break;
        __builtin_amdgcn_s_sleep(2);
    }
    CFENCE();
    return uu(wave_sum(lane >= 48 ? (float)(int)a.z : 0.f) >= 0.f ? 1u : 0u);
}
#define PROBE_T 1000
#define NPROBE 32
DEV u32 pol_probe(const Params& p, rsrc_t rs, int id) {
    const int lane = threadIdx.x & 63;
    const u32 off0 = W_PROBE + (u32)(id >> 2) * 16384u + (u32)(id & 3) * 128u;
    const u32 tend = rt32() + PROBE_T;
    st4s(rs, lane < 32 ? off0 + (u32)lane * 512 : OOB, 0, 0u); DRAIN();
    int calm = 0;
    int d[2] = {1 << 16, 1 << 16};
#pragma unroll 1
    for (int q = 0; q < NPROBE && ((q & 1) || (calm < 6 && (int)(rt32() - tend) < 0)); ++q) {
        const u32 half = (u32)(q ^ (q >> 1) ^ id) & 1u;
        const unsigned long long t0 = __builtin_amdgcn_s_memtime();
        const u32 v = ld4(rs, lane == 0 ? off0 + half * 8192 + (u32)(q >> 1) * 512 : OOB);
        asm volatile("s_waitcnt vmcnt(0)" :: "v"(v) : "memory");
        const int dt = (int)(__builtin_amdgcn_s_memtime() - t0);
        if (half) d[1] = min(d[1], dt); else d[0] = min(d[0], dt);
        calm += dt < 1100;
    }
    const u32 xcc = (u32)__builtin_amdgcn_s_getreg(20 | (3 << 11)) & 0xfu;
    return (u32)min(max((d[0] - d[1]) * ((xcc & 4u) ? -1 : 1), -250), 250);
}
DEV f32x2 ln_stats(const f32x2 (&x)[5]) {
    const float c = f_(uu(u_(x[0].x)));
    f32x2 d[5];
#pragma unroll
    for (int i = 0; i < 5; ++i) d[i] = x[i] - c;
    const f32x2 s2 = (d[0] + d[1]) + (d[2] + d[3]) + d[4], q2 = (d[0] * d[0] + d[1] * d[1]) + (d[2] * d[2] + d[3] * d[3] + d[4] * d[4]);
    float a = s2.x + s2.y, b = q2.x + q2.y, a2, b2;
#define LN_DPP(m) "v_add_f32_dpp %0, %0, %0 " m " row_mask:0xf bank_mask:0xf\n\tv_add_f32_dpp %1, %1, %1 " m " row_mask:0xf bank_mask:0xf\n\ts_nop 0\n\t"
    asm("s_nop 1\n\t" LN_DPP("quad_perm:[1,0,3,2]") LN_DPP("quad_perm:[2,3,0,1]") LN_DPP("row_ror:4")
        "v_add_f32_dpp %0, %0, %0 row_ror:8 row_mask:0xf bank_mask:0xf\n\tv_add_f32_dpp %1, %1, %1 row_ror:8 row_mask:0xf bank_mask:0xf\n\ts_nop 1\n\t"
        "v_permlane16_swap_b32 %0, %1\n\tv_add_f32 %0, %0, %1\n\tv_mov_b32 %1, %0\n\ts_nop 1\n\tv_permlane32_swap_b32 %0, %1\n\tv_add_f32 %0, %0, %1\n\ts_nop 0\n\t"
        "v_readlane_b32 %2, %0, 0\n\tv_readlane_b32 %3, %0, 16\n\ts_nop 1"
        : "+v"(a), "+v"(b), "=s"(a2), "=s"(b2));
    const float m = a2 * (1.0f / DM), var = fmaxf(b2 * (1.0f / DM) - m * m, 0.f);
    const float sd = rbf(__builtin_amdgcn_sqrtf(rbf(rbf(var) + 1e-5f)));
    return (f32x2){rbf(c + m), __builtin_amdgcn_rcpf(sd)};
}
DEV u32 pkv(f32x2 v) { u32 d; asm("v_cvt_pk_bf16_f32 %0, %1, %2" : "=v"(d) : "v"(v.x), "v"(v.y)); return d; }
DEV void store_h(rsrc_t rs, u32 base, int row, const u32 (&hp)[5]) {
    const u32 lane = threadIdx.x & 63;
    const u32 sw = (u32)(row & 15) * 16u, o8 = (lane >> 5) * 8192u + (((lane & 31u) * 8u) ^ sw), hb = base + (u32)(row >> 5) * (32 * DM * 2) + (u32)(row & 31) * 256u;
    st8s(rs, o8, hb, (v2u){hp[0], hp[1]});
    st8s(rs, o8, hb + 16384, (v2u){hp[2], hp[3]});
    st4s(rs, (lane * 4) ^ sw, hb + 32768, hp[4]);
}

#define DMA16(ldsb, vo, so) asm volatile("s_mov_b32 m0, %0\n\ts_nop 0\n\tbuffer_load_dwordx4 %1, %2, %3 offen sc1 lds" :: "s"(ldsb), "v"(vo), "s"(srd), "s"(so) : "memory")
#define DMA16I(ldsb, lo, vo, so, go) do { u32 so_; asm volatile("s_add_u32 m0, %1, %5\n\ts_add_u32 %0, %4, %6\n\tbuffer_load_dwordx4 %2, %3, %0 offen sc1 lds" \
                         : "=&s"(so_) : "s"(ldsb), "v"(vo), "s"(srd), "s"(so), "i"(lo), "i"(go) : "memory", "scc"); } while (0)
#define DMA32(ldsb, vo, so) asm volatile("s_mov_b32 m0, %0\n\ts_nop 0\n\tbuffer_load_dwordx4 %1, %2, %3 offen sc1 lds\n\tbuffer_load_dwordx4 %1, %2, %3 offen offset:1024 sc1 lds" \
                         :: "s"(ldsb), "v"(vo), "s"(srd), "s"(so) : "memory")
#define DMAIN(sr, ldsb, vo, so) asm volatile("s_mov_b32 m0, %0\n\ts_nop 0\n\tbuffer_load_dwordx4 %1, %2, %3 offen lds" :: "s"(ldsb), "v"(vo), "s"(sr), "s"(so) : "memory")
DEV v4u mk_srd(const void* q, u32 bytes) { return (v4u){uu((u32)(size_t)q), uu((u32)((size_t)q >> 32) & 0xffffu), bytes, 0x00020000u}; }
#define DMANT(sr, ldsb, vo, so) asm volatile("s_mov_b32 m0, %0\n\ts_nop 0\n\tbuffer_load_dwordx4 %1, %2, %3 offen nt lds" :: "s"(ldsb), "v"(vo), "s"(sr), "s"(so) : "memory")
#define AHEAD(sr, vo, so) asm volatile("buffer_load_dwordx4 %0, %1, %2, %3 offen" : "+v"(junk) : "v"(vo), "s"(sr), "s"(so) : "memory")
#define MK_SRD() const v4u srd = mk_srd(p.ws, W_END)

constexpr int CV_W = 40960;
template <bool B> struct CvL {
    static constexpr int NF = B ? 5 : 3, NK = B ? 4 : 2, KP = 544 + NK * 256, CH = NF * 1024 + 4 * KP;
};
constexpr int CV_Y = 4 * CvL<false>::CH, CV_YP = 272;
static_assert(CV_Y + 32 * CV_YP <= CV_W && 2 * CvL<true>::CH <= CV_Y, "conv lds");
DEV int cv_chan(int u, int i) { return 16 * (u % 40) + 4 * i + u / 40; }
template <bool B> DEV void cv_filt(const Params& p, u32 dst, int d, int i) {
    const u32 lane = threadIdx.x & 63;
    const v4u sf = mk_srd(p.ssmk + d * SEQ, SEQ * 2);
    if (i == 0) DMANT(sf, dst, lane < 4u ? OOB : lane * 16u - 64u, 0);
    else DMANT(sf, uu(dst + i * 1024), i == CvL<B>::NF - 1 && lane >= 4u ? OOB : lane * 16u, i * 1024 - 64);
}
DEV v4u cv_kvld(rsrc_t rs, int d, int i) { return ld16s(rs, (threadIdx.x & 63) * 16u, uu(W_KVT + (u32)d * (SEQ * 2) + i * 1024)); }
template <bool B> DEV void cv_kvput(lptr F, const v4u* k) {
    const int lane = threadIdx.x & 63;
#pragma unroll
    for (int i = 0; i < CvL<B>::NK; ++i) lds_w16(F + CvL<B>::NF * 1024 + (lane & 3) * CvL<B>::KP + 512 + i * 256 + (lane >> 2) * 16, k[i]);
}
template <bool B> DEV void cv_zero(lptr F) {
    const int lane = threadIdx.x & 63;
#pragma unroll
    for (int i = 0; i < 4; ++i) lds_w16(F + (lane >> 5) * CvL<B>::CH + CvL<B>::NF * 1024 + i * CvL<B>::KP + (lane & 31) * 16, (v4u){0u, 0u, 0u, 0u});
}
#define M32V(d, a, b) asm volatile("v_mfma_f32_32x32x16_bf16 %0, %1, %2, %0" : "+v"(d) : "v"(a), "v"(b))
template <bool B, class G> DEV void cv_run(const Params& p, rsrc_t rs, lptr F, int d, u32 D, f32x4 sp, float (&y)[16], G&& gap) {
    constexpr int KP = CvL<B>::KP, FO = B ? 2048 : 0;
    const int lane = threadIdx.x & 63, n = lane & 31, hh = lane >> 5, c = lane & 3;
    lptr fa = F + 64 + (32 * hh + 16 * (c & 1) + 8 * ((lane >> 4) & 1) + 4 * (c >> 1) - (lane & 12)) * 2;
    lptr kl = F + CvL<B>::NF * 1024 + (32 + n - hh) * 16;
    v4u dg;
#pragma unroll
    for (int q = 0; q < 4; ++q) { const int k = (lane & 35) ? -1 : (n & 4) + (n >> 3); dg[q] = k == 2 * q ? D : k == 2 * q + 1 ? D << 16 : 0u; }
    v4u l[4], h[4]; v2u fc[4], fh[4];
    f32x16 acc[4];
#pragma unroll
    for (int j = 0; j < 4; ++j) { l[j] = lds_r16(kl + j * KP); if constexpr (B) h[j] = lds_r16(kl + j * KP + 512); }
#pragma unroll
    for (int j = 0; j < 2; ++j) { fc[j] = lds_tr(fa + FO - 32 * j); if constexpr (B) fh[j] = lds_tr(fa - 32 * j); }
    asm volatile("s_nop 1" : "+v"(dg));
#pragma unroll
    for (int j = 0; j < 4; ++j) asm volatile("v_mfma_f32_32x32x16_bf16 %0, %1, %2, 0" : "=&v"(acc[j]) : "v"(dg), "v"(B ? h[j] : l[j]));
    constexpr int U = B ? 2 : 4;
    kl -= 32 * U; fa -= 32;
    auto body = [&](bool first) __attribute__((always_inline)) {
#pragma unroll
        for (int z = 0; z < U; ++z) {
            const int c = 2 * (z & 1);
            const v4u f = __builtin_shufflevector(fc[c], fc[c + 1], 0, 1, 2, 3), g = __builtin_shufflevector(fh[c], fh[c + 1], 0, 1, 2, 3);
#pragma unroll
            for (int j = 0; j < 4; ++j) {
                M32V(acc[j], f, l[j]); l[j] = lds_r16(kl + j * KP + 32 * (U - z - 1));
                if (j < 2) fc[(c ^ 2) + j] = lds_tr(fa + FO + (z + 1) * 128 + 32 - 32 * j);
                if (first && j > 0) gap(5 * z + j - 1);
                SCHED();
            }
            if constexpr (B) {
#pragma unroll
                for (int j = 0; j < 4; ++j) {
                    M32V(acc[j], g, h[j]); h[j] = lds_r16(kl + j * KP + 512 + 32 * (U - z - 1));
                    if (j < 2) fh[(c ^ 2) + j] = lds_tr(fa + (z + 1) * 128 + 32 - 32 * j);
                    if (first && j > 1) gap(5 * z + j + 1);
                    SCHED();
                }
            }
        }
        kl -= 32 * U; fa += 128 * U;
    };
    if constexpr (B) body(true);
#pragma unroll 1
    for (int m = B ? 2 * U : 0; m < 32; m += 2 * U) body(false);
    asm volatile("s_nop 11" : "+v"(acc[0]), "+v"(acc[1]), "+v"(acc[2]), "+v"(acc[3]));
    const float o[3] = {acc[1][15] + acc[2][14] + acc[3][13], acc[2][15] + acc[3][14], acc[3][15]};
    if constexpr (!B) st16(rs, lane == 63 ? W_SP + (u32)d * 16u : OOB, (v4u){u_(o[0]), u_(o[1]), u_(o[2]), 0u});
#pragma unroll
    for (int i = 0; i < 16; ++i) {
        float v = acc[0][i];
#pragma unroll
        for (int j = 1; j < 4; ++j) if (j <= i) v += acc[j][i - j];
        if (i < 3) { const float e = f_((u32)__builtin_amdgcn_ds_bpermute((hh ? lane - 32 : lane + 31) * 4, (int)u_(o[i]))); v += lane == 0 ? sp[i] : e; }
        y[i] = v;
    }
}
DEV void cv_put(lptr F, int pr, const float (&y0)[16], const float (&y1)[16]) {
    const int lane = threadIdx.x & 63;
#pragma unroll
    for (int i = 0; i < 16; ++i) *(LAS u32*)(F + CV_Y + (lane & 31) * CV_YP + (lane >> 5) * 128 + i * 8 + pr * 4) = pk(y0[i], y1[i]);
}
DEV void cv_out(rsrc_t rs, lptr F, int u, int blk0) {
    const int lane = threadIdx.x & 63;
    lptr q = F + CV_Y + ((lane >> 2) & 3) * CV_YP + ((lane >> 4) * 4 + (lane & 3)) * 8;
    const u32 yo = W_Y + (u32)(((lane >> 2) * 8 + blk0) * DM + 4 * u + (lane & 3)) * 16u;
#pragma unroll
    for (int i = 0; i < 8; ++i) { lptr a = q + (i >> 1) * (8 * CV_YP) + (i & 1) * 128; st16(rs, yo + (u32)((i & 1) * 128 + (i >> 1)) * (DM * 16), tr2(a, a + 4 * CV_YP)); }
}
struct KvIn { v2u k[19], v[16]; v4u s[2]; };
template <class F> DEV void kvt_load(const Params& p, int tile, KvIn& in, F&& mid) {
    const int lane = threadIdx.x & 63;
    const rsrc_t rk = mk_rsrc(p.bk, ACT), rv = mk_rsrc(p.bv, ACT), rh = mk_rsrc(p.shift, DM * 8);
    const int row = (tile / 10) * 64 + (lane >> 4) * 16, col = (tile % 10) * 64 + (lane & 15) * 4;
    const u32 vo = (u32)(row * DM + col) * 2u, vp = row > 0 ? vo - 3 * (DM * 2) : OOB;
#pragma unroll
    for (int i = 0; i < 2; ++i) in.s[i] = ldin16(rh, (u32)col * 8u + i * 16, 0);
#pragma unroll
    for (int i = 0; i < 19; ++i) { if (i == 7) mid(); in.k[i] = i < 3 ? ldin8(rk, vp, i * (DM * 2)) : ldin8(rk, vo, (i - 3) * (DM * 2)); if (i >= 3) in.v[i - 3] = ldin8(rv, vo, (i - 3) * (DM * 2)); }
}
DEV void kvt_make(rsrc_t rs, lptr xt, int tile, const KvIn& in) {
    const int lane = threadIdx.x & 63;
#pragma unroll
    for (int c = 0; c < 2; ++c) {
        const v4u sv = in.s[c];
        const f32x2 sh[4] = {{lo_f(sv.x), lo_f(sv.z)}, {hi_f(sv.x), hi_f(sv.z)}, {lo_f(sv.y), lo_f(sv.w)}, {hi_f(sv.y), hi_f(sv.w)}};
        f32x2 kf[19];
#pragma unroll
        for (int i = 0; i < 19; ++i) kf[i] = up2(in.k[i][c]);
        u32 kv[16];
#pragma unroll
        for (int i = 0; i < 16; ++i) kv[i] = pk2(((kf[i] * sh[3] + kf[i + 1] * sh[2]) + (kf[i + 2] * sh[1] + kf[i + 3] * sh[0])) * up2(in.v[i][c]));
#pragma unroll
        for (int q = 0; q < 8; ++q) {
            const u32 sel = (q & 1) ? 0x07060302u : 0x05040100u; const u32* z = kv + (q >> 1);
            *(LAS v2u*)(xt + ((2 * c + (q & 1)) * 16 + (lane & 15)) * 144 + (lane >> 5) * 64 + (q >> 1) * 16 + ((lane >> 4) & 1) * 8) =
                (v2u){__builtin_amdgcn_perm(z[4], z[0], sel), __builtin_amdgcn_perm(z[12], z[8], sel)};
        }
    }
    const u32 o = W_KVT + (u32)(((tile % 10) * 64 + 4 * (lane >> 3)) * SEQ + (tile / 10) * 64 + (lane & 7) * 8) * 2u;
#pragma unroll
    for (int i = 0; i < 8; ++i) st16(rs, o + (u32)(32 * (i & 1) + (i >> 1)) * (SEQ * 2), lds_r16(xt + (8 * i + (lane >> 3)) * 144 + (lane & 7) * 16));
}
struct RowX { v2u a, b; u32 c; };
DEV void ln_row(rsrc_t rs, int row, const RowX& r) {
    const f32x2 x[5] = {up2(r.a.x), up2(r.a.y), up2(r.b.x), up2(r.b.y), up2(r.c)};
    const f32x2 st = ln_stats(x);
    u32 hp[5];
#pragma unroll
    for (int q = 0; q < 5; ++q) hp[q] = pk2(up2(pkv(x[q] - st.x)) * st.y);
    store_h(rs, W_H1, row, hp);
}
DEV v4u mem0_load(const Params& p, int j) {
    const u32 gt = (u32)(j * 256 + threadIdx.x) * 16u, n = ((gt >> 14) << 9) + ((gt & 8191u) >> 4);
    return ldin16(mk_rsrc(p.mem0, 8 * DM * 2), n < 640u ? ((2 * ((n >> 2) & 3) + (n & 1)) * DM + (n >> 4) * 16 + (n & 2) * 4) * 2u : OOB, 0);
}
DEV void pre_rings(rsrc_t rs, int j, v4u m) {
    const u32 gi = (u32)(j * 256 + threadIdx.x), gt = gi * 16u, R = NPRE * 4096, n = ((gt >> 14) << 9) + ((gt & 8191u) >> 4);
#pragma unroll
    for (int i = 0; i < 4; ++i) m[i] = n >= 640u ? PSENT : m[i] == PSENT ? 0x7FC17FC1u : m[i];
    st4s(rs, gi - 36u * 256u < F_CLR ? foff(gi - 36u * 256u) : OOB, 0, 0u);
#pragma unroll 1
    for (u32 o = gt; o < MB; o += R) st16(rs, W_ORING + o, (v4u){PSENT, PSENT, PSENT, PSENT});
    const u32 mo = gt < 4 * 32768 + 16384 ? W_MRING + gt : OOB;
    st16(rs, mo, m);
    st16(rs, mo + MB / 4, m);
    static_assert(NPRE * 4096 >= 4 * 32768 + 16384, "memory ring");
}
constexpr int KT0 = NPRE * 4, LN0 = NPRE * 8;
struct Early { RowX r[2]; v4u m; u32 vote; };
DEV void conv_early(const Params& p, rsrc_t rs, int j, Early& e) {
    const int lane = threadIdx.x & 63, w = uni(threadIdx.x >> 6), u = j * 4 + w;
    if (j < NPRE) {
        const rsrc_t rx = mk_rsrc(p.x, ACT);
#pragma unroll
        for (int i = 0; i < 2; ++i) {
            const u32 so = uu((u + i * KT0) * (DM * 2));
            e.r[i].a = __builtin_amdgcn_raw_buffer_load_b64(rx, lane * 8u, so, 0); e.r[i].b = __builtin_amdgcn_raw_buffer_load_b64(rx, lane * 8u + 512u, so, 0);
            e.r[i].c = __builtin_amdgcn_raw_buffer_load_b32(rx, lane * 4u + 1024u, so, 0);
        }
        e.m = mem0_load(p, j);
    } else if (j < NCV && w > 0) e.vote = pol_probe(p, rs, u - NPRE * 4);
}
DEV void conv_stage(const Params& p, rsrc_t rs, lptr lds, int j, int g0, int ge, v2u ep, Early& e) {
    const int tid = threadIdx.x, lane = tid & 63, w = uni(tid >> 6), u = j * 4 + w;
    lptr F = lds + w * CV_W;
    const u32 fb = uu(w * CV_W);
    const rsrc_t rd = mk_rsrc(p.ssmD, DM * 2);
    float y0[16], y1[16];
    KvIn in;
    if (j < NPRE) {
        constexpr int CH = CvL<false>::CH;
        if (w == 1) LST(1500 + j, rt32());
        pre_rings(rs, j, e.m);
        ln_row(rs, u, e.r[0]); ln_row(rs, u + KT0, e.r[1]);
        if (w == 1) LST(1550 + j, rt32());
        CFENCE();
        kvt_load(p, u, in, [&] { VMW(13); BAR(); pre_post(rs, j, 1u, ep); if (w == 0) LST(1450 + j, rt32()); });
        if (j == 20) LST(260, rt32());
        const u32 D = __builtin_amdgcn_raw_buffer_load_b16(rd, (u32)cv_chan(u, lane & 3) * 2u, 0, 0);
#pragma unroll 1
        for (int i = 0; i < 4; ++i) { const int d = cv_chan(u, i); cv_filt<false>(p, fb + i * CH, d, 0); cv_filt<false>(p, fb + i * CH, d, 1); cv_filt<false>(p, fb + i * CH, d, 2); }
        kvt_make(rs, F + CV_Y, u, in);
        cv_zero<false>(F); cv_zero<false>(F + 2 * CH);
        DRAIN(); BAR();
        pre_post(rs, j, 2u, ep);
        if (j == 20) LST(262, rt32());
        pre_wait(rs, 0, NPRE, 2u, ep);
        if (j == 20) LST(281, rt32());
        {
            v4u k[8];
#pragma unroll
            for (int i = 0; i < 8; ++i) k[i] = cv_kvld(rs, cv_chan(u, i >> 1), i & 1);
#pragma unroll
            for (int i = 0; i < 4; ++i) cv_kvput<false>(F + i * CH, k + 2 * i);
        }
        if (j == 20) LST(286, rt32());
#pragma unroll 1
        for (int i = 0; i < 4; i += 2) {
            cv_run<false>(p, rs, F + i * CH, cv_chan(u, i), rdl(D, i), f32x4{}, y0, [](int) {});
            cv_run<false>(p, rs, F + (i + 1) * CH, cv_chan(u, i + 1), rdl(D, i + 1), f32x4{}, y1, [](int) {});
            cv_put(F, i >> 1, y0, y1);
        }
        if (j == 20) LST(287, rt32());
        cv_out(rs, F, u, 0);
        if (j == 20) LST(263, rt32());
    } else {
        st16(rs, lane == 0 ? foff(F_PRE + 48 + u - NPRE * 4) : OOB, (v4u){ep.x + 1u, ep.y, w > 0 ? e.vote : 0u, 0u});
#pragma unroll 1
        for (int t = u + 128; t < 320; t += 16) { kvt_load(p, t, in, [] {}); kvt_make(rs, F, t, in); }
    }
    DRAIN(); BAR();
    if (g0 < ge) {
        constexpr int CH = CvL<true>::CH;
        pre_post(rs, j, 3u, ep);
        cv_zero<true>(F);
        if (w == 0) { pre_poll(rs, 64, 32, 3u, ep); pre_poll(rs, 0, NCV, 3u, ep); }
        BAR();
        v4u k[4];
        auto fetch = [&](u32 dst, int d, int i) { if (i < 5) cv_filt<true>(p, dst, d, i); else if (i < 9) k[i - 5] = cv_kvld(rs, d, i - 5); };
#pragma unroll
        for (int i = 0; i < 9; ++i) fetch(fb, cv_chan(g0 * 4 + w, 0), i);
        const u32 ch = (u32)cv_chan(min((g0 + 8 * (lane >> 2)) * 4 + w, 159), lane & 3);
        u32 D = __builtin_amdgcn_raw_buffer_load_b16(rd, ch * 2u, 0, 0);
        v4u sp = ld16s(rs, W_SP + ch * 16u, 0);
        asm volatile("" : "+v"(D), "+v"(sp));
#pragma unroll 1
        for (int g = g0, n = 0; g < ge; g += 8) {
            const int un = g * 4 + w;
            if (g == 16) { LST(282, rt32()); LST(288, __builtin_amdgcn_s_memtime()); }
#pragma unroll 1
            for (int i = 0; i < 4; i += 2, n += 2) {
                const int d1 = (int)rdl(ch, n + 1), d2 = (int)rdl(ch, n + 2);
                cv_kvput<true>(F, k);
                cv_run<true>(p, rs, F, 0, rdl(D, n), (f32x4){f_(rdl(sp.x, n)), f_(rdl(sp.y, n)), f_(rdl(sp.z, n)), 0.f}, y0, [&](int q) { fetch(fb + CH, d1, q); });
                cv_kvput<true>(F + CH, k);
                cv_run<true>(p, rs, F + CH, 326, rdl(D, n + 1), (f32x4){f_(rdl(sp.x, n + 1)), f_(rdl(sp.y, n + 1)), f_(rdl(sp.z, n + 1)), 0.f}, y1, [&](int q) { fetch(fb, d2, q); });
                cv_put(F, i >> 1, y0, y1);
            }
            cv_out(rs, F, un, 4);
            if (g == 16) { LST(283, rt32()); LST(289, __builtin_amdgcn_s_memtime()); }
        }
        DRAIN(); BAR();
    }
    pre_post(rs, j, 5u, ep);
    if (j == 20) LST(284, rt32());
    if (j == 3) LST(285, rt32());
}

#define M32A(first, d, a, b) do { if (first) asm volatile("v_mfma_f32_32x32x16_bf16 %0, %1, %2, 0" : "=&v"(d) : "a"(a), "v"(b)); \
                                  else asm volatile("v_mfma_f32_32x32x16_bf16 %0, %1, %2, %0" : "+v"(d) : "a"(a), "v"(b)); } while (0)
#define M32B(first, d, a, b) do { if (first) asm volatile("v_mfma_f32_32x32x16_bf16 %0, %1, %2, 0" : "=&v"(d) : "v"(a), "a"(b)); \
                                  else asm volatile("v_mfma_f32_32x32x16_bf16 %0, %1, %2, %0" : "+v"(d) : "v"(a), "a"(b)); } while (0)
#define VCVT(d, a, b) asm volatile("v_cvt_pk_bf16_f32 %0, %1, %2" : "=v"(d) : "v"(a), "v"(b))
#define VADD(d, a, b) asm volatile("v_add_f32 %0, %1, %2" : "=v"(d) : "v"(a), "v"(b))
#define LD4A(d, vo, so) asm volatile("buffer_load_dword %0, %1, %2, %3 offen sc1" : "=v"(d) : "v"(vo), "s"(srd), "s"(so) : "memory")
DEV u32 wst_src(int lane, int ld) { return (u32)((lane >> 4) * ld + 8 * ((lane & 15) ^ ((lane >> 4) << 2))) * 2u; }
DEV u32 wst_rd(int lane, int w) { const int q4 = (lane >> 2) & 3; return (u32)(q4 * 256 + ((w ^ q4) * 64) + ((lane >> 4) & 1) * 32 + (lane & 3) * 8); }

constexpr int QK_SEL = 81920;
#define QK_LEAD 12
#define QK_SMP 29
#define QK_YB 26
#define QK_W0 2
DEV void role_qkv(const Params& p, lptr lds, int c, v2u ep) {
    const int tid = threadIdx.x, lane = tid & 63, w = uni(tid >> 6), hh = lane >> 5, r32 = lane & 31;
    const int m = c >> 5, h = c & 31;
    const rsrc_t rs = mk_rsrc(p.ws, W_END);
    v4u wt[40];
    {
        const v4u sw = mk_srd(m == 0 ? p.Wq : m == 1 ? p.Wk : p.Wv, DM * NQ * 2), sx = mk_srd(p.x, ACT);
        const u32 vo = wst_src(lane, NQ) + (u32)h * (DH * 2), x8 = (u32)lane * 8u, x4 = (u32)lane * 4u;
        RowX xr[5];
        auto wfill = [&](int i0, int n) {
#pragma unroll 1
            for (int i = i0; i < i0 + n; ++i) DMAIN(sw, uu((4 * i + w) * 1024), vo, uu((16 * i + 4 * w) * (NQ * 2)));
        };
        wfill(0, QK_W0);
#pragma unroll
        for (int i = 0; i < 5; ++i)
            asm volatile("buffer_load_dwordx2 %0, %3, %5, %6 offen\n\tbuffer_load_dwordx2 %1, %3, %5, %6 offen offset:512\n\tbuffer_load_dword %2, %4, %5, %6 offen offset:1024"
                         : "=&v"(xr[i].a), "=&v"(xr[i].b), "=&v"(xr[i].c) : "v"(x8), "v"(x4), "s"(sx), "s"(uu((LN0 + 4 * c + w + 384 * i) * (DM * 2))) : "memory");
        wfill(QK_W0, 10 - QK_W0);
        if (c == 3) LST(275, rt32());
        VMW(10 - QK_W0);
        if (c == 3) LST(276, rt32());
#pragma unroll
        for (int i = 0; i < 5; ++i) {
            asm volatile("" : "+v"(xr[i].a), "+v"(xr[i].b), "+v"(xr[i].c));
            const int row = LN0 + 4 * c + w + 384 * i;
            if (i < 4 || row < SEQ) ln_row(rs, row, xr[i]);
            wfill(10 + 6 * i, 6);
        }
        if (c == 3) LST(264, rt32());
        lptr tb = lds + hh * 2048 + wst_rd(lane, w);
        SFOR(k, 40)
            if constexpr (k == 0) { VMW(15); BAR(); }
            if constexpr (k == 28) {
                if (w == 0) pre_poll(rs, 0, NPRE, 1u, ep);
                DRAIN(); BAR();
                pre_post(rs, 96 + c, 1u, ep);
            }
            lptr q = tb + (k >> 3) * 32768 + (k & 7) * 4096; wt[k] = tr2(q, q + 1024); asm volatile("" : "+a"(wt[k]));
        SEND
    }
    if (c == 3) LST(268, rt32());
    if (w == 0) LST(1350 + c, rt32());
    bool dead = false;
    int n0, n1;
    const u32 t16 = (u32)(w * 2048 + lane * 16), wb = uu(w * 2048);
    u32 ar[16];
#pragma unroll
    for (int j = 0; j < 16; ++j) ar[j] = (u32)(r32 * 256 + ((2 * (j & 7) + hh) ^ (r32 & 15)) * 16 + (j >> 3) * 49152);
    MK_SRD();
#define XRD(y, ks) lds_r16(lds + ar[((ks) & 7) + ((ks) >= 24 ? 8 : 0)] + ((ks) >> 3) % 3 * 16384 + (y) * 8192)
#define QM0(y, b) asm volatile("s_add_u32 m0, %0, %1" :: "s"(wb), "i"((b) * 16384 + (y) * 8192) : "scc", "memory")
#define QDMA(i, vo) asm volatile("buffer_load_dwordx4 %0, %1, %2 offen offset:%3 sc1 lds" :: "v"(vo), "s"(srd), "s"(((i) >> 1) * 8192), "i"(((i) & 1) * 1024) : "memory")
    const u32 h2flag = (lane < 16 && w == 0) ? foff(F_H2 + lane) : OOB;
    const u32 ol = (m == 2 ? (u32)(2 * w + (r32 >> 4)) * 1024u : (u32)((r32 >> 4) * 4 + w) * 1024u) + (u32)(hh * 16 + (r32 & 15)) * 16u
                 + (m == 2 ? W_VO : m == 1 ? W_KQ : W_KQ + 8192u) + (u32)h * 16384u;
    const u32 fo = tid < 2 ? foff(F_QKV + tid * 96 + c) : OOB;
    lptr selw = lds + QK_SEL + (w == 0 ? 0 : 64), selr = lds + QK_SEL;
    PIN(selw);
    static_assert(QK_LEAD > 8 && QK_LEAD % 2 == 0 && LN0 == 10 * 32, "X = 8 comes up, before any item of layer 1");
    static_assert(W_H2 - W_H1 == 2 * 64 * (32 * DM * 2), "h");
    auto run = [&](auto isv) __attribute__((always_inline)) {
        constexpr bool V = decltype(isv)::value;
        constexpr int YB = QK_YB;
        f32x16 ax, ay = {};
        v4u x[8], pp, pq;
        u32 xv = t16 + W_H1, ox = ol, oy = OOB, nn = 1, lt = 0, fcnt = 0, fa = OOB;
        asm volatile("" : "+v"(lt), "+v"(nn));
#pragma unroll
        for (int z = 0; z < 5; ++z) x[z] = XRD(1, 40 - YB + z);
#define QPK(r_, a1, a_, q_) do { if constexpr (V) VCVT(r_[q_], a1[4 * (a_) + 8 * ((q_) >> 1) + 2 * ((q_) & 1)], a1[4 * (a_) + 8 * ((q_) >> 1) + 2 * ((q_) & 1) + 1]); \
                                 else VCVT(r_[q_], a1[4 * (a_) + (q_)], a1[4 * (a_) + (q_) + 8]); } while (0)
#define QPK4(r_, a1, a_) do { QPK(r_, a1, a_, 0); QPK(r_, a1, a_, 1); QPK(r_, a1, a_, 2); QPK(r_, a1, a_, 3); } while (0)
        for (;;) {
            u32 ls = uu(lt), yv, fv, nx, need;
            if (ls != 0u && uu(nn) < 256u) pre_wait(rs, 96, 96, 1u, ep);
            else if (ls != 0u) {
                n1 = (int)(uu(nn) >> 8);
                CFENCE();
                const u32 hf = h2flag + (u32)(~n1 & 1) * 2048u;
                u32 f2 = ld4(rs, hf);
                asm volatile("s_waitcnt vmcnt(0)" : "+v"(f2) :: "memory");
                *(LAS u32*)(selw + 16) = (u32)__builtin_amdgcn_ballot_w64(f2 < (u32)n1) & 0xFFFFu;
                BAR();
                st4s(rs, fa, 0, fcnt);
                fa = OOB;
                if (n1 > NSEG) { LDUMP(w == 0, 64 + c); __builtin_amdgcn_endpgm(); }
                if (uu(*(LAS u32*)(selr + 16)) != 0u) {
                    if (w == 0) wait_flags(rs, hf, (u32)n1, dead);
                    BAR();
                }
            }
#define QT(k) do {} while (0)
            QT(0);
            SCHED();
            QM0(0, 0); NOPS(0); QDMA(0, xv); QDMA(1, xv); QM0(0, 1);
            SFOR(z, 80)
                constexpr int ks = (z + 40 - YB) % 40, zx = z - YB;
                constexpr auto upto = [](int q) { const int d = (q - YB + 49) % 40; return q + 6 + (d == 0 || d == 2 ? 1 : d == 1 ? 2 : 0); };
                if constexpr (zx >= 0 && zx < 40) { if constexpr (V) M32B(ks == 0, ax, x[z & 7], wt[ks]); else M32A(ks == 0, ax, wt[ks], x[z & 7]); }
                else { if constexpr (V) M32B(ks == 0, ay, x[z & 7], wt[ks]); else M32A(ks == 0, ay, wt[ks], x[z & 7]); }
                if constexpr (z == 78) lt = *(LAS u32*)selr;
#pragma unroll
                for (int q = upto(z - 1); q < upto(z); ++q) x[q & 7] = XRD(q % 80 < YB || q % 80 >= YB + 40, (q + 40 - YB) % 40);
                SCHED();
                (void)&srd; (void)&wb;
                if constexpr (z < 2) QDMA(z + 2, xv);
                if constexpr (z >= 2 && z < 14 && (z & 3) == 2) QM0(0, (z + 6) >> 2);
                if constexpr (z >= 3 && z < 15 && (z & 1)) QDMA((z + 5) >> 1, xv);
                if constexpr (z == 4) { n0 = uni((int)nn); n1 = n0 >> 8; n0 &= 255; PINS(n0); PINS(n1); }
                if constexpr (z == 8) { nx = n0 < NSEG ? W_H1 + (u32)n0 * (32 * DM * 2) : OOB; PINS(nx); }
                if constexpr (z == 12) { yv = t16 + nx; PIN(yv); }
                if constexpr (zx == -6) { QT(1); asm volatile("s_waitcnt vmcnt(6) lgkmcnt(0)\n\ts_barrier" ::: "memory"); QT(2); }
                if constexpr (zx == -5) st4s(rs, fa, 0, fcnt);
                if constexpr (zx == -4 || zx == 0 || zx == 4) QM0(1, (zx + 4) >> 2);
                if constexpr (zx >= -3 && zx < 8 && (zx & 1)) QDMA((zx + 3) >> 1, yv);
                if constexpr (zx == 2) QPK4(pp, ay, 0);
                if constexpr (zx == 6) QPK4(pq, ay, 1);
                if constexpr (zx == 8) st16(rs, oy, pp);
                if constexpr (zx == 9) st16(rs, oy + 512, pq);
                if constexpr (zx == 10) { QT(3); asm volatile("s_waitcnt vmcnt(9)\n\ts_barrier" ::: "memory"); QT(4); }
                if constexpr (zx == 11 || zx == 15) QM0(1, (zx + 1) >> 2);
                if constexpr (zx >= 12 && zx < 20 && (zx & 1) == 0) QDMA(zx >> 1, yv);
                if constexpr (zx == 21) { fcnt = lane == 0 ? (u32)n0 : (u32)n1; fa = fo; asm volatile("" : "+v"(fcnt), "+v"(fa)); }
                if constexpr (zx == 22) { oy = (ol + (u32)n0 * (NH * 16384u)) | (yv & OOB); PIN(oy); }
                if constexpr (zx == 23) { n0 = min(n0 + 1, NSEG); PINS(n0); nx = (u32)min(NSEG, n1 + QK_LEAD); PINS(nx); }
                if constexpr (zx == 24)
                    asm volatile("s_cmp_ge_u32 %1, %0\n\ts_cselect_b32 %0, %4, %1\n\ts_addc_u32 %2, %2, 0\n\ts_cmp_lt_u32 %0, 64\n\ts_cselect_b32 %3, 0, %2"
                                 : "+s"(nx), "+s"(n0), "+s"(n1), "=&s"(need) : "s"(n1 + 64) : "scc");
                if constexpr (zx == 25) { n0 += 1 - (int)min(nx >> 6, 1u); PINS(n0); }
                if constexpr (zx == 26) { nn = (u32)(n0 | n1 << 8); PIN(nn); }
                if constexpr (zx == 27) { ls = W_H1 + (nx + (nx & 64u)) * (32 * DM * 2); PINS(ls); }
                if constexpr (zx == 28) { xv = t16 + ls; PIN(xv); need = nx == 8u ? 255u : need; PINS(need); }
                if constexpr (zx == 34) { asm volatile("s_waitcnt vmcnt(%0) lgkmcnt(0)\n\ts_barrier" :: "i"(QK_SMP < 34 ? 7 : 6) : "memory"); QT(5); }
                if constexpr (zx == QK_SMP) { const u32 po = (nx & 1u) * 2048u; LD4A(fv, h2flag, po); }
                if constexpr (zx == 42) QPK4(pp, ax, 0);
                if constexpr (zx == 43) st16(rs, ox, pp);
                if constexpr (zx == 44) QPK4(pq, ax, 1);
                if constexpr (zx == 45) st16(rs, ox + 512, pq);
                if constexpr (zx == 47) { ox = ol + nx * (NH * 16384u); PIN(ox); }
                if constexpr (z == 74) { asm volatile("s_waitcnt vmcnt(2)" : "+v"(fv) :: "memory"); QT(6); ls = (u32)__builtin_amdgcn_ballot_w64(fv < need) & 0xFFFFu; PINS(ls); }
                if constexpr (z == 75) { lt = ls; PIN(lt); *(LAS u32*)selw = lt; }
                if constexpr (z == 77) asm volatile("s_waitcnt lgkmcnt(2)\n\ts_barrier" ::: "memory");
                SCHED();
            SEND
        }
    };
    if (m == 2) run(IC<1>{}); else run(IC<0>{});
}

DEV void role_out(const Params& p, lptr lds, int L, int kq, int ng, v2u ep) {
    const int tid = threadIdx.x, lane = tid & 63, w = uni(tid >> 6), hh = lane >> 5, r32 = lane & 31;
    const rsrc_t rs = mk_rsrc(p.ws, W_END);
    v4u wo[64];
    bool dead = false;
    {
        const v4u sw = mk_srd(p.Wo, DM * NQ * 2);
        const u32 vo = wst_src(lane, DM) + (u32)ng * 256u;
        auto fill = [&](int b) {
#pragma unroll 1
            for (int i = 0; i < 8; ++i) DMAIN(sw, uu((b % 5) * 32768 + (4 * i + w) * 1024), vo, uu((kq * 1024 + b * 128 + 16 * i + 4 * w) * (DM * 2)));
        };
#pragma unroll 1
        for (int b = 0; b < 5; ++b) fill(b);
        lptr tb = lds + hh * 1024 + wst_rd(lane, w);
        SFOR(b, 8)
            VMW(b < 4 ? 32 : 56 - 8 * b); BAR();
            lptr q = tb + (b % 5) * 32768;
#pragma unroll
            for (int k = 0; k < 8; ++k) { lptr a = q + (k >> 1) * 8192 + (k & 1) * 2048; wo[8 * b + k] = tr2(a, a + 4096); if (b < 7) asm volatile("" : "+a"(wo[8 * b + k])); else PIN(wo[8 * b + k]); }
            if constexpr (b < 3) { BAR(); fill(b + 5); }
        SEND
    }
    pre_wait(rs, 0, NPRE, 1u, ep);
    if (kq == 0 && ng == 0) LST(272 + L, rt32());
    MK_SRD();
    const u32 ofl = lane < 8 ? foff(F_O + L * 32 + kq * 8 + lane) : OOB, l16 = (u32)lane * 16u;
    const u32 fo = (tid == 0) ? foff(F_P + L * 32 + kq * 5 + ng) : OOB;
#define OM0(dst, b) asm volatile("s_add_u32 m0, %0, %1" :: "s"(dst), "i"((b) * 8192) : "scc", "memory")
#define ODMA(i, vo) asm volatile("buffer_load_dwordx4 %0, %1, %2 offen offset:%3 sc1 lds" :: "v"(vo), "s"(srd), "s"(((i) >> 1) * 16384), "i"(((i) & 1) * 1024) : "memory")
    auto first = [&](u32 dst, u32 vo) {
#pragma unroll
        for (int b = 0; b < 5; ++b) { OM0(dst, b); NOPS(0); ODMA(2 * b, vo); ODMA(2 * b + 1, vo); }
    };
    u32 pa = W_P + (u32)(L * NSEG) * P_ITEM + (u32)(2 * hh * 4 + kq) * (DM * 4) + (u32)(ng * 128 + w * 32 + r32) * 4u, fv = 0;
    u32 ov = W_VO + 8192u + (u32)(L * NSEG * NH + kq * 8) * 16384u + (u32)w * 2048u + l16;
    bool early = false;
    for (int s = 0; s < NSEG; ++s) {
        const u32 dst = uu((s & 1) * 65536 + w * 2048), ndst = dst ^ 65536u, need = s + 1 < NSEG ? (u32)s + 2u : 255u;
        lptr A = lds + (s & 1) * 65536 + l16;
        if (!early) {
            if (!all_(ofl == OOB || fv >= (u32)s + 1)) wait_flags(rs, ofl, (u32)s + 1, dead);
            LS3(L == 0 && w == 1, 1, kq * 5 + ng);
            first(dst, ov);
        }
#define OT(k) do {} while (0)
        OM0(dst, 5);
        asm volatile("s_waitcnt vmcnt(8)\n\ts_barrier" ::: "memory");
        OT(0);
        v4u o[64];
#pragma unroll
        for (int k = 0; k < 4; ++k) o[k] = lds_r16(A + k * 1024);
        f32x16 acc;
        u32 far, nv;
        SCHED();
        SFOR(k, 64)
            if constexpr (k < 56) M32B(k == 0, acc, o[k], wo[k]); else M32V(acc, o[k], wo[k]);
            if constexpr (k < 60) o[k + 4] = lds_r16(A + (k + 4) * 1024);
            SCHED();
            (void)&srd; (void)&dst; (void)&ndst;
            if constexpr (k < 12 && (k & 1) == 0) ODMA(10 + k / 2, ov);
            if constexpr (k == 3) asm volatile("s_waitcnt vmcnt(6)\n\ts_barrier" ::: "memory");
            if constexpr (k == 3 || k == 7) OM0(dst, (k + 21) >> 2);
            if constexpr (k == 12) { ov += NH * 16384u; PIN(ov); }
            if constexpr (k == 19) asm volatile("s_waitcnt vmcnt(6)\n\ts_barrier" ::: "memory");
            if constexpr (k == 20) LD4A(fv, ofl, 0);
            if constexpr (k == 35) asm volatile("s_waitcnt vmcnt(1)\n\ts_barrier" ::: "memory");
            if constexpr (k == 40) { asm volatile("s_waitcnt vmcnt(0)" : "+v"(fv) :: "memory"); far = (u32)__builtin_amdgcn_ballot_w64(fv < need) & 255u; PINS(far); }
            if constexpr (k == 41) { nv = far ? OOB : ov; PIN(nv); }
            if constexpr (k >= 42 && k < 62 && (k & 3) == 2) OM0(ndst, (k - 42) >> 2);
            if constexpr (k >= 43 && k < 63 && (k & 1)) ODMA((k - 43) >> 1, nv);
            if constexpr (k == 48) LD4A(fv, ofl, 0);
            if constexpr (k == 4) OT(1);
            if constexpr (k == 20) OT(2);
            if constexpr (k == 36) OT(3);
            if constexpr (k == 41) OT(4);
            SCHED();
        SEND
        OT(5);
        asm volatile("s_nop 11" : "+v"(acc), "+v"(pa));
#pragma unroll
        for (int i = 0; i < 8; ++i) st4s(rs, pa, (u32)((4 * (i >> 1) + (i & 1)) * (4 * DM * 4)), pk(acc[2 * i], acc[2 * i + 1]));
        asm volatile("s_waitcnt vmcnt(15)" : "+v"(fv) :: "memory");
        OT(6);
        LS3(L == 0 && w == 1, 2, kq * 5 + ng);
        early = far == 0u || all_(ofl == OOB || fv >= need);
        if (far != 0u && early) { first(ndst, ov); VMW(10); }
        else { fv = ld4(rs, ofl); asm volatile("s_waitcnt vmcnt(0)" : "+v"(fv) :: "memory"); }
        BAR();
        st4s(rs, fo, 0, (u32)s + 1);
        pa += P_ITEM;
        LS2(L == 0 && w == 1, 160 + kq * 5 + ng);
    }
    LDUMP(L == 0 && w == 1, kq * 5 + ng);
}

DEV void ln_stats2(const f32x2 (&x)[10], f32x2& mean, f32x2& rstd) {
    const f32x2 c = {f_(uu(u_(x[0].x))), f_(uu(u_(x[0].y)))};
    f32x2 d[10];
#pragma unroll
    for (int i = 0; i < 10; ++i) d[i] = x[i] - c;
    f32x2 a = ((d[0] + d[1]) + (d[2] + d[3])) + ((d[4] + d[5]) + (d[6] + d[7])) + (d[8] + d[9]);
    f32x2 b = (d[0] * d[0] + d[2] * d[2] + d[4] * d[4] + d[6] * d[6] + d[8] * d[8]) + (d[1] * d[1] + d[3] * d[3] + d[5] * d[5] + d[7] * d[7] + d[9] * d[9]);
    float a0, a1, b0, b1;
#define LN_DP4(m) "v_add_f32_dpp %0, %0, %0 " m " row_mask:0xf bank_mask:0xf\n\tv_add_f32_dpp %1, %1, %1 " m " row_mask:0xf bank_mask:0xf\n\t" \
                  "v_add_f32_dpp %2, %2, %2 " m " row_mask:0xf bank_mask:0xf\n\tv_add_f32_dpp %3, %3, %3 " m " row_mask:0xf bank_mask:0xf\n\t"
    asm("s_nop 1\n\t" LN_DP4("quad_perm:[1,0,3,2]") LN_DP4("quad_perm:[2,3,0,1]") LN_DP4("row_ror:4") LN_DP4("row_ror:8")
        "v_permlane16_swap_b32 %0, %1\n\tv_add_f32 %0, %0, %1\n\tv_permlane16_swap_b32 %2, %3\n\tv_add_f32 %2, %2, %3\n\ts_nop 1\n\t"
        "v_permlane32_swap_b32 %0, %2\n\tv_add_f32 %0, %0, %2\n\ts_nop 0\n\t"
        "v_readlane_b32 %4, %0, 0\n\tv_readlane_b32 %5, %0, 16\n\tv_readlane_b32 %6, %0, 32\n\tv_readlane_b32 %7, %0, 48\n\ts_nop 1"
        : "+v"(a.x), "+v"(b.x), "+v"(a.y), "+v"(b.y), "=s"(a0), "=s"(b0), "=s"(a1), "=s"(b1));
    const f32x2 m = (f32x2){a0, a1} * (1.0f / DM), v = (f32x2){b0, b1} * (1.0f / DM) - m * m;
    const f32x2 q = up2(pkv(up2(pkv((f32x2){fmaxf(v.x, 0.f), fmaxf(v.y, 0.f)})) + 1e-5f));
    const f32x2 sd = up2(pk(__builtin_amdgcn_sqrtf(q.x), __builtin_amdgcn_sqrtf(q.y)));
    mean = up2(pkv(c + m)); rstd = (f32x2){__builtin_amdgcn_rcpf(sd.x), __builtin_amdgcn_rcpf(sd.y)};
}
DEV void role_row(const Params& p, lptr lds, int id, v2u ep) {
    const int lane = threadIdx.x & 63, w = uni(threadIdx.x >> 6);
    const int pr = id >> 2, L = id & 1, par = (id >> 1) & 1;
    const rsrc_t rs = mk_rsrc(p.ws, W_END), rx = mk_rsrc(p.x, ACT);
    bool dead = false;
    const u32 fp = lane < 20 ? foff(F_P + L * 32 + lane) : OOB, l16 = (u32)lane * 16u, l8 = (u32)lane * 8u, l4 = (u32)lane * 4u, fb = uu(w * CV_W);
    lptr F = lds + w * CV_W;
    MK_SRD();
    for (int s = par; s < NSEG; s += 2) {
        const u32 ro = (u32)(s * 32 + 2 * pr) * (DM * 2);
        if ((s & 6) == 0) {
            if ((s & 30) == 0) pre_poll(rs, 0, s < 32 ? NPRE : NCV, s < 32 ? 3u : 5u, ep);
            if (id == 0 && s == 0) LST(274, rt32());
            if (s == 48) st8s(rs, (id == 1 && lane == 0) ? foff(F_EPOCH) : OOB, 0, (v2u){ep.x + 8u, ep.y});
#pragma unroll 1
            for (int i = 0; i < 5; ++i) {
                const u32 d = fb + i * 2048, o = W_Y + (u32)(16 * pr + (s >> 3)) * 10240u + i * 2048;
                DMA32(d, l16, o); DMA32(d + 10240u, l16, o + 81920u);
            }
            DRAIN();
        }
        RowX xr[2];
        auto xload = [&](rsrc_t r, u32 o, auto aux) {
#pragma unroll
            for (int i = 0; i < 2; ++i) {
                xr[i].a = __builtin_amdgcn_raw_buffer_load_b64(r, l8, o + i * (DM * 2), aux.value); xr[i].b = __builtin_amdgcn_raw_buffer_load_b64(r, l8 + 512u, o + i * (DM * 2), aux.value);
                xr[i].c = __builtin_amdgcn_raw_buffer_load_b32(r, l4 + 1024u, o + i * (DM * 2), aux.value);
            }
        };
        if (L == 0) xload(rx, ro, IC<0>{});
        f32x2 y[10], x[10];
        {
            lptr ya = F + (s & 7) * 2 + l16, yc = F + (s & 7) * 2 + 2048 + (lane >> 1) * 16 + (lane & 1) * 5120;
#pragma unroll
            for (int i = 0; i < 10; ++i) {
                lptr q = i < 8 ? ya + (i & 3) * 2560 + (i >> 2) * 1024 : yc + (i & 1) * 2560;
                y[i] = (f32x2){lo_f(*(LAS u16*)q), lo_f(*(LAS u16*)(q + 10240))}; PIN(y[i]);
            }
        }
        wait_flags(rs, fp, (u32)s + 1, dead);
        LS3(L == 0, 3, ri);
        LMX(L == 0, 0, s, rt32());
        {
            const u32 o = uu(W_P + (u32)(L * NSEG + s) * P_ITEM + (u32)pr * (4 * DM * 4));
            if (L == 1) xload(rs, W_X2 + ro, IC<16>{});
            v4u a[4], b[4]; v2u c[4];
#pragma unroll
            for (int kq = 0; kq < 4; ++kq) { a[kq] = ld16s(rs, l16, o + kq * (DM * 4)); SCHED(); b[kq] = ld16s(rs, l16 + 1024u, o + kq * (DM * 4)); SCHED(); c[kq] = ld8s(rs, l8 + 2048u, o + kq * (DM * 4)); SCHED(); }
            const u32 u0[5] = {xr[0].a.x, xr[0].a.y, xr[0].b.x, xr[0].b.y, xr[0].c}, u1[5] = {xr[1].a.x, xr[1].a.y, xr[1].b.x, xr[1].b.y, xr[1].c};
#pragma unroll
            for (int i = 0; i < 5; ++i) { x[2 * i] = (f32x2){lo_f(u0[i]), lo_f(u1[i])}; x[2 * i + 1] = (f32x2){hi_f(u0[i]), hi_f(u1[i])}; }
#pragma unroll
            for (int kq = 0; kq < 4; ++kq)
#pragma unroll
                for (int i = 0; i < 10; ++i)
                    asm volatile("v_dot2_f32_bf16 %0, %2, %3, %0\n\tv_dot2_f32_bf16 %1, %2, %4, %1" : "+v"(x[i].x), "+v"(x[i].y)
                                 : "v"(i < 4 ? a[kq][i & 3] : i < 8 ? b[kq][i & 3] : c[kq][i & 1]), "s"(0x3F80u), "s"(0x3F800000u));
            asm volatile("s_nop 2" : "+v"(x[8]), "+v"(x[9]));
        }
        LS3(L == 0, 4, ri);
        f32x2 mean, rstd;
        ln_stats2(x, mean, rstd);
#pragma unroll
        for (int i = 0; i < 10; ++i) x[i] += (x[i] - mean) * (y[i] * rstd);
        u32 xp[2][5];
#pragma unroll
        for (int i = 0; i < 5; ++i) { xp[0][i] = pkv((f32x2){x[2 * i].x, x[2 * i + 1].x}); xp[1][i] = pkv((f32x2){x[2 * i].y, x[2 * i + 1].y}); }
        if (L == 0) {
#pragma unroll
            for (int i = 0; i < 5; ++i) { x[2 * i] = (f32x2){lo_f(xp[0][i]), lo_f(xp[1][i])}; x[2 * i + 1] = (f32x2){hi_f(xp[0][i]), hi_f(xp[1][i])}; }
            ln_stats2(x, mean, rstd);
#pragma unroll
            for (int i = 0; i < 10; ++i) x[i] = (x[i] - mean) * rstd;
#pragma unroll
            for (int r = 0; r < 2; ++r) {
                u32 hp[5];
#pragma unroll
                for (int i = 0; i < 5; ++i) hp[i] = pkv((f32x2){x[2 * i][r], x[2 * i + 1][r]});
                store_h(rs, W_H2, s * 32 + 2 * pr + r, hp);
            }
#pragma unroll
            for (int r = 0; r < 2; ++r) {
                st8s(rs, l8, W_X2 + ro + r * (DM * 2), (v2u){xp[r][0], xp[r][1]});
                st8s(rs, l8 + 512u, W_X2 + ro + r * (DM * 2), (v2u){xp[r][2], xp[r][3]});
                st4s(rs, l4 + 1024u, W_X2 + ro + r * (DM * 2), xp[r][4]);
            }
            LS3(L == 0, 5, ri);
            DRAIN();
            st4s(rs, lane == 0 ? foff(F_H2 + par * 16 + pr) : OOB, 0, (u32)s + 1);
        } else {
#pragma unroll
            for (int r = 0; r < 2; ++r) {
                u16* o_ = p.out + (size_t)(s * 32 + 2 * pr + r) * DM;
                *(v2u*)(o_ + 4 * lane) = (v2u){xp[r][0], xp[r][1]};
                *(v2u*)(o_ + 256 + 4 * lane) = (v2u){xp[r][2], xp[r][3]};
                *(u32*)(o_ + 512 + 2 * lane) = xp[r][4];
            }
        }
        if (pr == 0) LST(320 + L * 64 + s, rt32());
        LS2(L == 0, 224 + ri);
        LMX(L == 0, 1, s, rt32());
    }
    LDUMP(L == 0, 32 + ri);
}

constexpr float SCALE = 0.08838834764831845f, LOG2E = 1.4426950408889634f;
constexpr int C_K = 0, C_V = 8192, C_QX = 16384, C_MEM = 24576, C_Q = C_MEM + 10240, C_KM = C_Q + 2304, C_VM = C_KM + 2304, C_DUMP = C_VM + 2048,
              C_ZERO = C_DUMP + 1024, C_WL = C_ZERO + 128;
constexpr int NLF = 14;
static_assert(C_WL % 16 == 0 && C_WL + 4 * NLF * 2048 <= LDS_BYTES, "head lds");
#define CH_VIS 10
#define CH_H0 104
#define CH_W0 81
#define CH_MH 2
#define CH_MW 4
#define CH_REC 34
#define CH_RTY 32u
#define CH_RISE 12u
#define CH_FALL 3u
#define CH_PRE 9
#define CH_T0 190
#define CH_UP 2
#define CH_WX 6
#define WAIT_TXT(tmp, t4) "s_memrealtime vcc\n\t.p2align 5\n1:\n\ts_waitcnt lgkmcnt(0)\n\ts_sub_u32 " tmp ", " t4 ", vcc_lo\n\ts_memrealtime vcc\n\ts_cmpk_lt_u32 " tmp ", 400\n\ts_cbranch_scc1 1b\n\t" \
                          "s_add_u32 " tmp ", " tmp ", 4\n\ts_cselect_b32 " tmp ", " tmp ", 0\n2:\n\ts_sub_u32 " tmp ", " tmp ", 1\n\ts_cbranch_scc0 2b\n\t"
DEV void wait_until(u32 t) { u32 tmp; asm volatile(WAIT_TXT("%0", "%1") "s_waitcnt lgkmcnt(0)" : "=&s"(tmp) : "s"(uu(t - 4u)) : "vcc", "scc", "memory"); }
#define SMF_A(acc, a, b) asm volatile("v_smfmac_f32_16x16x64_bf16 %0, %1, %2, %3" : "+v"(acc) : "v"(a), "a"(b), "v"(idx))
#define SMF_V(acc, a, b) asm volatile("v_smfmac_f32_16x16x64_bf16 %0, %1, %2, %3" : "+v"(acc) : "v"(a), "v"(b), "v"(idx))
#define MF0(d, a, b) asm volatile("v_mfma_f32_16x16x32_bf16 %0, %1, %2, 0" : "=&v"(d) : "v"(a), "v"(b))
#define MF1(d, a, b) asm volatile("v_mfma_f32_16x16x32_bf16 %0, %1, %2, %0" : "+v"(d) : "v"(a), "v"(b))
#define MF0H(d, a, b) asm volatile("v_mfma_f32_16x16x16_bf16 %0, %1, %2, 0" : "=&v"(d) : "v"(a), "v"(b))
#define MF1H(d, a, b) asm volatile("v_mfma_f32_16x16x16_bf16 %0, %1, %2, %0" : "+v"(d) : "v"(a), "v"(b))
DEV v8u tr_frag(lptr p0, lptr p1, lptr p2, lptr p3) { return cat8(tr2(p0, p1), tr2(p2, p3)); }
constexpr int qslot(int ks) { return (ks & 1) && ks < 8 ? ks >> 1 : -1; }
constexpr int kslot(int ks) { return ks < 4 ? 4 + ks : -1; }
constexpr int vslot(int ks) { return ks >= 4 ? 4 + ks : -1; }
DEV u32 ring_map(u32 off, u32 half) { return ((off >> 13) << 14) + (half << 13) + (off & 8191u); }
#define ZRD(x, i) x = __builtin_bit_cast(f32x4, lds_r16(ZP + 16 * (i)))
#define VMAX(d, a, b) asm volatile("v_max_f32 %0, %1, %2" : "=v"(d) : "v"(a), "v"(b))
#define VMAX3(d, a, b, c_) asm volatile("v_max3_f32 %0, %1, %2, %3" : "=v"(d) : "v"(a), "v"(b), "v"(c_))
#define VEXP(d, a) asm volatile("v_exp_f32 %0, %1" : "=v"(d) : "v"(a))
#define VMULC(d, a) asm volatile("v_mul_f32 %0, 0x3e0293ee, %1" : "=v"(d) : "v"(a))
#define CADD(d, a, b) do { d = (a) + (b); PIN(d); } while (0)
#define CMULC(d, a) do { d = (a) * CEXP; PIN(d); } while (0)
#define CCVT(d, a, b) do { d = pk(a, b); PIN(d); } while (0)
#define LGK(n) __builtin_amdgcn_s_waitcnt(0xC07F | (n) << 8)
#define SEL(d, a, b) asm volatile("v_cndmask_b32 %0, %1, %2, %3" : "=v"(d) : "v"(a), "v"(b), "s"(himask))
#define QPERM(d, a, q) asm volatile("v_mov_b32_dpp %0, %1 quad_perm:" q " row_mask:0xf bank_mask:0xf" : "=v"(d) : "v"(a))

#define AL_H 3
#define AL_W 0
#define HD_SMP 56
DEV void role_head(const Params& p, lptr lds, int L, int h, v2u ep) {
    const int tid = threadIdx.x, lane = tid & 63, w = uni(tid >> 6), g = lane >> 4, r = lane & 15;
    const rsrc_t rs = mk_rsrc(p.ws, W_END);
    v8u wq0[10], wq1[10], wk0[10], wk1[10], wv0[10], wv1[10];
    lptr WL = lds + C_WL + w * (NLF * 2048) + lane * 16;
    {
        constexpr int GS = 1056, CS = 48 * GS;
        const v4u sq = mk_srd(p.Wq, DM * NQ * 2), sk = mk_srd(p.Wk, DM * NQ * 2), sv = mk_srd(p.Wv, DM * NQ * 2);
        const u32 vo = (u32)(g * NQ + h * DH + 8 * (r ^ ((g & 1) << 3))) * 2u;
        auto fill = [&](int ch) {
#pragma unroll 1
            for (int i = 0; i < 4; ++i) {
                const u32 dst = uu((ch % 3) * CS + (4 * i + w) * GS), so = uu((ch * 64 + 16 * i + 4 * w) * (NQ * 2));
                DMAIN(sq, dst, vo, so); DMAIN(sk, dst + 16 * GS, vo, so); DMAIN(sv, dst + 32 * GS, vo, so);
            }
        };
        fill(0); fill(1); fill(2);
        const int q4 = r >> 2;
        lptr tb = lds + (4 * (g >> 1) + 2 * (q4 >> 1) + (g & 1)) * GS + (q4 & 1) * 256 + ((w * 64 + (r & 3) * 8) ^ ((q4 & 1) << 7));
        SFOR(ch, 10)
            VMW(ch < 8 ? 24 : 108 - 12 * ch);
            BAR();
            auto frag = [&](int m, int t) { lptr b = tb + (ch % 3) * CS + m * 16 * GS + t * 32; return tr_frag(b, b + 512, b + 8 * GS, b + 8 * GS + 512); };
            wq0[ch] = frag(0, 0); wq1[ch] = frag(0, 1); wk0[ch] = frag(1, 0); wk1[ch] = frag(1, 1); wv0[ch] = frag(2, 0); wv1[ch] = frag(2, 1);
            asm volatile("" : "+a"(wq0[ch]), "+a"(wk0[ch]), "+v"(wv0[ch]), "+v"(wv1[ch]));
            if constexpr (qslot(ch) < 0) asm volatile("" : "+a"(wq1[ch])); else asm volatile("" : "+v"(wq1[ch]));
            if constexpr (kslot(ch) < 0) asm volatile("" : "+a"(wk1[ch])); else asm volatile("" : "+v"(wk1[ch]));
            BAR();
            if constexpr (ch < 7) fill(ch + 3);
        SEND
#pragma unroll
        for (int ch = 0; ch < 10; ++ch) {
            if (qslot(ch) >= 0) { lds_w16(WL + qslot(ch) * 2048, wq1[ch].lo); lds_w16(WL + qslot(ch) * 2048 + 1024, wq1[ch].hi); }
            if (kslot(ch) >= 0) { lds_w16(WL + kslot(ch) * 2048, wk1[ch].lo); lds_w16(WL + kslot(ch) * 2048 + 1024, wk1[ch].hi); }
            if (vslot(ch) >= 0) { lds_w16(WL + vslot(ch) * 2048, wv1[ch].lo); lds_w16(WL + vslot(ch) * 2048 + 1024, wv1[ch].hi); }
        }
        CFENCE();
    }
    const u32 idx = (r & 2) ? 0xEEEEu : 0x4444u;
    unsigned long long himask = 0xCCCCCCCCCCCCCCCCull;
    PINS(himask);
    bool dead = false;
    if (h == 3) LST(266 + L, rt32());
    if (w == 0) LST(1240 + L * 32 + h, rt32());
    if (L == 1) {
        KvIn in;
        kvt_load(p, KT0 + 4 * h + w, in, [] {}); kvt_make(rs, lds + w * 9216, KT0 + 4 * h + w, in);
        DRAIN(); BAR();
        pre_post(rs, 64 + h, 3u, ep);
    }
    const u32 pol0 = pol_wait(rs, ep);
    if (h == 3 && L == 0) LST(277, rt32());
    const u32 l16 = (u32)lane * 16u;
    const u32 mring = W_MRING + (u32)L * (MB / 4) + pol0 * 8192u, hint = mring + 4 * 32768u;
    const u32 fkh = lane < 3 ? foff(F_QKV + L * 96 + lane * 32 + h) : lane < 7 ? hint + (u32)(lane - 3) * 4u : OOB;
    u32 tm = 0;
    MK_SRD();
    u32 ksrc = uu(W_KQ + (u32)(L * (NSEG - 1) * NH + h) * 16384u + w * 2048), vsrc = ksrc + (W_VO - W_KQ);
    u32 dk = uu(C_K + w * 2048), dv = dk + (C_V - C_K), dq = dk + (C_QX - C_K), dm = uu(C_MEM + w * 2048), dm2 = uu(w < 2 ? C_MEM + 8192 + w * 1024 : C_DUMP);
    asm volatile("" : "+s"(dk), "+s"(dv), "+s"(dq), "+s"(dm), "+s"(dm2));
    auto keys_wait = [&](u32 need) {
#pragma unroll 1
        for (int n = 0; !dead; ++n) {
            const bool beat = need + 2 * L > 2u;
            if (beat) wait_until(tm);
            const u32 hv = ld4(rs, lane == 7 ? foff(F_ERR) : fkh);
            if (beat) { tm = rdl(hv, 5); wait_until(rdl(hv, 3) - 30u); }
            if (((u32)__builtin_amdgcn_ballot_w64(ld4(rs, fkh) < need) & 7u) == 0u) break;
            dead = n > SPIN_CAP || rdl(hv, 7) != 0u;
            st4s(rs, dead ? foff(F_ERR) : OOB, 0, 1u);
        }
        CFENCE();
        DMA32(dk, l16, ksrc); DMA32(dv, l16, vsrc); DMA32(dq, l16, ksrc ^ 8192u);
    };
    if (L == 0) { st4s(rs, tid == 0 ? hint + 4096u + (u32)h * 128u : OOB, 0, 0u); keys_wait(1u); }
    lptr qw = lds + C_Q + (2 * g) * 288 + (32 * w + 2 * r) * 2;
    lptr kw = qw + (C_KM - C_Q); PIN(kw);
    lptr qr = lds + C_Q + (2 * (r >> 2) + (r & 1)) * 288 + g * 16;
    lptr LB = lds + l16, VX = lds + C_V + w * 2048 + l16;
    PIN(VX);
    if (tid >= 64 && tid < 70) { const u32 z = tid == 68 ? 0x3F803F80u : tid == 69 ? PSENT : 0u; lds_w16(lds + C_ZERO + (tid - 64) * 16, (v4u){z, z, z, z}); }
    u32 zaddr = 0; PIN(zaddr);
    lptr ZP = lds + zaddr + C_ZERO;
    const float CEXP = SCALE * LOG2E;
#define LF_RD(slot, hf) lf[slot][hf] = lds_r16(WL + (slot) * 2048 + (hf) * 1024)
    BAR();
    const u32 ofl = tid == 0 ? foff(F_O + L * 32 + h) : OOB;
    u32 oso = (((g | L) & 1) ? OOB : 0u) + W_ORING + (u32)L * (MB / 2) + ring_map((u32)((r >> 3) * 32768 + h * 1024 + w * 256 + (g >> 1) * 128 + (r & 7) * 16), pol0);
    unsigned long long vis[8];
#pragma unroll
    for (int i = 0; i < 8; ++i) vis[i] = __builtin_amdgcn_ballot_w64(16 * (i >> 2) + 4 * g + (i & 3) <= 16 * (w & 1) + r);
    u32 missed = 0;
    for (int s = -L; s < NSEG;) {
        u32 sv = (u32)max(s, 0), ms = mring + (sv & 3u) * 32768u + w * 2048, ms2 = mring + (sv & 3u) * 32768u + 16384u + (w & 1) * 1024;
        asm volatile("" : "+s"(ms), "+s"(ms2)); PIN(sv);
        f32x4 aq0, aq1, ak0, ak1;
        v4u lf[NLF][2];
        ZRD(aq0, 0); ZRD(aq1, 1); ZRD(ak0, 2); ZRD(ak1, 3);
#pragma unroll
        for (int i = 0; i < 4; ++i) { LF_RD(i, 0); LF_RD(i, 1); }
        SCHED();
        v4u A[10];
#define HT(k) do {} while (0)
        v4u kx[8], qf[4], kmf[4], vx[2], ones, P;
        float fa, fb, fc, fd, inv, sx[4], t[4], tm0, tm1; u32 pa, pb, fl, e01, e23, pmm;
        f32x4 av0, av1, s0, s1, sm, oa, ob, rsum;
        v2u vm, pm;
#define LW(sl) cat8(lf[sl][0], lf[sl][1])
#define PQ(ks, tl) do { if constexpr (!(tl)) SMF_A(aq0, A[ks], wq0[ks]); else if constexpr (qslot(ks) < 0) SMF_A(aq1, A[ks], wq1[ks]); else { const v8u lw_ = LW(qslot(ks)); SMF_V(aq1, A[ks], lw_); } } while (0)
#define PK(ks, tl) do { if constexpr (!(tl)) SMF_A(ak0, A[ks], wk0[ks]); else if constexpr (kslot(ks) < 0) SMF_A(ak1, A[ks], wk1[ks]); else { const v8u lw_ = LW(kslot(ks)); SMF_V(ak1, A[ks], lw_); } } while (0)
#define PV(ks, tl) do { if constexpr (!(tl)) SMF_V(av0, A[ks], wv0[ks]); else if constexpr (vslot(ks) < 0) SMF_V(av1, A[ks], wv1[ks]); else { const v8u lw_ = LW(vslot(ks)); SMF_V(av1, A[ks], lw_); } } while (0)
#define KXR(i) kx[i] = lds_r16(LB + C_K + ((i) & 1) * 4096 + ((i) >> 1) * 1024)
#define XW(q_) do { *(LAS u32*)(q_) = pa; *(LAS u32*)((q_) + 288) = pb; } while (0)
        auto slot = [&](auto zc) __attribute__((always_inline)) {
            constexpr int z = decltype(zc)::value;
            if constexpr (z < 14) PQ(z >> 1, z & 1);
            else if constexpr (z < 17) PQ(z - 7, 0);
            else if constexpr (z < 20) PQ(z - 10, 1);
            else if constexpr (z < 34) PK((z - 20) >> 1, z & 1);
            else if constexpr (z < 37) PK(z - 27, 0);
            else if constexpr (z < 40) PK(z - 30, 1);
            else if constexpr (z < 42) { if constexpr (z == 40) MF0(s0, kx[0], qf[0]); else MF0(s1, kx[1], qf[0]); }
            else if constexpr (z < 48) { if constexpr (z & 1) MF1(s1, kx[z - 40], qf[(z - 40) >> 1]); else MF1(s0, kx[z - 40], qf[(z - 40) >> 1]); }
            else if constexpr (z < 54) PV(z - 48, 0);
            else if constexpr (z == 54) MF0(sm, kmf[0], qf[0]);
            else if constexpr (z < 58) MF1(sm, kmf[z - 54], qf[z - 54]);
            else if constexpr (z < 62) PV(z - 52, 0);
            else if constexpr (z < 64) PV(z - 62, 1);
            else if constexpr (z == 64) MF0(oa, vx[0], P);
            else if constexpr (z == 65) MF0(ob, vx[1], P);
            else if constexpr (z == 66) MF0(rsum, ones, P);
            else if constexpr (z < 75) PV(z < 70 ? z - 63 : z == 70 ? 2 : z == 71 ? 7 : z == 72 ? 3 : z - 65, 1);
            else { const v2u ones2 = ones.lo; MF1H(rsum, ones2, pm); }
            if constexpr (z < 6) A[4 + z] = lds_r16(LB + C_MEM + (4 + z) * 1024);
            if constexpr (z >= 6 && z < 10) LF_RD(4 + ((z - 6) >> 1), z & 1);
            if constexpr (z >= 14 && z < 18) LF_RD(6 + ((z - 14) >> 1), z & 1);
            if constexpr (z >= 22 && z < 28) KXR(z - 22);
            if constexpr (z == 28) XW(qw);
            if constexpr (z == 29 || z == 30) KXR(z - 23);
            if constexpr (z == 32 || z == 47) asm volatile("s_barrier" ::: "memory");
            if constexpr (z >= 33 && z < 37) qf[z - 33] = lds_r16(qr + (z - 33) * 64);
            if constexpr (z == 20) ZRD(av0, 0);
            if constexpr (z == 43) XW(kw);
            if constexpr (z == 44 || z == 45) vx[z - 44] = lds_r16(VX + (z - 44) * 1024);
            if constexpr (z >= 48 && z < 52) kmf[z - 48] = lds_r16(qr + (C_KM - C_Q) + (z - 48) * 64);
            if constexpr (z == 52) ZRD(av1, 1);
            if constexpr (z == 37) ones = lds_r16(ZP + 64);
            if constexpr (z >= 57 && z < 64) LF_RD(8 + ((z - 57) >> 1), (z - 57) & 1);
            if constexpr (z >= 65 && z < 70) LF_RD(8 + ((z - 58) >> 1), (z - 58) & 1);
            if constexpr (z == HD_SMP) LD4A(fl, fkh, 0);
            SCHED();
            if constexpr (z == 19) HT(2);
            if constexpr (z == 32) HT(3);
            if constexpr (z == 47) HT(4);
            if constexpr (z == 64) HT(5);
            if constexpr (z == 19) LGK(4);
            if constexpr (z == 21 || z == 41) LGK(1);
            if constexpr (z == 56 || z == 73) LGK(0);
            if constexpr (z == 31 || z == 46) LGK(2);
            if constexpr (z == 39) LGK(4);
            if constexpr (z >= 53 && z < 56) LGK(57 - z);
            if constexpr (z == 64) LGK(3);
            if constexpr (z == 68) LGK(5);
            if constexpr (z >= 70 && z < 73) LGK(74 - z);
            SCHED();
            if constexpr (z == 18) CADD(fa, aq0[0], aq0[2]);
            if constexpr (z == 19) CADD(fb, aq0[1], aq0[3]);
            if constexpr (z == 20) CMULC(fa, fa);
            if constexpr (z == 21) CADD(fc, aq1[0], aq1[2]);
            if constexpr (z == 22) CADD(fd, aq1[1], aq1[3]);
            if constexpr (z == 23) CMULC(fc, fc);
            if constexpr (z == 24) CMULC(fb, fb);
            if constexpr (z == 25) CMULC(fd, fd);
            if constexpr (z == 26) CCVT(pa, fa, fc);
            if constexpr (z == 27) CCVT(pb, fb, fd);
            if constexpr (z == 38) CADD(fa, ak0[0], ak0[2]);
            if constexpr (z == 39) CADD(fb, ak0[1], ak0[3]);
            if constexpr (z == 41) { CADD(fc, ak1[0], ak1[2]); CADD(fd, ak1[1], ak1[3]); }
            if constexpr (z == 42) { CCVT(pa, fa, fc); CCVT(pb, fb, fd); }
            if constexpr (z >= 49 && z < 53) SEL(sx[z - 49], s0[z - 49], s1[z - 49]);
            if constexpr (z >= 53 && z < 57) VEXP(t[z - 53], sx[z - 53]);
            if constexpr (z == 57) CCVT(e01, t[0], t[1]);
            if constexpr (z == 58) CCVT(e23, t[2], t[3]);
            if constexpr (z == 59) QPERM(P.x, e01, "[0,1,0,1]");
            if constexpr (z == 60) QPERM(P.z, e01, "[2,3,2,3]");
            if constexpr (z == 61) QPERM(P.y, e23, "[0,1,0,1]");
            if constexpr (z == 62) QPERM(P.w, e23, "[2,3,2,3]");
            if constexpr (z == 63) CADD(fa, av0[0], av0[2]);
            if constexpr (z == 64) { asm volatile("" :: "v"(sm)); VEXP(tm0, sm[0]); }
            if constexpr (z == 65) VEXP(tm1, sm[1]);
            if constexpr (z == 66) CADD(fb, av0[1], av0[3]);
            if constexpr (z == 67) CCVT(pmm, tm0, tm1);
            if constexpr (z >= 68 && z < 72) SEL(oa[z - 68], oa[z - 68], ob[z - 68]);
            if constexpr (z == 72) asm volatile("v_cndmask_b32 %0, %1, 0, %2" : "=v"(pm.x) : "v"(pmm), "s"(himask));
            if constexpr (z == 73) asm volatile("v_cndmask_b32 %0, 0, %1, %2" : "=v"(pm.y) : "v"(pmm), "s"(himask));
            if constexpr (z == 74) LGK(15);
            if constexpr (z == 75) CCVT(vm.x, fa, fb);
            SCHED();
        };
        {
            u32 tmp;
            asm volatile("s_mov_b32 m0, %2\n\t" WAIT_TXT("%0", "%1") "buffer_load_dwordx4 %4, %5, %6 offen sc1 lds\n\tbuffer_load_dwordx4 %4, %5, %6 offen offset:1024 sc1 lds\n\t"
                         "s_mov_b32 m0, %3\n\ts_nop 0\n\tbuffer_load_dwordx4 %4, %5, %7 offen sc1 lds\n\t.p2align 6\n\t.rept %8\n\ts_nop 0\n\t.endr\n\ts_waitcnt vmcnt(0) lgkmcnt(0)\n\ts_barrier"
                         : "=&s"(tmp) : "s"(uu(tm - 4u)), "s"(dm), "s"(dm2), "v"(l16), "s"(srd), "s"(ms), "s"(ms2), "i"(AL_H) : "vcc", "scc", "memory");
        }
        HT(1);
#pragma unroll
        for (int ks = 0; ks < 4; ++ks) A[ks] = lds_r16(LB + C_MEM + ks * 1024);
        SCHED();
        st4s(rs, ofl, 0, sv);
        SCHED();
        static_for<0, 76>(slot);
        asm volatile("s_nop 2\n\tv_add_f32 %1, %3, %5\n\tv_add_f32 %2, %4, %6\n\tv_cvt_pk_bf16_f32 %0, %1, %2"
                     : "=v"(vm.y), "=&v"(fc), "=&v"(fd) : "v"(av1[0]), "v"(av1[1]), "v"(av1[2]), "v"(av1[3]));
        __builtin_amdgcn_s_waitcnt(0xCF7F);
        asm volatile("s_nop 0\n\tv_mfma_f32_16x16x16_bf16 %0, %2, %3, %0\n\tv_rcp_f32 %1, %4" : "+v"(oa), "=&v"(inv) : "v"(vm), "v"(pm), "v"(rsum[0]), "v"(rsum));
        SCHED();
        if (__builtin_expect(__builtin_amdgcn_ballot_w64(u_(rsum[0]) - 0x21800000u > 0x3C000000u) != 0ull, 0)) {
            DRAIN(); SCHED();
            bool miss = false;
#pragma unroll 1
            for (int i = 0; i < 10; ++i) { const v4u a = lds_r16(LB + C_MEM + i * 1024); miss |= a.x == PSENT || a.y == PSENT || a.z == PSENT || a.w == PSENT; }
            if (missed < SPIN_CAP && !all_(!miss)) {
                *(LAS u32*)(lds + C_DUMP + (w == 0 ? 0 : 64)) = ld4(rs, foff(F_ERR));
                asm volatile("" : "+v"(fl) :: "memory");
                tm = rdl(fl, 5);
                BAR();
                missed = uu(*(LAS u32*)(lds + C_DUMP)) != 0u ? SPIN_CAP : missed + 1;
                st4s(rs, missed == SPIN_CAP ? foff(F_ERR) : OOB, 0, 1u);
                BAR();
                continue;
            }
            float mx = fmaxf(fmaxf(fmaxf(sx[0], sx[1]), fmaxf(sx[2], sx[3])), fmaxf(sm[0], sm[1]));
            mx = max32(max16(fmaxf(mx, dpp<0x4E>(mx))));
#pragma unroll
            for (int i = 0; i < 4; ++i) t[i] = __builtin_amdgcn_exp2f(sx[i] - mx);
            e01 = pk(t[0], t[1]); e23 = pk(t[2], t[3]);
            pmm = pk(__builtin_amdgcn_exp2f(sm[0] - mx), __builtin_amdgcn_exp2f(sm[1] - mx));
            asm volatile("v_cndmask_b32 %0, %2, 0, %3\n\tv_cndmask_b32 %1, 0, %2, %3" : "=&v"(pm.x), "=&v"(pm.y) : "v"(pmm), "s"(himask));
            asm volatile("s_nop 1" : "+v"(e01), "+v"(e23), "+v"(pm));
            QPERM(P.x, e01, "[0,1,0,1]"); QPERM(P.z, e01, "[2,3,2,3]"); QPERM(P.y, e23, "[0,1,0,1]"); QPERM(P.w, e23, "[2,3,2,3]");
            NOPS(1);
            MF0(rsum, ones, P); MF0(oa, vx[0], P); MF0(ob, vx[1], P);
            NOPS(7);
            { const v2u ones2 = ones.lo; MF1H(rsum, ones2, pm); }
#pragma unroll
            for (int i = 0; i < 4; ++i) SEL(oa[i], oa[i], ob[i]);
            NOPS(1);
            MF1H(oa, vm, pm);
            asm volatile("s_nop 7\n\tv_rcp_f32 %0, %1" : "=v"(inv) : "v"(rsum[0]), "v"(rsum));
        }
        {
            v4u oc; f32x2 u0 = oa.lo, u1 = oa.hi; const f32x2 i2 = {inv, inv};
            asm volatile("s_nop 0\n\tv_pk_mul_f32 %0, %0, %2\n\tv_pk_mul_f32 %1, %1, %2" : "+v"(u0), "+v"(u1) : "v"(i2));
            __builtin_amdgcn_s_waitcnt(0xCF7F);
            asm volatile("v_cvt_pk_bf16_f32 %0, %4, %5\n\tv_cvt_pk_bf16_f32 %2, %4, %5\n\tv_cvt_pk_bf16_f32 %1, %6, %7\n\tv_cvt_pk_bf16_f32 %3, %6, %7\n\t"
                         "v_permlane16_swap_b32 %0, %2\n\ts_nop 0\n\tv_permlane16_swap_b32 %1, %3"
                         : "=&v"(oc.x), "=&v"(oc.y), "=&v"(oc.z), "=&v"(oc.w) : "v"(u0.x), "v"(u0.y), "v"(u1.x), "v"(u1.y));
            st16(rs, oso, oc);
        }
        SCHED();
        unsigned long long clk;
        asm volatile("s_memrealtime %0" : "=s"(clk) :: "memory");
        HT(6);
        v4u tq[4], tk[8], tv[2];
        f32x4 c0, c1, cm;
        u32 oo, kvo; bool kvok;
        u32 ln = l16; PIN(ln);
        lptr QX = lds + C_QX + (w & 1) * 4096 + ln, VO = lds + C_V + (w ^ 1) * 2048 + ln, VMX = lds + C_VM + w * 512 + (ln >> 1);
#pragma unroll
        for (int i = 0; i < 2; ++i) { tq[i] = lds_r16(QX + i * 1024); kmf[i] = lds_r16(qr + (C_KM - C_Q) + i * 64); tk[2 * i] = lds_r16(LB + C_K + i * 1024); tk[2 * i + 1] = lds_r16(LB + C_K + 4096 + i * 1024); }
        *(LAS v2u*)VMX = vm;
        SCHED();
        SFOR(i, 12)
            if constexpr (i == 0) MF0(cm, kmf[0], tq[0]); else if constexpr (i == 1) MF0(c0, tk[0], tq[0]); else if constexpr (i == 2) MF0(c1, tk[1], tq[0]);
            else if constexpr (i % 3 == 0) MF1(cm, kmf[i / 3], tq[i / 3]); else if constexpr (i % 3 == 1) MF1(c0, tk[2 * (i / 3)], tq[i / 3]); else MF1(c1, tk[2 * (i / 3) + 1], tq[i / 3]);
            if constexpr (i < 2) kmf[2 + i] = lds_r16(qr + (C_KM - C_Q) + (2 + i) * 64);
            if constexpr (i == 0) { tq[2] = lds_r16(QX + 2048); tk[4] = lds_r16(LB + C_K + 2048); }
            if constexpr (i == 1) { tk[5] = lds_r16(LB + C_K + 6144); tq[3] = lds_r16(QX + 3072); }
            if constexpr (i == 2) { tk[6] = lds_r16(LB + C_K + 3072); tk[7] = lds_r16(LB + C_K + 7168); }
            if constexpr (i == 3) { tv[0] = lds_r16(VO); tv[1] = lds_r16(VO + 1024); }
            if constexpr (i == 4) { oo = ln + (ln & 0x300u) + ((vsrc ^ 8192u) + (u32)((w & 1) * 256) + ((u32)s & OOB)); PIN(oo); }
            if constexpr (i == 5) { ksrc += NH * 16384; vsrc += NH * 16384; PINS(ksrc); PINS(vsrc); }
            if constexpr (i == 9) { asm volatile("s_waitcnt vmcnt(1)" : "+v"(fl) :: "memory"); kvok = ((u32)__builtin_amdgcn_ballot_w64(fl < (u32)s + 2) & 7u) == 0u; }
            if constexpr (i == 10) { kvo = l16 | (kvok ? 0u : OOB); PIN(kvo); }
            SCHED();
        SEND
        asm volatile("s_waitcnt lgkmcnt(0)\n\ts_barrier" : "+s"(clk) :: "memory");
        const v2u tvm = *(LAS v2u*)(VMX + (512 - (w & 1) * 1024));
        const v4u pv = lds_r16(ZP + 80);
        DMA32(dk, kvo, ksrc);
        SCHED(); asm volatile("" :: "v"(cm));
        float u[10], m, m2;
        {
            float NEG = -1000000000.0f / SCALE; PIN(NEG);
#define MSK(d, a, i) asm volatile("v_cndmask_b32 %0, %1, %2, %3" : "=v"(d) : "v"(NEG), "v"(a), "s"(vis[i]))
#pragma unroll
            for (int i = 0; i < 4; ++i) MSK(u[i], c0[i], i);
#pragma unroll
            for (int i = 0; i < 4; ++i) MSK(u[4 + i], c1[i], 4 + i);
            asm volatile("v_max3_f32 %0, %2, %3, %10\n\tv_max3_f32 %1, %4, %5, %11\n\tv_max3_f32 %0, %0, %6, %7\n\tv_max3_f32 %1, %1, %8, %9\n\t"
                         "v_max_f32 %0, %0, %1\n\tv_mov_b32 %1, %0\n\ts_nop 1\n\tv_permlane16_swap_b32 %0, %1\n\tv_max_f32 %0, %0, %1\n\tv_mov_b32 %1, %0\n\ts_nop 1\n\t"
                         "v_permlane32_swap_b32 %0, %1\n\tv_max_f32 %0, %0, %1\n\tv_mul_f32 %0, 0xbe0293ee, %0"
                         : "=&v"(m), "=&v"(m2) : "v"(u[0]), "v"(u[1]), "v"(u[2]), "v"(u[3]), "v"(u[4]), "v"(u[5]), "v"(u[6]), "v"(u[7]), "v"(cm[0]), "v"(cm[1]));
        }
        DMA32(dv, kvo, vsrc);
        {
            v4u px; v2u p0, p1;
#define EX2(d, a, b) asm volatile("v_fmamk_f32 %0, %2, 0x3e0293ee, %4\n\tv_fmamk_f32 %1, %3, 0x3e0293ee, %4\n\tv_exp_f32 %0, %0\n\tv_exp_f32 %1, %1" : "=&v"(d.x), "=&v"(d.y) : "v"(a), "v"(b), "v"(m))
            f32x2 ex[5];
            EX2(ex[0], u[0], u[1]); EX2(ex[1], u[2], u[3]); EX2(ex[2], u[4], u[5]); EX2(ex[3], u[6], u[7]); EX2(ex[4], cm[0], cm[1]);
            asm volatile("v_cvt_pk_bf16_f32 %0, %8, %9\n\tv_cvt_pk_bf16_f32 %1, %10, %11\n\tv_cvt_pk_bf16_f32 %2, %12, %13\n\tv_cvt_pk_bf16_f32 %3, %14, %15\n\t"
                         "v_mov_b32 %5, 0\n\tv_mov_b32 %6, 0\n\tv_cvt_pk_bf16_f32 %4, %16, %17\n\tv_cvt_pk_bf16_f32 %7, %16, %17"
                         : "=&v"(px.x), "=&v"(px.y), "=&v"(px.z), "=&v"(px.w), "=&v"(p0.x), "=&v"(p0.y), "=&v"(p1.x), "=&v"(p1.y)
                         : "v"(ex[0].x), "v"(ex[0].y), "v"(ex[1].x), "v"(ex[1].y), "v"(ex[2].x), "v"(ex[2].y), "v"(ex[3].x), "v"(ex[3].y), "v"(ex[4].x), "v"(ex[4].y));
            f32x4 o[4], sum;
            const v2u ones2 = ones.lo;
            const u32 now = (u32)clk;
            u32 oz;
            SCHED();
            MF0(sum, ones, px); { const u32 q = ksrc ^ 8192u; DMA32(dq, kvo, q); } SCHED();
            MF0(o[0], vx[0], px);
            MF0(o[1], vx[1], px);
            MF0(o[2], tv[0], px); oz = (oso & ~(3u << 17)) | (u32)((s + 3) & 3) << 17; PIN(oz); SCHED();
            MF0(o[3], tv[1], px); oso = (oso & ~(3u << 17)) | (u32)((s + 1) & 3) << 17; PIN(oso); SCHED();
            MF1H(sum, ones2, p0);
            MF1H(o[0], vm, p0);
            MF1H(o[1], vm, p1); st16(rs, oz, pv); SCHED();
            MF1H(o[2], tvm, p0);
            MF1H(o[3], tvm, p1);
            asm volatile("s_nop 2\n\tv_rcp_f32 %0, %1\n\ts_nop 0" : "=v"(inv) : "v"(sum[0]), "v"(sum));
            asm volatile("" : "+v"(o[0]), "+v"(o[1]));
            st16(rs, oo, (v4u){pk2(o[0].lo * inv), pk2(o[0].hi * inv), pk2(o[1].lo * inv), pk2(o[1].hi * inv)});
            asm volatile("" : "+v"(o[2]), "+v"(o[3]));
            st16(rs, oo ^ 2048u, (v4u){pk2(o[2].lo * inv), pk2(o[2].hi * inv), pk2(o[3].lo * inv), pk2(o[3].hi * inv)});
            SCHED();
            LS2(L == 0 && w == 0, h);
            st4s(rs, ((h & 15) == 0 && tid == 192) ? hint + 128u + (u32)h * 8u : OOB, 0, (u32)s << 16 | min(missed, 1u) << 14 | min(now - tm, 0x3FFFu));
            tm = rdl(fl, 5);
            missed = missed == SPIN_CAP ? missed : 0u;
            if (__builtin_expect(s < 1 - L, 0)) {
                if (s < 0) oso -= (ln & 256u) ? 0u : OOB;
#pragma unroll 1
                for (int n = 0; n < (s < 0 ? 0 : 200); ++n) {
                    CFENCE();
                    tm = rdl(ld4(rs, fkh), 6);
                    if (tm != PSENT) break;
                }
            }
        }
        if (__builtin_expect(!kvok, 0) && s + 1 < NSEG) {
            st4s(rs, ln == 0 ? hint + 4096u + (u32)h * 128u : OOB, 0, (u32)s + 1u);
            keys_wait((u32)s + 2u);
        }
        ++s;
    }
    DRAIN(); BAR();
    st4s(rs, ofl, 0, NSEG);
    LDUMP(w == 0, 160 + L * 32 + h);
}

#define WO_NL 3
#define WO_ROT 0
#define WO_AH 1
DEV void role_wo(const Params& p, lptr lds, int j, v2u ep) {
    const int tid = threadIdx.x, lane = tid & 63, w = uni(tid >> 6), g = lane >> 4, r = lane & 15;
    const int rg = j & 1, cg = j >> 1, rot = WO_ROT;
    const rsrc_t rs = mk_rsrc(p.ws, W_END);
    v8u wo[8][5];
    {
        const v4u so_ = mk_srd(p.Wo, DM * NQ * 2);
        u32 vo[5];
#pragma unroll
        for (int i = 0; i < 5; ++i) { const int n = 64 * i + lane; vo[i] = (u32)((n / 5) * DM + cg * 40 + (n % 5) * 8) * 2u; }
        v4u junk = {};
        const u32 av = (u32)(j * 5120 + lane * 16);
        auto ahead = [&](int b) {
#pragma unroll
            for (int i = 0; i < 5; ++i) AHEAD(so_, av, uu((8 * w + b) * (DH * DM * 2) + i * 1024));
        };
        auto fill = [&](int b) {
            if (b + WO_AH < 8) ahead(b + WO_AH);
            const u32 dst = uu(w * 40960 + (b & 3) * 10240), src = uu((8 * w + ((b + rot) & 7)) * (DH * DM * 2));
#pragma unroll
            for (int i = 0; i < 10; ++i) DMAIN(so_, dst + i * 1024, vo[i % 5], src + (i / 5) * (64 * DM * 2));
        };
#pragma unroll 1
        for (int b = 0; b < WO_AH; ++b) ahead(b);
#pragma unroll 1
        for (int b = 0; b < 4; ++b) fill(b);
        const int q4 = r >> 2;
        lptr tb = lds + w * 40960 + (32 * (g >> 1) + 16 * (q4 >> 1) + 8 * ((r >> 1) & 1) + 4 * (g & 1) + (q4 & 1)) * 80 + (r & 1) * 8;
        SFOR(b, 8)
            VMW((b < 5 ? 30 : 70 - 10 * b) + (b + 1 + WO_AH < 8 ? 5 : 0) + (b + 2 + WO_AH < 8 ? 5 : 0) + (b + 3 + WO_AH < 8 ? 5 : 0));
            if constexpr (b + WO_AH == 7) PIN(junk);
#pragma unroll
            for (int t = 0; t < 5; ++t) {
                lptr q = tb + (b & 3) * 10240 + t * 16;
                wo[b][t] = tr_frag(q, q + 160, q + 64 * 80, q + 64 * 80 + 160);
                if (b * 5 + t < 32) asm volatile("" : "+a"(wo[b][t])); else asm volatile("" : "+v"(wo[b][t]));
            }
            CFENCE();
            if constexpr (b < 4) fill(b + 4);
        SEND
    }
    if (j == 0) LST(265, rt32());
    LST(1100 + j * 4 + w, rt32());
    const u32 idx = (r & 2) ? 0xEEEEu : 0x4444u;
    const u32 pol0 = pol_wait(rs, ep);
    BAR();
    const u32 l16 = (u32)lane * 16u;
    u32 oo[8];
#pragma unroll
    for (int i = 0; i < 8; ++i) oo[i] = uu(W_ORING + ring_map((u32)(rg * 32768 + (8 * w + ((i + rot) & 7)) * 1024), pol0));
    const u32 hint = W_MRING + 4 * 32768u + pol0 * 8192u;
    u32 hvo[2];
#pragma unroll
    for (int l = 0; l < 2; ++l) hvo[l] = lane < 2 ? hint + (u32)(l ^ 1) * (MB / 4) + (u32)lane * 4u : lane == 2 ? hint + (u32)l * (MB / 4) + 128u : lane == 3 ? foff(F_ERR) : lane == 4 ? hint + (u32)l * (MB / 4) + 256u :
                 (lane >= 32 && j == 0 && w == 3) ? hint + (u32)l * (MB / 4) + (u32)lane * 128u : OOB;
    lptr xw = r < 8 ? lds + w * 640 + (2 * (g & 1) + (g >> 1)) * 32 + r * 4 : lds + 8192 + lane * 4;
    const int G = ((5 * cg) >> 1) + w, tt = 2 * G + ((lane >> 3) & 1) - 5 * cg;
    const bool pub = w < 3 && lane < 32 && tt >= 0 && tt < 5;
    lptr xr = lds + (pub ? tt * 128 + (2 * (lane >> 4) + ((lane >> 2) & 1)) * 32 + (lane & 3) * 8 : 0);
    const u32 mpo = pub ? W_MRING + ring_map((u32)(G * 256 + rg * 128 + lane * 4), pol0) : OOB;
    constexpr u32 PER0 = CH_W0 + CH_MW + CH_H0 + CH_MH + 2 * CH_VIS, DW0 = CH_W0 + CH_VIS + CH_MW + CH_REC;
    const bool lead = j == 0 && w == 3;
    const u32 hso = (lead && lane == 0) ? hint : OOB;
    MK_SRD();
    u32 t = 0, tn = 0;
    int sn[2] = {0, 0};
    u32 hav = 8 * CH_H0, wav = 8 * CH_W0, mh = 8 * CH_MH, mw = 8 * CH_MW, ok[2] = {0u, 0u}, dw[2] = {DW0, DW0};
    u32 vo = l16, early = PSENT, tp = 0, fin, left, cnt = 0;
    unsigned long long clk = 0;
    {
        if (w == 0) LST(1310 + j, rt32());
        if (lead) {
#pragma unroll 1
            for (int n = 0; n < SPIN_CAP; ++n) {
                CFENCE();
                const u32 a = ld4(rs, foff(F_QKV + lane)), b = ld4(rs, lane < 32 ? foff(F_QKV + 64 + lane) : hvo[0]);
                if (all_(a != 0u && b != (lane < 32 ? 0u : PSENT))) break;
                __builtin_amdgcn_s_sleep(1);
            }
        }
        if (lead) {
            t = rt32() + CH_T0; tn = t + PER0 / 2 + CH_REC;
            st16(rs, lane < 2 ? hint + (u32)lane * (MB / 4) : OOB, (v4u){lane == 0 ? t : tn, PER0, (lane == 0 ? t : tn) + DW0, t + DW0});
        } else {
#pragma unroll 1
            for (int n = 0; n < SPIN_CAP; ++n) {
                CFENCE();
                const u32 hv = ld4(rs, hvo[1]);
                t = rdl(hv, 0);
                if (rdl(hv, 1) < 400u) break;
                __builtin_amdgcn_s_sleep(1);
            }
        }
        clk = t - 100u;
    }
    const u32 tm0 = t + DW0;
    u32 zero = 0, hv = 0xFFFFu, pst = 1, pmp = OOB;
    const u32 psent = PSENT;
    lptr sw = lds + 2560 + w * 4;
    asm volatile("" : "+v"(zero), "+v"(sw));
    auto turn = [&](auto lc) __attribute__((always_inline)) {
        constexpr int L = decltype(lc)::value, M = L ^ 1;
        u32 done, rep, rd, lost, dh, dn, nt, za, pdur, mp; v4u hw;
        const int sc = sn[L];
        auto books = [&](auto ic) __attribute__((always_inline)) {
            constexpr int i = decltype(ic)::value;
            if constexpr (i == 0) { rep = max(rdl(hv, 2), rdl(hv, 4)); PINS(rep); }
            if constexpr (i == 1) { lost = (u32)(__builtin_amdgcn_ballot_w64(hv == (u32)sn[M]) >> 32); PINS(lost); }
            if constexpr (i == 2) { lost |= (rep >> 16) ^ (u32)sn[M]; PINS(lost); }
            if constexpr (i == 3) { lost |= ok[M] ^ 2u; rd = rep & 0xFFFFu; PINS(lost); PINS(rd); }
            if constexpr (i == 4) { lost = lost == 0u ? CH_UP * 8u : 0u; PINS(lost); }
            if constexpr (i == 5) { pdur = ((u32)clk - tp) * 8u; PINS(pdur); pdur = min(pdur, wav + CH_RISE); PINS(pdur); }
            if constexpr (i == 6) { pdur = (done & ok[M]) ? pdur : wav; PINS(pdur); }
            if constexpr (i == 7) { wav = max(pdur, wav - CH_FALL); PINS(wav); }
            if constexpr (i == 8) { nt = min(rd * 8u, hav + CH_RISE); PINS(nt); }
            if constexpr (i == 9) { nt = rd < 0x3FFFu ? nt : hav; PINS(nt); }
            if constexpr (i == 10) { hav = max(nt, hav - CH_FALL); PINS(hav); }
            if constexpr (i == 11) { mw = max(mw - 1u, CH_MW * 8u); PINS(mw); }
            if constexpr (i == 12) { mh = max(mh - 1u, CH_MH * 8u); PINS(mh); }
            if constexpr (i == 13) { rd = rd >> 14 == 1u ? lost : 0u; PINS(rd); }
            if constexpr (i == 14) { mw += rd; mh += lost - rd; PINS(mw); PINS(mh); }
            if constexpr (i == 15) { ok[M] = done ? 1u : (ok[M] & 1u) << 1; PINS(ok[M]); }
            if constexpr (i == 16) { dh = (hav + mh) / 8u + (sn[1] < 3 ? CH_VIS + CH_PRE : CH_VIS); PINS(dh); }
            if constexpr (i == 17) { dn = (wav + mw) / 8u + CH_VIS; PINS(dn); }
            if constexpr (i == 18) { nt = dw[L] + dh + (u32)max((int)((u32)clk + CH_WX - t), 0); PINS(nt); }
            if constexpr (i == 19) { dh += dn; dw[L] = dn; sn[M] += (int)done; PINS(dh); PINS(sn[M]); }
            if constexpr (i == 20) { rd = tn - t + (dh >> 1); PINS(rd); }
            if constexpr (i == 21) { lost = (u32)sc - 1u < NSEG - 2 ? CH_RTY : 0u; PINS(lost); nt += ok[L] != 1u ? lost : 0u; PINS(nt); }
            if constexpr (i == 22) { nt = t + (u32)max((int)nt, (int)rd); PINS(nt); }
            if constexpr (i == 23) { hw.x = nt; hw.y = dh; PIN(hw.x); PIN(hw.y); }
            if constexpr (i == 24) { hw.z = nt + dn; hw.w = tm0; PIN(hw); }
            if constexpr (i == 25) st16(rs, hso + L * (MB / 4), hw);
        };
        {
#define WT(k) do {} while (0)
            WT(0);
            v4u d[8];
            {
                u32 tmp;
                asm volatile(WAIT_TXT("%1", "%2") "buffer_load_dwordx4 %0, %3, %4, %5 offen sc1\n\ts_waitcnt lgkmcnt(0)"
                             : "=&v"(d[0]), "=&s"(tmp) : "s"(uu(t - 4u)), "v"(vo), "s"(srd), "s"(oo[0]) : "vcc", "scc", "memory");
                PINS(clk);
            }
            SFOR(i, 7)
                asm volatile("buffer_load_dwordx4 %0, %1, %2, %3 offen sc1" : "=v"(d[i + 1]) : "v"(vo), "s"(srd), "s"(oo[i + 1]) : "memory");
                SCHED();
                if constexpr (i == 0) { done = uu(pst) ^ 1u; PINS(done); }
                static_for<3 * i, 3 * i + 3>(books);
                SCHED();
            SEND
            LD4A(hv, hvo[L], 0);
            SCHED();
            WT(1);
            static_for<21, 26>(books);
            za = done ? pmp : OOB;
            st4s(rs, za, 0, psent);
            *(LAS u32*)(sw + L * 4096) = zero;
            f32x4 acc[5] = {};
            mp = mpo + (L * (MB / 4) + (u32)((sc + 1) & 3) * 32768u); pmp = mpo + (L * (MB / 4) + (u32)(sc & 3) * 32768u);
            asm volatile("" : "+v"(acc[0]), "+v"(acc[1]), "+v"(acc[2]), "+v"(acc[3]), "+v"(acc[4]), "+v"(mp), "+v"(pmp));
            SCHED();
            float f0[5], f1[5]; unsigned long long bad;
#define WSW(o_) asm volatile("v_permlane32_swap_b32 %0, %1" : "+v"(f0[o_]), "+v"(f1[o_]))
#define WAD(o_) asm volatile("v_add_f32_dpp %0, %1, %0 row_ror:8 row_mask:0xf bank_mask:0xf" : "+v"(f0[o_]) : "v"(f1[o_]))
#define WFO(o_) do { VADD(f0[o_], acc[o_][0], acc[o_][2]); VADD(f1[o_], acc[o_][1], acc[o_][3]); } while (0)
#define WWR(o_) *(LAS float*)(xw + L * 4096 + (o_) * 128) = f0[o_]
            u32 th, per, so, erl = early;
            auto next = [&](auto ic_) __attribute__((always_inline)) {
                constexpr int i = decltype(ic_)::value;
                if constexpr (i == 0) { so = (u32)sn[M] << 17; PINS(so); }
                if constexpr (i == 1) { so = (so & 0x60000u) + M * (MB / 2); PINS(so); }
                if constexpr (i == 2) { so = sn[M] + 1 < NSEG ? so : OOB; PINS(so); }
                if constexpr (i == 3) { early = sn[M] + 1 < NSEG ? PSENT : 0u; PIN(early); }
                if constexpr (i == 4) { vo = l16 + so; PIN(vo); }
                if constexpr (i == 5 && L) { fin = (u32)(sn[0] + sn[1] + 2); PINS(fin); }
                if constexpr (i == 6 && L) { fin = (fin | uu(left)) >> 7; PIN(fin); }
                if constexpr (i == 6 && !L) { cnt += 1u; PIN(cnt); left = cnt >> 9; PIN(left); }
                if constexpr (i == 7) { PIN(hv); th = rdl(hv, 0); PINS(th); }
                if constexpr (i == 8) { per = rdl(hv, 1); PINS(per); so = th - t; PINS(so); }
                if constexpr (i == 9) { per = (int)so < 20 ? per : 0u; PINS(per); }
                if constexpr (i == 10) { th += per; PINS(th); if constexpr (L) { per = rdl(hv, 3); PINS(per); } }
                if constexpr (i == 11) { tp = t; t = lead ? tn : th; PINS(t); }
                if constexpr (i == 12) { if constexpr (L) { fin |= per; PIN(fin); } tn = nt; }
            };
            auto slot = [&](auto ic_) __attribute__((always_inline)) {
                constexpr int i = decltype(ic_)::value;
                constexpr int NL = WO_NL, I0 = 40 - 5 * NL, pc = i < I0 ? i / 5 : 8 - NL + (i - I0) % NL, oc = i < I0 ? i % 5 : (i - I0) / NL;
                if constexpr (oc == 0) asm volatile("s_waitcnt vmcnt(%1)" : "+v"(d[pc]) : "i"(10 - pc));
                if constexpr (pc * 5 + oc < 32) SMF_A(acc[oc], d[pc], wo[pc][oc]); else SMF_V(acc[oc], d[pc], wo[pc][oc]);
                SCHED();
                if constexpr (i == 0) WT(2);
                if constexpr (i >= 5 && i < 13 && i != 9) next(IC<i - 5 - (i > 9)>{});
                if constexpr (i == 20) WT(4);
                constexpr int e = i - (I0 + NL + 1), o = e < 0 ? 0 : e / 3;
                if constexpr (e >= 0 && e % 3 == 0) { CADD(f0[o], acc[o][0], acc[o][2]); if constexpr (o > 0) WAD(o - 1); }
                if constexpr (e >= 0 && e % 3 == 1) { if constexpr (o > 0) { WWR(o - 1); SCHED(); } CADD(f1[o], acc[o][1], acc[o][3]); }
                if constexpr (e >= 0 && e % 3 == 2) {
                    if constexpr (o == 0) asm volatile("v_cmp_u_f32 %0, %1, %2" : "=s"(bad) : "v"(f0[0]), "v"(f1[0]));
                    else LGK(15);
                    WSW(o);
                }
                SCHED();
            };
            asm volatile(".rept %0\n\ts_nop 0\n\t.endr" :: "i"(AL_W));
            static_for<0, 2>(slot);
            if (__builtin_expect(__builtin_amdgcn_ballot_w64(d[0].x == erl) != 0ull, 0)) {
                asm volatile("s_waitcnt vmcnt(3)" :: "v"(d[1]), "v"(d[2]), "v"(d[3]), "v"(d[4]), "v"(d[5]), "v"(d[6]), "v"(d[7]) : "memory");
                SCHED();
                *(LAS u32*)(sw + L * 4096) = 1u;
                static_for<0, 7>(next);
            } else {
            static_for<2, 40>(slot);
            WT(5);
            static_assert(WO_NL == 3, "tail");
            if (__builtin_expect(bad != 0ull, 0)) {
                bool miss = false;
#pragma unroll
                for (int i = 0; i < 8; ++i) miss |= d[i].x == PSENT;
                if (all_(!miss)) {
#pragma unroll
                    for (int i = 0; i < 8; ++i) miss |= d[i].y == PSENT || d[i].z == PSENT || d[i].w == PSENT;
                }
                *(LAS u32*)(sw + L * 4096) = all_(!miss) ? 0u : 1u;
            }
            asm volatile("v_permlane32_swap_b32 %0, %1\n\ts_nop 1\n\tv_add_f32_dpp %0, %1, %0 row_ror:8 row_mask:0xf bank_mask:0xf\n\tv_add_f32 %2, %4, %6\n\tv_add_f32 %3, %5, %7"
                         : "+v"(f0[3]), "+v"(f1[3]), "=&v"(f0[4]), "=&v"(f1[4]) : "v"(acc[4][0]), "v"(acc[4][1]), "v"(acc[4][2]), "v"(acc[4][3]));
            WWR(3); SCHED();
            asm volatile("s_nop 0\n\tv_permlane32_swap_b32 %0, %1\n\ts_nop 1\n\tv_add_f32_dpp %0, %1, %0 row_ror:8 row_mask:0xf bank_mask:0xf" : "+v"(f0[4]), "+v"(f1[4]));
            WWR(4);
            }
            WT(6);
            f32x2 a, b, c, e; u32 mq; v4u sv;
            asm volatile("s_waitcnt lgkmcnt(0)\n\ts_barrier\n\tds_read_b128 %4, %6 offset:%7+2560\n\t"
                         "ds_read_b64 %0, %5 offset:%7\n\tds_read_b64 %1, %5 offset:%7+640\n\tds_read_b64 %2, %5 offset:%7+1280\n\tds_read_b64 %3, %5 offset:%7+1920"
                         : "=&v"(a), "=&v"(b), "=&v"(c), "=&v"(e), "=&v"(sv) : "v"(xr), "v"(zero), "i"(L * 4096) : "memory");
            asm volatile("s_waitcnt lgkmcnt(4)\n\tv_or_b32 %4, %6, %7\n\tv_or3_b32 %4, %4, %8, %9\n\tv_lshl_or_b32 %5, %4, 31, %10\n\t"
                         "s_waitcnt lgkmcnt(2)\n\tv_pk_add_f32 %0, %0, %1\n\ts_waitcnt lgkmcnt(0)\n\tv_pk_add_f32 %2, %2, %3\n\ts_nop 0\n\tv_pk_add_f32 %0, %0, %2"
                         : "+v"(a), "+v"(b), "+v"(c), "+v"(e), "=&v"(pst), "=&v"(mq) : "v"(sv.x), "v"(sv.y), "v"(sv.z), "v"(sv.w), "v"(mp) : "memory");
            st4s(rs, mq, 0, pkv(a));
            SCHED();
            asm volatile("s_memrealtime %0" : "=s"(clk));
            VMW(3); SCHED();
            static_for<7, 13>(next);
            WT(7);
        }
    };
    do {
        turn(IC<0>{});
        turn(IC<1>{});
    } while (uu(fin) == 0u);
    if (sn[0] + 1 < NSEG || sn[1] + 1 < NSEG) st4s(rs, tid == 0 ? foff(F_ERR) : OOB, 0, 1u);
    if (w == 0) LST(1900 + j, rt32());
    LDUMP(w == 0, 224 + j);
}

__global__ void __launch_bounds__(256, 1) k_main(Params p) {
    lptr lds = (lptr)(u32)16 - 16;
    if (blockIdx.x == 0) LST(256, rt32());
    unsigned long long pc; int len;
    asm volatile("s_getpc_b64 %0\n.Lk_main_here:\n\ts_mov_b32 %1, .Lk_end_here-.Lk_main_here" : "=s"(pc), "=s"(len));
    const rsrc_t rs = mk_rsrc(p.ws, W_END);
    const v2u e0 = ld8s(rs, threadIdx.x == 0 ? foff(F_EPOCH) : OOB, 0);
    const v4u code = __builtin_amdgcn_raw_buffer_load_b128(mk_rsrc((const void*)(pc & ~15ull), (u32)max(len, 0)), (((blockIdx.x >> 3) & 31u) * 256u + threadIdx.x) * 16u, 0, 0);
    CFENCE();
    auto conv_id = [](int role) {
        const int x = role >> 5, i = role & 31, no = x < 5 ? 5 : 10, og = x < 5 ? x - 3 : 2 * (x - 4) + (i >= 5), r = (x - 3) * 5 + i - no, q = r / 3;
        return x < 3 || i >= no + 5 || r >= 24 ? NCV : i < no ? ((og & 1) ? (og >> 1) * 5 + i % 5 : NCV) : r % 3 == 0 ? 20 + (q & 1) * 20 + (q >> 1) : 16 + (r % 3) * 8 + q;
    };
    int role = (int)(((u32)__builtin_amdgcn_s_getreg(20 | (3 << 11)) & 7u) * 32u + (blockIdx.x >> 3));
    Early e;
    v4u rv;
#pragma unroll 1
    for (int n = 0;; ++n) {
        conv_early(p, rs, conv_id(role), e);
        if (n) break;
        CFENCE();
        if (threadIdx.x == 0) {
            GAS unsigned long long* slots = (GAS unsigned long long*)(p.ws + W_CTRL + F_SLOT * 128);
            const unsigned long long tag = (unsigned long long)e0.y << 32 | (e0.x + 1u);
            u32 mine = (u32)role;
#pragma unroll 1
            for (int k = 0; k < 256 && __hip_atomic_exchange(slots + mine * 16, tag, __ATOMIC_RELAXED, __HIP_MEMORY_SCOPE_AGENT) == tag; ++k) mine = (mine + 1u) & 255u;
            lds_w16(lds, (v4u){mine, e0.x, e0.y, 0u});
        }
        BAR();
        rv = lds_r16(lds);
        BAR();
        if (uni((int)rv.x) == role) break;
        role = uni((int)rv.x);
    }
    const int x = role >> 5, i = role & 31, w = uni(threadIdx.x >> 6), no = x < 5 ? 5 : 10, nr = x < 7 ? 5 : 4, j = conv_id(role);
    const v2u ep = {uu(rv.y), uu(rv.z)};
    asm volatile("" :: "v"(code));
    if (x < 2) { role_head(p, lds, i >> 4, x * 16 + (i & 15), ep); __builtin_amdgcn_endpgm(); }
    if (x == 2) { role_wo(p, lds, i, ep); __builtin_amdgcn_endpgm(); }
    if (i >= no + nr) { role_qkv(p, lds, (x < 5 ? (x - 3) * 22 : 44 + (x - 5) * 17) + i - no - nr, ep); __builtin_amdgcn_endpgm(); }
    const bool cv = j >= NPRE || (j >= 20 && j < 24);
    if (j < NCV) conv_stage(p, rs, lds, j, (j & 3) * 2 + (j >= NPRE), cv ? 40 : 0, ep, e);
    if (i >= no) {
        if (!cv) role_row(p, lds, (j - 24) * 4 + w, ep);
        __builtin_amdgcn_endpgm();
    }
    const int og = x < 5 ? x - 3 : 2 * (x - 4) + (i >= 5);
    role_out(p, lds, og & 1, og >> 1, i % 5, ep);
}
__global__ void k_end() { asm volatile(".Lk_end_here:"); }

extern "C" void kernel_launch(void* const* d_in, const int* in_sizes, int n_in, void* d_out, int out_size, void* d_ws, size_t ws_size,
                              hipStream_t stream) {
    Params p;
    p.x = (const u16*)d_in[0]; p.mem0 = (const u16*)d_in[1]; p.Wq = (const u16*)d_in[2]; p.Wk = (const u16*)d_in[3];
    p.Wv = (const u16*)d_in[4]; p.Wo = (const u16*)d_in[5];
    p.bk = (const u16*)d_in[8]; p.bv = (const u16*)d_in[9]; p.shift = (const u16*)d_in[10]; p.ssmk = (const u16*)d_in[11]; p.ssmD = (const u16*)d_in[12];
    p.out = (u16*)d_out;
    p.ws = (unsigned char*)(((size_t)d_ws + 2 * MB - 1) & ~(size_t)(2 * MB - 1));
    (void)hipFuncSetAttribute((const void*)k_main, hipFuncAttributeMaxDynamicSharedMemorySize, LDS_BYTES);
    hipLaunchKernelGGL(k_main, dim3(256), dim3(256), LDS_BYTES, stream, p);
}
```

```cpp
#include <hip/hip_runtime.h>
#include <hip/hip_bf16.h>

typedef unsigned short u16;
typedef unsigned int u32;
typedef u32 v2u __attribute__((ext_vector_type(2)));
typedef u32 v4u __attribute__((ext_vector_type(4)));
typedef u32 v8u __attribute__((ext_vector_type(8)));
typedef float f32x2 __attribute__((ext_vector_type(2)));
typedef float f32x4 __attribute__((ext_vector_type(4)));
typedef float f32x16 __attribute__((ext_vector_type(16)));
typedef __bf16 bf16x2 __attribute__((ext_vector_type(2)));
typedef __bf16 bf16x8 __attribute__((ext_vector_type(8)));
typedef short s16x4 __attribute__((ext_vector_type(4)));
#define LAS __attribute__((address_space(3)))
#define GAS __attribute__((address_space(1)))
typedef LAS unsigned char* lptr;
#define DEV __device__ __forceinline__

constexpr int SEQ = 2048, DM = 640, NH = 32, DH = 128, NSEG = 64, NQ = NH * DH;

constexpr u32 MB = 1u << 20;
constexpr u32 W_ORING = 0;
constexpr u32 W_MRING = MB;
constexpr u32 W_PROBE = 2 * MB - 65536;
constexpr u32 W_CTRL = 4 * MB;
constexpr u32 CTRL_BYTES = 128 * 1024;
constexpr u32 ACT = SEQ * DM * 2;
constexpr u32 W_H1 = 5 * MB, W_Y = W_H1 + ACT, W_H2 = W_Y + ACT, W_X2 = W_H2 + ACT;
constexpr u32 W_KQ = 16 * MB;
constexpr u32 W_VO = W_KQ + 64 * MB;
constexpr u32 W_P = W_VO + 64 * MB;
constexpr u32 P_ITEM = 16 * 4 * DM * 4;
constexpr u32 W_KVT = W_P + 4 * NSEG * P_ITEM;
constexpr u32 W_SP = W_KVT + ACT;
constexpr u32 W_END = W_KVT + 3 * MB;
static_assert((size_t)W_END + 2 * MB <= 256ull * MB, "workspace");
constexpr u32 F_ERR = 0, F_QKV = 16, F_O = F_QKV + 192, F_P = F_O + 64, F_H2 = F_P + 64, F_CLR = F_H2 + 32,
              F_EPOCH = F_CLR + 32, F_SLOT = F_EPOCH + 1, F_PRE = F_SLOT + 256, F_N = F_PRE + 192;
constexpr int NCV = 44, NPRE = 40;
static_assert(F_N * 128 <= CTRL_BYTES, "ctrl");
DEV u32 foff(u32 idx) { return W_CTRL + idx * 128u; }

constexpr int RM = 15;
constexpr u32 OOB = 0x80000000u;
constexpr u32 PSENT = 0x7F817F81u;
constexpr int SPIN_CAP = 40000;
constexpr int LDS_BYTES = 160 * 1024;

#define LDUMP(cond, id) do {} while (0)
#define LMX(cond, k, s_, v) do {} while (0)
#define LS3(cond, slot, idx) do {} while (0)
#define LST(idx, v) do {} while (0)
#define LS2(cond, idx) do {} while (0)
struct Params {
    const u16 *x, *mem0, *Wq, *Wk, *Wv, *Wo, *bk, *bv, *shift, *ssmk, *ssmD;
    u16* out;
    unsigned char* ws;
};

typedef __amdgpu_buffer_rsrc_t rsrc_t;
DEV rsrc_t mk_rsrc(const void* p, u32 bytes) { return __builtin_amdgcn_make_buffer_rsrc((void*)p, 0, bytes, 0x00020000); }
DEV v4u ld16s(rsrc_t r, u32 off, u32 so) { return __builtin_amdgcn_raw_buffer_load_b128(r, off, so, 16); }
DEV v2u ld8s(rsrc_t r, u32 off, u32 so) { return __builtin_amdgcn_raw_buffer_load_b64(r, off, so, 16); }
DEV v4u ldin16(rsrc_t r, u32 off, u32 so) { return __builtin_amdgcn_raw_buffer_load_b128(r, off, so, 2); }
DEV v2u ldin8(rsrc_t r, u32 off, u32 so) { return __builtin_amdgcn_raw_buffer_load_b64(r, off, so, 2); }
DEV u32 ld4(rsrc_t r, u32 off) { return __builtin_amdgcn_raw_buffer_load_b32(r, off, 0, 16); }
DEV void st16(rsrc_t r, u32 off, v4u v) { __builtin_amdgcn_raw_buffer_store_b128(v, r, off, 0, 16); }
DEV void st8s(rsrc_t r, u32 off, u32 so, v2u v) { __builtin_amdgcn_raw_buffer_store_b64(v, r, off, so, 16); }
DEV void st4s(rsrc_t r, u32 off, u32 so, u32 v) { __builtin_amdgcn_raw_buffer_store_b32(v, r, off, so, 16); }
#define DRAIN() asm volatile("s_waitcnt vmcnt(0)" ::: "memory")
#define VMW(n) asm volatile("s_waitcnt vmcnt(%0)" :: "i"(n) : "memory")
#define BAR() asm volatile("s_waitcnt lgkmcnt(0)\n\ts_barrier" ::: "memory")
#define SCHED() __builtin_amdgcn_sched_barrier(0)
#define CFENCE() asm volatile("" ::: "memory")
#define NOPS(n) asm volatile("s_nop %0" :: "i"(n))
#define PIN(x) asm volatile("" : "+v"(x))
#define PINS(x) asm volatile("" : "+s"(x))

template <int V> struct IC { static constexpr int value = V; };
template <int I, int N, class F> DEV void static_for(F&& f) { if constexpr (I < N) { f(IC<I>{}); static_for<I + 1, N>(f); } }
#define SFOR(var, n) static_for<0, n>([&](auto ic_) __attribute__((always_inline)) { constexpr int var = decltype(ic_)::value;
#define SEND });

DEV u32 pk(float a, float b) { bf16x2 v = {(__bf16)a, (__bf16)b}; return __builtin_bit_cast(u32, v); }
DEV float lo_f(u32 u) { return __builtin_bit_cast(float, u << 16); }
DEV float hi_f(u32 u) { return __builtin_bit_cast(float, u & 0xffff0000u); }
DEV f32x2 up2(u32 u) { return (f32x2){lo_f(u), hi_f(u)}; }
DEV u32 pk2(f32x2 v) { return pk(v.x, v.y); }
DEV float rbf(float a) { return (float)(__bf16)a; }
DEV v4u lds_r16(lptr p) { return *(LAS v4u*)p; }
DEV void lds_w16(lptr p, v4u v) { *(LAS v4u*)p = v; }
DEV v2u lds_tr(lptr p) { return __builtin_bit_cast(v2u, __builtin_amdgcn_ds_read_tr16_b64_v4i16((LAS s16x4*)p)); }
DEV v4u tr2(lptr p0, lptr p1) { const v2u a = lds_tr(p0), b = lds_tr(p1); return (v4u){a.x, a.y, b.x, b.y}; }
DEV v8u cat8(v4u a, v4u b) { return __builtin_shufflevector(a, b, 0, 1, 2, 3, 4, 5, 6, 7); }
DEV f32x4 mfma16(v4u a, v4u b, f32x4 c) { return __builtin_amdgcn_mfma_f32_16x16x32_bf16(__builtin_bit_cast(bf16x8, a), __builtin_bit_cast(bf16x8, b), c, 0, 0, 0); }
DEV f32x16 mfma32(v4u a, v4u b, f32x16 c) { return __builtin_amdgcn_mfma_f32_32x32x16_bf16(__builtin_bit_cast(bf16x8, a), __builtin_bit_cast(bf16x8, b), c, 0, 0, 0); }

DEV float f_(u32 u) { return __builtin_bit_cast(float, u); }
DEV u32 u_(float f) { return __builtin_bit_cast(u32, f); }
template <int CTRL> DEV float dpp(float v) { return f_((u32)__builtin_amdgcn_update_dpp(0, (int)u_(v), CTRL, 0xf, 0xf, false)); }
DEV float max16(float v) { auto r = __builtin_amdgcn_permlane16_swap(u_(v), u_(v), false, false); return fmaxf(f_(r[0]), f_(r[1])); }
DEV float max32(float v) { auto r = __builtin_amdgcn_permlane32_swap(u_(v), u_(v), false, false); return fmaxf(f_(r[0]), f_(r[1])); }
DEV float add16(float v) { auto r = __builtin_amdgcn_permlane16_swap(u_(v), u_(v), false, false); return f_(r[0]) + f_(r[1]); }
DEV float add32(float v) { auto r = __builtin_amdgcn_permlane32_swap(u_(v), u_(v), false, false); return f_(r[0]) + f_(r[1]); }
DEV float wave_sum(float v) {
    v += dpp<0xB1>(v);
    v += dpp<0x4E>(v);
    v += dpp<0x124>(v);
    v += dpp<0x128>(v);
    return add32(add16(v));
}
DEV bool all_(bool c) { return __builtin_amdgcn_ballot_w64(!c) == 0ull; }
DEV int uni(int v) { return __builtin_amdgcn_readfirstlane(v); }
DEV u32 uu(u32 v) { return (u32)__builtin_amdgcn_readfirstlane((int)v); }
DEV u32 rdl(u32 v, int l) { return (u32)__builtin_amdgcn_readlane((int)v, l); }
DEV u32 rt32() { return (u32)__builtin_amdgcn_s_memrealtime(); }

#define WSL 2
DEV void wait_flags(rsrc_t rs, u32 off, u32 want, bool& dead) {
    if (dead) return;
#pragma unroll 1
    for (int n = 0;; ++n) {
        CFENCE();
        const u32 v = ld4(rs, off);
        if (all_(off >= OOB || v >= want)) break;
        __builtin_amdgcn_s_sleep(WSL);
        if ((n & 63) == 63) { if (uni((int)ld4(rs, foff(F_ERR))) != 0) { dead = true; break; } }
        if (n > SPIN_CAP) { st4s(rs, foff(F_ERR), 0, 1u); dead = true; break; }
    }
    CFENCE();
}

DEV void pre_post(rsrc_t rs, int idx, u32 stage, v2u ep) { st8s(rs, threadIdx.x == 0 ? foff(F_PRE + idx) : OOB, 0, (v2u){ep.x + stage, ep.y}); }
DEV void pre_poll(rsrc_t rs, int first, int n, u32 stage, v2u ep) {
    const int lane = threadIdx.x & 63;
#pragma unroll 1
    for (int it = 0; it < SPIN_CAP; ++it) {
        CFENCE();
        const v2u a = ld8s(rs, foff(F_PRE + first + min(lane, n - 1)), 0), b = ld8s(rs, foff(F_PRE + first + min(lane + 64, n - 1)), 0);
        if (all_(a.y == ep.y && b.y == ep.y && max(a.x - ep.x - stage, b.x - ep.x - stage) < 8u - stage)) break;
        __builtin_amdgcn_s_sleep(2);
    }
    CFENCE();
}
DEV void pre_wait(rsrc_t rs, int first, int n, u32 stage, v2u ep) {
    if (uni(threadIdx.x >> 6) == 0) pre_poll(rs, first, n, stage, ep);
    BAR();
}
DEV u32 pol_wait(rsrc_t rs, v2u ep) {
    const int lane = threadIdx.x & 63;
    v4u a;
#pragma unroll 1
    for (int it = 0; it < SPIN_CAP; ++it) {
        CFENCE();
        a = ld16s(rs, foff(F_PRE + (lane < NPRE || lane >= 48 ? lane : 0)), 0);
        if (all_(a.y == ep.y && a.x - ep.x - 1u < 7u)) break;
        __builtin_amdgcn_s_sleep(2);
    }
    CFENCE();
    return uu(wave_sum(lane >= 48 ? (float)(int)a.z : 0.f) >= 0.f ? 1u : 0u);
}
#define PROBE_T 1000
#define NPROBE 32
DEV u32 pol_probe(const Params& p, rsrc_t rs, int id) {
    const int lane = threadIdx.x & 63;
    const u32 off0 = W_PROBE + (u32)(id >> 2) * 16384u + (u32)(id & 3) * 128u;
    const u32 tend = rt32() + PROBE_T;
    st4s(rs, lane < 32 ? off0 + (u32)lane * 512 : OOB, 0, 0u); DRAIN();
    int calm = 0;
    int d[2] = {1 << 16, 1 << 16};
#pragma unroll 1
    for (int q = 0; q < NPROBE && ((q & 1) || (calm < 6 && (int)(rt32() - tend) < 0)); ++q) {
        const u32 half = (u32)(q ^ (q >> 1) ^ id) & 1u;
        const unsigned long long t0 = __builtin_amdgcn_s_memtime();
        const u32 v = ld4(rs, lane == 0 ? off0 + half * 8192 + (u32)(q >> 1) * 512 : OOB);
        asm volatile("s_waitcnt vmcnt(0)" :: "v"(v) : "memory");
        const int dt = (int)(__builtin_amdgcn_s_memtime() - t0);
        if (half) d[1] = min(d[1], dt); else d[0] = min(d[0], dt);
        calm += dt < 1100;
    }
    const u32 xcc = (u32)__builtin_amdgcn_s_getreg(20 | (3 << 11)) & 0xfu;
    return (u32)min(max((d[0] - d[1]) * ((xcc & 4u) ? -1 : 1), -250), 250);
}
DEV f32x2 ln_stats(const f32x2 (&x)[5]) {
    const float c = f_(uu(u_(x[0].x)));
    f32x2 d[5];
#pragma unroll
    for (int i = 0; i < 5; ++i) d[i] = x[i] - c;
    const f32x2 s2 = (d[0] + d[1]) + (d[2] + d[3]) + d[4], q2 = (d[0] * d[0] + d[1] * d[1]) + (d[2] * d[2] + d[3] * d[3] + d[4] * d[4]);
    float a = s2.x + s2.y, b = q2.x + q2.y, a2, b2;
#define LN_DPP(m) "v_add_f32_dpp %0, %0, %0 " m " row_mask:0xf bank_mask:0xf\n\tv_add_f32_dpp %1, %1, %1 " m " row_mask:0xf bank_mask:0xf\n\ts_nop 0\n\t"
    asm("s_nop 1\n\t" LN_DPP("quad_perm:[1,0,3,2]") LN_DPP("quad_perm:[2,3,0,1]") LN_DPP("row_ror:4")
        "v_add_f32_dpp %0, %0, %0 row_ror:8 row_mask:0xf bank_mask:0xf\n\tv_add_f32_dpp %1, %1, %1 row_ror:8 row_mask:0xf bank_mask:0xf\n\ts_nop 1\n\t"
        "v_permlane16_swap_b32 %0, %1\n\tv_add_f32 %0, %0, %1\n\tv_mov_b32 %1, %0\n\ts_nop 1\n\tv_permlane32_swap_b32 %0, %1\n\tv_add_f32 %0, %0, %1\n\ts_nop 0\n\t"
        "v_readlane_b32 %2, %0, 0\n\tv_readlane_b32 %3, %0, 16\n\ts_nop 1"
        : "+v"(a), "+v"(b), "=s"(a2), "=s"(b2));
    const float m = a2 * (1.0f / DM), var = fmaxf(b2 * (1.0f / DM) - m * m, 0.f);
    const float sd = rbf(__builtin_amdgcn_sqrtf(rbf(rbf(var) + 1e-5f)));
    return (f32x2){rbf(c + m), __builtin_amdgcn_rcpf(sd)};
}
DEV u32 pkv(f32x2 v) { u32 d; asm("v_cvt_pk_bf16_f32 %0, %1, %2" : "=v"(d) : "v"(v.x), "v"(v.y)); return d; }
DEV void store_h(rsrc_t rs, u32 base, int row, const u32 (&hp)[5]) {
    const u32 lane = threadIdx.x & 63;
    const u32 sw = (u32)(row & 15) * 16u, o8 = (lane >> 5) * 8192u + (((lane & 31u) * 8u) ^ sw), hb = base + (u32)(row >> 5) * (32 * DM * 2) + (u32)(row & 31) * 256u;
    st8s(rs, o8, hb, (v2u){hp[0], hp[1]});
    st8s(rs, o8, hb + 16384, (v2u){hp[2], hp[3]});
    st4s(rs, (lane * 4) ^ sw, hb + 32768, hp[4]);
}

#define DMA16(ldsb, vo, so) asm volatile("s_mov_b32 m0, %0\n\ts_nop 0\n\tbuffer_load_dwordx4 %1, %2, %3 offen sc1 lds" :: "s"(ldsb), "v"(vo), "s"(srd), "s"(so) : "memory")
#define DMA16I(ldsb, lo, vo, so, go) do { u32 so_; asm volatile("s_add_u32 m0, %1, %5\n\ts_add_u32 %0, %4, %6\n\tbuffer_load_dwordx4 %2, %3, %0 offen sc1 lds" \
                         : "=&s"(so_) : "s"(ldsb), "v"(vo), "s"(srd), "s"(so), "i"(lo), "i"(go) : "memory", "scc"); } while (0)
#define DMA32(ldsb, vo, so) asm volatile("s_mov_b32 m0, %0\n\ts_nop 0\n\tbuffer_load_dwordx4 %1, %2, %3 offen sc1 lds\n\tbuffer_load_dwordx4 %1, %2, %3 offen offset:1024 sc1 lds" \
                         :: "s"(ldsb), "v"(vo), "s"(srd), "s"(so) : "memory")
#define DMAIN(sr, ldsb, vo, so) asm volatile("s_mov_b32 m0, %0\n\ts_nop 0\n\tbuffer_load_dwordx4 %1, %2, %3 offen lds" :: "s"(ldsb), "v"(vo), "s"(sr), "s"(so) : "memory")
DEV v4u mk_srd(const void* q, u32 bytes) { return (v4u){uu((u32)(size_t)q), uu((u32)((size_t)q >> 32) & 0xffffu), bytes, 0x00020000u}; }
#define DMANT(sr, ldsb, vo, so) asm volatile("s_mov_b32 m0, %0\n\ts_nop 0\n\tbuffer_load_dwordx4 %1, %2, %3 offen nt lds" :: "s"(ldsb), "v"(vo), "s"(sr), "s"(so) : "memory")
#define AHEAD(sr, vo, so) asm volatile("buffer_load_dwordx4 %0, %1, %2, %3 offen" : "+v"(junk) : "v"(vo), "s"(sr), "s"(so) : "memory")
#define MK_SRD() const v4u srd = mk_srd(p.ws, W_END)

constexpr int CV_W = 40960;
template <bool B> struct CvL {
    static constexpr int NF = B ? 5 : 3, NK = B ? 4 : 2, KP = 544 + NK * 256, CH = NF * 1024 + 4 * KP;
};
constexpr int CV_Y = 4 * CvL<false>::CH, CV_YP = 272;
static_assert(CV_Y + 32 * CV_YP <= CV_W && 2 * CvL<true>::CH <= CV_Y, "conv lds");
DEV int cv_chan(int u, int i) { return 16 * (u % 40) + 4 * i + u / 40; }
template <bool B> DEV void cv_filt(const Params& p, u32 dst, int d, int i) {
    const u32 lane = threadIdx.x & 63;
    const v4u sf = mk_srd(p.ssmk + d * SEQ, SEQ * 2);
    if (i == 0) DMANT(sf, dst, lane < 4u ? OOB : lane * 16u - 64u, 0);
    else DMANT(sf, uu(dst + i * 1024), i == CvL<B>::NF - 1 && lane >= 4u ? OOB : lane * 16u, i * 1024 - 64);
}
DEV v4u cv_kvld(rsrc_t rs, int d, int i) { return ld16s(rs, (threadIdx.x & 63) * 16u, uu(W_KVT + (u32)d * (SEQ * 2) + i * 1024)); }
template <bool B> DEV void cv_kvput(lptr F, const v4u* k) {
    const int lane = threadIdx.x & 63;
#pragma unroll
    for (int i = 0; i < CvL<B>::NK; ++i) lds_w16(F + CvL<B>::NF * 1024 + (lane & 3) * CvL<B>::KP + 512 + i * 256 + (lane >> 2) * 16, k[i]);
}
template <bool B> DEV void cv_zero(lptr F) {
    const int lane = threadIdx.x & 63;
#pragma unroll
    for (int i = 0; i < 4; ++i) lds_w16(F + (lane >> 5) * CvL<B>::CH + CvL<B>::NF * 1024 + i * CvL<B>::KP + (lane & 31) * 16, (v4u){0u, 0u, 0u, 0u});
}
#define M32V(d, a, b) asm volatile("v_mfma_f32_32x32x16_bf16 %0, %1, %2, %0" : "+v"(d) : "v"(a), "v"(b))
template <bool B, class G> DEV void cv_run(const Params& p, rsrc_t rs, lptr F, int d, u32 D, f32x4 sp, float (&y)[16], G&& gap) {
    constexpr int KP = CvL<B>::KP, FO = B ? 2048 : 0;
    const int lane = threadIdx.x & 63, n = lane & 31, hh = lane >> 5, c = lane & 3;
    lptr fa = F + 64 + (32 * hh + 16 * (c & 1) + 8 * ((lane >> 4) & 1) + 4 * (c >> 1) - (lane & 12)) * 2;
    lptr kl = F + CvL<B>::NF * 1024 + (32 + n - hh) * 16;
    v4u dg;
#pragma unroll
    for (int q = 0; q < 4; ++q) { const int k = (lane & 35) ? -1 : (n & 4) + (n >> 3); dg[q] = k == 2 * q ? D : k == 2 * q + 1 ? D << 16 : 0u; }
    v4u l[4], h[4]; v2u fc[4], fh[4];
    f32x16 acc[4];
#pragma unroll
    for (int j = 0; j < 4; ++j) { l[j] = lds_r16(kl + j * KP); if constexpr (B) h[j] = lds_r16(kl + j * KP + 512); }
#pragma unroll
    for (int j = 0; j < 2; ++j) { fc[j] = lds_tr(fa + FO - 32 * j); if constexpr (B) fh[j] = lds_tr(fa - 32 * j); }
    asm volatile("s_nop 1" : "+v"(dg));
#pragma unroll
    for (int j = 0; j < 4; ++j) asm volatile("v_mfma_f32_32x32x16_bf16 %0, %1, %2, 0" : "=&v"(acc[j]) : "v"(dg), "v"(B ? h[j] : l[j]));
    constexpr int U = B ? 2 : 4;
    kl -= 32 * U; fa -= 32;
    auto body = [&](bool first) __attribute__((always_inline)) {
#pragma unroll
        for (int z = 0; z < U; ++z) {
            const int c = 2 * (z & 1);
            const v4u f = __builtin_shufflevector(fc[c], fc[c + 1], 0, 1, 2, 3), g = __builtin_shufflevector(fh[c], fh[c + 1], 0, 1, 2, 3);
#pragma unroll
            for (int j = 0; j < 4; ++j) {
                M32V(acc[j], f, l[j]); l[j] = lds_r16(kl + j * KP + 32 * (U - z - 1));
                if (j < 2) fc[(c ^ 2) + j] = lds_tr(fa + FO + (z + 1) * 128 + 32 - 32 * j);
                if (first && j > 0) gap(5 * z + j - 1);
                SCHED();
            }
            if constexpr (B) {
#pragma unroll
                for (int j = 0; j < 4; ++j) {
                    M32V(acc[j], g, h[j]); h[j] = lds_r16(kl + j * KP + 512 + 32 * (U - z - 1));
                    if (j < 2) fh[(c ^ 2) + j] = lds_tr(fa + (z + 1) * 128 + 32 - 32 * j);
                    if (first && j > 1) gap(5 * z + j + 1);
                    SCHED();
                }
            }
        }
        kl -= 32 * U; fa += 128 * U;
    };
    if constexpr (B) body(true);
#pragma unroll 1
    for (int m = B ? 2 * U : 0; m < 32; m += 2 * U) body(false);
    asm volatile("s_nop 11" : "+v"(acc[0]), "+v"(acc[1]), "+v"(acc[2]), "+v"(acc[3]));
    const float o[3] = {acc[1][15] + acc[2][14] + acc[3][13], acc[2][15] + acc[3][14], acc[3][15]};
    if constexpr (!B) st16(rs, lane == 63 ? W_SP + (u32)d * 16u : OOB, (v4u){u_(o[0]), u_(o[1]), u_(o[2]), 0u});
#pragma unroll
    for (int i = 0; i < 16; ++i) {
        float v = acc[0][i];
#pragma unroll
        for (int j = 1; j < 4; ++j) if (j <= i) v += acc[j][i - j];
        if (i < 3) { const float e = f_((u32)__builtin_amdgcn_ds_bpermute((hh ? lane - 32 : lane + 31) * 4, (int)u_(o[i]))); v += lane == 0 ? sp[i] : e; }
        y[i] = v;
    }
}
DEV void cv_put(lptr F, int pr, const float (&y0)[16], const float (&y1)[16]) {
    const int lane = threadIdx.x & 63;
#pragma unroll
    for (int i = 0; i < 16; ++i) *(LAS u32*)(F + CV_Y + (lane & 31) * CV_YP + (lane >> 5) * 128 + i * 8 + pr * 4) = pk(y0[i], y1[i]);
}
DEV void cv_out(rsrc_t rs, lptr F, int u, int blk0) {
    const int lane = threadIdx.x & 63;
    lptr q = F + CV_Y + ((lane >> 2) & 3) * CV_YP + ((lane >> 4) * 4 + (lane & 3)) * 8;
    const u32 yo = W_Y + (u32)(((lane >> 2) * 8 + blk0) * DM + 4 * u + (lane & 3)) * 16u;
#pragma unroll
    for (int i = 0; i < 8; ++i) { lptr a = q + (i >> 1) * (8 * CV_YP) + (i & 1) * 128; st16(rs, yo + (u32)((i & 1) * 128 + (i >> 1)) * (DM * 16), tr2(a, a + 4 * CV_YP)); }
}
struct KvIn { v2u k[19], v[16]; v4u s[2]; };
template <class F> DEV void kvt_load(const Params& p, int tile, KvIn& in, F&& mid) {
    const int lane = threadIdx.x & 63;
    const rsrc_t rk = mk_rsrc(p.bk, ACT), rv = mk_rsrc(p.bv, ACT), rh = mk_rsrc(p.shift, DM * 8);
    const int row = (tile / 10) * 64 + (lane >> 4) * 16, col = (tile % 10) * 64 + (lane & 15) * 4;
    const u32 vo = (u32)(row * DM + col) * 2u, vp = row > 0 ? vo - 3 * (DM * 2) : OOB;
#pragma unroll
    for (int i = 0; i < 2; ++i) in.s[i] = ldin16(rh, (u32)col * 8u + i * 16, 0);
#pragma unroll
    for (int i = 0; i < 19; ++i) { if (i == 7) mid(); in.k[i] = i < 3 ? ldin8(rk, vp, i * (DM * 2)) : ldin8(rk, vo, (i - 3) * (DM * 2)); if (i >= 3) in.v[i - 3] = ldin8(rv, vo, (i - 3) * (DM * 2)); }
}
DEV void kvt_make(rsrc_t rs, lptr xt, int tile, const KvIn& in) {
    const int lane = threadIdx.x & 63;
#pragma unroll
    for (int c = 0; c < 2; ++c) {
        const v4u sv = in.s[c];
        const f32x2 sh[4] = {{lo_f(sv.x), lo_f(sv.z)}, {hi_f(sv.x), hi_f(sv.z)}, {lo_f(sv.y), lo_f(sv.w)}, {hi_f(sv.y), hi_f(sv.w)}};
        f32x2 kf[19];
#pragma unroll
        for (int i = 0; i < 19; ++i) kf[i] = up2(in.k[i][c]);
        u32 kv[16];
#pragma unroll
        for (int i = 0; i < 16; ++i) kv[i] = pk2(((kf[i] * sh[3] + kf[i + 1] * sh[2]) + (kf[i + 2] * sh[1] + kf[i + 3] * sh[0])) * up2(in.v[i][c]));
#pragma unroll
        for (int q = 0; q < 8; ++q) {
            const u32 sel = (q & 1) ? 0x07060302u : 0x05040100u; const u32* z = kv + (q >> 1);
            *(LAS v2u*)(xt + ((2 * c + (q & 1)) * 16 + (lane & 15)) * 144 + (lane >> 5) * 64 + (q >> 1) * 16 + ((lane >> 4) & 1) * 8) =
                (v2u){__builtin_amdgcn_perm(z[4], z[0], sel), __builtin_amdgcn_perm(z[12], z[8], sel)};
        }
    }
    const u32 o = W_KVT + (u32)(((tile % 10) * 64 + 4 * (lane >> 3)) * SEQ + (tile / 10) * 64 + (lane & 7) * 8) * 2u;
#pragma unroll
    for (int i = 0; i < 8; ++i) st16(rs, o + (u32)(32 * (i & 1) + (i >> 1)) * (SEQ * 2), lds_r16(xt + (8 * i + (lane >> 3)) * 144 + (lane & 7) * 16));
}
struct RowX { v2u a, b; u32 c; };
DEV void ln_row(rsrc_t rs, int row, const RowX& r) {
    const f32x2 x[5] = {up2(r.a.x), up2(r.a.y), up2(r.b.x), up2(r.b.y), up2(r.c)};
    const f32x2 st = ln_stats(x);
    u32 hp[5];
#pragma unroll
    for (int q = 0; q < 5; ++q) hp[q] = pk2(up2(pkv(x[q] - st.x)) * st.y);
    store_h(rs, W_H1, row, hp);
}
DEV v4u mem0_load(const Params& p, int j) {
    const u32 gt = (u32)(j * 256 + threadIdx.x) * 16u, n = ((gt >> 14) << 9) + ((gt & 8191u) >> 4);
    return ldin16(mk_rsrc(p.mem0, 8 * DM * 2), n < 640u ? ((2 * ((n >> 2) & 3) + (n & 1)) * DM + (n >> 4) * 16 + (n & 2) * 4) * 2u : OOB, 0);
}
DEV void pre_rings(rsrc_t rs, int j, v4u m) {
    const u32 gi = (u32)(j * 256 + threadIdx.x), gt = gi * 16u, R = NPRE * 4096, n = ((gt >> 14) << 9) + ((gt & 8191u) >> 4);
#pragma unroll
    for (int i = 0; i < 4; ++i) m[i] = n >= 640u ? PSENT : m[i] == PSENT ? 0x7FC17FC1u : m[i];
    st4s(rs, gi - 36u * 256u < F_CLR ? foff(gi - 36u * 256u) : OOB, 0, 0u);
#pragma unroll 1
    for (u32 o = gt; o < MB; o += R) st16(rs, W_ORING + o, (v4u){PSENT, PSENT, PSENT, PSENT});
    const u32 mo = gt < 4 * 32768 + 16384 ? W_MRING + gt : OOB;
    st16(rs, mo, m);
    st16(rs, mo + MB / 4, m);
    static_assert(NPRE * 4096 >= 4 * 32768 + 16384, "memory ring");
}
constexpr int KT0 = NPRE * 4, LN0 = NPRE * 8;
struct Early { RowX r[2]; v4u m; u32 vote; };
DEV void conv_early(const Params& p, rsrc_t rs, int j, Early& e) {
    const int lane = threadIdx.x & 63, w = uni(threadIdx.x >> 6), u = j * 4 + w;
    if (j < NPRE) {
        const rsrc_t rx = mk_rsrc(p.x, ACT);
#pragma unroll
        for (int i = 0; i < 2; ++i) {
            const u32 so = uu((u + i * KT0) * (DM * 2));
            e.r[i].a = __builtin_amdgcn_raw_buffer_load_b64(rx, lane * 8u, so, 0); e.r[i].b = __builtin_amdgcn_raw_buffer_load_b64(rx, lane * 8u + 512u, so, 0);
            e.r[i].c = __builtin_amdgcn_raw_buffer_load_b32(rx, lane * 4u + 1024u, so, 0);
        }
        e.m = mem0_load(p, j);
    } else if (j < NCV && w > 0) e.vote = pol_probe(p, rs, u - NPRE * 4);
}
DEV void conv_stage(const Params& p, rsrc_t rs, lptr lds, int j, int g0, int ge, v2u ep, Early& e) {
    const int tid = threadIdx.x, lane = tid & 63, w = uni(tid >> 6), u = j * 4 + w;
    lptr F = lds + w * CV_W;
    const u32 fb = uu(w * CV_W);
    const rsrc_t rd = mk_rsrc(p.ssmD, DM * 2);
    float y0[16], y1[16];
    KvIn in;
    if (j < NPRE) {
        constexpr int CH = CvL<false>::CH;
        if (w == 1) LST(1500 + j, rt32());
        pre_rings(rs, j, e.m);
        ln_row(rs, u, e.r[0]); ln_row(rs, u + KT0, e.r[1]);
        if (w == 1) LST(1550 + j, rt32());
        CFENCE();
        kvt_load(p, u, in, [&] { VMW(13); BAR(); pre_post(rs, j, 1u, ep); if (w == 0) LST(1450 + j, rt32()); });
        if (j == 20) LST(260, rt32());
        const u32 D = __builtin_amdgcn_raw_buffer_load_b16(rd, (u32)cv_chan(u, lane & 3) * 2u, 0, 0);
#pragma unroll 1
        for (int i = 0; i < 4; ++i) { const int d = cv_chan(u, i); cv_filt<false>(p, fb + i * CH, d, 0); cv_filt<false>(p, fb + i * CH, d, 1); cv_filt<false>(p, fb + i * CH, d, 2); }
        kvt_make(rs, F + CV_Y, u, in);
        cv_zero<false>(F); cv_zero<false>(F + 2 * CH);
        DRAIN(); BAR();
        pre_post(rs, j, 2u, ep);
        if (j == 20) LST(262, rt32());
        pre_wait(rs, 0, NPRE, 2u, ep);
        if (j == 20) LST(281, rt32());
        {
            v4u k[8];
#pragma unroll
            for (int i = 0; i < 8; ++i) k[i] = cv_kvld(rs, cv_chan(u, i >> 1), i & 1);
#pragma unroll
            for (int i = 0; i < 4; ++i) cv_kvput<false>(F + i * CH, k + 2 * i);
        }
        if (j == 20) LST(286, rt32());
#pragma unroll 1
        for (int i = 0; i < 4; i += 2) {
            cv_run<false>(p, rs, F + i * CH, cv_chan(u, i), rdl(D, i), f32x4{}, y0, [](int) {});
            cv_run<false>(p, rs, F + (i + 1) * CH, cv_chan(u, i + 1), rdl(D, i + 1), f32x4{}, y1, [](int) {});
            cv_put(F, i >> 1, y0, y1);
        }
        if (j == 20) LST(287, rt32());
        cv_out(rs, F, u, 0);
        if (j == 20) LST(263, rt32());
    } else {
        st16(rs, lane == 0 ? foff(F_PRE + 48 + u - NPRE * 4) : OOB, (v4u){ep.x + 1u, ep.y, w > 0 ? e.vote : 0u, 0u});
#pragma unroll 1
        for (int t = u + 128; t < 320; t += 16) { kvt_load(p, t, in, [] {}); kvt_make(rs, F, t, in); }
    }
    DRAIN(); BAR();
    if (g0 < ge) {
        constexpr int CH = CvL<true>::CH;
        pre_post(rs, j, 3u, ep);
        cv_zero<true>(F);
        if (w == 0) { pre_poll(rs, 64, 32, 3u, ep); pre_poll(rs, 0, NCV, 3u, ep); }
        BAR();
        v4u k[4];
        auto fetch = [&](u32 dst, int d, int i) { if (i < 5) cv_filt<true>(p, dst, d, i); else if (i < 9) k[i - 5] = cv_kvld(rs, d, i - 5); };
#pragma unroll
        for (int i = 0; i < 9; ++i) fetch(fb, cv_chan(g0 * 4 + w, 0), i);
        const u32 ch = (u32)cv_chan(min((g0 + 8 * (lane >> 2)) * 4 + w, 159), lane & 3);
        u32 D = __builtin_amdgcn_raw_buffer_load_b16(rd, ch * 2u, 0, 0);
        v4u sp = ld16s(rs, W_SP + ch * 16u, 0);
        asm volatile("" : "+v"(D), "+v"(sp));
#pragma unroll 1
        for (int g = g0, n = 0; g < ge; g += 8) {
            const int un = g * 4 + w;
            if (g == 16) { LST(282, rt32()); LST(288, __builtin_amdgcn_s_memtime()); }
#pragma unroll 1
            for (int i = 0; i < 4; i += 2, n += 2) {
                const int d1 = (int)rdl(ch, n + 1), d2 = (int)rdl(ch, n + 2);
                cv_kvput<true>(F, k);
                cv_run<true>(p, rs, F, 0, rdl(D, n), (f32x4){f_(rdl(sp.x, n)), f_(rdl(sp.y, n)), f_(rdl(sp.z, n)), 0.f}, y0, [&](int q) { fetch(fb + CH, d1, q); });
                cv_kvput<true>(F + CH, k);
                cv_run<true>(p, rs, F + CH, 326, rdl(D, n + 1), (f32x4){f_(rdl(sp.x, n + 1)), f_(rdl(sp.y, n + 1)), f_(rdl(sp.z, n + 1)), 0.f}, y1, [&](int q) { fetch(fb, d2, q); });
                cv_put(F, i >> 1, y0, y1);
            }
            cv_out(rs, F, un, 4);
            if (g == 16) { LST(283, rt32()); LST(289, __builtin_amdgcn_s_memtime()); }
        }
        DRAIN(); BAR();
    }
    pre_post(rs, j, 5u, ep);
    if (j == 20) LST(284, rt32());
    if (j == 3) LST(285, rt32());
}

#define M32A(first, d, a, b) do { if (first) asm volatile("v_mfma_f32_32x32x16_bf16 %0, %1, %2, 0" : "=&v"(d) : "a"(a), "v"(b)); \
                                  else asm volatile("v_mfma_f32_32x32x16_bf16 %0, %1, %2, %0" : "+v"(d) : "a"(a), "v"(b)); } while (0)
#define M32B(first, d, a, b) do { if (first) asm volatile("v_mfma_f32_32x32x16_bf16 %0, %1, %2, 0" : "=&v"(d) : "v"(a), "a"(b)); \
                                  else asm volatile("v_mfma_f32_32x32x16_bf16 %0, %1, %2, %0" : "+v"(d) : "v"(a), "a"(b)); } while (0)
#define VCVT(d, a, b) asm volatile("v_cvt_pk_bf16_f32 %0, %1, %2" : "=v"(d) : "v"(a), "v"(b))
#define VADD(d, a, b) asm volatile("v_add_f32 %0, %1, %2" : "=v"(d) : "v"(a), "v"(b))
#define LD4A(d, vo, so) asm volatile("buffer_load_dword %0, %1, %2, %3 offen sc1" : "=v"(d) : "v"(vo), "s"(srd), "s"(so) : "memory")
DEV u32 wst_src(int lane, int ld) { return (u32)((lane >> 4) * ld + 8 * ((lane & 15) ^ ((lane >> 4) << 2))) * 2u; }
DEV u32 wst_rd(int lane, int w) { const int q4 = (lane >> 2) & 3; return (u32)(q4 * 256 + ((w ^ q4) * 64) + ((lane >> 4) & 1) * 32 + (lane & 3) * 8); }

constexpr int QK_SEL = 81920;
#define QK_LEAD 12
#define QK_SMP 29
#define QK_YB 26
#define QK_W0 2
DEV void role_qkv(const Params& p, lptr lds, int c, v2u ep) {
    const int tid = threadIdx.x, lane = tid & 63, w = uni(tid >> 6), hh = lane >> 5, r32 = lane & 31;
    const int m = c >> 5, h = c & 31;
    const rsrc_t rs = mk_rsrc(p.ws, W_END);
    v4u wt[40];
    {
        const v4u sw = mk_srd(m == 0 ? p.Wq : m == 1 ? p.Wk : p.Wv, DM * NQ * 2), sx = mk_srd(p.x, ACT);
        const u32 vo = wst_src(lane, NQ) + (u32)h * (DH * 2), x8 = (u32)lane * 8u, x4 = (u32)lane * 4u;
        RowX xr[5];
        auto wfill = [&](int i0, int n) {
#pragma unroll 1
            for (int i = i0; i < i0 + n; ++i) DMAIN(sw, uu((4 * i + w) * 1024), vo, uu((16 * i + 4 * w) * (NQ * 2)));
        };
        wfill(0, QK_W0);
#pragma unroll
        for (int i = 0; i < 5; ++i)
            asm volatile("buffer_load_dwordx2 %0, %3, %5, %6 offen\n\tbuffer_load_dwordx2 %1, %3, %5, %6 offen offset:512\n\tbuffer_load_dword %2, %4, %5, %6 offen offset:1024"
                         : "=&v"(xr[i].a), "=&v"(xr[i].b), "=&v"(xr[i].c) : "v"(x8), "v"(x4), "s"(sx), "s"(uu((LN0 + 4 * c + w + 384 * i) * (DM * 2))) : "memory");
        wfill(QK_W0, 10 - QK_W0);
        if (c == 3) LST(275, rt32());
        VMW(10 - QK_W0);
        if (c == 3) LST(276, rt32());
#pragma unroll
        for (int i = 0; i < 5; ++i) {
            asm volatile("" : "+v"(xr[i].a), "+v"(xr[i].b), "+v"(xr[i].c));
            const int row = LN0 + 4 * c + w + 384 * i;
            if (i < 4 || row < SEQ) ln_row(rs, row, xr[i]);
            wfill(10 + 6 * i, 6);
        }
        if (c == 3) LST(264, rt32());
        lptr tb = lds + hh * 2048 + wst_rd(lane, w);
        SFOR(k, 40)
            if constexpr (k == 0) { VMW(15); BAR(); }
            if constexpr (k == 28) {
                if (w == 0) pre_poll(rs, 0, NPRE, 1u, ep);
                DRAIN(); BAR();
                pre_post(rs, 96 + c, 1u, ep);
            }
            lptr q = tb + (k >> 3) * 32768 + (k & 7) * 4096; wt[k] = tr2(q, q + 1024); asm volatile("" : "+a"(wt[k]));
        SEND
    }
    if (c == 3) LST(268, rt32());
    if (w == 0) LST(1350 + c, rt32());
    bool dead = false;
    int n0, n1;
    const u32 t16 = (u32)(w * 2048 + lane * 16), wb = uu(w * 2048);
    u32 ar[16];
#pragma unroll
    for (int j = 0; j < 16; ++j) ar[j] = (u32)(r32 * 256 + ((2 * (j & 7) + hh) ^ (r32 & 15)) * 16 + (j >> 3) * 49152);
    MK_SRD();
#define XRD(y, ks) lds_r16(lds + ar[((ks) & 7) + ((ks) >= 24 ? 8 : 0)] + ((ks) >> 3) % 3 * 16384 + (y) * 8192)
#define QM0(y, b) asm volatile("s_add_u32 m0, %0, %1" :: "s"(wb), "i"((b) * 16384 + (y) * 8192) : "scc", "memory")
#define QDMA(i, vo) asm volatile("buffer_load_dwordx4 %0, %1, %2 offen offset:%3 sc1 lds" :: "v"(vo), "s"(srd), "s"(((i) >> 1) * 8192), "i"(((i) & 1) * 1024) : "memory")
    const u32 h2flag = (lane < 16 && w == 0) ? foff(F_H2 + lane) : OOB;
    const u32 ol = (m == 2 ? (u32)(2 * w + (r32 >> 4)) * 1024u : (u32)((r32 >> 4) * 4 + w) * 1024u) + (u32)(hh * 16 + (r32 & 15)) * 16u
                 + (m == 2 ? W_VO : m == 1 ? W_KQ : W_KQ + 8192u) + (u32)h * 16384u;
    const u32 fo = tid < 2 ? foff(F_QKV + tid * 96 + c) : OOB;
    lptr selw = lds + QK_SEL + (w == 0 ? 0 : 64), selr = lds + QK_SEL;
    PIN(selw);
    static_assert(QK_LEAD > 8 && QK_LEAD % 2 == 0 && LN0 == 10 * 32, "X = 8 comes up, before any item of layer 1");
    static_assert(W_H2 - W_H1 == 2 * 64 * (32 * DM * 2), "h");
    auto run = [&](auto isv) __attribute__((always_inline)) {
        constexpr bool V = decltype(isv)::value;
        constexpr int YB = QK_YB;
        f32x16 ax, ay = {};
        v4u x[8], pp, pq;
        u32 xv = t16 + W_H1, ox = ol, oy = OOB, nn = 1, lt = 0, fcnt = 0, fa = OOB;
        asm volatile("" : "+v"(lt), "+v"(nn));
#pragma unroll
        for (int z = 0; z < 5; ++z) x[z] = XRD(1, 40 - YB + z);
#define QPK(r_, a1, a_, q_) do { if constexpr (V) VCVT(r_[q_], a1[4 * (a_) + 8 * ((q_) >> 1) + 2 * ((q_) & 1)], a1[4 * (a_) + 8 * ((q_) >> 1) + 2 * ((q_) & 1) + 1]); \
                                 else VCVT(r_[q_], a1[4 * (a_) + (q_)], a1[4 * (a_) + (q_) + 8]); } while (0)
#define QPK4(r_, a1, a_) do { QPK(r_, a1, a_, 0); QPK(r_, a1, a_, 1); QPK(r_, a1, a_, 2); QPK(r_, a1, a_, 3); } while (0)
        for (;;) {
            u32 ls = uu(lt), yv, fv, nx, need;
            if (ls != 0u && uu(nn) < 256u) pre_wait(rs, 96, 96, 1u, ep);
            else if (ls != 0u) {
                n1 = (int)(uu(nn) >> 8);
                CFENCE();
                const u32 hf = h2flag + (u32)(~n1 & 1) * 2048u;
                u32 f2 = ld4(rs, hf);
                asm volatile("s_waitcnt vmcnt(0)" : "+v"(f2) :: "memory");
                *(LAS u32*)(selw + 16) = (u32)__builtin_amdgcn_ballot_w64(f2 < (u32)n1) & 0xFFFFu;
                BAR();
                st4s(rs, fa, 0, fcnt);
                fa = OOB;
                if (n1 > NSEG) { LDUMP(w == 0, 64 + c); __builtin_amdgcn_endpgm(); }
                if (uu(*(LAS u32*)(selr + 16)) != 0u) {
                    if (w == 0) wait_flags(rs, hf, (u32)n1, dead);
                    BAR();
                }
            }
#define QT(k) do {} while (0)
            QT(0);
            SCHED();
            QM0(0, 0); NOPS(0); QDMA(0, xv); QDMA(1, xv); QM0(0, 1);
            SFOR(z, 80)
                constexpr int ks = (z + 40 - YB) % 40, zx = z - YB;
                constexpr auto upto = [](int q) { const int d = (q - YB + 49) % 40; return q + 6 + (d == 0 || d == 2 ? 1 : d == 1 ? 2 : 0); };
                if constexpr (zx >= 0 && zx < 40) { if constexpr (V) M32B(ks == 0, ax, x[z & 7], wt[ks]); else M32A(ks == 0, ax, wt[ks], x[z & 7]); }
                else { if constexpr (V) M32B(ks == 0, ay, x[z & 7], wt[ks]); else M32A(ks == 0, ay, wt[ks], x[z & 7]); }
                if constexpr (z == 78) lt = *(LAS u32*)selr;
#pragma unroll
                for (int q = upto(z - 1); q < upto(z); ++q) x[q & 7] = XRD(q % 80 < YB || q % 80 >= YB + 40, (q + 40 - YB) % 40);
                SCHED();
                (void)&srd; (void)&wb;
                if constexpr (z < 2) QDMA(z + 2, xv);
                if constexpr (z >= 2 && z < 14 && (z & 3) == 2) QM0(0, (z + 6) >> 2);
                if constexpr (z >= 3 && z < 15 && (z & 1)) QDMA((z + 5) >> 1, xv);
                if constexpr (z == 4) { n0 = uni((int)nn); n1 = n0 >> 8; n0 &= 255; PINS(n0); PINS(n1); }
                if constexpr (z == 8) { nx = n0 < NSEG ? W_H1 + (u32)n0 * (32 * DM * 2) : OOB; PINS(nx); }
                if constexpr (z == 12) { yv = t16 + nx; PIN(yv); }
                if constexpr (zx == -6) { QT(1); asm volatile("s_waitcnt vmcnt(6) lgkmcnt(0)\n\ts_barrier" ::: "memory"); QT(2); }
                if constexpr (zx == -5) st4s(rs, fa, 0, fcnt);
                if constexpr (zx == -4 || zx == 0 || zx == 4) QM0(1, (zx + 4) >> 2);
                if constexpr (zx >= -3 && zx < 8 && (zx & 1)) QDMA((zx + 3) >> 1, yv);
                if constexpr (zx == 2) QPK4(pp, ay, 0);
                if constexpr (zx == 6) QPK4(pq, ay, 1);
                if constexpr (zx == 8) st16(rs, oy, pp);
                if constexpr (zx == 9) st16(rs, oy + 512, pq);
                if constexpr (zx == 10) { QT(3); asm volatile("s_waitcnt vmcnt(9)\n\ts_barrier" ::: "memory"); QT(4); }
                if constexpr (zx == 11 || zx == 15) QM0(1, (zx + 1) >> 2);
                if constexpr (zx >= 12 && zx < 20 && (zx & 1) == 0) QDMA(zx >> 1, yv);
                if constexpr (zx == 21) { fcnt = lane == 0 ? (u32)n0 : (u32)n1; fa = fo; asm volatile("" : "+v"(fcnt), "+v"(fa)); }
                if constexpr (zx == 22) { oy = (ol + (u32)(n0 & RM) * (NH * 16384u)) | (yv & OOB); PIN(oy); }
                if constexpr (zx == 23) { n0 = min(n0 + 1, NSEG); PINS(n0); nx = (u32)min(NSEG, n1 + QK_LEAD); PINS(nx); }
                if constexpr (zx == 24)
                    asm volatile("s_cmp_ge_u32 %1, %0\n\ts_cselect_b32 %0, %4, %1\n\ts_addc_u32 %2, %2, 0\n\ts_cmp_lt_u32 %0, 64\n\ts_cselect_b32 %3, 0, %2"
                                 : "+s"(nx), "+s"(n0), "+s"(n1), "=&s"(need) : "s"(n1 + 64) : "scc");
                if constexpr (zx == 25) { n0 += 1 - (int)min(nx >> 6, 1u); PINS(n0); }
                if constexpr (zx == 26) { nn = (u32)(n0 | n1 << 8); PIN(nn); }
                if constexpr (zx == 27) { ls = W_H1 + (nx + (nx & 64u)) * (32 * DM * 2); PINS(ls); }
                if constexpr (zx == 28) { xv = t16 + ls; PIN(xv); need = nx == 8u ? 255u : need; PINS(need); }
                if constexpr (zx == 34) { asm volatile("s_waitcnt vmcnt(%0) lgkmcnt(0)\n\ts_barrier" :: "i"(QK_SMP < 34 ? 7 : 6) : "memory"); QT(5); }
                if constexpr (zx == QK_SMP) { const u32 po = (nx & 1u) * 2048u; LD4A(fv, h2flag, po); }
                if constexpr (zx == 42) QPK4(pp, ax, 0);
                if constexpr (zx == 43) st16(rs, ox, pp);
                if constexpr (zx == 44) QPK4(pq, ax, 1);
                if constexpr (zx == 45) st16(rs, ox + 512, pq);
                if constexpr (zx == 47) { ox = ol + (nx & (64u | RM)) * (NH * 16384u); PIN(ox); }
                if constexpr (z == 74) { asm volatile("s_waitcnt vmcnt(2)" : "+v"(fv) :: "memory"); QT(6); ls = (u32)__builtin_amdgcn_ballot_w64(fv < need) & 0xFFFFu; PINS(ls); }
                if constexpr (z == 75) { lt = ls; PIN(lt); *(LAS u32*)selw = lt; }
                if constexpr (z == 77) asm volatile("s_waitcnt lgkmcnt(2)\n\ts_barrier" ::: "memory");
                SCHED();
            SEND
        }
    };
    if (m == 2) run(IC<1>{}); else run(IC<0>{});
}

DEV void role_out(const Params& p, lptr lds, int L, int kq, int ng, v2u ep) {
    const int tid = threadIdx.x, lane = tid & 63, w = uni(tid >> 6), hh = lane >> 5, r32 = lane & 31;
    const rsrc_t rs = mk_rsrc(p.ws, W_END);
    v4u wo[64];
    bool dead = false;
    {
        const v4u sw = mk_srd(p.Wo, DM * NQ * 2);
        const u32 vo = wst_src(lane, DM) + (u32)ng * 256u;
        auto fill = [&](int b) {
#pragma unroll 1
            for (int i = 0; i < 8; ++i) DMAIN(sw, uu((b % 5) * 32768 + (4 * i + w) * 1024), vo, uu((kq * 1024 + b * 128 + 16 * i + 4 * w) * (DM * 2)));
        };
#pragma unroll 1
        for (int b = 0; b < 5; ++b) fill(b);
        lptr tb = lds + hh * 1024 + wst_rd(lane, w);
        SFOR(b, 8)
            VMW(b < 4 ? 32 : 56 - 8 * b); BAR();
            lptr q = tb + (b % 5) * 32768;
#pragma unroll
            for (int k = 0; k < 8; ++k) { lptr a = q + (k >> 1) * 8192 + (k & 1) * 2048; wo[8 * b + k] = tr2(a, a + 4096); if (b < 7) asm volatile("" : "+a"(wo[8 * b + k])); else PIN(wo[8 * b + k]); }
            if constexpr (b < 3) { BAR(); fill(b + 5); }
        SEND
    }
    pre_wait(rs, 0, NPRE, 1u, ep);
    if (kq == 0 && ng == 0) LST(272 + L, rt32());
    MK_SRD();
    const u32 ofl = lane < 8 ? foff(F_O + L * 32 + kq * 8 + lane) : OOB, l16 = (u32)lane * 16u;
    const u32 fo = (tid == 0) ? foff(F_P + L * 32 + kq * 5 + ng) : OOB;
#define OM0(dst, b) asm volatile("s_add_u32 m0, %0, %1" :: "s"(dst), "i"((b) * 8192) : "scc", "memory")
#define ODMA(i, vo) asm volatile("buffer_load_dwordx4 %0, %1, %2 offen offset:%3 sc1 lds" :: "v"(vo), "s"(srd), "s"(((i) >> 1) * 16384), "i"(((i) & 1) * 1024) : "memory")
    auto first = [&](u32 dst, u32 vo) {
#pragma unroll
        for (int b = 0; b < 5; ++b) { OM0(dst, b); NOPS(0); ODMA(2 * b, vo); ODMA(2 * b + 1, vo); }
    };
    u32 pa = W_P + (u32)(L * NSEG) * P_ITEM + (u32)(2 * hh * 4 + kq) * (DM * 4) + (u32)(ng * 128 + w * 32 + r32) * 4u, fv = 0;
    u32 ov = W_VO + 8192u + (u32)(L * NSEG * NH + kq * 8) * 16384u + (u32)w * 2048u + l16;
    bool early = false;
    for (int s = 0; s < NSEG; ++s) {
        const u32 dst = uu((s & 1) * 65536 + w * 2048), ndst = dst ^ 65536u, need = s + 1 < NSEG ? (u32)s + 2u : 255u;
        lptr A = lds + (s & 1) * 65536 + l16;
        if (!early) {
            if (!all_(ofl == OOB || fv >= (u32)s + 1)) wait_flags(rs, ofl, (u32)s + 1, dead);
            LS3(L == 0 && w == 1, 1, kq * 5 + ng);
            first(dst, ov);
        }
#define OT(k) do {} while (0)
        OM0(dst, 5);
        asm volatile("s_waitcnt vmcnt(8)\n\ts_barrier" ::: "memory");
        OT(0);
        v4u o[64];
#pragma unroll
        for (int k = 0; k < 4; ++k) o[k] = lds_r16(A + k * 1024);
        f32x16 acc;
        u32 far, nv;
        SCHED();
        SFOR(k, 64)
            if constexpr (k < 56) M32B(k == 0, acc, o[k], wo[k]); else M32V(acc, o[k], wo[k]);
            if constexpr (k < 60) o[k + 4] = lds_r16(A + (k + 4) * 1024);
            SCHED();
            (void)&srd; (void)&dst; (void)&ndst;
            if constexpr (k < 12 && (k & 1) == 0) ODMA(10 + k / 2, ov);
            if constexpr (k == 3) asm volatile("s_waitcnt vmcnt(6)\n\ts_barrier" ::: "memory");
            if constexpr (k == 3 || k == 7) OM0(dst, (k + 21) >> 2);
            if constexpr (k == 12) { ov += (u32)(((s + 1) & RM) - (s & RM)) * (NH * 16384u); PIN(ov); }
            if constexpr (k == 19) asm volatile("s_waitcnt vmcnt(6)\n\ts_barrier" ::: "memory");
            if constexpr (k == 20) LD4A(fv, ofl, 0);
            if constexpr (k == 35) asm volatile("s_waitcnt vmcnt(1)\n\ts_barrier" ::: "memory");
            if constexpr (k == 40) { asm volatile("s_waitcnt vmcnt(0)" : "+v"(fv) :: "memory"); far = (u32)__builtin_amdgcn_ballot_w64(fv < need) & 255u; PINS(far); }
            if constexpr (k == 41) { nv = far ? OOB : ov; PIN(nv); }
            if constexpr (k >= 42 && k < 62 && (k & 3) == 2) OM0(ndst, (k - 42) >> 2);
            if constexpr (k >= 43 && k < 63 && (k & 1)) ODMA((k - 43) >> 1, nv);
            if constexpr (k == 48) LD4A(fv, ofl, 0);
            if constexpr (k == 4) OT(1);
            if constexpr (k == 20) OT(2);
            if constexpr (k == 36) OT(3);
            if constexpr (k == 41) OT(4);
            SCHED();
        SEND
        OT(5);
        asm volatile("s_nop 11" : "+v"(acc), "+v"(pa));
#pragma unroll
        for (int i = 0; i < 8; ++i) st4s(rs, pa, (u32)((4 * (i >> 1) + (i & 1)) * (4 * DM * 4)), pk(acc[2 * i], acc[2 * i + 1]));
        asm volatile("s_waitcnt vmcnt(15)" : "+v"(fv) :: "memory");
        OT(6);
        LS3(L == 0 && w == 1, 2, kq * 5 + ng);
        early = far == 0u || all_(ofl == OOB || fv >= need);
        if (far != 0u && early) { first(ndst, ov); VMW(10); }
        else { fv = ld4(rs, ofl); asm volatile("s_waitcnt vmcnt(0)" : "+v"(fv) :: "memory"); }
        BAR();
        st4s(rs, fo, 0, (u32)s + 1);
        pa += (u32)(((s + 1) & RM) - (s & RM)) * P_ITEM;
        LS2(L == 0 && w == 1, 160 + kq * 5 + ng);
    }
    LDUMP(L == 0 && w == 1, kq * 5 + ng);
}

DEV void ln_stats2(const f32x2 (&x)[10], f32x2& mean, f32x2& rstd) {
    const f32x2 c = {f_(uu(u_(x[0].x))), f_(uu(u_(x[0].y)))};
    f32x2 d[10];
#pragma unroll
    for (int i = 0; i < 10; ++i) d[i] = x[i] - c;
    f32x2 a = ((d[0] + d[1]) + (d[2] + d[3])) + ((d[4] + d[5]) + (d[6] + d[7])) + (d[8] + d[9]);
    f32x2 b = (d[0] * d[0] + d[2] * d[2] + d[4] * d[4] + d[6] * d[6] + d[8] * d[8]) + (d[1] * d[1] + d[3] * d[3] + d[5] * d[5] + d[7] * d[7] + d[9] * d[9]);
    float a0, a1, b0, b1;
#define LN_DP4(m) "v_add_f32_dpp %0, %0, %0 " m " row_mask:0xf bank_mask:0xf\n\tv_add_f32_dpp %1, %1, %1 " m " row_mask:0xf bank_mask:0xf\n\t" \
                  "v_add_f32_dpp %2, %2, %2 " m " row_mask:0xf bank_mask:0xf\n\tv_add_f32_dpp %3, %3, %3 " m " row_mask:0xf bank_mask:0xf\n\t"
    asm("s_nop 1\n\t" LN_DP4("quad_perm:[1,0,3,2]") LN_DP4("quad_perm:[2,3,0,1]") LN_DP4("row_ror:4") LN_DP4("row_ror:8")
        "v_permlane16_swap_b32 %0, %1\n\tv_add_f32 %0, %0, %1\n\tv_permlane16_swap_b32 %2, %3\n\tv_add_f32 %2, %2, %3\n\ts_nop 1\n\t"
        "v_permlane32_swap_b32 %0, %2\n\tv_add_f32 %0, %0, %2\n\ts_nop 0\n\t"
        "v_readlane_b32 %4, %0, 0\n\tv_readlane_b32 %5, %0, 16\n\tv_readlane_b32 %6, %0, 32\n\tv_readlane_b32 %7, %0, 48\n\ts_nop 1"
        : "+v"(a.x), "+v"(b.x), "+v"(a.y), "+v"(b.y), "=s"(a0), "=s"(b0), "=s"(a1), "=s"(b1));
    const f32x2 m = (f32x2){a0, a1} * (1.0f / DM), v = (f32x2){b0, b1} * (1.0f / DM) - m * m;
    const f32x2 q = up2(pkv(up2(pkv((f32x2){fmaxf(v.x, 0.f), fmaxf(v.y, 0.f)})) + 1e-5f));
    const f32x2 sd = up2(pk(__builtin_amdgcn_sqrtf(q.x), __builtin_amdgcn_sqrtf(q.y)));
    mean = up2(pkv(c + m)); rstd = (f32x2){__builtin_amdgcn_rcpf(sd.x), __builtin_amdgcn_rcpf(sd.y)};
}
DEV void role_row(const Params& p, lptr lds, int id, v2u ep) {
    const int lane = threadIdx.x & 63, w = uni(threadIdx.x >> 6);
    const int pr = id >> 2, L = id & 1, par = (id >> 1) & 1;
    const rsrc_t rs = mk_rsrc(p.ws, W_END), rx = mk_rsrc(p.x, ACT);
    bool dead = false;
    const u32 fp = lane < 20 ? foff(F_P + L * 32 + lane) : OOB, l16 = (u32)lane * 16u, l8 = (u32)lane * 8u, l4 = (u32)lane * 4u, fb = uu(w * CV_W);
    lptr F = lds + w * CV_W;
    MK_SRD();
    for (int s = par; s < NSEG; s += 2) {
        const u32 ro = (u32)(s * 32 + 2 * pr) * (DM * 2);
        if ((s & 6) == 0) {
            if ((s & 30) == 0) pre_poll(rs, 0, s < 32 ? NPRE : NCV, s < 32 ? 3u : 5u, ep);
            if (id == 0 && s == 0) LST(274, rt32());
            if (s == 48) st8s(rs, (id == 1 && lane == 0) ? foff(F_EPOCH) : OOB, 0, (v2u){ep.x + 8u, ep.y});
#pragma unroll 1
            for (int i = 0; i < 5; ++i) {
                const u32 d = fb + i * 2048, o = W_Y + (u32)(16 * pr + (s >> 3)) * 10240u + i * 2048;
                DMA32(d, l16, o); DMA32(d + 10240u, l16, o + 81920u);
            }
            DRAIN();
        }
        RowX xr[2];
        auto xload = [&](rsrc_t r, u32 o, auto aux) {
#pragma unroll
            for (int i = 0; i < 2; ++i) {
                xr[i].a = __builtin_amdgcn_raw_buffer_load_b64(r, l8, o + i * (DM * 2), aux.value); xr[i].b = __builtin_amdgcn_raw_buffer_load_b64(r, l8 + 512u, o + i * (DM * 2), aux.value);
                xr[i].c = __builtin_amdgcn_raw_buffer_load_b32(r, l4 + 1024u, o + i * (DM * 2), aux.value);
            }
        };
        if (L == 0) xload(rx, ro, IC<0>{});
        f32x2 y[10], x[10];
        {
            lptr ya = F + (s & 7) * 2 + l16, yc = F + (s & 7) * 2 + 2048 + (lane >> 1) * 16 + (lane & 1) * 5120;
#pragma unroll
            for (int i = 0; i < 10; ++i) {
                lptr q = i < 8 ? ya + (i & 3) * 2560 + (i >> 2) * 1024 : yc + (i & 1) * 2560;
                y[i] = (f32x2){lo_f(*(LAS u16*)q), lo_f(*(LAS u16*)(q + 10240))}; PIN(y[i]);
            }
        }
        wait_flags(rs, fp, (u32)s + 1, dead);
        LS3(L == 0, 3, ri);
        LMX(L == 0, 0, s, rt32());
        {
            const u32 o = uu(W_P + (u32)(L * NSEG + (s & RM)) * P_ITEM + (u32)pr * (4 * DM * 4));
            if (L == 1) xload(rs, W_X2 + ro, IC<16>{});
            v4u a[4], b[4]; v2u c[4];
#pragma unroll
            for (int kq = 0; kq < 4; ++kq) { a[kq] = ld16s(rs, l16, o + kq * (DM * 4)); SCHED(); b[kq] = ld16s(rs, l16 + 1024u, o + kq * (DM * 4)); SCHED(); c[kq] = ld8s(rs, l8 + 2048u, o + kq * (DM * 4)); SCHED(); }
            const u32 u0[5] = {xr[0].a.x, xr[0].a.y, xr[0].b.x, xr[0].b.y, xr[0].c}, u1[5] = {xr[1].a.x, xr[1].a.y, xr[1].b.x, xr[1].b.y, xr[1].c};
#pragma unroll
            for (int i = 0; i < 5; ++i) { x[2 * i] = (f32x2){lo_f(u0[i]), lo_f(u1[i])}; x[2 * i + 1] = (f32x2){hi_f(u0[i]), hi_f(u1[i])}; }
#pragma unroll
            for (int kq = 0; kq < 4; ++kq)
#pragma unroll
                for (int i = 0; i < 10; ++i)
                    asm volatile("v_dot2_f32_bf16 %0, %2, %3, %0\n\tv_dot2_f32_bf16 %1, %2, %4, %1" : "+v"(x[i].x), "+v"(x[i].y)
                                 : "v"(i < 4 ? a[kq][i & 3] : i < 8 ? b[kq][i & 3] : c[kq][i & 1]), "s"(0x3F80u), "s"(0x3F800000u));
            asm volatile("s_nop 2" : "+v"(x[8]), "+v"(x[9]));
        }
        LS3(L == 0, 4, ri);
        f32x2 mean, rstd;
        ln_stats2(x, mean, rstd);
#pragma unroll
        for (int i = 0; i < 10; ++i) x[i] += (x[i] - mean) * (y[i] * rstd);
        u32 xp[2][5];
#pragma unroll
        for (int i = 0; i < 5; ++i) { xp[0][i] = pkv((f32x2){x[2 * i].x, x[2 * i + 1].x}); xp[1][i] = pkv((f32x2){x[2 * i].y, x[2 * i + 1].y}); }
        if (L == 0) {
#pragma unroll
            for (int i = 0; i < 5; ++i) { x[2 * i] = (f32x2){lo_f(xp[0][i]), lo_f(xp[1][i])}; x[2 * i + 1] = (f32x2){hi_f(xp[0][i]), hi_f(xp[1][i])}; }
            ln_stats2(x, mean, rstd);
#pragma unroll
            for (int i = 0; i < 10; ++i) x[i] = (x[i] - mean) * rstd;
#pragma unroll
            for (int r = 0; r < 2; ++r) {
                u32 hp[5];
#pragma unroll
                for (int i = 0; i < 5; ++i) hp[i] = pkv((f32x2){x[2 * i][r], x[2 * i + 1][r]});
                store_h(rs, W_H2, s * 32 + 2 * pr + r, hp);
            }
#pragma unroll
            for (int r = 0; r < 2; ++r) {
                st8s(rs, l8, W_X2 + ro + r * (DM * 2), (v2u){xp[r][0], xp[r][1]});
                st8s(rs, l8 + 512u, W_X2 + ro + r * (DM * 2), (v2u){xp[r][2], xp[r][3]});
                st4s(rs, l4 + 1024u, W_X2 + ro + r * (DM * 2), xp[r][4]);
            }
            LS3(L == 0, 5, ri);
            DRAIN();
            st4s(rs, lane == 0 ? foff(F_H2 + par * 16 + pr) : OOB, 0, (u32)s + 1);
        } else {
#pragma unroll
            for (int r = 0; r < 2; ++r) {
                u16* o_ = p.out + (size_t)(s * 32 + 2 * pr + r) * DM;
                *(v2u*)(o_ + 4 * lane) = (v2u){xp[r][0], xp[r][1]};
                *(v2u*)(o_ + 256 + 4 * lane) = (v2u){xp[r][2], xp[r][3]};
                *(u32*)(o_ + 512 + 2 * lane) = xp[r][4];
            }
        }
        if (pr == 0) LST(320 + L * 64 + s, rt32());
        LS2(L == 0, 224 + ri);
        LMX(L == 0, 1, s, rt32());
    }
    LDUMP(L == 0, 32 + ri);
}

constexpr float SCALE = 0.08838834764831845f, LOG2E = 1.4426950408889634f;
constexpr int C_K = 0, C_V = 8192, C_QX = 16384, C_MEM = 24576, C_Q = C_MEM + 10240, C_KM = C_Q + 2304, C_VM = C_KM + 2304, C_DUMP = C_VM + 2048,
              C_ZERO = C_DUMP + 1024, C_WL = C_ZERO + 128;
constexpr int NLF = 14;
static_assert(C_WL % 16 == 0 && C_WL + 4 * NLF * 2048 <= LDS_BYTES, "head lds");
#define CH_VIS 10
#define CH_H0 104
#define CH_W0 81
#define CH_MH 2
#define CH_MW 4
#define CH_REC 34
#define CH_RTY 32u
#define CH_RISE 12u
#define CH_FALL 3u
#define CH_PRE 9
#define CH_T0 190
#define CH_UP 2
#define CH_WX 6
#define WAIT_TXT(tmp, t4) "s_memrealtime vcc\n\t.p2align 5\n1:\n\ts_waitcnt lgkmcnt(0)\n\ts_sub_u32 " tmp ", " t4 ", vcc_lo\n\ts_memrealtime vcc\n\ts_cmpk_lt_u32 " tmp ", 400\n\ts_cbranch_scc1 1b\n\t" \
                          "s_add_u32 " tmp ", " tmp ", 4\n\ts_cselect_b32 " tmp ", " tmp ", 0\n2:\n\ts_sub_u32 " tmp ", " tmp ", 1\n\ts_cbranch_scc0 2b\n\t"
DEV void wait_until(u32 t) { u32 tmp; asm volatile(WAIT_TXT("%0", "%1") "s_waitcnt lgkmcnt(0)" : "=&s"(tmp) : "s"(uu(t - 4u)) : "vcc", "scc", "memory"); }
#define SMF_A(acc, a, b) asm volatile("v_smfmac_f32_16x16x64_bf16 %0, %1, %2, %3" : "+v"(acc) : "v"(a), "a"(b), "v"(idx))
#define SMF_V(acc, a, b) asm volatile("v_smfmac_f32_16x16x64_bf16 %0, %1, %2, %3" : "+v"(acc) : "v"(a), "v"(b), "v"(idx))
#define MF0(d, a, b) asm volatile("v_mfma_f32_16x16x32_bf16 %0, %1, %2, 0" : "=&v"(d) : "v"(a), "v"(b))
#define MF1(d, a, b) asm volatile("v_mfma_f32_16x16x32_bf16 %0, %1, %2, %0" : "+v"(d) : "v"(a), "v"(b))
#define MF0H(d, a, b) asm volatile("v_mfma_f32_16x16x16_bf16 %0, %1, %2, 0" : "=&v"(d) : "v"(a), "v"(b))
#define MF1H(d, a, b) asm volatile("v_mfma_f32_16x16x16_bf16 %0, %1, %2, %0" : "+v"(d) : "v"(a), "v"(b))
DEV v8u tr_frag(lptr p0, lptr p1, lptr p2, lptr p3) { return cat8(tr2(p0, p1), tr2(p2, p3)); }
constexpr int qslot(int ks) { return (ks & 1) && ks < 8 ? ks >> 1 : -1; }
constexpr int kslot(int ks) { return ks < 4 ? 4 + ks : -1; }
constexpr int vslot(int ks) { return ks >= 4 ? 4 + ks : -1; }
DEV u32 ring_map(u32 off, u32 half) { return ((off >> 13) << 14) + (half << 13) + (off & 8191u); }
#define ZRD(x, i) x = __builtin_bit_cast(f32x4, lds_r16(ZP + 16 * (i)))
#define VMAX(d, a, b) asm volatile("v_max_f32 %0, %1, %2" : "=v"(d) : "v"(a), "v"(b))
#define VMAX3(d, a, b, c_) asm volatile("v_max3_f32 %0, %1, %2, %3" : "=v"(d) : "v"(a), "v"(b), "v"(c_))
#define VEXP(d, a) asm volatile("v_exp_f32 %0, %1" : "=v"(d) : "v"(a))
#define VMULC(d, a) asm volatile("v_mul_f32 %0, 0x3e0293ee, %1" : "=v"(d) : "v"(a))
#define CADD(d, a, b) do { d = (a) + (b); PIN(d); } while (0)
#define CMULC(d, a) do { d = (a) * CEXP; PIN(d); } while (0)
#define CCVT(d, a, b) do { d = pk(a, b); PIN(d); } while (0)
#define LGK(n) __builtin_amdgcn_s_waitcnt(0xC07F | (n) << 8)
#define SEL(d, a, b) asm volatile("v_cndmask_b32 %0, %1, %2, %3" : "=v"(d) : "v"(a), "v"(b), "s"(himask))
#define QPERM(d, a, q) asm volatile("v_mov_b32_dpp %0, %1 quad_perm:" q " row_mask:0xf bank_mask:0xf" : "=v"(d) : "v"(a))

#define AL_H 3
#define AL_W 0
#define HD_SMP 56
DEV void role_head(const Params& p, lptr lds, int L, int h, v2u ep) {
    const int tid = threadIdx.x, lane = tid & 63, w = uni(tid >> 6), g = lane >> 4, r = lane & 15;
    const rsrc_t rs = mk_rsrc(p.ws, W_END);
    v8u wq0[10], wq1[10], wk0[10], wk1[10], wv0[10], wv1[10];
    lptr WL = lds + C_WL + w * (NLF * 2048) + lane * 16;
    {
        constexpr int GS = 1056, CS = 48 * GS;
        const v4u sq = mk_srd(p.Wq, DM * NQ * 2), sk = mk_srd(p.Wk, DM * NQ * 2), sv = mk_srd(p.Wv, DM * NQ * 2);
        const u32 vo = (u32)(g * NQ + h * DH + 8 * (r ^ ((g & 1) << 3))) * 2u;
        auto fill = [&](int ch) {
#pragma unroll 1
            for (int i = 0; i < 4; ++i) {
                const u32 dst = uu((ch % 3) * CS + (4 * i + w) * GS), so = uu((ch * 64 + 16 * i + 4 * w) * (NQ * 2));
                DMAIN(sq, dst, vo, so); DMAIN(sk, dst + 16 * GS, vo, so); DMAIN(sv, dst + 32 * GS, vo, so);
            }
        };
        fill(0); fill(1); fill(2);
        const int q4 = r >> 2;
        lptr tb = lds + (4 * (g >> 1) + 2 * (q4 >> 1) + (g & 1)) * GS + (q4 & 1) * 256 + ((w * 64 + (r & 3) * 8) ^ ((q4 & 1) << 7));
        SFOR(ch, 10)
            VMW(ch < 8 ? 24 : 108 - 12 * ch);
            BAR();
            auto frag = [&](int m, int t) { lptr b = tb + (ch % 3) * CS + m * 16 * GS + t * 32; return tr_frag(b, b + 512, b + 8 * GS, b + 8 * GS + 512); };
            wq0[ch] = frag(0, 0); wq1[ch] = frag(0, 1); wk0[ch] = frag(1, 0); wk1[ch] = frag(1, 1); wv0[ch] = frag(2, 0); wv1[ch] = frag(2, 1);
            asm volatile("" : "+a"(wq0[ch]), "+a"(wk0[ch]), "+v"(wv0[ch]), "+v"(wv1[ch]));
            if constexpr (qslot(ch) < 0) asm volatile("" : "+a"(wq1[ch])); else asm volatile("" : "+v"(wq1[ch]));
            if constexpr (kslot(ch) < 0) asm volatile("" : "+a"(wk1[ch])); else asm volatile("" : "+v"(wk1[ch]));
            BAR();
            if constexpr (ch < 7) fill(ch + 3);
        SEND
#pragma unroll
        for (int ch = 0; ch < 10; ++ch) {
            if (qslot(ch) >= 0) { lds_w16(WL + qslot(ch) * 2048, wq1[ch].lo); lds_w16(WL + qslot(ch) * 2048 + 1024, wq1[ch].hi); }
            if (kslot(ch) >= 0) { lds_w16(WL + kslot(ch) * 2048, wk1[ch].lo); lds_w16(WL + kslot(ch) * 2048 + 1024, wk1[ch].hi); }
            if (vslot(ch) >= 0) { lds_w16(WL + vslot(ch) * 2048, wv1[ch].lo); lds_w16(WL + vslot(ch) * 2048 + 1024, wv1[ch].hi); }
        }
        CFENCE();
    }
    const u32 idx = (r & 2) ? 0xEEEEu : 0x4444u;
    unsigned long long himask = 0xCCCCCCCCCCCCCCCCull;
    PINS(himask);
    bool dead = false;
    if (h == 3) LST(266 + L, rt32());
    if (w == 0) LST(1240 + L * 32 + h, rt32());
    if (L == 1) {
        KvIn in;
        kvt_load(p, KT0 + 4 * h + w, in, [] {}); kvt_make(rs, lds + w * 9216, KT0 + 4 * h + w, in);
        DRAIN(); BAR();
        pre_post(rs, 64 + h, 3u, ep);
    }
    const u32 pol0 = pol_wait(rs, ep);
    if (h == 3 && L == 0) LST(277, rt32());
    const u32 l16 = (u32)lane * 16u;
    const u32 mring = W_MRING + (u32)L * (MB / 4) + pol0 * 8192u, hint = mring + 4 * 32768u;
    const u32 fkh = lane < 3 ? foff(F_QKV + L * 96 + lane * 32 + h) : lane < 7 ? hint + (u32)(lane - 3) * 4u : OOB;
    u32 tm = 0;
    MK_SRD();
    u32 ksrc = uu(W_KQ + (u32)(L * (NSEG + RM) * NH + h) * 16384u + w * 2048), vsrc = ksrc + (W_VO - W_KQ);
    u32 dk = uu(C_K + w * 2048), dv = dk + (C_V - C_K), dq = dk + (C_QX - C_K), dm = uu(C_MEM + w * 2048), dm2 = uu(w < 2 ? C_MEM + 8192 + w * 1024 : C_DUMP);
    asm volatile("" : "+s"(dk), "+s"(dv), "+s"(dq), "+s"(dm), "+s"(dm2));
    auto keys_wait = [&](u32 need) {
#pragma unroll 1
        for (int n = 0; !dead; ++n) {
            const bool beat = need + 2 * L > 2u;
            if (beat) wait_until(tm);
            const u32 hv = ld4(rs, lane == 7 ? foff(F_ERR) : fkh);
            if (beat) { tm = rdl(hv, 5); wait_until(rdl(hv, 3) - 30u); }
            if (((u32)__builtin_amdgcn_ballot_w64(ld4(rs, fkh) < need) & 7u) == 0u) break;
            dead = n > SPIN_CAP || rdl(hv, 7) != 0u;
            st4s(rs, dead ? foff(F_ERR) : OOB, 0, 1u);
        }
        CFENCE();
        DMA32(dk, l16, ksrc); DMA32(dv, l16, vsrc); DMA32(dq, l16, ksrc ^ 8192u);
    };
    if (L == 0) { st4s(rs, tid == 0 ? hint + 4096u + (u32)h * 128u : OOB, 0, 0u); keys_wait(1u); }
    lptr qw = lds + C_Q + (2 * g) * 288 + (32 * w + 2 * r) * 2;
    lptr kw = qw + (C_KM - C_Q); PIN(kw);
    lptr qr = lds + C_Q + (2 * (r >> 2) + (r & 1)) * 288 + g * 16;
    lptr LB = lds + l16, VX = lds + C_V + w * 2048 + l16;
    PIN(VX);
    if (tid >= 64 && tid < 70) { const u32 z = tid == 68 ? 0x3F803F80u : tid == 69 ? PSENT : 0u; lds_w16(lds + C_ZERO + (tid - 64) * 16, (v4u){z, z, z, z}); }
    u32 zaddr = 0; PIN(zaddr);
    lptr ZP = lds + zaddr + C_ZERO;
    const float CEXP = SCALE * LOG2E;
#define LF_RD(slot, hf) lf[slot][hf] = lds_r16(WL + (slot) * 2048 + (hf) * 1024)
    BAR();
    const u32 ofl = tid == 0 ? foff(F_O + L * 32 + h) : OOB;
    u32 oso = (((g | L) & 1) ? OOB : 0u) + W_ORING + (u32)L * (MB / 2) + ring_map((u32)((r >> 3) * 32768 + h * 1024 + w * 256 + (g >> 1) * 128 + (r & 7) * 16), pol0);
    unsigned long long vis[8];
#pragma unroll
    for (int i = 0; i < 8; ++i) vis[i] = __builtin_amdgcn_ballot_w64(16 * (i >> 2) + 4 * g + (i & 3) <= 16 * (w & 1) + r);
    u32 missed = 0;
    for (int s = -L; s < NSEG;) {
        u32 sv = (u32)max(s, 0), ms = mring + (sv & 3u) * 32768u + w * 2048, ms2 = mring + (sv & 3u) * 32768u + 16384u + (w & 1) * 1024;
        asm volatile("" : "+s"(ms), "+s"(ms2)); PIN(sv);
        f32x4 aq0, aq1, ak0, ak1;
        v4u lf[NLF][2];
        ZRD(aq0, 0); ZRD(aq1, 1); ZRD(ak0, 2); ZRD(ak1, 3);
#pragma unroll
        for (int i = 0; i < 4; ++i) { LF_RD(i, 0); LF_RD(i, 1); }
        SCHED();
        v4u A[10];
#define HT(k) do {} while (0)
        v4u kx[8], qf[4], kmf[4], vx[2], ones, P;
        float fa, fb, fc, fd, inv, sx[4], t[4], tm0, tm1; u32 pa, pb, fl, e01, e23, pmm;
        f32x4 av0, av1, s0, s1, sm, oa, ob, rsum;
        v2u vm, pm;
#define LW(sl) cat8(lf[sl][0], lf[sl][1])
#define PQ(ks, tl) do { if constexpr (!(tl)) SMF_A(aq0, A[ks], wq0[ks]); else if constexpr (qslot(ks) < 0) SMF_A(aq1, A[ks], wq1[ks]); else { const v8u lw_ = LW(qslot(ks)); SMF_V(aq1, A[ks], lw_); } } while (0)
#define PK(ks, tl) do { if constexpr (!(tl)) SMF_A(ak0, A[ks], wk0[ks]); else if constexpr (kslot(ks) < 0) SMF_A(ak1, A[ks], wk1[ks]); else { const v8u lw_ = LW(kslot(ks)); SMF_V(ak1, A[ks], lw_); } } while (0)
#define PV(ks, tl) do { if constexpr (!(tl)) SMF_V(av0, A[ks], wv0[ks]); else if constexpr (vslot(ks) < 0) SMF_V(av1, A[ks], wv1[ks]); else { const v8u lw_ = LW(vslot(ks)); SMF_V(av1, A[ks], lw_); } } while (0)
#define KXR(i) kx[i] = lds_r16(LB + C_K + ((i) & 1) * 4096 + ((i) >> 1) * 1024)
#define XW(q_) do { *(LAS u32*)(q_) = pa; *(LAS u32*)((q_) + 288) = pb; } while (0)
        auto slot = [&](auto zc) __attribute__((always_inline)) {
            constexpr int z = decltype(zc)::value;
            if constexpr (z < 14) PQ(z >> 1, z & 1);
            else if constexpr (z < 17) PQ(z - 7, 0);
            else if constexpr (z < 20) PQ(z - 10, 1);
            else if constexpr (z < 34) PK((z - 20) >> 1, z & 1);
            else if constexpr (z < 37) PK(z - 27, 0);
            else if constexpr (z < 40) PK(z - 30, 1);
            else if constexpr (z < 42) { if constexpr (z == 40) MF0(s0, kx[0], qf[0]); else MF0(s1, kx[1], qf[0]); }
            else if constexpr (z < 48) { if constexpr (z & 1) MF1(s1, kx[z - 40], qf[(z - 40) >> 1]); else MF1(s0, kx[z - 40], qf[(z - 40) >> 1]); }
            else if constexpr (z < 54) PV(z - 48, 0);
            else if constexpr (z == 54) MF0(sm, kmf[0], qf[0]);
            else if constexpr (z < 58) MF1(sm, kmf[z - 54], qf[z - 54]);
            else if constexpr (z < 62) PV(z - 52, 0);
            else if constexpr (z < 64) PV(z - 62, 1);
            else if constexpr (z == 64) MF0(oa, vx[0], P);
            else if constexpr (z == 65) MF0(ob, vx[1], P);
            else if constexpr (z == 66) MF0(rsum, ones, P);
            else if constexpr (z < 75) PV(z < 70 ? z - 63 : z == 70 ? 2 : z == 71 ? 7 : z == 72 ? 3 : z - 65, 1);
            else { const v2u ones2 = ones.lo; MF1H(rsum, ones2, pm); }
            if constexpr (z < 6) A[4 + z] = lds_r16(LB + C_MEM + (4 + z) * 1024);
            if constexpr (z >= 6 && z < 10) LF_RD(4 + ((z - 6) >> 1), z & 1);
            if constexpr (z >= 14 && z < 18) LF_RD(6 + ((z - 14) >> 1), z & 1);
            if constexpr (z >= 22 && z < 28) KXR(z - 22);
            if constexpr (z == 28) XW(qw);
            if constexpr (z == 29 || z == 30) KXR(z - 23);
            if constexpr (z == 32 || z == 47) asm volatile("s_barrier" ::: "memory");
            if constexpr (z >= 33 && z < 37) qf[z - 33] = lds_r16(qr + (z - 33) * 64);
            if constexpr (z == 20) ZRD(av0, 0);
            if constexpr (z == 43) XW(kw);
            if constexpr (z == 44 || z == 45) vx[z - 44] = lds_r16(VX + (z - 44) * 1024);
            if constexpr (z >= 48 && z < 52) kmf[z - 48] = lds_r16(qr + (C_KM - C_Q) + (z - 48) * 64);
            if constexpr (z == 52) ZRD(av1, 1);
            if constexpr (z == 37) ones = lds_r16(ZP + 64);
            if constexpr (z >= 57 && z < 64) LF_RD(8 + ((z - 57) >> 1), (z - 57) & 1);
            if constexpr (z >= 65 && z < 70) LF_RD(8 + ((z - 58) >> 1), (z - 58) & 1);
            if constexpr (z == HD_SMP) LD4A(fl, fkh, 0);
            SCHED();
            if constexpr (z == 19) HT(2);
            if constexpr (z == 32) HT(3);
            if constexpr (z == 47) HT(4);
            if constexpr (z == 64) HT(5);
            if constexpr (z == 19) LGK(4);
            if constexpr (z == 21 || z == 41) LGK(1);
            if constexpr (z == 56 || z == 73) LGK(0);
            if constexpr (z == 31 || z == 46) LGK(2);
            if constexpr (z == 39) LGK(4);
            if constexpr (z >= 53 && z < 56) LGK(57 - z);
            if constexpr (z == 64) LGK(3);
            if constexpr (z == 68) LGK(5);
            if constexpr (z >= 70 && z < 73) LGK(74 - z);
            SCHED();
            if constexpr (z == 18) CADD(fa, aq0[0], aq0[2]);
            if constexpr (z == 19) CADD(fb, aq0[1], aq0[3]);
            if constexpr (z == 20) CMULC(fa, fa);
            if constexpr (z == 21) CADD(fc, aq1[0], aq1[2]);
            if constexpr (z == 22) CADD(fd, aq1[1], aq1[3]);
            if constexpr (z == 23) CMULC(fc, fc);
            if constexpr (z == 24) CMULC(fb, fb);
            if constexpr (z == 25) CMULC(fd, fd);
            if constexpr (z == 26) CCVT(pa, fa, fc);
            if constexpr (z == 27) CCVT(pb, fb, fd);
            if constexpr (z == 38) CADD(fa, ak0[0], ak0[2]);
            if constexpr (z == 39) CADD(fb, ak0[1], ak0[3]);
            if constexpr (z == 41) { CADD(fc, ak1[0], ak1[2]); CADD(fd, ak1[1], ak1[3]); }
            if constexpr (z == 42) { CCVT(pa, fa, fc); CCVT(pb, fb, fd); }
            if constexpr (z >= 49 && z < 53) SEL(sx[z - 49], s0[z - 49], s1[z - 49]);
            if constexpr (z >= 53 && z < 57) VEXP(t[z - 53], sx[z - 53]);
            if constexpr (z == 57) CCVT(e01, t[0], t[1]);
            if constexpr (z == 58) CCVT(e23, t[2], t[3]);
            if constexpr (z == 59) QPERM(P.x, e01, "[0,1,0,1]");
            if constexpr (z == 60) QPERM(P.z, e01, "[2,3,2,3]");
            if constexpr (z == 61) QPERM(P.y, e23, "[0,1,0,1]");
            if constexpr (z == 62) QPERM(P.w, e23, "[2,3,2,3]");
            if constexpr (z == 63) CADD(fa, av0[0], av0[2]);
            if constexpr (z == 64) { asm volatile("" :: "v"(sm)); VEXP(tm0, sm[0]); }
            if constexpr (z == 65) VEXP(tm1, sm[1]);
            if constexpr (z == 66) CADD(fb, av0[1], av0[3]);
            if constexpr (z == 67) CCVT(pmm, tm0, tm1);
            if constexpr (z >= 68 && z < 72) SEL(oa[z - 68], oa[z - 68], ob[z - 68]);
            if constexpr (z == 72) asm volatile("v_cndmask_b32 %0, %1, 0, %2" : "=v"(pm.x) : "v"(pmm), "s"(himask));
            if constexpr (z == 73) asm volatile("v_cndmask_b32 %0, 0, %1, %2" : "=v"(pm.y) : "v"(pmm), "s"(himask));
            if constexpr (z == 74) LGK(15);
            if constexpr (z == 75) CCVT(vm.x, fa, fb);
            SCHED();
        };
        {
            u32 tmp;
            asm volatile("s_mov_b32 m0, %2\n\t" WAIT_TXT("%0", "%1") "buffer_load_dwordx4 %4, %5, %6 offen sc1 lds\n\tbuffer_load_dwordx4 %4, %5, %6 offen offset:1024 sc1 lds\n\t"
                         "s_mov_b32 m0, %3\n\ts_nop 0\n\tbuffer_load_dwordx4 %4, %5, %7 offen sc1 lds\n\t.p2align 6\n\t.rept %8\n\ts_nop 0\n\t.endr\n\ts_waitcnt vmcnt(0) lgkmcnt(0)\n\ts_barrier"
                         : "=&s"(tmp) : "s"(uu(tm - 4u)), "s"(dm), "s"(dm2), "v"(l16), "s"(srd), "s"(ms), "s"(ms2), "i"(AL_H) : "vcc", "scc", "memory");
        }
        HT(1);
#pragma unroll
        for (int ks = 0; ks < 4; ++ks) A[ks] = lds_r16(LB + C_MEM + ks * 1024);
        SCHED();
        st4s(rs, ofl, 0, sv);
        SCHED();
        static_for<0, 76>(slot);
        asm volatile("s_nop 2\n\tv_add_f32 %1, %3, %5\n\tv_add_f32 %2, %4, %6\n\tv_cvt_pk_bf16_f32 %0, %1, %2"
                     : "=v"(vm.y), "=&v"(fc), "=&v"(fd) : "v"(av1[0]), "v"(av1[1]), "v"(av1[2]), "v"(av1[3]));
        __builtin_amdgcn_s_waitcnt(0xCF7F);
        asm volatile("s_nop 0\n\tv_mfma_f32_16x16x16_bf16 %0, %2, %3, %0\n\tv_rcp_f32 %1, %4" : "+v"(oa), "=&v"(inv) : "v"(vm), "v"(pm), "v"(rsum[0]), "v"(rsum));
        SCHED();
        if (__builtin_expect(__builtin_amdgcn_ballot_w64(u_(rsum[0]) - 0x21800000u > 0x3C000000u) != 0ull, 0)) {
            DRAIN(); SCHED();
            bool miss = false;
#pragma unroll 1
            for (int i = 0; i < 10; ++i) { const v4u a = lds_r16(LB + C_MEM + i * 1024); miss |= a.x == PSENT || a.y == PSENT || a.z == PSENT || a.w == PSENT; }
            if (missed < SPIN_CAP && !all_(!miss)) {
                *(LAS u32*)(lds + C_DUMP + (w == 0 ? 0 : 64)) = ld4(rs, foff(F_ERR));
                asm volatile("" : "+v"(fl) :: "memory");
                tm = rdl(fl, 5);
                BAR();
                missed = uu(*(LAS u32*)(lds + C_DUMP)) != 0u ? SPIN_CAP : missed + 1;
                st4s(rs, missed == SPIN_CAP ? foff(F_ERR) : OOB, 0, 1u);
                BAR();
                continue;
            }
            float mx = fmaxf(fmaxf(fmaxf(sx[0], sx[1]), fmaxf(sx[2], sx[3])), fmaxf(sm[0], sm[1]));
            mx = max32(max16(fmaxf(mx, dpp<0x4E>(mx))));
#pragma unroll
            for (int i = 0; i < 4; ++i) t[i] = __builtin_amdgcn_exp2f(sx[i] - mx);
            e01 = pk(t[0], t[1]); e23 = pk(t[2], t[3]);
            pmm = pk(__builtin_amdgcn_exp2f(sm[0] - mx), __builtin_amdgcn_exp2f(sm[1] - mx));
            asm volatile("v_cndmask_b32 %0, %2, 0, %3\n\tv_cndmask_b32 %1, 0, %2, %3" : "=&v"(pm.x), "=&v"(pm.y) : "v"(pmm), "s"(himask));
            asm volatile("s_nop 1" : "+v"(e01), "+v"(e23), "+v"(pm));
            QPERM(P.x, e01, "[0,1,0,1]"); QPERM(P.z, e01, "[2,3,2,3]"); QPERM(P.y, e23, "[0,1,0,1]"); QPERM(P.w, e23, "[2,3,2,3]");
            NOPS(1);
            MF0(rsum, ones, P); MF0(oa, vx[0], P); MF0(ob, vx[1], P);
            NOPS(7);
            { const v2u ones2 = ones.lo; MF1H(rsum, ones2, pm); }
#pragma unroll
            for (int i = 0; i < 4; ++i) SEL(oa[i], oa[i], ob[i]);
            NOPS(1);
            MF1H(oa, vm, pm);
            asm volatile("s_nop 7\n\tv_rcp_f32 %0, %1" : "=v"(inv) : "v"(rsum[0]), "v"(rsum));
        }
        {
            v4u oc; f32x2 u0 = oa.lo, u1 = oa.hi; const f32x2 i2 = {inv, inv};
            asm volatile("s_nop 0\n\tv_pk_mul_f32 %0, %0, %2\n\tv_pk_mul_f32 %1, %1, %2" : "+v"(u0), "+v"(u1) : "v"(i2));
            __builtin_amdgcn_s_waitcnt(0xCF7F);
            asm volatile("v_cvt_pk_bf16_f32 %0, %4, %5\n\tv_cvt_pk_bf16_f32 %2, %4, %5\n\tv_cvt_pk_bf16_f32 %1, %6, %7\n\tv_cvt_pk_bf16_f32 %3, %6, %7\n\t"
                         "v_permlane16_swap_b32 %0, %2\n\ts_nop 0\n\tv_permlane16_swap_b32 %1, %3"
                         : "=&v"(oc.x), "=&v"(oc.y), "=&v"(oc.z), "=&v"(oc.w) : "v"(u0.x), "v"(u0.y), "v"(u1.x), "v"(u1.y));
            st16(rs, oso, oc);
        }
        SCHED();
        unsigned long long clk;
        asm volatile("s_memrealtime %0" : "=s"(clk) :: "memory");
        HT(6);
        v4u tq[4], tk[8], tv[2];
        f32x4 c0, c1, cm;
        u32 oo, kvo; bool kvok;
        u32 ln = l16; PIN(ln);
        lptr QX = lds + C_QX + (w & 1) * 4096 + ln, VO = lds + C_V + (w ^ 1) * 2048 + ln, VMX = lds + C_VM + w * 512 + (ln >> 1);
#pragma unroll
        for (int i = 0; i < 2; ++i) { tq[i] = lds_r16(QX + i * 1024); kmf[i] = lds_r16(qr + (C_KM - C_Q) + i * 64); tk[2 * i] = lds_r16(LB + C_K + i * 1024); tk[2 * i + 1] = lds_r16(LB + C_K + 4096 + i * 1024); }
        *(LAS v2u*)VMX = vm;
        SCHED();
        SFOR(i, 12)
            if constexpr (i == 0) MF0(cm, kmf[0], tq[0]); else if constexpr (i == 1) MF0(c0, tk[0], tq[0]); else if constexpr (i == 2) MF0(c1, tk[1], tq[0]);
            else if constexpr (i % 3 == 0) MF1(cm, kmf[i / 3], tq[i / 3]); else if constexpr (i % 3 == 1) MF1(c0, tk[2 * (i / 3)], tq[i / 3]); else MF1(c1, tk[2 * (i / 3) + 1], tq[i / 3]);
            if constexpr (i < 2) kmf[2 + i] = lds_r16(qr + (C_KM - C_Q) + (2 + i) * 64);
            if constexpr (i == 0) { tq[2] = lds_r16(QX + 2048); tk[4] = lds_r16(LB + C_K + 2048); }
            if constexpr (i == 1) { tk[5] = lds_r16(LB + C_K + 6144); tq[3] = lds_r16(QX + 3072); }
            if constexpr (i == 2) { tk[6] = lds_r16(LB + C_K + 3072); tk[7] = lds_r16(LB + C_K + 7168); }
            if constexpr (i == 3) { tv[0] = lds_r16(VO); tv[1] = lds_r16(VO + 1024); }
            if constexpr (i == 4) { oo = ln + (ln & 0x300u) + ((vsrc ^ 8192u) + (u32)((w & 1) * 256) + ((u32)s & OOB)); PIN(oo); }
            if constexpr (i == 5) { const u32 d_ = (u32)(((s + 1) & RM) - (s & RM)) * (NH * 16384u); ksrc += d_; vsrc += d_; PINS(ksrc); PINS(vsrc); }
            if constexpr (i == 9) { asm volatile("s_waitcnt vmcnt(1)" : "+v"(fl) :: "memory"); kvok = ((u32)__builtin_amdgcn_ballot_w64(fl < (u32)s + 2) & 7u) == 0u; }
            if constexpr (i == 10) { kvo = l16 | (kvok ? 0u : OOB); PIN(kvo); }
            SCHED();
        SEND
        asm volatile("s_waitcnt lgkmcnt(0)\n\ts_barrier" : "+s"(clk) :: "memory");
        const v2u tvm = *(LAS v2u*)(VMX + (512 - (w & 1) * 1024));
        const v4u pv = lds_r16(ZP + 80);
        DMA32(dk, kvo, ksrc);
        SCHED(); asm volatile("" :: "v"(cm));
        float u[10], m, m2;
        {
            float NEG = -1000000000.0f / SCALE; PIN(NEG);
#define MSK(d, a, i) asm volatile("v_cndmask_b32 %0, %1, %2, %3" : "=v"(d) : "v"(NEG), "v"(a), "s"(vis[i]))
#pragma unroll
            for (int i = 0; i < 4; ++i) MSK(u[i], c0[i], i);
#pragma unroll
            for (int i = 0; i < 4; ++i) MSK(u[4 + i], c1[i], 4 + i);
            asm volatile("v_max3_f32 %0, %2, %3, %10\n\tv_max3_f32 %1, %4, %5, %11\n\tv_max3_f32 %0, %0, %6, %7\n\tv_max3_f32 %1, %1, %8, %9\n\t"
                         "v_max_f32 %0, %0, %1\n\tv_mov_b32 %1, %0\n\ts_nop 1\n\tv_permlane16_swap_b32 %0, %1\n\tv_max_f32 %0, %0, %1\n\tv_mov_b32 %1, %0\n\ts_nop 1\n\t"
                         "v_permlane32_swap_b32 %0, %1\n\tv_max_f32 %0, %0, %1\n\tv_mul_f32 %0, 0xbe0293ee, %0"
                         : "=&v"(m), "=&v"(m2) : "v"(u[0]), "v"(u[1]), "v"(u[2]), "v"(u[3]), "v"(u[4]), "v"(u[5]), "v"(u[6]), "v"(u[7]), "v"(cm[0]), "v"(cm[1]));
        }
        DMA32(dv, kvo, vsrc);
        {
            v4u px; v2u p0, p1;
#define EX2(d, a, b) asm volatile("v_fmamk_f32 %0, %2, 0x3e0293ee, %4\n\tv_fmamk_f32 %1, %3, 0x3e0293ee, %4\n\tv_exp_f32 %0, %0\n\tv_exp_f32 %1, %1" : "=&v"(d.x), "=&v"(d.y) : "v"(a), "v"(b), "v"(m))
            f32x2 ex[5];
            EX2(ex[0], u[0], u[1]); EX2(ex[1], u[2], u[3]); EX2(ex[2], u[4], u[5]); EX2(ex[3], u[6], u[7]); EX2(ex[4], cm[0], cm[1]);
            asm volatile("v_cvt_pk_bf16_f32 %0, %8, %9\n\tv_cvt_pk_bf16_f32 %1, %10, %11\n\tv_cvt_pk_bf16_f32 %2, %12, %13\n\tv_cvt_pk_bf16_f32 %3, %14, %15\n\t"
                         "v_mov_b32 %5, 0\n\tv_mov_b32 %6, 0\n\tv_cvt_pk_bf16_f32 %4, %16, %17\n\tv_cvt_pk_bf16_f32 %7, %16, %17"
                         : "=&v"(px.x), "=&v"(px.y), "=&v"(px.z), "=&v"(px.w), "=&v"(p0.x), "=&v"(p0.y), "=&v"(p1.x), "=&v"(p1.y)
                         : "v"(ex[0].x), "v"(ex[0].y), "v"(ex[1].x), "v"(ex[1].y), "v"(ex[2].x), "v"(ex[2].y), "v"(ex[3].x), "v"(ex[3].y), "v"(ex[4].x), "v"(ex[4].y));
            f32x4 o[4], sum;
            const v2u ones2 = ones.lo;
            const u32 now = (u32)clk;
            u32 oz;
            SCHED();
            MF0(sum, ones, px); { const u32 q = ksrc ^ 8192u; DMA32(dq, kvo, q); } SCHED();
            MF0(o[0], vx[0], px);
            MF0(o[1], vx[1], px);
            MF0(o[2], tv[0], px); oz = (oso & ~(3u << 17)) | (u32)((s + 3) & 3) << 17; PIN(oz); SCHED();
            MF0(o[3], tv[1], px); oso = (oso & ~(3u << 17)) | (u32)((s + 1) & 3) << 17; PIN(oso); SCHED();
            MF1H(sum, ones2, p0);
            MF1H(o[0], vm, p0);
            MF1H(o[1], vm, p1); st16(rs, oz, pv); SCHED();
            MF1H(o[2], tvm, p0);
            MF1H(o[3], tvm, p1);
            asm volatile("s_nop 2\n\tv_rcp_f32 %0, %1\n\ts_nop 0" : "=v"(inv) : "v"(sum[0]), "v"(sum));
            asm volatile("" : "+v"(o[0]), "+v"(o[1]));
            st16(rs, oo, (v4u){pk2(o[0].lo * inv), pk2(o[0].hi * inv), pk2(o[1].lo * inv), pk2(o[1].hi * inv)});
            asm volatile("" : "+v"(o[2]), "+v"(o[3]));
            st16(rs, oo ^ 2048u, (v4u){pk2(o[2].lo * inv), pk2(o[2].hi * inv), pk2(o[3].lo * inv), pk2(o[3].hi * inv)});
            SCHED();
            LS2(L == 0 && w == 0, h);
            st4s(rs, ((h & 15) == 0 && tid == 192) ? hint + 128u + (u32)h * 8u : OOB, 0, (u32)s << 16 | min(missed, 1u) << 14 | min(now - tm, 0x3FFFu));
            tm = rdl(fl, 5);
            missed = missed == SPIN_CAP ? missed : 0u;
            if (__builtin_expect(s < 1 - L, 0)) {
                if (s < 0) oso -= (ln & 256u) ? 0u : OOB;
#pragma unroll 1
                for (int n = 0; n < (s < 0 ? 0 : 200); ++n) {
                    CFENCE();
                    tm = rdl(ld4(rs, fkh), 6);
                    if (tm != PSENT) break;
                }
            }
        }
        if (__builtin_expect(!kvok, 0) && s + 1 < NSEG) {
            st4s(rs, ln == 0 ? hint + 4096u + (u32)h * 128u : OOB, 0, (u32)s + 1u);
            keys_wait((u32)s + 2u);
        }
        ++s;
    }
    DRAIN(); BAR();
    st4s(rs, ofl, 0, NSEG);
    LDUMP(w == 0, 160 + L * 32 + h);
}

#define WO_NL 3
#define WO_ROT 0
#define WO_AH 1
DEV void role_wo(const Params& p, lptr lds, int j, v2u ep) {
    const int tid = threadIdx.x, lane = tid & 63, w = uni(tid >> 6), g = lane >> 4, r = lane & 15;
    const int rg = j & 1, cg = j >> 1, rot = WO_ROT;
    const rsrc_t rs = mk_rsrc(p.ws, W_END);
    v8u wo[8][5];
    {
        const v4u so_ = mk_srd(p.Wo, DM * NQ * 2);
        u32 vo[5];
#pragma unroll
        for (int i = 0; i < 5; ++i) { const int n = 64 * i + lane; vo[i] = (u32)((n / 5) * DM + cg * 40 + (n % 5) * 8) * 2u; }
        v4u junk = {};
        const u32 av = (u32)(j * 5120 + lane * 16);
        auto ahead = [&](int b) {
#pragma unroll
            for (int i = 0; i < 5; ++i) AHEAD(so_, av, uu((8 * w + b) * (DH * DM * 2) + i * 1024));
        };
        auto fill = [&](int b) {
            if (b + WO_AH < 8) ahead(b + WO_AH);
            const u32 dst = uu(w * 40960 + (b & 3) * 10240), src = uu((8 * w + ((b + rot) & 7)) * (DH * DM * 2));
#pragma unroll
            for (int i = 0; i < 10; ++i) DMAIN(so_, dst + i * 1024, vo[i % 5], src + (i / 5) * (64 * DM * 2));
        };
#pragma unroll 1
        for (int b = 0; b < WO_AH; ++b) ahead(b);
#pragma unroll 1
        for (int b = 0; b < 4; ++b) fill(b);
        const int q4 = r >> 2;
        lptr tb = lds + w * 40960 + (32 * (g >> 1) + 16 * (q4 >> 1) + 8 * ((r >> 1) & 1) + 4 * (g & 1) + (q4 & 1)) * 80 + (r & 1) * 8;
        SFOR(b, 8)
            VMW((b < 5 ? 30 : 70 - 10 * b) + (b + 1 + WO_AH < 8 ? 5 : 0) + (b + 2 + WO_AH < 8 ? 5 : 0) + (b + 3 + WO_AH < 8 ? 5 : 0));
            if constexpr (b + WO_AH == 7) PIN(junk);
#pragma unroll
            for (int t = 0; t < 5; ++t) {
                lptr q = tb + (b & 3) * 10240 + t * 16;
                wo[b][t] = tr_frag(q, q + 160, q + 64 * 80, q + 64 * 80 + 160);
                if (b * 5 + t < 32) asm volatile("" : "+a"(wo[b][t])); else asm volatile("" : "+v"(wo[b][t]));
            }
            CFENCE();
            if constexpr (b < 4) fill(b + 4);
        SEND
    }
    if (j == 0) LST(265, rt32());
    LST(1100 + j * 4 + w, rt32());
    const u32 idx = (r & 2) ? 0xEEEEu : 0x4444u;
    const u32 pol0 = pol_wait(rs, ep);
    BAR();
    const u32 l16 = (u32)lane * 16u;
    u32 oo[8];
#pragma unroll
    for (int i = 0; i < 8; ++i) oo[i] = uu(W_ORING + ring_map((u32)(rg * 32768 + (8 * w + ((i + rot) & 7)) * 1024), pol0));
    const u32 hint = W_MRING + 4 * 32768u + pol0 * 8192u;
    u32 hvo[2];
#pragma unroll
    for (int l = 0; l < 2; ++l) hvo[l] = lane < 2 ? hint + (u32)(l ^ 1) * (MB / 4) + (u32)lane * 4u : lane == 2 ? hint + (u32)l * (MB / 4) + 128u : lane == 3 ? foff(F_ERR) : lane == 4 ? hint + (u32)l * (MB / 4) + 256u :
                 (lane >= 32 && j == 0 && w == 3) ? hint + (u32)l * (MB / 4) + (u32)lane * 128u : OOB;
    lptr xw = r < 8 ? lds + w * 640 + (2 * (g & 1) + (g >> 1)) * 32 + r * 4 : lds + 8192 + lane * 4;
    const int G = ((5 * cg) >> 1) + w, tt = 2 * G + ((lane >> 3) & 1) - 5 * cg;
    const bool pub = w < 3 && lane < 32 && tt >= 0 && tt < 5;
    lptr xr = lds + (pub ? tt * 128 + (2 * (lane >> 4) + ((lane >> 2) & 1)) * 32 + (lane & 3) * 8 : 0);
    const u32 mpo = pub ? W_MRING + ring_map((u32)(G * 256 + rg * 128 + lane * 4), pol0) : OOB;
    constexpr u32 PER0 = CH_W0 + CH_MW + CH_H0 + CH_MH + 2 * CH_VIS, DW0 = CH_W0 + CH_VIS + CH_MW + CH_REC;
    const bool lead = j == 0 && w == 3;
    const u32 hso = (lead && lane == 0) ? hint : OOB;
    MK_SRD();
    u32 t = 0, tn = 0;
    int sn[2] = {0, 0};
    u32 hav = 8 * CH_H0, wav = 8 * CH_W0, mh = 8 * CH_MH, mw = 8 * CH_MW, ok[2] = {0u, 0u}, dw[2] = {DW0, DW0};
    u32 vo = l16, early = PSENT, tp = 0, fin, left, cnt = 0;
    unsigned long long clk = 0;
    {
        if (w == 0) LST(1310 + j, rt32());
        if (lead) {
#pragma unroll 1
            for (int n = 0; n < SPIN_CAP; ++n) {
                CFENCE();
                const u32 a = ld4(rs, foff(F_QKV + lane)), b = ld4(rs, lane < 32 ? foff(F_QKV + 64 + lane) : hvo[0]);
                if (all_(a != 0u && b != (lane < 32 ? 0u : PSENT))) break;
                __builtin_amdgcn_s_sleep(1);
            }
        }
        if (lead) {
            t = rt32() + CH_T0; tn = t + PER0 / 2 + CH_REC;
            st16(rs, lane < 2 ? hint + (u32)lane * (MB / 4) : OOB, (v4u){lane == 0 ? t : tn, PER0, (lane == 0 ? t : tn) + DW0, t + DW0});
        } else {
#pragma unroll 1
            for (int n = 0; n < SPIN_CAP; ++n) {
                CFENCE();
                const u32 hv = ld4(rs, hvo[1]);
                t = rdl(hv, 0);
                if (rdl(hv, 1) < 400u) break;
                __builtin_amdgcn_s_sleep(1);
            }
        }
        clk = t - 100u;
    }
    const u32 tm0 = t + DW0;
    u32 zero = 0, hv = 0xFFFFu, pst = 1, pmp = OOB;
    const u32 psent = PSENT;
    lptr sw = lds + 2560 + w * 4;
    asm volatile("" : "+v"(zero), "+v"(sw));
    auto turn = [&](auto lc) __attribute__((always_inline)) {
        constexpr int L = decltype(lc)::value, M = L ^ 1;
        u32 done, rep, rd, lost, dh, dn, nt, za, pdur, mp; v4u hw;
        const int sc = sn[L];
        auto books = [&](auto ic) __attribute__((always_inline)) {
            constexpr int i = decltype(ic)::value;
            if constexpr (i == 0) { rep = max(rdl(hv, 2), rdl(hv, 4)); PINS(rep); }
            if constexpr (i == 1) { lost = (u32)(__builtin_amdgcn_ballot_w64(hv == (u32)sn[M]) >> 32); PINS(lost); }
            if constexpr (i == 2) { lost |= (rep >> 16) ^ (u32)sn[M]; PINS(lost); }
            if constexpr (i == 3) { lost |= ok[M] ^ 2u; rd = rep & 0xFFFFu; PINS(lost); PINS(rd); }
            if constexpr (i == 4) { lost = lost == 0u ? CH_UP * 8u : 0u; PINS(lost); }
            if constexpr (i == 5) { pdur = ((u32)clk - tp) * 8u; PINS(pdur); pdur = min(pdur, wav + CH_RISE); PINS(pdur); }
            if constexpr (i == 6) { pdur = (done & ok[M]) ? pdur : wav; PINS(pdur); }
            if constexpr (i == 7) { wav = max(pdur, wav - CH_FALL); PINS(wav); }
            if constexpr (i == 8) { nt = min(rd * 8u, hav + CH_RISE); PINS(nt); }
            if constexpr (i == 9) { nt = rd < 0x3FFFu ? nt : hav; PINS(nt); }
            if constexpr (i == 10) { hav = max(nt, hav - CH_FALL); PINS(hav); }
            if constexpr (i == 11) { mw = max(mw - 1u, CH_MW * 8u); PINS(mw); }
            if constexpr (i == 12) { mh = max(mh - 1u, CH_MH * 8u); PINS(mh); }
            if constexpr (i == 13) { rd = rd >> 14 == 1u ? lost : 0u; PINS(rd); }
            if constexpr (i == 14) { mw += rd; mh += lost - rd; PINS(mw); PINS(mh); }
            if constexpr (i == 15) { ok[M] = done ? 1u : (ok[M] & 1u) << 1; PINS(ok[M]); }
            if constexpr (i == 16) { dh = (hav + mh) / 8u + (sn[1] < 3 ? CH_VIS + CH_PRE : CH_VIS); PINS(dh); }
            if constexpr (i == 17) { dn = (wav + mw) / 8u + CH_VIS; PINS(dn); }
            if constexpr (i == 18) { nt = dw[L] + dh + (u32)max((int)((u32)clk + CH_WX - t), 0); PINS(nt); }
            if constexpr (i == 19) { dh += dn; dw[L] = dn; sn[M] += (int)done; PINS(dh); PINS(sn[M]); }
            if constexpr (i == 20) { rd = tn - t + (dh >> 1); PINS(rd); }
            if constexpr (i == 21) { lost = (u32)sc - 1u < NSEG - 2 ? CH_RTY : 0u; PINS(lost); nt += ok[L] != 1u ? lost : 0u; PINS(nt); }
            if constexpr (i == 22) { nt = t + (u32)max((int)nt, (int)rd); PINS(nt); }
            if constexpr (i == 23) { hw.x = nt; hw.y = dh; PIN(hw.x); PIN(hw.y); }
            if constexpr (i == 24) { hw.z = nt + dn; hw.w = tm0; PIN(hw); }
            if constexpr (i == 25) st16(rs, hso + L * (MB / 4), hw);
        };
        {
#define WT(k) do {} while (0)
            WT(0);
            v4u d[8];
            {
                u32 tmp;
                asm volatile(WAIT_TXT("%1", "%2") "buffer_load_dwordx4 %0, %3, %4, %5 offen sc1\n\ts_waitcnt lgkmcnt(0)"
                             : "=&v"(d[0]), "=&s"(tmp) : "s"(uu(t - 4u)), "v"(vo), "s"(srd), "s"(oo[0]) : "vcc", "scc", "memory");
                PINS(clk);
            }
            SFOR(i, 7)
                asm volatile("buffer_load_dwordx4 %0, %1, %2, %3 offen sc1" : "=v"(d[i + 1]) : "v"(vo), "s"(srd), "s"(oo[i + 1]) : "memory");
                SCHED();
                if constexpr (i == 0) { done = uu(pst) ^ 1u; PINS(done); }
                static_for<3 * i, 3 * i + 3>(books);
                SCHED();
            SEND
            LD4A(hv, hvo[L], 0);
            SCHED();
            WT(1);
            static_for<21, 26>(books);
            za = done ? pmp : OOB;
            st4s(rs, za, 0, psent);
            *(LAS u32*)(sw + L * 4096) = zero;
            f32x4 acc[5] = {};
            mp = mpo + (L * (MB / 4) + (u32)((sc + 1) & 3) * 32768u); pmp = mpo + (L * (MB / 4) + (u32)(sc & 3) * 32768u);
            asm volatile("" : "+v"(acc[0]), "+v"(acc[1]), "+v"(acc[2]), "+v"(acc[3]), "+v"(acc[4]), "+v"(mp), "+v"(pmp));
            SCHED();
            float f0[5], f1[5]; unsigned long long bad;
#define WSW(o_) asm volatile("v_permlane32_swap_b32 %0, %1" : "+v"(f0[o_]), "+v"(f1[o_]))
#define WAD(o_) asm volatile("v_add_f32_dpp %0, %1, %0 row_ror:8 row_mask:0xf bank_mask:0xf" : "+v"(f0[o_]) : "v"(f1[o_]))
#define WFO(o_) do { VADD(f0[o_], acc[o_][0], acc[o_][2]); VADD(f1[o_], acc[o_][1], acc[o_][3]); } while (0)
#define WWR(o_) *(LAS float*)(xw + L * 4096 + (o_) * 128) = f0[o_]
            u32 th, per, so, erl = early;
            auto next = [&](auto ic_) __attribute__((always_inline)) {
                constexpr int i = decltype(ic_)::value;
                if constexpr (i == 0) { so = (u32)sn[M] << 17; PINS(so); }
                if constexpr (i == 1) { so = (so & 0x60000u) + M * (MB / 2); PINS(so); }
                if constexpr (i == 2) { so = sn[M] + 1 < NSEG ? so : OOB; PINS(so); }
                if constexpr (i == 3) { early = sn[M] + 1 < NSEG ? PSENT : 0u; PIN(early); }
                if constexpr (i == 4) { vo = l16 + so; PIN(vo); }
                if constexpr (i == 5 && L) { fin = (u32)(sn[0] + sn[1] + 2); PINS(fin); }
                if constexpr (i == 6 && L) { fin = (fin | uu(left)) >> 7; PIN(fin); }
                if constexpr (i == 6 && !L) { cnt += 1u; PIN(cnt); left = cnt >> 9; PIN(left); }
                if constexpr (i == 7) { PIN(hv); th = rdl(hv, 0); PINS(th); }
                if constexpr (i == 8) { per = rdl(hv, 1); PINS(per); so = th - t; PINS(so); }
                if constexpr (i == 9) { per = (int)so < 20 ? per : 0u; PINS(per); }
                if constexpr (i == 10) { th += per; PINS(th); if constexpr (L) { per = rdl(hv, 3); PINS(per); } }
                if constexpr (i == 11) { tp = t; t = lead ? tn : th; PINS(t); }
                if constexpr (i == 12) { if constexpr (L) { fin |= per; PIN(fin); } tn = nt; }
            };
            auto slot = [&](auto ic_) __attribute__((always_inline)) {
                constexpr int i = decltype(ic_)::value;
                constexpr int NL = WO_NL, I0 = 40 - 5 * NL, pc = i < I0 ? i / 5 : 8 - NL + (i - I0) % NL, oc = i < I0 ? i % 5 : (i - I0) / NL;
                if constexpr (oc == 0) asm volatile("s_waitcnt vmcnt(%1)" : "+v"(d[pc]) : "i"(10 - pc));
                if constexpr (pc * 5 + oc < 32) SMF_A(acc[oc], d[pc], wo[pc][oc]); else SMF_V(acc[oc], d[pc], wo[pc][oc]);
                SCHED();
                if constexpr (i == 0) WT(2);
                if constexpr (i >= 5 && i < 13 && i != 9) next(IC<i - 5 - (i > 9)>{});
                if constexpr (i == 20) WT(4);
                constexpr int e = i - (I0 + NL + 1), o = e < 0 ? 0 : e / 3;
                if constexpr (e >= 0 && e % 3 == 0) { CADD(f0[o], acc[o][0], acc[o][2]); if constexpr (o > 0) WAD(o - 1); }
                if constexpr (e >= 0 && e % 3 == 1) { if constexpr (o > 0) { WWR(o - 1); SCHED(); } CADD(f1[o], acc[o][1], acc[o][3]); }
                if constexpr (e >= 0 && e % 3 == 2) {
                    if constexpr (o == 0) asm volatile("v_cmp_u_f32 %0, %1, %2" : "=s"(bad) : "v"(f0[0]), "v"(f1[0]));
                    else LGK(15);
                    WSW(o);
                }
                SCHED();
            };
            asm volatile(".rept %0\n\ts_nop 0\n\t.endr" :: "i"(AL_W));
            static_for<0, 2>(slot);
            if (__builtin_expect(__builtin_amdgcn_ballot_w64(d[0].x == erl) != 0ull, 0)) {
                asm volatile("s_waitcnt vmcnt(3)" :: "v"(d[1]), "v"(d[2]), "v"(d[3]), "v"(d[4]), "v"(d[5]), "v"(d[6]), "v"(d[7]) : "memory");
                SCHED();
                *(LAS u32*)(sw + L * 4096) = 1u;
                static_for<0, 7>(next);
            } else {
            static_for<2, 40>(slot);
            WT(5);
            static_assert(WO_NL == 3, "tail");
            if (__builtin_expect(bad != 0ull, 0)) {
                bool miss = false;
#pragma unroll
                for (int i = 0; i < 8; ++i) miss |= d[i].x == PSENT;
                if (all_(!miss)) {
#pragma unroll
                    for (int i = 0; i < 8; ++i) miss |= d[i].y == PSENT || d[i].z == PSENT || d[i].w == PSENT;
                }
                *(LAS u32*)(sw + L * 4096) = all_(!miss) ? 0u : 1u;
            }
            asm volatile("v_permlane32_swap_b32 %0, %1\n\ts_nop 1\n\tv_add_f32_dpp %0, %1, %0 row_ror:8 row_mask:0xf bank_mask:0xf\n\tv_add_f32 %2, %4, %6\n\tv_add_f32 %3, %5, %7"
                         : "+v"(f0[3]), "+v"(f1[3]), "=&v"(f0[4]), "=&v"(f1[4]) : "v"(acc[4][0]), "v"(acc[4][1]), "v"(acc[4][2]), "v"(acc[4][3]));
            WWR(3); SCHED();
            asm volatile("s_nop 0\n\tv_permlane32_swap_b32 %0, %1\n\ts_nop 1\n\tv_add_f32_dpp %0, %1, %0 row_ror:8 row_mask:0xf bank_mask:0xf" : "+v"(f0[4]), "+v"(f1[4]));
            WWR(4);
            }
            WT(6);
            f32x2 a, b, c, e; u32 mq; v4u sv;
            asm volatile("s_waitcnt lgkmcnt(0)\n\ts_barrier\n\tds_read_b128 %4, %6 offset:%7+2560\n\t"
                         "ds_read_b64 %0, %5 offset:%7\n\tds_read_b64 %1, %5 offset:%7+640\n\tds_read_b64 %2, %5 offset:%7+1280\n\tds_read_b64 %3, %5 offset:%7+1920"
                         : "=&v"(a), "=&v"(b), "=&v"(c), "=&v"(e), "=&v"(sv) : "v"(xr), "v"(zero), "i"(L * 4096) : "memory");
            asm volatile("s_waitcnt lgkmcnt(4)\n\tv_or_b32 %4, %6, %7\n\tv_or3_b32 %4, %4, %8, %9\n\tv_lshl_or_b32 %5, %4, 31, %10\n\t"
                         "s_waitcnt lgkmcnt(2)\n\tv_pk_add_f32 %0, %0, %1\n\ts_waitcnt lgkmcnt(0)\n\tv_pk_add_f32 %2, %2, %3\n\ts_nop 0\n\tv_pk_add_f32 %0, %0, %2"
                         : "+v"(a), "+v"(b), "+v"(c), "+v"(e), "=&v"(pst), "=&v"(mq) : "v"(sv.x), "v"(sv.y), "v"(sv.z), "v"(sv.w), "v"(mp) : "memory");
            st4s(rs, mq, 0, pkv(a));
            SCHED();
            asm volatile("s_memrealtime %0" : "=s"(clk));
            VMW(3); SCHED();
            static_for<7, 13>(next);
            WT(7);
        }
    };
    do {
        turn(IC<0>{});
        turn(IC<1>{});
    } while (uu(fin) == 0u);
    if (sn[0] + 1 < NSEG || sn[1] + 1 < NSEG) st4s(rs, tid == 0 ? foff(F_ERR) : OOB, 0, 1u);
    if (w == 0) LST(1900 + j, rt32());
    LDUMP(w == 0, 224 + j);
}

__global__ void __launch_bounds__(256, 1) k_main(Params p) {
    lptr lds = (lptr)(u32)16 - 16;
    if (blockIdx.x == 0) LST(256, rt32());
    unsigned long long pc; int len;
    asm volatile("s_getpc_b64 %0\n.Lk_main_here:\n\ts_mov_b32 %1, .Lk_end_here-.Lk_main_here" : "=s"(pc), "=s"(len));
    const rsrc_t rs = mk_rsrc(p.ws, W_END);
    const v2u e0 = ld8s(rs, threadIdx.x == 0 ? foff(F_EPOCH) : OOB, 0);
    const v4u code = __builtin_amdgcn_raw_buffer_load_b128(mk_rsrc((const void*)(pc & ~15ull), (u32)max(len, 0)), (((blockIdx.x >> 3) & 31u) * 256u + threadIdx.x) * 16u, 0, 0);
    CFENCE();
    auto conv_id = [](int role) {
        const int x = role >> 5, i = role & 31, no = x < 5 ? 5 : 10, og = x < 5 ? x - 3 : 2 * (x - 4) + (i >= 5), r = (x - 3) * 5 + i - no, q = r / 3;
        return x < 3 || i >= no + 5 || r >= 24 ? NCV : i < no ? ((og & 1) ? (og >> 1) * 5 + i % 5 : NCV) : r % 3 == 0 ? 20 + (q & 1) * 20 + (q >> 1) : 16 + (r % 3) * 8 + q;
    };
    int role = (int)(((u32)__builtin_amdgcn_s_getreg(20 | (3 << 11)) & 7u) * 32u + (blockIdx.x >> 3));
    Early e;
    v4u rv;
#pragma unroll 1
    for (int n = 0;; ++n) {
        conv_early(p, rs, conv_id(role), e);
        if (n) break;
        CFENCE();
        if (threadIdx.x == 0) {
            GAS unsigned long long* slots = (GAS unsigned long long*)(p.ws + W_CTRL + F_SLOT * 128);
            const unsigned long long tag = (unsigned long long)e0.y << 32 | (e0.x + 1u);
            u32 mine = (u32)role;
#pragma unroll 1
            for (int k = 0; k < 256 && __hip_atomic_exchange(slots + mine * 16, tag, __ATOMIC_RELAXED, __HIP_MEMORY_SCOPE_AGENT) == tag; ++k) mine = (mine + 1u) & 255u;
            lds_w16(lds, (v4u){mine, e0.x, e0.y, 0u});
        }
        BAR();
        rv = lds_r16(lds);
        BAR();
        if (uni((int)rv.x) == role) break;
        role = uni((int)rv.x);
    }
    const int x = role >> 5, i = role & 31, w = uni(threadIdx.x >> 6), no = x < 5 ? 5 : 10, nr = x < 7 ? 5 : 4, j = conv_id(role);
    const v2u ep = {uu(rv.y), uu(rv.z)};
    asm volatile("" :: "v"(code));
    if (x < 2) { role_head(p, lds, i >> 4, x * 16 + (i & 15), ep); __builtin_amdgcn_endpgm(); }
    if (x == 2) { role_wo(p, lds, i, ep); __builtin_amdgcn_endpgm(); }
    if (i >= no + nr) { role_qkv(p, lds, (x < 5 ? (x - 3) * 22 : 44 + (x - 5) * 17) + i - no - nr, ep); __builtin_amdgcn_endpgm(); }
    const bool cv = j >= NPRE || (j >= 20 && j < 24);
    if (j < NCV) conv_stage(p, rs, lds, j, (j & 3) * 2 + (j >= NPRE), cv ? 40 : 0, ep, e);
    if (i >= no) {
        if (!cv) role_row(p, lds, (j - 24) * 4 + w, ep);
        __builtin_amdgcn_endpgm();
    }
    const int og = x < 5 ? x - 3 : 2 * (x - 4) + (i >= 5);
    role_out(p, lds, og & 1, og >> 1, i % 5, ep);
}
__global__ void k_end() { asm volatile(".Lk_end_here:"); }

extern "C" void kernel_launch(void* const* d_in, const int* in_sizes, int n_in, void* d_out, int out_size, void* d_ws, size_t ws_size,
                              hipStream_t stream) {
    Params p;
    p.x = (const u16*)d_in[0]; p.mem0 = (const u16*)d_in[1]; p.Wq = (const u16*)d_in[2]; p.Wk = (const u16*)d_in[3];
    p.Wv = (const u16*)d_in[4]; p.Wo = (const u16*)d_in[5];
    p.bk = (const u16*)d_in[8]; p.bv = (const u16*)d_in[9]; p.shift = (const u16*)d_in[10]; p.ssmk = (const u16*)d_in[11]; p.ssmD = (const u16*)d_in[12];
    p.out = (u16*)d_out;
    p.ws = (unsigned char*)(((size_t)d_ws + 2 * MB - 1) & ~(size_t)(2 * MB - 1));
    (void)hipFuncSetAttribute((const void*)k_main, hipFuncAttributeMaxDynamicSharedMemorySize, LDS_BYTES);
    hipLaunchKernelGGL(k_main, dim3(256), dim3(256), LDS_BYTES, stream, p);
}
```

```cpp
#define MEAS 0
#include <hip/hip_runtime.h>
#include <hip/hip_bf16.h>

typedef unsigned short u16;
typedef unsigned int u32;
typedef u32 v2u __attribute__((ext_vector_type(2)));
typedef u32 v4u __attribute__((ext_vector_type(4)));
typedef u32 v8u __attribute__((ext_vector_type(8)));
typedef float f32x2 __attribute__((ext_vector_type(2)));
typedef float f32x4 __attribute__((ext_vector_type(4)));
typedef float f32x16 __attribute__((ext_vector_type(16)));
typedef __bf16 bf16x2 __attribute__((ext_vector_type(2)));
typedef __bf16 bf16x8 __attribute__((ext_vector_type(8)));
typedef short s16x4 __attribute__((ext_vector_type(4)));
#define LAS __attribute__((address_space(3)))
#define GAS __attribute__((address_space(1)))
typedef LAS unsigned char* lptr;
#define DEV __device__ __forceinline__

constexpr int SEQ = 2048, DM = 640, NH = 32, DH = 128, NSEG = 64, NQ = NH * DH;

constexpr u32 MB = 1u << 20;
constexpr u32 W_ORING = 0;
constexpr u32 W_MRING = MB;
constexpr u32 W_PROBE = 2 * MB - 65536;
constexpr u32 W_CTRL = 4 * MB;
constexpr u32 CTRL_BYTES = 128 * 1024;
constexpr u32 ACT = SEQ * DM * 2;
constexpr u32 W_H1 = 5 * MB, W_Y = W_H1 + ACT, W_H2 = W_Y + ACT, W_X2 = W_H2 + ACT;
constexpr u32 W_KQ = 16 * MB;
constexpr u32 W_VO = W_KQ + 64 * MB;
constexpr u32 W_P = W_VO + 64 * MB;
constexpr u32 P_ITEM = 16 * 4 * DM * 4;
constexpr u32 W_KVT = W_P + 4 * NSEG * P_ITEM;
constexpr u32 W_SP = W_KVT + ACT;
constexpr u32 W_END = W_KVT + 3 * MB;
static_assert((size_t)W_END + 2 * MB <= 256ull * MB, "workspace");
constexpr u32 F_ERR = 0, F_QKV = 16, F_O = F_QKV + 192, F_P = F_O + 64, F_H2 = F_P + 64, F_CLR = F_H2 + 32,
              F_EPOCH = F_CLR + 32, F_SLOT = F_EPOCH + 1, F_PRE = F_SLOT + 256, F_N = F_PRE + 192;
constexpr int NCV = 44, NPRE = 40;
static_assert(F_N * 128 <= CTRL_BYTES, "ctrl");
DEV u32 foff(u32 idx) { return W_CTRL + idx * 128u; }

constexpr u32 OOB = 0x80000000u;
constexpr u32 PSENT = 0x7F817F81u;
constexpr int SPIN_CAP = 40000;
constexpr int LDS_BYTES = 160 * 1024;

#define LDUMP(cond, id) do {} while (0)
#define LMX(cond, k, s_, v) do {} while (0)
#define LS3(cond, slot, idx) do {} while (0)
#define LST(idx, v) do {} while (0)
#define LS2(cond, idx) do {} while (0)
struct Params {
    const u16 *x, *mem0, *Wq, *Wk, *Wv, *Wo, *bk, *bv, *shift, *ssmk, *ssmD;
    u16* out;
    unsigned char* ws;
};

typedef __amdgpu_buffer_rsrc_t rsrc_t;
DEV rsrc_t mk_rsrc(const void* p, u32 bytes) { return __builtin_amdgcn_make_buffer_rsrc((void*)p, 0, bytes, 0x00020000); }
DEV v4u ld16s(rsrc_t r, u32 off, u32 so) { return __builtin_amdgcn_raw_buffer_load_b128(r, off, so, 16); }
DEV v2u ld8s(rsrc_t r, u32 off, u32 so) { return __builtin_amdgcn_raw_buffer_load_b64(r, off, so, 16); }
DEV v4u ldin16(rsrc_t r, u32 off, u32 so) { return __builtin_amdgcn_raw_buffer_load_b128(r, off, so, 2); }
DEV v2u ldin8(rsrc_t r, u32 off, u32 so) { return __builtin_amdgcn_raw_buffer_load_b64(r, off, so, 2); }
DEV u32 ld4(rsrc_t r, u32 off) { return __builtin_amdgcn_raw_buffer_load_b32(r, off, 0, 16); }
DEV void st16(rsrc_t r, u32 off, v4u v) { __builtin_amdgcn_raw_buffer_store_b128(v, r, off, 0, 16); }
DEV void st8s(rsrc_t r, u32 off, u32 so, v2u v) { __builtin_amdgcn_raw_buffer_store_b64(v, r, off, so, 16); }
DEV void st4s(rsrc_t r, u32 off, u32 so, u32 v) { __builtin_amdgcn_raw_buffer_store_b32(v, r, off, so, 16); }
#define DRAIN() asm volatile("s_waitcnt vmcnt(0)" ::: "memory")
#define VMW(n) asm volatile("s_waitcnt vmcnt(%0)" :: "i"(n) : "memory")
#define BAR() asm volatile("s_waitcnt lgkmcnt(0)\n\ts_barrier" ::: "memory")
#define SCHED() __builtin_amdgcn_sched_barrier(0)
#define CFENCE() asm volatile("" ::: "memory")
#define NOPS(n) asm volatile("s_nop %0" :: "i"(n))
#define PIN(x) asm volatile("" : "+v"(x))
#define PINS(x) asm volatile("" : "+s"(x))

template <int V> struct IC { static constexpr int value = V; };
template <int I, int N, class F> DEV void static_for(F&& f) { if constexpr (I < N) { f(IC<I>{}); static_for<I + 1, N>(f); } }
#define SFOR(var, n) static_for<0, n>([&](auto ic_) __attribute__((always_inline)) { constexpr int var = decltype(ic_)::value;
#define SEND });

DEV u32 pk(float a, float b) { bf16x2 v = {(__bf16)a, (__bf16)b}; return __builtin_bit_cast(u32, v); }
DEV float lo_f(u32 u) { return __builtin_bit_cast(float, u << 16); }
DEV float hi_f(u32 u) { return __builtin_bit_cast(float, u & 0xffff0000u); }
DEV f32x2 up2(u32 u) { return (f32x2){lo_f(u), hi_f(u)}; }
DEV u32 pk2(f32x2 v) { return pk(v.x, v.y); }
DEV float rbf(float a) { return (float)(__bf16)a; }
DEV v4u lds_r16(lptr p) { return *(LAS v4u*)p; }
DEV void lds_w16(lptr p, v4u v) { *(LAS v4u*)p = v; }
DEV v2u lds_tr(lptr p) { return __builtin_bit_cast(v2u, __builtin_amdgcn_ds_read_tr16_b64_v4i16((LAS s16x4*)p)); }
DEV v4u tr2(lptr p0, lptr p1) { const v2u a = lds_tr(p0), b = lds_tr(p1); return (v4u){a.x, a.y, b.x, b.y}; }
DEV v8u cat8(v4u a, v4u b) { return __builtin_shufflevector(a, b, 0, 1, 2, 3, 4, 5, 6, 7); }
DEV f32x4 mfma16(v4u a, v4u b, f32x4 c) { return __builtin_amdgcn_mfma_f32_16x16x32_bf16(__builtin_bit_cast(bf16x8, a), __builtin_bit_cast(bf16x8, b), c, 0, 0, 0); }
DEV f32x16 mfma32(v4u a, v4u b, f32x16 c) { return __builtin_amdgcn_mfma_f32_32x32x16_bf16(__builtin_bit_cast(bf16x8, a), __builtin_bit_cast(bf16x8, b), c, 0, 0, 0); }

DEV float f_(u32 u) { return __builtin_bit_cast(float, u); }
DEV u32 u_(float f) { return __builtin_bit_cast(u32, f); }
template <int CTRL> DEV float dpp(float v) { return f_((u32)__builtin_amdgcn_update_dpp(0, (int)u_(v), CTRL, 0xf, 0xf, false)); }
DEV float max16(float v) { auto r = __builtin_amdgcn_permlane16_swap(u_(v), u_(v), false, false); return fmaxf(f_(r[0]), f_(r[1])); }
DEV float max32(float v) { auto r = __builtin_amdgcn_permlane32_swap(u_(v), u_(v), false, false); return fmaxf(f_(r[0]), f_(r[1])); }
DEV float add16(float v) { auto r = __builtin_amdgcn_permlane16_swap(u_(v), u_(v), false, false); return f_(r[0]) + f_(r[1]); }
DEV float add32(float v) { auto r = __builtin_amdgcn_permlane32_swap(u_(v), u_(v), false, false); return f_(r[0]) + f_(r[1]); }
DEV float wave_sum(float v) {
    v += dpp<0xB1>(v);
    v += dpp<0x4E>(v);
    v += dpp<0x124>(v);
    v += dpp<0x128>(v);
    return add32(add16(v));
}
DEV bool all_(bool c) { return __builtin_amdgcn_ballot_w64(!c) == 0ull; }
DEV int uni(int v) { return __builtin_amdgcn_readfirstlane(v); }
DEV u32 uu(u32 v) { return (u32)__builtin_amdgcn_readfirstlane((int)v); }
DEV u32 rdl(u32 v, int l) { return (u32)__builtin_amdgcn_readlane((int)v, l); }
DEV u32 rt32() { return (u32)__builtin_amdgcn_s_memrealtime(); }

#define WSL 2
DEV void wait_flags(rsrc_t rs, u32 off, u32 want, bool& dead) {
    if (dead) return;
#pragma unroll 1
    for (int n = 0;; ++n) {
        CFENCE();
        const u32 v = ld4(rs, off);
        if (all_(off >= OOB || v >= want)) break;
        __builtin_amdgcn_s_sleep(WSL);
        if ((n & 63) == 63) { if (uni((int)ld4(rs, foff(F_ERR))) != 0) { dead = true; break; } }
        if (n > SPIN_CAP) { st4s(rs, foff(F_ERR), 0, 1u); dead = true; break; }
    }
    CFENCE();
}

DEV void pre_post(rsrc_t rs, int idx, u32 stage, v2u ep) { st8s(rs, threadIdx.x == 0 ? foff(F_PRE + idx) : OOB, 0, (v2u){ep.x + stage, ep.y}); }
DEV void pre_poll(rsrc_t rs, int first, int n, u32 stage, v2u ep) {
    const int lane = threadIdx.x & 63;
#pragma unroll 1
    for (int it = 0; it < SPIN_CAP; ++it) {
        CFENCE();
        const v2u a = ld8s(rs, foff(F_PRE + first + min(lane, n - 1)), 0), b = ld8s(rs, foff(F_PRE + first + min(lane + 64, n - 1)), 0);
        if (all_(a.y == ep.y && b.y == ep.y && max(a.x - ep.x - stage, b.x - ep.x - stage) < 8u - stage)) break;
        __builtin_amdgcn_s_sleep(2);
    }
    CFENCE();
}
DEV void pre_wait(rsrc_t rs, int first, int n, u32 stage, v2u ep) {
    if (uni(threadIdx.x >> 6) == 0) pre_poll(rs, first, n, stage, ep);
    BAR();
}
DEV u32 pol_wait(rsrc_t rs, v2u ep) {
    const int lane = threadIdx.x & 63;
    v4u a;
#pragma unroll 1
    for (int it = 0; it < SPIN_CAP; ++it) {
        CFENCE();
        a = ld16s(rs, foff(F_PRE + (lane < NPRE || lane >= 48 ? lane : 0)), 0);
        if (all_(a.y == ep.y && a.x - ep.x - 1u < 7u)) break;
        __builtin_amdgcn_s_sleep(2);
    }
    CFENCE();
    return uu(wave_sum(lane >= 48 ? (float)(int)a.z : 0.f) >= 0.f ? 1u : 0u);
}
#define PROBE_T 1000
#define NPROBE 32
DEV u32 pol_probe(const Params& p, rsrc_t rs, int id) {
    const int lane = threadIdx.x & 63;
    const u32 off0 = W_PROBE + (u32)(id >> 2) * 16384u + (u32)(id & 3) * 128u;
    const u32 tend = rt32() + PROBE_T;
    st4s(rs, lane < 32 ? off0 + (u32)lane * 512 : OOB, 0, 0u); DRAIN();
    int calm = 0;
    int d[2] = {1 << 16, 1 << 16};
#pragma unroll 1
    for (int q = 0; q < NPROBE && ((q & 1) || (calm < 6 && (int)(rt32() - tend) < 0)); ++q) {
        const u32 half = (u32)(q ^ (q >> 1) ^ id) & 1u;
        const unsigned long long t0 = __builtin_amdgcn_s_memtime();
        const u32 v = ld4(rs, lane == 0 ? off0 + half * 8192 + (u32)(q >> 1) * 512 : OOB);
        asm volatile("s_waitcnt vmcnt(0)" :: "v"(v) : "memory");
        const int dt = (int)(__builtin_amdgcn_s_memtime() - t0);
        if (half) d[1] = min(d[1], dt); else d[0] = min(d[0], dt);
        calm += dt < 1100;
    }
    const u32 xcc = (u32)__builtin_amdgcn_s_getreg(20 | (3 << 11)) & 0xfu;
    return (u32)min(max((d[0] - d[1]) * ((xcc & 4u) ? -1 : 1), -250), 250);
}
DEV f32x2 ln_stats(const f32x2 (&x)[5]) {
    const float c = f_(uu(u_(x[0].x)));
    f32x2 d[5];
#pragma unroll
    for (int i = 0; i < 5; ++i) d[i] = x[i] - c;
    const f32x2 s2 = (d[0] + d[1]) + (d[2] + d[3]) + d[4], q2 = (d[0] * d[0] + d[1] * d[1]) + (d[2] * d[2] + d[3] * d[3] + d[4] * d[4]);
    float a = s2.x + s2.y, b = q2.x + q2.y, a2, b2;
#define LN_DPP(m) "v_add_f32_dpp %0, %0, %0 " m " row_mask:0xf bank_mask:0xf\n\tv_add_f32_dpp %1, %1, %1 " m " row_mask:0xf bank_mask:0xf\n\ts_nop 0\n\t"
    asm("s_nop 1\n\t" LN_DPP("quad_perm:[1,0,3,2]") LN_DPP("quad_perm:[2,3,0,1]") LN_DPP("row_ror:4")
        "v_add_f32_dpp %0, %0, %0 row_ror:8 row_mask:0xf bank_mask:0xf\n\tv_add_f32_dpp %1, %1, %1 row_ror:8 row_mask:0xf bank_mask:0xf\n\ts_nop 1\n\t"
        "v_permlane16_swap_b32 %0, %1\n\tv_add_f32 %0, %0, %1\n\tv_mov_b32 %1, %0\n\ts_nop 1\n\tv_permlane32_swap_b32 %0, %1\n\tv_add_f32 %0, %0, %1\n\ts_nop 0\n\t"
        "v_readlane_b32 %2, %0, 0\n\tv_readlane_b32 %3, %0, 16\n\ts_nop 1"
        : "+v"(a), "+v"(b), "=s"(a2), "=s"(b2));
    const float m = a2 * (1.0f / DM), var = fmaxf(b2 * (1.0f / DM) - m * m, 0.f);
    const float sd = rbf(__builtin_amdgcn_sqrtf(rbf(rbf(var) + 1e-5f)));
    return (f32x2){rbf(c + m), __builtin_amdgcn_rcpf(sd)};
}
DEV u32 pkv(f32x2 v) { u32 d; asm("v_cvt_pk_bf16_f32 %0, %1, %2" : "=v"(d) : "v"(v.x), "v"(v.y)); return d; }
DEV void store_h(rsrc_t rs, u32 base, int row, const u32 (&hp)[5]) {
    const u32 lane = threadIdx.x & 63;
    const u32 sw = (u32)(row & 15) * 16u, o8 = (lane >> 5) * 8192u + (((lane & 31u) * 8u) ^ sw), hb = base + (u32)(row >> 5) * (32 * DM * 2) + (u32)(row & 31) * 256u;
    st8s(rs, o8, hb, (v2u){hp[0], hp[1]});
    st8s(rs, o8, hb + 16384, (v2u){hp[2], hp[3]});
    st4s(rs, (lane * 4) ^ sw, hb + 32768, hp[4]);
}

#define DMA16(ldsb, vo, so) asm volatile("s_mov_b32 m0, %0\n\ts_nop 0\n\tbuffer_load_dwordx4 %1, %2, %3 offen sc1 lds" :: "s"(ldsb), "v"(vo), "s"(srd), "s"(so) : "memory")
#define DMA16I(ldsb, lo, vo, so, go) do { u32 so_; asm volatile("s_add_u32 m0, %1, %5\n\ts_add_u32 %0, %4, %6\n\tbuffer_load_dwordx4 %2, %3, %0 offen sc1 lds" \
                         : "=&s"(so_) : "s"(ldsb), "v"(vo), "s"(srd), "s"(so), "i"(lo), "i"(go) : "memory", "scc"); } while (0)
#define DMA32(ldsb, vo, so) asm volatile("s_mov_b32 m0, %0\n\ts_nop 0\n\tbuffer_load_dwordx4 %1, %2, %3 offen sc1 lds\n\tbuffer_load_dwordx4 %1, %2, %3 offen offset:1024 sc1 lds" \
                         :: "s"(ldsb), "v"(vo), "s"(srd), "s"(so) : "memory")
#define DMAIN(sr, ldsb, vo, so) asm volatile("s_mov_b32 m0, %0\n\ts_nop 0\n\tbuffer_load_dwordx4 %1, %2, %3 offen lds" :: "s"(ldsb), "v"(vo), "s"(sr), "s"(so) : "memory")
DEV v4u mk_srd(const void* q, u32 bytes) { return (v4u){uu((u32)(size_t)q), uu((u32)((size_t)q >> 32) & 0xffffu), bytes, 0x00020000u}; }
#define DMANT(sr, ldsb, vo, so) asm volatile("s_mov_b32 m0, %0\n\ts_nop 0\n\tbuffer_load_dwordx4 %1, %2, %3 offen nt lds" :: "s"(ldsb), "v"(vo), "s"(sr), "s"(so) : "memory")
#define AHEAD(sr, vo, so) asm volatile("buffer_load_dwordx4 %0, %1, %2, %3 offen" : "+v"(junk) : "v"(vo), "s"(sr), "s"(so) : "memory")
#define MK_SRD() const v4u srd = mk_srd(p.ws, W_END)

constexpr int CV_W = 40960;
template <bool B> struct CvL {
    static constexpr int NF = B ? 5 : 3, NK = B ? 4 : 2, KP = 544 + NK * 256, CH = NF * 1024 + 4 * KP;
};
constexpr int CV_Y = 4 * CvL<false>::CH, CV_YP = 272;
static_assert(CV_Y + 32 * CV_YP <= CV_W && 2 * CvL<true>::CH <= CV_Y, "conv lds");
DEV int cv_chan(int u, int i) { return 16 * (u % 40) + 4 * i + u / 40; }
template <bool B> DEV void cv_filt(const Params& p, u32 dst, int d, int i) {
    const u32 lane = threadIdx.x & 63;
    const v4u sf = mk_srd(p.ssmk + d * SEQ, SEQ * 2);
    if (i == 0) DMANT(sf, dst, lane < 4u ? OOB : lane * 16u - 64u, 0);
    else DMANT(sf, uu(dst + i * 1024), i == CvL<B>::NF - 1 && lane >= 4u ? OOB : lane * 16u, i * 1024 - 64);
}
DEV v4u cv_kvld(rsrc_t rs, int d, int i) { return ld16s(rs, (threadIdx.x & 63) * 16u, uu(W_KVT + (u32)d * (SEQ * 2) + i * 1024)); }
template <bool B> DEV void cv_kvput(lptr F, const v4u* k) {
    const int lane = threadIdx.x & 63;
#pragma unroll
    for (int i = 0; i < CvL<B>::NK; ++i) lds_w16(F + CvL<B>::NF * 1024 + (lane & 3) * CvL<B>::KP + 512 + i * 256 + (lane >> 2) * 16, k[i]);
}
template <bool B> DEV void cv_zero(lptr F) {
    const int lane = threadIdx.x & 63;
#pragma unroll
    for (int i = 0; i < 4; ++i) lds_w16(F + (lane >> 5) * CvL<B>::CH + CvL<B>::NF * 1024 + i * CvL<B>::KP + (lane & 31) * 16, (v4u){0u, 0u, 0u, 0u});
}
#define M32V(d, a, b) asm volatile("v_mfma_f32_32x32x16_bf16 %0, %1, %2, %0" : "+v"(d) : "v"(a), "v"(b))
template <bool B, class G> DEV void cv_run(const Params& p, rsrc_t rs, lptr F, int d, u32 D, f32x4 sp, float (&y)[16], G&& gap) {
    constexpr int KP = CvL<B>::KP, FO = B ? 2048 : 0;
    const int lane = threadIdx.x & 63, n = lane & 31, hh = lane >> 5, c = lane & 3;
    lptr fa = F + 64 + (32 * hh + 16 * (c & 1) + 8 * ((lane >> 4) & 1) + 4 * (c >> 1) - (lane & 12)) * 2;
    lptr kl = F + CvL<B>::NF * 1024 + (32 + n - hh) * 16;
    v4u dg;
#pragma unroll
    for (int q = 0; q < 4; ++q) { const int k = (lane & 35) ? -1 : (n & 4) + (n >> 3); dg[q] = k == 2 * q ? D : k == 2 * q + 1 ? D << 16 : 0u; }
    v4u l[4], h[4]; v2u fc[4], fh[4];
    f32x16 acc[4];
#pragma unroll
    for (int j = 0; j < 4; ++j) { l[j] = lds_r16(kl + j * KP); if constexpr (B) h[j] = lds_r16(kl + j * KP + 512); }
#pragma unroll
    for (int j = 0; j < 2; ++j) { fc[j] = lds_tr(fa + FO - 32 * j); if constexpr (B) fh[j] = lds_tr(fa - 32 * j); }
    asm volatile("s_nop 1" : "+v"(dg));
#pragma unroll
    for (int j = 0; j < 4; ++j) asm volatile("v_mfma_f32_32x32x16_bf16 %0, %1, %2, 0" : "=&v"(acc[j]) : "v"(dg), "v"(B ? h[j] : l[j]));
    constexpr int U = B ? 2 : 4;
    kl -= 32 * U; fa -= 32;
    auto body = [&](bool first) __attribute__((always_inline)) {
#pragma unroll
        for (int z = 0; z < U; ++z) {
            const int c = 2 * (z & 1);
            const v4u f = __builtin_shufflevector(fc[c], fc[c + 1], 0, 1, 2, 3), g = __builtin_shufflevector(fh[c], fh[c + 1], 0, 1, 2, 3);
#pragma unroll
            for (int j = 0; j < 4; ++j) {
                M32V(acc[j], f, l[j]); l[j] = lds_r16(kl + j * KP + 32 * (U - z - 1));
                if (j < 2) fc[(c ^ 2) + j] = lds_tr(fa + FO + (z + 1) * 128 + 32 - 32 * j);
                if (first && j > 0) gap(5 * z + j - 1);
                SCHED();
            }
            if constexpr (B) {
#pragma unroll
                for (int j = 0; j < 4; ++j) {
                    M32V(acc[j], g, h[j]); h[j] = lds_r16(kl + j * KP + 512 + 32 * (U - z - 1));
                    if (j < 2) fh[(c ^ 2) + j] = lds_tr(fa + (z + 1) * 128 + 32 - 32 * j);
                    if (first && j > 1) gap(5 * z + j + 1);
                    SCHED();
                }
            }
        }
        kl -= 32 * U; fa += 128 * U;
    };
    if constexpr (B) body(true);
#pragma unroll 1
    for (int m = B ? 2 * U : 0; m < 32; m += 2 * U) body(false);
    asm volatile("s_nop 11" : "+v"(acc[0]), "+v"(acc[1]), "+v"(acc[2]), "+v"(acc[3]));
    const float o[3] = {acc[1][15] + acc[2][14] + acc[3][13], acc[2][15] + acc[3][14], acc[3][15]};
    if constexpr (!B) st16(rs, lane == 63 ? W_SP + (u32)d * 16u : OOB, (v4u){u_(o[0]), u_(o[1]), u_(o[2]), 0u});
#pragma unroll
    for (int i = 0; i < 16; ++i) {
        float v = acc[0][i];
#pragma unroll
        for (int j = 1; j < 4; ++j) if (j <= i) v += acc[j][i - j];
        if (i < 3) { const float e = f_((u32)__builtin_amdgcn_ds_bpermute((hh ? lane - 32 : lane + 31) * 4, (int)u_(o[i]))); v += lane == 0 ? sp[i] : e; }
        y[i] = v;
    }
}
DEV void cv_put(lptr F, int pr, const float (&y0)[16], const float (&y1)[16]) {
    const int lane = threadIdx.x & 63;
#pragma unroll
    for (int i = 0; i < 16; ++i) *(LAS u32*)(F + CV_Y + (lane & 31) * CV_YP + (lane >> 5) * 128 + i * 8 + pr * 4) = pk(y0[i], y1[i]);
}
DEV void cv_out(rsrc_t rs, lptr F, int u, int blk0) {
    const int lane = threadIdx.x & 63;
    lptr q = F + CV_Y + ((lane >> 2) & 3) * CV_YP + ((lane >> 4) * 4 + (lane & 3)) * 8;
    const u32 yo = W_Y + (u32)(((lane >> 2) * 8 + blk0) * DM + 4 * u + (lane & 3)) * 16u;
#pragma unroll
    for (int i = 0; i < 8; ++i) { lptr a = q + (i >> 1) * (8 * CV_YP) + (i & 1) * 128; st16(rs, yo + (u32)((i & 1) * 128 + (i >> 1)) * (DM * 16), tr2(a, a + 4 * CV_YP)); }
}
struct KvIn { v2u k[19], v[16]; v4u s[2]; };
template <class F> DEV void kvt_load(const Params& p, int tile, KvIn& in, F&& mid) {
    const int lane = threadIdx.x & 63;
    const rsrc_t rk = mk_rsrc(p.bk, ACT), rv = mk_rsrc(p.bv, ACT), rh = mk_rsrc(p.shift, DM * 8);
    const int row = (tile / 10) * 64 + (lane >> 4) * 16, col = (tile % 10) * 64 + (lane & 15) * 4;
    const u32 vo = (u32)(row * DM + col) * 2u, vp = row > 0 ? vo - 3 * (DM * 2) : OOB;
#pragma unroll
    for (int i = 0; i < 2; ++i) in.s[i] = ldin16(rh, (u32)col * 8u + i * 16, 0);
#pragma unroll
    for (int i = 0; i < 19; ++i) { if (i == 7) mid(); in.k[i] = i < 3 ? ldin8(rk, vp, i * (DM * 2)) : ldin8(rk, vo, (i - 3) * (DM * 2)); if (i >= 3) in.v[i - 3] = ldin8(rv, vo, (i - 3) * (DM * 2)); }
}
DEV void kvt_make(rsrc_t rs, lptr xt, int tile, const KvIn& in) {
    const int lane = threadIdx.x & 63;
#pragma unroll
    for (int c = 0; c < 2; ++c) {
        const v4u sv = in.s[c];
        const f32x2 sh[4] = {{lo_f(sv.x), lo_f(sv.z)}, {hi_f(sv.x), hi_f(sv.z)}, {lo_f(sv.y), lo_f(sv.w)}, {hi_f(sv.y), hi_f(sv.w)}};
        f32x2 kf[19];
#pragma unroll
        for (int i = 0; i < 19; ++i) kf[i] = up2(in.k[i][c]);
        u32 kv[16];
#pragma unroll
        for (int i = 0; i < 16; ++i) kv[i] = pk2(((kf[i] * sh[3] + kf[i + 1] * sh[2]) + (kf[i + 2] * sh[1] + kf[i + 3] * sh[0])) * up2(in.v[i][c]));
#pragma unroll
        for (int q = 0; q < 8; ++q) {
            const u32 sel = (q & 1) ? 0x07060302u : 0x05040100u; const u32* z = kv + (q >> 1);
            *(LAS v2u*)(xt + ((2 * c + (q & 1)) * 16 + (lane & 15)) * 144 + (lane >> 5) * 64 + (q >> 1) * 16 + ((lane >> 4) & 1) * 8) =
                (v2u){__builtin_amdgcn_perm(z[4], z[0], sel), __builtin_amdgcn_perm(z[12], z[8], sel)};
        }
    }
    const u32 o = W_KVT + (u32)(((tile % 10) * 64 + 4 * (lane >> 3)) * SEQ + (tile / 10) * 64 + (lane & 7) * 8) * 2u;
#pragma unroll
    for (int i = 0; i < 8; ++i) st16(rs, o + (u32)(32 * (i & 1) + (i >> 1)) * (SEQ * 2), lds_r16(xt + (8 * i + (lane >> 3)) * 144 + (lane & 7) * 16));
}
struct RowX { v2u a, b; u32 c; };
DEV void ln_row(rsrc_t rs, int row, const RowX& r) {
    const f32x2 x[5] = {up2(r.a.x), up2(r.a.y), up2(r.b.x), up2(r.b.y), up2(r.c)};
    const f32x2 st = ln_stats(x);
    u32 hp[5];
#pragma unroll
    for (int q = 0; q < 5; ++q) hp[q] = pk2(up2(pkv(x[q] - st.x)) * st.y);
    store_h(rs, W_H1, row, hp);
}
DEV v4u mem0_load(const Params& p, int j) {
    const u32 gt = (u32)(j * 256 + threadIdx.x) * 16u, n = ((gt >> 14) << 9) + ((gt & 8191u) >> 4);
    return ldin16(mk_rsrc(p.mem0, 8 * DM * 2), n < 640u ? ((2 * ((n >> 2) & 3) + (n & 1)) * DM + (n >> 4) * 16 + (n & 2) * 4) * 2u : OOB, 0);
}
DEV void pre_rings(rsrc_t rs, int j, v4u m) {
    const u32 gi = (u32)(j * 256 + threadIdx.x), gt = gi * 16u, R = NPRE * 4096, n = ((gt >> 14) << 9) + ((gt & 8191u) >> 4);
#pragma unroll
    for (int i = 0; i < 4; ++i) m[i] = n >= 640u ? PSENT : m[i] == PSENT ? 0x7FC17FC1u : m[i];
    st4s(rs, gi - 36u * 256u < F_CLR ? foff(gi - 36u * 256u) : OOB, 0, 0u);
#pragma unroll 1
    for (u32 o = gt; o < MB; o += R) st16(rs, W_ORING + o, (v4u){PSENT, PSENT, PSENT, PSENT});
    const u32 mo = gt < 4 * 32768 + 16384 ? W_MRING + gt : OOB;
    st16(rs, mo, m);
    st16(rs, mo + MB / 4, m);
    static_assert(NPRE * 4096 >= 4 * 32768 + 16384, "memory ring");
}
constexpr int KT0 = NPRE * 4, LN0 = NPRE * 8;
struct Early { RowX r[2]; v4u m; u32 vote; };
DEV void conv_early(const Params& p, rsrc_t rs, int j, Early& e) {
    const int lane = threadIdx.x & 63, w = uni(threadIdx.x >> 6), u = j * 4 + w;
    if (j < NPRE) {
        const rsrc_t rx = mk_rsrc(p.x, ACT);
#pragma unroll
        for (int i = 0; i < 2; ++i) {
            const u32 so = uu((u + i * KT0) * (DM * 2));
            e.r[i].a = __builtin_amdgcn_raw_buffer_load_b64(rx, lane * 8u, so, 0); e.r[i].b = __builtin_amdgcn_raw_buffer_load_b64(rx, lane * 8u + 512u, so, 0);
            e.r[i].c = __builtin_amdgcn_raw_buffer_load_b32(rx, lane * 4u + 1024u, so, 0);
        }
        e.m = mem0_load(p, j);
    } else if (j < NCV && w > 0) e.vote = pol_probe(p, rs, u - NPRE * 4);
}
DEV void conv_stage(const Params& p, rsrc_t rs, lptr lds, int j, int g0, int ge, v2u ep, Early& e) {
    const int tid = threadIdx.x, lane = tid & 63, w = uni(tid >> 6), u = j * 4 + w;
    lptr F = lds + w * CV_W;
    const u32 fb = uu(w * CV_W);
    const rsrc_t rd = mk_rsrc(p.ssmD, DM * 2);
    float y0[16], y1[16];
    KvIn in;
    if (j < NPRE) {
        constexpr int CH = CvL<false>::CH;
        if (w == 1) LST(1500 + j, rt32());
        pre_rings(rs, j, e.m);
        ln_row(rs, u, e.r[0]); ln_row(rs, u + KT0, e.r[1]);
        if (w == 1) LST(1550 + j, rt32());
        CFENCE();
        kvt_load(p, u, in, [&] { VMW(13); BAR(); pre_post(rs, j, 1u, ep); if (w == 0) LST(1450 + j, rt32()); });
        if (j == 20) LST(260, rt32());
        const u32 D = __builtin_amdgcn_raw_buffer_load_b16(rd, (u32)cv_chan(u, lane & 3) * 2u, 0, 0);
#pragma unroll 1
        for (int i = 0; i < 4; ++i) { const int d = cv_chan(u, i); cv_filt<false>(p, fb + i * CH, d, 0); cv_filt<false>(p, fb + i * CH, d, 1); cv_filt<false>(p, fb + i * CH, d, 2); }
        kvt_make(rs, F + CV_Y, u, in);
        cv_zero<false>(F); cv_zero<false>(F + 2 * CH);
        DRAIN(); BAR();
        pre_post(rs, j, 2u, ep);
        if (j == 20) LST(262, rt32());
        pre_wait(rs, 0, NPRE, 2u, ep);
        if (j == 20) LST(281, rt32());
        {
            v4u k[8];
#pragma unroll
            for (int i = 0; i < 8; ++i) k[i] = cv_kvld(rs, cv_chan(u, i >> 1), i & 1);
#pragma unroll
            for (int i = 0; i < 4; ++i) cv_kvput<false>(F + i * CH, k + 2 * i);
        }
        if (j == 20) LST(286, rt32());
#pragma unroll 1
        for (int i = 0; i < 4; i += 2) {
            cv_run<false>(p, rs, F + i * CH, cv_chan(u, i), rdl(D, i), f32x4{}, y0, [](int) {});
            cv_run<false>(p, rs, F + (i + 1) * CH, cv_chan(u, i + 1), rdl(D, i + 1), f32x4{}, y1, [](int) {});
            cv_put(F, i >> 1, y0, y1);
        }
        if (j == 20) LST(287, rt32());
        cv_out(rs, F, u, 0);
        if (j == 20) LST(263, rt32());
    } else {
        st16(rs, lane == 0 ? foff(F_PRE + 48 + u - NPRE * 4) : OOB, (v4u){ep.x + 1u, ep.y, w > 0 ? e.vote : 0u, 0u});
#pragma unroll 1
        for (int t = u + 128; t < 320; t += 16) { kvt_load(p, t, in, [] {}); kvt_make(rs, F, t, in); }
    }
    DRAIN(); BAR();
    if (g0 < ge) {
        constexpr int CH = CvL<true>::CH;
        pre_post(rs, j, 3u, ep);
        cv_zero<true>(F);
        if (w == 0) { pre_poll(rs, 64, 32, 3u, ep); pre_poll(rs, 0, NCV, 3u, ep); }
        BAR();
        v4u k[4];
        auto fetch = [&](u32 dst, int d, int i) { if (i < 5) cv_filt<true>(p, dst, d, i); else if (i < 9) k[i - 5] = cv_kvld(rs, d, i - 5); };
#pragma unroll
        for (int i = 0; i < 9; ++i) fetch(fb, cv_chan(g0 * 4 + w, 0), i);
        const u32 ch = (u32)cv_chan(min((g0 + 8 * (lane >> 2)) * 4 + w, 159), lane & 3);
        u32 D = __builtin_amdgcn_raw_buffer_load_b16(rd, ch * 2u, 0, 0);
        v4u sp = ld16s(rs, W_SP + ch * 16u, 0);
        asm volatile("" : "+v"(D), "+v"(sp));
#pragma unroll 1
        for (int g = g0, n = 0; g < ge; g += 8) {
            const int un = g * 4 + w;
            if (g == 16) { LST(282, rt32()); LST(288, __builtin_amdgcn_s_memtime()); }
#pragma unroll 1
            for (int i = 0; i < 4; i += 2, n += 2) {
                const int d1 = (int)rdl(ch, n + 1), d2 = (int)rdl(ch, n + 2);
                cv_kvput<true>(F, k);
                cv_run<true>(p, rs, F, 0, rdl(D, n), (f32x4){f_(rdl(sp.x, n)), f_(rdl(sp.y, n)), f_(rdl(sp.z, n)), 0.f}, y0, [&](int q) { fetch(fb + CH, d1, q); });
                cv_kvput<true>(F + CH, k);
                cv_run<true>(p, rs, F + CH, 326, rdl(D, n + 1), (f32x4){f_(rdl(sp.x, n + 1)), f_(rdl(sp.y, n + 1)), f_(rdl(sp.z, n + 1)), 0.f}, y1, [&](int q) { fetch(fb, d2, q); });
                cv_put(F, i >> 1, y0, y1);
            }
            cv_out(rs, F, un, 4);
            if (g == 16) { LST(283, rt32()); LST(289, __builtin_amdgcn_s_memtime()); }
        }
        DRAIN(); BAR();
    }
    pre_post(rs, j, 5u, ep);
    if (j == 20) LST(284, rt32());
    if (j == 3) LST(285, rt32());
}

#define M32A(first, d, a, b) do { if (first) asm volatile("v_mfma_f32_32x32x16_bf16 %0, %1, %2, 0" : "=&v"(d) : "a"(a), "v"(b)); \
                                  else asm volatile("v_mfma_f32_32x32x16_bf16 %0, %1, %2, %0" : "+v"(d) : "a"(a), "v"(b)); } while (0)
#define M32B(first, d, a, b) do { if (first) asm volatile("v_mfma_f32_32x32x16_bf16 %0, %1, %2, 0" : "=&v"(d) : "v"(a), "a"(b)); \
                                  else asm volatile("v_mfma_f32_32x32x16_bf16 %0, %1, %2, %0" : "+v"(d) : "v"(a), "a"(b)); } while (0)
#define VCVT(d, a, b) asm volatile("v_cvt_pk_bf16_f32 %0, %1, %2" : "=v"(d) : "v"(a), "v"(b))
#define VADD(d, a, b) asm volatile("v_add_f32 %0, %1, %2" : "=v"(d) : "v"(a), "v"(b))
#define LD4A(d, vo, so) asm volatile("buffer_load_dword %0, %1, %2, %3 offen sc1" : "=v"(d) : "v"(vo), "s"(srd), "s"(so) : "memory")
DEV u32 wst_src(int lane, int ld) { return (u32)((lane >> 4) * ld + 8 * ((lane & 15) ^ ((lane >> 4) << 2))) * 2u; }
DEV u32 wst_rd(int lane, int w) { const int q4 = (lane >> 2) & 3; return (u32)(q4 * 256 + ((w ^ q4) * 64) + ((lane >> 4) & 1) * 32 + (lane & 3) * 8); }

constexpr int QK_SEL = 81920;
#define QK_LEAD 12
#define QK_SMP 29
#define QK_YB 26
#define QK_W0 2
DEV void role_qkv(const Params& p, lptr lds, int c, v2u ep) {
    const int tid = threadIdx.x, lane = tid & 63, w = uni(tid >> 6), hh = lane >> 5, r32 = lane & 31;
    const int m = c >> 5, h = c & 31;
    const rsrc_t rs = mk_rsrc(p.ws, W_END);
    v4u wt[40];
    {
        const v4u sw = mk_srd(m == 0 ? p.Wq : m == 1 ? p.Wk : p.Wv, DM * NQ * 2), sx = mk_srd(p.x, ACT);
        const u32 vo = wst_src(lane, NQ) + (u32)h * (DH * 2), x8 = (u32)lane * 8u, x4 = (u32)lane * 4u;
        RowX xr[5];
        auto wfill = [&](int i0, int n) {
#pragma unroll 1
            for (int i = i0; i < i0 + n; ++i) DMAIN(sw, uu((4 * i + w) * 1024), vo, uu((16 * i + 4 * w) * (NQ * 2)));
        };
        wfill(0, QK_W0);
#pragma unroll
        for (int i = 0; i < 5; ++i)
            asm volatile("buffer_load_dwordx2 %0, %3, %5, %6 offen\n\tbuffer_load_dwordx2 %1, %3, %5, %6 offen offset:512\n\tbuffer_load_dword %2, %4, %5, %6 offen offset:1024"
                         : "=&v"(xr[i].a), "=&v"(xr[i].b), "=&v"(xr[i].c) : "v"(x8), "v"(x4), "s"(sx), "s"(uu((LN0 + 4 * c + w + 384 * i) * (DM * 2))) : "memory");
        wfill(QK_W0, 10 - QK_W0);
        if (c == 3) LST(275, rt32());
        VMW(10 - QK_W0);
        if (c == 3) LST(276, rt32());
#pragma unroll
        for (int i = 0; i < 5; ++i) {
            asm volatile("" : "+v"(xr[i].a), "+v"(xr[i].b), "+v"(xr[i].c));
            const int row = LN0 + 4 * c + w + 384 * i;
            if (i < 4 || row < SEQ) ln_row(rs, row, xr[i]);
            wfill(10 + 6 * i, 6);
        }
        if (c == 3) LST(264, rt32());
        lptr tb = lds + hh * 2048 + wst_rd(lane, w);
        SFOR(k, 40)
            if constexpr (k == 0) { VMW(15); BAR(); }
            if constexpr (k == 28) {
                if (w == 0) pre_poll(rs, 0, NPRE, 1u, ep);
                DRAIN(); BAR();
                pre_post(rs, 96 + c, 1u, ep);
            }
            lptr q = tb + (k >> 3) * 32768 + (k & 7) * 4096; wt[k] = tr2(q, q + 1024); asm volatile("" : "+a"(wt[k]));
        SEND
    }
    if (c == 3) LST(268, rt32());
    if (w == 0) LST(1350 + c, rt32());
    bool dead = false;
    int n0, n1;
    const u32 t16 = (u32)(w * 2048 + lane * 16), wb = uu(w * 2048);
    u32 ar[16];
#pragma unroll
    for (int j = 0; j < 16; ++j) ar[j] = (u32)(r32 * 256 + ((2 * (j & 7) + hh) ^ (r32 & 15)) * 16 + (j >> 3) * 49152);
    MK_SRD();
#define XRD(y, ks) lds_r16(lds + ar[((ks) & 7) + ((ks) >= 24 ? 8 : 0)] + ((ks) >> 3) % 3 * 16384 + (y) * 8192)
#define QM0(y, b) asm volatile("s_add_u32 m0, %0, %1" :: "s"(wb), "i"((b) * 16384 + (y) * 8192) : "scc", "memory")
#define QDMA(i, vo) asm volatile("buffer_load_dwordx4 %0, %1, %2 offen offset:%3 sc1 lds" :: "v"(vo), "s"(srd), "s"(((i) >> 1) * 8192), "i"(((i) & 1) * 1024) : "memory")
    const u32 h2flag = (lane < 16 && w == 0) ? foff(F_H2 + lane) : OOB;
    const u32 ol = (m == 2 ? (u32)(2 * w + (r32 >> 4)) * 1024u : (u32)((r32 >> 4) * 4 + w) * 1024u) + (u32)(hh * 16 + (r32 & 15)) * 16u
                 + (m == 2 ? W_VO : m == 1 ? W_KQ : W_KQ + 8192u) + (u32)h * 16384u;
    const u32 fo = tid < 2 ? foff(F_QKV + tid * 96 + c) : OOB;
    lptr selw = lds + QK_SEL + (w == 0 ? 0 : 64), selr = lds + QK_SEL;
    PIN(selw);
    static_assert(QK_LEAD > 8 && QK_LEAD % 2 == 0 && LN0 == 10 * 32, "X = 8 comes up, before any item of layer 1");
    static_assert(W_H2 - W_H1 == 2 * 64 * (32 * DM * 2), "h");
    auto run = [&](auto isv) __attribute__((always_inline)) {
        constexpr bool V = decltype(isv)::value;
        constexpr int YB = QK_YB;
        f32x16 ax, ay = {};
        v4u x[8], pp, pq;
        u32 xv = t16 + W_H1, ox = ol, oy = OOB, nn = 1, lt = 0, fcnt = 0, fa = OOB;
        asm volatile("" : "+v"(lt), "+v"(nn));
#pragma unroll
        for (int z = 0; z < 5; ++z) x[z] = XRD(1, 40 - YB + z);
#define QPK(r_, a1, a_, q_) do { if constexpr (V) VCVT(r_[q_], a1[4 * (a_) + 8 * ((q_) >> 1) + 2 * ((q_) & 1)], a1[4 * (a_) + 8 * ((q_) >> 1) + 2 * ((q_) & 1) + 1]); \
                                 else VCVT(r_[q_], a1[4 * (a_) + (q_)], a1[4 * (a_) + (q_) + 8]); } while (0)
#define QPK4(r_, a1, a_) do { QPK(r_, a1, a_, 0); QPK(r_, a1, a_, 1); QPK(r_, a1, a_, 2); QPK(r_, a1, a_, 3); } while (0)
        for (;;) {
            u32 ls = uu(lt), yv, fv, nx, need;
            if (ls != 0u && uu(nn) < 256u) pre_wait(rs, 96, 96, 1u, ep);
            else if (ls != 0u) {
                n1 = (int)(uu(nn) >> 8);
                CFENCE();
                const u32 hf = h2flag + (u32)(~n1 & 1) * 2048u;
                u32 f2 = ld4(rs, hf);
                asm volatile("s_waitcnt vmcnt(0)" : "+v"(f2) :: "memory");
                *(LAS u32*)(selw + 16) = (u32)__builtin_amdgcn_ballot_w64(f2 < (u32)n1) & 0xFFFFu;
                BAR();
                st4s(rs, fa, 0, fcnt);
                fa = OOB;
                if (n1 > NSEG) { LDUMP(w == 0, 64 + c); __builtin_amdgcn_endpgm(); }
                if (uu(*(LAS u32*)(selr + 16)) != 0u) {
                    if (w == 0) wait_flags(rs, hf, (u32)n1, dead);
                    BAR();
                }
            }
#define QT(k) do {} while (0)
            QT(0);
            SCHED();
            QM0(0, 0); NOPS(0); QDMA(0, xv); QDMA(1, xv); QM0(0, 1);
            SFOR(z, 80)
                constexpr int ks = (z + 40 - YB) % 40, zx = z - YB;
                constexpr auto upto = [](int q) { const int d = (q - YB + 49) % 40; return q + 6 + (d == 0 || d == 2 ? 1 : d == 1 ? 2 : 0); };
                if constexpr (zx >= 0 && zx < 40) { if constexpr (V) M32B(ks == 0, ax, x[z & 7], wt[ks]); else M32A(ks == 0, ax, wt[ks], x[z & 7]); }
                else { if constexpr (V) M32B(ks == 0, ay, x[z & 7], wt[ks]); else M32A(ks == 0, ay, wt[ks], x[z & 7]); }
                if constexpr (z == 78) lt = *(LAS u32*)selr;
#pragma unroll
                for (int q = upto(z - 1); q < upto(z); ++q) x[q & 7] = XRD(q % 80 < YB || q % 80 >= YB + 40, (q + 40 - YB) % 40);
                SCHED();
                (void)&srd; (void)&wb;
                if constexpr (z < 2) QDMA(z + 2, xv);
                if constexpr (z >= 2 && z < 14 && (z & 3) == 2) QM0(0, (z + 6) >> 2);
                if constexpr (z >= 3 && z < 15 && (z & 1)) QDMA((z + 5) >> 1, xv);
                if constexpr (z == 4) { n0 = uni((int)nn); n1 = n0 >> 8; n0 &= 255; PINS(n0); PINS(n1); }
                if constexpr (z == 8) { nx = n0 < NSEG ? W_H1 + (u32)n0 * (32 * DM * 2) : OOB; PINS(nx); }
                if constexpr (z == 12) { yv = t16 + nx; PIN(yv); }
                if constexpr (zx == -6) { QT(1); asm volatile("s_waitcnt vmcnt(6) lgkmcnt(0)\n\ts_barrier" ::: "memory"); QT(2); }
                if constexpr (zx == -5) st4s(rs, fa, 0, fcnt);
                if constexpr (zx == -4 || zx == 0 || zx == 4) QM0(1, (zx + 4) >> 2);
                if constexpr (zx >= -3 && zx < 8 && (zx & 1)) QDMA((zx + 3) >> 1, yv);
                if constexpr (zx == 2) QPK4(pp, ay, 0);
                if constexpr (zx == 6) QPK4(pq, ay, 1);
                if constexpr (zx == 8) st16(rs, oy, pp);
                if constexpr (zx == 9) st16(rs, oy + 512, pq);
                if constexpr (zx == 10) { QT(3); asm volatile("s_waitcnt vmcnt(9)\n\ts_barrier" ::: "memory"); QT(4); }
                if constexpr (zx == 11 || zx == 15) QM0(1, (zx + 1) >> 2);
                if constexpr (zx >= 12 && zx < 20 && (zx & 1) == 0) QDMA(zx >> 1, yv);
                if constexpr (zx == 21) { fcnt = lane == 0 ? (u32)n0 : (u32)n1; fa = fo; asm volatile("" : "+v"(fcnt), "+v"(fa)); }
                if constexpr (zx == 22) { oy = (ol + (u32)n0 * (NH * 16384u)) | (yv & OOB); PIN(oy); }
                if constexpr (zx == 23) { n0 = min(n0 + 1, NSEG); PINS(n0); nx = (u32)min(NSEG, n1 + QK_LEAD); PINS(nx); }
                if constexpr (zx == 24)
                    asm volatile("s_cmp_ge_u32 %1, %0\n\ts_cselect_b32 %0, %4, %1\n\ts_addc_u32 %2, %2, 0\n\ts_cmp_lt_u32 %0, 64\n\ts_cselect_b32 %3, 0, %2"
                                 : "+s"(nx), "+s"(n0), "+s"(n1), "=&s"(need) : "s"(n1 + 64) : "scc");
                if constexpr (zx == 25) { n0 += 1 - (int)min(nx >> 6, 1u); PINS(n0); }
                if constexpr (zx == 26) { nn = (u32)(n0 | n1 << 8); PIN(nn); }
                if constexpr (zx == 27) { ls = W_H1 + (nx + (nx & 64u)) * (32 * DM * 2); PINS(ls); }
                if constexpr (zx == 28) { xv = t16 + ls; PIN(xv); need = nx == 8u ? 255u : need; PINS(need); }
                if constexpr (zx == 34) { asm volatile("s_waitcnt vmcnt(%0) lgkmcnt(0)\n\ts_barrier" :: "i"(QK_SMP < 34 ? 7 : 6) : "memory"); QT(5); }
                if constexpr (zx == QK_SMP) { const u32 po = (nx & 1u) * 2048u; LD4A(fv, h2flag, po); }
                if constexpr (zx == 42) QPK4(pp, ax, 0);
                if constexpr (zx == 43) st16(rs, ox, pp);
                if constexpr (zx == 44) QPK4(pq, ax, 1);
                if constexpr (zx == 45) st16(rs, ox + 512, pq);
                if constexpr (zx == 47) { ox = ol + nx * (NH * 16384u); PIN(ox); }
                if constexpr (z == 74) { asm volatile("s_waitcnt vmcnt(2)" : "+v"(fv) :: "memory"); QT(6); ls = (u32)__builtin_amdgcn_ballot_w64(fv < need) & 0xFFFFu; PINS(ls); }
                if constexpr (z == 75) { lt = ls; PIN(lt); *(LAS u32*)selw = lt; }
                if constexpr (z == 77) asm volatile("s_waitcnt lgkmcnt(2)\n\ts_barrier" ::: "memory");
                SCHED();
            SEND
        }
    };
    if (m == 2) run(IC<1>{}); else run(IC<0>{});
}

DEV void role_out(const Params& p, lptr lds, int L, int kq, int ng, v2u ep) {
    const int tid = threadIdx.x, lane = tid & 63, w = uni(tid >> 6), hh = lane >> 5, r32 = lane & 31;
    const rsrc_t rs = mk_rsrc(p.ws, W_END);
    v4u wo[64];
    bool dead = false;
    {
        const v4u sw = mk_srd(p.Wo, DM * NQ * 2);
        const u32 vo = wst_src(lane, DM) + (u32)ng * 256u;
        auto fill = [&](int b) {
#pragma unroll 1
            for (int i = 0; i < 8; ++i) DMAIN(sw, uu((b % 5) * 32768 + (4 * i + w) * 1024), vo, uu((kq * 1024 + b * 128 + 16 * i + 4 * w) * (DM * 2)));
        };
#pragma unroll 1
        for (int b = 0; b < 5; ++b) fill(b);
        lptr tb = lds + hh * 1024 + wst_rd(lane, w);
        SFOR(b, 8)
            VMW(b < 4 ? 32 : 56 - 8 * b); BAR();
            lptr q = tb + (b % 5) * 32768;
#pragma unroll
            for (int k = 0; k < 8; ++k) { lptr a = q + (k >> 1) * 8192 + (k & 1) * 2048; wo[8 * b + k] = tr2(a, a + 4096); if (b < 7) asm volatile("" : "+a"(wo[8 * b + k])); else PIN(wo[8 * b + k]); }
            if constexpr (b < 3) { BAR(); fill(b + 5); }
        SEND
    }
    pre_wait(rs, 0, NPRE, 1u, ep);
    if (kq == 0 && ng == 0) LST(272 + L, rt32());
    MK_SRD();
    const u32 ofl = lane < 8 ? foff(F_O + L * 32 + kq * 8 + lane) : OOB, l16 = (u32)lane * 16u;
    const u32 fo = (tid == 0) ? foff(F_P + L * 32 + kq * 5 + ng) : OOB;
#define OM0(dst, b) asm volatile("s_add_u32 m0, %0, %1" :: "s"(dst), "i"((b) * 8192) : "scc", "memory")
#define ODMA(i, vo) asm volatile("buffer_load_dwordx4 %0, %1, %2 offen offset:%3 sc1 lds" :: "v"(vo), "s"(srd), "s"(((i) >> 1) * 16384), "i"(((i) & 1) * 1024) : "memory")
    auto first = [&](u32 dst, u32 vo) {
#pragma unroll
        for (int b = 0; b < 5; ++b) { OM0(dst, b); NOPS(0); ODMA(2 * b, vo); ODMA(2 * b + 1, vo); }
    };
    u32 pa = W_P + (u32)(L * NSEG) * P_ITEM + (u32)(2 * hh * 4 + kq) * (DM * 4) + (u32)(ng * 128 + w * 32 + r32) * 4u, fv = 0;
    u32 ov = W_VO + 8192u + (u32)(L * NSEG * NH + kq * 8) * 16384u + (u32)w * 2048u + l16;
    bool early = false;
    for (int s = 0; s < NSEG; ++s) {
        const u32 dst = uu((s & 1) * 65536 + w * 2048), ndst = dst ^ 65536u, need = s + 1 < NSEG ? (u32)s + 2u : 255u;
        lptr A = lds + (s & 1) * 65536 + l16;
        if (!early) {
            if (!all_(ofl == OOB || fv >= (u32)s + 1)) wait_flags(rs, ofl, (u32)s + 1, dead);
            LS3(L == 0 && w == 1, 1, kq * 5 + ng);
            first(dst, ov);
        }
#define OT(k) do {} while (0)
        OM0(dst, 5);
        asm volatile("s_waitcnt vmcnt(8)\n\ts_barrier" ::: "memory");
        OT(0);
        v4u o[64];
#pragma unroll
        for (int k = 0; k < 4; ++k) o[k] = lds_r16(A + k * 1024);
        f32x16 acc;
        u32 far, nv;
        SCHED();
        SFOR(k, 64)
            if constexpr (k < 56) M32B(k == 0, acc, o[k], wo[k]); else M32V(acc, o[k], wo[k]);
            if constexpr (k < 60) o[k + 4] = lds_r16(A + (k + 4) * 1024);
            SCHED();
            (void)&srd; (void)&dst; (void)&ndst;
            if constexpr (k < 12 && (k & 1) == 0) ODMA(10 + k / 2, ov);
            if constexpr (k == 3) asm volatile("s_waitcnt vmcnt(6)\n\ts_barrier" ::: "memory");
            if constexpr (k == 3 || k == 7) OM0(dst, (k + 21) >> 2);
            if constexpr (k == 12) { ov += NH * 16384u; PIN(ov); }
            if constexpr (k == 19) asm volatile("s_waitcnt vmcnt(6)\n\ts_barrier" ::: "memory");
            if constexpr (k == 20) LD4A(fv, ofl, 0);
            if constexpr (k == 35) asm volatile("s_waitcnt vmcnt(1)\n\ts_barrier" ::: "memory");
            if constexpr (k == 40) { asm volatile("s_waitcnt vmcnt(0)" : "+v"(fv) :: "memory"); far = (u32)__builtin_amdgcn_ballot_w64(fv < need) & 255u; PINS(far); }
            if constexpr (k == 41) { nv = far ? OOB : ov; PIN(nv); }
            if constexpr (k >= 42 && k < 62 && (k & 3) == 2) OM0(ndst, (k - 42) >> 2);
            if constexpr (k >= 43 && k < 63 && (k & 1)) ODMA((k - 43) >> 1, nv);
            if constexpr (k == 48) LD4A(fv, ofl, 0);
            if constexpr (k == 4) OT(1);
            if constexpr (k == 20) OT(2);
            if constexpr (k == 36) OT(3);
            if constexpr (k == 41) OT(4);
            SCHED();
        SEND
        OT(5);
        asm volatile("s_nop 11" : "+v"(acc), "+v"(pa));
#pragma unroll
        for (int i = 0; i < 8; ++i) st4s(rs, pa, (u32)((4 * (i >> 1) + (i & 1)) * (4 * DM * 4)), pk(acc[2 * i], acc[2 * i + 1]));
        asm volatile("s_waitcnt vmcnt(15)" : "+v"(fv) :: "memory");
        OT(6);
        LS3(L == 0 && w == 1, 2, kq * 5 + ng);
        early = far == 0u || all_(ofl == OOB || fv >= need);
        if (far != 0u && early) { first(ndst, ov); VMW(10); }
        else { fv = ld4(rs, ofl); asm volatile("s_waitcnt vmcnt(0)" : "+v"(fv) :: "memory"); }
        BAR();
        st4s(rs, fo, 0, (u32)s + 1);
        pa += P_ITEM;
        LS2(L == 0 && w == 1, 160 + kq * 5 + ng);
    }
    LDUMP(L == 0 && w == 1, kq * 5 + ng);
}

DEV void ln_stats2(const f32x2 (&x)[10], f32x2& mean, f32x2& rstd) {
    const f32x2 c = {f_(uu(u_(x[0].x))), f_(uu(u_(x[0].y)))};
    f32x2 d[10];
#pragma unroll
    for (int i = 0; i < 10; ++i) d[i] = x[i] - c;
    f32x2 a = ((d[0] + d[1]) + (d[2] + d[3])) + ((d[4] + d[5]) + (d[6] + d[7])) + (d[8] + d[9]);
    f32x2 b = (d[0] * d[0] + d[2] * d[2] + d[4] * d[4] + d[6] * d[6] + d[8] * d[8]) + (d[1] * d[1] + d[3] * d[3] + d[5] * d[5] + d[7] * d[7] + d[9] * d[9]);
    float a0, a1, b0, b1;
#define LN_DP4(m) "v_add_f32_dpp %0, %0, %0 " m " row_mask:0xf bank_mask:0xf\n\tv_add_f32_dpp %1, %1, %1 " m " row_mask:0xf bank_mask:0xf\n\t" \
                  "v_add_f32_dpp %2, %2, %2 " m " row_mask:0xf bank_mask:0xf\n\tv_add_f32_dpp %3, %3, %3 " m " row_mask:0xf bank_mask:0xf\n\t"
    asm("s_nop 1\n\t" LN_DP4("quad_perm:[1,0,3,2]") LN_DP4("quad_perm:[2,3,0,1]") LN_DP4("row_ror:4") LN_DP4("row_ror:8")
        "v_permlane16_swap_b32 %0, %1\n\tv_add_f32 %0, %0, %1\n\tv_permlane16_swap_b32 %2, %3\n\tv_add_f32 %2, %2, %3\n\ts_nop 1\n\t"
        "v_permlane32_swap_b32 %0, %2\n\tv_add_f32 %0, %0, %2\n\ts_nop 0\n\t"
        "v_readlane_b32 %4, %0, 0\n\tv_readlane_b32 %5, %0, 16\n\tv_readlane_b32 %6, %0, 32\n\tv_readlane_b32 %7, %0, 48\n\ts_nop 1"
        : "+v"(a.x), "+v"(b.x), "+v"(a.y), "+v"(b.y), "=s"(a0), "=s"(b0), "=s"(a1), "=s"(b1));
    const f32x2 m = (f32x2){a0, a1} * (1.0f / DM), v = (f32x2){b0, b1} * (1.0f / DM) - m * m;
    const f32x2 q = up2(pkv(up2(pkv((f32x2){fmaxf(v.x, 0.f), fmaxf(v.y, 0.f)})) + 1e-5f));
    const f32x2 sd = up2(pk(__builtin_amdgcn_sqrtf(q.x), __builtin_amdgcn_sqrtf(q.y)));
    mean = up2(pkv(c + m)); rstd = (f32x2){__builtin_amdgcn_rcpf(sd.x), __builtin_amdgcn_rcpf(sd.y)};
}
DEV void role_row(const Params& p, lptr lds, int id, v2u ep) {
    const int lane = threadIdx.x & 63, w = uni(threadIdx.x >> 6);
    const int pr = id >> 2, L = id & 1, par = (id >> 1) & 1;
    const rsrc_t rs = mk_rsrc(p.ws, W_END), rx = mk_rsrc(p.x, ACT);
    bool dead = false;
    const u32 fp = lane < 20 ? foff(F_P + L * 32 + lane) : OOB, l16 = (u32)lane * 16u, l8 = (u32)lane * 8u, l4 = (u32)lane * 4u, fb = uu(w * CV_W);
    lptr F = lds + w * CV_W;
    MK_SRD();
    for (int s = par; s < NSEG; s += 2) {
        const u32 ro = (u32)(s * 32 + 2 * pr) * (DM * 2);
        if ((s & 6) == 0) {
            if ((s & 30) == 0) pre_poll(rs, 0, s < 32 ? NPRE : NCV, s < 32 ? 3u : 5u, ep);
            if (id == 0 && s == 0) LST(274, rt32());
            if (s == 48) st8s(rs, (id == 1 && lane == 0) ? foff(F_EPOCH) : OOB, 0, (v2u){ep.x + 8u, ep.y});
#pragma unroll 1
            for (int i = 0; i < 5; ++i) {
                const u32 d = fb + i * 2048, o = W_Y + (u32)(16 * pr + (s >> 3)) * 10240u + i * 2048;
                DMA32(d, l16, o); DMA32(d + 10240u, l16, o + 81920u);
            }
            DRAIN();
        }
        RowX xr[2];
        auto xload = [&](rsrc_t r, u32 o, auto aux) {
#pragma unroll
            for (int i = 0; i < 2; ++i) {
                xr[i].a = __builtin_amdgcn_raw_buffer_load_b64(r, l8, o + i * (DM * 2), aux.value); xr[i].b = __builtin_amdgcn_raw_buffer_load_b64(r, l8 + 512u, o + i * (DM * 2), aux.value);
                xr[i].c = __builtin_amdgcn_raw_buffer_load_b32(r, l4 + 1024u, o + i * (DM * 2), aux.value);
            }
        };
        if (L == 0) xload(rx, ro, IC<0>{});
        f32x2 y[10], x[10];
        {
            lptr ya = F + (s & 7) * 2 + l16, yc = F + (s & 7) * 2 + 2048 + (lane >> 1) * 16 + (lane & 1) * 5120;
#pragma unroll
            for (int i = 0; i < 10; ++i) {
                lptr q = i < 8 ? ya + (i & 3) * 2560 + (i >> 2) * 1024 : yc + (i & 1) * 2560;
                y[i] = (f32x2){lo_f(*(LAS u16*)q), lo_f(*(LAS u16*)(q + 10240))}; PIN(y[i]);
            }
        }
        wait_flags(rs, fp, (u32)s + 1, dead);
        LS3(L == 0, 3, ri);
        LMX(L == 0, 0, s, rt32());
        {
            const u32 o = uu(W_P + (u32)(L * NSEG + s) * P_ITEM + (u32)pr * (4 * DM * 4));
            if (L == 1) xload(rs, W_X2 + ro, IC<16>{});
            v4u a[4], b[4]; v2u c[4];
#pragma unroll
            for (int kq = 0; kq < 4; ++kq) { a[kq] = ld16s(rs, l16, o + kq * (DM * 4)); SCHED(); b[kq] = ld16s(rs, l16 + 1024u, o + kq * (DM * 4)); SCHED(); c[kq] = ld8s(rs, l8 + 2048u, o + kq * (DM * 4)); SCHED(); }
            const u32 u0[5] = {xr[0].a.x, xr[0].a.y, xr[0].b.x, xr[0].b.y, xr[0].c}, u1[5] = {xr[1].a.x, xr[1].a.y, xr[1].b.x, xr[1].b.y, xr[1].c};
#pragma unroll
            for (int i = 0; i < 5; ++i) { x[2 * i] = (f32x2){lo_f(u0[i]), lo_f(u1[i])}; x[2 * i + 1] = (f32x2){hi_f(u0[i]), hi_f(u1[i])}; }
#pragma unroll
            for (int kq = 0; kq < 4; ++kq)
#pragma unroll
                for (int i = 0; i < 10; ++i)
                    asm volatile("v_dot2_f32_bf16 %0, %2, %3, %0\n\tv_dot2_f32_bf16 %1, %2, %4, %1" : "+v"(x[i].x), "+v"(x[i].y)
                                 : "v"(i < 4 ? a[kq][i & 3] : i < 8 ? b[kq][i & 3] : c[kq][i & 1]), "s"(0x3F80u), "s"(0x3F800000u));
            asm volatile("s_nop 2" : "+v"(x[8]), "+v"(x[9]));
        }
        LS3(L == 0, 4, ri);
        f32x2 mean, rstd;
        ln_stats2(x, mean, rstd);
#pragma unroll
        for (int i = 0; i < 10; ++i) x[i] += (x[i] - mean) * (y[i] * rstd);
        u32 xp[2][5];
#pragma unroll
        for (int i = 0; i < 5; ++i) { xp[0][i] = pkv((f32x2){x[2 * i].x, x[2 * i + 1].x}); xp[1][i] = pkv((f32x2){x[2 * i].y, x[2 * i + 1].y}); }
        if (L == 0) {
#pragma unroll
            for (int i = 0; i < 5; ++i) { x[2 * i] = (f32x2){lo_f(xp[0][i]), lo_f(xp[1][i])}; x[2 * i + 1] = (f32x2){hi_f(xp[0][i]), hi_f(xp[1][i])}; }
            ln_stats2(x, mean, rstd);
#pragma unroll
            for (int i = 0; i < 10; ++i) x[i] = (x[i] - mean) * rstd;
#pragma unroll
            for (int r = 0; r < 2; ++r) {
                u32 hp[5];
#pragma unroll
                for (int i = 0; i < 5; ++i) hp[i] = pkv((f32x2){x[2 * i][r], x[2 * i + 1][r]});
                store_h(rs, W_H2, s * 32 + 2 * pr + r, hp);
            }
#pragma unroll
            for (int r = 0; r < 2; ++r) {
                st8s(rs, l8, W_X2 + ro + r * (DM * 2), (v2u){xp[r][0], xp[r][1]});
                st8s(rs, l8 + 512u, W_X2 + ro + r * (DM * 2), (v2u){xp[r][2], xp[r][3]});
                st4s(rs, l4 + 1024u, W_X2 + ro + r * (DM * 2), xp[r][4]);
            }
            LS3(L == 0, 5, ri);
            DRAIN();
            st4s(rs, lane == 0 ? foff(F_H2 + par * 16 + pr) : OOB, 0, (u32)s + 1);
        } else {
#pragma unroll
            for (int r = 0; r < 2; ++r) {
                u16* o_ = p.out + (size_t)(s * 32 + 2 * pr + r) * DM;
                *(v2u*)(o_ + 4 * lane) = (v2u){xp[r][0], xp[r][1]};
                *(v2u*)(o_ + 256 + 4 * lane) = (v2u){xp[r][2], xp[r][3]};
                *(u32*)(o_ + 512 + 2 * lane) = xp[r][4];
            }
        }
        if (pr == 0) LST(320 + L * 64 + s, rt32());
        LS2(L == 0, 224 + ri);
        LMX(L == 0, 1, s, rt32());
    }
    LDUMP(L == 0, 32 + ri);
}

constexpr float SCALE = 0.08838834764831845f, LOG2E = 1.4426950408889634f;
constexpr int C_K = 0, C_V = 8192, C_QX = 16384, C_MEM = 24576, C_Q = C_MEM + 10240, C_KM = C_Q + 2304, C_VM = C_KM + 2304, C_DUMP = C_VM + 2048,
              C_ZERO = C_DUMP + 1024, C_WL = C_ZERO + 128;
constexpr int NLF = 14;
static_assert(C_WL % 16 == 0 && C_WL + 4 * NLF * 2048 <= LDS_BYTES, "head lds");
#define CH_VIS 10
#define CH_H0 104
#define CH_W0 81
#define CH_MH 2
#define CH_MW 4
#define CH_REC 34
#define CH_RTY 32u
#define CH_RISE 12u
#define CH_FALL 3u
#define CH_PRE 9
#define CH_T0 190
#define CH_UP 2
#define CH_WX 6
#define WAIT_TXT(tmp, t4) "s_memrealtime vcc\n\t.p2align 5\n1:\n\ts_waitcnt lgkmcnt(0)\n\ts_sub_u32 " tmp ", " t4 ", vcc_lo\n\ts_memrealtime vcc\n\ts_cmpk_lt_u32 " tmp ", 400\n\ts_cbranch_scc1 1b\n\t" \
                          "s_add_u32 " tmp ", " tmp ", 4\n\ts_cselect_b32 " tmp ", " tmp ", 0\n2:\n\ts_sub_u32 " tmp ", " tmp ", 1\n\ts_cbranch_scc0 2b\n\t"
DEV void wait_until(u32 t) { u32 tmp; asm volatile(WAIT_TXT("%0", "%1") "s_waitcnt lgkmcnt(0)" : "=&s"(tmp) : "s"(uu(t - 4u)) : "vcc", "scc", "memory"); }
#define SMF_A(acc, a, b) asm volatile("v_smfmac_f32_16x16x64_bf16 %0, %1, %2, %3" : "+v"(acc) : "v"(a), "a"(b), "v"(idx))
#define SMF_V(acc, a, b) asm volatile("v_smfmac_f32_16x16x64_bf16 %0, %1, %2, %3" : "+v"(acc) : "v"(a), "v"(b), "v"(idx))
#define MF0(d, a, b) asm volatile("v_mfma_f32_16x16x32_bf16 %0, %1, %2, 0" : "=&v"(d) : "v"(a), "v"(b))
#define MF1(d, a, b) asm volatile("v_mfma_f32_16x16x32_bf16 %0, %1, %2, %0" : "+v"(d) : "v"(a), "v"(b))
#define MF0H(d, a, b) asm volatile("v_mfma_f32_16x16x16_bf16 %0, %1, %2, 0" : "=&v"(d) : "v"(a), "v"(b))
#define MF1H(d, a, b) asm volatile("v_mfma_f32_16x16x16_bf16 %0, %1, %2, %0" : "+v"(d) : "v"(a), "v"(b))
DEV v8u tr_frag(lptr p0, lptr p1, lptr p2, lptr p3) { return cat8(tr2(p0, p1), tr2(p2, p3)); }
constexpr int qslot(int ks) { return (ks & 1) && ks < 8 ? ks >> 1 : -1; }
constexpr int kslot(int ks) { return ks < 4 ? 4 + ks : -1; }
constexpr int vslot(int ks) { return ks >= 4 ? 4 + ks : -1; }
DEV u32 ring_map(u32 off, u32 half) { return ((off >> 13) << 14) + (half << 13) + (off & 8191u); }
#define ZRD(x, i) x = __builtin_bit_cast(f32x4, lds_r16(ZP + 16 * (i)))
#define VMAX(d, a, b) asm volatile("v_max_f32 %0, %1, %2" : "=v"(d) : "v"(a), "v"(b))
#define VMAX3(d, a, b, c_) asm volatile("v_max3_f32 %0, %1, %2, %3" : "=v"(d) : "v"(a), "v"(b), "v"(c_))
#define VEXP(d, a) asm volatile("v_exp_f32 %0, %1" : "=v"(d) : "v"(a))
#define VMULC(d, a) asm volatile("v_mul_f32 %0, 0x3e0293ee, %1" : "=v"(d) : "v"(a))
#define CADD(d, a, b) do { d = (a) + (b); PIN(d); } while (0)
#define CMULC(d, a) do { d = (a) * CEXP; PIN(d); } while (0)
#define CCVT(d, a, b) do { d = pk(a, b); PIN(d); } while (0)
#define LGK(n) __builtin_amdgcn_s_waitcnt(0xC07F | (n) << 8)
#define SEL(d, a, b) asm volatile("v_cndmask_b32 %0, %1, %2, %3" : "=v"(d) : "v"(a), "v"(b), "s"(himask))
#define QPERM(d, a, q) asm volatile("v_mov_b32_dpp %0, %1 quad_perm:" q " row_mask:0xf bank_mask:0xf" : "=v"(d) : "v"(a))

#define AL_H 3
#define AL_W 0
#define HD_SMP 56
DEV void role_head(const Params& p, lptr lds, int L, int h, v2u ep) {
    const int tid = threadIdx.x, lane = tid & 63, w = uni(tid >> 6), g = lane >> 4, r = lane & 15;
    const rsrc_t rs = mk_rsrc(p.ws, W_END);
    v8u wq0[10], wq1[10], wk0[10], wk1[10], wv0[10], wv1[10];
    lptr WL = lds + C_WL + w * (NLF * 2048) + lane * 16;
    {
        constexpr int GS = 1056, CS = 48 * GS;
        const v4u sq = mk_srd(p.Wq, DM * NQ * 2), sk = mk_srd(p.Wk, DM * NQ * 2), sv = mk_srd(p.Wv, DM * NQ * 2);
        const u32 vo = (u32)(g * NQ + h * DH + 8 * (r ^ ((g & 1) << 3))) * 2u;
        auto fill = [&](int ch) {
#pragma unroll 1
            for (int i = 0; i < 4; ++i) {
                const u32 dst = uu((ch % 3) * CS + (4 * i + w) * GS), so = uu((ch * 64 + 16 * i + 4 * w) * (NQ * 2));
                DMAIN(sq, dst, vo, so); DMAIN(sk, dst + 16 * GS, vo, so); DMAIN(sv, dst + 32 * GS, vo, so);
            }
        };
        fill(0); fill(1); fill(2);
        const int q4 = r >> 2;
        lptr tb = lds + (4 * (g >> 1) + 2 * (q4 >> 1) + (g & 1)) * GS + (q4 & 1) * 256 + ((w * 64 + (r & 3) * 8) ^ ((q4 & 1) << 7));
        SFOR(ch, 10)
            VMW(ch < 8 ? 24 : 108 - 12 * ch);
            BAR();
            auto frag = [&](int m, int t) { lptr b = tb + (ch % 3) * CS + m * 16 * GS + t * 32; return tr_frag(b, b + 512, b + 8 * GS, b + 8 * GS + 512); };
            wq0[ch] = frag(0, 0); wq1[ch] = frag(0, 1); wk0[ch] = frag(1, 0); wk1[ch] = frag(1, 1); wv0[ch] = frag(2, 0); wv1[ch] = frag(2, 1);
            asm volatile("" : "+a"(wq0[ch]), "+a"(wk0[ch]), "+v"(wv0[ch]), "+v"(wv1[ch]));
            if constexpr (qslot(ch) < 0) asm volatile("" : "+a"(wq1[ch])); else asm volatile("" : "+v"(wq1[ch]));
            if constexpr (kslot(ch) < 0) asm volatile("" : "+a"(wk1[ch])); else asm volatile("" : "+v"(wk1[ch]));
            BAR();
            if constexpr (ch < 7) fill(ch + 3);
        SEND
#pragma unroll
        for (int ch = 0; ch < 10; ++ch) {
            if (qslot(ch) >= 0) { lds_w16(WL + qslot(ch) * 2048, wq1[ch].lo); lds_w16(WL + qslot(ch) * 2048 + 1024, wq1[ch].hi); }
            if (kslot(ch) >= 0) { lds_w16(WL + kslot(ch) * 2048, wk1[ch].lo); lds_w16(WL + kslot(ch) * 2048 + 1024, wk1[ch].hi); }
            if (vslot(ch) >= 0) { lds_w16(WL + vslot(ch) * 2048, wv1[ch].lo); lds_w16(WL + vslot(ch) * 2048 + 1024, wv1[ch].hi); }
        }
        CFENCE();
    }
    const u32 idx = (r & 2) ? 0xEEEEu : 0x4444u;
    unsigned long long himask = 0xCCCCCCCCCCCCCCCCull;
    PINS(himask);
    bool dead = false;
    if (h == 3) LST(266 + L, rt32());
    if (w == 0) LST(1240 + L * 32 + h, rt32());
    if (L == 1) {
        KvIn in;
        kvt_load(p, KT0 + 4 * h + w, in, [] {}); kvt_make(rs, lds + w * 9216, KT0 + 4 * h + w, in);
        DRAIN(); BAR();
        pre_post(rs, 64 + h, 3u, ep);
    }
    const u32 pol0 = pol_wait(rs, ep);
    if (h == 3 && L == 0) LST(277, rt32());
    const u32 l16 = (u32)lane * 16u;
    const u32 mring = W_MRING + (u32)L * (MB / 4) + pol0 * 8192u, hint = mring + 4 * 32768u;
    const u32 fkh = lane < 3 ? foff(F_QKV + L * 96 + lane * 32 + h) : lane < 7 ? hint + (u32)(lane - 3) * 4u : OOB;
    u32 tm = 0;
    MK_SRD();
    u32 ksrc = uu(W_KQ + (u32)(L * (NSEG - 1) * NH + h) * 16384u + w * 2048), vsrc = ksrc + (W_VO - W_KQ);
    u32 dk = uu(C_K + w * 2048), dv = dk + (C_V - C_K), dq = dk + (C_QX - C_K), dm = uu(C_MEM + w * 2048), dm2 = uu(w < 2 ? C_MEM + 8192 + w * 1024 : C_DUMP);
    asm volatile("" : "+s"(dk), "+s"(dv), "+s"(dq), "+s"(dm), "+s"(dm2));
    auto keys_wait = [&](u32 need) {
#pragma unroll 1
        for (int n = 0; !dead; ++n) {
            const bool beat = need + 2 * L > 2u;
            if (beat) wait_until(tm);
            const u32 hv = ld4(rs, lane == 7 ? foff(F_ERR) : fkh);
            if (beat) { tm = rdl(hv, 5); wait_until(rdl(hv, 3) - 30u); }
            if (((u32)__builtin_amdgcn_ballot_w64(ld4(rs, fkh) < need) & 7u) == 0u) break;
            dead = n > SPIN_CAP || rdl(hv, 7) != 0u;
            st4s(rs, dead ? foff(F_ERR) : OOB, 0, 1u);
        }
        CFENCE();
        DMA32(dk, l16, ksrc); DMA32(dv, l16, vsrc); DMA32(dq, l16, ksrc ^ 8192u);
    };
    if (L == 0) { st4s(rs, tid == 0 ? hint + 4096u + (u32)h * 128u : OOB, 0, 0u); keys_wait(1u); }
    lptr qw = lds + C_Q + (2 * g) * 288 + (32 * w + 2 * r) * 2;
    lptr kw = qw + (C_KM - C_Q); PIN(kw);
    lptr qr = lds + C_Q + (2 * (r >> 2) + (r & 1)) * 288 + g * 16;
    lptr LB = lds + l16, VX = lds + C_V + w * 2048 + l16;
    PIN(VX);
    if (tid >= 64 && tid < 70) { const u32 z = tid == 68 ? 0x3F803F80u : tid == 69 ? PSENT : 0u; lds_w16(lds + C_ZERO + (tid - 64) * 16, (v4u){z, z, z, z}); }
    u32 zaddr = 0; PIN(zaddr);
    lptr ZP = lds + zaddr + C_ZERO;
    const float CEXP = SCALE * LOG2E;
#define LF_RD(slot, hf) lf[slot][hf] = lds_r16(WL + (slot) * 2048 + (hf) * 1024)
    BAR();
    const u32 ofl = tid == 0 ? foff(F_O + L * 32 + h) : OOB;
    u32 oso = (((g | L) & 1) ? OOB : 0u) + W_ORING + (u32)L * (MB / 2) + ring_map((u32)((r >> 3) * 32768 + h * 1024 + w * 256 + (g >> 1) * 128 + (r & 7) * 16), pol0);
    unsigned long long vis[8];
#pragma unroll
    for (int i = 0; i < 8; ++i) vis[i] = __builtin_amdgcn_ballot_w64(16 * (i >> 2) + 4 * g + (i & 3) <= 16 * (w & 1) + r);
    u32 missed = 0;
    for (int s = -L; s < NSEG;) {
        u32 sv = (u32)max(s, 0), ms = mring + (sv & 3u) * 32768u + w * 2048, ms2 = mring + (sv & 3u) * 32768u + 16384u + (w & 1) * 1024;
        asm volatile("" : "+s"(ms), "+s"(ms2)); PIN(sv);
        f32x4 aq0, aq1, ak0, ak1;
        v4u lf[NLF][2];
        ZRD(aq0, 0); ZRD(aq1, 1); ZRD(ak0, 2); ZRD(ak1, 3);
#pragma unroll
        for (int i = 0; i < 4; ++i) { LF_RD(i, 0); LF_RD(i, 1); }
        SCHED();
        v4u A[10];
#define HT(k) do {} while (0)
        v4u kx[8], qf[4], kmf[4], vx[2], ones, P;
        float fa, fb, fc, fd, inv, sx[4], t[4], tm0, tm1; u32 pa, pb, fl, e01, e23, pmm;
        f32x4 av0, av1, s0, s1, sm, oa, ob, rsum;
        v2u vm, pm;
#define LW(sl) cat8(lf[sl][0], lf[sl][1])
#define PQ(ks, tl) do { if constexpr (!(tl)) SMF_A(aq0, A[ks], wq0[ks]); else if constexpr (qslot(ks) < 0) SMF_A(aq1, A[ks], wq1[ks]); else { const v8u lw_ = LW(qslot(ks)); SMF_V(aq1, A[ks], lw_); } } while (0)
#define PK(ks, tl) do { if constexpr (!(tl)) SMF_A(ak0, A[ks], wk0[ks]); else if constexpr (kslot(ks) < 0) SMF_A(ak1, A[ks], wk1[ks]); else { const v8u lw_ = LW(kslot(ks)); SMF_V(ak1, A[ks], lw_); } } while (0)
#define PV(ks, tl) do { if constexpr (!(tl)) SMF_V(av0, A[ks], wv0[ks]); else if constexpr (vslot(ks) < 0) SMF_V(av1, A[ks], wv1[ks]); else { const v8u lw_ = LW(vslot(ks)); SMF_V(av1, A[ks], lw_); } } while (0)
#define KXR(i) kx[i] = lds_r16(LB + C_K + ((i) & 1) * 4096 + ((i) >> 1) * 1024)
#define XW(q_) do { *(LAS u32*)(q_) = pa; *(LAS u32*)((q_) + 288) = pb; } while (0)
        auto slot = [&](auto zc) __attribute__((always_inline)) {
            constexpr int z = decltype(zc)::value;
            if constexpr (z < 14) PQ(z >> 1, z & 1);
            else if constexpr (z < 17) PQ(z - 7, 0);
            else if constexpr (z < 20) PQ(z - 10, 1);
            else if constexpr (z < 34) PK((z - 20) >> 1, z & 1);
            else if constexpr (z < 37) PK(z - 27, 0);
            else if constexpr (z < 40) PK(z - 30, 1);
            else if constexpr (z < 42) { if constexpr (z == 40) MF0(s0, kx[0], qf[0]); else MF0(s1, kx[1], qf[0]); }
            else if constexpr (z < 48) { if constexpr (z & 1) MF1(s1, kx[z - 40], qf[(z - 40) >> 1]); else MF1(s0, kx[z - 40], qf[(z - 40) >> 1]); }
            else if constexpr (z < 54) PV(z - 48, 0);
            else if constexpr (z == 54) MF0(sm, kmf[0], qf[0]);
            else if constexpr (z < 58) MF1(sm, kmf[z - 54], qf[z - 54]);
            else if constexpr (z < 62) PV(z - 52, 0);
            else if constexpr (z < 64) PV(z - 62, 1);
            else if constexpr (z == 64) MF0(oa, vx[0], P);
            else if constexpr (z == 65) MF0(ob, vx[1], P);
            else if constexpr (z == 66) MF0(rsum, ones, P);
            else if constexpr (z < 75) PV(z < 70 ? z - 63 : z == 70 ? 2 : z == 71 ? 7 : z == 72 ? 3 : z - 65, 1);
            else { const v2u ones2 = ones.lo; MF1H(rsum, ones2, pm); }
            if constexpr (z < 6) A[4 + z] = lds_r16(LB + C_MEM + (4 + z) * 1024);
            if constexpr (z >= 6 && z < 10) LF_RD(4 + ((z - 6) >> 1), z & 1);
            if constexpr (z >= 14 && z < 18) LF_RD(6 + ((z - 14) >> 1), z & 1);
            if constexpr (z >= 22 && z < 28) KXR(z - 22);
            if constexpr (z == 28) XW(qw);
            if constexpr (z == 29 || z == 30) KXR(z - 23);
            if constexpr (z == 32 || z == 47) asm volatile("s_barrier" ::: "memory");
            if constexpr (z >= 33 && z < 37) qf[z - 33] = lds_r16(qr + (z - 33) * 64);
            if constexpr (z == 20) ZRD(av0, 0);
            if constexpr (z == 43) XW(kw);
            if constexpr (z == 44 || z == 45) vx[z - 44] = lds_r16(VX + (z - 44) * 1024);
            if constexpr (z >= 48 && z < 52) kmf[z - 48] = lds_r16(qr + (C_KM - C_Q) + (z - 48) * 64);
            if constexpr (z == 52) ZRD(av1, 1);
            if constexpr (z == 37) ones = lds_r16(ZP + 64);
            if constexpr (z >= 57 && z < 64) LF_RD(8 + ((z - 57) >> 1), (z - 57) & 1);
            if constexpr (z >= 65 && z < 70) LF_RD(8 + ((z - 58) >> 1), (z - 58) & 1);
            if constexpr (z == HD_SMP) LD4A(fl, fkh, 0);
            SCHED();
            if constexpr (z == 19) HT(2);
            if constexpr (z == 32) HT(3);
            if constexpr (z == 47) HT(4);
            if constexpr (z == 64) HT(5);
            if constexpr (z == 19) LGK(4);
            if constexpr (z == 21 || z == 41) LGK(1);
            if constexpr (z == 56 || z == 73) LGK(0);
            if constexpr (z == 31 || z == 46) LGK(2);
            if constexpr (z == 39) LGK(4);
            if constexpr (z >= 53 && z < 56) LGK(57 - z);
            if constexpr (z == 64) LGK(3);
            if constexpr (z == 68) LGK(5);
            if constexpr (z >= 70 && z < 73) LGK(74 - z);
            SCHED();
            if constexpr (z == 18) CADD(fa, aq0[0], aq0[2]);
            if constexpr (z == 19) CADD(fb, aq0[1], aq0[3]);
            if constexpr (z == 20) CMULC(fa, fa);
            if constexpr (z == 21) CADD(fc, aq1[0], aq1[2]);
            if constexpr (z == 22) CADD(fd, aq1[1], aq1[3]);
            if constexpr (z == 23) CMULC(fc, fc);
            if constexpr (z == 24) CMULC(fb, fb);
            if constexpr (z == 25) CMULC(fd, fd);
            if constexpr (z == 26) CCVT(pa, fa, fc);
            if constexpr (z == 27) CCVT(pb, fb, fd);
            if constexpr (z == 38) CADD(fa, ak0[0], ak0[2]);
            if constexpr (z == 39) CADD(fb, ak0[1], ak0[3]);
            if constexpr (z == 41) { CADD(fc, ak1[0], ak1[2]); CADD(fd, ak1[1], ak1[3]); }
            if constexpr (z == 42) { CCVT(pa, fa, fc); CCVT(pb, fb, fd); }
            if constexpr (z >= 49 && z < 53) SEL(sx[z - 49], s0[z - 49], s1[z - 49]);
            if constexpr (z >= 53 && z < 57) VEXP(t[z - 53], sx[z - 53]);
            if constexpr (z == 57) CCVT(e01, t[0], t[1]);
            if constexpr (z == 58) CCVT(e23, t[2], t[3]);
            if constexpr (z == 59) QPERM(P.x, e01, "[0,1,0,1]");
            if constexpr (z == 60) QPERM(P.z, e01, "[2,3,2,3]");
            if constexpr (z == 61) QPERM(P.y, e23, "[0,1,0,1]");
            if constexpr (z == 62) QPERM(P.w, e23, "[2,3,2,3]");
            if constexpr (z == 63) CADD(fa, av0[0], av0[2]);
            if constexpr (z == 64) { asm volatile("" :: "v"(sm)); VEXP(tm0, sm[0]); }
            if constexpr (z == 65) VEXP(tm1, sm[1]);
            if constexpr (z == 66) CADD(fb, av0[1], av0[3]);
            if constexpr (z == 67) CCVT(pmm, tm0, tm1);
            if constexpr (z >= 68 && z < 72) SEL(oa[z - 68], oa[z - 68], ob[z - 68]);
            if constexpr (z == 72) asm volatile("v_cndmask_b32 %0, %1, 0, %2" : "=v"(pm.x) : "v"(pmm), "s"(himask));
            if constexpr (z == 73) asm volatile("v_cndmask_b32 %0, 0, %1, %2" : "=v"(pm.y) : "v"(pmm), "s"(himask));
            if constexpr (z == 74) LGK(15);
            if constexpr (z == 75) CCVT(vm.x, fa, fb);
            SCHED();
        };
        {
            u32 tmp;
            asm volatile("s_mov_b32 m0, %2\n\t" WAIT_TXT("%0", "%1") "buffer_load_dwordx4 %4, %5, %6 offen sc1 lds\n\tbuffer_load_dwordx4 %4, %5, %6 offen offset:1024 sc1 lds\n\t"
                         "s_mov_b32 m0, %3\n\ts_nop 0\n\tbuffer_load_dwordx4 %4, %5, %7 offen sc1 lds\n\t.p2align 6\n\t.rept %8\n\ts_nop 0\n\t.endr\n\ts_waitcnt vmcnt(0) lgkmcnt(0)\n\ts_barrier"
                         : "=&s"(tmp) : "s"(uu(tm - 4u)), "s"(dm), "s"(dm2), "v"(l16), "s"(srd), "s"(ms), "s"(ms2), "i"(AL_H) : "vcc", "scc", "memory");
        }
        HT(1);
#pragma unroll
        for (int ks = 0; ks < 4; ++ks) A[ks] = lds_r16(LB + C_MEM + ks * 1024);
        SCHED();
        st4s(rs, ofl, 0, sv);
        SCHED();
        static_for<0, 76>(slot);
        asm volatile("s_nop 2\n\tv_add_f32 %1, %3, %5\n\tv_add_f32 %2, %4, %6\n\tv_cvt_pk_bf16_f32 %0, %1, %2"
                     : "=v"(vm.y), "=&v"(fc), "=&v"(fd) : "v"(av1[0]), "v"(av1[1]), "v"(av1[2]), "v"(av1[3]));
        __builtin_amdgcn_s_waitcnt(0xCF7F);
        asm volatile("s_nop 0\n\tv_mfma_f32_16x16x16_bf16 %0, %2, %3, %0\n\tv_rcp_f32 %1, %4" : "+v"(oa), "=&v"(inv) : "v"(vm), "v"(pm), "v"(rsum[0]), "v"(rsum));
        SCHED();
        if (__builtin_expect(__builtin_amdgcn_ballot_w64(u_(rsum[0]) - 0x21800000u > 0x3C000000u) != 0ull, 0)) {
            DRAIN(); SCHED();
            bool miss = false;
#pragma unroll 1
            for (int i = 0; i < 10; ++i) { const v4u a = lds_r16(LB + C_MEM + i * 1024); miss |= a.x == PSENT || a.y == PSENT || a.z == PSENT || a.w == PSENT; }
            if (missed < SPIN_CAP && !all_(!miss)) {
                *(LAS u32*)(lds + C_DUMP + (w == 0 ? 0 : 64)) = ld4(rs, foff(F_ERR));
                asm volatile("" : "+v"(fl) :: "memory");
                tm = rdl(fl, 5);
                BAR();
                missed = uu(*(LAS u32*)(lds + C_DUMP)) != 0u ? SPIN_CAP : missed + 1;
                st4s(rs, missed == SPIN_CAP ? foff(F_ERR) : OOB, 0, 1u);
                BAR();
                continue;
            }
            float mx = fmaxf(fmaxf(fmaxf(sx[0], sx[1]), fmaxf(sx[2], sx[3])), fmaxf(sm[0], sm[1]));
            mx = max32(max16(fmaxf(mx, dpp<0x4E>(mx))));
#pragma unroll
            for (int i = 0; i < 4; ++i) t[i] = __builtin_amdgcn_exp2f(sx[i] - mx);
            e01 = pk(t[0], t[1]); e23 = pk(t[2], t[3]);
            pmm = pk(__builtin_amdgcn_exp2f(sm[0] - mx), __builtin_amdgcn_exp2f(sm[1] - mx));
            asm volatile("v_cndmask_b32 %0, %2, 0, %3\n\tv_cndmask_b32 %1, 0, %2, %3" : "=&v"(pm.x), "=&v"(pm.y) : "v"(pmm), "s"(himask));
            asm volatile("s_nop 1" : "+v"(e01), "+v"(e23), "+v"(pm));
            QPERM(P.x, e01, "[0,1,0,1]"); QPERM(P.z, e01, "[2,3,2,3]"); QPERM(P.y, e23, "[0,1,0,1]"); QPERM(P.w, e23, "[2,3,2,3]");
            NOPS(1);
            MF0(rsum, ones, P); MF0(oa, vx[0], P); MF0(ob, vx[1], P);
            NOPS(7);
            { const v2u ones2 = ones.lo; MF1H(rsum, ones2, pm); }
#pragma unroll
            for (int i = 0; i < 4; ++i) SEL(oa[i], oa[i], ob[i]);
            NOPS(1);
            MF1H(oa, vm, pm);
            asm volatile("s_nop 7\n\tv_rcp_f32 %0, %1" : "=v"(inv) : "v"(rsum[0]), "v"(rsum));
        }
        {
            v4u oc; f32x2 u0 = oa.lo, u1 = oa.hi; const f32x2 i2 = {inv, inv};
            asm volatile("s_nop 0\n\tv_pk_mul_f32 %0, %0, %2\n\tv_pk_mul_f32 %1, %1, %2" : "+v"(u0), "+v"(u1) : "v"(i2));
            __builtin_amdgcn_s_waitcnt(0xCF7F);
            asm volatile("v_cvt_pk_bf16_f32 %0, %4, %5\n\tv_cvt_pk_bf16_f32 %2, %4, %5\n\tv_cvt_pk_bf16_f32 %1, %6, %7\n\tv_cvt_pk_bf16_f32 %3, %6, %7\n\t"
                         "v_permlane16_swap_b32 %0, %2\n\ts_nop 0\n\tv_permlane16_swap_b32 %1, %3"
                         : "=&v"(oc.x), "=&v"(oc.y), "=&v"(oc.z), "=&v"(oc.w) : "v"(u0.x), "v"(u0.y), "v"(u1.x), "v"(u1.y));
            st16(rs, oso, oc);
        }
        SCHED();
        unsigned long long clk;
        asm volatile("s_memrealtime %0" : "=s"(clk) :: "memory");
        HT(6);
        v4u tq[4], tk[8], tv[2];
        f32x4 c0, c1, cm;
        u32 oo, kvo; bool kvok;
        u32 ln = l16; PIN(ln);
        lptr QX = lds + C_QX + (w & 1) * 4096 + ln, VO = lds + C_V + (w ^ 1) * 2048 + ln, VMX = lds + C_VM + w * 512 + (ln >> 1);
#pragma unroll
        for (int i = 0; i < 2; ++i) { tq[i] = lds_r16(QX + i * 1024); kmf[i] = lds_r16(qr + (C_KM - C_Q) + i * 64); tk[2 * i] = lds_r16(LB + C_K + i * 1024); tk[2 * i + 1] = lds_r16(LB + C_K + 4096 + i * 1024); }
        *(LAS v2u*)VMX = vm;
        SCHED();
        SFOR(i, 12)
            if constexpr (i == 0) MF0(cm, kmf[0], tq[0]); else if constexpr (i == 1) MF0(c0, tk[0], tq[0]); else if constexpr (i == 2) MF0(c1, tk[1], tq[0]);
            else if constexpr (i % 3 == 0) MF1(cm, kmf[i / 3], tq[i / 3]); else if constexpr (i % 3 == 1) MF1(c0, tk[2 * (i / 3)], tq[i / 3]); else MF1(c1, tk[2 * (i / 3) + 1], tq[i / 3]);
            if constexpr (i < 2) kmf[2 + i] = lds_r16(qr + (C_KM - C_Q) + (2 + i) * 64);
            if constexpr (i == 0) { tq[2] = lds_r16(QX + 2048); tk[4] = lds_r16(LB + C_K + 2048); }
            if constexpr (i == 1) { tk[5] = lds_r16(LB + C_K + 6144); tq[3] = lds_r16(QX + 3072); }
            if constexpr (i == 2) { tk[6] = lds_r16(LB + C_K + 3072); tk[7] = lds_r16(LB + C_K + 7168); }
            if constexpr (i == 3) { tv[0] = lds_r16(VO); tv[1] = lds_r16(VO + 1024); }
            if constexpr (i == 4) { oo = ln + (ln & 0x300u) + ((vsrc ^ 8192u) + (u32)((w & 1) * 256) + ((u32)s & OOB)); PIN(oo); }
            if constexpr (i == 5) { ksrc += NH * 16384; vsrc += NH * 16384; PINS(ksrc); PINS(vsrc); }
            if constexpr (i == 9) { asm volatile("s_waitcnt vmcnt(1)" : "+v"(fl) :: "memory"); kvok = ((u32)__builtin_amdgcn_ballot_w64(fl < (u32)s + 2) & 7u) == 0u; }
            if constexpr (i == 10) { kvo = l16 | (kvok ? 0u : OOB); PIN(kvo); }
            SCHED();
        SEND
        asm volatile("s_waitcnt lgkmcnt(0)\n\ts_barrier" : "+s"(clk) :: "memory");
        const v2u tvm = *(LAS v2u*)(VMX + (512 - (w & 1) * 1024));
        const v4u pv = lds_r16(ZP + 80);
        DMA32(dk, kvo, ksrc);
        SCHED(); asm volatile("" :: "v"(cm));
        float u[10], m, m2;
        {
            float NEG = -1000000000.0f / SCALE; PIN(NEG);
#define MSK(d, a, i) asm volatile("v_cndmask_b32 %0, %1, %2, %3" : "=v"(d) : "v"(NEG), "v"(a), "s"(vis[i]))
#pragma unroll
            for (int i = 0; i < 4; ++i) MSK(u[i], c0[i], i);
#pragma unroll
            for (int i = 0; i < 4; ++i) MSK(u[4 + i], c1[i], 4 + i);
            asm volatile("v_max3_f32 %0, %2, %3, %10\n\tv_max3_f32 %1, %4, %5, %11\n\tv_max3_f32 %0, %0, %6, %7\n\tv_max3_f32 %1, %1, %8, %9\n\t"
                         "v_max_f32 %0, %0, %1\n\tv_mov_b32 %1, %0\n\ts_nop 1\n\tv_permlane16_swap_b32 %0, %1\n\tv_max_f32 %0, %0, %1\n\tv_mov_b32 %1, %0\n\ts_nop 1\n\t"
                         "v_permlane32_swap_b32 %0, %1\n\tv_max_f32 %0, %0, %1\n\tv_mul_f32 %0, 0xbe0293ee, %0"
                         : "=&v"(m), "=&v"(m2) : "v"(u[0]), "v"(u[1]), "v"(u[2]), "v"(u[3]), "v"(u[4]), "v"(u[5]), "v"(u[6]), "v"(u[7]), "v"(cm[0]), "v"(cm[1]));
        }
        DMA32(dv, kvo, vsrc);
        {
            v4u px; v2u p0, p1;
#define EX2(d, a, b) asm volatile("v_fmamk_f32 %0, %2, 0x3e0293ee, %4\n\tv_fmamk_f32 %1, %3, 0x3e0293ee, %4\n\tv_exp_f32 %0, %0\n\tv_exp_f32 %1, %1" : "=&v"(d.x), "=&v"(d.y) : "v"(a), "v"(b), "v"(m))
            f32x2 ex[5];
            EX2(ex[0], u[0], u[1]); EX2(ex[1], u[2], u[3]); EX2(ex[2], u[4], u[5]); EX2(ex[3], u[6], u[7]); EX2(ex[4], cm[0], cm[1]);
            asm volatile("v_cvt_pk_bf16_f32 %0, %8, %9\n\tv_cvt_pk_bf16_f32 %1, %10, %11\n\tv_cvt_pk_bf16_f32 %2, %12, %13\n\tv_cvt_pk_bf16_f32 %3, %14, %15\n\t"
                         "v_mov_b32 %5, 0\n\tv_mov_b32 %6, 0\n\tv_cvt_pk_bf16_f32 %4, %16, %17\n\tv_cvt_pk_bf16_f32 %7, %16, %17"
                         : "=&v"(px.x), "=&v"(px.y), "=&v"(px.z), "=&v"(px.w), "=&v"(p0.x), "=&v"(p0.y), "=&v"(p1.x), "=&v"(p1.y)
                         : "v"(ex[0].x), "v"(ex[0].y), "v"(ex[1].x), "v"(ex[1].y), "v"(ex[2].x), "v"(ex[2].y), "v"(ex[3].x), "v"(ex[3].y), "v"(ex[4].x), "v"(ex[4].y));
            f32x4 o[4], sum;
            const v2u ones2 = ones.lo;
            const u32 now = (u32)clk;
            u32 oz;
            SCHED();
            MF0(sum, ones, px); { const u32 q = ksrc ^ 8192u; DMA32(dq, kvo, q); } SCHED();
            MF0(o[0], vx[0], px);
            MF0(o[1], vx[1], px);
            MF0(o[2], tv[0], px); oz = (oso & ~(3u << 17)) | (u32)((s + 3) & 3) << 17; PIN(oz); SCHED();
            MF0(o[3], tv[1], px); oso = (oso & ~(3u << 17)) | (u32)((s + 1) & 3) << 17; PIN(oso); SCHED();
            MF1H(sum, ones2, p0);
            MF1H(o[0], vm, p0);
            MF1H(o[1], vm, p1); st16(rs, oz, pv); SCHED();
            MF1H(o[2], tvm, p0);
            MF1H(o[3], tvm, p1);
            asm volatile("s_nop 2\n\tv_rcp_f32 %0, %1\n\ts_nop 0" : "=v"(inv) : "v"(sum[0]), "v"(sum));
            asm volatile("" : "+v"(o[0]), "+v"(o[1]));
            st16(rs, oo, (v4u){pk2(o[0].lo * inv), pk2(o[0].hi * inv), pk2(o[1].lo * inv), pk2(o[1].hi * inv)});
            asm volatile("" : "+v"(o[2]), "+v"(o[3]));
            st16(rs, oo ^ 2048u, (v4u){pk2(o[2].lo * inv), pk2(o[2].hi * inv), pk2(o[3].lo * inv), pk2(o[3].hi * inv)});
            SCHED();
            LS2(L == 0 && w == 0, h);
            st4s(rs, ((h & 15) == 0 && tid == 192) ? hint + 128u + (u32)h * 8u : OOB, 0, (u32)s << 16 | min(missed, 1u) << 14 | min(now - tm, 0x3FFFu));
            tm = rdl(fl, 5);
            missed = missed == SPIN_CAP ? missed : 0u;
            if (__builtin_expect(s < 1 - L, 0)) {
                if (s < 0) oso -= (ln & 256u) ? 0u : OOB;
#pragma unroll 1
                for (int n = 0; n < (s < 0 ? 0 : 200); ++n) {
                    CFENCE();
                    tm = rdl(ld4(rs, fkh), 6);
                    if (tm != PSENT) break;
                }
            }
        }
        if (__builtin_expect(!kvok, 0) && s + 1 < NSEG) {
            st4s(rs, ln == 0 ? hint + 4096u + (u32)h * 128u : OOB, 0, (u32)s + 1u);
            keys_wait((u32)s + 2u);
        }
        ++s;
    }
    DRAIN(); BAR();
    st4s(rs, ofl, 0, NSEG);
    LDUMP(w == 0, 160 + L * 32 + h);
}

#define WO_NL 3
#define WO_ROT 0
#define WO_AH 1
DEV void role_wo(const Params& p, lptr lds, int j, v2u ep, u32 tent) {
    const u32 trole = rt32();
    const int tid = threadIdx.x, lane = tid & 63, w = uni(tid >> 6), g = lane >> 4, r = lane & 15;
    const int rg = j & 1, cg = j >> 1, rot = WO_ROT;
    const rsrc_t rs = mk_rsrc(p.ws, W_END);
    v8u wo[8][5];
    {
        const v4u so_ = mk_srd(p.Wo, DM * NQ * 2);
        u32 vo[5];
#pragma unroll
        for (int i = 0; i < 5; ++i) { const int n = 64 * i + lane; vo[i] = (u32)((n / 5) * DM + cg * 40 + (n % 5) * 8) * 2u; }
        v4u junk = {};
        const u32 av = (u32)(j * 5120 + lane * 16);
        auto ahead = [&](int b) {
#pragma unroll
            for (int i = 0; i < 5; ++i) AHEAD(so_, av, uu((8 * w + b) * (DH * DM * 2) + i * 1024));
        };
        auto fill = [&](int b) {
            if (b + WO_AH < 8) ahead(b + WO_AH);
            const u32 dst = uu(w * 40960 + (b & 3) * 10240), src = uu((8 * w + ((b + rot) & 7)) * (DH * DM * 2));
#pragma unroll
            for (int i = 0; i < 10; ++i) DMAIN(so_, dst + i * 1024, vo[i % 5], src + (i / 5) * (64 * DM * 2));
        };
#pragma unroll 1
        for (int b = 0; b < WO_AH; ++b) ahead(b);
#pragma unroll 1
        for (int b = 0; b < 4; ++b) fill(b);
        const int q4 = r >> 2;
        lptr tb = lds + w * 40960 + (32 * (g >> 1) + 16 * (q4 >> 1) + 8 * ((r >> 1) & 1) + 4 * (g & 1) + (q4 & 1)) * 80 + (r & 1) * 8;
        SFOR(b, 8)
            VMW((b < 5 ? 30 : 70 - 10 * b) + (b + 1 + WO_AH < 8 ? 5 : 0) + (b + 2 + WO_AH < 8 ? 5 : 0) + (b + 3 + WO_AH < 8 ? 5 : 0));
            if constexpr (b + WO_AH == 7) PIN(junk);
#pragma unroll
            for (int t = 0; t < 5; ++t) {
                lptr q = tb + (b & 3) * 10240 + t * 16;
                wo[b][t] = tr_frag(q, q + 160, q + 64 * 80, q + 64 * 80 + 160);
                if (b * 5 + t < 32) asm volatile("" : "+a"(wo[b][t])); else asm volatile("" : "+v"(wo[b][t]));
            }
            CFENCE();
            if constexpr (b < 4) fill(b + 4);
        SEND
    }
    if (j == 0) LST(265, rt32());
    LST(1100 + j * 4 + w, rt32());
    const u32 idx = (r & 2) ? 0xEEEEu : 0x4444u;
    const u32 twts = rt32();
    const u32 pol0 = pol_wait(rs, ep);
    BAR();
    const u32 l16 = (u32)lane * 16u;
    u32 oo[8];
#pragma unroll
    for (int i = 0; i < 8; ++i) oo[i] = uu(W_ORING + ring_map((u32)(rg * 32768 + (8 * w + ((i + rot) & 7)) * 1024), pol0));
    const u32 hint = W_MRING + 4 * 32768u + pol0 * 8192u;
    u32 hvo[2];
#pragma unroll
    for (int l = 0; l < 2; ++l) hvo[l] = lane < 2 ? hint + (u32)(l ^ 1) * (MB / 4) + (u32)lane * 4u : lane == 2 ? hint + (u32)l * (MB / 4) + 128u : lane == 3 ? foff(F_ERR) : lane == 4 ? hint + (u32)l * (MB / 4) + 256u :
                 (lane >= 32 && j == 0 && w == 3) ? hint + (u32)l * (MB / 4) + (u32)lane * 128u : OOB;
    lptr xw = r < 8 ? lds + w * 640 + (2 * (g & 1) + (g >> 1)) * 32 + r * 4 : lds + 8192 + lane * 4;
    const int G = ((5 * cg) >> 1) + w, tt = 2 * G + ((lane >> 3) & 1) - 5 * cg;
    const bool pub = w < 3 && lane < 32 && tt >= 0 && tt < 5;
    lptr xr = lds + (pub ? tt * 128 + (2 * (lane >> 4) + ((lane >> 2) & 1)) * 32 + (lane & 3) * 8 : 0);
    const u32 mpo = pub ? W_MRING + ring_map((u32)(G * 256 + rg * 128 + lane * 4), pol0) : OOB;
    constexpr u32 PER0 = CH_W0 + CH_MW + CH_H0 + CH_MH + 2 * CH_VIS, DW0 = CH_W0 + CH_VIS + CH_MW + CH_REC;
    const bool lead = j == 0 && w == 3;
    const u32 hso = (lead && lane == 0) ? hint : OOB;
    MK_SRD();
    u32 t = 0, tn = 0;
    int sn[2] = {0, 0};
    u32 hav = 8 * CH_H0, wav = 8 * CH_W0, mh = 8 * CH_MH, mw = 8 * CH_MW, ok[2] = {0u, 0u}, dw[2] = {DW0, DW0};
    u32 vo = l16, early = PSENT, tp = 0, fin, left, cnt = 0;
    unsigned long long clk = 0;
    {
        if (w == 0) LST(1310 + j, rt32());
        if (lead) {
#pragma unroll 1
            for (int n = 0; n < SPIN_CAP; ++n) {
                CFENCE();
                const u32 a = ld4(rs, foff(F_QKV + lane)), b = ld4(rs, lane < 32 ? foff(F_QKV + 64 + lane) : hvo[0]);
                if (all_(a != 0u && b != (lane < 32 ? 0u : PSENT))) break;
                __builtin_amdgcn_s_sleep(1);
            }
        }
        if (lead) {
            t = rt32() + CH_T0; tn = t + PER0 / 2 + CH_REC;
            st16(rs, lane < 2 ? hint + (u32)lane * (MB / 4) : OOB, (v4u){lane == 0 ? t : tn, PER0, (lane == 0 ? t : tn) + DW0, t + DW0});
        } else {
#pragma unroll 1
            for (int n = 0; n < SPIN_CAP; ++n) {
                CFENCE();
                const u32 hv = ld4(rs, hvo[1]);
                t = rdl(hv, 0);
                if (rdl(hv, 1) < 400u) break;
                __builtin_amdgcn_s_sleep(1);
            }
        }
        clk = t - 100u;
    }
    const u32 tm0 = t + DW0;
    u32 zero = 0, hv = 0xFFFFu, pst = 1, pmp = OOB;
    const u32 psent = PSENT;
    lptr sw = lds + 2560 + w * 4;
    asm volatile("" : "+v"(zero), "+v"(sw));
    auto turn = [&](auto lc) __attribute__((always_inline)) {
        constexpr int L = decltype(lc)::value, M = L ^ 1;
        u32 done, rep, rd, lost, dh, dn, nt, za, pdur, mp; v4u hw;
        const int sc = sn[L];
        auto books = [&](auto ic) __attribute__((always_inline)) {
            constexpr int i = decltype(ic)::value;
            if constexpr (i == 0) { rep = max(rdl(hv, 2), rdl(hv, 4)); PINS(rep); }
            if constexpr (i == 1) { lost = (u32)(__builtin_amdgcn_ballot_w64(hv == (u32)sn[M]) >> 32); PINS(lost); }
            if constexpr (i == 2) { lost |= (rep >> 16) ^ (u32)sn[M]; PINS(lost); }
            if constexpr (i == 3) { lost |= ok[M] ^ 2u; rd = rep & 0xFFFFu; PINS(lost); PINS(rd); }
            if constexpr (i == 4) { lost = lost == 0u ? CH_UP * 8u : 0u; PINS(lost); }
            if constexpr (i == 5) { pdur = ((u32)clk - tp) * 8u; PINS(pdur); pdur = min(pdur, wav + CH_RISE); PINS(pdur); }
            if constexpr (i == 6) { pdur = (done & ok[M]) ? pdur : wav; PINS(pdur); }
            if constexpr (i == 7) { wav = max(pdur, wav - CH_FALL); PINS(wav); }
            if constexpr (i == 8) { nt = min(rd * 8u, hav + CH_RISE); PINS(nt); }
            if constexpr (i == 9) { nt = rd < 0x3FFFu ? nt : hav; PINS(nt); }
            if constexpr (i == 10) { hav = max(nt, hav - CH_FALL); PINS(hav); }
            if constexpr (i == 11) { mw = max(mw - 1u, CH_MW * 8u); PINS(mw); }
            if constexpr (i == 12) { mh = max(mh - 1u, CH_MH * 8u); PINS(mh); }
            if constexpr (i == 13) { rd = rd >> 14 == 1u ? lost : 0u; PINS(rd); }
            if constexpr (i == 14) { mw += rd; mh += lost - rd; PINS(mw); PINS(mh); }
            if constexpr (i == 15) { ok[M] = done ? 1u : (ok[M] & 1u) << 1; PINS(ok[M]); }
            if constexpr (i == 16) { dh = (hav + mh) / 8u + (sn[1] < 3 ? CH_VIS + CH_PRE : CH_VIS); PINS(dh); }
            if constexpr (i == 17) { dn = (wav + mw) / 8u + CH_VIS; PINS(dn); }
            if constexpr (i == 18) { nt = dw[L] + dh + (u32)max((int)((u32)clk + CH_WX - t), 0); PINS(nt); }
            if constexpr (i == 19) { dh += dn; dw[L] = dn; sn[M] += (int)done; PINS(dh); PINS(sn[M]); }
            if constexpr (i == 20) { rd = tn - t + (dh >> 1); PINS(rd); }
            if constexpr (i == 21) { lost = (u32)sc - 1u < NSEG - 2 ? CH_RTY : 0u; PINS(lost); nt += ok[L] != 1u ? lost : 0u; PINS(nt); }
            if constexpr (i == 22) { nt = t + (u32)max((int)nt, (int)rd); PINS(nt); }
            if constexpr (i == 23) { hw.x = nt; hw.y = dh; PIN(hw.x); PIN(hw.y); }
            if constexpr (i == 24) { hw.z = nt + dn; hw.w = tm0; PIN(hw); }
            if constexpr (i == 25) st16(rs, hso + L * (MB / 4), hw);
        };
        {
#define WT(k) do {} while (0)
            WT(0);
            v4u d[8];
            {
                u32 tmp;
                asm volatile(WAIT_TXT("%1", "%2") "buffer_load_dwordx4 %0, %3, %4, %5 offen sc1\n\ts_waitcnt lgkmcnt(0)"
                             : "=&v"(d[0]), "=&s"(tmp) : "s"(uu(t - 4u)), "v"(vo), "s"(srd), "s"(oo[0]) : "vcc", "scc", "memory");
                PINS(clk);
            }
            SFOR(i, 7)
                asm volatile("buffer_load_dwordx4 %0, %1, %2, %3 offen sc1" : "=v"(d[i + 1]) : "v"(vo), "s"(srd), "s"(oo[i + 1]) : "memory");
                SCHED();
                if constexpr (i == 0) { done = uu(pst) ^ 1u; PINS(done); }
                static_for<3 * i, 3 * i + 3>(books);
                SCHED();
            SEND
            LD4A(hv, hvo[L], 0);
            SCHED();
            WT(1);
            static_for<21, 26>(books);
            za = done ? pmp : OOB;
            st4s(rs, za, 0, psent);
            *(LAS u32*)(sw + L * 4096) = zero;
            f32x4 acc[5] = {};
            mp = mpo + (L * (MB / 4) + (u32)((sc + 1) & 3) * 32768u); pmp = mpo + (L * (MB / 4) + (u32)(sc & 3) * 32768u);
            asm volatile("" : "+v"(acc[0]), "+v"(acc[1]), "+v"(acc[2]), "+v"(acc[3]), "+v"(acc[4]), "+v"(mp), "+v"(pmp));
            SCHED();
            float f0[5], f1[5]; unsigned long long bad;
#define WSW(o_) asm volatile("v_permlane32_swap_b32 %0, %1" : "+v"(f0[o_]), "+v"(f1[o_]))
#define WAD(o_) asm volatile("v_add_f32_dpp %0, %1, %0 row_ror:8 row_mask:0xf bank_mask:0xf" : "+v"(f0[o_]) : "v"(f1[o_]))
#define WFO(o_) do { VADD(f0[o_], acc[o_][0], acc[o_][2]); VADD(f1[o_], acc[o_][1], acc[o_][3]); } while (0)
#define WWR(o_) *(LAS float*)(xw + L * 4096 + (o_) * 128) = f0[o_]
            u32 th, per, so, erl = early;
            auto next = [&](auto ic_) __attribute__((always_inline)) {
                constexpr int i = decltype(ic_)::value;
                if constexpr (i == 0) { so = (u32)sn[M] << 17; PINS(so); }
                if constexpr (i == 1) { so = (so & 0x60000u) + M * (MB / 2); PINS(so); }
                if constexpr (i == 2) { so = sn[M] + 1 < NSEG ? so : OOB; PINS(so); }
                if constexpr (i == 3) { early = sn[M] + 1 < NSEG ? PSENT : 0u; PIN(early); }
                if constexpr (i == 4) { vo = l16 + so; PIN(vo); }
                if constexpr (i == 5 && L) { fin = (u32)(sn[0] + sn[1] + 2); PINS(fin); }
                if constexpr (i == 6 && L) { fin = (fin | uu(left)) >> 7; PIN(fin); }
                if constexpr (i == 6 && !L) { cnt += 1u; PIN(cnt); left = cnt >> 9; PIN(left); }
                if constexpr (i == 7) { PIN(hv); th = rdl(hv, 0); PINS(th); }
                if constexpr (i == 8) { per = rdl(hv, 1); PINS(per); so = th - t; PINS(so); }
                if constexpr (i == 9) { per = (int)so < 20 ? per : 0u; PINS(per); }
                if constexpr (i == 10) { th += per; PINS(th); if constexpr (L) { per = rdl(hv, 3); PINS(per); } }
                if constexpr (i == 11) { tp = t; t = lead ? tn : th; PINS(t); }
                if constexpr (i == 12) { if constexpr (L) { fin |= per; PIN(fin); } tn = nt; }
            };
            auto slot = [&](auto ic_) __attribute__((always_inline)) {
                constexpr int i = decltype(ic_)::value;
                constexpr int NL = WO_NL, I0 = 40 - 5 * NL, pc = i < I0 ? i / 5 : 8 - NL + (i - I0) % NL, oc = i < I0 ? i % 5 : (i - I0) / NL;
                if constexpr (oc == 0) asm volatile("s_waitcnt vmcnt(%1)" : "+v"(d[pc]) : "i"(10 - pc));
                if constexpr (pc * 5 + oc < 32) SMF_A(acc[oc], d[pc], wo[pc][oc]); else SMF_V(acc[oc], d[pc], wo[pc][oc]);
                SCHED();
                if constexpr (i == 0) WT(2);
                if constexpr (i >= 5 && i < 13 && i != 9) next(IC<i - 5 - (i > 9)>{});
                if constexpr (i == 20) WT(4);
                constexpr int e = i - (I0 + NL + 1), o = e < 0 ? 0 : e / 3;
                if constexpr (e >= 0 && e % 3 == 0) { CADD(f0[o], acc[o][0], acc[o][2]); if constexpr (o > 0) WAD(o - 1); }
                if constexpr (e >= 0 && e % 3 == 1) { if constexpr (o > 0) { WWR(o - 1); SCHED(); } CADD(f1[o], acc[o][1], acc[o][3]); }
                if constexpr (e >= 0 && e % 3 == 2) {
                    if constexpr (o == 0) asm volatile("v_cmp_u_f32 %0, %1, %2" : "=s"(bad) : "v"(f0[0]), "v"(f1[0]));
                    else LGK(15);
                    WSW(o);
                }
                SCHED();
            };
            asm volatile(".rept %0\n\ts_nop 0\n\t.endr" :: "i"(AL_W));
            static_for<0, 2>(slot);
            if (__builtin_expect(__builtin_amdgcn_ballot_w64(d[0].x == erl) != 0ull, 0)) {
                asm volatile("s_waitcnt vmcnt(3)" :: "v"(d[1]), "v"(d[2]), "v"(d[3]), "v"(d[4]), "v"(d[5]), "v"(d[6]), "v"(d[7]) : "memory");
                SCHED();
                *(LAS u32*)(sw + L * 4096) = 1u;
                static_for<0, 7>(next);
            } else {
            static_for<2, 40>(slot);
            WT(5);
            static_assert(WO_NL == 3, "tail");
            if (__builtin_expect(bad != 0ull, 0)) {
                bool miss = false;
#pragma unroll
                for (int i = 0; i < 8; ++i) miss |= d[i].x == PSENT;
                if (all_(!miss)) {
#pragma unroll
                    for (int i = 0; i < 8; ++i) miss |= d[i].y == PSENT || d[i].z == PSENT || d[i].w == PSENT;
                }
                *(LAS u32*)(sw + L * 4096) = all_(!miss) ? 0u : 1u;
            }
            asm volatile("v_permlane32_swap_b32 %0, %1\n\ts_nop 1\n\tv_add_f32_dpp %0, %1, %0 row_ror:8 row_mask:0xf bank_mask:0xf\n\tv_add_f32 %2, %4, %6\n\tv_add_f32 %3, %5, %7"
                         : "+v"(f0[3]), "+v"(f1[3]), "=&v"(f0[4]), "=&v"(f1[4]) : "v"(acc[4][0]), "v"(acc[4][1]), "v"(acc[4][2]), "v"(acc[4][3]));
            WWR(3); SCHED();
            asm volatile("s_nop 0\n\tv_permlane32_swap_b32 %0, %1\n\ts_nop 1\n\tv_add_f32_dpp %0, %1, %0 row_ror:8 row_mask:0xf bank_mask:0xf" : "+v"(f0[4]), "+v"(f1[4]));
            WWR(4);
            }
            WT(6);
            f32x2 a, b, c, e; u32 mq; v4u sv;
            asm volatile("s_waitcnt lgkmcnt(0)\n\ts_barrier\n\tds_read_b128 %4, %6 offset:%7+2560\n\t"
                         "ds_read_b64 %0, %5 offset:%7\n\tds_read_b64 %1, %5 offset:%7+640\n\tds_read_b64 %2, %5 offset:%7+1280\n\tds_read_b64 %3, %5 offset:%7+1920"
                         : "=&v"(a), "=&v"(b), "=&v"(c), "=&v"(e), "=&v"(sv) : "v"(xr), "v"(zero), "i"(L * 4096) : "memory");
            asm volatile("s_waitcnt lgkmcnt(4)\n\tv_or_b32 %4, %6, %7\n\tv_or3_b32 %4, %4, %8, %9\n\tv_lshl_or_b32 %5, %4, 31, %10\n\t"
                         "s_waitcnt lgkmcnt(2)\n\tv_pk_add_f32 %0, %0, %1\n\ts_waitcnt lgkmcnt(0)\n\tv_pk_add_f32 %2, %2, %3\n\ts_nop 0\n\tv_pk_add_f32 %0, %0, %2"
                         : "+v"(a), "+v"(b), "+v"(c), "+v"(e), "=&v"(pst), "=&v"(mq) : "v"(sv.x), "v"(sv.y), "v"(sv.z), "v"(sv.w), "v"(mp) : "memory");
            st4s(rs, mq, 0, pkv(a));
            SCHED();
            asm volatile("s_memrealtime %0" : "=s"(clk));
            VMW(3); SCHED();
            static_for<7, 13>(next);
            WT(7);
        }
    };
    u32 v0 = 0, v1 = 0;
    do {
        turn(IC<0>{});
        turn(IC<1>{});
        { u32 n_ = rt32(); v1 = sn[1] == 0 ? n_ : v1; v0 = sn[0] < 63 ? n_ : v0; asm volatile("" : "+v"(v0), "+v"(v1)); }
    } while (uu(fin) == 0u);
    if (sn[0] + 1 < NSEG || sn[1] + 1 < NSEG) st4s(rs, tid == 0 ? foff(F_ERR) : OOB, 0, 1u);
    if (lead) {
        const u32 m = MEAS == 0 ? 0u : MEAS == 1 ? tm0 - DW0 - tent : MEAS == 2 ? uu(v0) - tent : MEAS == 3 ? uu(v1) - tent : MEAS == 4 ? rt32() - tent : MEAS == 5 ? twts - tent : trole - tent;
        for (int n = 0; n < 1000000 && (int)(rt32() - (tent + 30000u + m)) < 0; ++n) __builtin_amdgcn_s_sleep(1);
    }
    LDUMP(w == 0, 224 + j);
}

__global__ void __launch_bounds__(256, 1) k_main(Params p) {
    lptr lds = (lptr)(u32)16 - 16;
    const u32 tent = rt32();
    unsigned long long pc; int len;
    asm volatile("s_getpc_b64 %0\n.Lk_main_here:\n\ts_mov_b32 %1, .Lk_end_here-.Lk_main_here" : "=s"(pc), "=s"(len));
    const rsrc_t rs = mk_rsrc(p.ws, W_END);
    const v2u e0 = ld8s(rs, threadIdx.x == 0 ? foff(F_EPOCH) : OOB, 0);
    const v4u code = __builtin_amdgcn_raw_buffer_load_b128(mk_rsrc((const void*)(pc & ~15ull), (u32)max(len, 0)), (((blockIdx.x >> 3) & 31u) * 256u + threadIdx.x) * 16u, 0, 0);
    CFENCE();
    auto conv_id = [](int role) {
        const int x = role >> 5, i = role & 31, no = x < 5 ? 5 : 10, og = x < 5 ? x - 3 : 2 * (x - 4) + (i >= 5), r = (x - 3) * 5 + i - no, q = r / 3;
        return x < 3 || i >= no + 5 || r >= 24 ? NCV : i < no ? ((og & 1) ? (og >> 1) * 5 + i % 5 : NCV) : r % 3 == 0 ? 20 + (q & 1) * 20 + (q >> 1) : 16 + (r % 3) * 8 + q;
    };
    int role = (int)(((u32)__builtin_amdgcn_s_getreg(20 | (3 << 11)) & 7u) * 32u + (blockIdx.x >> 3));
    Early e;
    v4u rv;
#pragma unroll 1
    for (int n = 0;; ++n) {
        conv_early(p, rs, conv_id(role), e);
        if (n) break;
        CFENCE();
        if (threadIdx.x == 0) {
            GAS unsigned long long* slots = (GAS unsigned long long*)(p.ws + W_CTRL + F_SLOT * 128);
            const unsigned long long tag = (unsigned long long)e0.y << 32 | (e0.x + 1u);
            u32 mine = (u32)role;
#pragma unroll 1
            for (int k = 0; k < 256 && __hip_atomic_exchange(slots + mine * 16, tag, __ATOMIC_RELAXED, __HIP_MEMORY_SCOPE_AGENT) == tag; ++k) mine = (mine + 1u) & 255u;
            lds_w16(lds, (v4u){mine, e0.x, e0.y, 0u});
        }
        BAR();
        rv = lds_r16(lds);
        BAR();
        if (uni((int)rv.x) == role) break;
        role = uni((int)rv.x);
    }
    const int x = role >> 5, i = role & 31, w = uni(threadIdx.x >> 6), no = x < 5 ? 5 : 10, nr = x < 7 ? 5 : 4, j = conv_id(role);
    const v2u ep = {uu(rv.y), uu(rv.z)};
    asm volatile("" :: "v"(code));
    if (x < 2) { role_head(p, lds, i >> 4, x * 16 + (i & 15), ep); __builtin_amdgcn_endpgm(); }
    if (x == 2) { role_wo(p, lds, i, ep, tent); __builtin_amdgcn_endpgm(); }
    if (i >= no + nr) { role_qkv(p, lds, (x < 5 ? (x - 3) * 22 : 44 + (x - 5) * 17) + i - no - nr, ep); __builtin_amdgcn_endpgm(); }
    const bool cv = j >= NPRE || (j >= 20 && j < 24);
    if (j < NCV) conv_stage(p, rs, lds, j, (j & 3) * 2 + (j >= NPRE), cv ? 40 : 0, ep, e);
    if (i >= no) {
        if (!cv) role_row(p, lds, (j - 24) * 4 + w, ep);
        __builtin_amdgcn_endpgm();
    }
    const int og = x < 5 ? x - 3 : 2 * (x - 4) + (i >= 5);
    role_out(p, lds, og & 1, og >> 1, i % 5, ep);
}
__global__ void k_end() { asm volatile(".Lk_end_here:"); }

extern "C" void kernel_launch(void* const* d_in, const int* in_sizes, int n_in, void* d_out, int out_size, void* d_ws, size_t ws_size,
                              hipStream_t stream) {
    Params p;
    p.x = (const u16*)d_in[0]; p.mem0 = (const u16*)d_in[1]; p.Wq = (const u16*)d_in[2]; p.Wk = (const u16*)d_in[3];
    p.Wv = (const u16*)d_in[4]; p.Wo = (const u16*)d_in[5];
    p.bk = (const u16*)d_in[8]; p.bv = (const u16*)d_in[9]; p.shift = (const u16*)d_in[10]; p.ssmk = (const u16*)d_in[11]; p.ssmD = (const u16*)d_in[12];
    p.out = (u16*)d_out;
    p.ws = (unsigned char*)(((size_t)d_ws + 2 * MB - 1) & ~(size_t)(2 * MB - 1));
    (void)hipFuncSetAttribute((const void*)k_main, hipFuncAttributeMaxDynamicSharedMemorySize, LDS_BYTES);
    hipLaunchKernelGGL(k_main, dim3(256), dim3(256), LDS_BYTES, stream, p);
}
```

```cpp
#include <hip/hip_runtime.h>
#include <hip/hip_bf16.h>

typedef unsigned short u16;
typedef unsigned int u32;
typedef u32 v2u __attribute__((ext_vector_type(2)));
typedef u32 v4u __attribute__((ext_vector_type(4)));
typedef u32 v8u __attribute__((ext_vector_type(8)));
typedef float f32x2 __attribute__((ext_vector_type(2)));
typedef float f32x4 __attribute__((ext_vector_type(4)));
typedef float f32x16 __attribute__((ext_vector_type(16)));
typedef __bf16 bf16x2 __attribute__((ext_vector_type(2)));
typedef __bf16 bf16x8 __attribute__((ext_vector_type(8)));
typedef short s16x4 __attribute__((ext_vector_type(4)));
#define LAS __attribute__((address_space(3)))
#define GAS __attribute__((address_space(1)))
typedef LAS unsigned char* lptr;
#define DEV __device__ __forceinline__

constexpr int SEQ = 2048, DM = 640, NH = 32, DH = 128, NSEG = 64, NQ = NH * DH;

constexpr u32 MB = 1u << 20;
constexpr u32 W_ORING = 0;
constexpr u32 W_MRING = MB;
constexpr u32 W_PROBE = 2 * MB - 65536;
constexpr u32 W_CTRL = 4 * MB;
constexpr u32 CTRL_BYTES = 128 * 1024;
constexpr u32 ACT = SEQ * DM * 2;
constexpr u32 W_H1 = 5 * MB, W_Y = W_H1 + ACT, W_H2 = W_Y + ACT, W_X2 = W_H2 + ACT;
constexpr u32 W_KQ = 16 * MB;
constexpr u32 W_VO = W_KQ + 64 * MB;
constexpr u32 W_P = W_VO + 64 * MB;
constexpr u32 P_ITEM = 16 * 4 * DM * 4;
constexpr u32 W_KVT = W_P + 4 * NSEG * P_ITEM;
constexpr u32 W_SP = W_KVT + ACT;
constexpr u32 W_END = W_KVT + 3 * MB;
static_assert((size_t)W_END + 2 * MB <= 256ull * MB, "workspace");
constexpr u32 F_ERR = 0, F_QKV = 16, F_O = F_QKV + 192, F_P = F_O + 64, F_H2 = F_P + 64, F_CLR = F_H2 + 32,
              F_EPOCH = F_CLR + 32, F_SLOT = F_EPOCH + 1, F_PRE = F_SLOT + 256, F_N = F_PRE + 192;
constexpr int NCV = 44, NPRE = 40;
static_assert(F_N * 128 <= CTRL_BYTES, "ctrl");
DEV u32 foff(u32 idx) { return W_CTRL + idx * 128u; }

constexpr u32 OOB = 0x80000000u;
constexpr u32 PSENT = 0x7F817F81u;
constexpr int SPIN_CAP = 40000;
constexpr int LDS_BYTES = 160 * 1024;

#define LDUMP(cond, id) do {} while (0)
#define LMX(cond, k, s_, v) do {} while (0)
#define LS3(cond, slot, idx) do {} while (0)
#define LST(idx, v) do {} while (0)
#define LS2(cond, idx) do {} while (0)
struct Params {
    const u16 *x, *mem0, *Wq, *Wk, *Wv, *Wo, *bk, *bv, *shift, *ssmk, *ssmD;
    u16* out;
    unsigned char* ws;
};

typedef __amdgpu_buffer_rsrc_t rsrc_t;
DEV rsrc_t mk_rsrc(const void* p, u32 bytes) { return __builtin_amdgcn_make_buffer_rsrc((void*)p, 0, bytes, 0x00020000); }
DEV v4u ld16s(rsrc_t r, u32 off, u32 so) { return __builtin_amdgcn_raw_buffer_load_b128(r, off, so, 16); }
DEV v2u ld8s(rsrc_t r, u32 off, u32 so) { return __builtin_amdgcn_raw_buffer_load_b64(r, off, so, 16); }
DEV v4u ldin16(rsrc_t r, u32 off, u32 so) { return __builtin_amdgcn_raw_buffer_load_b128(r, off, so, 2); }
DEV v2u ldin8(rsrc_t r, u32 off, u32 so) { return __builtin_amdgcn_raw_buffer_load_b64(r, off, so, 2); }
DEV u32 ld4(rsrc_t r, u32 off) { return __builtin_amdgcn_raw_buffer_load_b32(r, off, 0, 16); }
DEV void st16(rsrc_t r, u32 off, v4u v) { __builtin_amdgcn_raw_buffer_store_b128(v, r, off, 0, 16); }
DEV void st8s(rsrc_t r, u32 off, u32 so, v2u v) { __builtin_amdgcn_raw_buffer_store_b64(v, r, off, so, 16); }
DEV void st4s(rsrc_t r, u32 off, u32 so, u32 v) { __builtin_amdgcn_raw_buffer_store_b32(v, r, off, so, 16); }
#define DRAIN() asm volatile("s_waitcnt vmcnt(0)" ::: "memory")
#define VMW(n) asm volatile("s_waitcnt vmcnt(%0)" :: "i"(n) : "memory")
#define BAR() asm volatile("s_waitcnt lgkmcnt(0)\n\ts_barrier" ::: "memory")
#define SCHED() __builtin_amdgcn_sched_barrier(0)
#define CFENCE() asm volatile("" ::: "memory")
#define NOPS(n) asm volatile("s_nop %0" :: "i"(n))
#define PIN(x) asm volatile("" : "+v"(x))
#define PINS(x) asm volatile("" : "+s"(x))

template <int V> struct IC { static constexpr int value = V; };
template <int I, int N, class F> DEV void static_for(F&& f) { if constexpr (I < N) { f(IC<I>{}); static_for<I + 1, N>(f); } }
#define SFOR(var, n) static_for<0, n>([&](auto ic_) __attribute__((always_inline)) { constexpr int var = decltype(ic_)::value;
#define SEND });

DEV u32 pk(float a, float b) { bf16x2 v = {(__bf16)a, (__bf16)b}; return __builtin_bit_cast(u32, v); }
DEV float lo_f(u32 u) { return __builtin_bit_cast(float, u << 16); }
DEV float hi_f(u32 u) { return __builtin_bit_cast(float, u & 0xffff0000u); }
DEV f32x2 up2(u32 u) { return (f32x2){lo_f(u), hi_f(u)}; }
DEV u32 pk2(f32x2 v) { return pk(v.x, v.y); }
DEV float rbf(float a) { return (float)(__bf16)a; }
DEV v4u lds_r16(lptr p) { return *(LAS v4u*)p; }
DEV void lds_w16(lptr p, v4u v) { *(LAS v4u*)p = v; }
DEV v2u lds_tr(lptr p) { return __builtin_bit_cast(v2u, __builtin_amdgcn_ds_read_tr16_b64_v4i16((LAS s16x4*)p)); }
DEV v4u tr2(lptr p0, lptr p1) { const v2u a = lds_tr(p0), b = lds_tr(p1); return (v4u){a.x, a.y, b.x, b.y}; }
DEV v8u cat8(v4u a, v4u b) { return __builtin_shufflevector(a, b, 0, 1, 2, 3, 4, 5, 6, 7); }
DEV f32x4 mfma16(v4u a, v4u b, f32x4 c) { return __builtin_amdgcn_mfma_f32_16x16x32_bf16(__builtin_bit_cast(bf16x8, a), __builtin_bit_cast(bf16x8, b), c, 0, 0, 0); }
DEV f32x16 mfma32(v4u a, v4u b, f32x16 c) { return __builtin_amdgcn_mfma_f32_32x32x16_bf16(__builtin_bit_cast(bf16x8, a), __builtin_bit_cast(bf16x8, b), c, 0, 0, 0); }

DEV float f_(u32 u) { return __builtin_bit_cast(float, u); }
DEV u32 u_(float f) { return __builtin_bit_cast(u32, f); }
template <int CTRL> DEV float dpp(float v) { return f_((u32)__builtin_amdgcn_update_dpp(0, (int)u_(v), CTRL, 0xf, 0xf, false)); }
DEV float max16(float v) { auto r = __builtin_amdgcn_permlane16_swap(u_(v), u_(v), false, false); return fmaxf(f_(r[0]), f_(r[1])); }
DEV float max32(float v) { auto r = __builtin_amdgcn_permlane32_swap(u_(v), u_(v), false, false); return fmaxf(f_(r[0]), f_(r[1])); }
DEV float add16(float v) { auto r = __builtin_amdgcn_permlane16_swap(u_(v), u_(v), false, false); return f_(r[0]) + f_(r[1]); }
DEV float add32(float v) { auto r = __builtin_amdgcn_permlane32_swap(u_(v), u_(v), false, false); return f_(r[0]) + f_(r[1]); }
DEV float wave_sum(float v) {
    v += dpp<0xB1>(v);
    v += dpp<0x4E>(v);
    v += dpp<0x124>(v);
    v += dpp<0x128>(v);
    return add32(add16(v));
}
DEV bool all_(bool c) { return __builtin_amdgcn_ballot_w64(!c) == 0ull; }
DEV int uni(int v) { return __builtin_amdgcn_readfirstlane(v); }
DEV u32 uu(u32 v) { return (u32)__builtin_amdgcn_readfirstlane((int)v); }
DEV u32 rdl(u32 v, int l) { return (u32)__builtin_amdgcn_readlane((int)v, l); }
DEV u32 rt32() { return (u32)__builtin_amdgcn_s_memrealtime(); }

#define WSL 2
DEV void wait_flags(rsrc_t rs, u32 off, u32 want, bool& dead) {
    if (dead) return;
#pragma unroll 1
    for (int n = 0;; ++n) {
        CFENCE();
        const u32 v = ld4(rs, off);
        if (all_(off >= OOB || v >= want)) break;
        __builtin_amdgcn_s_sleep(WSL);
        if ((n & 63) == 63) { if (uni((int)ld4(rs, foff(F_ERR))) != 0) { dead = true; break; } }
        if (n > SPIN_CAP) { st4s(rs, foff(F_ERR), 0, 1u); dead = true; break; }
    }
    CFENCE();
}

DEV void pre_post(rsrc_t rs, int idx, u32 stage, v2u ep) { st8s(rs, threadIdx.x == 0 ? foff(F_PRE + idx) : OOB, 0, (v2u){ep.x + stage, ep.y}); }
DEV void pre_poll(rsrc_t rs, int first, int n, u32 stage, v2u ep) {
    const int lane = threadIdx.x & 63;
#pragma unroll 1
    for (int it = 0; it < SPIN_CAP; ++it) {
        CFENCE();
        const v2u a = ld8s(rs, foff(F_PRE + first + min(lane, n - 1)), 0), b = ld8s(rs, foff(F_PRE + first + min(lane + 64, n - 1)), 0);
        if (all_(a.y == ep.y && b.y == ep.y && max(a.x - ep.x - stage, b.x - ep.x - stage) < 8u - stage)) break;
        __builtin_amdgcn_s_sleep(2);
    }
    CFENCE();
}
DEV void pre_wait(rsrc_t rs, int first, int n, u32 stage, v2u ep) {
    if (uni(threadIdx.x >> 6) == 0) pre_poll(rs, first, n, stage, ep);
    BAR();
}
DEV u32 pol_wait(rsrc_t rs, v2u ep) {
    const int lane = threadIdx.x & 63;
    v4u a;
#pragma unroll 1
    for (int it = 0; it < SPIN_CAP; ++it) {
        CFENCE();
        a = ld16s(rs, foff(F_PRE + (lane < NPRE || lane >= 48 ? lane : 0)), 0);
        if (all_(a.y == ep.y && a.x - ep.x - 1u < 7u)) break;
        __builtin_amdgcn_s_sleep(2);
    }
    CFENCE();
    return uu(wave_sum(lane >= 48 ? (float)(int)a.z : 0.f) >= 0.f ? 1u : 0u);
}
#define PROBE_T 1000
#define NPROBE 32
DEV u32 pol_probe(const Params& p, rsrc_t rs, int id) {
    const int lane = threadIdx.x & 63;
    const u32 off0 = W_PROBE + (u32)(id >> 2) * 16384u + (u32)(id & 3) * 128u;
    const u32 tend = rt32() + PROBE_T;
    st4s(rs, lane < 32 ? off0 + (u32)lane * 512 : OOB, 0, 0u); DRAIN();
    int calm = 0;
    int d[2] = {1 << 16, 1 << 16};
#pragma unroll 1
    for (int q = 0; q < NPROBE && ((q & 1) || (calm < 6 && (int)(rt32() - tend) < 0)); ++q) {
        const u32 half = (u32)(q ^ (q >> 1) ^ id) & 1u;
        const unsigned long long t0 = __builtin_amdgcn_s_memtime();
        const u32 v = ld4(rs, lane == 0 ? off0 + half * 8192 + (u32)(q >> 1) * 512 : OOB);
        asm volatile("s_waitcnt vmcnt(0)" :: "v"(v) : "memory");
        const int dt = (int)(__builtin_amdgcn_s_memtime() - t0);
        if (half) d[1] = min(d[1], dt); else d[0] = min(d[0], dt);
        calm += dt < 1100;
    }
    const u32 xcc = (u32)__builtin_amdgcn_s_getreg(20 | (3 << 11)) & 0xfu;
    return (u32)min(max((d[0] - d[1]) * ((xcc & 4u) ? -1 : 1), -250), 250);
}
DEV f32x2 ln_stats(const f32x2 (&x)[5]) {
    const float c = f_(uu(u_(x[0].x)));
    f32x2 d[5];
#pragma unroll
    for (int i = 0; i < 5; ++i) d[i] = x[i] - c;
    const f32x2 s2 = (d[0] + d[1]) + (d[2] + d[3]) + d[4], q2 = (d[0] * d[0] + d[1] * d[1]) + (d[2] * d[2] + d[3] * d[3] + d[4] * d[4]);
    float a = s2.x + s2.y, b = q2.x + q2.y, a2, b2;
#define LN_DPP(m) "v_add_f32_dpp %0, %0, %0 " m " row_mask:0xf bank_mask:0xf\n\tv_add_f32_dpp %1, %1, %1 " m " row_mask:0xf bank_mask:0xf\n\ts_nop 0\n\t"
    asm("s_nop 1\n\t" LN_DPP("quad_perm:[1,0,3,2]") LN_DPP("quad_perm:[2,3,0,1]") LN_DPP("row_ror:4")
        "v_add_f32_dpp %0, %0, %0 row_ror:8 row_mask:0xf bank_mask:0xf\n\tv_add_f32_dpp %1, %1, %1 row_ror:8 row_mask:0xf bank_mask:0xf\n\ts_nop 1\n\t"
        "v_permlane16_swap_b32 %0, %1\n\tv_add_f32 %0, %0, %1\n\tv_mov_b32 %1, %0\n\ts_nop 1\n\tv_permlane32_swap_b32 %0, %1\n\tv_add_f32 %0, %0, %1\n\ts_nop 0\n\t"
        "v_readlane_b32 %2, %0, 0\n\tv_readlane_b32 %3, %0, 16\n\ts_nop 1"
        : "+v"(a), "+v"(b), "=s"(a2), "=s"(b2));
    const float m = a2 * (1.0f / DM), var = fmaxf(b2 * (1.0f / DM) - m * m, 0.f);
    const float sd = rbf(__builtin_amdgcn_sqrtf(rbf(rbf(var) + 1e-5f)));
    return (f32x2){rbf(c + m), __builtin_amdgcn_rcpf(sd)};
}
DEV u32 pkv(f32x2 v) { u32 d; asm("v_cvt_pk_bf16_f32 %0, %1, %2" : "=v"(d) : "v"(v.x), "v"(v.y)); return d; }
DEV void store_h(rsrc_t rs, u32 base, int row, const u32 (&hp)[5]) {
    const u32 lane = threadIdx.x & 63;
    const u32 sw = (u32)(row & 15) * 16u, o8 = (lane >> 5) * 8192u + (((lane & 31u) * 8u) ^ sw), hb = base + (u32)(row >> 5) * (32 * DM * 2) + (u32)(row & 31) * 256u;
    st8s(rs, o8, hb, (v2u){hp[0], hp[1]});
    st8s(rs, o8, hb + 16384, (v2u){hp[2], hp[3]});
    st4s(rs, (lane * 4) ^ sw, hb + 32768, hp[4]);
}

#define DMA16(ldsb, vo, so) asm volatile("s_mov_b32 m0, %0\n\ts_nop 0\n\tbuffer_load_dwordx4 %1, %2, %3 offen sc1 lds" :: "s"(ldsb), "v"(vo), "s"(srd), "s"(so) : "memory")
#define DMA16I(ldsb, lo, vo, so, go) do { u32 so_; asm volatile("s_add_u32 m0, %1, %5\n\ts_add_u32 %0, %4, %6\n\tbuffer_load_dwordx4 %2, %3, %0 offen sc1 lds" \
                         : "=&s"(so_) : "s"(ldsb), "v"(vo), "s"(srd), "s"(so), "i"(lo), "i"(go) : "memory", "scc"); } while (0)
#define DMA32(ldsb, vo, so) asm volatile("s_mov_b32 m0, %0\n\ts_nop 0\n\tbuffer_load_dwordx4 %1, %2, %3 offen sc1 lds\n\tbuffer_load_dwordx4 %1, %2, %3 offen offset:1024 sc1 lds" \
                         :: "s"(ldsb), "v"(vo), "s"(srd), "s"(so) : "memory")
#define DMAIN(sr, ldsb, vo, so) asm volatile("s_mov_b32 m0, %0\n\ts_nop 0\n\tbuffer_load_dwordx4 %1, %2, %3 offen lds" :: "s"(ldsb), "v"(vo), "s"(sr), "s"(so) : "memory")
DEV v4u mk_srd(const void* q, u32 bytes) { return (v4u){uu((u32)(size_t)q), uu((u32)((size_t)q >> 32) & 0xffffu), bytes, 0x00020000u}; }
#define DMANT(sr, ldsb, vo, so) asm volatile("s_mov_b32 m0, %0\n\ts_nop 0\n\tbuffer_load_dwordx4 %1, %2, %3 offen nt lds" :: "s"(ldsb), "v"(vo), "s"(sr), "s"(so) : "memory")
#define AHEAD(sr, vo, so) asm volatile("buffer_load_dwordx4 %0, %1, %2, %3 offen" : "+v"(junk) : "v"(vo), "s"(sr), "s"(so) : "memory")
#define MK_SRD() const v4u srd = mk_srd(p.ws, W_END)

constexpr int CV_W = 40960;
template <bool B> struct CvL {
    static constexpr int NF = B ? 5 : 3, NK = B ? 4 : 2, KP = 544 + NK * 256, CH = NF * 1024 + 4 * KP;
};
constexpr int CV_Y = 4 * CvL<false>::CH, CV_YP = 272;
static_assert(CV_Y + 32 * CV_YP <= CV_W && 2 * CvL<true>::CH <= CV_Y, "conv lds");
DEV int cv_chan(int u, int i) { return 16 * (u % 40) + 4 * i + u / 40; }
template <bool B> DEV void cv_filt(const Params& p, u32 dst, int d, int i) {
    const u32 lane = threadIdx.x & 63;
    const v4u sf = mk_srd(p.ssmk + d * SEQ, SEQ * 2);
    if (i == 0) DMANT(sf, dst, lane < 4u ? OOB : lane * 16u - 64u, 0);
    else DMANT(sf, uu(dst + i * 1024), i == CvL<B>::NF - 1 && lane >= 4u ? OOB : lane * 16u, i * 1024 - 64);
}
DEV v4u cv_kvld(rsrc_t rs, int d, int i) { return ld16s(rs, (threadIdx.x & 63) * 16u, uu(W_KVT + (u32)d * (SEQ * 2) + i * 1024)); }
template <bool B> DEV void cv_kvput(lptr F, const v4u* k) {
    const int lane = threadIdx.x & 63;
#pragma unroll
    for (int i = 0; i < CvL<B>::NK; ++i) lds_w16(F + CvL<B>::NF * 1024 + (lane & 3) * CvL<B>::KP + 512 + i * 256 + (lane >> 2) * 16, k[i]);
}
template <bool B> DEV void cv_zero(lptr F) {
    const int lane = threadIdx.x & 63;
#pragma unroll
    for (int i = 0; i < 4; ++i) lds_w16(F + (lane >> 5) * CvL<B>::CH + CvL<B>::NF * 1024 + i * CvL<B>::KP + (lane & 31) * 16, (v4u){0u, 0u, 0u, 0u});
}
#define M32V(d, a, b) asm volatile("v_mfma_f32_32x32x16_bf16 %0, %1, %2, %0" : "+v"(d) : "v"(a), "v"(b))
template <bool B, class G> DEV void cv_run(const Params& p, rsrc_t rs, lptr F, int d, u32 D, f32x4 sp, float (&y)[16], G&& gap) {
    constexpr int KP = CvL<B>::KP, FO = B ? 2048 : 0;
    const int lane = threadIdx.x & 63, n = lane & 31, hh = lane >> 5, c = lane & 3;
    lptr fa = F + 64 + (32 * hh + 16 * (c & 1) + 8 * ((lane >> 4) & 1) + 4 * (c >> 1) - (lane & 12)) * 2;
    lptr kl = F + CvL<B>::NF * 1024 + (32 + n - hh) * 16;
    v4u dg;
#pragma unroll
    for (int q = 0; q < 4; ++q) { const int k = (lane & 35) ? -1 : (n & 4) + (n >> 3); dg[q] = k == 2 * q ? D : k == 2 * q + 1 ? D << 16 : 0u; }
    v4u l[4], h[4]; v2u fc[4], fh[4];
    f32x16 acc[4];
#pragma unroll
    for (int j = 0; j < 4; ++j) { l[j] = lds_r16(kl + j * KP); if constexpr (B) h[j] = lds_r16(kl + j * KP + 512); }
#pragma unroll
    for (int j = 0; j < 2; ++j) { fc[j] = lds_tr(fa + FO - 32 * j); if constexpr (B) fh[j] = lds_tr(fa - 32 * j); }
    asm volatile("s_nop 1" : "+v"(dg));
#pragma unroll
    for (int j = 0; j < 4; ++j) asm volatile("v_mfma_f32_32x32x16_bf16 %0, %1, %2, 0" : "=&v"(acc[j]) : "v"(dg), "v"(B ? h[j] : l[j]));
    constexpr int U = B ? 2 : 4;
    kl -= 32 * U; fa -= 32;
    auto body = [&](bool first) __attribute__((always_inline)) {
#pragma unroll
        for (int z = 0; z < U; ++z) {
            const int c = 2 * (z & 1);
            const v4u f = __builtin_shufflevector(fc[c], fc[c + 1], 0, 1, 2, 3), g = __builtin_shufflevector(fh[c], fh[c + 1], 0, 1, 2, 3);
#pragma unroll
            for (int j = 0; j < 4; ++j) {
                M32V(acc[j], f, l[j]); l[j] = lds_r16(kl + j * KP + 32 * (U - z - 1));
                if (j < 2) fc[(c ^ 2) + j] = lds_tr(fa + FO + (z + 1) * 128 + 32 - 32 * j);
                if (first && j > 0) gap(5 * z + j - 1);
                SCHED();
            }
            if constexpr (B) {
#pragma unroll
                for (int j = 0; j < 4; ++j) {
                    M32V(acc[j], g, h[j]); h[j] = lds_r16(kl + j * KP + 512 + 32 * (U - z - 1));
                    if (j < 2) fh[(c ^ 2) + j] = lds_tr(fa + (z + 1) * 128 + 32 - 32 * j);
                    if (first && j > 1) gap(5 * z + j + 1);
                    SCHED();
                }
            }
        }
        kl -= 32 * U; fa += 128 * U;
    };
    if constexpr (B) body(true);
#pragma unroll 1
    for (int m = B ? 2 * U : 0; m < 32; m += 2 * U) body(false);
    asm volatile("s_nop 11" : "+v"(acc[0]), "+v"(acc[1]), "+v"(acc[2]), "+v"(acc[3]));
    const float o[3] = {acc[1][15] + acc[2][14] + acc[3][13], acc[2][15] + acc[3][14], acc[3][15]};
    if constexpr (!B) st16(rs, lane == 63 ? W_SP + (u32)d * 16u : OOB, (v4u){u_(o[0]), u_(o[1]), u_(o[2]), 0u});
#pragma unroll
    for (int i = 0; i < 16; ++i) {
        float v = acc[0][i];
#pragma unroll
        for (int j = 1; j < 4; ++j) if (j <= i) v += acc[j][i - j];
        if (i < 3) { const float e = f_((u32)__builtin_amdgcn_ds_bpermute((hh ? lane - 32 : lane + 31) * 4, (int)u_(o[i]))); v += lane == 0 ? sp[i] : e; }
        y[i] = v;
    }
}
DEV void cv_put(lptr F, int pr, const float (&y0)[16], const float (&y1)[16]) {
    const int lane = threadIdx.x & 63;
#pragma unroll
    for (int i = 0; i < 16; ++i) *(LAS u32*)(F + CV_Y + (lane & 31) * CV_YP + (lane >> 5) * 128 + i * 8 + pr * 4) = pk(y0[i], y1[i]);
}
DEV void cv_out(rsrc_t rs, lptr F, int u, int blk0) {
    const int lane = threadIdx.x & 63;
    lptr q = F + CV_Y + ((lane >> 2) & 3) * CV_YP + ((lane >> 4) * 4 + (lane & 3)) * 8;
    const u32 yo = W_Y + (u32)(((lane >> 2) * 8 + blk0) * DM + 4 * u + (lane & 3)) * 16u;
#pragma unroll
    for (int i = 0; i < 8; ++i) { lptr a = q + (i >> 1) * (8 * CV_YP) + (i & 1) * 128; st16(rs, yo + (u32)((i & 1) * 128 + (i >> 1)) * (DM * 16), tr2(a, a + 4 * CV_YP)); }
}
struct KvIn { v2u k[19], v[16]; v4u s[2]; };
template <class F> DEV void kvt_load(const Params& p, int tile, KvIn& in, F&& mid) {
    const int lane = threadIdx.x & 63;
    const rsrc_t rk = mk_rsrc(p.bk, ACT), rv = mk_rsrc(p.bv, ACT), rh = mk_rsrc(p.shift, DM * 8);
    const int row = (tile / 10) * 64 + (lane >> 4) * 16, col = (tile % 10) * 64 + (lane & 15) * 4;
    const u32 vo = (u32)(row * DM + col) * 2u, vp = row > 0 ? vo - 3 * (DM * 2) : OOB;
#pragma unroll
    for (int i = 0; i < 2; ++i) in.s[i] = ldin16(rh, (u32)col * 8u + i * 16, 0);
#pragma unroll
    for (int i = 0; i < 19; ++i) { if (i == 7) mid(); in.k[i] = i < 3 ? ldin8(rk, vp, i * (DM * 2)) : ldin8(rk, vo, (i - 3) * (DM * 2)); if (i >= 3) in.v[i - 3] = ldin8(rv, vo, (i - 3) * (DM * 2)); }
}
DEV void kvt_make(rsrc_t rs, lptr xt, int tile, const KvIn& in) {
    const int lane = threadIdx.x & 63;
#pragma unroll
    for (int c = 0; c < 2; ++c) {
        const v4u sv = in.s[c];
        const f32x2 sh[4] = {{lo_f(sv.x), lo_f(sv.z)}, {hi_f(sv.x), hi_f(sv.z)}, {lo_f(sv.y), lo_f(sv.w)}, {hi_f(sv.y), hi_f(sv.w)}};
        f32x2 kf[19];
#pragma unroll
        for (int i = 0; i < 19; ++i) kf[i] = up2(in.k[i][c]);
        u32 kv[16];
#pragma unroll
        for (int i = 0; i < 16; ++i) kv[i] = pk2(((kf[i] * sh[3] + kf[i + 1] * sh[2]) + (kf[i + 2] * sh[1] + kf[i + 3] * sh[0])) * up2(in.v[i][c]));
#pragma unroll
        for (int q = 0; q < 8; ++q) {
            const u32 sel = (q & 1) ? 0x07060302u : 0x05040100u; const u32* z = kv + (q >> 1);
            *(LAS v2u*)(xt + ((2 * c + (q & 1)) * 16 + (lane & 15)) * 144 + (lane >> 5) * 64 + (q >> 1) * 16 + ((lane >> 4) & 1) * 8) =
                (v2u){__builtin_amdgcn_perm(z[4], z[0], sel), __builtin_amdgcn_perm(z[12], z[8], sel)};
        }
    }
    const u32 o = W_KVT + (u32)(((tile % 10) * 64 + 4 * (lane >> 3)) * SEQ + (tile / 10) * 64 + (lane & 7) * 8) * 2u;
#pragma unroll
    for (int i = 0; i < 8; ++i) st16(rs, o + (u32)(32 * (i & 1) + (i >> 1)) * (SEQ * 2), lds_r16(xt + (8 * i + (lane >> 3)) * 144 + (lane & 7) * 16));
}
struct RowX { v2u a, b; u32 c; };
DEV void ln_row(rsrc_t rs, int row, const RowX& r) {
    const f32x2 x[5] = {up2(r.a.x), up2(r.a.y), up2(r.b.x), up2(r.b.y), up2(r.c)};
    const f32x2 st = ln_stats(x);
    u32 hp[5];
#pragma unroll
    for (int q = 0; q < 5; ++q) hp[q] = pk2(up2(pkv(x[q] - st.x)) * st.y);
    store_h(rs, W_H1, row, hp);
}
DEV v4u mem0_load(const Params& p, int j) {
    const u32 gt = (u32)(j * 256 + threadIdx.x) * 16u, n = ((gt >> 14) << 9) + ((gt & 8191u) >> 4);
    return ldin16(mk_rsrc(p.mem0, 8 * DM * 2), n < 640u ? ((2 * ((n >> 2) & 3) + (n & 1)) * DM + (n >> 4) * 16 + (n & 2) * 4) * 2u : OOB, 0);
}
DEV void pre_rings(rsrc_t rs, int j, v4u m) {
    const u32 gi = (u32)(j * 256 + threadIdx.x), gt = gi * 16u, R = NPRE * 4096, n = ((gt >> 14) << 9) + ((gt & 8191u) >> 4);
#pragma unroll
    for (int i = 0; i < 4; ++i) m[i] = n >= 640u ? PSENT : m[i] == PSENT ? 0x7FC17FC1u : m[i];
    st4s(rs, gi - 36u * 256u < F_CLR ? foff(gi - 36u * 256u) : OOB, 0, 0u);
#pragma unroll 1
    for (u32 o = gt; o < MB; o += R) st16(rs, W_ORING + o, (v4u){PSENT, PSENT, PSENT, PSENT});
    const u32 mo = gt < 4 * 32768 + 16384 ? W_MRING + gt : OOB;
    st16(rs, mo, m);
    st16(rs, mo + MB / 4, m);
    static_assert(NPRE * 4096 >= 4 * 32768 + 16384, "memory ring");
}
constexpr int KT0 = NPRE * 4, LN0 = NPRE * 8;
struct Early { RowX r[2]; v4u m; u32 vote; };
DEV void conv_early(const Params& p, rsrc_t rs, int j, Early& e) {
    const int lane = threadIdx.x & 63, w = uni(threadIdx.x >> 6), u = j * 4 + w;
    if (j < NPRE) {
        const rsrc_t rx = mk_rsrc(p.x, ACT);
#pragma unroll
        for (int i = 0; i < 2; ++i) {
            const u32 so = uu((u + i * KT0) * (DM * 2));
            e.r[i].a = __builtin_amdgcn_raw_buffer_load_b64(rx, lane * 8u, so, 0); e.r[i].b = __builtin_amdgcn_raw_buffer_load_b64(rx, lane * 8u + 512u, so, 0);
            e.r[i].c = __builtin_amdgcn_raw_buffer_load_b32(rx, lane * 4u + 1024u, so, 0);
        }
        e.m = mem0_load(p, j);
    } else if (j < NCV && w > 0) e.vote = pol_probe(p, rs, u - NPRE * 4);
}
DEV void conv_stage(const Params& p, rsrc_t rs, lptr lds, int j, int g0, int ge, v2u ep, Early& e) {
    const int tid = threadIdx.x, lane = tid & 63, w = uni(tid >> 6), u = j * 4 + w;
    lptr F = lds + w * CV_W;
    const u32 fb = uu(w * CV_W);
    const rsrc_t rd = mk_rsrc(p.ssmD, DM * 2);
    float y0[16], y1[16];
    KvIn in;
    if (j < NPRE) {
        constexpr int CH = CvL<false>::CH;
        if (w == 1) LST(1500 + j, rt32());
        pre_rings(rs, j, e.m);
        ln_row(rs, u, e.r[0]); ln_row(rs, u + KT0, e.r[1]);
        if (w == 1) LST(1550 + j, rt32());
        CFENCE();
        kvt_load(p, u, in, [&] { VMW(13); BAR(); pre_post(rs, j, 1u, ep); if (w == 0) LST(1450 + j, rt32()); });
        if (j == 20) LST(260, rt32());
        const u32 D = __builtin_amdgcn_raw_buffer_load_b16(rd, (u32)cv_chan(u, lane & 3) * 2u, 0, 0);
#pragma unroll 1
        for (int i = 0; i < 4; ++i) { const int d = cv_chan(u, i); cv_filt<false>(p, fb + i * CH, d, 0); cv_filt<false>(p, fb + i * CH, d, 1); cv_filt<false>(p, fb + i * CH, d, 2); }
        kvt_make(rs, F + CV_Y, u, in);
        cv_zero<false>(F); cv_zero<false>(F + 2 * CH);
        DRAIN(); BAR();
        pre_post(rs, j, 2u, ep);
        if (j == 20) LST(262, rt32());
        pre_wait(rs, 0, NPRE, 2u, ep);
        if (j == 20) LST(281, rt32());
        {
            v4u k[8];
#pragma unroll
            for (int i = 0; i < 8; ++i) k[i] = cv_kvld(rs, cv_chan(u, i >> 1), i & 1);
#pragma unroll
            for (int i = 0; i < 4; ++i) cv_kvput<false>(F + i * CH, k + 2 * i);
        }
        if (j == 20) LST(286, rt32());
#pragma unroll 1
        for (int i = 0; i < 4; i += 2) {
            cv_run<false>(p, rs, F + i * CH, cv_chan(u, i), rdl(D, i), f32x4{}, y0, [](int) {});
            cv_run<false>(p, rs, F + (i + 1) * CH, cv_chan(u, i + 1), rdl(D, i + 1), f32x4{}, y1, [](int) {});
            cv_put(F, i >> 1, y0, y1);
        }
        if (j == 20) LST(287, rt32());
        cv_out(rs, F, u, 0);
        if (j == 20) LST(263, rt32());
    } else {
        st16(rs, lane == 0 ? foff(F_PRE + 48 + u - NPRE * 4) : OOB, (v4u){ep.x + 1u, ep.y, w > 0 ? e.vote : 0u, 0u});
#pragma unroll 1
        for (int t = u + 128; t < 320; t += 16) { kvt_load(p, t, in, [] {}); kvt_make(rs, F, t, in); }
    }
    DRAIN(); BAR();
    if (g0 < ge) {
        constexpr int CH = CvL<true>::CH;
        pre_post(rs, j, 3u, ep);
        cv_zero<true>(F);
        if (w == 0) { pre_poll(rs, 64, 32, 3u, ep); pre_poll(rs, 0, NCV, 3u, ep); }
        BAR();
        v4u k[4];
        auto fetch = [&](u32 dst, int d, int i) { if (i < 5) cv_filt<true>(p, dst, d, i); else if (i < 9) k[i - 5] = cv_kvld(rs, d, i - 5); };
#pragma unroll
        for (int i = 0; i < 9; ++i) fetch(fb, cv_chan(g0 * 4 + w, 0), i);
        const u32 ch = (u32)cv_chan(min((g0 + 8 * (lane >> 2)) * 4 + w, 159), lane & 3);
        u32 D = __builtin_amdgcn_raw_buffer_load_b16(rd, ch * 2u, 0, 0);
        v4u sp = ld16s(rs, W_SP + ch * 16u, 0);
        asm volatile("" : "+v"(D), "+v"(sp));
#pragma unroll 1
        for (int g = g0, n = 0; g < ge; g += 8) {
            const int un = g * 4 + w;
            if (g == 16) { LST(282, rt32()); LST(288, __builtin_amdgcn_s_memtime()); }
#pragma unroll 1
            for (int i = 0; i < 4; i += 2, n += 2) {
                const int d1 = (int)rdl(ch, n + 1), d2 = (int)rdl(ch, n + 2);
                cv_kvput<true>(F, k);
                cv_run<true>(p, rs, F, 0, rdl(D, n), (f32x4){f_(rdl(sp.x, n)), f_(rdl(sp.y, n)), f_(rdl(sp.z, n)), 0.f}, y0, [&](int q) { fetch(fb + CH, d1, q); });
                cv_kvput<true>(F + CH, k);
                cv_run<true>(p, rs, F + CH, 326, rdl(D, n + 1), (f32x4){f_(rdl(sp.x, n + 1)), f_(rdl(sp.y, n + 1)), f_(rdl(sp.z, n + 1)), 0.f}, y1, [&](int q) { fetch(fb, d2, q); });
                cv_put(F, i >> 1, y0, y1);
            }
            cv_out(rs, F, un, 4);
            if (g == 16) { LST(283, rt32()); LST(289, __builtin_amdgcn_s_memtime()); }
        }
        DRAIN(); BAR();
    }
    pre_post(rs, j, 5u, ep);
    if (j == 20) LST(284, rt32());
    if (j == 3) LST(285, rt32());
}

#define M32A(first, d, a, b) do { if (first) asm volatile("v_mfma_f32_32x32x16_bf16 %0, %1, %2, 0" : "=&v"(d) : "a"(a), "v"(b)); \
                                  else asm volatile("v_mfma_f32_32x32x16_bf16 %0, %1, %2, %0" : "+v"(d) : "a"(a), "v"(b)); } while (0)
#define M32B(first, d, a, b) do { if (first) asm volatile("v_mfma_f32_32x32x16_bf16 %0, %1, %2, 0" : "=&v"(d) : "v"(a), "a"(b)); \
                                  else asm volatile("v_mfma_f32_32x32x16_bf16 %0, %1, %2, %0" : "+v"(d) : "v"(a), "a"(b)); } while (0)
#define VCVT(d, a, b) asm volatile("v_cvt_pk_bf16_f32 %0, %1, %2" : "=v"(d) : "v"(a), "v"(b))
#define VADD(d, a, b) asm volatile("v_add_f32 %0, %1, %2" : "=v"(d) : "v"(a), "v"(b))
#define LD4A(d, vo, so) asm volatile("buffer_load_dword %0, %1, %2, %3 offen sc1" : "=v"(d) : "v"(vo), "s"(srd), "s"(so) : "memory")
DEV u32 wst_src(int lane, int ld) { return (u32)((lane >> 4) * ld + 8 * ((lane & 15) ^ ((lane >> 4) << 2))) * 2u; }
DEV u32 wst_rd(int lane, int w) { const int q4 = (lane >> 2) & 3; return (u32)(q4 * 256 + ((w ^ q4) * 64) + ((lane >> 4) & 1) * 32 + (lane & 3) * 8); }

constexpr int QK_SEL = 81920;
#define QK_LEAD 12
#define QK_SMP 29
#define QK_YB 26
#define QK_W0 2
DEV void role_qkv(const Params& p, lptr lds, int c, v2u ep) {
    const int tid = threadIdx.x, lane = tid & 63, w = uni(tid >> 6), hh = lane >> 5, r32 = lane & 31;
    const int m = c >> 5, h = c & 31;
    const rsrc_t rs = mk_rsrc(p.ws, W_END);
    v4u wt[40];
    {
        const v4u sw = mk_srd(m == 0 ? p.Wq : m == 1 ? p.Wk : p.Wv, DM * NQ * 2), sx = mk_srd(p.x, ACT);
        const u32 vo = wst_src(lane, NQ) + (u32)h * (DH * 2), x8 = (u32)lane * 8u, x4 = (u32)lane * 4u;
        RowX xr[5];
        auto wfill = [&](int i0, int n) {
#pragma unroll 1
            for (int i = i0; i < i0 + n; ++i) DMAIN(sw, uu((4 * i + w) * 1024), vo, uu((16 * i + 4 * w) * (NQ * 2)));
        };
        wfill(0, QK_W0);
#pragma unroll
        for (int i = 0; i < 5; ++i)
            asm volatile("buffer_load_dwordx2 %0, %3, %5, %6 offen\n\tbuffer_load_dwordx2 %1, %3, %5, %6 offen offset:512\n\tbuffer_load_dword %2, %4, %5, %6 offen offset:1024"
                         : "=&v"(xr[i].a), "=&v"(xr[i].b), "=&v"(xr[i].c) : "v"(x8), "v"(x4), "s"(sx), "s"(uu((LN0 + 4 * c + w + 384 * i) * (DM * 2))) : "memory");
        wfill(QK_W0, 10 - QK_W0);
        if (c == 3) LST(275, rt32());
        VMW(10 - QK_W0);
        if (c == 3) LST(276, rt32());
#pragma unroll
        for (int i = 0; i < 5; ++i) {
            asm volatile("" : "+v"(xr[i].a), "+v"(xr[i].b), "+v"(xr[i].c));
            const int row = LN0 + 4 * c + w + 384 * i;
            if (i < 4 || row < SEQ) ln_row(rs, row, xr[i]);
            wfill(10 + 6 * i, 6);
        }
        if (c == 3) LST(264, rt32());
        lptr tb = lds + hh * 2048 + wst_rd(lane, w);
        SFOR(k, 40)
            if constexpr (k == 0) { VMW(15); BAR(); }
            if constexpr (k == 28) {
                if (w == 0) pre_poll(rs, 0, NPRE, 1u, ep);
                DRAIN(); BAR();
                pre_post(rs, 96 + c, 1u, ep);
            }
            lptr q = tb + (k >> 3) * 32768 + (k & 7) * 4096; wt[k] = tr2(q, q + 1024); asm volatile("" : "+a"(wt[k]));
        SEND
    }
    if (c == 3) LST(268, rt32());
    if (w == 0) LST(1350 + c, rt32());
    bool dead = false;
    int n0, n1;
    const u32 t16 = (u32)(w * 2048 + lane * 16), wb = uu(w * 2048);
    u32 ar[16];
#pragma unroll
    for (int j = 0; j < 16; ++j) ar[j] = (u32)(r32 * 256 + ((2 * (j & 7) + hh) ^ (r32 & 15)) * 16 + (j >> 3) * 49152);
    MK_SRD();
#define XRD(y, ks) lds_r16(lds + ar[((ks) & 7) + ((ks) >= 24 ? 8 : 0)] + ((ks) >> 3) % 3 * 16384 + (y) * 8192)
#define QM0(y, b) asm volatile("s_add_u32 m0, %0, %1" :: "s"(wb), "i"((b) * 16384 + (y) * 8192) : "scc", "memory")
#define QDMA(i, vo) asm volatile("buffer_load_dwordx4 %0, %1, %2 offen offset:%3 sc1 lds" :: "v"(vo), "s"(srd), "s"(((i) >> 1) * 8192), "i"(((i) & 1) * 1024) : "memory")
    const u32 h2flag = (lane < 16 && w == 0) ? foff(F_H2 + lane) : OOB;
    const u32 ol = (m == 2 ? (u32)(2 * w + (r32 >> 4)) * 1024u : (u32)((r32 >> 4) * 4 + w) * 1024u) + (u32)(hh * 16 + (r32 & 15)) * 16u
                 + (m == 2 ? W_VO : m == 1 ? W_KQ : W_KQ + 8192u) + (u32)h * 16384u;
    const u32 fo = tid < 2 ? foff(F_QKV + tid * 96 + c) : OOB;
    lptr selw = lds + QK_SEL + (w == 0 ? 0 : 64), selr = lds + QK_SEL;
    PIN(selw);
    static_assert(QK_LEAD > 8 && QK_LEAD % 2 == 0 && LN0 == 10 * 32, "X = 8 comes up, before any item of layer 1");
    static_assert(W_H2 - W_H1 == 2 * 64 * (32 * DM * 2), "h");
    auto run = [&](auto isv) __attribute__((always_inline)) {
        constexpr bool V = decltype(isv)::value;
        constexpr int YB = QK_YB;
        f32x16 ax, ay = {};
        v4u x[8], pp, pq;
        u32 xv = t16 + W_H1, ox = ol, oy = OOB, nn = 1, lt = 0, fcnt = 0, fa = OOB;
        asm volatile("" : "+v"(lt), "+v"(nn));
#pragma unroll
        for (int z = 0; z < 5; ++z) x[z] = XRD(1, 40 - YB + z);
#define QPK(r_, a1, a_, q_) do { if constexpr (V) VCVT(r_[q_], a1[4 * (a_) + 8 * ((q_) >> 1) + 2 * ((q_) & 1)], a1[4 * (a_) + 8 * ((q_) >> 1) + 2 * ((q_) & 1) + 1]); \
                                 else VCVT(r_[q_], a1[4 * (a_) + (q_)], a1[4 * (a_) + (q_) + 8]); } while (0)
#define QPK4(r_, a1, a_) do { QPK(r_, a1, a_, 0); QPK(r_, a1, a_, 1); QPK(r_, a1, a_, 2); QPK(r_, a1, a_, 3); } while (0)
        for (;;) {
            u32 ls = uu(lt), yv, fv, nx, need;
            if (ls != 0u && uu(nn) < 256u) pre_wait(rs, 96, 96, 1u, ep);
            else if (ls != 0u) {
                n1 = (int)(uu(nn) >> 8);
                CFENCE();
                const u32 hf = h2flag + (u32)(~n1 & 1) * 2048u;
                u32 f2 = ld4(rs, hf);
                asm volatile("s_waitcnt vmcnt(0)" : "+v"(f2) :: "memory");
                *(LAS u32*)(selw + 16) = (u32)__builtin_amdgcn_ballot_w64(f2 < (u32)n1) & 0xFFFFu;
                BAR();
                st4s(rs, fa, 0, fcnt);
                fa = OOB;
                if (n1 > NSEG) { LDUMP(w == 0, 64 + c); __builtin_amdgcn_endpgm(); }
                if (uu(*(LAS u32*)(selr + 16)) != 0u) {
                    if (w == 0) wait_flags(rs, hf, (u32)n1, dead);
                    BAR();
                }
            }
#define QT(k) do {} while (0)
            QT(0);
            SCHED();
            QM0(0, 0); NOPS(0); QDMA(0, xv); QDMA(1, xv); QM0(0, 1);
            SFOR(z, 80)
                constexpr int ks = (z + 40 - YB) % 40, zx = z - YB;
                constexpr auto upto = [](int q) { const int d = (q - YB + 49) % 40; return q + 6 + (d == 0 || d == 2 ? 1 : d == 1 ? 2 : 0); };
                if constexpr (zx >= 0 && zx < 40) { if constexpr (V) M32B(ks == 0, ax, x[z & 7], wt[ks]); else M32A(ks == 0, ax, wt[ks], x[z & 7]); }
                else { if constexpr (V) M32B(ks == 0, ay, x[z & 7], wt[ks]); else M32A(ks == 0, ay, wt[ks], x[z & 7]); }
                if constexpr (z == 78) lt = *(LAS u32*)selr;
#pragma unroll
                for (int q = upto(z - 1); q < upto(z); ++q) x[q & 7] = XRD(q % 80 < YB || q % 80 >= YB + 40, (q + 40 - YB) % 40);
                SCHED();
                (void)&srd; (void)&wb;
                if constexpr (z < 2) QDMA(z + 2, xv);
                if constexpr (z >= 2 && z < 14 && (z & 3) == 2) QM0(0, (z + 6) >> 2);
                if constexpr (z >= 3 && z < 15 && (z & 1)) QDMA((z + 5) >> 1, xv);
                if constexpr (z == 4) { n0 = uni((int)nn); n1 = n0 >> 8; n0 &= 255; PINS(n0); PINS(n1); }
                if constexpr (z == 8) { nx = n0 < NSEG ? W_H1 + (u32)n0 * (32 * DM * 2) : OOB; PINS(nx); }
                if constexpr (z == 12) { yv = t16 + nx; PIN(yv); }
                if constexpr (zx == -6) { QT(1); asm volatile("s_waitcnt vmcnt(6) lgkmcnt(0)\n\ts_barrier" ::: "memory"); QT(2); }
                if constexpr (zx == -5) st4s(rs, fa, 0, fcnt);
                if constexpr (zx == -4 || zx == 0 || zx == 4) QM0(1, (zx + 4) >> 2);
                if constexpr (zx >= -3 && zx < 8 && (zx & 1)) QDMA((zx + 3) >> 1, yv);
                if constexpr (zx == 2) QPK4(pp, ay, 0);
                if constexpr (zx == 6) QPK4(pq, ay, 1);
                if constexpr (zx == 8) st16(rs, oy, pp);
                if constexpr (zx == 9) st16(rs, oy + 512, pq);
                if constexpr (zx == 10) { QT(3); asm volatile("s_waitcnt vmcnt(9)\n\ts_barrier" ::: "memory"); QT(4); }
                if constexpr (zx == 11 || zx == 15) QM0(1, (zx + 1) >> 2);
                if constexpr (zx >= 12 && zx < 20 && (zx & 1) == 0) QDMA(zx >> 1, yv);
                if constexpr (zx == 21) { fcnt = lane == 0 ? (u32)n0 : (u32)n1; fa = fo; asm volatile("" : "+v"(fcnt), "+v"(fa)); }
                if constexpr (zx == 22) { oy = (ol + (u32)n0 * (NH * 16384u)) | (yv & OOB); PIN(oy); }
                if constexpr (zx == 23) { n0 = min(n0 + 1, NSEG); PINS(n0); nx = (u32)min(NSEG, n1 + QK_LEAD); PINS(nx); }
                if constexpr (zx == 24)
                    asm volatile("s_cmp_ge_u32 %1, %0\n\ts_cselect_b32 %0, %4, %1\n\ts_addc_u32 %2, %2, 0\n\ts_cmp_lt_u32 %0, 64\n\ts_cselect_b32 %3, 0, %2"
                                 : "+s"(nx), "+s"(n0), "+s"(n1), "=&s"(need) : "s"(n1 + 64) : "scc");
                if constexpr (zx == 25) { n0 += 1 - (int)min(nx >> 6, 1u); PINS(n0); }
                if constexpr (zx == 26) { nn = (u32)(n0 | n1 << 8); PIN(nn); }
                if constexpr (zx == 27) { ls = W_H1 + (nx + (nx & 64u)) * (32 * DM * 2); PINS(ls); }
                if constexpr (zx == 28) { xv = t16 + ls; PIN(xv); need = nx == 8u ? 255u : need; PINS(need); }
                if constexpr (zx == 34) { asm volatile("s_waitcnt vmcnt(%0) lgkmcnt(0)\n\ts_barrier" :: "i"(QK_SMP < 34 ? 7 : 6) : "memory"); QT(5); }
                if constexpr (zx == QK_SMP) { const u32 po = (nx & 1u) * 2048u; LD4A(fv, h2flag, po); }
                if constexpr (zx == 42) QPK4(pp, ax, 0);
                if constexpr (zx == 43) st16(rs, ox, pp);
                if constexpr (zx == 44) QPK4(pq, ax, 1);
                if constexpr (zx == 45) st16(rs, ox + 512, pq);
                if constexpr (zx == 47) { ox = ol + nx * (NH * 16384u); PIN(ox); }
                if constexpr (z == 74) { asm volatile("s_waitcnt vmcnt(2)" : "+v"(fv) :: "memory"); QT(6); ls = (u32)__builtin_amdgcn_ballot_w64(fv < need) & 0xFFFFu; PINS(ls); }
                if constexpr (z == 75) { lt = ls; PIN(lt); *(LAS u32*)selw = lt; }
                if constexpr (z == 77) asm volatile("s_waitcnt lgkmcnt(2)\n\ts_barrier" ::: "memory");
                SCHED();
            SEND
        }
    };
    if (m == 2) run(IC<1>{}); else run(IC<0>{});
}

DEV void role_out(const Params& p, lptr lds, int L, int kq, int ng, v2u ep) {
    const int tid = threadIdx.x, lane = tid & 63, w = uni(tid >> 6), hh = lane >> 5, r32 = lane & 31;
    const rsrc_t rs = mk_rsrc(p.ws, W_END);
    v4u wo[64];
    bool dead = false;
    {
        const v4u sw = mk_srd(p.Wo, DM * NQ * 2);
        const u32 vo = wst_src(lane, DM) + (u32)ng * 256u;
        auto fill = [&](int b) {
#pragma unroll 1
            for (int i = 0; i < 8; ++i) DMAIN(sw, uu((b % 5) * 32768 + (4 * i + w) * 1024), vo, uu((kq * 1024 + b * 128 + 16 * i + 4 * w) * (DM * 2)));
        };
#pragma unroll 1
        for (int b = 0; b < 5; ++b) fill(b);
        lptr tb = lds + hh * 1024 + wst_rd(lane, w);
        SFOR(b, 8)
            VMW(b < 4 ? 32 : 56 - 8 * b); BAR();
            lptr q = tb + (b % 5) * 32768;
#pragma unroll
            for (int k = 0; k < 8; ++k) { lptr a = q + (k >> 1) * 8192 + (k & 1) * 2048; wo[8 * b + k] = tr2(a, a + 4096); if (b < 7) asm volatile("" : "+a"(wo[8 * b + k])); else PIN(wo[8 * b + k]); }
            if constexpr (b < 3) { BAR(); fill(b + 5); }
        SEND
    }
    pre_wait(rs, 0, NPRE, 1u, ep);
    if (kq == 0 && ng == 0) LST(272 + L, rt32());
    MK_SRD();
    const u32 ofl = lane < 8 ? foff(F_O + L * 32 + kq * 8 + lane) : OOB, l16 = (u32)lane * 16u;
    const u32 fo = (tid == 0) ? foff(F_P + L * 32 + kq * 5 + ng) : OOB;
#define OM0(dst, b) asm volatile("s_add_u32 m0, %0, %1" :: "s"(dst), "i"((b) * 8192) : "scc", "memory")
#define ODMA(i, vo) asm volatile("buffer_load_dwordx4 %0, %1, %2 offen offset:%3 sc1 lds" :: "v"(vo), "s"(srd), "s"(((i) >> 1) * 16384), "i"(((i) & 1) * 1024) : "memory")
    auto first = [&](u32 dst, u32 vo) {
#pragma unroll
        for (int b = 0; b < 5; ++b) { OM0(dst, b); NOPS(0); ODMA(2 * b, vo); ODMA(2 * b + 1, vo); }
    };
    u32 pa = W_P + (u32)(L * NSEG) * P_ITEM + (u32)(2 * hh * 4 + kq) * (DM * 4) + (u32)(ng * 128 + w * 32 + r32) * 4u, fv = 0;
    u32 ov = W_VO + 8192u + (u32)(L * NSEG * NH + kq * 8) * 16384u + (u32)w * 2048u + l16;
    bool early = false;
    for (int s = 0; s < NSEG; ++s) {
        const u32 dst = uu((s & 1) * 65536 + w * 2048), ndst = dst ^ 65536u, need = s + 1 < NSEG ? (u32)s + 2u : 255u;
        lptr A = lds + (s & 1) * 65536 + l16;
        if (!early) {
            if (!all_(ofl == OOB || fv >= (u32)s + 1)) wait_flags(rs, ofl, (u32)s + 1, dead);
            LS3(L == 0 && w == 1, 1, kq * 5 + ng);
            first(dst, ov);
        }
#define OT(k) do {} while (0)
        OM0(dst, 5);
        asm volatile("s_waitcnt vmcnt(8)\n\ts_barrier" ::: "memory");
        OT(0);
        v4u o[64];
#pragma unroll
        for (int k = 0; k < 4; ++k) o[k] = lds_r16(A + k * 1024);
        f32x16 acc;
        u32 far, nv;
        SCHED();
        SFOR(k, 64)
            if constexpr (k < 56) M32B(k == 0, acc, o[k], wo[k]); else M32V(acc, o[k], wo[k]);
            if constexpr (k < 60) o[k + 4] = lds_r16(A + (k + 4) * 1024);
            SCHED();
            (void)&srd; (void)&dst; (void)&ndst;
            if constexpr (k < 12 && (k & 1) == 0) ODMA(10 + k / 2, ov);
            if constexpr (k == 3) asm volatile("s_waitcnt vmcnt(6)\n\ts_barrier" ::: "memory");
            if constexpr (k == 3 || k == 7) OM0(dst, (k + 21) >> 2);
            if constexpr (k == 12) { ov += NH * 16384u; PIN(ov); }
            if constexpr (k == 19) asm volatile("s_waitcnt vmcnt(6)\n\ts_barrier" ::: "memory");
            if constexpr (k == 20) LD4A(fv, ofl, 0);
            if constexpr (k == 35) asm volatile("s_waitcnt vmcnt(1)\n\ts_barrier" ::: "memory");
            if constexpr (k == 40) { asm volatile("s_waitcnt vmcnt(0)" : "+v"(fv) :: "memory"); far = (u32)__builtin_amdgcn_ballot_w64(fv < need) & 255u; PINS(far); }
            if constexpr (k == 41) { nv = far ? OOB : ov; PIN(nv); }
            if constexpr (k >= 42 && k < 62 && (k & 3) == 2) OM0(ndst, (k - 42) >> 2);
            if constexpr (k >= 43 && k < 63 && (k & 1)) ODMA((k - 43) >> 1, nv);
            if constexpr (k == 48) LD4A(fv, ofl, 0);
            if constexpr (k == 4) OT(1);
            if constexpr (k == 20) OT(2);
            if constexpr (k == 36) OT(3);
            if constexpr (k == 41) OT(4);
            SCHED();
        SEND
        OT(5);
        asm volatile("s_nop 11" : "+v"(acc), "+v"(pa));
#pragma unroll
        for (int i = 0; i < 8; ++i) st4s(rs, pa, (u32)((4 * (i >> 1) + (i & 1)) * (4 * DM * 4)), pk(acc[2 * i], acc[2 * i + 1]));
        asm volatile("s_waitcnt vmcnt(15)" : "+v"(fv) :: "memory");
        OT(6);
        LS3(L == 0 && w == 1, 2, kq * 5 + ng);
        early = far == 0u || all_(ofl == OOB || fv >= need);
        if (far != 0u && early) { first(ndst, ov); VMW(10); }
        else { fv = ld4(rs, ofl); asm volatile("s_waitcnt vmcnt(0)" : "+v"(fv) :: "memory"); }
        BAR();
        st4s(rs, fo, 0, (u32)s + 1);
        pa += P_ITEM;
        LS2(L == 0 && w == 1, 160 + kq * 5 + ng);
    }
    LDUMP(L == 0 && w == 1, kq * 5 + ng);
}

DEV void ln_stats2(const f32x2 (&x)[10], f32x2& mean, f32x2& rstd) {
    const f32x2 c = {f_(uu(u_(x[0].x))), f_(uu(u_(x[0].y)))};
    f32x2 d[10];
#pragma unroll
    for (int i = 0; i < 10; ++i) d[i] = x[i] - c;
    f32x2 a = ((d[0] + d[1]) + (d[2] + d[3])) + ((d[4] + d[5]) + (d[6] + d[7])) + (d[8] + d[9]);
    f32x2 b = (d[0] * d[0] + d[2] * d[2] + d[4] * d[4] + d[6] * d[6] + d[8] * d[8]) + (d[1] * d[1] + d[3] * d[3] + d[5] * d[5] + d[7] * d[7] + d[9] * d[9]);
    float a0, a1, b0, b1;
#define LN_DP4(m) "v_add_f32_dpp %0, %0, %0 " m " row_mask:0xf bank_mask:0xf\n\tv_add_f32_dpp %1, %1, %1 " m " row_mask:0xf bank_mask:0xf\n\t" \
                  "v_add_f32_dpp %2, %2, %2 " m " row_mask:0xf bank_mask:0xf\n\tv_add_f32_dpp %3, %3, %3 " m " row_mask:0xf bank_mask:0xf\n\t"
    asm("s_nop 1\n\t" LN_DP4("quad_perm:[1,0,3,2]") LN_DP4("quad_perm:[2,3,0,1]") LN_DP4("row_ror:4") LN_DP4("row_ror:8")
        "v_permlane16_swap_b32 %0, %1\n\tv_add_f32 %0, %0, %1\n\tv_permlane16_swap_b32 %2, %3\n\tv_add_f32 %2, %2, %3\n\ts_nop 1\n\t"
        "v_permlane32_swap_b32 %0, %2\n\tv_add_f32 %0, %0, %2\n\ts_nop 0\n\t"
        "v_readlane_b32 %4, %0, 0\n\tv_readlane_b32 %5, %0, 16\n\tv_readlane_b32 %6, %0, 32\n\tv_readlane_b32 %7, %0, 48\n\ts_nop 1"
        : "+v"(a.x), "+v"(b.x), "+v"(a.y), "+v"(b.y), "=s"(a0), "=s"(b0), "=s"(a1), "=s"(b1));
    const f32x2 m = (f32x2){a0, a1} * (1.0f / DM), v = (f32x2){b0, b1} * (1.0f / DM) - m * m;
    const f32x2 q = up2(pkv(up2(pkv((f32x2){fmaxf(v.x, 0.f), fmaxf(v.y, 0.f)})) + 1e-5f));
    const f32x2 sd = up2(pk(__builtin_amdgcn_sqrtf(q.x), __builtin_amdgcn_sqrtf(q.y)));
    mean = up2(pkv(c + m)); rstd = (f32x2){__builtin_amdgcn_rcpf(sd.x), __builtin_amdgcn_rcpf(sd.y)};
}
DEV void role_row(const Params& p, lptr lds, int id, v2u ep) {
    const int lane = threadIdx.x & 63, w = uni(threadIdx.x >> 6);
    const int pr = id >> 2, L = id & 1, par = (id >> 1) & 1;
    const rsrc_t rs = mk_rsrc(p.ws, W_END), rx = mk_rsrc(p.x, ACT), ro_ = mk_rsrc(p.out, ACT);
    bool dead = false;
    const u32 fp = lane < 20 ? foff(F_P + L * 32 + lane) : OOB, l16 = (u32)lane * 16u, l8 = (u32)lane * 8u, l4 = (u32)lane * 4u, fb = uu(w * CV_W);
    lptr F = lds + w * CV_W;
    MK_SRD();
    for (int s = par; s < NSEG; s += 2) {
        const u32 ro = (u32)(s * 32 + 2 * pr) * (DM * 2);
        if ((s & 6) == 0) {
            if ((s & 30) == 0) pre_poll(rs, 0, s < 32 ? NPRE : NCV, s < 32 ? 3u : 5u, ep);
            if (id == 0 && s == 0) LST(274, rt32());
            if (s == 48) st8s(rs, (id == 1 && lane == 0) ? foff(F_EPOCH) : OOB, 0, (v2u){ep.x + 8u, ep.y});
#pragma unroll 1
            for (int i = 0; i < 5; ++i) {
                const u32 d = fb + i * 2048, o = W_Y + (u32)(16 * pr + (s >> 3)) * 10240u + i * 2048;
                DMA32(d, l16, o); DMA32(d + 10240u, l16, o + 81920u);
            }
            DRAIN();
        }
        RowX xr[2];
        auto xload = [&](rsrc_t r, u32 o, auto aux) {
#pragma unroll
            for (int i = 0; i < 2; ++i) {
                xr[i].a = __builtin_amdgcn_raw_buffer_load_b64(r, l8, o + i * (DM * 2), aux.value); xr[i].b = __builtin_amdgcn_raw_buffer_load_b64(r, l8 + 512u, o + i * (DM * 2), aux.value);
                xr[i].c = __builtin_amdgcn_raw_buffer_load_b32(r, l4 + 1024u, o + i * (DM * 2), aux.value);
            }
        };
        if (L == 0) xload(rx, ro, IC<0>{});
        f32x2 y[10], x[10];
        {
            lptr ya = F + (s & 7) * 2 + l16, yc = F + (s & 7) * 2 + 2048 + (lane >> 1) * 16 + (lane & 1) * 5120;
#pragma unroll
            for (int i = 0; i < 10; ++i) {
                lptr q = i < 8 ? ya + (i & 3) * 2560 + (i >> 2) * 1024 : yc + (i & 1) * 2560;
                y[i] = (f32x2){lo_f(*(LAS u16*)q), lo_f(*(LAS u16*)(q + 10240))}; PIN(y[i]);
            }
        }
        wait_flags(rs, fp, (u32)s + 1, dead);
        LS3(L == 0, 3, ri);
        LMX(L == 0, 0, s, rt32());
        {
            const u32 o = uu(W_P + (u32)(L * NSEG + s) * P_ITEM + (u32)pr * (4 * DM * 4));
            if (L == 1) xload(rs, W_X2 + ro, IC<16>{});
            v4u a[4], b[4]; v2u c[4];
#pragma unroll
            for (int kq = 0; kq < 4; ++kq) { a[kq] = ld16s(rs, l16, o + kq * (DM * 4)); SCHED(); b[kq] = ld16s(rs, l16 + 1024u, o + kq * (DM * 4)); SCHED(); c[kq] = ld8s(rs, l8 + 2048u, o + kq * (DM * 4)); SCHED(); }
            const u32 u0[5] = {xr[0].a.x, xr[0].a.y, xr[0].b.x, xr[0].b.y, xr[0].c}, u1[5] = {xr[1].a.x, xr[1].a.y, xr[1].b.x, xr[1].b.y, xr[1].c};
#pragma unroll
            for (int i = 0; i < 5; ++i) { x[2 * i] = (f32x2){lo_f(u0[i]), lo_f(u1[i])}; x[2 * i + 1] = (f32x2){hi_f(u0[i]), hi_f(u1[i])}; }
#pragma unroll
            for (int kq = 0; kq < 4; ++kq)
#pragma unroll
                for (int i = 0; i < 10; ++i)
                    asm volatile("v_dot2_f32_bf16 %0, %2, %3, %0\n\tv_dot2_f32_bf16 %1, %2, %4, %1" : "+v"(x[i].x), "+v"(x[i].y)
                                 : "v"(i < 4 ? a[kq][i & 3] : i < 8 ? b[kq][i & 3] : c[kq][i & 1]), "s"(0x3F80u), "s"(0x3F800000u));
            asm volatile("s_nop 2" : "+v"(x[8]), "+v"(x[9]));
        }
        LS3(L == 0, 4, ri);
        f32x2 mean, rstd;
        ln_stats2(x, mean, rstd);
#pragma unroll
        for (int i = 0; i < 10; ++i) x[i] += (x[i] - mean) * (y[i] * rstd);
        u32 xp[2][5];
#pragma unroll
        for (int i = 0; i < 5; ++i) { xp[0][i] = pkv((f32x2){x[2 * i].x, x[2 * i + 1].x}); xp[1][i] = pkv((f32x2){x[2 * i].y, x[2 * i + 1].y}); }
        if (L == 0) {
#pragma unroll
            for (int i = 0; i < 5; ++i) { x[2 * i] = (f32x2){lo_f(xp[0][i]), lo_f(xp[1][i])}; x[2 * i + 1] = (f32x2){hi_f(xp[0][i]), hi_f(xp[1][i])}; }
            ln_stats2(x, mean, rstd);
#pragma unroll
            for (int i = 0; i < 10; ++i) x[i] = (x[i] - mean) * rstd;
#pragma unroll
            for (int r = 0; r < 2; ++r) {
                u32 hp[5];
#pragma unroll
                for (int i = 0; i < 5; ++i) hp[i] = pkv((f32x2){x[2 * i][r], x[2 * i + 1][r]});
                store_h(rs, W_H2, s * 32 + 2 * pr + r, hp);
            }
#pragma unroll
            for (int r = 0; r < 2; ++r) {
                st8s(rs, l8, W_X2 + ro + r * (DM * 2), (v2u){xp[r][0], xp[r][1]});
                st8s(rs, l8 + 512u, W_X2 + ro + r * (DM * 2), (v2u){xp[r][2], xp[r][3]});
                st4s(rs, l4 + 1024u, W_X2 + ro + r * (DM * 2), xp[r][4]);
            }
            LS3(L == 0, 5, ri);
            DRAIN();
            st4s(rs, lane == 0 ? foff(F_H2 + par * 16 + pr) : OOB, 0, (u32)s + 1);
        } else {
#pragma unroll
            for (int r = 0; r < 2; ++r) {
                st8s(ro_, l8, ro + r * (DM * 2), (v2u){xp[r][0], xp[r][1]});
                st8s(ro_, l8 + 512u, ro + r * (DM * 2), (v2u){xp[r][2], xp[r][3]});
                st4s(ro_, l4 + 1024u, ro + r * (DM * 2), xp[r][4]);
            }
        }
        if (pr == 0) LST(320 + L * 64 + s, rt32());
        LS2(L == 0, 224 + ri);
        LMX(L == 0, 1, s, rt32());
    }
    LDUMP(L == 0, 32 + ri);
}

constexpr float SCALE = 0.08838834764831845f, LOG2E = 1.4426950408889634f;
constexpr int C_K = 0, C_V = 8192, C_QX = 16384, C_MEM = 24576, C_Q = C_MEM + 10240, C_KM = C_Q + 2304, C_VM = C_KM + 2304, C_DUMP = C_VM + 2048,
              C_ZERO = C_DUMP + 1024, C_WL = C_ZERO + 128;
constexpr int NLF = 14;
static_assert(C_WL % 16 == 0 && C_WL + 4 * NLF * 2048 <= LDS_BYTES, "head lds");
#define CH_VIS 10
#define CH_H0 104
#define CH_W0 81
#define CH_MH 2
#define CH_MW 4
#define CH_REC 34
#define CH_RTY 32u
#define CH_RISE 12u
#define CH_FALL 3u
#define CH_PRE 9
#define CH_T0 190
#define CH_UP 2
#define CH_WX 6
#define WAIT_TXT(tmp, t4) "s_memrealtime vcc\n\t.p2align 5\n1:\n\ts_waitcnt lgkmcnt(0)\n\ts_sub_u32 " tmp ", " t4 ", vcc_lo\n\ts_memrealtime vcc\n\ts_cmpk_lt_u32 " tmp ", 400\n\ts_cbranch_scc1 1b\n\t" \
                          "s_add_u32 " tmp ", " tmp ", 4\n\ts_cselect_b32 " tmp ", " tmp ", 0\n2:\n\ts_sub_u32 " tmp ", " tmp ", 1\n\ts_cbranch_scc0 2b\n\t"
DEV void wait_until(u32 t) { u32 tmp; asm volatile(WAIT_TXT("%0", "%1") "s_waitcnt lgkmcnt(0)" : "=&s"(tmp) : "s"(uu(t - 4u)) : "vcc", "scc", "memory"); }
#define SMF_A(acc, a, b) asm volatile("v_smfmac_f32_16x16x64_bf16 %0, %1, %2, %3" : "+v"(acc) : "v"(a), "a"(b), "v"(idx))
#define SMF_V(acc, a, b) asm volatile("v_smfmac_f32_16x16x64_bf16 %0, %1, %2, %3" : "+v"(acc) : "v"(a), "v"(b), "v"(idx))
#define MF0(d, a, b) asm volatile("v_mfma_f32_16x16x32_bf16 %0, %1, %2, 0" : "=&v"(d) : "v"(a), "v"(b))
#define MF1(d, a, b) asm volatile("v_mfma_f32_16x16x32_bf16 %0, %1, %2, %0" : "+v"(d) : "v"(a), "v"(b))
#define MF0H(d, a, b) asm volatile("v_mfma_f32_16x16x16_bf16 %0, %1, %2, 0" : "=&v"(d) : "v"(a), "v"(b))
#define MF1H(d, a, b) asm volatile("v_mfma_f32_16x16x16_bf16 %0, %1, %2, %0" : "+v"(d) : "v"(a), "v"(b))
DEV v8u tr_frag(lptr p0, lptr p1, lptr p2, lptr p3) { return cat8(tr2(p0, p1), tr2(p2, p3)); }
constexpr int qslot(int ks) { return (ks & 1) && ks < 8 ? ks >> 1 : -1; }
constexpr int kslot(int ks) { return ks < 4 ? 4 + ks : -1; }
constexpr int vslot(int ks) { return ks >= 4 ? 4 + ks : -1; }
DEV u32 ring_map(u32 off, u32 half) { return ((off >> 13) << 14) + (half << 13) + (off & 8191u); }
#define ZRD(x, i) x = __builtin_bit_cast(f32x4, lds_r16(ZP + 16 * (i)))
#define VMAX(d, a, b) asm volatile("v_max_f32 %0, %1, %2" : "=v"(d) : "v"(a), "v"(b))
#define VMAX3(d, a, b, c_) asm volatile("v_max3_f32 %0, %1, %2, %3" : "=v"(d) : "v"(a), "v"(b), "v"(c_))
#define VEXP(d, a) asm volatile("v_exp_f32 %0, %1" : "=v"(d) : "v"(a))
#define VMULC(d, a) asm volatile("v_mul_f32 %0, 0x3e0293ee, %1" : "=v"(d) : "v"(a))
#define CADD(d, a, b) do { d = (a) + (b); PIN(d); } while (0)
#define CMULC(d, a) do { d = (a) * CEXP; PIN(d); } while (0)
#define CCVT(d, a, b) do { d = pk(a, b); PIN(d); } while (0)
#define LGK(n) __builtin_amdgcn_s_waitcnt(0xC07F | (n) << 8)
#define SEL(d, a, b) asm volatile("v_cndmask_b32 %0, %1, %2, %3" : "=v"(d) : "v"(a), "v"(b), "s"(himask))
#define QPERM(d, a, q) asm volatile("v_mov_b32_dpp %0, %1 quad_perm:" q " row_mask:0xf bank_mask:0xf" : "=v"(d) : "v"(a))

#define AL_H 3
#define AL_W 0
#define HD_SMP 56
DEV void role_head(const Params& p, lptr lds, int L, int h, v2u ep) {
    const int tid = threadIdx.x, lane = tid & 63, w = uni(tid >> 6), g = lane >> 4, r = lane & 15;
    const rsrc_t rs = mk_rsrc(p.ws, W_END);
    v8u wq0[10], wq1[10], wk0[10], wk1[10], wv0[10], wv1[10];
    lptr WL = lds + C_WL + w * (NLF * 2048) + lane * 16;
    {
        constexpr int GS = 1056, CS = 48 * GS;
        const v4u sq = mk_srd(p.Wq, DM * NQ * 2), sk = mk_srd(p.Wk, DM * NQ * 2), sv = mk_srd(p.Wv, DM * NQ * 2);
        const u32 vo = (u32)(g * NQ + h * DH + 8 * (r ^ ((g & 1) << 3))) * 2u;
        auto fill = [&](int ch) {
#pragma unroll 1
            for (int i = 0; i < 4; ++i) {
                const u32 dst = uu((ch % 3) * CS + (4 * i + w) * GS), so = uu((ch * 64 + 16 * i + 4 * w) * (NQ * 2));
                DMAIN(sq, dst, vo, so); DMAIN(sk, dst + 16 * GS, vo, so); DMAIN(sv, dst + 32 * GS, vo, so);
            }
        };
        fill(0); fill(1); fill(2);
        const int q4 = r >> 2;
        lptr tb = lds + (4 * (g >> 1) + 2 * (q4 >> 1) + (g & 1)) * GS + (q4 & 1) * 256 + ((w * 64 + (r & 3) * 8) ^ ((q4 & 1) << 7));
        SFOR(ch, 10)
            VMW(ch < 8 ? 24 : 108 - 12 * ch);
            BAR();
            auto frag = [&](int m, int t) { lptr b = tb + (ch % 3) * CS + m * 16 * GS + t * 32; return tr_frag(b, b + 512, b + 8 * GS, b + 8 * GS + 512); };
            wq0[ch] = frag(0, 0); wq1[ch] = frag(0, 1); wk0[ch] = frag(1, 0); wk1[ch] = frag(1, 1); wv0[ch] = frag(2, 0); wv1[ch] = frag(2, 1);
            asm volatile("" : "+a"(wq0[ch]), "+a"(wk0[ch]), "+v"(wv0[ch]), "+v"(wv1[ch]));
            if constexpr (qslot(ch) < 0) asm volatile("" : "+a"(wq1[ch])); else asm volatile("" : "+v"(wq1[ch]));
            if constexpr (kslot(ch) < 0) asm volatile("" : "+a"(wk1[ch])); else asm volatile("" : "+v"(wk1[ch]));
            BAR();
            if constexpr (ch < 7) fill(ch + 3);
        SEND
#pragma unroll
        for (int ch = 0; ch < 10; ++ch) {
            if (qslot(ch) >= 0) { lds_w16(WL + qslot(ch) * 2048, wq1[ch].lo); lds_w16(WL + qslot(ch) * 2048 + 1024, wq1[ch].hi); }
            if (kslot(ch) >= 0) { lds_w16(WL + kslot(ch) * 2048, wk1[ch].lo); lds_w16(WL + kslot(ch) * 2048 + 1024, wk1[ch].hi); }
            if (vslot(ch) >= 0) { lds_w16(WL + vslot(ch) * 2048, wv1[ch].lo); lds_w16(WL + vslot(ch) * 2048 + 1024, wv1[ch].hi); }
        }
        CFENCE();
    }
    const u32 idx = (r & 2) ? 0xEEEEu : 0x4444u;
    unsigned long long himask = 0xCCCCCCCCCCCCCCCCull;
    PINS(himask);
    bool dead = false;
    if (h == 3) LST(266 + L, rt32());
    if (w == 0) LST(1240 + L * 32 + h, rt32());
    if (L == 1) {
        KvIn in;
        kvt_load(p, KT0 + 4 * h + w, in, [] {}); kvt_make(rs, lds + w * 9216, KT0 + 4 * h + w, in);
        DRAIN(); BAR();
        pre_post(rs, 64 + h, 3u, ep);
    }
    const u32 pol0 = pol_wait(rs, ep);
    if (h == 3 && L == 0) LST(277, rt32());
    const u32 l16 = (u32)lane * 16u;
    const u32 mring = W_MRING + (u32)L * (MB / 4) + pol0 * 8192u, hint = mring + 4 * 32768u;
    const u32 fkh = lane < 3 ? foff(F_QKV + L * 96 + lane * 32 + h) : lane < 7 ? hint + (u32)(lane - 3) * 4u : OOB;
    u32 tm = 0;
    MK_SRD();
    u32 ksrc = uu(W_KQ + (u32)(L * (NSEG - 1) * NH + h) * 16384u + w * 2048), vsrc = ksrc + (W_VO - W_KQ);
    u32 dk = uu(C_K + w * 2048), dv = dk + (C_V - C_K), dq = dk + (C_QX - C_K), dm = uu(C_MEM + w * 2048), dm2 = uu(w < 2 ? C_MEM + 8192 + w * 1024 : C_DUMP);
    asm volatile("" : "+s"(dk), "+s"(dv), "+s"(dq), "+s"(dm), "+s"(dm2));
    auto keys_wait = [&](u32 need) {
#pragma unroll 1
        for (int n = 0; !dead; ++n) {
            const bool beat = need + 2 * L > 2u;
            if (beat) wait_until(tm);
            const u32 hv = ld4(rs, lane == 7 ? foff(F_ERR) : fkh);
            if (beat) { tm = rdl(hv, 5); wait_until(rdl(hv, 3) - 30u); }
            if (((u32)__builtin_amdgcn_ballot_w64(ld4(rs, fkh) < need) & 7u) == 0u) break;
            dead = n > SPIN_CAP || rdl(hv, 7) != 0u;
            st4s(rs, dead ? foff(F_ERR) : OOB, 0, 1u);
        }
        CFENCE();
        DMA32(dk, l16, ksrc); DMA32(dv, l16, vsrc); DMA32(dq, l16, ksrc ^ 8192u);
    };
    if (L == 0) { st4s(rs, tid == 0 ? hint + 4096u + (u32)h * 128u : OOB, 0, 0u); keys_wait(1u); }
    lptr qw = lds + C_Q + (2 * g) * 288 + (32 * w + 2 * r) * 2;
    lptr kw = qw + (C_KM - C_Q); PIN(kw);
    lptr qr = lds + C_Q + (2 * (r >> 2) + (r & 1)) * 288 + g * 16;
    lptr LB = lds + l16, VX = lds + C_V + w * 2048 + l16;
    PIN(VX);
    if (tid >= 64 && tid < 70) { const u32 z = tid == 68 ? 0x3F803F80u : tid == 69 ? PSENT : 0u; lds_w16(lds + C_ZERO + (tid - 64) * 16, (v4u){z, z, z, z}); }
    u32 zaddr = 0; PIN(zaddr);
    lptr ZP = lds + zaddr + C_ZERO;
    const float CEXP = SCALE * LOG2E;
#define LF_RD(slot, hf) lf[slot][hf] = lds_r16(WL + (slot) * 2048 + (hf) * 1024)
    BAR();
    const u32 ofl = tid == 0 ? foff(F_O + L * 32 + h) : OOB;
    u32 oso = (((g | L) & 1) ? OOB : 0u) + W_ORING + (u32)L * (MB / 2) + ring_map((u32)((r >> 3) * 32768 + h * 1024 + w * 256 + (g >> 1) * 128 + (r & 7) * 16), pol0);
    unsigned long long vis[8];
#pragma unroll
    for (int i = 0; i < 8; ++i) vis[i] = __builtin_amdgcn_ballot_w64(16 * (i >> 2) + 4 * g + (i & 3) <= 16 * (w & 1) + r);
    u32 missed = 0;
    for (int s = -L; s < NSEG;) {
        u32 sv = (u32)max(s, 0), ms = mring + (sv & 3u) * 32768u + w * 2048, ms2 = mring + (sv & 3u) * 32768u + 16384u + (w & 1) * 1024;
        asm volatile("" : "+s"(ms), "+s"(ms2)); PIN(sv);
        f32x4 aq0, aq1, ak0, ak1;
        v4u lf[NLF][2];
        ZRD(aq0, 0); ZRD(aq1, 1); ZRD(ak0, 2); ZRD(ak1, 3);
#pragma unroll
        for (int i = 0; i < 4; ++i) { LF_RD(i, 0); LF_RD(i, 1); }
        SCHED();
        v4u A[10];
#define HT(k) do {} while (0)
        v4u kx[8], qf[4], kmf[4], vx[2], ones, P;
        float fa, fb, fc, fd, inv, sx[4], t[4], tm0, tm1; u32 pa, pb, fl, e01, e23, pmm;
        f32x4 av0, av1, s0, s1, sm, oa, ob, rsum;
        v2u vm, pm;
#define LW(sl) cat8(lf[sl][0], lf[sl][1])
#define PQ(ks, tl) do { if constexpr (!(tl)) SMF_A(aq0, A[ks], wq0[ks]); else if constexpr (qslot(ks) < 0) SMF_A(aq1, A[ks], wq1[ks]); else { const v8u lw_ = LW(qslot(ks)); SMF_V(aq1, A[ks], lw_); } } while (0)
#define PK(ks, tl) do { if constexpr (!(tl)) SMF_A(ak0, A[ks], wk0[ks]); else if constexpr (kslot(ks) < 0) SMF_A(ak1, A[ks], wk1[ks]); else { const v8u lw_ = LW(kslot(ks)); SMF_V(ak1, A[ks], lw_); } } while (0)
#define PV(ks, tl) do { if constexpr (!(tl)) SMF_V(av0, A[ks], wv0[ks]); else if constexpr (vslot(ks) < 0) SMF_V(av1, A[ks], wv1[ks]); else { const v8u lw_ = LW(vslot(ks)); SMF_V(av1, A[ks], lw_); } } while (0)
#define KXR(i) kx[i] = lds_r16(LB + C_K + ((i) & 1) * 4096 + ((i) >> 1) * 1024)
#define XW(q_) do { *(LAS u32*)(q_) = pa; *(LAS u32*)((q_) + 288) = pb; } while (0)
        auto slot = [&](auto zc) __attribute__((always_inline)) {
            constexpr int z = decltype(zc)::value;
            if constexpr (z < 14) PQ(z >> 1, z & 1);
            else if constexpr (z < 17) PQ(z - 7, 0);
            else if constexpr (z < 20) PQ(z - 10, 1);
            else if constexpr (z < 34) PK((z - 20) >> 1, z & 1);
            else if constexpr (z < 37) PK(z - 27, 0);
            else if constexpr (z < 40) PK(z - 30, 1);
            else if constexpr (z < 42) { if constexpr (z == 40) MF0(s0, kx[0], qf[0]); else MF0(s1, kx[1], qf[0]); }
            else if constexpr (z < 48) { if constexpr (z & 1) MF1(s1, kx[z - 40], qf[(z - 40) >> 1]); else MF1(s0, kx[z - 40], qf[(z - 40) >> 1]); }
            else if constexpr (z < 54) PV(z - 48, 0);
            else if constexpr (z == 54) MF0(sm, kmf[0], qf[0]);
            else if constexpr (z < 58) MF1(sm, kmf[z - 54], qf[z - 54]);
            else if constexpr (z < 62) PV(z - 52, 0);
            else if constexpr (z < 64) PV(z - 62, 1);
            else if constexpr (z == 64) MF0(oa, vx[0], P);
            else if constexpr (z == 65) MF0(ob, vx[1], P);
            else if constexpr (z == 66) MF0(rsum, ones, P);
            else if constexpr (z < 75) PV(z < 70 ? z - 63 : z == 70 ? 2 : z == 71 ? 7 : z == 72 ? 3 : z - 65, 1);
            else { const v2u ones2 = ones.lo; MF1H(rsum, ones2, pm); }
            if constexpr (z < 6) A[4 + z] = lds_r16(LB + C_MEM + (4 + z) * 1024);
            if constexpr (z >= 6 && z < 10) LF_RD(4 + ((z - 6) >> 1), z & 1);
            if constexpr (z >= 14 && z < 18) LF_RD(6 + ((z - 14) >> 1), z & 1);
            if constexpr (z >= 22 && z < 28) KXR(z - 22);
            if constexpr (z == 28) XW(qw);
            if constexpr (z == 29 || z == 30) KXR(z - 23);
            if constexpr (z == 32 || z == 47) asm volatile("s_barrier" ::: "memory");
            if constexpr (z >= 33 && z < 37) qf[z - 33] = lds_r16(qr + (z - 33) * 64);
            if constexpr (z == 20) ZRD(av0, 0);
            if constexpr (z == 43) XW(kw);
            if constexpr (z == 44 || z == 45) vx[z - 44] = lds_r16(VX + (z - 44) * 1024);
            if constexpr (z >= 48 && z < 52) kmf[z - 48] = lds_r16(qr + (C_KM - C_Q) + (z - 48) * 64);
            if constexpr (z == 52) ZRD(av1, 1);
            if constexpr (z == 37) ones = lds_r16(ZP + 64);
            if constexpr (z >= 57 && z < 64) LF_RD(8 + ((z - 57) >> 1), (z - 57) & 1);
            if constexpr (z >= 65 && z < 70) LF_RD(8 + ((z - 58) >> 1), (z - 58) & 1);
            if constexpr (z == HD_SMP) LD4A(fl, fkh, 0);
            SCHED();
            if constexpr (z == 19) HT(2);
            if constexpr (z == 32) HT(3);
            if constexpr (z == 47) HT(4);
            if constexpr (z == 64) HT(5);
            if constexpr (z == 19) LGK(4);
            if constexpr (z == 21 || z == 41) LGK(1);
            if constexpr (z == 56 || z == 73) LGK(0);
            if constexpr (z == 31 || z == 46) LGK(2);
            if constexpr (z == 39) LGK(4);
            if constexpr (z >= 53 && z < 56) LGK(57 - z);
            if constexpr (z == 64) LGK(3);
            if constexpr (z == 68) LGK(5);
            if constexpr (z >= 70 && z < 73) LGK(74 - z);
            SCHED();
            if constexpr (z == 18) CADD(fa, aq0[0], aq0[2]);
            if constexpr (z == 19) CADD(fb, aq0[1], aq0[3]);
            if constexpr (z == 20) CMULC(fa, fa);
            if constexpr (z == 21) CADD(fc, aq1[0], aq1[2]);
            if constexpr (z == 22) CADD(fd, aq1[1], aq1[3]);
            if constexpr (z == 23) CMULC(fc, fc);
            if constexpr (z == 24) CMULC(fb, fb);
            if constexpr (z == 25) CMULC(fd, fd);
            if constexpr (z == 26) CCVT(pa, fa, fc);
            if constexpr (z == 27) CCVT(pb, fb, fd);
            if constexpr (z == 38) CADD(fa, ak0[0], ak0[2]);
            if constexpr (z == 39) CADD(fb, ak0[1], ak0[3]);
            if constexpr (z == 41) { CADD(fc, ak1[0], ak1[2]); CADD(fd, ak1[1], ak1[3]); }
            if constexpr (z == 42) { CCVT(pa, fa, fc); CCVT(pb, fb, fd); }
            if constexpr (z >= 49 && z < 53) SEL(sx[z - 49], s0[z - 49], s1[z - 49]);
            if constexpr (z >= 53 && z < 57) VEXP(t[z - 53], sx[z - 53]);
            if constexpr (z == 57) CCVT(e01, t[0], t[1]);
            if constexpr (z == 58) CCVT(e23, t[2], t[3]);
            if constexpr (z == 59) QPERM(P.x, e01, "[0,1,0,1]");
            if constexpr (z == 60) QPERM(P.z, e01, "[2,3,2,3]");
            if constexpr (z == 61) QPERM(P.y, e23, "[0,1,0,1]");
            if constexpr (z == 62) QPERM(P.w, e23, "[2,3,2,3]");
            if constexpr (z == 63) CADD(fa, av0[0], av0[2]);
            if constexpr (z == 64) { asm volatile("" :: "v"(sm)); VEXP(tm0, sm[0]); }
            if constexpr (z == 65) VEXP(tm1, sm[1]);
            if constexpr (z == 66) CADD(fb, av0[1], av0[3]);
            if constexpr (z == 67) CCVT(pmm, tm0, tm1);
            if constexpr (z >= 68 && z < 72) SEL(oa[z - 68], oa[z - 68], ob[z - 68]);
            if constexpr (z == 72) asm volatile("v_cndmask_b32 %0, %1, 0, %2" : "=v"(pm.x) : "v"(pmm), "s"(himask));
            if constexpr (z == 73) asm volatile("v_cndmask_b32 %0, 0, %1, %2" : "=v"(pm.y) : "v"(pmm), "s"(himask));
            if constexpr (z == 74) LGK(15);
            if constexpr (z == 75) CCVT(vm.x, fa, fb);
            SCHED();
        };
        {
            u32 tmp;
            asm volatile("s_mov_b32 m0, %2\n\t" WAIT_TXT("%0", "%1") "buffer_load_dwordx4 %4, %5, %6 offen sc1 lds\n\tbuffer_load_dwordx4 %4, %5, %6 offen offset:1024 sc1 lds\n\t"
                         "s_mov_b32 m0, %3\n\ts_nop 0\n\tbuffer_load_dwordx4 %4, %5, %7 offen sc1 lds\n\t.p2align 6\n\t.rept %8\n\ts_nop 0\n\t.endr\n\ts_waitcnt vmcnt(0) lgkmcnt(0)\n\ts_barrier"
                         : "=&s"(tmp) : "s"(uu(tm - 4u)), "s"(dm), "s"(dm2), "v"(l16), "s"(srd), "s"(ms), "s"(ms2), "i"(AL_H) : "vcc", "scc", "memory");
        }
        HT(1);
#pragma unroll
        for (int ks = 0; ks < 4; ++ks) A[ks] = lds_r16(LB + C_MEM + ks * 1024);
        SCHED();
        st4s(rs, ofl, 0, sv);
        SCHED();
        static_for<0, 76>(slot);
        asm volatile("s_nop 2\n\tv_add_f32 %1, %3, %5\n\tv_add_f32 %2, %4, %6\n\tv_cvt_pk_bf16_f32 %0, %1, %2"
                     : "=v"(vm.y), "=&v"(fc), "=&v"(fd) : "v"(av1[0]), "v"(av1[1]), "v"(av1[2]), "v"(av1[3]));
        __builtin_amdgcn_s_waitcnt(0xCF7F);
        asm volatile("s_nop 0\n\tv_mfma_f32_16x16x16_bf16 %0, %2, %3, %0\n\tv_rcp_f32 %1, %4" : "+v"(oa), "=&v"(inv) : "v"(vm), "v"(pm), "v"(rsum[0]), "v"(rsum));
        SCHED();
        if (__builtin_expect(__builtin_amdgcn_ballot_w64(u_(rsum[0]) - 0x21800000u > 0x3C000000u) != 0ull, 0)) {
            DRAIN(); SCHED();
            bool miss = false;
#pragma unroll 1
            for (int i = 0; i < 10; ++i) { const v4u a = lds_r16(LB + C_MEM + i * 1024); miss |= a.x == PSENT || a.y == PSENT || a.z == PSENT || a.w == PSENT; }
            if (missed < SPIN_CAP && !all_(!miss)) {
                *(LAS u32*)(lds + C_DUMP + (w == 0 ? 0 : 64)) = ld4(rs, foff(F_ERR));
                asm volatile("" : "+v"(fl) :: "memory");
                tm = rdl(fl, 5);
                BAR();
                missed = uu(*(LAS u32*)(lds + C_DUMP)) != 0u ? SPIN_CAP : missed + 1;
                st4s(rs, missed == SPIN_CAP ? foff(F_ERR) : OOB, 0, 1u);
                BAR();
                continue;
            }
            float mx = fmaxf(fmaxf(fmaxf(sx[0], sx[1]), fmaxf(sx[2], sx[3])), fmaxf(sm[0], sm[1]));
            mx = max32(max16(fmaxf(mx, dpp<0x4E>(mx))));
#pragma unroll
            for (int i = 0; i < 4; ++i) t[i] = __builtin_amdgcn_exp2f(sx[i] - mx);
            e01 = pk(t[0], t[1]); e23 = pk(t[2], t[3]);
            pmm = pk(__builtin_amdgcn_exp2f(sm[0] - mx), __builtin_amdgcn_exp2f(sm[1] - mx));
            asm volatile("v_cndmask_b32 %0, %2, 0, %3\n\tv_cndmask_b32 %1, 0, %2, %3" : "=&v"(pm.x), "=&v"(pm.y) : "v"(pmm), "s"(himask));
            asm volatile("s_nop 1" : "+v"(e01), "+v"(e23), "+v"(pm));
            QPERM(P.x, e01, "[0,1,0,1]"); QPERM(P.z, e01, "[2,3,2,3]"); QPERM(P.y, e23, "[0,1,0,1]"); QPERM(P.w, e23, "[2,3,2,3]");
            NOPS(1);
            MF0(rsum, ones, P); MF0(oa, vx[0], P); MF0(ob, vx[1], P);
            NOPS(7);
            { const v2u ones2 = ones.lo; MF1H(rsum, ones2, pm); }
#pragma unroll
            for (int i = 0; i < 4; ++i) SEL(oa[i], oa[i], ob[i]);
            NOPS(1);
            MF1H(oa, vm, pm);
            asm volatile("s_nop 7\n\tv_rcp_f32 %0, %1" : "=v"(inv) : "v"(rsum[0]), "v"(rsum));
        }
        {
            v4u oc; f32x2 u0 = oa.lo, u1 = oa.hi; const f32x2 i2 = {inv, inv};
            asm volatile("s_nop 0\n\tv_pk_mul_f32 %0, %0, %2\n\tv_pk_mul_f32 %1, %1, %2" : "+v"(u0), "+v"(u1) : "v"(i2));
            __builtin_amdgcn_s_waitcnt(0xCF7F);
            asm volatile("v_cvt_pk_bf16_f32 %0, %4, %5\n\tv_cvt_pk_bf16_f32 %2, %4, %5\n\tv_cvt_pk_bf16_f32 %1, %6, %7\n\tv_cvt_pk_bf16_f32 %3, %6, %7\n\t"
                         "v_permlane16_swap_b32 %0, %2\n\ts_nop 0\n\tv_permlane16_swap_b32 %1, %3"
                         : "=&v"(oc.x), "=&v"(oc.y), "=&v"(oc.z), "=&v"(oc.w) : "v"(u0.x), "v"(u0.y), "v"(u1.x), "v"(u1.y));
            st16(rs, oso, oc);
        }
        SCHED();
        unsigned long long clk;
        asm volatile("s_memrealtime %0" : "=s"(clk) :: "memory");
        HT(6);
        v4u tq[4], tk[8], tv[2];
        f32x4 c0, c1, cm;
        u32 oo, kvo; bool kvok;
        u32 ln = l16; PIN(ln);
        lptr QX = lds + C_QX + (w & 1) * 4096 + ln, VO = lds + C_V + (w ^ 1) * 2048 + ln, VMX = lds + C_VM + w * 512 + (ln >> 1);
#pragma unroll
        for (int i = 0; i < 2; ++i) { tq[i] = lds_r16(QX + i * 1024); kmf[i] = lds_r16(qr + (C_KM - C_Q) + i * 64); tk[2 * i] = lds_r16(LB + C_K + i * 1024); tk[2 * i + 1] = lds_r16(LB + C_K + 4096 + i * 1024); }
        *(LAS v2u*)VMX = vm;
        SCHED();
        SFOR(i, 12)
            if constexpr (i == 0) MF0(cm, kmf[0], tq[0]); else if constexpr (i == 1) MF0(c0, tk[0], tq[0]); else if constexpr (i == 2) MF0(c1, tk[1], tq[0]);
            else if constexpr (i % 3 == 0) MF1(cm, kmf[i / 3], tq[i / 3]); else if constexpr (i % 3 == 1) MF1(c0, tk[2 * (i / 3)], tq[i / 3]); else MF1(c1, tk[2 * (i / 3) + 1], tq[i / 3]);
            if constexpr (i < 2) kmf[2 + i] = lds_r16(qr + (C_KM - C_Q) + (2 + i) * 64);
            if constexpr (i == 0) { tq[2] = lds_r16(QX + 2048); tk[4] = lds_r16(LB + C_K + 2048); }
            if constexpr (i == 1) { tk[5] = lds_r16(LB + C_K + 6144); tq[3] = lds_r16(QX + 3072); }
            if constexpr (i == 2) { tk[6] = lds_r16(LB + C_K + 3072); tk[7] = lds_r16(LB + C_K + 7168); }
            if constexpr (i == 3) { tv[0] = lds_r16(VO); tv[1] = lds_r16(VO + 1024); }
            if constexpr (i == 4) { oo = ln + (ln & 0x300u) + ((vsrc ^ 8192u) + (u32)((w & 1) * 256) + ((u32)s & OOB)); PIN(oo); }
            if constexpr (i == 5) { ksrc += NH * 16384; vsrc += NH * 16384; PINS(ksrc); PINS(vsrc); }
            if constexpr (i == 9) { asm volatile("s_waitcnt vmcnt(1)" : "+v"(fl) :: "memory"); kvok = ((u32)__builtin_amdgcn_ballot_w64(fl < (u32)s + 2) & 7u) == 0u; }
            if constexpr (i == 10) { kvo = l16 | (kvok ? 0u : OOB); PIN(kvo); }
            SCHED();
        SEND
        asm volatile("s_waitcnt lgkmcnt(0)\n\ts_barrier" : "+s"(clk) :: "memory");
        const v2u tvm = *(LAS v2u*)(VMX + (512 - (w & 1) * 1024));
        const v4u pv = lds_r16(ZP + 80);
        DMA32(dk, kvo, ksrc);
        SCHED(); asm volatile("" :: "v"(cm));
        float u[10], m, m2;
        {
            float NEG = -1000000000.0f / SCALE; PIN(NEG);
#define MSK(d, a, i) asm volatile("v_cndmask_b32 %0, %1, %2, %3" : "=v"(d) : "v"(NEG), "v"(a), "s"(vis[i]))
#pragma unroll
            for (int i = 0; i < 4; ++i) MSK(u[i], c0[i], i);
#pragma unroll
            for (int i = 0; i < 4; ++i) MSK(u[4 + i], c1[i], 4 + i);
            asm volatile("v_max3_f32 %0, %2, %3, %10\n\tv_max3_f32 %1, %4, %5, %11\n\tv_max3_f32 %0, %0, %6, %7\n\tv_max3_f32 %1, %1, %8, %9\n\t"
                         "v_max_f32 %0, %0, %1\n\tv_mov_b32 %1, %0\n\ts_nop 1\n\tv_permlane16_swap_b32 %0, %1\n\tv_max_f32 %0, %0, %1\n\tv_mov_b32 %1, %0\n\ts_nop 1\n\t"
                         "v_permlane32_swap_b32 %0, %1\n\tv_max_f32 %0, %0, %1\n\tv_mul_f32 %0, 0xbe0293ee, %0"
                         : "=&v"(m), "=&v"(m2) : "v"(u[0]), "v"(u[1]), "v"(u[2]), "v"(u[3]), "v"(u[4]), "v"(u[5]), "v"(u[6]), "v"(u[7]), "v"(cm[0]), "v"(cm[1]));
        }
        DMA32(dv, kvo, vsrc);
        {
            v4u px; v2u p0, p1;
#define EX2(d, a, b) asm volatile("v_fmamk_f32 %0, %2, 0x3e0293ee, %4\n\tv_fmamk_f32 %1, %3, 0x3e0293ee, %4\n\tv_exp_f32 %0, %0\n\tv_exp_f32 %1, %1" : "=&v"(d.x), "=&v"(d.y) : "v"(a), "v"(b), "v"(m))
            f32x2 ex[5];
            EX2(ex[0], u[0], u[1]); EX2(ex[1], u[2], u[3]); EX2(ex[2], u[4], u[5]); EX2(ex[3], u[6], u[7]); EX2(ex[4], cm[0], cm[1]);
            asm volatile("v_cvt_pk_bf16_f32 %0, %8, %9\n\tv_cvt_pk_bf16_f32 %1, %10, %11\n\tv_cvt_pk_bf16_f32 %2, %12, %13\n\tv_cvt_pk_bf16_f32 %3, %14, %15\n\t"
                         "v_mov_b32 %5, 0\n\tv_mov_b32 %6, 0\n\tv_cvt_pk_bf16_f32 %4, %16, %17\n\tv_cvt_pk_bf16_f32 %7, %16, %17"
                         : "=&v"(px.x), "=&v"(px.y), "=&v"(px.z), "=&v"(px.w), "=&v"(p0.x), "=&v"(p0.y), "=&v"(p1.x), "=&v"(p1.y)
                         : "v"(ex[0].x), "v"(ex[0].y), "v"(ex[1].x), "v"(ex[1].y), "v"(ex[2].x), "v"(ex[2].y), "v"(ex[3].x), "v"(ex[3].y), "v"(ex[4].x), "v"(ex[4].y));
            f32x4 o[4], sum;
            const v2u ones2 = ones.lo;
            const u32 now = (u32)clk;
            u32 oz;
            SCHED();
            MF0(sum, ones, px); { const u32 q = ksrc ^ 8192u; DMA32(dq, kvo, q); } SCHED();
            MF0(o[0], vx[0], px);
            MF0(o[1], vx[1], px);
            MF0(o[2], tv[0], px); oz = (oso & ~(3u << 17)) | (u32)((s + 3) & 3) << 17; PIN(oz); SCHED();
            MF0(o[3], tv[1], px); oso = (oso & ~(3u << 17)) | (u32)((s + 1) & 3) << 17; PIN(oso); SCHED();
            MF1H(sum, ones2, p0);
            MF1H(o[0], vm, p0);
            MF1H(o[1], vm, p1); st16(rs, oz, pv); SCHED();
            MF1H(o[2], tvm, p0);
            MF1H(o[3], tvm, p1);
            asm volatile("s_nop 2\n\tv_rcp_f32 %0, %1\n\ts_nop 0" : "=v"(inv) : "v"(sum[0]), "v"(sum));
            asm volatile("" : "+v"(o[0]), "+v"(o[1]));
            st16(rs, oo, (v4u){pk2(o[0].lo * inv), pk2(o[0].hi * inv), pk2(o[1].lo * inv), pk2(o[1].hi * inv)});
            asm volatile("" : "+v"(o[2]), "+v"(o[3]));
            st16(rs, oo ^ 2048u, (v4u){pk2(o[2].lo * inv), pk2(o[2].hi * inv), pk2(o[3].lo * inv), pk2(o[3].hi * inv)});
            SCHED();
            LS2(L == 0 && w == 0, h);
            st4s(rs, ((h & 15) == 0 && tid == 192) ? hint + 128u + (u32)h * 8u : OOB, 0, (u32)s << 16 | min(missed, 1u) << 14 | min(now - tm, 0x3FFFu));
            tm = rdl(fl, 5);
            missed = missed == SPIN_CAP ? missed : 0u;
            if (__builtin_expect(s < 1 - L, 0)) {
                if (s < 0) oso -= (ln & 256u) ? 0u : OOB;
#pragma unroll 1
                for (int n = 0; n < (s < 0 ? 0 : 200); ++n) {
                    CFENCE();
                    tm = rdl(ld4(rs, fkh), 6);
                    if (tm != PSENT) break;
                }
            }
        }
        if (__builtin_expect(!kvok, 0) && s + 1 < NSEG) {
            st4s(rs, ln == 0 ? hint + 4096u + (u32)h * 128u : OOB, 0, (u32)s + 1u);
            keys_wait((u32)s + 2u);
        }
        ++s;
    }
    DRAIN(); BAR();
    st4s(rs, ofl, 0, NSEG);
    LDUMP(w == 0, 160 + L * 32 + h);
}

#define WO_NL 3
#define WO_ROT 0
#define WO_AH 1
DEV void role_wo(const Params& p, lptr lds, int j, v2u ep) {
    const int tid = threadIdx.x, lane = tid & 63, w = uni(tid >> 6), g = lane >> 4, r = lane & 15;
    const int rg = j & 1, cg = j >> 1, rot = WO_ROT;
    const rsrc_t rs = mk_rsrc(p.ws, W_END);
    v8u wo[8][5];
    {
        const v4u so_ = mk_srd(p.Wo, DM * NQ * 2);
        u32 vo[5];
#pragma unroll
        for (int i = 0; i < 5; ++i) { const int n = 64 * i + lane; vo[i] = (u32)((n / 5) * DM + cg * 40 + (n % 5) * 8) * 2u; }
        v4u junk = {};
        const u32 av = (u32)(j * 5120 + lane * 16);
        auto ahead = [&](int b) {
#pragma unroll
            for (int i = 0; i < 5; ++i) AHEAD(so_, av, uu((8 * w + b) * (DH * DM * 2) + i * 1024));
        };
        auto fill = [&](int b) {
            if (b + WO_AH < 8) ahead(b + WO_AH);
            const u32 dst = uu(w * 40960 + (b & 3) * 10240), src = uu((8 * w + ((b + rot) & 7)) * (DH * DM * 2));
#pragma unroll
            for (int i = 0; i < 10; ++i) DMAIN(so_, dst + i * 1024, vo[i % 5], src + (i / 5) * (64 * DM * 2));
        };
#pragma unroll 1
        for (int b = 0; b < WO_AH; ++b) ahead(b);
#pragma unroll 1
        for (int b = 0; b < 4; ++b) fill(b);
        const int q4 = r >> 2;
        lptr tb = lds + w * 40960 + (32 * (g >> 1) + 16 * (q4 >> 1) + 8 * ((r >> 1) & 1) + 4 * (g & 1) + (q4 & 1)) * 80 + (r & 1) * 8;
        SFOR(b, 8)
            VMW((b < 5 ? 30 : 70 - 10 * b) + (b + 1 + WO_AH < 8 ? 5 : 0) + (b + 2 + WO_AH < 8 ? 5 : 0) + (b + 3 + WO_AH < 8 ? 5 : 0));
            if constexpr (b + WO_AH == 7) PIN(junk);
#pragma unroll
            for (int t = 0; t < 5; ++t) {
                lptr q = tb + (b & 3) * 10240 + t * 16;
                wo[b][t] = tr_frag(q, q + 160, q + 64 * 80, q + 64 * 80 + 160);
                if (b * 5 + t < 32) asm volatile("" : "+a"(wo[b][t])); else asm volatile("" : "+v"(wo[b][t]));
            }
            CFENCE();
            if constexpr (b < 4) fill(b + 4);
        SEND
    }
    if (j == 0) LST(265, rt32());
    LST(1100 + j * 4 + w, rt32());
    const u32 idx = (r & 2) ? 0xEEEEu : 0x4444u;
    const u32 pol0 = pol_wait(rs, ep);
    BAR();
    const u32 l16 = (u32)lane * 16u;
    u32 oo[8];
#pragma unroll
    for (int i = 0; i < 8; ++i) oo[i] = uu(W_ORING + ring_map((u32)(rg * 32768 + (8 * w + ((i + rot) & 7)) * 1024), pol0));
    const u32 hint = W_MRING + 4 * 32768u + pol0 * 8192u;
    u32 hvo[2];
#pragma unroll
    for (int l = 0; l < 2; ++l) hvo[l] = lane < 2 ? hint + (u32)(l ^ 1) * (MB / 4) + (u32)lane * 4u : lane == 2 ? hint + (u32)l * (MB / 4) + 128u : lane == 3 ? foff(F_ERR) : lane == 4 ? hint + (u32)l * (MB / 4) + 256u :
                 (lane >= 32 && j == 0 && w == 3) ? hint + (u32)l * (MB / 4) + (u32)lane * 128u : OOB;
    lptr xw = r < 8 ? lds + w * 640 + (2 * (g & 1) + (g >> 1)) * 32 + r * 4 : lds + 8192 + lane * 4;
    const int G = ((5 * cg) >> 1) + w, tt = 2 * G + ((lane >> 3) & 1) - 5 * cg;
    const bool pub = w < 3 && lane < 32 && tt >= 0 && tt < 5;
    lptr xr = lds + (pub ? tt * 128 + (2 * (lane >> 4) + ((lane >> 2) & 1)) * 32 + (lane & 3) * 8 : 0);
    const u32 mpo = pub ? W_MRING + ring_map((u32)(G * 256 + rg * 128 + lane * 4), pol0) : OOB;
    constexpr u32 PER0 = CH_W0 + CH_MW + CH_H0 + CH_MH + 2 * CH_VIS, DW0 = CH_W0 + CH_VIS + CH_MW + CH_REC;
    const bool lead = j == 0 && w == 3;
    const u32 hso = (lead && lane == 0) ? hint : OOB;
    MK_SRD();
    u32 t = 0, tn = 0;
    int sn[2] = {0, 0};
    u32 hav = 8 * CH_H0, wav = 8 * CH_W0, mh = 8 * CH_MH, mw = 8 * CH_MW, ok[2] = {0u, 0u}, dw[2] = {DW0, DW0};
    u32 vo = l16, early = PSENT, tp = 0, fin, left, cnt = 0;
    unsigned long long clk = 0;
    {
        if (w == 0) LST(1310 + j, rt32());
        if (lead) {
#pragma unroll 1
            for (int n = 0; n < SPIN_CAP; ++n) {
                CFENCE();
                const u32 a = ld4(rs, foff(F_QKV + lane)), b = ld4(rs, lane < 32 ? foff(F_QKV + 64 + lane) : hvo[0]);
                if (all_(a != 0u && b != (lane < 32 ? 0u : PSENT))) break;
                __builtin_amdgcn_s_sleep(1);
            }
        }
        if (lead) {
            t = rt32() + CH_T0; tn = t + PER0 / 2 + CH_REC;
            st16(rs, lane < 2 ? hint + (u32)lane * (MB / 4) : OOB, (v4u){lane == 0 ? t : tn, PER0, (lane == 0 ? t : tn) + DW0, t + DW0});
        } else {
#pragma unroll 1
            for (int n = 0; n < SPIN_CAP; ++n) {
                CFENCE();
                const u32 hv = ld4(rs, hvo[1]);
                t = rdl(hv, 0);
                if (rdl(hv, 1) < 400u) break;
                __builtin_amdgcn_s_sleep(1);
            }
        }
        clk = t - 100u;
    }
    const u32 tm0 = t + DW0;
    u32 zero = 0, hv = 0xFFFFu, pst = 1, pmp = OOB;
    const u32 psent = PSENT;
    lptr sw = lds + 2560 + w * 4;
    asm volatile("" : "+v"(zero), "+v"(sw));
    auto turn = [&](auto lc) __attribute__((always_inline)) {
        constexpr int L = decltype(lc)::value, M = L ^ 1;
        u32 done, rep, rd, lost, dh, dn, nt, za, pdur, mp; v4u hw;
        const int sc = sn[L];
        auto books = [&](auto ic) __attribute__((always_inline)) {
            constexpr int i = decltype(ic)::value;
            if constexpr (i == 0) { rep = max(rdl(hv, 2), rdl(hv, 4)); PINS(rep); }
            if constexpr (i == 1) { lost = (u32)(__builtin_amdgcn_ballot_w64(hv == (u32)sn[M]) >> 32); PINS(lost); }
            if constexpr (i == 2) { lost |= (rep >> 16) ^ (u32)sn[M]; PINS(lost); }
            if constexpr (i == 3) { lost |= ok[M] ^ 2u; rd = rep & 0xFFFFu; PINS(lost); PINS(rd); }
            if constexpr (i == 4) { lost = lost == 0u ? CH_UP * 8u : 0u; PINS(lost); }
            if constexpr (i == 5) { pdur = ((u32)clk - tp) * 8u; PINS(pdur); pdur = min(pdur, wav + CH_RISE); PINS(pdur); }
            if constexpr (i == 6) { pdur = (done & ok[M]) ? pdur : wav; PINS(pdur); }
            if constexpr (i == 7) { wav = max(pdur, wav - CH_FALL); PINS(wav); }
            if constexpr (i == 8) { nt = min(rd * 8u, hav + CH_RISE); PINS(nt); }
            if constexpr (i == 9) { nt = rd < 0x3FFFu ? nt : hav; PINS(nt); }
            if constexpr (i == 10) { hav = max(nt, hav - CH_FALL); PINS(hav); }
            if constexpr (i == 11) { mw = max(mw - 1u, CH_MW * 8u); PINS(mw); }
            if constexpr (i == 12) { mh = max(mh - 1u, CH_MH * 8u); PINS(mh); }
            if constexpr (i == 13) { rd = rd >> 14 == 1u ? lost : 0u; PINS(rd); }
            if constexpr (i == 14) { mw += rd; mh += lost - rd; PINS(mw); PINS(mh); }
            if constexpr (i == 15) { ok[M] = done ? 1u : (ok[M] & 1u) << 1; PINS(ok[M]); }
            if constexpr (i == 16) { dh = (hav + mh) / 8u + (sn[1] < 3 ? CH_VIS + CH_PRE : CH_VIS); PINS(dh); }
            if constexpr (i == 17) { dn = (wav + mw) / 8u + CH_VIS; PINS(dn); }
            if constexpr (i == 18) { nt = dw[L] + dh + (u32)max((int)((u32)clk + CH_WX - t), 0); PINS(nt); }
            if constexpr (i == 19) { dh += dn; dw[L] = dn; sn[M] += (int)done; PINS(dh); PINS(sn[M]); }
            if constexpr (i == 20) { rd = tn - t + (dh >> 1); PINS(rd); }
            if constexpr (i == 21) { lost = (u32)sc - 1u < NSEG - 2 ? CH_RTY : 0u; PINS(lost); nt += ok[L] != 1u ? lost : 0u; PINS(nt); }
            if constexpr (i == 22) { nt = t + (u32)max((int)nt, (int)rd); PINS(nt); }
            if constexpr (i == 23) { hw.x = nt; hw.y = dh; PIN(hw.x); PIN(hw.y); }
            if constexpr (i == 24) { hw.z = nt + dn; hw.w = tm0; PIN(hw); }
            if constexpr (i == 25) st16(rs, hso + L * (MB / 4), hw);
        };
        {
#define WT(k) do {} while (0)
            WT(0);
            v4u d[8];
            {
                u32 tmp;
                asm volatile(WAIT_TXT("%1", "%2") "buffer_load_dwordx4 %0, %3, %4, %5 offen sc1\n\ts_waitcnt lgkmcnt(0)"
                             : "=&v"(d[0]), "=&s"(tmp) : "s"(uu(t - 4u)), "v"(vo), "s"(srd), "s"(oo[0]) : "vcc", "scc", "memory");
                PINS(clk);
            }
            SFOR(i, 7)
                asm volatile("buffer_load_dwordx4 %0, %1, %2, %3 offen sc1" : "=v"(d[i + 1]) : "v"(vo), "s"(srd), "s"(oo[i + 1]) : "memory");
                SCHED();
                if constexpr (i == 0) { done = uu(pst) ^ 1u; PINS(done); }
                static_for<3 * i, 3 * i + 3>(books);
                SCHED();
            SEND
            LD4A(hv, hvo[L], 0);
            SCHED();
            WT(1);
            static_for<21, 26>(books);
            za = done ? pmp : OOB;
            st4s(rs, za, 0, psent);
            *(LAS u32*)(sw + L * 4096) = zero;
            f32x4 acc[5] = {};
            mp = mpo + (L * (MB / 4) + (u32)((sc + 1) & 3) * 32768u); pmp = mpo + (L * (MB / 4) + (u32)(sc & 3) * 32768u);
            asm volatile("" : "+v"(acc[0]), "+v"(acc[1]), "+v"(acc[2]), "+v"(acc[3]), "+v"(acc[4]), "+v"(mp), "+v"(pmp));
            SCHED();
            float f0[5], f1[5]; unsigned long long bad;
#define WSW(o_) asm volatile("v_permlane32_swap_b32 %0, %1" : "+v"(f0[o_]), "+v"(f1[o_]))
#define WAD(o_) asm volatile("v_add_f32_dpp %0, %1, %0 row_ror:8 row_mask:0xf bank_mask:0xf" : "+v"(f0[o_]) : "v"(f1[o_]))
#define WFO(o_) do { VADD(f0[o_], acc[o_][0], acc[o_][2]); VADD(f1[o_], acc[o_][1], acc[o_][3]); } while (0)
#define WWR(o_) *(LAS float*)(xw + L * 4096 + (o_) * 128) = f0[o_]
            u32 th, per, so, erl = early;
            auto next = [&](auto ic_) __attribute__((always_inline)) {
                constexpr int i = decltype(ic_)::value;
                if constexpr (i == 0) { so = (u32)sn[M] << 17; PINS(so); }
                if constexpr (i == 1) { so = (so & 0x60000u) + M * (MB / 2); PINS(so); }
                if constexpr (i == 2) { so = sn[M] + 1 < NSEG ? so : OOB; PINS(so); }
                if constexpr (i == 3) { early = sn[M] + 1 < NSEG ? PSENT : 0u; PIN(early); }
                if constexpr (i == 4) { vo = l16 + so; PIN(vo); }
                if constexpr (i == 5 && L) { fin = (u32)(sn[0] + sn[1] + 2); PINS(fin); }
                if constexpr (i == 6 && L) { fin = (fin | uu(left)) >> 7; PIN(fin); }
                if constexpr (i == 6 && !L) { cnt += 1u; PIN(cnt); left = cnt >> 9; PIN(left); }
                if constexpr (i == 7) { PIN(hv); th = rdl(hv, 0); PINS(th); }
                if constexpr (i == 8) { per = rdl(hv, 1); PINS(per); so = th - t; PINS(so); }
                if constexpr (i == 9) { per = (int)so < 20 ? per : 0u; PINS(per); }
                if constexpr (i == 10) { th += per; PINS(th); if constexpr (L) { per = rdl(hv, 3); PINS(per); } }
                if constexpr (i == 11) { tp = t; t = lead ? tn : th; PINS(t); }
                if constexpr (i == 12) { if constexpr (L) { fin |= per; PIN(fin); } tn = nt; }
            };
            auto slot = [&](auto ic_) __attribute__((always_inline)) {
                constexpr int i = decltype(ic_)::value;
                constexpr int NL = WO_NL, I0 = 40 - 5 * NL, pc = i < I0 ? i / 5 : 8 - NL + (i - I0) % NL, oc = i < I0 ? i % 5 : (i - I0) / NL;
                if constexpr (oc == 0) asm volatile("s_waitcnt vmcnt(%1)" : "+v"(d[pc]) : "i"(10 - pc));
                if constexpr (pc * 5 + oc < 32) SMF_A(acc[oc], d[pc], wo[pc][oc]); else SMF_V(acc[oc], d[pc], wo[pc][oc]);
                SCHED();
                if constexpr (i == 0) WT(2);
                if constexpr (i >= 5 && i < 13 && i != 9) next(IC<i - 5 - (i > 9)>{});
                if constexpr (i == 20) WT(4);
                constexpr int e = i - (I0 + NL + 1), o = e < 0 ? 0 : e / 3;
                if constexpr (e >= 0 && e % 3 == 0) { CADD(f0[o], acc[o][0], acc[o][2]); if constexpr (o > 0) WAD(o - 1); }
                if constexpr (e >= 0 && e % 3 == 1) { if constexpr (o > 0) { WWR(o - 1); SCHED(); } CADD(f1[o], acc[o][1], acc[o][3]); }
                if constexpr (e >= 0 && e % 3 == 2) {
                    if constexpr (o == 0) asm volatile("v_cmp_u_f32 %0, %1, %2" : "=s"(bad) : "v"(f0[0]), "v"(f1[0]));
                    else LGK(15);
                    WSW(o);
                }
                SCHED();
            };
            asm volatile(".rept %0\n\ts_nop 0\n\t.endr" :: "i"(AL_W));
            static_for<0, 2>(slot);
            if (__builtin_expect(__builtin_amdgcn_ballot_w64(d[0].x == erl) != 0ull, 0)) {
                asm volatile("s_waitcnt vmcnt(3)" :: "v"(d[1]), "v"(d[2]), "v"(d[3]), "v"(d[4]), "v"(d[5]), "v"(d[6]), "v"(d[7]) : "memory");
                SCHED();
                *(LAS u32*)(sw + L * 4096) = 1u;
                static_for<0, 7>(next);
            } else {
            static_for<2, 40>(slot);
            WT(5);
            static_assert(WO_NL == 3, "tail");
            if (__builtin_expect(bad != 0ull, 0)) {
                bool miss = false;
#pragma unroll
                for (int i = 0; i < 8; ++i) miss |= d[i].x == PSENT;
                if (all_(!miss)) {
#pragma unroll
                    for (int i = 0; i < 8; ++i) miss |= d[i].y == PSENT || d[i].z == PSENT || d[i].w == PSENT;
                }
                *(LAS u32*)(sw + L * 4096) = all_(!miss) ? 0u : 1u;
            }
            asm volatile("v_permlane32_swap_b32 %0, %1\n\ts_nop 1\n\tv_add_f32_dpp %0, %1, %0 row_ror:8 row_mask:0xf bank_mask:0xf\n\tv_add_f32 %2, %4, %6\n\tv_add_f32 %3, %5, %7"
                         : "+v"(f0[3]), "+v"(f1[3]), "=&v"(f0[4]), "=&v"(f1[4]) : "v"(acc[4][0]), "v"(acc[4][1]), "v"(acc[4][2]), "v"(acc[4][3]));
            WWR(3); SCHED();
            asm volatile("s_nop 0\n\tv_permlane32_swap_b32 %0, %1\n\ts_nop 1\n\tv_add_f32_dpp %0, %1, %0 row_ror:8 row_mask:0xf bank_mask:0xf" : "+v"(f0[4]), "+v"(f1[4]));
            WWR(4);
            }
            WT(6);
            f32x2 a, b, c, e; u32 mq; v4u sv;
            asm volatile("s_waitcnt lgkmcnt(0)\n\ts_barrier\n\tds_read_b128 %4, %6 offset:%7+2560\n\t"
                         "ds_read_b64 %0, %5 offset:%7\n\tds_read_b64 %1, %5 offset:%7+640\n\tds_read_b64 %2, %5 offset:%7+1280\n\tds_read_b64 %3, %5 offset:%7+1920"
                         : "=&v"(a), "=&v"(b), "=&v"(c), "=&v"(e), "=&v"(sv) : "v"(xr), "v"(zero), "i"(L * 4096) : "memory");
            asm volatile("s_waitcnt lgkmcnt(4)\n\tv_or_b32 %4, %6, %7\n\tv_or3_b32 %4, %4, %8, %9\n\tv_lshl_or_b32 %5, %4, 31, %10\n\t"
                         "s_waitcnt lgkmcnt(2)\n\tv_pk_add_f32 %0, %0, %1\n\ts_waitcnt lgkmcnt(0)\n\tv_pk_add_f32 %2, %2, %3\n\ts_nop 0\n\tv_pk_add_f32 %0, %0, %2"
                         : "+v"(a), "+v"(b), "+v"(c), "+v"(e), "=&v"(pst), "=&v"(mq) : "v"(sv.x), "v"(sv.y), "v"(sv.z), "v"(sv.w), "v"(mp) : "memory");
            st4s(rs, mq, 0, pkv(a));
            SCHED();
            asm volatile("s_memrealtime %0" : "=s"(clk));
            VMW(3); SCHED();
            static_for<7, 13>(next);
            WT(7);
        }
    };
    do {
        turn(IC<0>{});
        turn(IC<1>{});
    } while (uu(fin) == 0u);
    if (sn[0] + 1 < NSEG || sn[1] + 1 < NSEG) st4s(rs, tid == 0 ? foff(F_ERR) : OOB, 0, 1u);
    if (w == 0) LST(1900 + j, rt32());
    LDUMP(w == 0, 224 + j);
}

__global__ void __launch_bounds__(256, 1) k_main(Params p) {
    lptr lds = (lptr)(u32)16 - 16;
    if (blockIdx.x == 0) LST(256, rt32());
    unsigned long long pc; int len;
    asm volatile("s_getpc_b64 %0\n.Lk_main_here:\n\ts_mov_b32 %1, .Lk_end_here-.Lk_main_here" : "=s"(pc), "=s"(len));
    const rsrc_t rs = mk_rsrc(p.ws, W_END);
    const v2u e0 = ld8s(rs, threadIdx.x == 0 ? foff(F_EPOCH) : OOB, 0);
    const v4u code = __builtin_amdgcn_raw_buffer_load_b128(mk_rsrc((const void*)(pc & ~15ull), (u32)max(len, 0)), (((blockIdx.x >> 3) & 31u) * 256u + threadIdx.x) * 16u, 0, 0);
    CFENCE();
    auto conv_id = [](int role) {
        const int x = role >> 5, i = role & 31, no = x < 5 ? 5 : 10, og = x < 5 ? x - 3 : 2 * (x - 4) + (i >= 5), r = (x - 3) * 5 + i - no, q = r / 3;
        return x < 3 || i >= no + 5 || r >= 24 ? NCV : i < no ? ((og & 1) ? (og >> 1) * 5 + i % 5 : NCV) : r % 3 == 0 ? 20 + (q & 1) * 20 + (q >> 1) : 16 + (r % 3) * 8 + q;
    };
    int role = (int)(((u32)__builtin_amdgcn_s_getreg(20 | (3 << 11)) & 7u) * 32u + (blockIdx.x >> 3));
    Early e;
    v4u rv;
#pragma unroll 1
    for (int n = 0;; ++n) {
        conv_early(p, rs, conv_id(role), e);
        if (n) break;
        CFENCE();
        if (threadIdx.x == 0) {
            GAS unsigned long long* slots = (GAS unsigned long long*)(p.ws + W_CTRL + F_SLOT * 128);
            const unsigned long long tag = (unsigned long long)e0.y << 32 | (e0.x + 1u);
            u32 mine = (u32)role;
#pragma unroll 1
            for (int k = 0; k < 256 && __hip_atomic_exchange(slots + mine * 16, tag, __ATOMIC_RELAXED, __HIP_MEMORY_SCOPE_AGENT) == tag; ++k) mine = (mine + 1u) & 255u;
            lds_w16(lds, (v4u){mine, e0.x, e0.y, 0u});
        }
        BAR();
        rv = lds_r16(lds);
        BAR();
        if (uni((int)rv.x) == role) break;
        role = uni((int)rv.x);
    }
    const int x = role >> 5, i = role & 31, w = uni(threadIdx.x >> 6), no = x < 5 ? 5 : 10, nr = x < 7 ? 5 : 4, j = conv_id(role);
    const v2u ep = {uu(rv.y), uu(rv.z)};
    asm volatile("" :: "v"(code));
    if (x < 2) { role_head(p, lds, i >> 4, x * 16 + (i & 15), ep); __builtin_amdgcn_endpgm(); }
    if (x == 2) { role_wo(p, lds, i, ep); __builtin_amdgcn_endpgm(); }
    if (i >= no + nr) { role_qkv(p, lds, (x < 5 ? (x - 3) * 22 : 44 + (x - 5) * 17) + i - no - nr, ep); __builtin_amdgcn_endpgm(); }
    const bool cv = j >= NPRE || (j >= 20 && j < 24);
    if (j < NCV) conv_stage(p, rs, lds, j, (j & 3) * 2 + (j >= NPRE), cv ? 40 : 0, ep, e);
    if (i >= no) {
        if (!cv) role_row(p, lds, (j - 24) * 4 + w, ep);
        __builtin_amdgcn_endpgm();
    }
    const int og = x < 5 ? x - 3 : 2 * (x - 4) + (i >= 5);
    role_out(p, lds, og & 1, og >> 1, i % 5, ep);
}
__global__ void k_end() { asm volatile(".Lk_end_here:"); }

extern "C" void kernel_launch(void* const* d_in, const int* in_sizes, int n_in, void* d_out, int out_size, void* d_ws, size_t ws_size,
                              hipStream_t stream) {
    Params p;
    p.x = (const u16*)d_in[0]; p.mem0 = (const u16*)d_in[1]; p.Wq = (const u16*)d_in[2]; p.Wk = (const u16*)d_in[3];
    p.Wv = (const u16*)d_in[4]; p.Wo = (const u16*)d_in[5];
    p.bk = (const u16*)d_in[8]; p.bv = (const u16*)d_in[9]; p.shift = (const u16*)d_in[10]; p.ssmk = (const u16*)d_in[11]; p.ssmD = (const u16*)d_in[12];
    p.out = (u16*)d_out;
    p.ws = (unsigned char*)(((size_t)d_ws + 2 * MB - 1) & ~(size_t)(2 * MB - 1));
    (void)hipFuncSetAttribute((const void*)k_main, hipFuncAttributeMaxDynamicSharedMemorySize, LDS_BYTES);
    hipLaunchKernelGGL(k_main, dim3(256), dim3(256), LDS_BYTES, stream, p);
}
```

```cpp
#include <hip/hip_runtime.h>
#include <hip/hip_bf16.h>

typedef unsigned short u16;
typedef unsigned int u32;
typedef u32 v2u __attribute__((ext_vector_type(2)));
typedef u32 v4u __attribute__((ext_vector_type(4)));
typedef u32 v8u __attribute__((ext_vector_type(8)));
typedef float f32x2 __attribute__((ext_vector_type(2)));
typedef float f32x4 __attribute__((ext_vector_type(4)));
typedef float f32x16 __attribute__((ext_vector_type(16)));
typedef __bf16 bf16x2 __attribute__((ext_vector_type(2)));
typedef __bf16 bf16x8 __attribute__((ext_vector_type(8)));
typedef short s16x4 __attribute__((ext_vector_type(4)));
#define LAS __attribute__((address_space(3)))
#define GAS __attribute__((address_space(1)))
typedef LAS unsigned char* lptr;
#define DEV __device__ __forceinline__

constexpr int SEQ = 2048, DM = 640, NH = 32, DH = 128, NSEG = 64, NQ = NH * DH;

constexpr u32 MB = 1u << 20;
constexpr u32 W_ORING = 0;
constexpr u32 W_MRING = MB;
constexpr u32 W_PROBE = 2 * MB - 65536;
constexpr u32 W_CTRL = 4 * MB;
constexpr u32 CTRL_BYTES = 128 * 1024;
constexpr u32 ACT = SEQ * DM * 2;
constexpr u32 W_H1 = 5 * MB, W_Y = W_H1 + ACT, W_H2 = W_Y + ACT, W_X2 = W_H2 + ACT;
constexpr u32 W_KQ = 16 * MB;
constexpr u32 W_VO = W_KQ + 64 * MB;
constexpr u32 W_P = W_VO + 64 * MB;
constexpr u32 P_ITEM = 16 * 4 * DM * 4;
constexpr u32 W_KVT = W_P + 4 * NSEG * P_ITEM;
constexpr u32 W_SP = W_KVT + ACT;
constexpr u32 W_END = W_KVT + 3 * MB;
static_assert((size_t)W_END + 2 * MB <= 256ull * MB, "workspace");
constexpr u32 F_ERR = 0, F_QKV = 16, F_O = F_QKV + 192, F_P = F_O + 64, F_H2 = F_P + 64, F_CLR = F_H2 + 32,
              F_EPOCH = F_CLR + 32, F_SLOT = F_EPOCH + 1, F_PRE = F_SLOT + 256, F_N = F_PRE + 192;
constexpr int NCV = 44, NPRE = 40;
static_assert(F_N * 128 <= CTRL_BYTES, "ctrl");
DEV u32 foff(u32 idx) { return W_CTRL + idx * 128u; }

constexpr u32 OOB = 0x80000000u;
constexpr u32 PSENT = 0x7F817F81u;
constexpr int SPIN_CAP = 40000;
constexpr int LDS_BYTES = 160 * 1024;

#define LDUMP(cond, id) do {} while (0)
#define LMX(cond, k, s_, v) do {} while (0)
#define LS3(cond, slot, idx) do {} while (0)
#define LST(idx, v) do {} while (0)
#define LS2(cond, idx) do {} while (0)
struct Params {
    const u16 *x, *mem0, *Wq, *Wk, *Wv, *Wo, *bk, *bv, *shift, *ssmk, *ssmD;
    u16* out;
    unsigned char* ws;
};

#define INAUX 2
#define INNT "nt"
typedef __amdgpu_buffer_rsrc_t rsrc_t;
DEV rsrc_t mk_rsrc(const void* p, u32 bytes) { return __builtin_amdgcn_make_buffer_rsrc((void*)p, 0, bytes, 0x00020000); }
DEV v4u ld16s(rsrc_t r, u32 off, u32 so) { return __builtin_amdgcn_raw_buffer_load_b128(r, off, so, 16); }
DEV v2u ld8s(rsrc_t r, u32 off, u32 so) { return __builtin_amdgcn_raw_buffer_load_b64(r, off, so, 16); }
DEV v4u ldin16(rsrc_t r, u32 off, u32 so) { return __builtin_amdgcn_raw_buffer_load_b128(r, off, so, INAUX); }
DEV v2u ldin8(rsrc_t r, u32 off, u32 so) { return __builtin_amdgcn_raw_buffer_load_b64(r, off, so, INAUX); }
DEV u32 ld4(rsrc_t r, u32 off) { return __builtin_amdgcn_raw_buffer_load_b32(r, off, 0, 16); }
DEV void st16(rsrc_t r, u32 off, v4u v) { __builtin_amdgcn_raw_buffer_store_b128(v, r, off, 0, 16); }
DEV void st8s(rsrc_t r, u32 off, u32 so, v2u v) { __builtin_amdgcn_raw_buffer_store_b64(v, r, off, so, 16); }
DEV void st4s(rsrc_t r, u32 off, u32 so, u32 v) { __builtin_amdgcn_raw_buffer_store_b32(v, r, off, so, 16); }
#define DRAIN() asm volatile("s_waitcnt vmcnt(0)" ::: "memory")
#define VMW(n) asm volatile("s_waitcnt vmcnt(%0)" :: "i"(n) : "memory")
#define BAR() asm volatile("s_waitcnt lgkmcnt(0)\n\ts_barrier" ::: "memory")
#define SCHED() __builtin_amdgcn_sched_barrier(0)
#define CFENCE() asm volatile("" ::: "memory")
#define NOPS(n) asm volatile("s_nop %0" :: "i"(n))
#define PIN(x) asm volatile("" : "+v"(x))
#define PINS(x) asm volatile("" : "+s"(x))

template <int V> struct IC { static constexpr int value = V; };
template <int I, int N, class F> DEV void static_for(F&& f) { if constexpr (I < N) { f(IC<I>{}); static_for<I + 1, N>(f); } }
#define SFOR(var, n) static_for<0, n>([&](auto ic_) __attribute__((always_inline)) { constexpr int var = decltype(ic_)::value;
#define SEND });

DEV u32 pk(float a, float b) { bf16x2 v = {(__bf16)a, (__bf16)b}; return __builtin_bit_cast(u32, v); }
DEV float lo_f(u32 u) { return __builtin_bit_cast(float, u << 16); }
DEV float hi_f(u32 u) { return __builtin_bit_cast(float, u & 0xffff0000u); }
DEV f32x2 up2(u32 u) { return (f32x2){lo_f(u), hi_f(u)}; }
DEV u32 pk2(f32x2 v) { return pk(v.x, v.y); }
DEV float rbf(float a) { return (float)(__bf16)a; }
DEV v4u lds_r16(lptr p) { return *(LAS v4u*)p; }
DEV void lds_w16(lptr p, v4u v) { *(LAS v4u*)p = v; }
DEV v2u lds_tr(lptr p) { return __builtin_bit_cast(v2u, __builtin_amdgcn_ds_read_tr16_b64_v4i16((LAS s16x4*)p)); }
DEV v4u tr2(lptr p0, lptr p1) { const v2u a = lds_tr(p0), b = lds_tr(p1); return (v4u){a.x, a.y, b.x, b.y}; }
DEV v8u cat8(v4u a, v4u b) { return __builtin_shufflevector(a, b, 0, 1, 2, 3, 4, 5, 6, 7); }
DEV f32x4 mfma16(v4u a, v4u b, f32x4 c) { return __builtin_amdgcn_mfma_f32_16x16x32_bf16(__builtin_bit_cast(bf16x8, a), __builtin_bit_cast(bf16x8, b), c, 0, 0, 0); }
DEV f32x16 mfma32(v4u a, v4u b, f32x16 c) { return __builtin_amdgcn_mfma_f32_32x32x16_bf16(__builtin_bit_cast(bf16x8, a), __builtin_bit_cast(bf16x8, b), c, 0, 0, 0); }

DEV float f_(u32 u) { return __builtin_bit_cast(float, u); }
DEV u32 u_(float f) { return __builtin_bit_cast(u32, f); }
template <int CTRL> DEV float dpp(float v) { return f_((u32)__builtin_amdgcn_update_dpp(0, (int)u_(v), CTRL, 0xf, 0xf, false)); }
DEV float max16(float v) { auto r = __builtin_amdgcn_permlane16_swap(u_(v), u_(v), false, false); return fmaxf(f_(r[0]), f_(r[1])); }
DEV float max32(float v) { auto r = __builtin_amdgcn_permlane32_swap(u_(v), u_(v), false, false); return fmaxf(f_(r[0]), f_(r[1])); }
DEV float add16(float v) { auto r = __builtin_amdgcn_permlane16_swap(u_(v), u_(v), false, false); return f_(r[0]) + f_(r[1]); }
DEV float add32(float v) { auto r = __builtin_amdgcn_permlane32_swap(u_(v), u_(v), false, false); return f_(r[0]) + f_(r[1]); }
DEV float wave_sum(float v) {
    v += dpp<0xB1>(v);
    v += dpp<0x4E>(v);
    v += dpp<0x124>(v);
    v += dpp<0x128>(v);
    return add32(add16(v));
}
DEV bool all_(bool c) { return __builtin_amdgcn_ballot_w64(!c) == 0ull; }
DEV int uni(int v) { return __builtin_amdgcn_readfirstlane(v); }
DEV u32 uu(u32 v) { return (u32)__builtin_amdgcn_readfirstlane((int)v); }
DEV u32 rdl(u32 v, int l) { return (u32)__builtin_amdgcn_readlane((int)v, l); }
DEV u32 rt32() { return (u32)__builtin_amdgcn_s_memrealtime(); }

#define WSL 2
DEV void wait_flags(rsrc_t rs, u32 off, u32 want, bool& dead) {
    if (dead) return;
#pragma unroll 1
    for (int n = 0;; ++n) {
        CFENCE();
        const u32 v = ld4(rs, off);
        if (all_(off >= OOB || v >= want)) break;
        __builtin_amdgcn_s_sleep(WSL);
        if ((n & 63) == 63) { if (uni((int)ld4(rs, foff(F_ERR))) != 0) { dead = true; break; } }
        if (n > SPIN_CAP) { st4s(rs, foff(F_ERR), 0, 1u); dead = true; break; }
    }
    CFENCE();
}

DEV void pre_post(rsrc_t rs, int idx, u32 stage, v2u ep) { st8s(rs, threadIdx.x == 0 ? foff(F_PRE + idx) : OOB, 0, (v2u){ep.x + stage, ep.y}); }
DEV void pre_poll(rsrc_t rs, int first, int n, u32 stage, v2u ep) {
    const int lane = threadIdx.x & 63;
#pragma unroll 1
    for (int it = 0; it < SPIN_CAP; ++it) {
        CFENCE();
        const v2u a = ld8s(rs, foff(F_PRE + first + min(lane, n - 1)), 0), b = ld8s(rs, foff(F_PRE + first + min(lane + 64, n - 1)), 0);
        if (all_(a.y == ep.y && b.y == ep.y && max(a.x - ep.x - stage, b.x - ep.x - stage) < 8u - stage)) break;
        __builtin_amdgcn_s_sleep(2);
    }
    CFENCE();
}
DEV void pre_wait(rsrc_t rs, int first, int n, u32 stage, v2u ep) {
    if (uni(threadIdx.x >> 6) == 0) pre_poll(rs, first, n, stage, ep);
    BAR();
}
DEV u32 pol_wait(rsrc_t rs, v2u ep) {
    const int lane = threadIdx.x & 63;
    v4u a;
#pragma unroll 1
    for (int it = 0; it < SPIN_CAP; ++it) {
        CFENCE();
        a = ld16s(rs, foff(F_PRE + (lane < NPRE || lane >= 48 ? lane : 0)), 0);
        if (all_(a.y == ep.y && a.x - ep.x - 1u < 7u)) break;
        __builtin_amdgcn_s_sleep(2);
    }
    CFENCE();
    return uu(wave_sum(lane >= 48 ? (float)(int)a.z : 0.f) >= 0.f ? 1u : 0u);
}
#define PROBE_C 5
#define PROBE_T 780
#define NPROBE 32
DEV u32 pol_probe(const Params& p, rsrc_t rs, int id) {
    const int lane = threadIdx.x & 63;
    const u32 off0 = W_PROBE + (u32)(id >> 2) * 16384u + (u32)(id & 3) * 128u;
    const u32 tend = rt32() + PROBE_T;
    st4s(rs, lane < 32 ? off0 + (u32)lane * 512 : OOB, 0, 0u); DRAIN();
    int calm = 0;
    int d[2] = {1 << 16, 1 << 16};
#pragma unroll 1
    for (int q = 0; q < NPROBE && ((q & 1) || (calm < PROBE_C && (int)(rt32() - tend) < 0)); ++q) {
        const u32 half = (u32)(q ^ (q >> 1) ^ id) & 1u;
        const unsigned long long t0 = __builtin_amdgcn_s_memtime();
        const u32 v = ld4(rs, lane == 0 ? off0 + half * 8192 + (u32)(q >> 1) * 512 : OOB);
        asm volatile("s_waitcnt vmcnt(0)" :: "v"(v) : "memory");
        const int dt = (int)(__builtin_amdgcn_s_memtime() - t0);
        if (half) d[1] = min(d[1], dt); else d[0] = min(d[0], dt);
        calm += dt < 1100;
    }
    const u32 xcc = (u32)__builtin_amdgcn_s_getreg(20 | (3 << 11)) & 0xfu;
    return (u32)min(max((d[0] - d[1]) * ((xcc & 4u) ? -1 : 1), -250), 250);
}
DEV f32x2 ln_stats(const f32x2 (&x)[5]) {
    const float c = f_(uu(u_(x[0].x)));
    f32x2 d[5];
#pragma unroll
    for (int i = 0; i < 5; ++i) d[i] = x[i] - c;
    const f32x2 s2 = (d[0] + d[1]) + (d[2] + d[3]) + d[4], q2 = (d[0] * d[0] + d[1] * d[1]) + (d[2] * d[2] + d[3] * d[3] + d[4] * d[4]);
    float a = s2.x + s2.y, b = q2.x + q2.y, a2, b2;
#define LN_DPP(m) "v_add_f32_dpp %0, %0, %0 " m " row_mask:0xf bank_mask:0xf\n\tv_add_f32_dpp %1, %1, %1 " m " row_mask:0xf bank_mask:0xf\n\ts_nop 0\n\t"
    asm("s_nop 1\n\t" LN_DPP("quad_perm:[1,0,3,2]") LN_DPP("quad_perm:[2,3,0,1]") LN_DPP("row_ror:4")
        "v_add_f32_dpp %0, %0, %0 row_ror:8 row_mask:0xf bank_mask:0xf\n\tv_add_f32_dpp %1, %1, %1 row_ror:8 row_mask:0xf bank_mask:0xf\n\ts_nop 1\n\t"
        "v_permlane16_swap_b32 %0, %1\n\tv_add_f32 %0, %0, %1\n\tv_mov_b32 %1, %0\n\ts_nop 1\n\tv_permlane32_swap_b32 %0, %1\n\tv_add_f32 %0, %0, %1\n\ts_nop 0\n\t"
        "v_readlane_b32 %2, %0, 0\n\tv_readlane_b32 %3, %0, 16\n\ts_nop 1"
        : "+v"(a), "+v"(b), "=s"(a2), "=s"(b2));
    const float m = a2 * (1.0f / DM), var = fmaxf(b2 * (1.0f / DM) - m * m, 0.f);
    const float sd = rbf(__builtin_amdgcn_sqrtf(rbf(rbf(var) + 1e-5f)));
    return (f32x2){rbf(c + m), __builtin_amdgcn_rcpf(sd)};
}
DEV u32 pkv(f32x2 v) { u32 d; asm("v_cvt_pk_bf16_f32 %0, %1, %2" : "=v"(d) : "v"(v.x), "v"(v.y)); return d; }
DEV void store_h(rsrc_t rs, u32 base, int row, const u32 (&hp)[5]) {
    const u32 lane = threadIdx.x & 63;
    const u32 sw = (u32)(row & 15) * 16u, o8 = (lane >> 5) * 8192u + (((lane & 31u) * 8u) ^ sw), hb = base + (u32)(row >> 5) * (32 * DM * 2) + (u32)(row & 31) * 256u;
    st8s(rs, o8, hb, (v2u){hp[0], hp[1]});
    st8s(rs, o8, hb + 16384, (v2u){hp[2], hp[3]});
    st4s(rs, (lane * 4) ^ sw, hb + 32768, hp[4]);
}

#define DMA16(ldsb, vo, so) asm volatile("s_mov_b32 m0, %0\n\ts_nop 0\n\tbuffer_load_dwordx4 %1, %2, %3 offen sc1 lds" :: "s"(ldsb), "v"(vo), "s"(srd), "s"(so) : "memory")
#define DMA16I(ldsb, lo, vo, so, go) do { u32 so_; asm volatile("s_add_u32 m0, %1, %5\n\ts_add_u32 %0, %4, %6\n\tbuffer_load_dwordx4 %2, %3, %0 offen sc1 lds" \
                         : "=&s"(so_) : "s"(ldsb), "v"(vo), "s"(srd), "s"(so), "i"(lo), "i"(go) : "memory", "scc"); } while (0)
#define DMA32(ldsb, vo, so) asm volatile("s_mov_b32 m0, %0\n\ts_nop 0\n\tbuffer_load_dwordx4 %1, %2, %3 offen sc1 lds\n\tbuffer_load_dwordx4 %1, %2, %3 offen offset:1024 sc1 lds" \
                         :: "s"(ldsb), "v"(vo), "s"(srd), "s"(so) : "memory")
#define DMAIN(sr, ldsb, vo, so) asm volatile("s_mov_b32 m0, %0\n\ts_nop 0\n\tbuffer_load_dwordx4 %1, %2, %3 offen lds" :: "s"(ldsb), "v"(vo), "s"(sr), "s"(so) : "memory")
DEV v4u mk_srd(const void* q, u32 bytes) { return (v4u){uu((u32)(size_t)q), uu((u32)((size_t)q >> 32) & 0xffffu), bytes, 0x00020000u}; }
#define DMANT(sr, ldsb, vo, so) asm volatile("s_mov_b32 m0, %0\n\ts_nop 0\n\tbuffer_load_dwordx4 %1, %2, %3 offen " INNT " lds" :: "s"(ldsb), "v"(vo), "s"(sr), "s"(so) : "memory")
#define AHEAD(sr, vo, so) asm volatile("buffer_load_dwordx4 %0, %1, %2, %3 offen" : "+v"(junk) : "v"(vo), "s"(sr), "s"(so) : "memory")
#define MK_SRD() const v4u srd = mk_srd(p.ws, W_END)

constexpr int CV_W = 40960;
template <bool B> struct CvL {
    static constexpr int NF = B ? 5 : 3, NK = B ? 4 : 2, KP = 544 + NK * 256, CH = NF * 1024 + 4 * KP;
};
constexpr int CV_Y = 4 * CvL<false>::CH, CV_YP = 272;
static_assert(CV_Y + 32 * CV_YP <= CV_W && 2 * CvL<true>::CH <= CV_Y, "conv lds");
DEV int cv_chan(int u, int i) { return 16 * (u % 40) + 4 * i + u / 40; }
template <bool B> DEV void cv_filt(const Params& p, u32 dst, int d, int i) {
    const u32 lane = threadIdx.x & 63;
    const v4u sf = mk_srd(p.ssmk + d * SEQ, SEQ * 2);
    if (i == 0) DMANT(sf, dst, lane < 4u ? OOB : lane * 16u - 64u, 0);
    else DMANT(sf, uu(dst + i * 1024), i == CvL<B>::NF - 1 && lane >= 4u ? OOB : lane * 16u, i * 1024 - 64);
}
DEV v4u cv_kvld(rsrc_t rs, int d, int i) { return ld16s(rs, (threadIdx.x & 63) * 16u, uu(W_KVT + (u32)d * (SEQ * 2) + i * 1024)); }
template <bool B> DEV void cv_kvput(lptr F, const v4u* k) {
    const int lane = threadIdx.x & 63;
#pragma unroll
    for (int i = 0; i < CvL<B>::NK; ++i) lds_w16(F + CvL<B>::NF * 1024 + (lane & 3) * CvL<B>::KP + 512 + i * 256 + (lane >> 2) * 16, k[i]);
}
template <bool B> DEV void cv_zero(lptr F) {
    const int lane = threadIdx.x & 63;
#pragma unroll
    for (int i = 0; i < 4; ++i) lds_w16(F + (lane >> 5) * CvL<B>::CH + CvL<B>::NF * 1024 + i * CvL<B>::KP + (lane & 31) * 16, (v4u){0u, 0u, 0u, 0u});
}
#define M32V(d, a, b) asm volatile("v_mfma_f32_32x32x16_bf16 %0, %1, %2, %0" : "+v"(d) : "v"(a), "v"(b))
template <bool B, class G> DEV void cv_run(const Params& p, rsrc_t rs, lptr F, int d, u32 D, f32x4 sp, float (&y)[16], G&& gap) {
    constexpr int KP = CvL<B>::KP, FO = B ? 2048 : 0;
    const int lane = threadIdx.x & 63, n = lane & 31, hh = lane >> 5, c = lane & 3;
    lptr fa = F + 64 + (32 * hh + 16 * (c & 1) + 8 * ((lane >> 4) & 1) + 4 * (c >> 1) - (lane & 12)) * 2;
    lptr kl = F + CvL<B>::NF * 1024 + (32 + n - hh) * 16;
    v4u dg;
#pragma unroll
    for (int q = 0; q < 4; ++q) { const int k = (lane & 35) ? -1 : (n & 4) + (n >> 3); dg[q] = k == 2 * q ? D : k == 2 * q + 1 ? D << 16 : 0u; }
    v4u l[4], h[4]; v2u fc[4], fh[4];
    f32x16 acc[4];
#pragma unroll
    for (int j = 0; j < 4; ++j) { l[j] = lds_r16(kl + j * KP); if constexpr (B) h[j] = lds_r16(kl + j * KP + 512); }
#pragma unroll
    for (int j = 0; j < 2; ++j) { fc[j] = lds_tr(fa + FO - 32 * j); if constexpr (B) fh[j] = lds_tr(fa - 32 * j); }
    asm volatile("s_nop 1" : "+v"(dg));
#pragma unroll
    for (int j = 0; j < 4; ++j) asm volatile("v_mfma_f32_32x32x16_bf16 %0, %1, %2, 0" : "=&v"(acc[j]) : "v"(dg), "v"(B ? h[j] : l[j]));
    constexpr int U = B ? 2 : 4;
    kl -= 32 * U; fa -= 32;
    auto body = [&](bool first) __attribute__((always_inline)) {
#pragma unroll
        for (int z = 0; z < U; ++z) {
            const int c = 2 * (z & 1);
            const v4u f = __builtin_shufflevector(fc[c], fc[c + 1], 0, 1, 2, 3), g = __builtin_shufflevector(fh[c], fh[c + 1], 0, 1, 2, 3);
#pragma unroll
            for (int j = 0; j < 4; ++j) {
                M32V(acc[j], f, l[j]); l[j] = lds_r16(kl + j * KP + 32 * (U - z - 1));
                if (j < 2) fc[(c ^ 2) + j] = lds_tr(fa + FO + (z + 1) * 128 + 32 - 32 * j);
                if (first && j > 0) gap(5 * z + j - 1);
                SCHED();
            }
            if constexpr (B) {
#pragma unroll
                for (int j = 0; j < 4; ++j) {
                    M32V(acc[j], g, h[j]); h[j] = lds_r16(kl + j * KP + 512 + 32 * (U - z - 1));
                    if (j < 2) fh[(c ^ 2) + j] = lds_tr(fa + (z + 1) * 128 + 32 - 32 * j);
                    if (first && j > 1) gap(5 * z + j + 1);
                    SCHED();
                }
            }
        }
        kl -= 32 * U; fa += 128 * U;
    };
    if constexpr (B) body(true);
#pragma unroll 1
    for (int m = B ? 2 * U : 0; m < 32; m += 2 * U) body(false);
    asm volatile("s_nop 11" : "+v"(acc[0]), "+v"(acc[1]), "+v"(acc[2]), "+v"(acc[3]));
    const float o[3] = {acc[1][15] + acc[2][14] + acc[3][13], acc[2][15] + acc[3][14], acc[3][15]};
    if constexpr (!B) st16(rs, lane == 63 ? W_SP + (u32)d * 16u : OOB, (v4u){u_(o[0]), u_(o[1]), u_(o[2]), 0u});
#pragma unroll
    for (int i = 0; i < 16; ++i) {
        float v = acc[0][i];
#pragma unroll
        for (int j = 1; j < 4; ++j) if (j <= i) v += acc[j][i - j];
        if (i < 3) { const float e = f_((u32)__builtin_amdgcn_ds_bpermute((hh ? lane - 32 : lane + 31) * 4, (int)u_(o[i]))); v += lane == 0 ? sp[i] : e; }
        y[i] = v;
    }
}
DEV void cv_put(lptr F, int pr, const float (&y0)[16], const float (&y1)[16]) {
    const int lane = threadIdx.x & 63;
#pragma unroll
    for (int i = 0; i < 16; ++i) *(LAS u32*)(F + CV_Y + (lane & 31) * CV_YP + (lane >> 5) * 128 + i * 8 + pr * 4) = pk(y0[i], y1[i]);
}
DEV void cv_out(rsrc_t rs, lptr F, int u, int blk0) {
    const int lane = threadIdx.x & 63;
    lptr q = F + CV_Y + ((lane >> 2) & 3) * CV_YP + ((lane >> 4) * 4 + (lane & 3)) * 8;
    const u32 yo = W_Y + (u32)(((lane >> 2) * 8 + blk0) * DM + 4 * u + (lane & 3)) * 16u;
#pragma unroll
    for (int i = 0; i < 8; ++i) { lptr a = q + (i >> 1) * (8 * CV_YP) + (i & 1) * 128; st16(rs, yo + (u32)((i & 1) * 128 + (i >> 1)) * (DM * 16), tr2(a, a + 4 * CV_YP)); }
}
struct KvIn { v2u k[19], v[16]; v4u s[2]; };
template <class F> DEV void kvt_load(const Params& p, int tile, KvIn& in, F&& mid) {
    const int lane = threadIdx.x & 63;
    const rsrc_t rk = mk_rsrc(p.bk, ACT), rv = mk_rsrc(p.bv, ACT), rh = mk_rsrc(p.shift, DM * 8);
    const int row = (tile / 10) * 64 + (lane >> 4) * 16, col = (tile % 10) * 64 + (lane & 15) * 4;
    const u32 vo = (u32)(row * DM + col) * 2u, vp = row > 0 ? vo - 3 * (DM * 2) : OOB;
#pragma unroll
    for (int i = 0; i < 2; ++i) in.s[i] = ldin16(rh, (u32)col * 8u + i * 16, 0);
#pragma unroll
    for (int i = 0; i < 19; ++i) { if (i == 7) mid(); in.k[i] = i < 3 ? ldin8(rk, vp, i * (DM * 2)) : ldin8(rk, vo, (i - 3) * (DM * 2)); if (i >= 3) in.v[i - 3] = ldin8(rv, vo, (i - 3) * (DM * 2)); }
}
DEV void kvt_make(rsrc_t rs, lptr xt, int tile, const KvIn& in) {
    const int lane = threadIdx.x & 63;
#pragma unroll
    for (int c = 0; c < 2; ++c) {
        const v4u sv = in.s[c];
        const f32x2 sh[4] = {{lo_f(sv.x), lo_f(sv.z)}, {hi_f(sv.x), hi_f(sv.z)}, {lo_f(sv.y), lo_f(sv.w)}, {hi_f(sv.y), hi_f(sv.w)}};
        f32x2 kf[19];
#pragma unroll
        for (int i = 0; i < 19; ++i) kf[i] = up2(in.k[i][c]);
        u32 kv[16];
#pragma unroll
        for (int i = 0; i < 16; ++i) kv[i] = pk2(((kf[i] * sh[3] + kf[i + 1] * sh[2]) + (kf[i + 2] * sh[1] + kf[i + 3] * sh[0])) * up2(in.v[i][c]));
#pragma unroll
        for (int q = 0; q < 8; ++q) {
            const u32 sel = (q & 1) ? 0x07060302u : 0x05040100u; const u32* z = kv + (q >> 1);
            *(LAS v2u*)(xt + ((2 * c + (q & 1)) * 16 + (lane & 15)) * 144 + (lane >> 5) * 64 + (q >> 1) * 16 + ((lane >> 4) & 1) * 8) =
                (v2u){__builtin_amdgcn_perm(z[4], z[0], sel), __builtin_amdgcn_perm(z[12], z[8], sel)};
        }
    }
    const u32 o = W_KVT + (u32)(((tile % 10) * 64 + 4 * (lane >> 3)) * SEQ + (tile / 10) * 64 + (lane & 7) * 8) * 2u;
#pragma unroll
    for (int i = 0; i < 8; ++i) st16(rs, o + (u32)(32 * (i & 1) + (i >> 1)) * (SEQ * 2), lds_r16(xt + (8 * i + (lane >> 3)) * 144 + (lane & 7) * 16));
}
struct RowX { v2u a, b; u32 c; };
DEV void ln_row(rsrc_t rs, int row, const RowX& r) {
    const f32x2 x[5] = {up2(r.a.x), up2(r.a.y), up2(r.b.x), up2(r.b.y), up2(r.c)};
    const f32x2 st = ln_stats(x);
    u32 hp[5];
#pragma unroll
    for (int q = 0; q < 5; ++q) hp[q] = pk2(up2(pkv(x[q] - st.x)) * st.y);
    store_h(rs, W_H1, row, hp);
}
DEV v4u mem0_load(const Params& p, int j) {
    const u32 gt = (u32)(j * 256 + threadIdx.x) * 16u, n = ((gt >> 14) << 9) + ((gt & 8191u) >> 4);
    return ldin16(mk_rsrc(p.mem0, 8 * DM * 2), n < 640u ? ((2 * ((n >> 2) & 3) + (n & 1)) * DM + (n >> 4) * 16 + (n & 2) * 4) * 2u : OOB, 0);
}
DEV void pre_rings(rsrc_t rs, int j, v4u m) {
    const u32 gi = (u32)(j * 256 + threadIdx.x), gt = gi * 16u, R = NPRE * 4096, n = ((gt >> 14) << 9) + ((gt & 8191u) >> 4);
#pragma unroll
    for (int i = 0; i < 4; ++i) m[i] = n >= 640u ? PSENT : m[i] == PSENT ? 0x7FC17FC1u : m[i];
    st4s(rs, gi - 36u * 256u < F_CLR ? foff(gi - 36u * 256u) : OOB, 0, 0u);
#pragma unroll 1
    for (u32 o = gt; o < MB; o += R) st16(rs, W_ORING + o, (v4u){PSENT, PSENT, PSENT, PSENT});
    const u32 mo = gt < 4 * 32768 + 16384 ? W_MRING + gt : OOB;
    st16(rs, mo, m);
    st16(rs, mo + MB / 4, m);
    static_assert(NPRE * 4096 >= 4 * 32768 + 16384, "memory ring");
}
constexpr int KT0 = NPRE * 4, LN0 = NPRE * 8;
struct Early { RowX r[2]; v4u m; u32 vote; };
DEV void conv_early(const Params& p, rsrc_t rs, int j, Early& e) {
    const int lane = threadIdx.x & 63, w = uni(threadIdx.x >> 6), u = j * 4 + w;
    if (j < NPRE) {
        const rsrc_t rx = mk_rsrc(p.x, ACT);
#pragma unroll
        for (int i = 0; i < 2; ++i) {
            const u32 so = uu((u + i * KT0) * (DM * 2));
            e.r[i].a = __builtin_amdgcn_raw_buffer_load_b64(rx, lane * 8u, so, 0); e.r[i].b = __builtin_amdgcn_raw_buffer_load_b64(rx, lane * 8u + 512u, so, 0);
            e.r[i].c = __builtin_amdgcn_raw_buffer_load_b32(rx, lane * 4u + 1024u, so, 0);
        }
        e.m = mem0_load(p, j);
    } else if (j < NCV && w > 0) e.vote = pol_probe(p, rs, u - NPRE * 4);
}
DEV void conv_stage(const Params& p, rsrc_t rs, lptr lds, int j, int g0, int ge, v2u ep, Early& e) {
    const int tid = threadIdx.x, lane = tid & 63, w = uni(tid >> 6), u = j * 4 + w;
    lptr F = lds + w * CV_W;
    const u32 fb = uu(w * CV_W);
    const rsrc_t rd = mk_rsrc(p.ssmD, DM * 2);
    float y0[16], y1[16];
    KvIn in;
    if (j < NPRE) {
        constexpr int CH = CvL<false>::CH;
        if (w == 1) LST(1500 + j, rt32());
        pre_rings(rs, j, e.m);
        ln_row(rs, u, e.r[0]); ln_row(rs, u + KT0, e.r[1]);
        if (w == 1) LST(1550 + j, rt32());
        CFENCE();
        kvt_load(p, u, in, [&] { VMW(13); BAR(); pre_post(rs, j, 1u, ep); if (w == 0) LST(1450 + j, rt32()); });
        if (j == 20) LST(260, rt32());
        const u32 D = __builtin_amdgcn_raw_buffer_load_b16(rd, (u32)cv_chan(u, lane & 3) * 2u, 0, 0);
#pragma unroll 1
        for (int i = 0; i < 4; ++i) { const int d = cv_chan(u, i); cv_filt<false>(p, fb + i * CH, d, 0); cv_filt<false>(p, fb + i * CH, d, 1); cv_filt<false>(p, fb + i * CH, d, 2); }
        kvt_make(rs, F + CV_Y, u, in);
        cv_zero<false>(F); cv_zero<false>(F + 2 * CH);
        DRAIN(); BAR();
        pre_post(rs, j, 2u, ep);
        if (j == 20) LST(262, rt32());
        pre_wait(rs, 0, NPRE, 2u, ep);
        if (j == 20) LST(281, rt32());
        {
            v4u k[8];
#pragma unroll
            for (int i = 0; i < 8; ++i) k[i] = cv_kvld(rs, cv_chan(u, i >> 1), i & 1);
#pragma unroll
            for (int i = 0; i < 4; ++i) cv_kvput<false>(F + i * CH, k + 2 * i);
        }
        if (j == 20) LST(286, rt32());
#pragma unroll 1
        for (int i = 0; i < 4; i += 2) {
            cv_run<false>(p, rs, F + i * CH, cv_chan(u, i), rdl(D, i), f32x4{}, y0, [](int) {});
            cv_run<false>(p, rs, F + (i + 1) * CH, cv_chan(u, i + 1), rdl(D, i + 1), f32x4{}, y1, [](int) {});
            cv_put(F, i >> 1, y0, y1);
        }
        if (j == 20) LST(287, rt32());
        cv_out(rs, F, u, 0);
        if (j == 20) LST(263, rt32());
    } else {
        st16(rs, lane == 0 ? foff(F_PRE + 48 + u - NPRE * 4) : OOB, (v4u){ep.x + 1u, ep.y, w > 0 ? e.vote : 0u, 0u});
#pragma unroll 1
        for (int t = u + 128; t < 320; t += 16) { kvt_load(p, t, in, [] {}); kvt_make(rs, F, t, in); }
    }
    DRAIN(); BAR();
    if (g0 < ge) {
        constexpr int CH = CvL<true>::CH;
        pre_post(rs, j, 3u, ep);
        cv_zero<true>(F);
        if (w == 0) { pre_poll(rs, 64, 32, 3u, ep); pre_poll(rs, 0, NCV, 3u, ep); }
        BAR();
        v4u k[4];
        auto fetch = [&](u32 dst, int d, int i) { if (i < 5) cv_filt<true>(p, dst, d, i); else if (i < 9) k[i - 5] = cv_kvld(rs, d, i - 5); };
#pragma unroll
        for (int i = 0; i < 9; ++i) fetch(fb, cv_chan(g0 * 4 + w, 0), i);
        const u32 ch = (u32)cv_chan(min((g0 + 8 * (lane >> 2)) * 4 + w, 159), lane & 3);
        u32 D = __builtin_amdgcn_raw_buffer_load_b16(rd, ch * 2u, 0, 0);
        v4u sp = ld16s(rs, W_SP + ch * 16u, 0);
        asm volatile("" : "+v"(D), "+v"(sp));
#pragma unroll 1
        for (int g = g0, n = 0; g < ge; g += 8) {
            const int un = g * 4 + w;
            if (g == 16) { LST(282, rt32()); LST(288, __builtin_amdgcn_s_memtime()); }
#pragma unroll 1
            for (int i = 0; i < 4; i += 2, n += 2) {
                const int d1 = (int)rdl(ch, n + 1), d2 = (int)rdl(ch, n + 2);
                cv_kvput<true>(F, k);
                cv_run<true>(p, rs, F, 0, rdl(D, n), (f32x4){f_(rdl(sp.x, n)), f_(rdl(sp.y, n)), f_(rdl(sp.z, n)), 0.f}, y0, [&](int q) { fetch(fb + CH, d1, q); });
                cv_kvput<true>(F + CH, k);
                cv_run<true>(p, rs, F + CH, 326, rdl(D, n + 1), (f32x4){f_(rdl(sp.x, n + 1)), f_(rdl(sp.y, n + 1)), f_(rdl(sp.z, n + 1)), 0.f}, y1, [&](int q) { fetch(fb, d2, q); });
                cv_put(F, i >> 1, y0, y1);
            }
            cv_out(rs, F, un, 4);
            if (g == 16) { LST(283, rt32()); LST(289, __builtin_amdgcn_s_memtime()); }
        }
        DRAIN(); BAR();
    }
    pre_post(rs, j, 5u, ep);
    if (j == 20) LST(284, rt32());
    if (j == 3) LST(285, rt32());
}

#define M32A(first, d, a, b) do { if (first) asm volatile("v_mfma_f32_32x32x16_bf16 %0, %1, %2, 0" : "=&v"(d) : "a"(a), "v"(b)); \
                                  else asm volatile("v_mfma_f32_32x32x16_bf16 %0, %1, %2, %0" : "+v"(d) : "a"(a), "v"(b)); } while (0)
#define M32B(first, d, a, b) do { if (first) asm volatile("v_mfma_f32_32x32x16_bf16 %0, %1, %2, 0" : "=&v"(d) : "v"(a), "a"(b)); \
                                  else asm volatile("v_mfma_f32_32x32x16_bf16 %0, %1, %2, %0" : "+v"(d) : "v"(a), "a"(b)); } while (0)
#define VCVT(d, a, b) asm volatile("v_cvt_pk_bf16_f32 %0, %1, %2" : "=v"(d) : "v"(a), "v"(b))
#define VADD(d, a, b) asm volatile("v_add_f32 %0, %1, %2" : "=v"(d) : "v"(a), "v"(b))
#define LD4A(d, vo, so) asm volatile("buffer_load_dword %0, %1, %2, %3 offen sc1" : "=v"(d) : "v"(vo), "s"(srd), "s"(so) : "memory")
DEV u32 wst_src(int lane, int ld) { return (u32)((lane >> 4) * ld + 8 * ((lane & 15) ^ ((lane >> 4) << 2))) * 2u; }
DEV u32 wst_rd(int lane, int w) { const int q4 = (lane >> 2) & 3; return (u32)(q4 * 256 + ((w ^ q4) * 64) + ((lane >> 4) & 1) * 32 + (lane & 3) * 8); }

constexpr int QK_SEL = 81920;
#define QK_LEAD 12
#define QK_SMP 29
#define QK_YB 26
#define QK_W0 2
DEV void role_qkv(const Params& p, lptr lds, int c, v2u ep) {
    const int tid = threadIdx.x, lane = tid & 63, w = uni(tid >> 6), hh = lane >> 5, r32 = lane & 31;
    const int m = c >> 5, h = c & 31;
    const rsrc_t rs = mk_rsrc(p.ws, W_END);
    v4u wt[40];
    {
        const v4u sw = mk_srd(m == 0 ? p.Wq : m == 1 ? p.Wk : p.Wv, DM * NQ * 2), sx = mk_srd(p.x, ACT);
        const u32 vo = wst_src(lane, NQ) + (u32)h * (DH * 2), x8 = (u32)lane * 8u, x4 = (u32)lane * 4u;
        RowX xr[5];
        auto wfill = [&](int i0, int n) {
#pragma unroll 1
            for (int i = i0; i < i0 + n; ++i) DMAIN(sw, uu((4 * i + w) * 1024), vo, uu((16 * i + 4 * w) * (NQ * 2)));
        };
        wfill(0, QK_W0);
#pragma unroll
        for (int i = 0; i < 5; ++i)
            asm volatile("buffer_load_dwordx2 %0, %3, %5, %6 offen\n\tbuffer_load_dwordx2 %1, %3, %5, %6 offen offset:512\n\tbuffer_load_dword %2, %4, %5, %6 offen offset:1024"
                         : "=&v"(xr[i].a), "=&v"(xr[i].b), "=&v"(xr[i].c) : "v"(x8), "v"(x4), "s"(sx), "s"(uu((LN0 + 4 * c + w + 384 * i) * (DM * 2))) : "memory");
        wfill(QK_W0, 10 - QK_W0);
        if (c == 3) LST(275, rt32());
        VMW(10 - QK_W0);
        if (c == 3) LST(276, rt32());
#pragma unroll
        for (int i = 0; i < 5; ++i) {
            asm volatile("" : "+v"(xr[i].a), "+v"(xr[i].b), "+v"(xr[i].c));
            const int row = LN0 + 4 * c + w + 384 * i;
            if (i < 4 || row < SEQ) ln_row(rs, row, xr[i]);
            wfill(10 + 6 * i, 6);
        }
        if (c == 3) LST(264, rt32());
        lptr tb = lds + hh * 2048 + wst_rd(lane, w);
        SFOR(k, 40)
            if constexpr (k == 0) { VMW(15); BAR(); }
            if constexpr (k == 28) {
                if (w == 0) pre_poll(rs, 0, NPRE, 1u, ep);
                DRAIN(); BAR();
                pre_post(rs, 96 + c, 1u, ep);
            }
            lptr q = tb + (k >> 3) * 32768 + (k & 7) * 4096; wt[k] = tr2(q, q + 1024); asm volatile("" : "+a"(wt[k]));
        SEND
    }
    if (c == 3) LST(268, rt32());
    if (w == 0) LST(1350 + c, rt32());
    bool dead = false;
    int n0, n1;
    const u32 t16 = (u32)(w * 2048 + lane * 16), wb = uu(w * 2048);
    u32 ar[16];
#pragma unroll
    for (int j = 0; j < 16; ++j) ar[j] = (u32)(r32 * 256 + ((2 * (j & 7) + hh) ^ (r32 & 15)) * 16 + (j >> 3) * 49152);
    MK_SRD();
#define XRD(y, ks) lds_r16(lds + ar[((ks) & 7) + ((ks) >= 24 ? 8 : 0)] + ((ks) >> 3) % 3 * 16384 + (y) * 8192)
#define QM0(y, b) asm volatile("s_add_u32 m0, %0, %1" :: "s"(wb), "i"((b) * 16384 + (y) * 8192) : "scc", "memory")
#define QDMA(i, vo) asm volatile("buffer_load_dwordx4 %0, %1, %2 offen offset:%3 sc1 lds" :: "v"(vo), "s"(srd), "s"(((i) >> 1) * 8192), "i"(((i) & 1) * 1024) : "memory")
    const u32 h2flag = (lane < 16 && w == 0) ? foff(F_H2 + lane) : OOB;
    const u32 ol = (m == 2 ? (u32)(2 * w + (r32 >> 4)) * 1024u : (u32)((r32 >> 4) * 4 + w) * 1024u) + (u32)(hh * 16 + (r32 & 15)) * 16u
                 + (m == 2 ? W_VO : m == 1 ? W_KQ : W_KQ + 8192u) + (u32)h * 16384u;
    const u32 fo = tid < 2 ? foff(F_QKV + tid * 96 + c) : OOB;
    lptr selw = lds + QK_SEL + (w == 0 ? 0 : 64), selr = lds + QK_SEL;
    PIN(selw);
    static_assert(QK_LEAD > 8 && QK_LEAD % 2 == 0 && LN0 == 10 * 32, "X = 8 comes up, before any item of layer 1");
    static_assert(W_H2 - W_H1 == 2 * 64 * (32 * DM * 2), "h");
    auto run = [&](auto isv) __attribute__((always_inline)) {
        constexpr bool V = decltype(isv)::value;
        constexpr int YB = QK_YB;
        f32x16 ax, ay = {};
        v4u x[8], pp, pq;
        u32 xv = t16 + W_H1, ox = ol, oy = OOB, nn = 1, lt = 0, fcnt = 0, fa = OOB;
        asm volatile("" : "+v"(lt), "+v"(nn));
#pragma unroll
        for (int z = 0; z < 5; ++z) x[z] = XRD(1, 40 - YB + z);
#define QPK(r_, a1, a_, q_) do { if constexpr (V) VCVT(r_[q_], a1[4 * (a_) + 8 * ((q_) >> 1) + 2 * ((q_) & 1)], a1[4 * (a_) + 8 * ((q_) >> 1) + 2 * ((q_) & 1) + 1]); \
                                 else VCVT(r_[q_], a1[4 * (a_) + (q_)], a1[4 * (a_) + (q_) + 8]); } while (0)
#define QPK4(r_, a1, a_) do { QPK(r_, a1, a_, 0); QPK(r_, a1, a_, 1); QPK(r_, a1, a_, 2); QPK(r_, a1, a_, 3); } while (0)
        for (;;) {
            u32 ls = uu(lt), yv, fv, nx, need;
            if (ls != 0u && uu(nn) < 256u) pre_wait(rs, 96, 96, 1u, ep);
            else if (ls != 0u) {
                n1 = (int)(uu(nn) >> 8);
                CFENCE();
                const u32 hf = h2flag + (u32)(~n1 & 1) * 2048u;
                u32 f2 = ld4(rs, hf);
                asm volatile("s_waitcnt vmcnt(0)" : "+v"(f2) :: "memory");
                *(LAS u32*)(selw + 16) = (u32)__builtin_amdgcn_ballot_w64(f2 < (u32)n1) & 0xFFFFu;
                BAR();
                st4s(rs, fa, 0, fcnt);
                fa = OOB;
                if (n1 > NSEG) { LDUMP(w == 0, 64 + c); __builtin_amdgcn_endpgm(); }
                if (uu(*(LAS u32*)(selr + 16)) != 0u) {
                    if (w == 0) wait_flags(rs, hf, (u32)n1, dead);
                    BAR();
                }
            }
#define QT(k) do {} while (0)
            QT(0);
            SCHED();
            QM0(0, 0); NOPS(0); QDMA(0, xv); QDMA(1, xv); QM0(0, 1);
            SFOR(z, 80)
                constexpr int ks = (z + 40 - YB) % 40, zx = z - YB;
                constexpr auto upto = [](int q) { const int d = (q - YB + 49) % 40; return q + 6 + (d == 0 || d == 2 ? 1 : d == 1 ? 2 : 0); };
                if constexpr (zx >= 0 && zx < 40) { if constexpr (V) M32B(ks == 0, ax, x[z & 7], wt[ks]); else M32A(ks == 0, ax, wt[ks], x[z & 7]); }
                else { if constexpr (V) M32B(ks == 0, ay, x[z & 7], wt[ks]); else M32A(ks == 0, ay, wt[ks], x[z & 7]); }
                if constexpr (z == 78) lt = *(LAS u32*)selr;
#pragma unroll
                for (int q = upto(z - 1); q < upto(z); ++q) x[q & 7] = XRD(q % 80 < YB || q % 80 >= YB + 40, (q + 40 - YB) % 40);
                SCHED();
                (void)&srd; (void)&wb;
                if constexpr (z < 2) QDMA(z + 2, xv);
                if constexpr (z >= 2 && z < 14 && (z & 3) == 2) QM0(0, (z + 6) >> 2);
                if constexpr (z >= 3 && z < 15 && (z & 1)) QDMA((z + 5) >> 1, xv);
                if constexpr (z == 4) { n0 = uni((int)nn); n1 = n0 >> 8; n0 &= 255; PINS(n0); PINS(n1); }
                if constexpr (z == 8) { nx = n0 < NSEG ? W_H1 + (u32)n0 * (32 * DM * 2) : OOB; PINS(nx); }
                if constexpr (z == 12) { yv = t16 + nx; PIN(yv); }
                if constexpr (zx == -6) { QT(1); asm volatile("s_waitcnt vmcnt(6) lgkmcnt(0)\n\ts_barrier" ::: "memory"); QT(2); }
                if constexpr (zx == -5) st4s(rs, fa, 0, fcnt);
                if constexpr (zx == -4 || zx == 0 || zx == 4) QM0(1, (zx + 4) >> 2);
                if constexpr (zx >= -3 && zx < 8 && (zx & 1)) QDMA((zx + 3) >> 1, yv);
                if constexpr (zx == 2) QPK4(pp, ay, 0);
                if constexpr (zx == 6) QPK4(pq, ay, 1);
                if constexpr (zx == 8) st16(rs, oy, pp);
                if constexpr (zx == 9) st16(rs, oy + 512, pq);
                if constexpr (zx == 10) { QT(3); asm volatile("s_waitcnt vmcnt(9)\n\ts_barrier" ::: "memory"); QT(4); }
                if constexpr (zx == 11 || zx == 15) QM0(1, (zx + 1) >> 2);
                if constexpr (zx >= 12 && zx < 20 && (zx & 1) == 0) QDMA(zx >> 1, yv);
                if constexpr (zx == 21) { fcnt = lane == 0 ? (u32)n0 : (u32)n1; fa = fo; asm volatile("" : "+v"(fcnt), "+v"(fa)); }
                if constexpr (zx == 22) { oy = (ol + (u32)n0 * (NH * 16384u)) | (yv & OOB); PIN(oy); }
                if constexpr (zx == 23) { n0 = min(n0 + 1, NSEG); PINS(n0); nx = (u32)min(NSEG, n1 + QK_LEAD); PINS(nx); }
                if constexpr (zx == 24)
                    asm volatile("s_cmp_ge_u32 %1, %0\n\ts_cselect_b32 %0, %4, %1\n\ts_addc_u32 %2, %2, 0\n\ts_cmp_lt_u32 %0, 64\n\ts_cselect_b32 %3, 0, %2"
                                 : "+s"(nx), "+s"(n0), "+s"(n1), "=&s"(need) : "s"(n1 + 64) : "scc");
                if constexpr (zx == 25) { n0 += 1 - (int)min(nx >> 6, 1u); PINS(n0); }
                if constexpr (zx == 26) { nn = (u32)(n0 | n1 << 8); PIN(nn); }
                if constexpr (zx == 27) { ls = W_H1 + (nx + (nx & 64u)) * (32 * DM * 2); PINS(ls); }
                if constexpr (zx == 28) { xv = t16 + ls; PIN(xv); need = nx == 8u ? 255u : need; PINS(need); }
                if constexpr (zx == 34) { asm volatile("s_waitcnt vmcnt(%0) lgkmcnt(0)\n\ts_barrier" :: "i"(QK_SMP < 34 ? 7 : 6) : "memory"); QT(5); }
                if constexpr (zx == QK_SMP) { const u32 po = (nx & 1u) * 2048u; LD4A(fv, h2flag, po); }
                if constexpr (zx == 42) QPK4(pp, ax, 0);
                if constexpr (zx == 43) st16(rs, ox, pp);
                if constexpr (zx == 44) QPK4(pq, ax, 1);
                if constexpr (zx == 45) st16(rs, ox + 512, pq);
                if constexpr (zx == 47) { ox = ol + nx * (NH * 16384u); PIN(ox); }
                if constexpr (z == 74) { asm volatile("s_waitcnt vmcnt(2)" : "+v"(fv) :: "memory"); QT(6); ls = (u32)__builtin_amdgcn_ballot_w64(fv < need) & 0xFFFFu; PINS(ls); }
                if constexpr (z == 75) { lt = ls; PIN(lt); *(LAS u32*)selw = lt; }
                if constexpr (z == 77) asm volatile("s_waitcnt lgkmcnt(2)\n\ts_barrier" ::: "memory");
                SCHED();
            SEND
        }
    };
    if (m == 2) run(IC<1>{}); else run(IC<0>{});
}

DEV void role_out(const Params& p, lptr lds, int L, int kq, int ng, v2u ep) {
    const int tid = threadIdx.x, lane = tid & 63, w = uni(tid >> 6), hh = lane >> 5, r32 = lane & 31;
    const rsrc_t rs = mk_rsrc(p.ws, W_END);
    v4u wo[64];
    bool dead = false;
    {
        const v4u sw = mk_srd(p.Wo, DM * NQ * 2);
        const u32 vo = wst_src(lane, DM) + (u32)ng * 256u;
        auto fill = [&](int b) {
#pragma unroll 1
            for (int i = 0; i < 8; ++i) DMAIN(sw, uu((b % 5) * 32768 + (4 * i + w) * 1024), vo, uu((kq * 1024 + b * 128 + 16 * i + 4 * w) * (DM * 2)));
        };
#pragma unroll 1
        for (int b = 0; b < 5; ++b) fill(b);
        lptr tb = lds + hh * 1024 + wst_rd(lane, w);
        SFOR(b, 8)
            VMW(b < 4 ? 32 : 56 - 8 * b); BAR();
            lptr q = tb + (b % 5) * 32768;
#pragma unroll
            for (int k = 0; k < 8; ++k) { lptr a = q + (k >> 1) * 8192 + (k & 1) * 2048; wo[8 * b + k] = tr2(a, a + 4096); if (b < 7) asm volatile("" : "+a"(wo[8 * b + k])); else PIN(wo[8 * b + k]); }
            if constexpr (b < 3) { BAR(); fill(b + 5); }
        SEND
    }
    pre_wait(rs, 0, NPRE, 1u, ep);
    if (kq == 0 && ng == 0) LST(272 + L, rt32());
    MK_SRD();
    const u32 ofl = lane < 8 ? foff(F_O + L * 32 + kq * 8 + lane) : OOB, l16 = (u32)lane * 16u;
    const u32 fo = (tid == 0) ? foff(F_P + L * 32 + kq * 5 + ng) : OOB;
#define OM0(dst, b) asm volatile("s_add_u32 m0, %0, %1" :: "s"(dst), "i"((b) * 8192) : "scc", "memory")
#define ODMA(i, vo) asm volatile("buffer_load_dwordx4 %0, %1, %2 offen offset:%3 sc1 lds" :: "v"(vo), "s"(srd), "s"(((i) >> 1) * 16384), "i"(((i) & 1) * 1024) : "memory")
    auto first = [&](u32 dst, u32 vo) {
#pragma unroll
        for (int b = 0; b < 5; ++b) { OM0(dst, b); NOPS(0); ODMA(2 * b, vo); ODMA(2 * b + 1, vo); }
    };
    u32 pa = W_P + (u32)(L * NSEG) * P_ITEM + (u32)(2 * hh * 4 + kq) * (DM * 4) + (u32)(ng * 128 + w * 32 + r32) * 4u, fv = 0;
    u32 ov = W_VO + 8192u + (u32)(L * NSEG * NH + kq * 8) * 16384u + (u32)w * 2048u + l16;
    bool early = false;
    for (int s = 0; s < NSEG; ++s) {
        const u32 dst = uu((s & 1) * 65536 + w * 2048), ndst = dst ^ 65536u, need = s + 1 < NSEG ? (u32)s + 2u : 255u;
        lptr A = lds + (s & 1) * 65536 + l16;
        if (!early) {
            if (!all_(ofl == OOB || fv >= (u32)s + 1)) wait_flags(rs, ofl, (u32)s + 1, dead);
            LS3(L == 0 && w == 1, 1, kq * 5 + ng);
            first(dst, ov);
        }
#define OT(k) do {} while (0)
        OM0(dst, 5);
        asm volatile("s_waitcnt vmcnt(8)\n\ts_barrier" ::: "memory");
        OT(0);
        v4u o[64];
#pragma unroll
        for (int k = 0; k < 4; ++k) o[k] = lds_r16(A + k * 1024);
        f32x16 acc;
        u32 far, nv;
        SCHED();
        SFOR(k, 64)
            if constexpr (k < 56) M32B(k == 0, acc, o[k], wo[k]); else M32V(acc, o[k], wo[k]);
            if constexpr (k < 60) o[k + 4] = lds_r16(A + (k + 4) * 1024);
            SCHED();
            (void)&srd; (void)&dst; (void)&ndst;
            if constexpr (k < 12 && (k & 1) == 0) ODMA(10 + k / 2, ov);
            if constexpr (k == 3) asm volatile("s_waitcnt vmcnt(6)\n\ts_barrier" ::: "memory");
            if constexpr (k == 3 || k == 7) OM0(dst, (k + 21) >> 2);
            if constexpr (k == 12) { ov += NH * 16384u; PIN(ov); }
            if constexpr (k == 19) asm volatile("s_waitcnt vmcnt(6)\n\ts_barrier" ::: "memory");
            if constexpr (k == 20) LD4A(fv, ofl, 0);
            if constexpr (k == 35) asm volatile("s_waitcnt vmcnt(1)\n\ts_barrier" ::: "memory");
            if constexpr (k == 40) { asm volatile("s_waitcnt vmcnt(0)" : "+v"(fv) :: "memory"); far = (u32)__builtin_amdgcn_ballot_w64(fv < need) & 255u; PINS(far); }
            if constexpr (k == 41) { nv = far ? OOB : ov; PIN(nv); }
            if constexpr (k >= 42 && k < 62 && (k & 3) == 2) OM0(ndst, (k - 42) >> 2);
            if constexpr (k >= 43 && k < 63 && (k & 1)) ODMA((k - 43) >> 1, nv);
            if constexpr (k == 48) LD4A(fv, ofl, 0);
            if constexpr (k == 4) OT(1);
            if constexpr (k == 20) OT(2);
            if constexpr (k == 36) OT(3);
            if constexpr (k == 41) OT(4);
            SCHED();
        SEND
        OT(5);
        asm volatile("s_nop 11" : "+v"(acc), "+v"(pa));
#pragma unroll
        for (int i = 0; i < 8; ++i) st4s(rs, pa, (u32)((4 * (i >> 1) + (i & 1)) * (4 * DM * 4)), pk(acc[2 * i], acc[2 * i + 1]));
        asm volatile("s_waitcnt vmcnt(15)" : "+v"(fv) :: "memory");
        OT(6);
        LS3(L == 0 && w == 1, 2, kq * 5 + ng);
        early = far == 0u || all_(ofl == OOB || fv >= need);
        if (far != 0u && early) { first(ndst, ov); VMW(10); }
        else { fv = ld4(rs, ofl); asm volatile("s_waitcnt vmcnt(0)" : "+v"(fv) :: "memory"); }
        BAR();
        st4s(rs, fo, 0, (u32)s + 1);
        pa += P_ITEM;
        LS2(L == 0 && w == 1, 160 + kq * 5 + ng);
    }
    LDUMP(L == 0 && w == 1, kq * 5 + ng);
}

DEV void ln_stats2(const f32x2 (&x)[10], f32x2& mean, f32x2& rstd) {
    const f32x2 c = {f_(uu(u_(x[0].x))), f_(uu(u_(x[0].y)))};
    f32x2 d[10];
#pragma unroll
    for (int i = 0; i < 10; ++i) d[i] = x[i] - c;
    f32x2 a = ((d[0] + d[1]) + (d[2] + d[3])) + ((d[4] + d[5]) + (d[6] + d[7])) + (d[8] + d[9]);
    f32x2 b = (d[0] * d[0] + d[2] * d[2] + d[4] * d[4] + d[6] * d[6] + d[8] * d[8]) + (d[1] * d[1] + d[3] * d[3] + d[5] * d[5] + d[7] * d[7] + d[9] * d[9]);
    float a0, a1, b0, b1;
#define LN_DP4(m) "v_add_f32_dpp %0, %0, %0 " m " row_mask:0xf bank_mask:0xf\n\tv_add_f32_dpp %1, %1, %1 " m " row_mask:0xf bank_mask:0xf\n\t" \
                  "v_add_f32_dpp %2, %2, %2 " m " row_mask:0xf bank_mask:0xf\n\tv_add_f32_dpp %3, %3, %3 " m " row_mask:0xf bank_mask:0xf\n\t"
    asm("s_nop 1\n\t" LN_DP4("quad_perm:[1,0,3,2]") LN_DP4("quad_perm:[2,3,0,1]") LN_DP4("row_ror:4") LN_DP4("row_ror:8")
        "v_permlane16_swap_b32 %0, %1\n\tv_add_f32 %0, %0, %1\n\tv_permlane16_swap_b32 %2, %3\n\tv_add_f32 %2, %2, %3\n\ts_nop 1\n\t"
        "v_permlane32_swap_b32 %0, %2\n\tv_add_f32 %0, %0, %2\n\ts_nop 0\n\t"
        "v_readlane_b32 %4, %0, 0\n\tv_readlane_b32 %5, %0, 16\n\tv_readlane_b32 %6, %0, 32\n\tv_readlane_b32 %7, %0, 48\n\ts_nop 1"
        : "+v"(a.x), "+v"(b.x), "+v"(a.y), "+v"(b.y), "=s"(a0), "=s"(b0), "=s"(a1), "=s"(b1));
    const f32x2 m = (f32x2){a0, a1} * (1.0f / DM), v = (f32x2){b0, b1} * (1.0f / DM) - m * m;
    const f32x2 q = up2(pkv(up2(pkv((f32x2){fmaxf(v.x, 0.f), fmaxf(v.y, 0.f)})) + 1e-5f));
    const f32x2 sd = up2(pk(__builtin_amdgcn_sqrtf(q.x), __builtin_amdgcn_sqrtf(q.y)));
    mean = up2(pkv(c + m)); rstd = (f32x2){__builtin_amdgcn_rcpf(sd.x), __builtin_amdgcn_rcpf(sd.y)};
}
DEV void role_row(const Params& p, lptr lds, int id, v2u ep) {
    const int lane = threadIdx.x & 63, w = uni(threadIdx.x >> 6);
    const int pr = id >> 2, L = id & 1, par = (id >> 1) & 1;
    const rsrc_t rs = mk_rsrc(p.ws, W_END), rx = mk_rsrc(p.x, ACT), ro_ = mk_rsrc(p.out, ACT);
    bool dead = false;
    const u32 fp = lane < 20 ? foff(F_P + L * 32 + lane) : OOB, l16 = (u32)lane * 16u, l8 = (u32)lane * 8u, l4 = (u32)lane * 4u, fb = uu(w * CV_W);
    lptr F = lds + w * CV_W;
    MK_SRD();
    for (int s = par; s < NSEG; s += 2) {
        const u32 ro = (u32)(s * 32 + 2 * pr) * (DM * 2);
        if ((s & 6) == 0) {
            if ((s & 30) == 0) pre_poll(rs, 0, s < 32 ? NPRE : NCV, s < 32 ? 3u : 5u, ep);
            if (id == 0 && s == 0) LST(274, rt32());
            if (s == 48) st8s(rs, (id == 1 && lane == 0) ? foff(F_EPOCH) : OOB, 0, (v2u){ep.x + 8u, ep.y});
#pragma unroll 1
            for (int i = 0; i < 5; ++i) {
                const u32 d = fb + i * 2048, o = W_Y + (u32)(16 * pr + (s >> 3)) * 10240u + i * 2048;
                DMA32(d, l16, o); DMA32(d + 10240u, l16, o + 81920u);
            }
            DRAIN();
        }
        RowX xr[2];
        auto xload = [&](rsrc_t r, u32 o, auto aux) {
#pragma unroll
            for (int i = 0; i < 2; ++i) {
                xr[i].a = __builtin_amdgcn_raw_buffer_load_b64(r, l8, o + i * (DM * 2), aux.value); xr[i].b = __builtin_amdgcn_raw_buffer_load_b64(r, l8 + 512u, o + i * (DM * 2), aux.value);
                xr[i].c = __builtin_amdgcn_raw_buffer_load_b32(r, l4 + 1024u, o + i * (DM * 2), aux.value);
            }
        };
        if (L == 0) xload(rx, ro, IC<0>{});
        f32x2 y[10], x[10];
        {
            lptr ya = F + (s & 7) * 2 + l16, yc = F + (s & 7) * 2 + 2048 + (lane >> 1) * 16 + (lane & 1) * 5120;
#pragma unroll
            for (int i = 0; i < 10; ++i) {
                lptr q = i < 8 ? ya + (i & 3) * 2560 + (i >> 2) * 1024 : yc + (i & 1) * 2560;
                y[i] = (f32x2){lo_f(*(LAS u16*)q), lo_f(*(LAS u16*)(q + 10240))}; PIN(y[i]);
            }
        }
        wait_flags(rs, fp, (u32)s + 1, dead);
        LS3(L == 0, 3, ri);
        LMX(L == 0, 0, s, rt32());
        {
            const u32 o = uu(W_P + (u32)(L * NSEG + s) * P_ITEM + (u32)pr * (4 * DM * 4));
            if (L == 1) xload(rs, W_X2 + ro, IC<16>{});
            v4u a[4], b[4]; v2u c[4];
#pragma unroll
            for (int kq = 0; kq < 4; ++kq) { a[kq] = ld16s(rs, l16, o + kq * (DM * 4)); SCHED(); b[kq] = ld16s(rs, l16 + 1024u, o + kq * (DM * 4)); SCHED(); c[kq] = ld8s(rs, l8 + 2048u, o + kq * (DM * 4)); SCHED(); }
            const u32 u0[5] = {xr[0].a.x, xr[0].a.y, xr[0].b.x, xr[0].b.y, xr[0].c}, u1[5] = {xr[1].a.x, xr[1].a.y, xr[1].b.x, xr[1].b.y, xr[1].c};
#pragma unroll
            for (int i = 0; i < 5; ++i) { x[2 * i] = (f32x2){lo_f(u0[i]), lo_f(u1[i])}; x[2 * i + 1] = (f32x2){hi_f(u0[i]), hi_f(u1[i])}; }
#pragma unroll
            for (int kq = 0; kq < 4; ++kq)
#pragma unroll
                for (int i = 0; i < 10; ++i)
                    asm volatile("v_dot2_f32_bf16 %0, %2, %3, %0\n\tv_dot2_f32_bf16 %1, %2, %4, %1" : "+v"(x[i].x), "+v"(x[i].y)
                                 : "v"(i < 4 ? a[kq][i & 3] : i < 8 ? b[kq][i & 3] : c[kq][i & 1]), "s"(0x3F80u), "s"(0x3F800000u));
            asm volatile("s_nop 2" : "+v"(x[8]), "+v"(x[9]));
        }
        LS3(L == 0, 4, ri);
        f32x2 mean, rstd;
        ln_stats2(x, mean, rstd);
#pragma unroll
        for (int i = 0; i < 10; ++i) x[i] += (x[i] - mean) * (y[i] * rstd);
        u32 xp[2][5];
#pragma unroll
        for (int i = 0; i < 5; ++i) { xp[0][i] = pkv((f32x2){x[2 * i].x, x[2 * i + 1].x}); xp[1][i] = pkv((f32x2){x[2 * i].y, x[2 * i + 1].y}); }
        if (L == 0) {
#pragma unroll
            for (int i = 0; i < 5; ++i) { x[2 * i] = (f32x2){lo_f(xp[0][i]), lo_f(xp[1][i])}; x[2 * i + 1] = (f32x2){hi_f(xp[0][i]), hi_f(xp[1][i])}; }
            ln_stats2(x, mean, rstd);
#pragma unroll
            for (int i = 0; i < 10; ++i) x[i] = (x[i] - mean) * rstd;
#pragma unroll
            for (int r = 0; r < 2; ++r) {
                u32 hp[5];
#pragma unroll
                for (int i = 0; i < 5; ++i) hp[i] = pkv((f32x2){x[2 * i][r], x[2 * i + 1][r]});
                store_h(rs, W_H2, s * 32 + 2 * pr + r, hp);
            }
#pragma unroll
            for (int r = 0; r < 2; ++r) {
                st8s(rs, l8, W_X2 + ro + r * (DM * 2), (v2u){xp[r][0], xp[r][1]});
                st8s(rs, l8 + 512u, W_X2 + ro + r * (DM * 2), (v2u){xp[r][2], xp[r][3]});
                st4s(rs, l4 + 1024u, W_X2 + ro + r * (DM * 2), xp[r][4]);
            }
            LS3(L == 0, 5, ri);
            DRAIN();
            st4s(rs, lane == 0 ? foff(F_H2 + par * 16 + pr) : OOB, 0, (u32)s + 1);
        } else {
#pragma unroll
            for (int r = 0; r < 2; ++r) {
                st8s(ro_, l8, ro + r * (DM * 2), (v2u){xp[r][0], xp[r][1]});
                st8s(ro_, l8 + 512u, ro + r * (DM * 2), (v2u){xp[r][2], xp[r][3]});
                st4s(ro_, l4 + 1024u, ro + r * (DM * 2), xp[r][4]);
            }
        }
        if (pr == 0) LST(320 + L * 64 + s, rt32());
        LS2(L == 0, 224 + ri);
        LMX(L == 0, 1, s, rt32());
    }
    LDUMP(L == 0, 32 + ri);
}

constexpr float SCALE = 0.08838834764831845f, LOG2E = 1.4426950408889634f;
constexpr int C_K = 0, C_V = 8192, C_QX = 16384, C_MEM = 24576, C_Q = C_MEM + 10240, C_KM = C_Q + 2304, C_VM = C_KM + 2304, C_DUMP = C_VM + 2048,
              C_ZERO = C_DUMP + 1024, C_WL = C_ZERO + 128;
constexpr int NLF = 14;
static_assert(C_WL % 16 == 0 && C_WL + 4 * NLF * 2048 <= LDS_BYTES, "head lds");
#define CH_VIS 10
#define CH_H0 104
#define CH_W0 89
#define CH_MH 2
#define CH_MW 3
#define CH_REC 28
#define CH_RTY 32u
#define CH_RISE 20u
#define CH_FALL 3u
#define CH_PRE 9
#define CH_PRW 2
#define CH_T0 190
#define CH_UP 2
#define CH_WX 6
#define WAIT_TXT(tmp, t4) "s_memrealtime vcc\n\t.p2align 5\n1:\n\ts_waitcnt lgkmcnt(0)\n\ts_sub_u32 " tmp ", " t4 ", vcc_lo\n\ts_memrealtime vcc\n\ts_cmpk_lt_u32 " tmp ", 400\n\ts_cbranch_scc1 1b\n\t" \
                          "s_add_u32 " tmp ", " tmp ", 4\n\ts_cselect_b32 " tmp ", " tmp ", 0\n2:\n\ts_sub_u32 " tmp ", " tmp ", 1\n\ts_cbranch_scc0 2b\n\t"
DEV void wait_until(u32 t) { u32 tmp; asm volatile(WAIT_TXT("%0", "%1") "s_waitcnt lgkmcnt(0)" : "=&s"(tmp) : "s"(uu(t - 4u)) : "vcc", "scc", "memory"); }
#define SMF_A(acc, a, b) asm volatile("v_smfmac_f32_16x16x64_bf16 %0, %1, %2, %3" : "+v"(acc) : "v"(a), "a"(b), "v"(idx))
#define SMF_V(acc, a, b) asm volatile("v_smfmac_f32_16x16x64_bf16 %0, %1, %2, %3" : "+v"(acc) : "v"(a), "v"(b), "v"(idx))
#define MF0(d, a, b) asm volatile("v_mfma_f32_16x16x32_bf16 %0, %1, %2, 0" : "=&v"(d) : "v"(a), "v"(b))
#define MF1(d, a, b) asm volatile("v_mfma_f32_16x16x32_bf16 %0, %1, %2, %0" : "+v"(d) : "v"(a), "v"(b))
#define MF0H(d, a, b) asm volatile("v_mfma_f32_16x16x16_bf16 %0, %1, %2, 0" : "=&v"(d) : "v"(a), "v"(b))
#define MF1H(d, a, b) asm volatile("v_mfma_f32_16x16x16_bf16 %0, %1, %2, %0" : "+v"(d) : "v"(a), "v"(b))
DEV v8u tr_frag(lptr p0, lptr p1, lptr p2, lptr p3) { return cat8(tr2(p0, p1), tr2(p2, p3)); }
constexpr int qslot(int ks) { return (ks & 1) && ks < 8 ? ks >> 1 : -1; }
constexpr int kslot(int ks) { return ks < 4 ? 4 + ks : -1; }
constexpr int vslot(int ks) { return ks >= 4 ? 4 + ks : -1; }
DEV u32 ring_map(u32 off, u32 half) { return ((off >> 13) << 14) + (half << 13) + (off & 8191u); }
#define ZRD(x, i) x = __builtin_bit_cast(f32x4, lds_r16(ZP + 16 * (i)))
#define VMAX(d, a, b) asm volatile("v_max_f32 %0, %1, %2" : "=v"(d) : "v"(a), "v"(b))
#define VMAX3(d, a, b, c_) asm volatile("v_max3_f32 %0, %1, %2, %3" : "=v"(d) : "v"(a), "v"(b), "v"(c_))
#define VEXP(d, a) asm volatile("v_exp_f32 %0, %1" : "=v"(d) : "v"(a))
#define VMULC(d, a) asm volatile("v_mul_f32 %0, 0x3e0293ee, %1" : "=v"(d) : "v"(a))
#define CADD(d, a, b) do { d = (a) + (b); PIN(d); } while (0)
#define CMULC(d, a) do { d = (a) * CEXP; PIN(d); } while (0)
#define CCVT(d, a, b) do { d = pk(a, b); PIN(d); } while (0)
#define LGK(n) __builtin_amdgcn_s_waitcnt(0xC07F | (n) << 8)
#define SEL(d, a, b) asm volatile("v_cndmask_b32 %0, %1, %2, %3" : "=v"(d) : "v"(a), "v"(b), "s"(himask))
#define QPERM(d, a, q) asm volatile("v_mov_b32_dpp %0, %1 quad_perm:" q " row_mask:0xf bank_mask:0xf" : "=v"(d) : "v"(a))

#define AL_H 3
#define AL_W 0
#define HD_SMP 56
DEV void role_head(const Params& p, lptr lds, int L, int h, v2u ep) {
    const int tid = threadIdx.x, lane = tid & 63, w = uni(tid >> 6), g = lane >> 4, r = lane & 15;
    const rsrc_t rs = mk_rsrc(p.ws, W_END);
    v8u wq0[10], wq1[10], wk0[10], wk1[10], wv0[10], wv1[10];
    lptr WL = lds + C_WL + w * (NLF * 2048) + lane * 16;
    {
        constexpr int GS = 1056, CS = 48 * GS;
        const v4u sq = mk_srd(p.Wq, DM * NQ * 2), sk = mk_srd(p.Wk, DM * NQ * 2), sv = mk_srd(p.Wv, DM * NQ * 2);
        const u32 vo = (u32)(g * NQ + h * DH + 8 * (r ^ ((g & 1) << 3))) * 2u;
        auto fill = [&](int ch) {
#pragma unroll 1
            for (int i = 0; i < 4; ++i) {
                const u32 dst = uu((ch % 3) * CS + (4 * i + w) * GS), so = uu((ch * 64 + 16 * i + 4 * w) * (NQ * 2));
                DMAIN(sq, dst, vo, so); DMAIN(sk, dst + 16 * GS, vo, so); DMAIN(sv, dst + 32 * GS, vo, so);
            }
        };
        fill(0); fill(1); fill(2);
        const int q4 = r >> 2;
        lptr tb = lds + (4 * (g >> 1) + 2 * (q4 >> 1) + (g & 1)) * GS + (q4 & 1) * 256 + ((w * 64 + (r & 3) * 8) ^ ((q4 & 1) << 7));
        SFOR(ch, 10)
            VMW(ch < 8 ? 24 : 108 - 12 * ch);
            BAR();
            auto frag = [&](int m, int t) { lptr b = tb + (ch % 3) * CS + m * 16 * GS + t * 32; return tr_frag(b, b + 512, b + 8 * GS, b + 8 * GS + 512); };
            wq0[ch] = frag(0, 0); wq1[ch] = frag(0, 1); wk0[ch] = frag(1, 0); wk1[ch] = frag(1, 1); wv0[ch] = frag(2, 0); wv1[ch] = frag(2, 1);
            asm volatile("" : "+a"(wq0[ch]), "+a"(wk0[ch]), "+v"(wv0[ch]), "+v"(wv1[ch]));
            if constexpr (qslot(ch) < 0) asm volatile("" : "+a"(wq1[ch])); else asm volatile("" : "+v"(wq1[ch]));
            if constexpr (kslot(ch) < 0) asm volatile("" : "+a"(wk1[ch])); else asm volatile("" : "+v"(wk1[ch]));
            BAR();
            if constexpr (ch < 7) fill(ch + 3);
        SEND
#pragma unroll
        for (int ch = 0; ch < 10; ++ch) {
            if (qslot(ch) >= 0) { lds_w16(WL + qslot(ch) * 2048, wq1[ch].lo); lds_w16(WL + qslot(ch) * 2048 + 1024, wq1[ch].hi); }
            if (kslot(ch) >= 0) { lds_w16(WL + kslot(ch) * 2048, wk1[ch].lo); lds_w16(WL + kslot(ch) * 2048 + 1024, wk1[ch].hi); }
            if (vslot(ch) >= 0) { lds_w16(WL + vslot(ch) * 2048, wv1[ch].lo); lds_w16(WL + vslot(ch) * 2048 + 1024, wv1[ch].hi); }
        }
        CFENCE();
    }
    const u32 idx = (r & 2) ? 0xEEEEu : 0x4444u;
    unsigned long long himask = 0xCCCCCCCCCCCCCCCCull;
    PINS(himask);
    bool dead = false;
    if (h == 3) LST(266 + L, rt32());
    if (w == 0) LST(1240 + L * 32 + h, rt32());
    if (L == 1) {
        KvIn in;
        kvt_load(p, KT0 + 4 * h + w, in, [] {}); kvt_make(rs, lds + w * 9216, KT0 + 4 * h + w, in);
        DRAIN(); BAR();
        pre_post(rs, 64 + h, 3u, ep);
    }
    const u32 pol0 = pol_wait(rs, ep);
    if (h == 3 && L == 0) LST(277, rt32());
    const u32 l16 = (u32)lane * 16u;
    const u32 mring = W_MRING + (u32)L * (MB / 4) + pol0 * 8192u, hint = mring + 4 * 32768u;
    const u32 fkh = lane < 3 ? foff(F_QKV + L * 96 + lane * 32 + h) : lane < 7 ? hint + (u32)(lane - 3) * 4u : OOB;
    u32 tm = 0;
    MK_SRD();
    u32 ksrc = uu(W_KQ + (u32)(L * (NSEG - 1) * NH + h) * 16384u + w * 2048), vsrc = ksrc + (W_VO - W_KQ);
    u32 dk = uu(C_K + w * 2048), dv = dk + (C_V - C_K), dq = dk + (C_QX - C_K), dm = uu(C_MEM + w * 2048), dm2 = uu(w < 2 ? C_MEM + 8192 + w * 1024 : C_DUMP);
    asm volatile("" : "+s"(dk), "+s"(dv), "+s"(dq), "+s"(dm), "+s"(dm2));
    auto keys_wait = [&](u32 need) {
#pragma unroll 1
        for (int n = 0; !dead; ++n) {
            const bool beat = need + 2 * L > 2u;
            if (beat) wait_until(tm);
            const u32 hv = ld4(rs, lane == 7 ? foff(F_ERR) : fkh);
            if (beat) { tm = rdl(hv, 5); wait_until(rdl(hv, 3) - 30u); }
            if (((u32)__builtin_amdgcn_ballot_w64(ld4(rs, fkh) < need) & 7u) == 0u) break;
            dead = n > SPIN_CAP || rdl(hv, 7) != 0u;
            st4s(rs, dead ? foff(F_ERR) : OOB, 0, 1u);
        }
        CFENCE();
        DMA32(dk, l16, ksrc); DMA32(dv, l16, vsrc); DMA32(dq, l16, ksrc ^ 8192u);
    };
    if (L == 0) { st4s(rs, tid == 0 ? hint + 4096u + (u32)h * 128u : OOB, 0, 0u); keys_wait(1u); }
    lptr qw = lds + C_Q + (2 * g) * 288 + (32 * w + 2 * r) * 2;
    lptr kw = qw + (C_KM - C_Q); PIN(kw);
    lptr qr = lds + C_Q + (2 * (r >> 2) + (r & 1)) * 288 + g * 16;
    lptr LB = lds + l16, VX = lds + C_V + w * 2048 + l16;
    PIN(VX);
    if (tid >= 64 && tid < 70) { const u32 z = tid == 68 ? 0x3F803F80u : tid == 69 ? PSENT : 0u; lds_w16(lds + C_ZERO + (tid - 64) * 16, (v4u){z, z, z, z}); }
    u32 zaddr = 0; PIN(zaddr);
    lptr ZP = lds + zaddr + C_ZERO;
    const float CEXP = SCALE * LOG2E;
#define LF_RD(slot, hf) lf[slot][hf] = lds_r16(WL + (slot) * 2048 + (hf) * 1024)
    BAR();
    const u32 ofl = tid == 0 ? foff(F_O + L * 32 + h) : OOB;
    u32 oso = (((g | L) & 1) ? OOB : 0u) + W_ORING + (u32)L * (MB / 2) + ring_map((u32)((r >> 3) * 32768 + h * 1024 + w * 256 + (g >> 1) * 128 + (r & 7) * 16), pol0);
    unsigned long long vis[8];
#pragma unroll
    for (int i = 0; i < 8; ++i) vis[i] = __builtin_amdgcn_ballot_w64(16 * (i >> 2) + 4 * g + (i & 3) <= 16 * (w & 1) + r);
    u32 missed = 0;
    for (int s = -L; s < NSEG;) {
        u32 sv = (u32)max(s, 0), ms = mring + (sv & 3u) * 32768u + w * 2048, ms2 = mring + (sv & 3u) * 32768u + 16384u + (w & 1) * 1024;
        asm volatile("" : "+s"(ms), "+s"(ms2)); PIN(sv);
        f32x4 aq0, aq1, ak0, ak1;
        v4u lf[NLF][2];
        ZRD(aq0, 0); ZRD(aq1, 1); ZRD(ak0, 2); ZRD(ak1, 3);
#pragma unroll
        for (int i = 0; i < 4; ++i) { LF_RD(i, 0); LF_RD(i, 1); }
        SCHED();
        v4u A[10];
#define HT(k) do {} while (0)
        v4u kx[8], qf[4], kmf[4], vx[2], ones, P;
        float fa, fb, fc, fd, inv, sx[4], t[4], tm0, tm1; u32 pa, pb, fl, e01, e23, pmm;
        f32x4 av0, av1, s0, s1, sm, oa, ob, rsum;
        v2u vm, pm;
#define LW(sl) cat8(lf[sl][0], lf[sl][1])
#define PQ(ks, tl) do { if constexpr (!(tl)) SMF_A(aq0, A[ks], wq0[ks]); else if constexpr (qslot(ks) < 0) SMF_A(aq1, A[ks], wq1[ks]); else { const v8u lw_ = LW(qslot(ks)); SMF_V(aq1, A[ks], lw_); } } while (0)
#define PK(ks, tl) do { if constexpr (!(tl)) SMF_A(ak0, A[ks], wk0[ks]); else if constexpr (kslot(ks) < 0) SMF_A(ak1, A[ks], wk1[ks]); else { const v8u lw_ = LW(kslot(ks)); SMF_V(ak1, A[ks], lw_); } } while (0)
#define PV(ks, tl) do { if constexpr (!(tl)) SMF_V(av0, A[ks], wv0[ks]); else if constexpr (vslot(ks) < 0) SMF_V(av1, A[ks], wv1[ks]); else { const v8u lw_ = LW(vslot(ks)); SMF_V(av1, A[ks], lw_); } } while (0)
#define KXR(i) kx[i] = lds_r16(LB + C_K + ((i) & 1) * 4096 + ((i) >> 1) * 1024)
#define XW(q_) do { *(LAS u32*)(q_) = pa; *(LAS u32*)((q_) + 288) = pb; } while (0)
        auto slot = [&](auto zc) __attribute__((always_inline)) {
            constexpr int z = decltype(zc)::value;
            if constexpr (z < 14) PQ(z >> 1, z & 1);
            else if constexpr (z < 17) PQ(z - 7, 0);
            else if constexpr (z < 20) PQ(z - 10, 1);
            else if constexpr (z < 34) PK((z - 20) >> 1, z & 1);
            else if constexpr (z < 37) PK(z - 27, 0);
            else if constexpr (z < 40) PK(z - 30, 1);
            else if constexpr (z < 42) { if constexpr (z == 40) MF0(s0, kx[0], qf[0]); else MF0(s1, kx[1], qf[0]); }
            else if constexpr (z < 48) { if constexpr (z & 1) MF1(s1, kx[z - 40], qf[(z - 40) >> 1]); else MF1(s0, kx[z - 40], qf[(z - 40) >> 1]); }
            else if constexpr (z < 54) PV(z - 48, 0);
            else if constexpr (z == 54) MF0(sm, kmf[0], qf[0]);
            else if constexpr (z < 58) MF1(sm, kmf[z - 54], qf[z - 54]);
            else if constexpr (z < 62) PV(z - 52, 0);
            else if constexpr (z < 64) PV(z - 62, 1);
            else if constexpr (z == 64) MF0(oa, vx[0], P);
            else if constexpr (z == 65) MF0(ob, vx[1], P);
            else if constexpr (z == 66) MF0(rsum, ones, P);
            else if constexpr (z < 75) PV(z < 70 ? z - 63 : z == 70 ? 2 : z == 71 ? 7 : z == 72 ? 3 : z - 65, 1);
            else { const v2u ones2 = ones.lo; MF1H(rsum, ones2, pm); }
            if constexpr (z < 6) A[4 + z] = lds_r16(LB + C_MEM + (4 + z) * 1024);
            if constexpr (z >= 6 && z < 10) LF_RD(4 + ((z - 6) >> 1), z & 1);
            if constexpr (z >= 14 && z < 18) LF_RD(6 + ((z - 14) >> 1), z & 1);
            if constexpr (z >= 22 && z < 28) KXR(z - 22);
            if constexpr (z == 28) XW(qw);
            if constexpr (z == 29 || z == 30) KXR(z - 23);
            if constexpr (z == 32 || z == 47) asm volatile("s_barrier" ::: "memory");
            if constexpr (z >= 33 && z < 37) qf[z - 33] = lds_r16(qr + (z - 33) * 64);
            if constexpr (z == 20) ZRD(av0, 0);
            if constexpr (z == 43) XW(kw);
            if constexpr (z == 44 || z == 45) vx[z - 44] = lds_r16(VX + (z - 44) * 1024);
            if constexpr (z >= 48 && z < 52) kmf[z - 48] = lds_r16(qr + (C_KM - C_Q) + (z - 48) * 64);
            if constexpr (z == 52) ZRD(av1, 1);
            if constexpr (z == 37) ones = lds_r16(ZP + 64);
            if constexpr (z >= 57 && z < 64) LF_RD(8 + ((z - 57) >> 1), (z - 57) & 1);
            if constexpr (z >= 65 && z < 70) LF_RD(8 + ((z - 58) >> 1), (z - 58) & 1);
            if constexpr (z == HD_SMP) LD4A(fl, fkh, 0);
            SCHED();
            if constexpr (z == 19) HT(2);
            if constexpr (z == 32) HT(3);
            if constexpr (z == 47) HT(4);
            if constexpr (z == 64) HT(5);
            if constexpr (z == 19) LGK(4);
            if constexpr (z == 21 || z == 41) LGK(1);
            if constexpr (z == 56 || z == 73) LGK(0);
            if constexpr (z == 31 || z == 46) LGK(2);
            if constexpr (z == 39) LGK(4);
            if constexpr (z >= 53 && z < 56) LGK(57 - z);
            if constexpr (z == 64) LGK(3);
            if constexpr (z == 68) LGK(5);
            if constexpr (z >= 70 && z < 73) LGK(74 - z);
            SCHED();
            if constexpr (z == 18) CADD(fa, aq0[0], aq0[2]);
            if constexpr (z == 19) CADD(fb, aq0[1], aq0[3]);
            if constexpr (z == 20) CMULC(fa, fa);
            if constexpr (z == 21) CADD(fc, aq1[0], aq1[2]);
            if constexpr (z == 22) CADD(fd, aq1[1], aq1[3]);
            if constexpr (z == 23) CMULC(fc, fc);
            if constexpr (z == 24) CMULC(fb, fb);
            if constexpr (z == 25) CMULC(fd, fd);
            if constexpr (z == 26) CCVT(pa, fa, fc);
            if constexpr (z == 27) CCVT(pb, fb, fd);
            if constexpr (z == 38) CADD(fa, ak0[0], ak0[2]);
            if constexpr (z == 39) CADD(fb, ak0[1], ak0[3]);
            if constexpr (z == 41) { CADD(fc, ak1[0], ak1[2]); CADD(fd, ak1[1], ak1[3]); }
            if constexpr (z == 42) { CCVT(pa, fa, fc); CCVT(pb, fb, fd); }
            if constexpr (z >= 49 && z < 53) SEL(sx[z - 49], s0[z - 49], s1[z - 49]);
            if constexpr (z >= 53 && z < 57) VEXP(t[z - 53], sx[z - 53]);
            if constexpr (z == 57) CCVT(e01, t[0], t[1]);
            if constexpr (z == 58) CCVT(e23, t[2], t[3]);
            if constexpr (z == 59) QPERM(P.x, e01, "[0,1,0,1]");
            if constexpr (z == 60) QPERM(P.z, e01, "[2,3,2,3]");
            if constexpr (z == 61) QPERM(P.y, e23, "[0,1,0,1]");
            if constexpr (z == 62) QPERM(P.w, e23, "[2,3,2,3]");
            if constexpr (z == 63) CADD(fa, av0[0], av0[2]);
            if constexpr (z == 64) { asm volatile("" :: "v"(sm)); VEXP(tm0, sm[0]); }
            if constexpr (z == 65) VEXP(tm1, sm[1]);
            if constexpr (z == 66) CADD(fb, av0[1], av0[3]);
            if constexpr (z == 67) CCVT(pmm, tm0, tm1);
            if constexpr (z >= 68 && z < 72) SEL(oa[z - 68], oa[z - 68], ob[z - 68]);
            if constexpr (z == 72) asm volatile("v_cndmask_b32 %0, %1, 0, %2" : "=v"(pm.x) : "v"(pmm), "s"(himask));
            if constexpr (z == 73) asm volatile("v_cndmask_b32 %0, 0, %1, %2" : "=v"(pm.y) : "v"(pmm), "s"(himask));
            if constexpr (z == 74) LGK(15);
            if constexpr (z == 75) CCVT(vm.x, fa, fb);
            SCHED();
        };
        {
            u32 tmp;
            asm volatile("s_mov_b32 m0, %2\n\t" WAIT_TXT("%0", "%1") "buffer_load_dwordx4 %4, %5, %6 offen sc1 lds\n\tbuffer_load_dwordx4 %4, %5, %6 offen offset:1024 sc1 lds\n\t"
                         "s_mov_b32 m0, %3\n\ts_nop 0\n\tbuffer_load_dwordx4 %4, %5, %7 offen sc1 lds\n\t.p2align 6\n\t.rept %8\n\ts_nop 0\n\t.endr\n\ts_waitcnt vmcnt(0) lgkmcnt(0)\n\ts_barrier"
                         : "=&s"(tmp) : "s"(uu(tm - 4u)), "s"(dm), "s"(dm2), "v"(l16), "s"(srd), "s"(ms), "s"(ms2), "i"(AL_H) : "vcc", "scc", "memory");
        }
        HT(1);
#pragma unroll
        for (int ks = 0; ks < 4; ++ks) A[ks] = lds_r16(LB + C_MEM + ks * 1024);
        SCHED();
        st4s(rs, ofl, 0, sv);
        SCHED();
        static_for<0, 76>(slot);
        asm volatile("s_nop 2\n\tv_add_f32 %1, %3, %5\n\tv_add_f32 %2, %4, %6\n\tv_cvt_pk_bf16_f32 %0, %1, %2"
                     : "=v"(vm.y), "=&v"(fc), "=&v"(fd) : "v"(av1[0]), "v"(av1[1]), "v"(av1[2]), "v"(av1[3]));
        __builtin_amdgcn_s_waitcnt(0xCF7F);
        asm volatile("s_nop 0\n\tv_mfma_f32_16x16x16_bf16 %0, %2, %3, %0\n\tv_rcp_f32 %1, %4" : "+v"(oa), "=&v"(inv) : "v"(vm), "v"(pm), "v"(rsum[0]), "v"(rsum));
        SCHED();
        if (__builtin_expect(__builtin_amdgcn_ballot_w64(u_(rsum[0]) - 0x21800000u > 0x3C000000u) != 0ull, 0)) {
            DRAIN(); SCHED();
            bool miss = false;
#pragma unroll 1
            for (int i = 0; i < 10; ++i) { const v4u a = lds_r16(LB + C_MEM + i * 1024); miss |= a.x == PSENT || a.y == PSENT || a.z == PSENT || a.w == PSENT; }
            if (missed < SPIN_CAP && !all_(!miss)) {
                *(LAS u32*)(lds + C_DUMP + (w == 0 ? 0 : 64)) = ld4(rs, foff(F_ERR));
                asm volatile("" : "+v"(fl) :: "memory");
                tm = rdl(fl, 5);
                BAR();
                missed = uu(*(LAS u32*)(lds + C_DUMP)) != 0u ? SPIN_CAP : missed + 1;
                st4s(rs, missed == SPIN_CAP ? foff(F_ERR) : OOB, 0, 1u);
                BAR();
                continue;
            }
            float mx = fmaxf(fmaxf(fmaxf(sx[0], sx[1]), fmaxf(sx[2], sx[3])), fmaxf(sm[0], sm[1]));
            mx = max32(max16(fmaxf(mx, dpp<0x4E>(mx))));
#pragma unroll
            for (int i = 0; i < 4; ++i) t[i] = __builtin_amdgcn_exp2f(sx[i] - mx);
            e01 = pk(t[0], t[1]); e23 = pk(t[2], t[3]);
            pmm = pk(__builtin_amdgcn_exp2f(sm[0] - mx), __builtin_amdgcn_exp2f(sm[1] - mx));
            asm volatile("v_cndmask_b32 %0, %2, 0, %3\n\tv_cndmask_b32 %1, 0, %2, %3" : "=&v"(pm.x), "=&v"(pm.y) : "v"(pmm), "s"(himask));
            asm volatile("s_nop 1" : "+v"(e01), "+v"(e23), "+v"(pm));
            QPERM(P.x, e01, "[0,1,0,1]"); QPERM(P.z, e01, "[2,3,2,3]"); QPERM(P.y, e23, "[0,1,0,1]"); QPERM(P.w, e23, "[2,3,2,3]");
            NOPS(1);
            MF0(rsum, ones, P); MF0(oa, vx[0], P); MF0(ob, vx[1], P);
            NOPS(7);
            { const v2u ones2 = ones.lo; MF1H(rsum, ones2, pm); }
#pragma unroll
            for (int i = 0; i < 4; ++i) SEL(oa[i], oa[i], ob[i]);
            NOPS(1);
            MF1H(oa, vm, pm);
            asm volatile("s_nop 7\n\tv_rcp_f32 %0, %1" : "=v"(inv) : "v"(rsum[0]), "v"(rsum));
        }
        {
            v4u oc; f32x2 u0 = oa.lo, u1 = oa.hi; const f32x2 i2 = {inv, inv};
            asm volatile("s_nop 0\n\tv_pk_mul_f32 %0, %0, %2\n\tv_pk_mul_f32 %1, %1, %2" : "+v"(u0), "+v"(u1) : "v"(i2));
            __builtin_amdgcn_s_waitcnt(0xCF7F);
            asm volatile("v_cvt_pk_bf16_f32 %0, %4, %5\n\tv_cvt_pk_bf16_f32 %2, %4, %5\n\tv_cvt_pk_bf16_f32 %1, %6, %7\n\tv_cvt_pk_bf16_f32 %3, %6, %7\n\t"
                         "v_permlane16_swap_b32 %0, %2\n\ts_nop 0\n\tv_permlane16_swap_b32 %1, %3"
                         : "=&v"(oc.x), "=&v"(oc.y), "=&v"(oc.z), "=&v"(oc.w) : "v"(u0.x), "v"(u0.y), "v"(u1.x), "v"(u1.y));
            st16(rs, oso, oc);
        }
        SCHED();
        unsigned long long clk;
        asm volatile("s_memrealtime %0" : "=s"(clk) :: "memory");
        HT(6);
        v4u tq[4], tk[8], tv[2];
        f32x4 c0, c1, cm;
        u32 oo, kvo; bool kvok;
        u32 ln = l16; PIN(ln);
        lptr QX = lds + C_QX + (w & 1) * 4096 + ln, VO = lds + C_V + (w ^ 1) * 2048 + ln, VMX = lds + C_VM + w * 512 + (ln >> 1);
#pragma unroll
        for (int i = 0; i < 2; ++i) { tq[i] = lds_r16(QX + i * 1024); kmf[i] = lds_r16(qr + (C_KM - C_Q) + i * 64); tk[2 * i] = lds_r16(LB + C_K + i * 1024); tk[2 * i + 1] = lds_r16(LB + C_K + 4096 + i * 1024); }
        *(LAS v2u*)VMX = vm;
        SCHED();
        SFOR(i, 12)
            if constexpr (i == 0) MF0(cm, kmf[0], tq[0]); else if constexpr (i == 1) MF0(c0, tk[0], tq[0]); else if constexpr (i == 2) MF0(c1, tk[1], tq[0]);
            else if constexpr (i % 3 == 0) MF1(cm, kmf[i / 3], tq[i / 3]); else if constexpr (i % 3 == 1) MF1(c0, tk[2 * (i / 3)], tq[i / 3]); else MF1(c1, tk[2 * (i / 3) + 1], tq[i / 3]);
            if constexpr (i < 2) kmf[2 + i] = lds_r16(qr + (C_KM - C_Q) + (2 + i) * 64);
            if constexpr (i == 0) { tq[2] = lds_r16(QX + 2048); tk[4] = lds_r16(LB + C_K + 2048); }
            if constexpr (i == 1) { tk[5] = lds_r16(LB + C_K + 6144); tq[3] = lds_r16(QX + 3072); }
            if constexpr (i == 2) { tk[6] = lds_r16(LB + C_K + 3072); tk[7] = lds_r16(LB + C_K + 7168); }
            if constexpr (i == 3) { tv[0] = lds_r16(VO); tv[1] = lds_r16(VO + 1024); }
            if constexpr (i == 4) { oo = ln + (ln & 0x300u) + ((vsrc ^ 8192u) + (u32)((w & 1) * 256) + ((u32)s & OOB)); PIN(oo); }
            if constexpr (i == 5) { ksrc += NH * 16384; vsrc += NH * 16384; PINS(ksrc); PINS(vsrc); }
            if constexpr (i == 9) { asm volatile("s_waitcnt vmcnt(1)" : "+v"(fl) :: "memory"); kvok = ((u32)__builtin_amdgcn_ballot_w64(fl < (u32)s + 2) & 7u) == 0u; }
            if constexpr (i == 10) { kvo = l16 | (kvok ? 0u : OOB); PIN(kvo); }
            SCHED();
        SEND
        asm volatile("s_waitcnt lgkmcnt(0)\n\ts_barrier" : "+s"(clk) :: "memory");
        const v2u tvm = *(LAS v2u*)(VMX + (512 - (w & 1) * 1024));
        const v4u pv = lds_r16(ZP + 80);
        DMA32(dk, kvo, ksrc);
        SCHED(); asm volatile("" :: "v"(cm));
        float u[10], m, m2;
        {
            float NEG = -1000000000.0f / SCALE; PIN(NEG);
#define MSK(d, a, i) asm volatile("v_cndmask_b32 %0, %1, %2, %3" : "=v"(d) : "v"(NEG), "v"(a), "s"(vis[i]))
#pragma unroll
            for (int i = 0; i < 4; ++i) MSK(u[i], c0[i], i);
#pragma unroll
            for (int i = 0; i < 4; ++i) MSK(u[4 + i], c1[i], 4 + i);
            asm volatile("v_max3_f32 %0, %2, %3, %10\n\tv_max3_f32 %1, %4, %5, %11\n\tv_max3_f32 %0, %0, %6, %7\n\tv_max3_f32 %1, %1, %8, %9\n\t"
                         "v_max_f32 %0, %0, %1\n\tv_mov_b32 %1, %0\n\ts_nop 1\n\tv_permlane16_swap_b32 %0, %1\n\tv_max_f32 %0, %0, %1\n\tv_mov_b32 %1, %0\n\ts_nop 1\n\t"
                         "v_permlane32_swap_b32 %0, %1\n\tv_max_f32 %0, %0, %1\n\tv_mul_f32 %0, 0xbe0293ee, %0"
                         : "=&v"(m), "=&v"(m2) : "v"(u[0]), "v"(u[1]), "v"(u[2]), "v"(u[3]), "v"(u[4]), "v"(u[5]), "v"(u[6]), "v"(u[7]), "v"(cm[0]), "v"(cm[1]));
        }
        DMA32(dv, kvo, vsrc);
        {
            v4u px; v2u p0, p1;
#define EX2(d, a, b) asm volatile("v_fmamk_f32 %0, %2, 0x3e0293ee, %4\n\tv_fmamk_f32 %1, %3, 0x3e0293ee, %4\n\tv_exp_f32 %0, %0\n\tv_exp_f32 %1, %1" : "=&v"(d.x), "=&v"(d.y) : "v"(a), "v"(b), "v"(m))
            f32x2 ex[5];
            EX2(ex[0], u[0], u[1]); EX2(ex[1], u[2], u[3]); EX2(ex[2], u[4], u[5]); EX2(ex[3], u[6], u[7]); EX2(ex[4], cm[0], cm[1]);
            asm volatile("v_cvt_pk_bf16_f32 %0, %8, %9\n\tv_cvt_pk_bf16_f32 %1, %10, %11\n\tv_cvt_pk_bf16_f32 %2, %12, %13\n\tv_cvt_pk_bf16_f32 %3, %14, %15\n\t"
                         "v_mov_b32 %5, 0\n\tv_mov_b32 %6, 0\n\tv_cvt_pk_bf16_f32 %4, %16, %17\n\tv_cvt_pk_bf16_f32 %7, %16, %17"
                         : "=&v"(px.x), "=&v"(px.y), "=&v"(px.z), "=&v"(px.w), "=&v"(p0.x), "=&v"(p0.y), "=&v"(p1.x), "=&v"(p1.y)
                         : "v"(ex[0].x), "v"(ex[0].y), "v"(ex[1].x), "v"(ex[1].y), "v"(ex[2].x), "v"(ex[2].y), "v"(ex[3].x), "v"(ex[3].y), "v"(ex[4].x), "v"(ex[4].y));
            f32x4 o[4], sum;
            const v2u ones2 = ones.lo;
            const u32 now = (u32)clk;
            u32 oz;
            SCHED();
            MF0(sum, ones, px); { const u32 q = ksrc ^ 8192u; DMA32(dq, kvo, q); } SCHED();
            MF0(o[0], vx[0], px);
            MF0(o[1], vx[1], px);
            MF0(o[2], tv[0], px); oz = (oso & ~(3u << 17)) | (u32)((s + 3) & 3) << 17; PIN(oz); SCHED();
            MF0(o[3], tv[1], px); oso = (oso & ~(3u << 17)) | (u32)((s + 1) & 3) << 17; PIN(oso); SCHED();
            MF1H(sum, ones2, p0);
            MF1H(o[0], vm, p0);
            MF1H(o[1], vm, p1); st16(rs, oz, pv); SCHED();
            MF1H(o[2], tvm, p0);
            MF1H(o[3], tvm, p1);
            asm volatile("s_nop 2\n\tv_rcp_f32 %0, %1\n\ts_nop 0" : "=v"(inv) : "v"(sum[0]), "v"(sum));
            asm volatile("" : "+v"(o[0]), "+v"(o[1]));
            st16(rs, oo, (v4u){pk2(o[0].lo * inv), pk2(o[0].hi * inv), pk2(o[1].lo * inv), pk2(o[1].hi * inv)});
            asm volatile("" : "+v"(o[2]), "+v"(o[3]));
            st16(rs, oo ^ 2048u, (v4u){pk2(o[2].lo * inv), pk2(o[2].hi * inv), pk2(o[3].lo * inv), pk2(o[3].hi * inv)});
            SCHED();
            LS2(L == 0 && w == 0, h);
            st4s(rs, ((h & 15) == 0 && tid == 192) ? hint + 128u + (u32)h * 8u : OOB, 0, (u32)s << 16 | min(missed, 1u) << 14 | min(now - tm, 0x3FFFu));
            tm = rdl(fl, 5);
            missed = missed == SPIN_CAP ? missed : 0u;
            if (__builtin_expect(s < 1 - L, 0)) {
                if (s < 0) oso -= (ln & 256u) ? 0u : OOB;
#pragma unroll 1
                for (int n = 0; n < (s < 0 ? 0 : 200); ++n) {
                    CFENCE();
                    tm = rdl(ld4(rs, fkh), 6);
                    if (tm != PSENT) break;
                }
            }
        }
        if (__builtin_expect(!kvok, 0) && s + 1 < NSEG) {
            st4s(rs, ln == 0 ? hint + 4096u + (u32)h * 128u : OOB, 0, (u32)s + 1u);
            keys_wait((u32)s + 2u);
        }
        ++s;
    }
    DRAIN(); BAR();
    st4s(rs, ofl, 0, NSEG);
    LDUMP(w == 0, 160 + L * 32 + h);
}

#define WO_NL 3
#define WO_ROT 0
#define WO_AH 1
DEV void role_wo(const Params& p, lptr lds, int j, v2u ep) {
    const int tid = threadIdx.x, lane = tid & 63, w = uni(tid >> 6), g = lane >> 4, r = lane & 15;
    const int rg = j & 1, cg = j >> 1, rot = WO_ROT;
    const rsrc_t rs = mk_rsrc(p.ws, W_END);
    v8u wo[8][5];
    {
        const v4u so_ = mk_srd(p.Wo, DM * NQ * 2);
        u32 vo[5];
#pragma unroll
        for (int i = 0; i < 5; ++i) { const int n = 64 * i + lane; vo[i] = (u32)((n / 5) * DM + cg * 40 + (n % 5) * 8) * 2u; }
        v4u junk = {};
        const u32 av = (u32)(j * 5120 + lane * 16);
        auto ahead = [&](int b) {
#pragma unroll
            for (int i = 0; i < 5; ++i) AHEAD(so_, av, uu((8 * w + b) * (DH * DM * 2) + i * 1024));
        };
        auto fill = [&](int b) {
            if (b + WO_AH < 8) ahead(b + WO_AH);
            const u32 dst = uu(w * 40960 + (b & 3) * 10240), src = uu((8 * w + ((b + rot) & 7)) * (DH * DM * 2));
#pragma unroll
            for (int i = 0; i < 10; ++i) DMAIN(so_, dst + i * 1024, vo[i % 5], src + (i / 5) * (64 * DM * 2));
        };
#pragma unroll 1
        for (int b = 0; b < WO_AH; ++b) ahead(b);
#pragma unroll 1
        for (int b = 0; b < 4; ++b) fill(b);
        const int q4 = r >> 2;
        lptr tb = lds + w * 40960 + (32 * (g >> 1) + 16 * (q4 >> 1) + 8 * ((r >> 1) & 1) + 4 * (g & 1) + (q4 & 1)) * 80 + (r & 1) * 8;
        SFOR(b, 8)
            VMW((b < 5 ? 30 : 70 - 10 * b) + (b + 1 + WO_AH < 8 ? 5 : 0) + (b + 2 + WO_AH < 8 ? 5 : 0) + (b + 3 + WO_AH < 8 ? 5 : 0));
            if constexpr (b + WO_AH == 7) PIN(junk);
#pragma unroll
            for (int t = 0; t < 5; ++t) {
                lptr q = tb + (b & 3) * 10240 + t * 16;
                wo[b][t] = tr_frag(q, q + 160, q + 64 * 80, q + 64 * 80 + 160);
                if (b * 5 + t < 32) asm volatile("" : "+a"(wo[b][t])); else asm volatile("" : "+v"(wo[b][t]));
            }
            CFENCE();
            if constexpr (b < 4) fill(b + 4);
        SEND
    }
    if (j == 0) LST(265, rt32());
    LST(1100 + j * 4 + w, rt32());
    const u32 idx = (r & 2) ? 0xEEEEu : 0x4444u;
    const u32 pol0 = pol_wait(rs, ep);
    BAR();
    const u32 l16 = (u32)lane * 16u;
    u32 oo[8];
#pragma unroll
    for (int i = 0; i < 8; ++i) oo[i] = uu(W_ORING + ring_map((u32)(rg * 32768 + (8 * w + ((i + rot) & 7)) * 1024), pol0));
    const u32 hint = W_MRING + 4 * 32768u + pol0 * 8192u;
    u32 hvo[2];
#pragma unroll
    for (int l = 0; l < 2; ++l) hvo[l] = lane < 2 ? hint + (u32)(l ^ 1) * (MB / 4) + (u32)lane * 4u : lane == 2 ? hint + (u32)l * (MB / 4) + 128u : lane == 3 ? foff(F_ERR) : lane == 4 ? hint + (u32)l * (MB / 4) + 256u :
                 (lane >= 32 && j == 0 && w == 3) ? hint + (u32)l * (MB / 4) + (u32)lane * 128u : OOB;
    lptr xw = r < 8 ? lds + w * 640 + (2 * (g & 1) + (g >> 1)) * 32 + r * 4 : lds + 8192 + lane * 4;
    const int G = ((5 * cg) >> 1) + w, tt = 2 * G + ((lane >> 3) & 1) - 5 * cg;
    const bool pub = w < 3 && lane < 32 && tt >= 0 && tt < 5;
    lptr xr = lds + (pub ? tt * 128 + (2 * (lane >> 4) + ((lane >> 2) & 1)) * 32 + (lane & 3) * 8 : 0);
    const u32 mpo = pub ? W_MRING + ring_map((u32)(G * 256 + rg * 128 + lane * 4), pol0) : OOB;
    constexpr u32 PER0 = CH_W0 + CH_MW + CH_H0 + CH_MH + 2 * CH_VIS, DW0 = CH_W0 + CH_VIS + CH_MW + CH_REC;
    const bool lead = j == 0 && w == 3;
    const u32 hso = (lead && lane == 0) ? hint : OOB;
    MK_SRD();
    u32 t = 0, tn = 0;
    int sn[2] = {0, 0};
    u32 hav = 8 * CH_H0, wav = 8 * CH_W0, mh = 8 * CH_MH, mw = 8 * CH_MW, ok[2] = {0u, 0u}, dw[2] = {DW0, DW0};
    u32 vo = l16, early = PSENT, tp = 0, fin, left, cnt = 0;
    unsigned long long clk = 0;
    {
        if (w == 0) LST(1310 + j, rt32());
        if (lead) {
#pragma unroll 1
            for (int n = 0; n < SPIN_CAP; ++n) {
                CFENCE();
                const u32 a = ld4(rs, foff(F_QKV + lane)), b = ld4(rs, lane < 32 ? foff(F_QKV + 64 + lane) : hvo[0]);
                if (all_(a != 0u && b != (lane < 32 ? 0u : PSENT))) break;
                __builtin_amdgcn_s_sleep(1);
            }
        }
        if (lead) {
            t = rt32() + CH_T0; tn = t + PER0 / 2 + CH_REC;
            st16(rs, lane < 2 ? hint + (u32)lane * (MB / 4) : OOB, (v4u){lane == 0 ? t : tn, PER0, (lane == 0 ? t : tn) + DW0, t + DW0});
        } else {
#pragma unroll 1
            for (int n = 0; n < SPIN_CAP; ++n) {
                CFENCE();
                const u32 hv = ld4(rs, hvo[1]);
                t = rdl(hv, 0);
                if (rdl(hv, 1) < 400u) break;
                __builtin_amdgcn_s_sleep(1);
            }
        }
        clk = t - 100u;
    }
    const u32 tm0 = t + DW0;
    u32 zero = 0, hv = 0xFFFFu, pst = 1, pmp = OOB;
    const u32 psent = PSENT;
    lptr sw = lds + 2560 + w * 4;
    asm volatile("" : "+v"(zero), "+v"(sw));
    auto turn = [&](auto lc) __attribute__((always_inline)) {
        constexpr int L = decltype(lc)::value, M = L ^ 1;
        u32 done, rep, rd, lost, dh, dn, nt, za, pdur, mp; v4u hw;
        const int sc = sn[L];
        auto books = [&](auto ic) __attribute__((always_inline)) {
            constexpr int i = decltype(ic)::value;
            if constexpr (i == 0) { rep = max(rdl(hv, 2), rdl(hv, 4)); PINS(rep); }
            if constexpr (i == 1) { lost = (u32)(__builtin_amdgcn_ballot_w64(hv == (u32)sn[M]) >> 32); PINS(lost); }
            if constexpr (i == 2) { lost |= (rep >> 16) ^ (u32)sn[M]; PINS(lost); }
            if constexpr (i == 3) { lost |= ok[M] ^ 2u; rd = rep & 0xFFFFu; PINS(lost); PINS(rd); }
            if constexpr (i == 4) { lost = lost == 0u ? CH_UP * 8u : 0u; PINS(lost); }
            if constexpr (i == 5) { pdur = ((u32)clk - tp) * 8u; PINS(pdur); pdur = min(pdur, wav + CH_RISE); PINS(pdur); }
            if constexpr (i == 6) { pdur = (done & ok[M]) ? pdur : wav; PINS(pdur); }
            if constexpr (i == 7) { wav = max(pdur, wav - CH_FALL); PINS(wav); }
            if constexpr (i == 8) { nt = min(rd * 8u, hav + CH_RISE); PINS(nt); }
            if constexpr (i == 9) { nt = rd < 0x3FFFu ? nt : hav; PINS(nt); }
            if constexpr (i == 10) { hav = max(nt, hav - CH_FALL); PINS(hav); }
            if constexpr (i == 11) { mw = max(mw - 1u, CH_MW * 8u); PINS(mw); }
            if constexpr (i == 12) { mh = max(mh - 1u, CH_MH * 8u); PINS(mh); }
            if constexpr (i == 13) { rd = rd >> 14 == 1u ? lost : 0u; PINS(rd); }
            if constexpr (i == 14) { mw += rd; mh += lost - rd; PINS(mw); PINS(mh); }
            if constexpr (i == 15) { ok[M] = done ? 1u : (ok[M] & 1u) << 1; PINS(ok[M]); }
            if constexpr (i == 16) { dh = (hav + mh) / 8u + (sn[1] < 3 ? CH_VIS + CH_PRE : CH_VIS); PINS(dh); }
            if constexpr (i == 17) { dn = (wav + mw) / 8u + (sn[1] < 3 ? CH_VIS + CH_PRW : CH_VIS); PINS(dn); }
            if constexpr (i == 18) { nt = dw[L] + dh + (u32)max((int)((u32)clk + CH_WX - t), 0); PINS(nt); }
            if constexpr (i == 19) { dh += dn; dw[L] = dn; sn[M] += (int)done; PINS(dh); PINS(sn[M]); }
            if constexpr (i == 20) { rd = tn - t + (dh >> 1); PINS(rd); }
            if constexpr (i == 21) { lost = (u32)sc - 1u < NSEG - 2 ? CH_RTY : 0u; PINS(lost); nt += ok[L] != 1u ? lost : 0u; PINS(nt); }
            if constexpr (i == 22) { nt = t + (u32)max((int)nt, (int)rd); PINS(nt); }
            if constexpr (i == 23) { hw.x = nt; hw.y = dh; PIN(hw.x); PIN(hw.y); }
            if constexpr (i == 24) { hw.z = nt + dn; hw.w = tm0; PIN(hw); }
            if constexpr (i == 25) st16(rs, hso + L * (MB / 4), hw);
        };
        {
#define WT(k) do {} while (0)
            WT(0);
            v4u d[8];
            {
                u32 tmp;
                asm volatile(WAIT_TXT("%1", "%2") "buffer_load_dwordx4 %0, %3, %4, %5 offen sc1\n\ts_waitcnt lgkmcnt(0)"
                             : "=&v"(d[0]), "=&s"(tmp) : "s"(uu(t - 4u)), "v"(vo), "s"(srd), "s"(oo[0]) : "vcc", "scc", "memory");
                PINS(clk);
            }
            SFOR(i, 7)
                asm volatile("buffer_load_dwordx4 %0, %1, %2, %3 offen sc1" : "=v"(d[i + 1]) : "v"(vo), "s"(srd), "s"(oo[i + 1]) : "memory");
                SCHED();
                if constexpr (i == 0) { done = uu(pst) ^ 1u; PINS(done); }
                static_for<3 * i, 3 * i + 3>(books);
                SCHED();
            SEND
            LD4A(hv, hvo[L], 0);
            SCHED();
            WT(1);
            static_for<21, 26>(books);
            za = done ? pmp : OOB;
            st4s(rs, za, 0, psent);
            *(LAS u32*)(sw + L * 4096) = zero;
            f32x4 acc[5] = {};
            mp = mpo + (L * (MB / 4) + (u32)((sc + 1) & 3) * 32768u); pmp = mpo + (L * (MB / 4) + (u32)(sc & 3) * 32768u);
            asm volatile("" : "+v"(acc[0]), "+v"(acc[1]), "+v"(acc[2]), "+v"(acc[3]), "+v"(acc[4]), "+v"(mp), "+v"(pmp));
            SCHED();
            float f0[5], f1[5]; unsigned long long bad;
#define WSW(o_) asm volatile("v_permlane32_swap_b32 %0, %1" : "+v"(f0[o_]), "+v"(f1[o_]))
#define WAD(o_) asm volatile("v_add_f32_dpp %0, %1, %0 row_ror:8 row_mask:0xf bank_mask:0xf" : "+v"(f0[o_]) : "v"(f1[o_]))
#define WFO(o_) do { VADD(f0[o_], acc[o_][0], acc[o_][2]); VADD(f1[o_], acc[o_][1], acc[o_][3]); } while (0)
#define WWR(o_) *(LAS float*)(xw + L * 4096 + (o_) * 128) = f0[o_]
            u32 th, per, so, erl = early;
            auto next = [&](auto ic_) __attribute__((always_inline)) {
                constexpr int i = decltype(ic_)::value;
                if constexpr (i == 0) { so = (u32)sn[M] << 17; PINS(so); }
                if constexpr (i == 1) { so = (so & 0x60000u) + M * (MB / 2); PINS(so); }
                if constexpr (i == 2) { so = sn[M] + 1 < NSEG ? so : OOB; PINS(so); }
                if constexpr (i == 3) { early = sn[M] + 1 < NSEG ? PSENT : 0u; PIN(early); }
                if constexpr (i == 4) { vo = l16 + so; PIN(vo); }
                if constexpr (i == 5 && L) { fin = (u32)(sn[0] + sn[1] + 2); PINS(fin); }
                if constexpr (i == 6 && L) { fin = (fin | uu(left)) >> 7; PIN(fin); }
                if constexpr (i == 6 && !L) { cnt += 1u; PIN(cnt); left = cnt >> 9; PIN(left); }
                if constexpr (i == 7) { PIN(hv); th = rdl(hv, 0); PINS(th); }
                if constexpr (i == 8) { per = rdl(hv, 1); PINS(per); so = th - t; PINS(so); }
                if constexpr (i == 9) { per = (int)so < 20 ? per : 0u; PINS(per); }
                if constexpr (i == 10) { th += per; PINS(th); if constexpr (L) { per = rdl(hv, 3); PINS(per); } }
                if constexpr (i == 11) { tp = t; t = lead ? tn : th; PINS(t); }
                if constexpr (i == 12) { if constexpr (L) { fin |= per; PIN(fin); } tn = nt; }
            };
            auto slot = [&](auto ic_) __attribute__((always_inline)) {
                constexpr int i = decltype(ic_)::value;
                constexpr int NL = WO_NL, I0 = 40 - 5 * NL, pc = i < I0 ? i / 5 : 8 - NL + (i - I0) % NL, oc = i < I0 ? i % 5 : (i - I0) / NL;
                if constexpr (oc == 0) asm volatile("s_waitcnt vmcnt(%1)" : "+v"(d[pc]) : "i"(10 - pc));
                if constexpr (pc * 5 + oc < 32) SMF_A(acc[oc], d[pc], wo[pc][oc]); else SMF_V(acc[oc], d[pc], wo[pc][oc]);
                SCHED();
                if constexpr (i == 0) WT(2);
                if constexpr (i >= 5 && i < 13 && i != 9) next(IC<i - 5 - (i > 9)>{});
                if constexpr (i == 20) WT(4);
                constexpr int e = i - (I0 + NL + 1), o = e < 0 ? 0 : e / 3;
                if constexpr (e >= 0 && e % 3 == 0) { CADD(f0[o], acc[o][0], acc[o][2]); if constexpr (o > 0) WAD(o - 1); }
                if constexpr (e >= 0 && e % 3 == 1) { if constexpr (o > 0) { WWR(o - 1); SCHED(); } CADD(f1[o], acc[o][1], acc[o][3]); }
                if constexpr (e >= 0 && e % 3 == 2) {
                    if constexpr (o == 0) asm volatile("v_cmp_u_f32 %0, %1, %2" : "=s"(bad) : "v"(f0[0]), "v"(f1[0]));
                    else LGK(15);
                    WSW(o);
                }
                SCHED();
            };
            asm volatile(".rept %0\n\ts_nop 0\n\t.endr" :: "i"(AL_W));
            static_for<0, 2>(slot);
            if (__builtin_expect(__builtin_amdgcn_ballot_w64(d[0].x == erl) != 0ull, 0)) {
                asm volatile("s_waitcnt vmcnt(3)" :: "v"(d[1]), "v"(d[2]), "v"(d[3]), "v"(d[4]), "v"(d[5]), "v"(d[6]), "v"(d[7]) : "memory");
                SCHED();
                *(LAS u32*)(sw + L * 4096) = 1u;
                static_for<0, 7>(next);
            } else {
            static_for<2, 40>(slot);
            WT(5);
            static_assert(WO_NL == 3, "tail");
            if (__builtin_expect(bad != 0ull, 0)) {
                bool miss = false;
#pragma unroll
                for (int i = 0; i < 8; ++i) miss |= d[i].x == PSENT;
                if (all_(!miss)) {
#pragma unroll
                    for (int i = 0; i < 8; ++i) miss |= d[i].y == PSENT || d[i].z == PSENT || d[i].w == PSENT;
                }
                *(LAS u32*)(sw + L * 4096) = all_(!miss) ? 0u : 1u;
            }
            asm volatile("v_permlane32_swap_b32 %0, %1\n\ts_nop 1\n\tv_add_f32_dpp %0, %1, %0 row_ror:8 row_mask:0xf bank_mask:0xf\n\tv_add_f32 %2, %4, %6\n\tv_add_f32 %3, %5, %7"
                         : "+v"(f0[3]), "+v"(f1[3]), "=&v"(f0[4]), "=&v"(f1[4]) : "v"(acc[4][0]), "v"(acc[4][1]), "v"(acc[4][2]), "v"(acc[4][3]));
            WWR(3); SCHED();
            asm volatile("s_nop 0\n\tv_permlane32_swap_b32 %0, %1\n\ts_nop 1\n\tv_add_f32_dpp %0, %1, %0 row_ror:8 row_mask:0xf bank_mask:0xf" : "+v"(f0[4]), "+v"(f1[4]));
            WWR(4);
            }
            WT(6);
            f32x2 a, b, c, e; u32 mq; v4u sv;
            asm volatile("s_waitcnt lgkmcnt(0)\n\ts_barrier\n\tds_read_b128 %4, %6 offset:%7+2560\n\t"
                         "ds_read_b64 %0, %5 offset:%7\n\tds_read_b64 %1, %5 offset:%7+640\n\tds_read_b64 %2, %5 offset:%7+1280\n\tds_read_b64 %3, %5 offset:%7+1920"
                         : "=&v"(a), "=&v"(b), "=&v"(c), "=&v"(e), "=&v"(sv) : "v"(xr), "v"(zero), "i"(L * 4096) : "memory");
            asm volatile("s_waitcnt lgkmcnt(4)\n\tv_or_b32 %4, %6, %7\n\tv_or3_b32 %4, %4, %8, %9\n\tv_lshl_or_b32 %5, %4, 31, %10\n\t"
                         "s_waitcnt lgkmcnt(2)\n\tv_pk_add_f32 %0, %0, %1\n\ts_waitcnt lgkmcnt(0)\n\tv_pk_add_f32 %2, %2, %3\n\ts_nop 0\n\tv_pk_add_f32 %0, %0, %2"
                         : "+v"(a), "+v"(b), "+v"(c), "+v"(e), "=&v"(pst), "=&v"(mq) : "v"(sv.x), "v"(sv.y), "v"(sv.z), "v"(sv.w), "v"(mp) : "memory");
            st4s(rs, mq, 0, pkv(a));
            SCHED();
            asm volatile("s_memrealtime %0" : "=s"(clk));
            VMW(3); SCHED();
            static_for<7, 13>(next);
            WT(7);
        }
    };
    do {
        turn(IC<0>{});
        turn(IC<1>{});
    } while (uu(fin) == 0u);
    if (sn[0] + 1 < NSEG || sn[1] + 1 < NSEG) st4s(rs, tid == 0 ? foff(F_ERR) : OOB, 0, 1u);
    if (w == 0) LST(1900 + j, rt32());
    LDUMP(w == 0, 224 + j);
}

__global__ void __launch_bounds__(256, 1) k_main(Params p) {
    lptr lds = (lptr)(u32)16 - 16;
    if (blockIdx.x == 0) LST(256, rt32());
    unsigned long long pc; int len;
    asm volatile("s_getpc_b64 %0\n.Lk_main_here:\n\ts_mov_b32 %1, .Lk_end_here-.Lk_main_here" : "=s"(pc), "=s"(len));
    const rsrc_t rs = mk_rsrc(p.ws, W_END);
    const v2u e0 = ld8s(rs, threadIdx.x == 0 ? foff(F_EPOCH) : OOB, 0);
    const v4u code = __builtin_amdgcn_raw_buffer_load_b128(mk_rsrc((const void*)(pc & ~15ull), (u32)max(len, 0)), (((blockIdx.x >> 3) & 31u) * 256u + threadIdx.x) * 16u, 0, 0);
    CFENCE();
    auto conv_id = [](int role) {
        const int x = role >> 5, i = role & 31, no = x < 5 ? 5 : 10, og = x < 5 ? x - 3 : 2 * (x - 4) + (i >= 5), r = (x - 3) * 5 + i - no, q = r / 3;
        return x < 3 || i >= no + 5 || r >= 24 ? NCV : i < no ? ((og & 1) ? (og >> 1) * 5 + i % 5 : NCV) : r % 3 == 0 ? 20 + (q & 1) * 20 + (q >> 1) : 16 + (r % 3) * 8 + q;
    };
    int role = (int)(((u32)__builtin_amdgcn_s_getreg(20 | (3 << 11)) & 7u) * 32u + (blockIdx.x >> 3));
    Early e;
    v4u rv;
#pragma unroll 1
    for (int n = 0;; ++n) {
        conv_early(p, rs, conv_id(role), e);
        if (n) break;
        CFENCE();
        if (threadIdx.x == 0) {
            GAS unsigned long long* slots = (GAS unsigned long long*)(p.ws + W_CTRL + F_SLOT * 128);
            const unsigned long long tag = (unsigned long long)e0.y << 32 | (e0.x + 1u);
            u32 mine = (u32)role;
#pragma unroll 1
            for (int k = 0; k < 256 && __hip_atomic_exchange(slots + mine * 16, tag, __ATOMIC_RELAXED, __HIP_MEMORY_SCOPE_AGENT) == tag; ++k) mine = (mine + 1u) & 255u;
            lds_w16(lds, (v4u){mine, e0.x, e0.y, 0u});
        }
        BAR();
        rv = lds_r16(lds);
        BAR();
        if (uni((int)rv.x) == role) break;
        role = uni((int)rv.x);
    }
    const int x = role >> 5, i = role & 31, w = uni(threadIdx.x >> 6), no = x < 5 ? 5 : 10, nr = x < 7 ? 5 : 4, j = conv_id(role);
    const v2u ep = {uu(rv.y), uu(rv.z)};
    asm volatile("" :: "v"(code));
    if (x < 2) { role_head(p, lds, i >> 4, x * 16 + (i & 15), ep); __builtin_amdgcn_endpgm(); }
    if (x == 2) { role_wo(p, lds, i, ep); __builtin_amdgcn_endpgm(); }
    if (i >= no + nr) { role_qkv(p, lds, (x < 5 ? (x - 3) * 22 : 44 + (x - 5) * 17) + i - no - nr, ep); __builtin_amdgcn_endpgm(); }
    const bool cv = j >= NPRE || (j >= 20 && j < 24);
    if (j < NCV) conv_stage(p, rs, lds, j, (j & 3) * 2 + (j >= NPRE), cv ? 40 : 0, ep, e);
    if (i >= no) {
        if (!cv) role_row(p, lds, (j - 24) * 4 + w, ep);
        __builtin_amdgcn_endpgm();
    }
    const int og = x < 5 ? x - 3 : 2 * (x - 4) + (i >= 5);
    role_out(p, lds, og & 1, og >> 1, i % 5, ep);
}
__global__ void k_end() { asm volatile(".Lk_end_here:"); }

extern "C" void kernel_launch(void* const* d_in, const int* in_sizes, int n_in, void* d_out, int out_size, void* d_ws, size_t ws_size,
                              hipStream_t stream) {
    Params p;
    p.x = (const u16*)d_in[0]; p.mem0 = (const u16*)d_in[1]; p.Wq = (const u16*)d_in[2]; p.Wk = (const u16*)d_in[3];
    p.Wv = (const u16*)d_in[4]; p.Wo = (const u16*)d_in[5];
    p.bk = (const u16*)d_in[8]; p.bv = (const u16*)d_in[9]; p.shift = (const u16*)d_in[10]; p.ssmk = (const u16*)d_in[11]; p.ssmD = (const u16*)d_in[12];
    p.out = (u16*)d_out;
    p.ws = (unsigned char*)(((size_t)d_ws + 2 * MB - 1) & ~(size_t)(2 * MB - 1));
    (void)hipFuncSetAttribute((const void*)k_main, hipFuncAttributeMaxDynamicSharedMemorySize, LDS_BYTES);
    hipLaunchKernelGGL(k_main, dim3(256), dim3(256), LDS_BYTES, stream, p);
}
```

```cpp
#include <hip/hip_runtime.h>
#include <hip/hip_bf16.h>

typedef unsigned short u16;
typedef unsigned int u32;
typedef u32 v2u __attribute__((ext_vector_type(2)));
typedef u32 v4u __attribute__((ext_vector_type(4)));
typedef u32 v8u __attribute__((ext_vector_type(8)));
typedef float f32x2 __attribute__((ext_vector_type(2)));
typedef float f32x4 __attribute__((ext_vector_type(4)));
typedef float f32x16 __attribute__((ext_vector_type(16)));
typedef __bf16 bf16x2 __attribute__((ext_vector_type(2)));
typedef __bf16 bf16x8 __attribute__((ext_vector_type(8)));
typedef short s16x4 __attribute__((ext_vector_type(4)));
#define LAS __attribute__((address_space(3)))
#define GAS __attribute__((address_space(1)))
typedef LAS unsigned char* lptr;
#define DEV __device__ __forceinline__

constexpr int SEQ = 2048, DM = 640, NH = 32, DH = 128, NSEG = 64, NQ = NH * DH;

constexpr u32 MB = 1u << 20;
constexpr u32 W_MEM0 = 2 * MB;
constexpr u32 W_CTRL = 4 * MB;
constexpr u32 CTRL_BYTES = 128 * 1024;
constexpr u32 ACT = SEQ * DM * 2;
constexpr u32 W_H1 = 5 * MB, W_Y = W_H1 + ACT, W_H2 = W_Y + ACT, W_X2 = W_H2 + ACT;
constexpr u32 W_KQ = 16 * MB;
constexpr u32 W_VO = W_KQ + 64 * MB;
constexpr u32 W_P = W_VO + 64 * MB;
constexpr u32 P_ITEM = 16 * 4 * DM * 4;
constexpr u32 W_KVT = W_P + 4 * NSEG * P_ITEM;
constexpr u32 W_SP = W_KVT + ACT;
constexpr u32 W_END = W_KVT + 3 * MB;
static_assert((size_t)W_END + 2 * MB <= 256ull * MB, "workspace");
constexpr u32 F_ERR = 0, F_QKV = 16, F_O = F_QKV + 192, F_P = F_O + 64, F_H2 = F_P + 64, F_CLR = F_H2 + 32,
              F_EPOCH = F_CLR + 32, F_SLOT = F_EPOCH + 1, F_PRE = F_SLOT + 256, F_N = F_PRE + 192;
constexpr int NCV = 44, NPRE = 40;
static_assert(F_N * 128 <= CTRL_BYTES, "ctrl");
DEV u32 foff(u32 idx) { return W_CTRL + idx * 128u; }

constexpr u32 OOB = 0x80000000u;
constexpr u32 PSENT = 0x7F817F81u;
constexpr int SPIN_CAP = 40000;
constexpr int LDS_BYTES = 160 * 1024;

#define LDUMP(cond, id) do {} while (0)
#define LMX(cond, k, s_, v) do {} while (0)
#define LS3(cond, slot, idx) do {} while (0)
#define LST(idx, v) do {} while (0)
#define LS2(cond, idx) do {} while (0)
struct Params {
    const u16 *x, *mem0, *Wq, *Wk, *Wv, *Wo, *bk, *bv, *shift, *ssmk, *ssmD;
    u16* out;
    unsigned char* ws;
};

#define INAUX 2
#define INNT "nt"
typedef __amdgpu_buffer_rsrc_t rsrc_t;
DEV rsrc_t mk_rsrc(const void* p, u32 bytes) { return __builtin_amdgcn_make_buffer_rsrc((void*)p, 0, bytes, 0x00020000); }
DEV v4u ld16s(rsrc_t r, u32 off, u32 so) { return __builtin_amdgcn_raw_buffer_load_b128(r, off, so, 16); }
DEV v2u ld8s(rsrc_t r, u32 off, u32 so) { return __builtin_amdgcn_raw_buffer_load_b64(r, off, so, 16); }
DEV v4u ldin16(rsrc_t r, u32 off, u32 so) { return __builtin_amdgcn_raw_buffer_load_b128(r, off, so, INAUX); }
DEV v2u ldin8(rsrc_t r, u32 off, u32 so) { return __builtin_amdgcn_raw_buffer_load_b64(r, off, so, INAUX); }
DEV u32 ld4(rsrc_t r, u32 off) { return __builtin_amdgcn_raw_buffer_load_b32(r, off, 0, 16); }
DEV void st16(rsrc_t r, u32 off, v4u v) { __builtin_amdgcn_raw_buffer_store_b128(v, r, off, 0, 16); }
DEV void st8s(rsrc_t r, u32 off, u32 so, v2u v) { __builtin_amdgcn_raw_buffer_store_b64(v, r, off, so, 16); }
DEV void st4s(rsrc_t r, u32 off, u32 so, u32 v) { __builtin_amdgcn_raw_buffer_store_b32(v, r, off, so, 16); }
#define DRAIN() asm volatile("s_waitcnt vmcnt(0)" ::: "memory")
#define VMW(n) asm volatile("s_waitcnt vmcnt(%0)" :: "i"(n) : "memory")
#define BAR() asm volatile("s_waitcnt lgkmcnt(0)\n\ts_barrier" ::: "memory")
#define SCHED() __builtin_amdgcn_sched_barrier(0)
#define CFENCE() asm volatile("" ::: "memory")
#define NOPS(n) asm volatile("s_nop %0" :: "i"(n))
#define PIN(x) asm volatile("" : "+v"(x))
#define PINS(x) asm volatile("" : "+s"(x))

template <int V> struct IC { static constexpr int value = V; };
template <int I, int N, class F> DEV void static_for(F&& f) { if constexpr (I < N) { f(IC<I>{}); static_for<I + 1, N>(f); } }
#define SFOR(var, n) static_for<0, n>([&](auto ic_) __attribute__((always_inline)) { constexpr int var = decltype(ic_)::value;
#define SEND });

DEV u32 pk(float a, float b) { bf16x2 v = {(__bf16)a, (__bf16)b}; return __builtin_bit_cast(u32, v); }
DEV float lo_f(u32 u) { return __builtin_bit_cast(float, u << 16); }
DEV float hi_f(u32 u) { return __builtin_bit_cast(float, u & 0xffff0000u); }
DEV f32x2 up2(u32 u) { return (f32x2){lo_f(u), hi_f(u)}; }
DEV u32 pk2(f32x2 v) { return pk(v.x, v.y); }
DEV float rbf(float a) { return (float)(__bf16)a; }
DEV v4u lds_r16(lptr p) { return *(LAS v4u*)p; }
DEV void lds_w16(lptr p, v4u v) { *(LAS v4u*)p = v; }
DEV v2u lds_tr(lptr p) { return __builtin_bit_cast(v2u, __builtin_amdgcn_ds_read_tr16_b64_v4i16((LAS s16x4*)p)); }
DEV v4u tr2(lptr p0, lptr p1) { const v2u a = lds_tr(p0), b = lds_tr(p1); return (v4u){a.x, a.y, b.x, b.y}; }
DEV v8u cat8(v4u a, v4u b) { return __builtin_shufflevector(a, b, 0, 1, 2, 3, 4, 5, 6, 7); }
DEV f32x4 mfma16(v4u a, v4u b, f32x4 c) { return __builtin_amdgcn_mfma_f32_16x16x32_bf16(__builtin_bit_cast(bf16x8, a), __builtin_bit_cast(bf16x8, b), c, 0, 0, 0); }
DEV f32x16 mfma32(v4u a, v4u b, f32x16 c) { return __builtin_amdgcn_mfma_f32_32x32x16_bf16(__builtin_bit_cast(bf16x8, a), __builtin_bit_cast(bf16x8, b), c, 0, 0, 0); }

DEV float f_(u32 u) { return __builtin_bit_cast(float, u); }
DEV u32 u_(float f) { return __builtin_bit_cast(u32, f); }
template <int CTRL> DEV float dpp(float v) { return f_((u32)__builtin_amdgcn_update_dpp(0, (int)u_(v), CTRL, 0xf, 0xf, false)); }
DEV float max16(float v) { auto r = __builtin_amdgcn_permlane16_swap(u_(v), u_(v), false, false); return fmaxf(f_(r[0]), f_(r[1])); }
DEV float max32(float v) { auto r = __builtin_amdgcn_permlane32_swap(u_(v), u_(v), false, false); return fmaxf(f_(r[0]), f_(r[1])); }
DEV float add16(float v) { auto r = __builtin_amdgcn_permlane16_swap(u_(v), u_(v), false, false); return f_(r[0]) + f_(r[1]); }
DEV float add32(float v) { auto r = __builtin_amdgcn_permlane32_swap(u_(v), u_(v), false, false); return f_(r[0]) + f_(r[1]); }
DEV float wave_sum(float v) {
    v += dpp<0xB1>(v);
    v += dpp<0x4E>(v);
    v += dpp<0x124>(v);
    v += dpp<0x128>(v);
    return add32(add16(v));
}
DEV bool all_(bool c) { return __builtin_amdgcn_ballot_w64(!c) == 0ull; }
DEV int uni(int v) { return __builtin_amdgcn_readfirstlane(v); }
DEV u32 uu(u32 v) { return (u32)__builtin_amdgcn_readfirstlane((int)v); }
DEV u32 rdl(u32 v, int l) { return (u32)__builtin_amdgcn_readlane((int)v, l); }
DEV u32 rt32() { return (u32)__builtin_amdgcn_s_memrealtime(); }

#define WSL 2
DEV void wait_flags(rsrc_t rs, u32 off, u32 want, bool& dead) {
    if (dead) return;
#pragma unroll 1
    for (int n = 0;; ++n) {
        CFENCE();
        const u32 v = ld4(rs, off);
        if (all_(off >= OOB || v >= want)) break;
        __builtin_amdgcn_s_sleep(WSL);
        if ((n & 63) == 63) { if (uni((int)ld4(rs, foff(F_ERR))) != 0) { dead = true; break; } }
        if (n > SPIN_CAP) { st4s(rs, foff(F_ERR), 0, 1u); dead = true; break; }
    }
    CFENCE();
}

DEV void pre_post(rsrc_t rs, int idx, u32 stage, v2u ep) { st8s(rs, threadIdx.x == 0 ? foff(F_PRE + idx) : OOB, 0, (v2u){ep.x + stage, ep.y}); }
DEV void pre_poll(rsrc_t rs, int first, int n, u32 stage, v2u ep) {
    const int lane = threadIdx.x & 63;
#pragma unroll 1
    for (int it = 0; it < SPIN_CAP; ++it) {
        CFENCE();
        const v2u a = ld8s(rs, foff(F_PRE + first + min(lane, n - 1)), 0), b = ld8s(rs, foff(F_PRE + first + min(lane + 64, n - 1)), 0);
        if (all_(a.y == ep.y && b.y == ep.y && max(a.x - ep.x - stage, b.x - ep.x - stage) < 8u - stage)) break;
        __builtin_amdgcn_s_sleep(2);
    }
    CFENCE();
}
DEV void pre_wait(rsrc_t rs, int first, int n, u32 stage, v2u ep) {
    if (uni(threadIdx.x >> 6) == 0) pre_poll(rs, first, n, stage, ep);
    BAR();
}
#define POLX 0u
DEV u32 pol_wait(rsrc_t rs, v2u ep) {
    const int lane = threadIdx.x & 63;
    v4u a;
#pragma unroll 1
    for (int it = 0; it < SPIN_CAP; ++it) {
        CFENCE();
        a = ld16s(rs, foff(F_PRE + (lane < NPRE || lane >= 48 ? lane : 0)), 0);
        if (all_(a.y == ep.y && a.x - ep.x - 2u < 6u)) break;
        __builtin_amdgcn_s_sleep(2);
    }
    CFENCE();
    return uu((wave_sum(lane >= 48 ? (float)(int)a.z : 0.f) >= 0.f ? 8192u : 0u) + (wave_sum(lane >= 48 ? (float)(int)a.w : 0.f) >= 0.f ? 16384u : 0u)) ^ POLX;
}
#define PROBE_C 7
#define PROBE_T 620
DEV v2u pol_probe(rsrc_t rs, int id) {
    const int lane = threadIdx.x & 63;
    const u32 off0 = (u32)id * 32768u, tend = rt32() + PROBE_T;
    st4s(rs, lane < 32 ? off0 + (u32)lane * 1024u : OOB, 0, PSENT); DRAIN();
    int d = 1 << 16, calm = 0;
#pragma unroll 1
    for (int q = 0; q < 32 && ((q & 3) || (calm < PROBE_C && (int)(rt32() - tend) < 0)); ++q) {
        const int kind = (q ^ (q >> 2) ^ id) & 3;
        const unsigned long long t0 = __builtin_amdgcn_s_memtime();
        const u32 v = ld4(rs, lane == 0 ? off0 + (u32)(kind * 8192 + (q >> 2) * 1024) : OOB);
        asm volatile("s_waitcnt vmcnt(0)" :: "v"(v) : "memory");
        const int dt = (int)(__builtin_amdgcn_s_memtime() - t0);
        d = lane == kind ? min(d, dt) : d;
        calm += dt < 1100;
    }
    const int d0 = (int)rdl(d, 0), d1 = (int)rdl(d, 1), d2 = (int)rdl(d, 2), d3 = (int)rdl(d, 3), a = min(d0, d2) - min(d1, d3);
    const int b0 = min(max(d0 - d2, -80), 80), b1 = min(max(d1 - d3, -80), 80);
    const u32 xcc = (u32)__builtin_amdgcn_s_getreg(20 | (3 << 11));
    return (v2u){(u32)min(max((xcc & 4u) ? -a : a, -250), 250), (u32)((xcc & 1u) ? b0 + b1 : a > 0 ? b0 - b1 : b1 - b0)};
}
DEV f32x2 ln_stats(const f32x2 (&x)[5]) {
    const float c = f_(uu(u_(x[0].x)));
    f32x2 d[5];
#pragma unroll
    for (int i = 0; i < 5; ++i) d[i] = x[i] - c;
    const f32x2 s2 = (d[0] + d[1]) + (d[2] + d[3]) + d[4], q2 = (d[0] * d[0] + d[1] * d[1]) + (d[2] * d[2] + d[3] * d[3] + d[4] * d[4]);
    float a = s2.x + s2.y, b = q2.x + q2.y, a2, b2;
#define LN_DPP(m) "v_add_f32_dpp %0, %0, %0 " m " row_mask:0xf bank_mask:0xf\n\tv_add_f32_dpp %1, %1, %1 " m " row_mask:0xf bank_mask:0xf\n\ts_nop 0\n\t"
    asm("s_nop 1\n\t" LN_DPP("quad_perm:[1,0,3,2]") LN_DPP("quad_perm:[2,3,0,1]") LN_DPP("row_ror:4")
        "v_add_f32_dpp %0, %0, %0 row_ror:8 row_mask:0xf bank_mask:0xf\n\tv_add_f32_dpp %1, %1, %1 row_ror:8 row_mask:0xf bank_mask:0xf\n\ts_nop 1\n\t"
        "v_permlane16_swap_b32 %0, %1\n\tv_add_f32 %0, %0, %1\n\tv_mov_b32 %1, %0\n\ts_nop 1\n\tv_permlane32_swap_b32 %0, %1\n\tv_add_f32 %0, %0, %1\n\ts_nop 0\n\t"
        "v_readlane_b32 %2, %0, 0\n\tv_readlane_b32 %3, %0, 16\n\ts_nop 1"
        : "+v"(a), "+v"(b), "=s"(a2), "=s"(b2));
    const float m = a2 * (1.0f / DM), var = fmaxf(b2 * (1.0f / DM) - m * m, 0.f);
    const float sd = rbf(__builtin_amdgcn_sqrtf(rbf(rbf(var) + 1e-5f)));
    return (f32x2){rbf(c + m), __builtin_amdgcn_rcpf(sd)};
}
DEV u32 pkv(f32x2 v) { u32 d; asm("v_cvt_pk_bf16_f32 %0, %1, %2" : "=v"(d) : "v"(v.x), "v"(v.y)); return d; }
DEV void store_h(rsrc_t rs, u32 base, int row, const u32 (&hp)[5]) {
    const u32 lane = threadIdx.x & 63;
    const u32 sw = (u32)(row & 15) * 16u, o8 = (lane >> 5) * 8192u + (((lane & 31u) * 8u) ^ sw), hb = base + (u32)(row >> 5) * (32 * DM * 2) + (u32)(row & 31) * 256u;
    st8s(rs, o8, hb, (v2u){hp[0], hp[1]});
    st8s(rs, o8, hb + 16384, (v2u){hp[2], hp[3]});
    st4s(rs, (lane * 4) ^ sw, hb + 32768, hp[4]);
}

#define DMA16(ldsb, vo, so) asm volatile("s_mov_b32 m0, %0\n\ts_nop 0\n\tbuffer_load_dwordx4 %1, %2, %3 offen sc1 lds" :: "s"(ldsb), "v"(vo), "s"(srd), "s"(so) : "memory")
#define DMA16I(ldsb, lo, vo, so, go) do { u32 so_; asm volatile("s_add_u32 m0, %1, %5\n\ts_add_u32 %0, %4, %6\n\tbuffer_load_dwordx4 %2, %3, %0 offen sc1 lds" \
                         : "=&s"(so_) : "s"(ldsb), "v"(vo), "s"(srd), "s"(so), "i"(lo), "i"(go) : "memory", "scc"); } while (0)
#define DMA32(ldsb, vo, so) asm volatile("s_mov_b32 m0, %0\n\ts_nop 0\n\tbuffer_load_dwordx4 %1, %2, %3 offen sc1 lds\n\tbuffer_load_dwordx4 %1, %2, %3 offen offset:1024 sc1 lds" \
                         :: "s"(ldsb), "v"(vo), "s"(srd), "s"(so) : "memory")
#define DMAIN(sr, ldsb, vo, so) asm volatile("s_mov_b32 m0, %0\n\ts_nop 0\n\tbuffer_load_dwordx4 %1, %2, %3 offen lds" :: "s"(ldsb), "v"(vo), "s"(sr), "s"(so) : "memory")
DEV v4u mk_srd(const void* q, u32 bytes) { return (v4u){uu((u32)(size_t)q), uu((u32)((size_t)q >> 32) & 0xffffu), bytes, 0x00020000u}; }
#define DMANT(sr, ldsb, vo, so) asm volatile("s_mov_b32 m0, %0\n\ts_nop 0\n\tbuffer_load_dwordx4 %1, %2, %3 offen " INNT " lds" :: "s"(ldsb), "v"(vo), "s"(sr), "s"(so) : "memory")
#define AHEAD(sr, vo, so) asm volatile("buffer_load_dwordx4 %0, %1, %2, %3 offen" : "+v"(junk) : "v"(vo), "s"(sr), "s"(so) : "memory")
#define MK_SRD() const v4u srd = mk_srd(p.ws, W_END)

constexpr int CV_W = 40960;
template <bool B> struct CvL {
    static constexpr int NF = B ? 5 : 3, NK = B ? 4 : 2, KP = 544 + NK * 256, CH = NF * 1024 + 4 * KP;
};
constexpr int CV_Y = 4 * CvL<false>::CH, CV_YP = 272;
static_assert(CV_Y + 32 * CV_YP <= CV_W && 2 * CvL<true>::CH <= CV_Y, "conv lds");
DEV int cv_chan(int u, int i) { return 16 * (u % 40) + 4 * i + u / 40; }
template <bool B> DEV void cv_filt(const Params& p, u32 dst, int d, int i) {
    const u32 lane = threadIdx.x & 63;
    const v4u sf = mk_srd(p.ssmk + d * SEQ, SEQ * 2);
    if (i == 0) DMANT(sf, dst, lane < 4u ? OOB : lane * 16u - 64u, 0);
    else DMANT(sf, uu(dst + i * 1024), i == CvL<B>::NF - 1 && lane >= 4u ? OOB : lane * 16u, i * 1024 - 64);
}
DEV v4u cv_kvld(rsrc_t rs, int d, int i) { return ld16s(rs, (threadIdx.x & 63) * 16u, uu(W_KVT + (u32)d * (SEQ * 2) + i * 1024)); }
template <bool B> DEV void cv_kvput(lptr F, const v4u* k) {
    const int lane = threadIdx.x & 63;
#pragma unroll
    for (int i = 0; i < CvL<B>::NK; ++i) lds_w16(F + CvL<B>::NF * 1024 + (lane & 3) * CvL<B>::KP + 512 + i * 256 + (lane >> 2) * 16, k[i]);
}
template <bool B> DEV void cv_zero(lptr F) {
    const int lane = threadIdx.x & 63;
#pragma unroll
    for (int i = 0; i < 4; ++i) lds_w16(F + (lane >> 5) * CvL<B>::CH + CvL<B>::NF * 1024 + i * CvL<B>::KP + (lane & 31) * 16, (v4u){0u, 0u, 0u, 0u});
}
#define M32V(d, a, b) asm volatile("v_mfma_f32_32x32x16_bf16 %0, %1, %2, %0" : "+v"(d) : "v"(a), "v"(b))
template <bool B, class G> DEV void cv_run(const Params& p, rsrc_t rs, lptr F, int d, u32 D, f32x4 sp, float (&y)[16], G&& gap) {
    constexpr int KP = CvL<B>::KP, FO = B ? 2048 : 0;
    const int lane = threadIdx.x & 63, n = lane & 31, hh = lane >> 5, c = lane & 3;
    lptr fa = F + 64 + (32 * hh + 16 * (c & 1) + 8 * ((lane >> 4) & 1) + 4 * (c >> 1) - (lane & 12)) * 2;
    lptr kl = F + CvL<B>::NF * 1024 + (32 + n - hh) * 16;
    v4u dg;
#pragma unroll
    for (int q = 0; q < 4; ++q) { const int k = (lane & 35) ? -1 : (n & 4) + (n >> 3); dg[q] = k == 2 * q ? D : k == 2 * q + 1 ? D << 16 : 0u; }
    v4u l[4], h[4]; v2u fc[4], fh[4];
    f32x16 acc[4];
#pragma unroll
    for (int j = 0; j < 4; ++j) { l[j] = lds_r16(kl + j * KP); if constexpr (B) h[j] = lds_r16(kl + j * KP + 512); }
#pragma unroll
    for (int j = 0; j < 2; ++j) { fc[j] = lds_tr(fa + FO - 32 * j); if constexpr (B) fh[j] = lds_tr(fa - 32 * j); }
    asm volatile("s_nop 1" : "+v"(dg));
#pragma unroll
    for (int j = 0; j < 4; ++j) asm volatile("v_mfma_f32_32x32x16_bf16 %0, %1, %2, 0" : "=&v"(acc[j]) : "v"(dg), "v"(B ? h[j] : l[j]));
    constexpr int U = B ? 2 : 4;
    kl -= 32 * U; fa -= 32;
    auto body = [&](bool first) __attribute__((always_inline)) {
#pragma unroll
        for (int z = 0; z < U; ++z) {
            const int c = 2 * (z & 1);
            const v4u f = __builtin_shufflevector(fc[c], fc[c + 1], 0, 1, 2, 3), g = __builtin_shufflevector(fh[c], fh[c + 1], 0, 1, 2, 3);
#pragma unroll
            for (int j = 0; j < 4; ++j) {
                M32V(acc[j], f, l[j]); l[j] = lds_r16(kl + j * KP + 32 * (U - z - 1));
                if (j < 2) fc[(c ^ 2) + j] = lds_tr(fa + FO + (z + 1) * 128 + 32 - 32 * j);
                if (first && j > 0) gap(5 * z + j - 1);
                SCHED();
            }
            if constexpr (B) {
#pragma unroll
                for (int j = 0; j < 4; ++j) {
                    M32V(acc[j], g, h[j]); h[j] = lds_r16(kl + j * KP + 512 + 32 * (U - z - 1));
                    if (j < 2) fh[(c ^ 2) + j] = lds_tr(fa + (z + 1) * 128 + 32 - 32 * j);
                    if (first && j > 1) gap(5 * z + j + 1);
                    SCHED();
                }
            }
        }
        kl -= 32 * U; fa += 128 * U;
    };
    if constexpr (B) body(true);
#pragma unroll 1
    for (int m = B ? 2 * U : 0; m < 32; m += 2 * U) body(false);
    asm volatile("s_nop 11" : "+v"(acc[0]), "+v"(acc[1]), "+v"(acc[2]), "+v"(acc[3]));
    const float o[3] = {acc[1][15] + acc[2][14] + acc[3][13], acc[2][15] + acc[3][14], acc[3][15]};
    if constexpr (!B) st16(rs, lane == 63 ? W_SP + (u32)d * 16u : OOB, (v4u){u_(o[0]), u_(o[1]), u_(o[2]), 0u});
#pragma unroll
    for (int i = 0; i < 16; ++i) {
        float v = acc[0][i];
#pragma unroll
        for (int j = 1; j < 4; ++j) if (j <= i) v += acc[j][i - j];
        if (i < 3) { const float e = f_((u32)__builtin_amdgcn_ds_bpermute((hh ? lane - 32 : lane + 31) * 4, (int)u_(o[i]))); v += lane == 0 ? sp[i] : e; }
        y[i] = v;
    }
}
DEV void cv_put(lptr F, int pr, const float (&y0)[16], const float (&y1)[16]) {
    const int lane = threadIdx.x & 63;
#pragma unroll
    for (int i = 0; i < 16; ++i) *(LAS u32*)(F + CV_Y + (lane & 31) * CV_YP + (lane >> 5) * 128 + i * 8 + pr * 4) = pk(y0[i], y1[i]);
}
DEV void cv_out(rsrc_t rs, lptr F, int u, int blk0) {
    const int lane = threadIdx.x & 63;
    lptr q = F + CV_Y + ((lane >> 2) & 3) * CV_YP + ((lane >> 4) * 4 + (lane & 3)) * 8;
    const u32 yo = W_Y + (u32)(((lane >> 2) * 8 + blk0) * DM + 4 * u + (lane & 3)) * 16u;
#pragma unroll
    for (int i = 0; i < 8; ++i) { lptr a = q + (i >> 1) * (8 * CV_YP) + (i & 1) * 128; st16(rs, yo + (u32)((i & 1) * 128 + (i >> 1)) * (DM * 16), tr2(a, a + 4 * CV_YP)); }
}
struct KvIn { v2u k[19], v[16]; v4u s[2]; };
template <class F> DEV void kvt_load(const Params& p, int tile, KvIn& in, F&& mid) {
    const int lane = threadIdx.x & 63;
    const rsrc_t rk = mk_rsrc(p.bk, ACT), rv = mk_rsrc(p.bv, ACT), rh = mk_rsrc(p.shift, DM * 8);
    const int row = (tile / 10) * 64 + (lane >> 4) * 16, col = (tile % 10) * 64 + (lane & 15) * 4;
    const u32 vo = (u32)(row * DM + col) * 2u, vp = row > 0 ? vo - 3 * (DM * 2) : OOB;
#pragma unroll
    for (int i = 0; i < 2; ++i) in.s[i] = ldin16(rh, (u32)col * 8u + i * 16, 0);
#pragma unroll
    for (int i = 0; i < 19; ++i) { if (i == 7) mid(); in.k[i] = i < 3 ? ldin8(rk, vp, i * (DM * 2)) : ldin8(rk, vo, (i - 3) * (DM * 2)); if (i >= 3) in.v[i - 3] = ldin8(rv, vo, (i - 3) * (DM * 2)); }
}
DEV void kvt_make(rsrc_t rs, lptr xt, int tile, const KvIn& in) {
    const int lane = threadIdx.x & 63;
#pragma unroll
    for (int c = 0; c < 2; ++c) {
        const v4u sv = in.s[c];
        const f32x2 sh[4] = {{lo_f(sv.x), lo_f(sv.z)}, {hi_f(sv.x), hi_f(sv.z)}, {lo_f(sv.y), lo_f(sv.w)}, {hi_f(sv.y), hi_f(sv.w)}};
        f32x2 kf[19];
#pragma unroll
        for (int i = 0; i < 19; ++i) kf[i] = up2(in.k[i][c]);
        u32 kv[16];
#pragma unroll
        for (int i = 0; i < 16; ++i) kv[i] = pk2(((kf[i] * sh[3] + kf[i + 1] * sh[2]) + (kf[i + 2] * sh[1] + kf[i + 3] * sh[0])) * up2(in.v[i][c]));
#pragma unroll
        for (int q = 0; q < 8; ++q) {
            const u32 sel = (q & 1) ? 0x07060302u : 0x05040100u; const u32* z = kv + (q >> 1);
            *(LAS v2u*)(xt + ((2 * c + (q & 1)) * 16 + (lane & 15)) * 144 + (lane >> 5) * 64 + (q >> 1) * 16 + ((lane >> 4) & 1) * 8) =
                (v2u){__builtin_amdgcn_perm(z[4], z[0], sel), __builtin_amdgcn_perm(z[12], z[8], sel)};
        }
    }
    const u32 o = W_KVT + (u32)(((tile % 10) * 64 + 4 * (lane >> 3)) * SEQ + (tile / 10) * 64 + (lane & 7) * 8) * 2u;
#pragma unroll
    for (int i = 0; i < 8; ++i) st16(rs, o + (u32)(32 * (i & 1) + (i >> 1)) * (SEQ * 2), lds_r16(xt + (8 * i + (lane >> 3)) * 144 + (lane & 7) * 16));
}
struct RowX { v2u a, b; u32 c; };
DEV void ln_row(rsrc_t rs, int row, const RowX& r) {
    const f32x2 x[5] = {up2(r.a.x), up2(r.a.y), up2(r.b.x), up2(r.b.y), up2(r.c)};
    const f32x2 st = ln_stats(x);
    u32 hp[5];
#pragma unroll
    for (int q = 0; q < 5; ++q) hp[q] = pk2(up2(pkv(x[q] - st.x)) * st.y);
    store_h(rs, W_H1, row, hp);
}
DEV v4u mem0_load(const Params& p, int j) {
    const u32 n = (u32)(j * 256 + threadIdx.x);
    return ldin16(mk_rsrc(p.mem0, 8 * DM * 2), n < 640u ? ((2 * ((n >> 2) & 3) + (n & 1)) * DM + (n >> 4) * 16 + (n & 2) * 4) * 2u : OOB, 0);
}
DEV void pre_flags(rsrc_t rs, int j, v4u m) {
    const u32 n = (u32)(j * 256 + threadIdx.x);
#pragma unroll
    for (int i = 0; i < 4; ++i) m[i] = m[i] == PSENT ? 0x7FC17FC1u : m[i];
    st4s(rs, n - 36u * 256u < F_CLR ? foff(n - 36u * 256u) : OOB, 0, 0u);
    st16(rs, n < 640u ? W_MEM0 + (n >> 9) * 32768u + (n & 511u) * 16u : OOB, m);
}
constexpr int KT0 = NPRE * 4, LN0 = NPRE * 8;
struct Early { RowX r[2]; v4u m; };
DEV void conv_early(const Params& p, rsrc_t rs, int j, Early& e) {
    const int lane = threadIdx.x & 63, w = uni(threadIdx.x >> 6), u = j * 4 + w;
    if (j < NPRE) {
        const rsrc_t rx = mk_rsrc(p.x, ACT);
#pragma unroll
        for (int i = 0; i < 2; ++i) {
            const u32 so = uu((u + i * KT0) * (DM * 2));
            e.r[i].a = __builtin_amdgcn_raw_buffer_load_b64(rx, lane * 8u, so, 0); e.r[i].b = __builtin_amdgcn_raw_buffer_load_b64(rx, lane * 8u + 512u, so, 0);
            e.r[i].c = __builtin_amdgcn_raw_buffer_load_b32(rx, lane * 4u + 1024u, so, 0);
        }
        e.m = mem0_load(p, j);
    }
}
DEV void conv_stage(const Params& p, rsrc_t rs, lptr lds, int j, int g0, int ge, v2u ep, Early& e) {
    const int tid = threadIdx.x, lane = tid & 63, w = uni(tid >> 6), u = j * 4 + w;
    lptr F = lds + w * CV_W;
    const u32 fb = uu(w * CV_W);
    const rsrc_t rd = mk_rsrc(p.ssmD, DM * 2);
    float y0[16], y1[16];
    KvIn in;
    if (j < NPRE) {
        constexpr int CH = CvL<false>::CH;
        if (w == 1) LST(1500 + j, rt32());
        pre_flags(rs, j, e.m);
        ln_row(rs, u, e.r[0]); ln_row(rs, u + KT0, e.r[1]);
        if (w == 1) LST(1550 + j, rt32());
        CFENCE();
        kvt_load(p, u, in, [&] { VMW(13); BAR(); pre_post(rs, j, 1u, ep); if (w == 0) LST(1450 + j, rt32()); });
        if (j == 20) LST(260, rt32());
#pragma unroll 1
        for (u32 o = (u32)(j * 256 + tid) * 16u; o < 2 * MB; o += NPRE * 4096) st16(rs, o, (v4u){PSENT, PSENT, PSENT, PSENT});
        const u32 D = __builtin_amdgcn_raw_buffer_load_b16(rd, (u32)cv_chan(u, lane & 3) * 2u, 0, 0);
#pragma unroll 1
        for (int i = 0; i < 4; ++i) { const int d = cv_chan(u, i); cv_filt<false>(p, fb + i * CH, d, 0); cv_filt<false>(p, fb + i * CH, d, 1); cv_filt<false>(p, fb + i * CH, d, 2); }
        kvt_make(rs, F + CV_Y, u, in);
        cv_zero<false>(F); cv_zero<false>(F + 2 * CH);
        DRAIN(); BAR();
        pre_post(rs, j, 2u, ep);
        if (j == 20) LST(262, rt32());
        pre_wait(rs, 0, NPRE, 2u, ep);
        if (j == 20) LST(281, rt32());
        {
            v4u k[8];
#pragma unroll
            for (int i = 0; i < 8; ++i) k[i] = cv_kvld(rs, cv_chan(u, i >> 1), i & 1);
#pragma unroll
            for (int i = 0; i < 4; ++i) cv_kvput<false>(F + i * CH, k + 2 * i);
        }
        if (j == 20) LST(286, rt32());
#pragma unroll 1
        for (int i = 0; i < 4; i += 2) {
            cv_run<false>(p, rs, F + i * CH, cv_chan(u, i), rdl(D, i), f32x4{}, y0, [](int) {});
            cv_run<false>(p, rs, F + (i + 1) * CH, cv_chan(u, i + 1), rdl(D, i + 1), f32x4{}, y1, [](int) {});
            cv_put(F, i >> 1, y0, y1);
        }
        if (j == 20) LST(287, rt32());
        cv_out(rs, F, u, 0);
        if (j == 20) LST(263, rt32());
    } else {
        const v2u vote = pol_probe(rs, u - NPRE * 4);
        st16(rs, lane == 0 ? foff(F_PRE + 48 + u - NPRE * 4) : OOB, (v4u){ep.x + 2u, ep.y, vote.x, vote.y});
#pragma unroll 1
        for (int t = u + 128; t < 320; t += 16) { kvt_load(p, t, in, [] {}); kvt_make(rs, F, t, in); }
    }
    DRAIN(); BAR();
    if (g0 < ge) {
        constexpr int CH = CvL<true>::CH;
        pre_post(rs, j, 3u, ep);
        cv_zero<true>(F);
        if (w == 0) { pre_poll(rs, 64, 32, 3u, ep); pre_poll(rs, 0, NCV, 3u, ep); }
        BAR();
        v4u k[4];
        auto fetch = [&](u32 dst, int d, int i) { if (i < 5) cv_filt<true>(p, dst, d, i); else if (i < 9) k[i - 5] = cv_kvld(rs, d, i - 5); };
#pragma unroll
        for (int i = 0; i < 9; ++i) fetch(fb, cv_chan(g0 * 4 + w, 0), i);
        const u32 ch = (u32)cv_chan(min((g0 + 8 * (lane >> 2)) * 4 + w, 159), lane & 3);
        u32 D = __builtin_amdgcn_raw_buffer_load_b16(rd, ch * 2u, 0, 0);
        v4u sp = ld16s(rs, W_SP + ch * 16u, 0);
        asm volatile("" : "+v"(D), "+v"(sp));
#pragma unroll 1
        for (int g = g0, n = 0; g < ge; g += 8) {
            const int un = g * 4 + w;
            if (g == 16) { LST(282, rt32()); LST(288, __builtin_amdgcn_s_memtime()); }
#pragma unroll 1
            for (int i = 0; i < 4; i += 2, n += 2) {
                const int d1 = (int)rdl(ch, n + 1), d2 = (int)rdl(ch, n + 2);
                cv_kvput<true>(F, k);
                cv_run<true>(p, rs, F, 0, rdl(D, n), (f32x4){f_(rdl(sp.x, n)), f_(rdl(sp.y, n)), f_(rdl(sp.z, n)), 0.f}, y0, [&](int q) { fetch(fb + CH, d1, q); });
                cv_kvput<true>(F + CH, k);
                cv_run<true>(p, rs, F + CH, 326, rdl(D, n + 1), (f32x4){f_(rdl(sp.x, n + 1)), f_(rdl(sp.y, n + 1)), f_(rdl(sp.z, n + 1)), 0.f}, y1, [&](int q) { fetch(fb, d2, q); });
                cv_put(F, i >> 1, y0, y1);
            }
            cv_out(rs, F, un, 4);
            if (g == 16) { LST(283, rt32()); LST(289, __builtin_amdgcn_s_memtime()); }
        }
        DRAIN(); BAR();
    }
    pre_post(rs, j, 5u, ep);
    if (j == 20) LST(284, rt32());
    if (j == 3) LST(285, rt32());
}

#define M32A(first, d, a, b) do { if (first) asm volatile("v_mfma_f32_32x32x16_bf16 %0, %1, %2, 0" : "=&v"(d) : "a"(a), "v"(b)); \
                                  else asm volatile("v_mfma_f32_32x32x16_bf16 %0, %1, %2, %0" : "+v"(d) : "a"(a), "v"(b)); } while (0)
#define M32B(first, d, a, b) do { if (first) asm volatile("v_mfma_f32_32x32x16_bf16 %0, %1, %2, 0" : "=&v"(d) : "v"(a), "a"(b)); \
                                  else asm volatile("v_mfma_f32_32x32x16_bf16 %0, %1, %2, %0" : "+v"(d) : "v"(a), "a"(b)); } while (0)
#define VCVT(d, a, b) asm volatile("v_cvt_pk_bf16_f32 %0, %1, %2" : "=v"(d) : "v"(a), "v"(b))
#define VADD(d, a, b) asm volatile("v_add_f32 %0, %1, %2" : "=v"(d) : "v"(a), "v"(b))
#define LD4A(d, vo, so) asm volatile("buffer_load_dword %0, %1, %2, %3 offen sc1" : "=v"(d) : "v"(vo), "s"(srd), "s"(so) : "memory")
DEV u32 wst_src(int lane, int ld) { return (u32)((lane >> 4) * ld + 8 * ((lane & 15) ^ ((lane >> 4) << 2))) * 2u; }
DEV u32 wst_rd(int lane, int w) { const int q4 = (lane >> 2) & 3; return (u32)(q4 * 256 + ((w ^ q4) * 64) + ((lane >> 4) & 1) * 32 + (lane & 3) * 8); }

constexpr int QK_SEL = 81920;
#define QK_LEAD 12
#define QK_SMP 29
#define QK_YB 26
#define QK_W0 2
DEV void role_qkv(const Params& p, lptr lds, int c, v2u ep) {
    const int tid = threadIdx.x, lane = tid & 63, w = uni(tid >> 6), hh = lane >> 5, r32 = lane & 31;
    const int m = c >> 5, h = c & 31;
    const rsrc_t rs = mk_rsrc(p.ws, W_END);
    v4u wt[40];
    {
        const v4u sw = mk_srd(m == 0 ? p.Wq : m == 1 ? p.Wk : p.Wv, DM * NQ * 2), sx = mk_srd(p.x, ACT);
        const u32 vo = wst_src(lane, NQ) + (u32)h * (DH * 2), x8 = (u32)lane * 8u, x4 = (u32)lane * 4u;
        RowX xr[5];
        auto wfill = [&](int i0, int n) {
#pragma unroll 1
            for (int i = i0; i < i0 + n; ++i) DMAIN(sw, uu((4 * i + w) * 1024), vo, uu((16 * i + 4 * w) * (NQ * 2)));
        };
        wfill(0, QK_W0);
#pragma unroll
        for (int i = 0; i < 5; ++i)
            asm volatile("buffer_load_dwordx2 %0, %3, %5, %6 offen\n\tbuffer_load_dwordx2 %1, %3, %5, %6 offen offset:512\n\tbuffer_load_dword %2, %4, %5, %6 offen offset:1024"
                         : "=&v"(xr[i].a), "=&v"(xr[i].b), "=&v"(xr[i].c) : "v"(x8), "v"(x4), "s"(sx), "s"(uu((LN0 + 4 * c + w + 384 * i) * (DM * 2))) : "memory");
        wfill(QK_W0, 10 - QK_W0);
        if (c == 3) LST(275, rt32());
        VMW(10 - QK_W0);
        if (c == 3) LST(276, rt32());
#pragma unroll
        for (int i = 0; i < 5; ++i) {
            asm volatile("" : "+v"(xr[i].a), "+v"(xr[i].b), "+v"(xr[i].c));
            const int row = LN0 + 4 * c + w + 384 * i;
            if (i < 4 || row < SEQ) ln_row(rs, row, xr[i]);
            wfill(10 + 6 * i, 6);
        }
        if (c == 3) LST(264, rt32());
        lptr tb = lds + hh * 2048 + wst_rd(lane, w);
        SFOR(k, 40)
            if constexpr (k == 0) { VMW(15); BAR(); }
            if constexpr (k == 28) {
                if (w == 0) pre_poll(rs, 0, NPRE, 1u, ep);
                DRAIN(); BAR();
                pre_post(rs, 96 + c, 1u, ep);
            }
            lptr q = tb + (k >> 3) * 32768 + (k & 7) * 4096; wt[k] = tr2(q, q + 1024); asm volatile("" : "+a"(wt[k]));
        SEND
    }
    if (c == 3) LST(268, rt32());
    if (w == 0) LST(1350 + c, rt32());
    bool dead = false;
    int n0, n1;
    const u32 t16 = (u32)(w * 2048 + lane * 16), wb = uu(w * 2048);
    u32 ar[16];
#pragma unroll
    for (int j = 0; j < 16; ++j) ar[j] = (u32)(r32 * 256 + ((2 * (j & 7) + hh) ^ (r32 & 15)) * 16 + (j >> 3) * 49152);
    MK_SRD();
#define XRD(y, ks) lds_r16(lds + ar[((ks) & 7) + ((ks) >= 24 ? 8 : 0)] + ((ks) >> 3) % 3 * 16384 + (y) * 8192)
#define QM0(y, b) asm volatile("s_add_u32 m0, %0, %1" :: "s"(wb), "i"((b) * 16384 + (y) * 8192) : "scc", "memory")
#define QDMA(i, vo) asm volatile("buffer_load_dwordx4 %0, %1, %2 offen offset:%3 sc1 lds" :: "v"(vo), "s"(srd), "s"(((i) >> 1) * 8192), "i"(((i) & 1) * 1024) : "memory")
    const u32 h2flag = (lane < 16 && w == 0) ? foff(F_H2 + lane) : OOB;
    const u32 ol = (m == 2 ? (u32)(2 * w + (r32 >> 4)) * 1024u : (u32)((r32 >> 4) * 4 + w) * 1024u) + (u32)(hh * 16 + (r32 & 15)) * 16u
                 + (m == 2 ? W_VO : m == 1 ? W_KQ : W_KQ + 8192u) + (u32)h * 16384u;
    const u32 fo = tid < 2 ? foff(F_QKV + tid * 96 + c) : OOB;
    lptr selw = lds + QK_SEL + (w == 0 ? 0 : 64), selr = lds + QK_SEL;
    PIN(selw);
    static_assert(QK_LEAD > 8 && QK_LEAD % 2 == 0 && LN0 == 10 * 32, "X = 8 comes up, before any item of layer 1");
    static_assert(W_H2 - W_H1 == 2 * 64 * (32 * DM * 2), "h");
    auto run = [&](auto isv) __attribute__((always_inline)) {
        constexpr bool V = decltype(isv)::value;
        constexpr int YB = QK_YB;
        f32x16 ax, ay = {};
        v4u x[8], pp, pq;
        u32 xv = t16 + W_H1, ox = ol, oy = OOB, nn = 1, lt = 0, fcnt = 0, fa = OOB;
        asm volatile("" : "+v"(lt), "+v"(nn));
#pragma unroll
        for (int z = 0; z < 5; ++z) x[z] = XRD(1, 40 - YB + z);
#define QPK(r_, a1, a_, q_) do { if constexpr (V) VCVT(r_[q_], a1[4 * (a_) + 8 * ((q_) >> 1) + 2 * ((q_) & 1)], a1[4 * (a_) + 8 * ((q_) >> 1) + 2 * ((q_) & 1) + 1]); \
                                 else VCVT(r_[q_], a1[4 * (a_) + (q_)], a1[4 * (a_) + (q_) + 8]); } while (0)
#define QPK4(r_, a1, a_) do { QPK(r_, a1, a_, 0); QPK(r_, a1, a_, 1); QPK(r_, a1, a_, 2); QPK(r_, a1, a_, 3); } while (0)
        for (;;) {
            u32 ls = uu(lt), yv, fv, nx, need;
            if (ls != 0u && uu(nn) < 256u) pre_wait(rs, 96, 96, 1u, ep);
            else if (ls != 0u) {
                n1 = (int)(uu(nn) >> 8);
                CFENCE();
                const u32 hf = h2flag + (u32)(~n1 & 1) * 2048u;
                u32 f2 = ld4(rs, hf);
                asm volatile("s_waitcnt vmcnt(0)" : "+v"(f2) :: "memory");
                *(LAS u32*)(selw + 16) = (u32)__builtin_amdgcn_ballot_w64(f2 < (u32)n1) & 0xFFFFu;
                BAR();
                st4s(rs, fa, 0, fcnt);
                fa = OOB;
                if (n1 > NSEG) { LDUMP(w == 0, 64 + c); __builtin_amdgcn_endpgm(); }
                if (uu(*(LAS u32*)(selr + 16)) != 0u) {
                    if (w == 0) wait_flags(rs, hf, (u32)n1, dead);
                    BAR();
                }
            }
#define QT(k) do {} while (0)
            QT(0);
            SCHED();
            QM0(0, 0); NOPS(0); QDMA(0, xv); QDMA(1, xv); QM0(0, 1);
            SFOR(z, 80)
                constexpr int ks = (z + 40 - YB) % 40, zx = z - YB;
                constexpr auto upto = [](int q) { const int d = (q - YB + 49) % 40; return q + 6 + (d == 0 || d == 2 ? 1 : d == 1 ? 2 : 0); };
                if constexpr (zx >= 0 && zx < 40) { if constexpr (V) M32B(ks == 0, ax, x[z & 7], wt[ks]); else M32A(ks == 0, ax, wt[ks], x[z & 7]); }
                else { if constexpr (V) M32B(ks == 0, ay, x[z & 7], wt[ks]); else M32A(ks == 0, ay, wt[ks], x[z & 7]); }
                if constexpr (z == 78) lt = *(LAS u32*)selr;
#pragma unroll
                for (int q = upto(z - 1); q < upto(z); ++q) x[q & 7] = XRD(q % 80 < YB || q % 80 >= YB + 40, (q + 40 - YB) % 40);
                SCHED();
                (void)&srd; (void)&wb;
                if constexpr (z < 2) QDMA(z + 2, xv);
                if constexpr (z >= 2 && z < 14 && (z & 3) == 2) QM0(0, (z + 6) >> 2);
                if constexpr (z >= 3 && z < 15 && (z & 1)) QDMA((z + 5) >> 1, xv);
                if constexpr (z == 4) { n0 = uni((int)nn); n1 = n0 >> 8; n0 &= 255; PINS(n0); PINS(n1); }
                if constexpr (z == 8) { nx = n0 < NSEG ? W_H1 + (u32)n0 * (32 * DM * 2) : OOB; PINS(nx); }
                if constexpr (z == 12) { yv = t16 + nx; PIN(yv); }
                if constexpr (zx == -6) { QT(1); asm volatile("s_waitcnt vmcnt(6) lgkmcnt(0)\n\ts_barrier" ::: "memory"); QT(2); }
                if constexpr (zx == -5) st4s(rs, fa, 0, fcnt);
                if constexpr (zx == -4 || zx == 0 || zx == 4) QM0(1, (zx + 4) >> 2);
                if constexpr (zx >= -3 && zx < 8 && (zx & 1)) QDMA((zx + 3) >> 1, yv);
                if constexpr (zx == 2) QPK4(pp, ay, 0);
                if constexpr (zx == 6) QPK4(pq, ay, 1);
                if constexpr (zx == 8) st16(rs, oy, pp);
                if constexpr (zx == 9) st16(rs, oy + 512, pq);
                if constexpr (zx == 10) { QT(3); asm volatile("s_waitcnt vmcnt(9)\n\ts_barrier" ::: "memory"); QT(4); }
                if constexpr (zx == 11 || zx == 15) QM0(1, (zx + 1) >> 2);
                if constexpr (zx >= 12 && zx < 20 && (zx & 1) == 0) QDMA(zx >> 1, yv);
                if constexpr (zx == 21) { fcnt = lane == 0 ? (u32)n0 : (u32)n1; fa = fo; asm volatile("" : "+v"(fcnt), "+v"(fa)); }
                if constexpr (zx == 22) { oy = (ol + (u32)n0 * (NH * 16384u)) | (yv & OOB); PIN(oy); }
                if constexpr (zx == 23) { n0 = min(n0 + 1, NSEG); PINS(n0); nx = (u32)min(NSEG, n1 + QK_LEAD); PINS(nx); }
                if constexpr (zx == 24)
                    asm volatile("s_cmp_ge_u32 %1, %0\n\ts_cselect_b32 %0, %4, %1\n\ts_addc_u32 %2, %2, 0\n\ts_cmp_lt_u32 %0, 64\n\ts_cselect_b32 %3, 0, %2"
                                 : "+s"(nx), "+s"(n0), "+s"(n1), "=&s"(need) : "s"(n1 + 64) : "scc");
                if constexpr (zx == 25) { n0 += 1 - (int)min(nx >> 6, 1u); PINS(n0); }
                if constexpr (zx == 26) { nn = (u32)(n0 | n1 << 8); PIN(nn); }
                if constexpr (zx == 27) { ls = W_H1 + (nx + (nx & 64u)) * (32 * DM * 2); PINS(ls); }
                if constexpr (zx == 28) { xv = t16 + ls; PIN(xv); need = nx == 8u ? 255u : need; PINS(need); }
                if constexpr (zx == 34) { asm volatile("s_waitcnt vmcnt(%0) lgkmcnt(0)\n\ts_barrier" :: "i"(QK_SMP < 34 ? 7 : 6) : "memory"); QT(5); }
                if constexpr (zx == QK_SMP) { const u32 po = (nx & 1u) * 2048u; LD4A(fv, h2flag, po); }
                if constexpr (zx == 42) QPK4(pp, ax, 0);
                if constexpr (zx == 43) st16(rs, ox, pp);
                if constexpr (zx == 44) QPK4(pq, ax, 1);
                if constexpr (zx == 45) st16(rs, ox + 512, pq);
                if constexpr (zx == 47) { ox = ol + nx * (NH * 16384u); PIN(ox); }
                if constexpr (z == 74) { asm volatile("s_waitcnt vmcnt(2)" : "+v"(fv) :: "memory"); QT(6); ls = (u32)__builtin_amdgcn_ballot_w64(fv < need) & 0xFFFFu; PINS(ls); }
                if constexpr (z == 75) { lt = ls; PIN(lt); *(LAS u32*)selw = lt; }
                if constexpr (z == 77) asm volatile("s_waitcnt lgkmcnt(2)\n\ts_barrier" ::: "memory");
                SCHED();
            SEND
        }
    };
    if (m == 2) run(IC<1>{}); else run(IC<0>{});
}

DEV void role_out(const Params& p, lptr lds, int L, int kq, int ng, v2u ep) {
    const int tid = threadIdx.x, lane = tid & 63, w = uni(tid >> 6), hh = lane >> 5, r32 = lane & 31;
    const rsrc_t rs = mk_rsrc(p.ws, W_END);
    v4u wo[64];
    bool dead = false;
    {
        const v4u sw = mk_srd(p.Wo, DM * NQ * 2);
        const u32 vo = wst_src(lane, DM) + (u32)ng * 256u;
        auto fill = [&](int b) {
#pragma unroll 1
            for (int i = 0; i < 8; ++i) DMAIN(sw, uu((b % 5) * 32768 + (4 * i + w) * 1024), vo, uu((kq * 1024 + b * 128 + 16 * i + 4 * w) * (DM * 2)));
        };
#pragma unroll 1
        for (int b = 0; b < 5; ++b) fill(b);
        lptr tb = lds + hh * 1024 + wst_rd(lane, w);
        SFOR(b, 8)
            VMW(b < 4 ? 32 : 56 - 8 * b); BAR();
            lptr q = tb + (b % 5) * 32768;
#pragma unroll
            for (int k = 0; k < 8; ++k) { lptr a = q + (k >> 1) * 8192 + (k & 1) * 2048; wo[8 * b + k] = tr2(a, a + 4096); if (b < 7) asm volatile("" : "+a"(wo[8 * b + k])); else PIN(wo[8 * b + k]); }
            if constexpr (b < 3) { BAR(); fill(b + 5); }
        SEND
    }
    pre_wait(rs, 0, NPRE, 1u, ep);
    if (kq == 0 && ng == 0) LST(272 + L, rt32());
    MK_SRD();
    const u32 ofl = lane < 8 ? foff(F_O + L * 32 + kq * 8 + lane) : OOB, l16 = (u32)lane * 16u;
    const u32 fo = (tid == 0) ? foff(F_P + L * 32 + kq * 5 + ng) : OOB;
#define OM0(dst, b) asm volatile("s_add_u32 m0, %0, %1" :: "s"(dst), "i"((b) * 8192) : "scc", "memory")
#define ODMA(i, vo) asm volatile("buffer_load_dwordx4 %0, %1, %2 offen offset:%3 sc1 lds" :: "v"(vo), "s"(srd), "s"(((i) >> 1) * 16384), "i"(((i) & 1) * 1024) : "memory")
    auto first = [&](u32 dst, u32 vo) {
#pragma unroll
        for (int b = 0; b < 5; ++b) { OM0(dst, b); NOPS(0); ODMA(2 * b, vo); ODMA(2 * b + 1, vo); }
    };
    u32 pa = W_P + (u32)(L * NSEG) * P_ITEM + (u32)(2 * hh * 4 + kq) * (DM * 4) + (u32)(ng * 128 + w * 32 + r32) * 4u, fv = 0;
    u32 ov = W_VO + 8192u + (u32)(L * NSEG * NH + kq * 8) * 16384u + (u32)w * 2048u + l16;
    bool early = false;
    for (int s = 0; s < NSEG; ++s) {
        const u32 dst = uu((s & 1) * 65536 + w * 2048), ndst = dst ^ 65536u, need = s + 1 < NSEG ? (u32)s + 2u : 255u;
        lptr A = lds + (s & 1) * 65536 + l16;
        if (!early) {
            if (!all_(ofl == OOB || fv >= (u32)s + 1)) wait_flags(rs, ofl, (u32)s + 1, dead);
            LS3(L == 0 && w == 1, 1, kq * 5 + ng);
            first(dst, ov);
        }
#define OT(k) do {} while (0)
        OM0(dst, 5);
        asm volatile("s_waitcnt vmcnt(8)\n\ts_barrier" ::: "memory");
        OT(0);
        v4u o[64];
#pragma unroll
        for (int k = 0; k < 4; ++k) o[k] = lds_r16(A + k * 1024);
        f32x16 acc;
        u32 far, nv;
        SCHED();
        SFOR(k, 64)
            if constexpr (k < 56) M32B(k == 0, acc, o[k], wo[k]); else M32V(acc, o[k], wo[k]);
            if constexpr (k < 60) o[k + 4] = lds_r16(A + (k + 4) * 1024);
            SCHED();
            (void)&srd; (void)&dst; (void)&ndst;
            if constexpr (k < 12 && (k & 1) == 0) ODMA(10 + k / 2, ov);
            if constexpr (k == 3) asm volatile("s_waitcnt vmcnt(6)\n\ts_barrier" ::: "memory");
            if constexpr (k == 3 || k == 7) OM0(dst, (k + 21) >> 2);
            if constexpr (k == 12) { ov += NH * 16384u; PIN(ov); }
            if constexpr (k == 19) asm volatile("s_waitcnt vmcnt(6)\n\ts_barrier" ::: "memory");
            if constexpr (k == 20) LD4A(fv, ofl, 0);
            if constexpr (k == 35) asm volatile("s_waitcnt vmcnt(1)\n\ts_barrier" ::: "memory");
            if constexpr (k == 40) { asm volatile("s_waitcnt vmcnt(0)" : "+v"(fv) :: "memory"); far = (u32)__builtin_amdgcn_ballot_w64(fv < need) & 255u; PINS(far); }
            if constexpr (k == 41) { nv = far ? OOB : ov; PIN(nv); }
            if constexpr (k >= 42 && k < 62 && (k & 3) == 2) OM0(ndst, (k - 42) >> 2);
            if constexpr (k >= 43 && k < 63 && (k & 1)) ODMA((k - 43) >> 1, nv);
            if constexpr (k == 48) LD4A(fv, ofl, 0);
            if constexpr (k == 4) OT(1);
            if constexpr (k == 20) OT(2);
            if constexpr (k == 36) OT(3);
            if constexpr (k == 41) OT(4);
            SCHED();
        SEND
        OT(5);
        asm volatile("s_nop 11" : "+v"(acc), "+v"(pa));
#pragma unroll
        for (int i = 0; i < 8; ++i) st4s(rs, pa, (u32)((4 * (i >> 1) + (i & 1)) * (4 * DM * 4)), pk(acc[2 * i], acc[2 * i + 1]));
        asm volatile("s_waitcnt vmcnt(15)" : "+v"(fv) :: "memory");
        OT(6);
        LS3(L == 0 && w == 1, 2, kq * 5 + ng);
        early = far == 0u || all_(ofl == OOB || fv >= need);
        if (far != 0u && early) { first(ndst, ov); VMW(10); }
        else { fv = ld4(rs, ofl); asm volatile("s_waitcnt vmcnt(0)" : "+v"(fv) :: "memory"); }
        BAR();
        st4s(rs, fo, 0, (u32)s + 1);
        pa += P_ITEM;
        LS2(L == 0 && w == 1, 160 + kq * 5 + ng);
    }
    LDUMP(L == 0 && w == 1, kq * 5 + ng);
}

DEV void ln_stats2(const f32x2 (&x)[10], f32x2& mean, f32x2& rstd) {
    const f32x2 c = {f_(uu(u_(x[0].x))), f_(uu(u_(x[0].y)))};
    f32x2 d[10];
#pragma unroll
    for (int i = 0; i < 10; ++i) d[i] = x[i] - c;
    f32x2 a = ((d[0] + d[1]) + (d[2] + d[3])) + ((d[4] + d[5]) + (d[6] + d[7])) + (d[8] + d[9]);
    f32x2 b = (d[0] * d[0] + d[2] * d[2] + d[4] * d[4] + d[6] * d[6] + d[8] * d[8]) + (d[1] * d[1] + d[3] * d[3] + d[5] * d[5] + d[7] * d[7] + d[9] * d[9]);
    float a0, a1, b0, b1;
#define LN_DP4(m) "v_add_f32_dpp %0, %0, %0 " m " row_mask:0xf bank_mask:0xf\n\tv_add_f32_dpp %1, %1, %1 " m " row_mask:0xf bank_mask:0xf\n\t" \
                  "v_add_f32_dpp %2, %2, %2 " m " row_mask:0xf bank_mask:0xf\n\tv_add_f32_dpp %3, %3, %3 " m " row_mask:0xf bank_mask:0xf\n\t"
    asm("s_nop 1\n\t" LN_DP4("quad_perm:[1,0,3,2]") LN_DP4("quad_perm:[2,3,0,1]") LN_DP4("row_ror:4") LN_DP4("row_ror:8")
        "v_permlane16_swap_b32 %0, %1\n\tv_add_f32 %0, %0, %1\n\tv_permlane16_swap_b32 %2, %3\n\tv_add_f32 %2, %2, %3\n\ts_nop 1\n\t"
        "v_permlane32_swap_b32 %0, %2\n\tv_add_f32 %0, %0, %2\n\ts_nop 0\n\t"
        "v_readlane_b32 %4, %0, 0\n\tv_readlane_b32 %5, %0, 16\n\tv_readlane_b32 %6, %0, 32\n\tv_readlane_b32 %7, %0, 48\n\ts_nop 1"
        : "+v"(a.x), "+v"(b.x), "+v"(a.y), "+v"(b.y), "=s"(a0), "=s"(b0), "=s"(a1), "=s"(b1));
    const f32x2 m = (f32x2){a0, a1} * (1.0f / DM), v = (f32x2){b0, b1} * (1.0f / DM) - m * m;
    const f32x2 q = up2(pkv(up2(pkv((f32x2){fmaxf(v.x, 0.f), fmaxf(v.y, 0.f)})) + 1e-5f));
    const f32x2 sd = up2(pk(__builtin_amdgcn_sqrtf(q.x), __builtin_amdgcn_sqrtf(q.y)));
    mean = up2(pkv(c + m)); rstd = (f32x2){__builtin_amdgcn_rcpf(sd.x), __builtin_amdgcn_rcpf(sd.y)};
}
DEV void role_row(const Params& p, lptr lds, int id, v2u ep) {
    const int lane = threadIdx.x & 63, w = uni(threadIdx.x >> 6);
    const int pr = id >> 2, L = id & 1, par = (id >> 1) & 1;
    const rsrc_t rs = mk_rsrc(p.ws, W_END), rx = mk_rsrc(p.x, ACT), ro_ = mk_rsrc(p.out, ACT);
    bool dead = false;
    const u32 fp = lane < 20 ? foff(F_P + L * 32 + lane) : OOB, l16 = (u32)lane * 16u, l8 = (u32)lane * 8u, l4 = (u32)lane * 4u, fb = uu(w * CV_W);
    lptr F = lds + w * CV_W;
    MK_SRD();
    for (int s = par; s < NSEG; s += 2) {
        const u32 ro = (u32)(s * 32 + 2 * pr) * (DM * 2);
        if ((s & 6) == 0) {
            if ((s & 30) == 0) pre_poll(rs, 0, s < 32 ? NPRE : NCV, s < 32 ? 3u : 5u, ep);
            if (id == 0 && s == 0) LST(274, rt32());
            if (s == 48) st8s(rs, (id == 1 && lane == 0) ? foff(F_EPOCH) : OOB, 0, (v2u){ep.x + 8u, ep.y});
#pragma unroll 1
            for (int i = 0; i < 5; ++i) {
                const u32 d = fb + i * 2048, o = W_Y + (u32)(16 * pr + (s >> 3)) * 10240u + i * 2048;
                DMA32(d, l16, o); DMA32(d + 10240u, l16, o + 81920u);
            }
            DRAIN();
        }
        RowX xr[2];
        auto xload = [&](rsrc_t r, u32 o, auto aux) {
#pragma unroll
            for (int i = 0; i < 2; ++i) {
                xr[i].a = __builtin_amdgcn_raw_buffer_load_b64(r, l8, o + i * (DM * 2), aux.value); xr[i].b = __builtin_amdgcn_raw_buffer_load_b64(r, l8 + 512u, o + i * (DM * 2), aux.value);
                xr[i].c = __builtin_amdgcn_raw_buffer_load_b32(r, l4 + 1024u, o + i * (DM * 2), aux.value);
            }
        };
        if (L == 0) xload(rx, ro, IC<0>{});
        f32x2 y[10], x[10];
        {
            lptr ya = F + (s & 7) * 2 + l16, yc = F + (s & 7) * 2 + 2048 + (lane >> 1) * 16 + (lane & 1) * 5120;
#pragma unroll
            for (int i = 0; i < 10; ++i) {
                lptr q = i < 8 ? ya + (i & 3) * 2560 + (i >> 2) * 1024 : yc + (i & 1) * 2560;
                y[i] = (f32x2){lo_f(*(LAS u16*)q), lo_f(*(LAS u16*)(q + 10240))}; PIN(y[i]);
            }
        }
        wait_flags(rs, fp, (u32)s + 1, dead);
        LS3(L == 0, 3, ri);
        LMX(L == 0, 0, s, rt32());
        {
            const u32 o = uu(W_P + (u32)(L * NSEG + s) * P_ITEM + (u32)pr * (4 * DM * 4));
            if (L == 1) xload(rs, W_X2 + ro, IC<16>{});
            v4u a[4], b[4]; v2u c[4];
#pragma unroll
            for (int kq = 0; kq < 4; ++kq) { a[kq] = ld16s(rs, l16, o + kq * (DM * 4)); SCHED(); b[kq] = ld16s(rs, l16 + 1024u, o + kq * (DM * 4)); SCHED(); c[kq] = ld8s(rs, l8 + 2048u, o + kq * (DM * 4)); SCHED(); }
            const u32 u0[5] = {xr[0].a.x, xr[0].a.y, xr[0].b.x, xr[0].b.y, xr[0].c}, u1[5] = {xr[1].a.x, xr[1].a.y, xr[1].b.x, xr[1].b.y, xr[1].c};
#pragma unroll
            for (int i = 0; i < 5; ++i) { x[2 * i] = (f32x2){lo_f(u0[i]), lo_f(u1[i])}; x[2 * i + 1] = (f32x2){hi_f(u0[i]), hi_f(u1[i])}; }
#pragma unroll
            for (int kq = 0; kq < 4; ++kq)
#pragma unroll
                for (int i = 0; i < 10; ++i)
                    asm volatile("v_dot2_f32_bf16 %0, %2, %3, %0\n\tv_dot2_f32_bf16 %1, %2, %4, %1" : "+v"(x[i].x), "+v"(x[i].y)
                                 : "v"(i < 4 ? a[kq][i & 3] : i < 8 ? b[kq][i & 3] : c[kq][i & 1]), "s"(0x3F80u), "s"(0x3F800000u));
            asm volatile("s_nop 2" : "+v"(x[8]), "+v"(x[9]));
        }
        LS3(L == 0, 4, ri);
        f32x2 mean, rstd;
        ln_stats2(x, mean, rstd);
#pragma unroll
        for (int i = 0; i < 10; ++i) x[i] += (x[i] - mean) * (y[i] * rstd);
        u32 xp[2][5];
#pragma unroll
        for (int i = 0; i < 5; ++i) { xp[0][i] = pkv((f32x2){x[2 * i].x, x[2 * i + 1].x}); xp[1][i] = pkv((f32x2){x[2 * i].y, x[2 * i + 1].y}); }
        if (L == 0) {
#pragma unroll
            for (int i = 0; i < 5; ++i) { x[2 * i] = (f32x2){lo_f(xp[0][i]), lo_f(xp[1][i])}; x[2 * i + 1] = (f32x2){hi_f(xp[0][i]), hi_f(xp[1][i])}; }
            ln_stats2(x, mean, rstd);
#pragma unroll
            for (int i = 0; i < 10; ++i) x[i] = (x[i] - mean) * rstd;
#pragma unroll
            for (int r = 0; r < 2; ++r) {
                u32 hp[5];
#pragma unroll
                for (int i = 0; i < 5; ++i) hp[i] = pkv((f32x2){x[2 * i][r], x[2 * i + 1][r]});
                store_h(rs, W_H2, s * 32 + 2 * pr + r, hp);
            }
#pragma unroll
            for (int r = 0; r < 2; ++r) {
                st8s(rs, l8, W_X2 + ro + r * (DM * 2), (v2u){xp[r][0], xp[r][1]});
                st8s(rs, l8 + 512u, W_X2 + ro + r * (DM * 2), (v2u){xp[r][2], xp[r][3]});
                st4s(rs, l4 + 1024u, W_X2 + ro + r * (DM * 2), xp[r][4]);
            }
            LS3(L == 0, 5, ri);
            DRAIN();
            st4s(rs, lane == 0 ? foff(F_H2 + par * 16 + pr) : OOB, 0, (u32)s + 1);
        } else {
#pragma unroll
            for (int r = 0; r < 2; ++r) {
                st8s(ro_, l8, ro + r * (DM * 2), (v2u){xp[r][0], xp[r][1]});
                st8s(ro_, l8 + 512u, ro + r * (DM * 2), (v2u){xp[r][2], xp[r][3]});
                st4s(ro_, l4 + 1024u, ro + r * (DM * 2), xp[r][4]);
            }
        }
        if (pr == 0) LST(320 + L * 64 + s, rt32());
        LS2(L == 0, 224 + ri);
        LMX(L == 0, 1, s, rt32());
    }
    LDUMP(L == 0, 32 + ri);
}

constexpr float SCALE = 0.08838834764831845f, LOG2E = 1.4426950408889634f;
constexpr int C_K = 0, C_V = 8192, C_QX = 16384, C_MEM = 24576, C_Q = C_MEM + 10240, C_KM = C_Q + 2304, C_VM = C_KM + 2304, C_DUMP = C_VM + 2048,
              C_ZERO = C_DUMP + 1024, C_WL = C_ZERO + 128;
constexpr int NLF = 14;
static_assert(C_WL % 16 == 0 && C_WL + 4 * NLF * 2048 <= LDS_BYTES, "head lds");
#define CH_VIS 8
#define CH_H0 104
#define CH_W0 89
#define CH_MH 3
#define CH_MW 3
#define CH_REC 28
#define CH_RTY 32u
#define CH_RISE 20u
#define CH_FALL 3u
#define CH_PRE 9
#define CH_PRW 2
#define CH_T0 190
#define CH_UP 2
#define CH_WX 6
#define WAIT_TXT(tmp, t4) "s_memrealtime vcc\n\t.p2align 5\n1:\n\ts_waitcnt lgkmcnt(0)\n\ts_sub_u32 " tmp ", " t4 ", vcc_lo\n\ts_memrealtime vcc\n\ts_cmpk_lt_u32 " tmp ", 400\n\ts_cbranch_scc1 1b\n\t" \
                          "s_add_u32 " tmp ", " tmp ", 4\n\ts_cselect_b32 " tmp ", " tmp ", 0\n2:\n\ts_sub_u32 " tmp ", " tmp ", 1\n\ts_cbranch_scc0 2b\n\t"
DEV void wait_until(u32 t) { u32 tmp; asm volatile(WAIT_TXT("%0", "%1") "s_waitcnt lgkmcnt(0)" : "=&s"(tmp) : "s"(uu(t - 4u)) : "vcc", "scc", "memory"); }
#define SMF_A(acc, a, b) asm volatile("v_smfmac_f32_16x16x64_bf16 %0, %1, %2, %3" : "+v"(acc) : "v"(a), "a"(b), "v"(idx))
#define SMF_V(acc, a, b) asm volatile("v_smfmac_f32_16x16x64_bf16 %0, %1, %2, %3" : "+v"(acc) : "v"(a), "v"(b), "v"(idx))
#define MF0(d, a, b) asm volatile("v_mfma_f32_16x16x32_bf16 %0, %1, %2, 0" : "=&v"(d) : "v"(a), "v"(b))
#define MF1(d, a, b) asm volatile("v_mfma_f32_16x16x32_bf16 %0, %1, %2, %0" : "+v"(d) : "v"(a), "v"(b))
#define MF0H(d, a, b) asm volatile("v_mfma_f32_16x16x16_bf16 %0, %1, %2, 0" : "=&v"(d) : "v"(a), "v"(b))
#define MF1H(d, a, b) asm volatile("v_mfma_f32_16x16x16_bf16 %0, %1, %2, %0" : "+v"(d) : "v"(a), "v"(b))
DEV v8u tr_frag(lptr p0, lptr p1, lptr p2, lptr p3) { return cat8(tr2(p0, p1), tr2(p2, p3)); }
constexpr int qslot(int ks) { return (ks & 1) && ks < 8 ? ks >> 1 : -1; }
constexpr int kslot(int ks) { return ks < 4 ? 4 + ks : -1; }
constexpr int vslot(int ks) { return ks >= 4 ? 4 + ks : -1; }
DEV u32 ring_map(u32 off) { return ((off >> 13) << 15) + (off & 8191u); }
DEV u32 oslot(int s) { return (u32)(s & 3) << 18 | (u32)(s & 2) << 13; }
#define ZRD(x, i) x = __builtin_bit_cast(f32x4, lds_r16(ZP + 16 * (i)))
#define VMAX(d, a, b) asm volatile("v_max_f32 %0, %1, %2" : "=v"(d) : "v"(a), "v"(b))
#define VMAX3(d, a, b, c_) asm volatile("v_max3_f32 %0, %1, %2, %3" : "=v"(d) : "v"(a), "v"(b), "v"(c_))
#define VEXP(d, a) asm volatile("v_exp_f32 %0, %1" : "=v"(d) : "v"(a))
#define VMULC(d, a) asm volatile("v_mul_f32 %0, 0x3e0293ee, %1" : "=v"(d) : "v"(a))
#define CADD(d, a, b) do { d = (a) + (b); PIN(d); } while (0)
#define CMULC(d, a) do { d = (a) * CEXP; PIN(d); } while (0)
#define CCVT(d, a, b) do { d = pk(a, b); PIN(d); } while (0)
#define LGK(n) __builtin_amdgcn_s_waitcnt(0xC07F | (n) << 8)
#define SEL(d, a, b) asm volatile("v_cndmask_b32 %0, %1, %2, %3" : "=v"(d) : "v"(a), "v"(b), "s"(himask))
#define QPERM(d, a, q) asm volatile("v_mov_b32_dpp %0, %1 quad_perm:" q " row_mask:0xf bank_mask:0xf" : "=v"(d) : "v"(a))

#define AL_H 3
#define HD_SMP 56
DEV void role_head(const Params& p, lptr lds, int L, int h, v2u ep) {
    const int tid = threadIdx.x, lane = tid & 63, w = uni(tid >> 6), g = lane >> 4, r = lane & 15;
    const rsrc_t rs = mk_rsrc(p.ws, W_END);
    v8u wq0[10], wq1[10], wk0[10], wk1[10], wv0[10], wv1[10];
    lptr WL = lds + C_WL + w * (NLF * 2048) + lane * 16;
    {
        constexpr int GS = 1056, CS = 48 * GS;
        const v4u sq = mk_srd(p.Wq, DM * NQ * 2), sk = mk_srd(p.Wk, DM * NQ * 2), sv = mk_srd(p.Wv, DM * NQ * 2);
        const u32 vo = (u32)(g * NQ + h * DH + 8 * (r ^ ((g & 1) << 3))) * 2u;
        auto fill = [&](int ch) {
#pragma unroll 1
            for (int i = 0; i < 4; ++i) {
                const u32 dst = uu((ch % 3) * CS + (4 * i + w) * GS), so = uu((ch * 64 + 16 * i + 4 * w) * (NQ * 2));
                DMAIN(sq, dst, vo, so); DMAIN(sk, dst + 16 * GS, vo, so); DMAIN(sv, dst + 32 * GS, vo, so);
            }
        };
        fill(0); fill(1); fill(2);
        const int q4 = r >> 2;
        lptr tb = lds + (4 * (g >> 1) + 2 * (q4 >> 1) + (g & 1)) * GS + (q4 & 1) * 256 + ((w * 64 + (r & 3) * 8) ^ ((q4 & 1) << 7));
        SFOR(ch, 10)
            VMW(ch < 8 ? 24 : 108 - 12 * ch);
            BAR();
            auto frag = [&](int m, int t) { lptr b = tb + (ch % 3) * CS + m * 16 * GS + t * 32; return tr_frag(b, b + 512, b + 8 * GS, b + 8 * GS + 512); };
            wq0[ch] = frag(0, 0); wq1[ch] = frag(0, 1); wk0[ch] = frag(1, 0); wk1[ch] = frag(1, 1); wv0[ch] = frag(2, 0); wv1[ch] = frag(2, 1);
            asm volatile("" : "+a"(wq0[ch]), "+a"(wk0[ch]), "+v"(wv0[ch]), "+v"(wv1[ch]));
            if constexpr (qslot(ch) < 0) asm volatile("" : "+a"(wq1[ch])); else asm volatile("" : "+v"(wq1[ch]));
            if constexpr (kslot(ch) < 0) asm volatile("" : "+a"(wk1[ch])); else asm volatile("" : "+v"(wk1[ch]));
            BAR();
            if constexpr (ch < 7) fill(ch + 3);
        SEND
#pragma unroll
        for (int ch = 0; ch < 10; ++ch) {
            if (qslot(ch) >= 0) { lds_w16(WL + qslot(ch) * 2048, wq1[ch].lo); lds_w16(WL + qslot(ch) * 2048 + 1024, wq1[ch].hi); }
            if (kslot(ch) >= 0) { lds_w16(WL + kslot(ch) * 2048, wk1[ch].lo); lds_w16(WL + kslot(ch) * 2048 + 1024, wk1[ch].hi); }
            if (vslot(ch) >= 0) { lds_w16(WL + vslot(ch) * 2048, wv1[ch].lo); lds_w16(WL + vslot(ch) * 2048 + 1024, wv1[ch].hi); }
        }
        CFENCE();
    }
    const u32 idx = (r & 2) ? 0xEEEEu : 0x4444u;
    unsigned long long himask = 0xCCCCCCCCCCCCCCCCull;
    PINS(himask);
    bool dead = false;
    if (h == 3) LST(266 + L, rt32());
    if (w == 0) LST(1240 + L * 32 + h, rt32());
    if (L == 1) {
        KvIn in;
        kvt_load(p, KT0 + 4 * h + w, in, [] {}); kvt_make(rs, lds + w * 9216, KT0 + 4 * h + w, in);
        DRAIN(); BAR();
        pre_post(rs, 64 + h, 3u, ep);
    }
    const u32 pol0 = pol_wait(rs, ep);
    if (h == 3 && L == 0) LST(277, rt32());
    const u32 l16 = (u32)lane * 16u;
    const u32 mring = (u32)L * MB + (pol0 ^ 16384u), hint = mring + 4 * 65536u;
    const u32 fkh = lane < 3 ? foff(F_QKV + L * 96 + lane * 32 + h) : lane < 7 ? hint + (u32)(lane - 3) * 4u : OOB;
    u32 tm = 0;
    MK_SRD();
    u32 ksrc = uu(W_KQ + (u32)(L * (NSEG - 1) * NH + h) * 16384u + w * 2048), vsrc = ksrc + (W_VO - W_KQ);
    u32 dk = uu(C_K + w * 2048), dv = dk + (C_V - C_K), dq = dk + (C_QX - C_K), dm = uu(C_MEM + w * 2048), dm2 = uu(w < 2 ? C_MEM + 8192 + w * 1024 : C_DUMP);
    asm volatile("" : "+s"(dk), "+s"(dv), "+s"(dq), "+s"(dm), "+s"(dm2));
    auto keys_wait = [&](u32 need) {
#pragma unroll 1
        for (int n = 0; !dead; ++n) {
            const bool beat = need + 2 * L > 2u;
            if (beat) wait_until(tm);
            const u32 hv = ld4(rs, lane == 7 ? foff(F_ERR) : fkh);
            if (beat) { tm = rdl(hv, 5); wait_until(rdl(hv, 3) - 30u); }
            if (((u32)__builtin_amdgcn_ballot_w64(ld4(rs, fkh) < need) & 7u) == 0u) break;
            dead = n > SPIN_CAP || rdl(hv, 7) != 0u;
            st4s(rs, dead ? foff(F_ERR) : OOB, 0, 1u);
        }
        CFENCE();
        DMA32(dk, l16, ksrc); DMA32(dv, l16, vsrc); DMA32(dq, l16, ksrc ^ 8192u);
    };
    if (L == 0) { st4s(rs, tid == 0 ? hint + 4096u + (u32)h * 128u : OOB, 0, 0u); keys_wait(1u); }
    lptr qw = lds + C_Q + (2 * g) * 288 + (32 * w + 2 * r) * 2;
    lptr kw = qw + (C_KM - C_Q); PIN(kw);
    lptr qr = lds + C_Q + (2 * (r >> 2) + (r & 1)) * 288 + g * 16;
    lptr LB = lds + l16, VX = lds + C_V + w * 2048 + l16;
    PIN(VX);
    if (tid >= 64 && tid < 70) { const u32 z = tid == 68 ? 0x3F803F80u : tid == 69 ? PSENT : 0u; lds_w16(lds + C_ZERO + (tid - 64) * 16, (v4u){z, z, z, z}); }
    u32 zaddr = 0; PIN(zaddr);
    lptr ZP = lds + zaddr + C_ZERO;
    const float CEXP = SCALE * LOG2E;
#define LF_RD(slot, hf) lf[slot][hf] = lds_r16(WL + (slot) * 2048 + (hf) * 1024)
    BAR();
    const u32 ofl = tid == 0 ? foff(F_O + L * 32 + h) : OOB;
    u32 oso = (((g | L) & 1) ? OOB : 0u) + (u32)L * MB + (pol0 ^ oslot(-L)) + ring_map((u32)((r >> 3) * 32768 + h * 1024 + w * 256 + (g >> 1) * 128 + (r & 7) * 16));
    unsigned long long vis[8];
#pragma unroll
    for (int i = 0; i < 8; ++i) vis[i] = __builtin_amdgcn_ballot_w64(16 * (i >> 2) + 4 * g + (i & 3) <= 16 * (w & 1) + r);
    u32 missed = 0;
    for (int s = -L; s < NSEG;) {
        u32 sv = (u32)max(s, 0), ms = s > 0 ? mring + (sv & 3u) * 65536u : W_MEM0, ms2 = ms + 32768u + (w & 1) * 1024;
        ms += w * 2048;
        asm volatile("" : "+s"(ms), "+s"(ms2)); PIN(sv);
        f32x4 aq0, aq1, ak0, ak1;
        v4u lf[NLF][2];
        ZRD(aq0, 0); ZRD(aq1, 1); ZRD(ak0, 2); ZRD(ak1, 3);
#pragma unroll
        for (int i = 0; i < 4; ++i) { LF_RD(i, 0); LF_RD(i, 1); }
        SCHED();
        v4u A[10];
#define HT(k) do {} while (0)
        v4u kx[8], qf[4], kmf[4], vx[2], ones, P;
        float fa, fb, fc, fd, inv, sx[4], t[4], tm0, tm1; u32 pa, pb, fl, e01, e23, pmm;
        f32x4 av0, av1, s0, s1, sm, oa, ob, rsum;
        v2u vm, pm;
#define LW(sl) cat8(lf[sl][0], lf[sl][1])
#define PQ(ks, tl) do { if constexpr (!(tl)) SMF_A(aq0, A[ks], wq0[ks]); else if constexpr (qslot(ks) < 0) SMF_A(aq1, A[ks], wq1[ks]); else { const v8u lw_ = LW(qslot(ks)); SMF_V(aq1, A[ks], lw_); } } while (0)
#define PK(ks, tl) do { if constexpr (!(tl)) SMF_A(ak0, A[ks], wk0[ks]); else if constexpr (kslot(ks) < 0) SMF_A(ak1, A[ks], wk1[ks]); else { const v8u lw_ = LW(kslot(ks)); SMF_V(ak1, A[ks], lw_); } } while (0)
#define PV(ks, tl) do { if constexpr (!(tl)) SMF_V(av0, A[ks], wv0[ks]); else if constexpr (vslot(ks) < 0) SMF_V(av1, A[ks], wv1[ks]); else { const v8u lw_ = LW(vslot(ks)); SMF_V(av1, A[ks], lw_); } } while (0)
#define KXR(i) kx[i] = lds_r16(LB + C_K + ((i) & 1) * 4096 + ((i) >> 1) * 1024)
#define XW(q_) do { *(LAS u32*)(q_) = pa; *(LAS u32*)((q_) + 288) = pb; } while (0)
        auto slot = [&](auto zc) __attribute__((always_inline)) {
            constexpr int z = decltype(zc)::value;
            if constexpr (z < 14) PQ(z >> 1, z & 1);
            else if constexpr (z < 17) PQ(z - 7, 0);
            else if constexpr (z < 20) PQ(z - 10, 1);
            else if constexpr (z < 34) PK((z - 20) >> 1, z & 1);
            else if constexpr (z < 37) PK(z - 27, 0);
            else if constexpr (z < 40) PK(z - 30, 1);
            else if constexpr (z < 42) { if constexpr (z == 40) MF0(s0, kx[0], qf[0]); else MF0(s1, kx[1], qf[0]); }
            else if constexpr (z < 48) { if constexpr (z & 1) MF1(s1, kx[z - 40], qf[(z - 40) >> 1]); else MF1(s0, kx[z - 40], qf[(z - 40) >> 1]); }
            else if constexpr (z < 54) PV(z - 48, 0);
            else if constexpr (z == 54) MF0(sm, kmf[0], qf[0]);
            else if constexpr (z < 58) MF1(sm, kmf[z - 54], qf[z - 54]);
            else if constexpr (z < 62) PV(z - 52, 0);
            else if constexpr (z < 64) PV(z - 62, 1);
            else if constexpr (z == 64) MF0(oa, vx[0], P);
            else if constexpr (z == 65) MF0(ob, vx[1], P);
            else if constexpr (z == 66) MF0(rsum, ones, P);
            else if constexpr (z < 75) PV(z < 70 ? z - 63 : z == 70 ? 2 : z == 71 ? 7 : z == 72 ? 3 : z - 65, 1);
            else { const v2u ones2 = ones.lo; MF1H(rsum, ones2, pm); }
            if constexpr (z < 6) A[4 + z] = lds_r16(LB + C_MEM + (4 + z) * 1024);
            if constexpr (z >= 6 && z < 10) LF_RD(4 + ((z - 6) >> 1), z & 1);
            if constexpr (z >= 14 && z < 18) LF_RD(6 + ((z - 14) >> 1), z & 1);
            if constexpr (z >= 22 && z < 28) KXR(z - 22);
            if constexpr (z == 28) XW(qw);
            if constexpr (z == 29 || z == 30) KXR(z - 23);
            if constexpr (z == 32 || z == 47) asm volatile("s_barrier" ::: "memory");
            if constexpr (z >= 33 && z < 37) qf[z - 33] = lds_r16(qr + (z - 33) * 64);
            if constexpr (z == 20) ZRD(av0, 0);
            if constexpr (z == 43) XW(kw);
            if constexpr (z == 44 || z == 45) vx[z - 44] = lds_r16(VX + (z - 44) * 1024);
            if constexpr (z >= 48 && z < 52) kmf[z - 48] = lds_r16(qr + (C_KM - C_Q) + (z - 48) * 64);
            if constexpr (z == 52) ZRD(av1, 1);
            if constexpr (z == 37) ones = lds_r16(ZP + 64);
            if constexpr (z >= 57 && z < 64) LF_RD(8 + ((z - 57) >> 1), (z - 57) & 1);
            if constexpr (z >= 65 && z < 70) LF_RD(8 + ((z - 58) >> 1), (z - 58) & 1);
            if constexpr (z == HD_SMP) LD4A(fl, fkh, 0);
            SCHED();
            if constexpr (z == 19) HT(2);
            if constexpr (z == 32) HT(3);
            if constexpr (z == 47) HT(4);
            if constexpr (z == 64) HT(5);
            if constexpr (z == 19) LGK(4);
            if constexpr (z == 21 || z == 41) LGK(1);
            if constexpr (z == 56 || z == 73) LGK(0);
            if constexpr (z == 31 || z == 46) LGK(2);
            if constexpr (z == 39) LGK(4);
            if constexpr (z >= 53 && z < 56) LGK(57 - z);
            if constexpr (z == 64) LGK(3);
            if constexpr (z == 68) LGK(5);
            if constexpr (z >= 70 && z < 73) LGK(74 - z);
            SCHED();
            if constexpr (z == 18) CADD(fa, aq0[0], aq0[2]);
            if constexpr (z == 19) CADD(fb, aq0[1], aq0[3]);
            if constexpr (z == 20) CMULC(fa, fa);
            if constexpr (z == 21) CADD(fc, aq1[0], aq1[2]);
            if constexpr (z == 22) CADD(fd, aq1[1], aq1[3]);
            if constexpr (z == 23) CMULC(fc, fc);
            if constexpr (z == 24) CMULC(fb, fb);
            if constexpr (z == 25) CMULC(fd, fd);
            if constexpr (z == 26) CCVT(pa, fa, fc);
            if constexpr (z == 27) CCVT(pb, fb, fd);
            if constexpr (z == 38) CADD(fa, ak0[0], ak0[2]);
            if constexpr (z == 39) CADD(fb, ak0[1], ak0[3]);
            if constexpr (z == 41) { CADD(fc, ak1[0], ak1[2]); CADD(fd, ak1[1], ak1[3]); }
            if constexpr (z == 42) { CCVT(pa, fa, fc); CCVT(pb, fb, fd); }
            if constexpr (z >= 49 && z < 53) SEL(sx[z - 49], s0[z - 49], s1[z - 49]);
            if constexpr (z >= 53 && z < 57) VEXP(t[z - 53], sx[z - 53]);
            if constexpr (z == 57) CCVT(e01, t[0], t[1]);
            if constexpr (z == 58) CCVT(e23, t[2], t[3]);
            if constexpr (z == 59) QPERM(P.x, e01, "[0,1,0,1]");
            if constexpr (z == 60) QPERM(P.z, e01, "[2,3,2,3]");
            if constexpr (z == 61) QPERM(P.y, e23, "[0,1,0,1]");
            if constexpr (z == 62) QPERM(P.w, e23, "[2,3,2,3]");
            if constexpr (z == 63) CADD(fa, av0[0], av0[2]);
            if constexpr (z == 64) { asm volatile("" :: "v"(sm)); VEXP(tm0, sm[0]); }
            if constexpr (z == 65) VEXP(tm1, sm[1]);
            if constexpr (z == 66) CADD(fb, av0[1], av0[3]);
            if constexpr (z == 67) CCVT(pmm, tm0, tm1);
            if constexpr (z >= 68 && z < 72) SEL(oa[z - 68], oa[z - 68], ob[z - 68]);
            if constexpr (z == 72) asm volatile("v_cndmask_b32 %0, %1, 0, %2" : "=v"(pm.x) : "v"(pmm), "s"(himask));
            if constexpr (z == 73) asm volatile("v_cndmask_b32 %0, 0, %1, %2" : "=v"(pm.y) : "v"(pmm), "s"(himask));
            if constexpr (z == 74) LGK(15);
            if constexpr (z == 75) CCVT(vm.x, fa, fb);
            SCHED();
        };
        {
            u32 tmp;
            asm volatile("s_mov_b32 m0, %2\n\t" WAIT_TXT("%0", "%1") "buffer_load_dwordx4 %4, %5, %6 offen sc1 lds\n\tbuffer_load_dwordx4 %4, %5, %6 offen offset:1024 sc1 lds\n\t"
                         "s_mov_b32 m0, %3\n\ts_nop 0\n\tbuffer_load_dwordx4 %4, %5, %7 offen sc1 lds\n\t.p2align 6\n\t.rept %8\n\ts_nop 0\n\t.endr\n\ts_waitcnt vmcnt(0) lgkmcnt(0)\n\ts_barrier"
                         : "=&s"(tmp) : "s"(uu(tm - 4u)), "s"(dm), "s"(dm2), "v"(l16), "s"(srd), "s"(ms), "s"(ms2), "i"(AL_H) : "vcc", "scc", "memory");
        }
        HT(1);
#pragma unroll
        for (int ks = 0; ks < 4; ++ks) A[ks] = lds_r16(LB + C_MEM + ks * 1024);
        SCHED();
        st4s(rs, ofl, 0, sv);
        SCHED();
        static_for<0, 76>(slot);
        asm volatile("s_nop 2\n\tv_add_f32 %1, %3, %5\n\tv_add_f32 %2, %4, %6\n\tv_cvt_pk_bf16_f32 %0, %1, %2"
                     : "=v"(vm.y), "=&v"(fc), "=&v"(fd) : "v"(av1[0]), "v"(av1[1]), "v"(av1[2]), "v"(av1[3]));
        __builtin_amdgcn_s_waitcnt(0xCF7F);
        asm volatile("s_nop 0\n\tv_mfma_f32_16x16x16_bf16 %0, %2, %3, %0\n\tv_rcp_f32 %1, %4" : "+v"(oa), "=&v"(inv) : "v"(vm), "v"(pm), "v"(rsum[0]), "v"(rsum));
        SCHED();
        if (__builtin_expect(__builtin_amdgcn_ballot_w64(u_(rsum[0]) - 0x21800000u > 0x3C000000u) != 0ull, 0)) {
            DRAIN(); SCHED();
            bool miss = false;
#pragma unroll 1
            for (int i = 0; i < 10; ++i) { const v4u a = lds_r16(LB + C_MEM + i * 1024); miss |= a.x == PSENT || a.y == PSENT || a.z == PSENT || a.w == PSENT; }
            if (missed < SPIN_CAP && !all_(!miss)) {
                *(LAS u32*)(lds + C_DUMP + (w == 0 ? 0 : 64)) = ld4(rs, foff(F_ERR));
                asm volatile("" : "+v"(fl) :: "memory");
                tm = rdl(fl, 5);
                BAR();
                missed = uu(*(LAS u32*)(lds + C_DUMP)) != 0u ? SPIN_CAP : missed + 1;
                st4s(rs, missed == SPIN_CAP ? foff(F_ERR) : OOB, 0, 1u);
                BAR();
                continue;
            }
            float mx = fmaxf(fmaxf(fmaxf(sx[0], sx[1]), fmaxf(sx[2], sx[3])), fmaxf(sm[0], sm[1]));
            mx = max32(max16(fmaxf(mx, dpp<0x4E>(mx))));
#pragma unroll
            for (int i = 0; i < 4; ++i) t[i] = __builtin_amdgcn_exp2f(sx[i] - mx);
            e01 = pk(t[0], t[1]); e23 = pk(t[2], t[3]);
            pmm = pk(__builtin_amdgcn_exp2f(sm[0] - mx), __builtin_amdgcn_exp2f(sm[1] - mx));
            asm volatile("v_cndmask_b32 %0, %2, 0, %3\n\tv_cndmask_b32 %1, 0, %2, %3" : "=&v"(pm.x), "=&v"(pm.y) : "v"(pmm), "s"(himask));
            asm volatile("s_nop 1" : "+v"(e01), "+v"(e23), "+v"(pm));
            QPERM(P.x, e01, "[0,1,0,1]"); QPERM(P.z, e01, "[2,3,2,3]"); QPERM(P.y, e23, "[0,1,0,1]"); QPERM(P.w, e23, "[2,3,2,3]");
            NOPS(1);
            MF0(rsum, ones, P); MF0(oa, vx[0], P); MF0(ob, vx[1], P);
            NOPS(7);
            { const v2u ones2 = ones.lo; MF1H(rsum, ones2, pm); }
#pragma unroll
            for (int i = 0; i < 4; ++i) SEL(oa[i], oa[i], ob[i]);
            NOPS(1);
            MF1H(oa, vm, pm);
            asm volatile("s_nop 7\n\tv_rcp_f32 %0, %1" : "=v"(inv) : "v"(rsum[0]), "v"(rsum));
        }
        {
            v4u oc; f32x2 u0 = oa.lo, u1 = oa.hi; const f32x2 i2 = {inv, inv};
            asm volatile("s_nop 0\n\tv_pk_mul_f32 %0, %0, %2\n\tv_pk_mul_f32 %1, %1, %2" : "+v"(u0), "+v"(u1) : "v"(i2));
            __builtin_amdgcn_s_waitcnt(0xCF7F);
            asm volatile("v_cvt_pk_bf16_f32 %0, %4, %5\n\tv_cvt_pk_bf16_f32 %2, %4, %5\n\tv_cvt_pk_bf16_f32 %1, %6, %7\n\tv_cvt_pk_bf16_f32 %3, %6, %7\n\t"
                         "v_permlane16_swap_b32 %0, %2\n\ts_nop 0\n\tv_permlane16_swap_b32 %1, %3"
                         : "=&v"(oc.x), "=&v"(oc.y), "=&v"(oc.z), "=&v"(oc.w) : "v"(u0.x), "v"(u0.y), "v"(u1.x), "v"(u1.y));
            st16(rs, oso, oc);
        }
        SCHED();
        unsigned long long clk;
        asm volatile("s_memrealtime %0" : "=s"(clk) :: "memory");
        HT(6);
        v4u tq[4], tk[8], tv[2];
        f32x4 c0, c1, cm;
        u32 oo, kvo; bool kvok;
        u32 ln = l16; PIN(ln);
        lptr QX = lds + C_QX + (w & 1) * 4096 + ln, VO = lds + C_V + (w ^ 1) * 2048 + ln, VMX = lds + C_VM + w * 512 + (ln >> 1);
#pragma unroll
        for (int i = 0; i < 2; ++i) { tq[i] = lds_r16(QX + i * 1024); kmf[i] = lds_r16(qr + (C_KM - C_Q) + i * 64); tk[2 * i] = lds_r16(LB + C_K + i * 1024); tk[2 * i + 1] = lds_r16(LB + C_K + 4096 + i * 1024); }
        *(LAS v2u*)VMX = vm;
        SCHED();
        SFOR(i, 12)
            if constexpr (i == 0) MF0(cm, kmf[0], tq[0]); else if constexpr (i == 1) MF0(c0, tk[0], tq[0]); else if constexpr (i == 2) MF0(c1, tk[1], tq[0]);
            else if constexpr (i % 3 == 0) MF1(cm, kmf[i / 3], tq[i / 3]); else if constexpr (i % 3 == 1) MF1(c0, tk[2 * (i / 3)], tq[i / 3]); else MF1(c1, tk[2 * (i / 3) + 1], tq[i / 3]);
            if constexpr (i < 2) kmf[2 + i] = lds_r16(qr + (C_KM - C_Q) + (2 + i) * 64);
            if constexpr (i == 0) { tq[2] = lds_r16(QX + 2048); tk[4] = lds_r16(LB + C_K + 2048); }
            if constexpr (i == 1) { tk[5] = lds_r16(LB + C_K + 6144); tq[3] = lds_r16(QX + 3072); }
            if constexpr (i == 2) { tk[6] = lds_r16(LB + C_K + 3072); tk[7] = lds_r16(LB + C_K + 7168); }
            if constexpr (i == 3) { tv[0] = lds_r16(VO); tv[1] = lds_r16(VO + 1024); }
            if constexpr (i == 4) { oo = ln + (ln & 0x300u) + ((vsrc ^ 8192u) + (u32)((w & 1) * 256) + ((u32)s & OOB)); PIN(oo); }
            if constexpr (i == 5) { ksrc += NH * 16384; vsrc += NH * 16384; PINS(ksrc); PINS(vsrc); }
            if constexpr (i == 9) { asm volatile("s_waitcnt vmcnt(1)" : "+v"(fl) :: "memory"); kvok = ((u32)__builtin_amdgcn_ballot_w64(fl < (u32)s + 2) & 7u) == 0u; }
            if constexpr (i == 10) { kvo = l16 | (kvok ? 0u : OOB); PIN(kvo); }
            SCHED();
        SEND
        asm volatile("s_waitcnt lgkmcnt(0)\n\ts_barrier" : "+s"(clk) :: "memory");
        const v2u tvm = *(LAS v2u*)(VMX + (512 - (w & 1) * 1024));
        const v4u pv = lds_r16(ZP + 80);
        DMA32(dk, kvo, ksrc);
        SCHED(); asm volatile("" :: "v"(cm));
        float u[10], m, m2;
        {
            float NEG = -1000000000.0f / SCALE; PIN(NEG);
#define MSK(d, a, i) asm volatile("v_cndmask_b32 %0, %1, %2, %3" : "=v"(d) : "v"(NEG), "v"(a), "s"(vis[i]))
#pragma unroll
            for (int i = 0; i < 4; ++i) MSK(u[i], c0[i], i);
#pragma unroll
            for (int i = 0; i < 4; ++i) MSK(u[4 + i], c1[i], 4 + i);
            asm volatile("v_max3_f32 %0, %2, %3, %10\n\tv_max3_f32 %1, %4, %5, %11\n\tv_max3_f32 %0, %0, %6, %7\n\tv_max3_f32 %1, %1, %8, %9\n\t"
                         "v_max_f32 %0, %0, %1\n\tv_mov_b32 %1, %0\n\ts_nop 1\n\tv_permlane16_swap_b32 %0, %1\n\tv_max_f32 %0, %0, %1\n\tv_mov_b32 %1, %0\n\ts_nop 1\n\t"
                         "v_permlane32_swap_b32 %0, %1\n\tv_max_f32 %0, %0, %1\n\tv_mul_f32 %0, 0xbe0293ee, %0"
                         : "=&v"(m), "=&v"(m2) : "v"(u[0]), "v"(u[1]), "v"(u[2]), "v"(u[3]), "v"(u[4]), "v"(u[5]), "v"(u[6]), "v"(u[7]), "v"(cm[0]), "v"(cm[1]));
        }
        DMA32(dv, kvo, vsrc);
        {
            v4u px; v2u p0, p1;
#define EX2(d, a, b) asm volatile("v_fmamk_f32 %0, %2, 0x3e0293ee, %4\n\tv_fmamk_f32 %1, %3, 0x3e0293ee, %4\n\tv_exp_f32 %0, %0\n\tv_exp_f32 %1, %1" : "=&v"(d.x), "=&v"(d.y) : "v"(a), "v"(b), "v"(m))
            f32x2 ex[5];
            EX2(ex[0], u[0], u[1]); EX2(ex[1], u[2], u[3]); EX2(ex[2], u[4], u[5]); EX2(ex[3], u[6], u[7]); EX2(ex[4], cm[0], cm[1]);
            asm volatile("v_cvt_pk_bf16_f32 %0, %8, %9\n\tv_cvt_pk_bf16_f32 %1, %10, %11\n\tv_cvt_pk_bf16_f32 %2, %12, %13\n\tv_cvt_pk_bf16_f32 %3, %14, %15\n\t"
                         "v_mov_b32 %5, 0\n\tv_mov_b32 %6, 0\n\tv_cvt_pk_bf16_f32 %4, %16, %17\n\tv_cvt_pk_bf16_f32 %7, %16, %17"
                         : "=&v"(px.x), "=&v"(px.y), "=&v"(px.z), "=&v"(px.w), "=&v"(p0.x), "=&v"(p0.y), "=&v"(p1.x), "=&v"(p1.y)
                         : "v"(ex[0].x), "v"(ex[0].y), "v"(ex[1].x), "v"(ex[1].y), "v"(ex[2].x), "v"(ex[2].y), "v"(ex[3].x), "v"(ex[3].y), "v"(ex[4].x), "v"(ex[4].y));
            f32x4 o[4], sum;
            const v2u ones2 = ones.lo;
            const u32 now = (u32)clk;
            u32 oz;
            SCHED();
            MF0(sum, ones, px); { const u32 q = ksrc ^ 8192u; DMA32(dq, kvo, q); } SCHED();
            MF0(o[0], vx[0], px);
            MF0(o[1], vx[1], px);
            MF0(o[2], tv[0], px); oz = oso ^ (oslot(s) ^ oslot(s + 3)); PIN(oz); SCHED();
            MF0(o[3], tv[1], px); oso ^= oslot(s) ^ oslot(s + 1); PIN(oso); SCHED();
            MF1H(sum, ones2, p0);
            MF1H(o[0], vm, p0);
            MF1H(o[1], vm, p1); st16(rs, oz, pv); SCHED();
            MF1H(o[2], tvm, p0);
            MF1H(o[3], tvm, p1);
            asm volatile("s_nop 2\n\tv_rcp_f32 %0, %1\n\ts_nop 0" : "=v"(inv) : "v"(sum[0]), "v"(sum));
            asm volatile("" : "+v"(o[0]), "+v"(o[1]));
            st16(rs, oo, (v4u){pk2(o[0].lo * inv), pk2(o[0].hi * inv), pk2(o[1].lo * inv), pk2(o[1].hi * inv)});
            asm volatile("" : "+v"(o[2]), "+v"(o[3]));
            st16(rs, oo ^ 2048u, (v4u){pk2(o[2].lo * inv), pk2(o[2].hi * inv), pk2(o[3].lo * inv), pk2(o[3].hi * inv)});
            SCHED();
            LS2(L == 0 && w == 0, h);
            st4s(rs, ((h & 15) == 0 && tid == 192) ? hint + 128u + (u32)h * 8u : OOB, 0, (u32)s << 16 | min(missed, 1u) << 14 | min(now - tm, 0x3FFFu));
            tm = rdl(fl, 5);
            missed = missed == SPIN_CAP ? missed : 0u;
            if (__builtin_expect(s < 1 - L, 0)) {
                if (s < 0) oso -= (ln & 256u) ? 0u : OOB;
#pragma unroll 1
                for (int n = 0; n < (s < 0 ? 0 : 200); ++n) {
                    CFENCE();
                    tm = rdl(ld4(rs, fkh), 6);
                    if (tm != PSENT) break;
                }
            }
        }
        if (__builtin_expect(!kvok, 0) && s + 1 < NSEG) {
            st4s(rs, ln == 0 ? hint + 4096u + (u32)h * 128u : OOB, 0, (u32)s + 1u);
            keys_wait((u32)s + 2u);
        }
        ++s;
    }
    DRAIN(); BAR();
    st4s(rs, ofl, 0, NSEG);
    LDUMP(w == 0, 160 + L * 32 + h);
}

#define WO_NL 3
#define WO_ROT 0
#define WO_AH 1
DEV void role_wo(const Params& p, lptr lds, int j, v2u ep) {
    const int tid = threadIdx.x, lane = tid & 63, w = uni(tid >> 6), g = lane >> 4, r = lane & 15;
    const int rg = j & 1, cg = j >> 1, rot = WO_ROT;
    const rsrc_t rs = mk_rsrc(p.ws, W_END);
    v8u wo[8][5];
    {
        const v4u so_ = mk_srd(p.Wo, DM * NQ * 2);
        u32 vo[5];
#pragma unroll
        for (int i = 0; i < 5; ++i) { const int n = 64 * i + lane; vo[i] = (u32)((n / 5) * DM + cg * 40 + (n % 5) * 8) * 2u; }
        v4u junk = {};
        const u32 av = (u32)(j * 5120 + lane * 16);
        auto ahead = [&](int b) {
#pragma unroll
            for (int i = 0; i < 5; ++i) AHEAD(so_, av, uu((8 * w + b) * (DH * DM * 2) + i * 1024));
        };
        auto fill = [&](int b) {
            if (b + WO_AH < 8) ahead(b + WO_AH);
            const u32 dst = uu(w * 40960 + (b & 3) * 10240), src = uu((8 * w + ((b + rot) & 7)) * (DH * DM * 2));
#pragma unroll
            for (int i = 0; i < 10; ++i) DMAIN(so_, dst + i * 1024, vo[i % 5], src + (i / 5) * (64 * DM * 2));
        };
#pragma unroll 1
        for (int b = 0; b < WO_AH; ++b) ahead(b);
#pragma unroll 1
        for (int b = 0; b < 4; ++b) fill(b);
        const int q4 = r >> 2;
        lptr tb = lds + w * 40960 + (32 * (g >> 1) + 16 * (q4 >> 1) + 8 * ((r >> 1) & 1) + 4 * (g & 1) + (q4 & 1)) * 80 + (r & 1) * 8;
        SFOR(b, 8)
            VMW((b < 5 ? 30 : 70 - 10 * b) + (b + 1 + WO_AH < 8 ? 5 : 0) + (b + 2 + WO_AH < 8 ? 5 : 0) + (b + 3 + WO_AH < 8 ? 5 : 0));
            if constexpr (b + WO_AH == 7) PIN(junk);
#pragma unroll
            for (int t = 0; t < 5; ++t) {
                lptr q = tb + (b & 3) * 10240 + t * 16;
                wo[b][t] = tr_frag(q, q + 160, q + 64 * 80, q + 64 * 80 + 160);
                if (b * 5 + t < 32) asm volatile("" : "+a"(wo[b][t])); else asm volatile("" : "+v"(wo[b][t]));
            }
            CFENCE();
            if constexpr (b < 4) fill(b + 4);
        SEND
    }
    if (j == 0) LST(265, rt32());
    LST(1100 + j * 4 + w, rt32());
    const u32 idx = (r & 2) ? 0xEEEEu : 0x4444u;
    const u32 pol0 = pol_wait(rs, ep);
    BAR();
    const u32 l16 = (u32)lane * 16u;
    u32 oo[8];
#pragma unroll
    for (int i = 0; i < 8; ++i) oo[i] = uu(ring_map((u32)(rg * 32768 + (8 * w + ((i + rot) & 7)) * 1024)));
    const u32 hint = (pol0 ^ 16384u) + 4 * 65536u;
    u32 hvo[2];
#pragma unroll
    for (int l = 0; l < 2; ++l) hvo[l] = lane < 2 ? hint + (u32)(l ^ 1) * MB + (u32)lane * 4u : lane == 2 ? hint + (u32)l * MB + 128u : lane == 3 ? foff(F_ERR) : lane == 4 ? hint + (u32)l * MB + 256u :
                 (lane >= 32 && j == 0 && w == 3) ? hint + (u32)l * MB + (u32)lane * 128u : OOB;
    lptr xw = r < 8 ? lds + w * 640 + (2 * (g & 1) + (g >> 1)) * 32 + r * 4 : lds + 8192 + lane * 4;
    const int G = ((5 * cg) >> 1) + w, tt = 2 * G + ((lane >> 3) & 1) - 5 * cg;
    const bool pub = w < 3 && lane < 32 && tt >= 0 && tt < 5;
    lptr xr = lds + (pub ? tt * 128 + (2 * (lane >> 4) + ((lane >> 2) & 1)) * 32 + (lane & 3) * 8 : 0);
    const u32 mpo = pub ? (pol0 ^ 16384u) + ring_map((u32)(G * 256 + rg * 128 + lane * 4)) : OOB;
    constexpr u32 PER0 = CH_W0 + CH_MW + CH_H0 + CH_MH + 2 * CH_VIS, DW0 = CH_W0 + CH_VIS + CH_MW + CH_REC;
    const bool lead = j == 0 && w == 3;
    const u32 hso = (lead && lane == 0) ? hint : OOB;
    MK_SRD();
    u32 t = 0, tn = 0;
    int sn[2] = {0, 0};
    u32 hav = 8 * CH_H0, wav = 8 * CH_W0, mh = 8 * CH_MH, mw = 8 * CH_MW, ok[2] = {0u, 0u}, dw[2] = {DW0, DW0};
    u32 vo = l16 + pol0, early = PSENT, tp = 0, fin, left, cnt = 0;
    unsigned long long clk = 0;
    {
        if (w == 0) LST(1310 + j, rt32());
        if (lead) {
#pragma unroll 1
            for (int n = 0; n < SPIN_CAP; ++n) {
                CFENCE();
                const u32 a = ld4(rs, foff(F_QKV + lane)), b = ld4(rs, lane < 32 ? foff(F_QKV + 64 + lane) : hvo[0]);
                if (all_(a != 0u && b != (lane < 32 ? 0u : PSENT))) break;
                __builtin_amdgcn_s_sleep(1);
            }
        }
        if (lead) {
            t = rt32() + CH_T0; tn = t + PER0 / 2 + CH_REC;
            st16(rs, lane < 2 ? hint + (u32)lane * MB : OOB, (v4u){lane == 0 ? t : tn, PER0, (lane == 0 ? t : tn) + DW0, t + DW0});
        } else {
#pragma unroll 1
            for (int n = 0; n < SPIN_CAP; ++n) {
                CFENCE();
                const u32 hv = ld4(rs, hvo[1]);
                t = rdl(hv, 0);
                if (rdl(hv, 1) < 400u) break;
                __builtin_amdgcn_s_sleep(1);
            }
        }
        clk = t - 100u;
    }
    const u32 tm0 = t + DW0;
    u32 zero = 0, hv = 0xFFFFu, pst = 1, pmp = OOB;
    const u32 psent = PSENT;
    lptr sw = lds + 2560 + w * 4;
    asm volatile("" : "+v"(zero), "+v"(sw));
    auto turn = [&](auto lc) __attribute__((always_inline)) {
        constexpr int L = decltype(lc)::value, M = L ^ 1;
        u32 done, rep, rd, lost, dh, dn, nt, za, pdur, mp; v4u hw;
        const int sc = sn[L];
        auto books = [&](auto ic) __attribute__((always_inline)) {
            constexpr int i = decltype(ic)::value;
            if constexpr (i == 0) { rep = max(rdl(hv, 2), rdl(hv, 4)); PINS(rep); }
            if constexpr (i == 1) { lost = (u32)(__builtin_amdgcn_ballot_w64(hv == (u32)sn[M]) >> 32); PINS(lost); }
            if constexpr (i == 2) { lost |= (rep >> 16) ^ (u32)sn[M]; PINS(lost); }
            if constexpr (i == 3) { lost |= ok[M] ^ 2u; rd = rep & 0xFFFFu; PINS(lost); PINS(rd); }
            if constexpr (i == 4) { lost = lost == 0u ? CH_UP * 8u : 0u; PINS(lost); }
            if constexpr (i == 5) { pdur = ((u32)clk - tp) * 8u; PINS(pdur); pdur = min(pdur, wav + CH_RISE); PINS(pdur); }
            if constexpr (i == 6) { pdur = (done & ok[M]) ? pdur : wav; PINS(pdur); }
            if constexpr (i == 7) { wav = max(pdur, wav - CH_FALL); PINS(wav); }
            if constexpr (i == 8) { nt = min(rd * 8u, hav + CH_RISE); PINS(nt); }
            if constexpr (i == 9) { nt = rd < 0x3FFFu ? nt : hav; PINS(nt); }
            if constexpr (i == 10) { hav = max(nt, hav - CH_FALL); PINS(hav); }
            if constexpr (i == 11) { mw = max(mw - 1u, CH_MW * 8u); PINS(mw); }
            if constexpr (i == 12) { mh = max(mh - 1u, CH_MH * 8u); PINS(mh); }
            if constexpr (i == 13) { rd = rd >> 14 == 1u ? lost : 0u; PINS(rd); }
            if constexpr (i == 14) { mw += rd; mh += lost - rd; PINS(mw); PINS(mh); }
            if constexpr (i == 15) { ok[M] = done ? 1u : (ok[M] & 1u) << 1; PINS(ok[M]); }
            if constexpr (i == 16) { dh = (hav + mh) / 8u + (sn[1] < 3 ? CH_VIS + CH_PRE : CH_VIS); PINS(dh); }
            if constexpr (i == 17) { dn = (wav + mw) / 8u + (sn[1] < 3 ? CH_VIS + CH_PRW : CH_VIS); PINS(dn); }
            if constexpr (i == 18) { nt = dw[L] + dh + (u32)max((int)((u32)clk + CH_WX - t), 0); PINS(nt); }
            if constexpr (i == 19) { dh += dn; dw[L] = dn; sn[M] += (int)done; PINS(dh); PINS(sn[M]); }
            if constexpr (i == 20) { rd = tn - t + (dh >> 1); PINS(rd); }
            if constexpr (i == 21) { lost = (u32)sc - 1u < NSEG - 2 ? CH_RTY : 0u; PINS(lost); nt += ok[L] != 1u ? lost : 0u; PINS(nt); }
            if constexpr (i == 22) { nt = t + (u32)max((int)nt, (int)rd); PINS(nt); }
            if constexpr (i == 23) { hw.x = nt; hw.y = dh; PIN(hw.x); PIN(hw.y); }
            if constexpr (i == 24) { hw.z = nt + dn; hw.w = tm0; PIN(hw); }
            if constexpr (i == 25) st16(rs, hso + L * MB, hw);
        };
        {
#define WT(k) do {} while (0)
            v4u d[8];
            {
                u32 tmp;
                asm volatile(WAIT_TXT("%1", "%2") "buffer_load_dwordx4 %0, %3, %4, %5 offen sc1\n\ts_waitcnt lgkmcnt(0)"
                             : "=&v"(d[0]), "=&s"(tmp) : "s"(uu(t - 4u)), "v"(vo), "s"(srd), "s"(oo[0]) : "vcc", "scc", "memory");
                PINS(clk);
                WT(0);
            }
            SFOR(i, 7)
                asm volatile("buffer_load_dwordx4 %0, %1, %2, %3 offen sc1" : "=v"(d[i + 1]) : "v"(vo), "s"(srd), "s"(oo[i + 1]) : "memory");
                SCHED();
                if constexpr (i == 0) { done = uu(pst) ^ 1u; PINS(done); }
                static_for<3 * i, 3 * i + 3>(books);
                SCHED();
            SEND
            LD4A(hv, hvo[L], 0);
            SCHED();
            WT(1);
            static_for<21, 26>(books);
            za = done ? pmp : OOB;
            st4s(rs, za, 0, psent);
            *(LAS u32*)(sw + L * 4096) = zero;
            f32x4 acc[5] = {};
            mp = mpo + (L * MB + (u32)((sc + 1) & 3) * 65536u); pmp = mpo + (L * MB + (u32)(sc & 3) * 65536u);
            asm volatile("" : "+v"(acc[0]), "+v"(acc[1]), "+v"(acc[2]), "+v"(acc[3]), "+v"(acc[4]), "+v"(mp), "+v"(pmp));
            SCHED();
            float f0[5], f1[5]; unsigned long long bad;
#define WSW(o_) asm volatile("v_permlane32_swap_b32 %0, %1" : "+v"(f0[o_]), "+v"(f1[o_]))
#define WAD(o_) asm volatile("v_add_f32_dpp %0, %1, %0 row_ror:8 row_mask:0xf bank_mask:0xf" : "+v"(f0[o_]) : "v"(f1[o_]))
#define WFO(o_) do { VADD(f0[o_], acc[o_][0], acc[o_][2]); VADD(f1[o_], acc[o_][1], acc[o_][3]); } while (0)
#define WWR(o_) *(LAS float*)(xw + L * 4096 + (o_) * 128) = f0[o_]
            u32 th, per, so, erl = early;
            auto next = [&](auto ic_) __attribute__((always_inline)) {
                constexpr int i = decltype(ic_)::value;
                if constexpr (i == 0) { so = (u32)(sn[M] & 3) * 0x42000u; PINS(so); }
                if constexpr (i == 1) { so = (so & 0xC4000u) ^ (pol0 + M * MB); PINS(so); }
                if constexpr (i == 2) { so = sn[M] + 1 < NSEG ? so : OOB; PINS(so); }
                if constexpr (i == 3) { early = sn[M] + 1 < NSEG ? PSENT : 0u; PIN(early); }
                if constexpr (i == 4) { vo = l16 + so; PIN(vo); }
                if constexpr (i == 5 && L) { fin = (u32)(sn[0] + sn[1] + 2); PINS(fin); }
                if constexpr (i == 6 && L) { fin = (fin | uu(left)) >> 7; PIN(fin); }
                if constexpr (i == 6 && !L) { cnt += 1u; PIN(cnt); left = cnt >> 9; PIN(left); }
                if constexpr (i == 7) { PIN(hv); th = rdl(hv, 0); PINS(th); }
                if constexpr (i == 8) { per = rdl(hv, 1); PINS(per); so = th - t; PINS(so); }
                if constexpr (i == 9) { per = (int)so < 20 ? per : 0u; PINS(per); }
                if constexpr (i == 10) { th += per; PINS(th); if constexpr (L) { per = rdl(hv, 3); PINS(per); } }
                if constexpr (i == 11) { tp = t; t = lead ? tn : th; PINS(t); }
                if constexpr (i == 12) { if constexpr (L) { fin |= per; PIN(fin); } tn = nt; }
            };
            auto slot = [&](auto ic_) __attribute__((always_inline)) {
                constexpr int i = decltype(ic_)::value;
                constexpr int NL = WO_NL, I0 = 40 - 5 * NL, pc = i < I0 ? i / 5 : 8 - NL + (i - I0) % NL, oc = i < I0 ? i % 5 : (i - I0) / NL;
                if constexpr (oc == 0) asm volatile("s_waitcnt vmcnt(%1)" : "+v"(d[pc]) : "i"(10 - pc));
                if constexpr (pc * 5 + oc < 32) SMF_A(acc[oc], d[pc], wo[pc][oc]); else SMF_V(acc[oc], d[pc], wo[pc][oc]);
                SCHED();
                if constexpr (i == 0) WT(2);
                if constexpr (i >= 5 && i < 13 && i != 9) next(IC<i - 5 - (i > 9)>{});
                if constexpr (i == 20) WT(4);
                constexpr int e = i - (I0 + NL + 1), o = e < 0 ? 0 : e / 3;
                if constexpr (e >= 0 && e % 3 == 0) { CADD(f0[o], acc[o][0], acc[o][2]); if constexpr (o > 0) WAD(o - 1); }
                if constexpr (e >= 0 && e % 3 == 1) { if constexpr (o > 0) { WWR(o - 1); SCHED(); } CADD(f1[o], acc[o][1], acc[o][3]); }
                if constexpr (e >= 0 && e % 3 == 2) {
                    if constexpr (o == 0) asm volatile("v_cmp_u_f32 %0, %1, %2" : "=s"(bad) : "v"(f0[0]), "v"(f1[0]));
                    else LGK(15);
                    WSW(o);
                }
                SCHED();
            };
            WT(3);
            static_for<0, 2>(slot);
            if (__builtin_expect(__builtin_amdgcn_ballot_w64(d[0].x == erl) != 0ull, 0)) {
                asm volatile("s_waitcnt vmcnt(3)" :: "v"(d[1]), "v"(d[2]), "v"(d[3]), "v"(d[4]), "v"(d[5]), "v"(d[6]), "v"(d[7]) : "memory");
                SCHED();
                *(LAS u32*)(sw + L * 4096) = 1u;
                static_for<0, 7>(next);
            } else {
            static_for<2, 40>(slot);
            WT(5);
            static_assert(WO_NL == 3, "tail");
            if (__builtin_expect(bad != 0ull, 0)) {
                bool miss = false;
#pragma unroll
                for (int i = 0; i < 8; ++i) miss |= d[i].x == PSENT;
                if (all_(!miss)) {
#pragma unroll
                    for (int i = 0; i < 8; ++i) miss |= d[i].y == PSENT || d[i].z == PSENT || d[i].w == PSENT;
                }
                *(LAS u32*)(sw + L * 4096) = all_(!miss) ? 0u : 1u;
            }
            asm volatile("v_permlane32_swap_b32 %0, %1\n\ts_nop 1\n\tv_add_f32_dpp %0, %1, %0 row_ror:8 row_mask:0xf bank_mask:0xf\n\tv_add_f32 %2, %4, %6\n\tv_add_f32 %3, %5, %7"
                         : "+v"(f0[3]), "+v"(f1[3]), "=&v"(f0[4]), "=&v"(f1[4]) : "v"(acc[4][0]), "v"(acc[4][1]), "v"(acc[4][2]), "v"(acc[4][3]));
            WWR(3); SCHED();
            asm volatile("s_nop 0\n\tv_permlane32_swap_b32 %0, %1\n\ts_nop 1\n\tv_add_f32_dpp %0, %1, %0 row_ror:8 row_mask:0xf bank_mask:0xf" : "+v"(f0[4]), "+v"(f1[4]));
            WWR(4);
            }
            WT(6);
            f32x2 a, b, c, e; u32 mq; v4u sv;
            asm volatile("s_waitcnt lgkmcnt(0)\n\ts_barrier\n\tds_read_b128 %4, %6 offset:%7+2560\n\t"
                         "ds_read_b64 %0, %5 offset:%7\n\tds_read_b64 %1, %5 offset:%7+640\n\tds_read_b64 %2, %5 offset:%7+1280\n\tds_read_b64 %3, %5 offset:%7+1920"
                         : "=&v"(a), "=&v"(b), "=&v"(c), "=&v"(e), "=&v"(sv) : "v"(xr), "v"(zero), "i"(L * 4096) : "memory");
            asm volatile("s_waitcnt lgkmcnt(4)\n\tv_or_b32 %4, %6, %7\n\tv_or3_b32 %4, %4, %8, %9\n\tv_lshl_or_b32 %5, %4, 31, %10\n\t"
                         "s_waitcnt lgkmcnt(2)\n\tv_pk_add_f32 %0, %0, %1\n\ts_waitcnt lgkmcnt(0)\n\tv_pk_add_f32 %2, %2, %3\n\ts_nop 0\n\tv_pk_add_f32 %0, %0, %2"
                         : "+v"(a), "+v"(b), "+v"(c), "+v"(e), "=&v"(pst), "=&v"(mq) : "v"(sv.x), "v"(sv.y), "v"(sv.z), "v"(sv.w), "v"(mp) : "memory");
            st4s(rs, mq, 0, pkv(a));
            SCHED();
            asm volatile("s_memrealtime %0" : "=s"(clk));
            WT(7);
            VMW(3); SCHED();
            static_for<7, 13>(next);
        }
    };
    do {
        turn(IC<0>{});
        turn(IC<1>{});
    } while (uu(fin) == 0u);
    if (sn[0] + 1 < NSEG || sn[1] + 1 < NSEG) st4s(rs, tid == 0 ? foff(F_ERR) : OOB, 0, 1u);
    if (w == 0) LST(1900 + j, rt32());
    LDUMP(w == 0, 224 + j);
}

__global__ void __launch_bounds__(256, 1) k_main(Params p) {
    lptr lds = (lptr)(u32)16 - 16;
    if (blockIdx.x == 0) LST(256, rt32());
    unsigned long long pc; int len;
    asm volatile("s_getpc_b64 %0\n.Lk_main_here:\n\ts_mov_b32 %1, .Lk_end_here-.Lk_main_here" : "=s"(pc), "=s"(len));
    const rsrc_t rs = mk_rsrc(p.ws, W_END);
    const v2u e0 = ld8s(rs, threadIdx.x == 0 ? foff(F_EPOCH) : OOB, 0);
    const v4u code = __builtin_amdgcn_raw_buffer_load_b128(mk_rsrc((const void*)(pc & ~15ull), (u32)max(len, 0)), (((blockIdx.x >> 3) & 31u) * 256u + threadIdx.x) * 16u, 0, 0);
    CFENCE();
    auto conv_id = [](int role) {
        const int x = role >> 5, i = role & 31, no = x < 5 ? 5 : 10, og = x < 5 ? x - 3 : 2 * (x - 4) + (i >= 5), r = (x - 3) * 5 + i - no, q = r / 3;
        return x < 3 || i >= no + 5 || r >= 24 ? NCV : i < no ? ((og & 1) ? (og >> 1) * 5 + i % 5 : NCV) : r % 3 == 0 ? 20 + (q & 1) * 20 + (q >> 1) : 16 + (r % 3) * 8 + q;
    };
    int role = (int)(((u32)__builtin_amdgcn_s_getreg(20 | (3 << 11)) & 7u) * 32u + (blockIdx.x >> 3));
    Early e;
    v4u rv;
#pragma unroll 1
    for (int n = 0;; ++n) {
        conv_early(p, rs, conv_id(role), e);
        if (n) break;
        CFENCE();
        if (threadIdx.x == 0) {
            GAS unsigned long long* slots = (GAS unsigned long long*)(p.ws + W_CTRL + F_SLOT * 128);
            const unsigned long long tag = (unsigned long long)e0.y << 32 | (e0.x + 1u);
            u32 mine = (u32)role;
#pragma unroll 1
            for (int k = 0; k < 256 && __hip_atomic_exchange(slots + mine * 16, tag, __ATOMIC_RELAXED, __HIP_MEMORY_SCOPE_AGENT) == tag; ++k) mine = (mine + 1u) & 255u;
            lds_w16(lds, (v4u){mine, e0.x, e0.y, 0u});
        }
        BAR();
        rv = lds_r16(lds);
        BAR();
        if (uni((int)rv.x) == role) break;
        role = uni((int)rv.x);
    }
    const int x = role >> 5, i = role & 31, w = uni(threadIdx.x >> 6), no = x < 5 ? 5 : 10, nr = x < 7 ? 5 : 4, j = conv_id(role);
    const v2u ep = {uu(rv.y), uu(rv.z)};
    asm volatile("" :: "v"(code));
    if (x == 1) { role_wo(p, lds, i, ep); __builtin_amdgcn_endpgm(); }
    if (x < 3) { role_head(p, lds, i >> 4, x * 8 + (i & 15), ep); __builtin_amdgcn_endpgm(); }
    if (i >= no + nr) { role_qkv(p, lds, (x < 5 ? (x - 3) * 22 : 44 + (x - 5) * 17) + i - no - nr, ep); __builtin_amdgcn_endpgm(); }
    const bool cv = j >= NPRE || (j >= 20 && j < 24);
    if (j < NCV) conv_stage(p, rs, lds, j, (j & 3) * 2 + (j >= NPRE), cv ? 40 : 0, ep, e);
    if (i >= no) {
        if (!cv) role_row(p, lds, (j - 24) * 4 + w, ep);
        __builtin_amdgcn_endpgm();
    }
    const int og = x < 5 ? x - 3 : 2 * (x - 4) + (i >= 5);
    role_out(p, lds, og & 1, og >> 1, i % 5, ep);
}
__global__ void k_end() { asm volatile(".Lk_end_here:"); }

extern "C" void kernel_launch(void* const* d_in, const int* in_sizes, int n_in, void* d_out, int out_size, void* d_ws, size_t ws_size,
                              hipStream_t stream) {
    Params p;
    p.x = (const u16*)d_in[0]; p.mem0 = (const u16*)d_in[1]; p.Wq = (const u16*)d_in[2]; p.Wk = (const u16*)d_in[3];
    p.Wv = (const u16*)d_in[4]; p.Wo = (const u16*)d_in[5];
    p.bk = (const u16*)d_in[8]; p.bv = (const u16*)d_in[9]; p.shift = (const u16*)d_in[10]; p.ssmk = (const u16*)d_in[11]; p.ssmD = (const u16*)d_in[12];
    p.out = (u16*)d_out;
    p.ws = (unsigned char*)(((size_t)d_ws + 2 * MB - 1) & ~(size_t)(2 * MB - 1));
    (void)hipFuncSetAttribute((const void*)k_main, hipFuncAttributeMaxDynamicSharedMemorySize, LDS_BYTES);
    hipLaunchKernelGGL(k_main, dim3(256), dim3(256), LDS_BYTES, stream, p);
}
```

```cpp
#include <hip/hip_runtime.h>
#include <hip/hip_bf16.h>

typedef unsigned short u16;
typedef unsigned int u32;
typedef u32 v2u __attribute__((ext_vector_type(2)));
typedef u32 v4u __attribute__((ext_vector_type(4)));
typedef u32 v8u __attribute__((ext_vector_type(8)));
typedef float f32x2 __attribute__((ext_vector_type(2)));
typedef float f32x4 __attribute__((ext_vector_type(4)));
typedef float f32x16 __attribute__((ext_vector_type(16)));
typedef __bf16 bf16x2 __attribute__((ext_vector_type(2)));
typedef __bf16 bf16x8 __attribute__((ext_vector_type(8)));
typedef short s16x4 __attribute__((ext_vector_type(4)));
#define LAS __attribute__((address_space(3)))
#define GAS __attribute__((address_space(1)))
typedef LAS unsigned char* lptr;
#define DEV __device__ __forceinline__

constexpr int SEQ = 2048, DM = 640, NH = 32, DH = 128, NSEG = 64, NQ = NH * DH;

constexpr u32 MB = 1u << 20;
constexpr u32 W_MEM0 = 2 * MB;
constexpr u32 W_CTRL = 4 * MB;
constexpr u32 CTRL_BYTES = 128 * 1024;
constexpr u32 ACT = SEQ * DM * 2;
constexpr u32 W_H1 = 5 * MB, W_Y = W_H1 + ACT, W_H2 = W_Y + ACT, W_X2 = W_H2 + ACT;
constexpr u32 W_KQ = 16 * MB;
constexpr u32 W_VO = W_KQ + 64 * MB;
constexpr u32 W_P = W_VO + 64 * MB;
constexpr u32 P_ITEM = 16 * 4 * DM * 4;
constexpr u32 W_KVT = W_P + 4 * NSEG * P_ITEM;
constexpr u32 W_SP = W_KVT + ACT;
constexpr u32 W_END = W_KVT + 3 * MB;
static_assert((size_t)W_END + 2 * MB <= 256ull * MB, "workspace");
constexpr u32 F_ERR = 0, F_QKV = 16, F_O = F_QKV + 192, F_P = F_O + 64, F_H2 = F_P + 64, F_CLR = F_H2 + 32,
              F_EPOCH = F_CLR + 32, F_SLOT = F_EPOCH + 1, F_PRE = F_SLOT + 256, F_N = F_PRE + 192;
constexpr int NCV = 44, NPRE = 40;
static_assert(F_N * 128 <= CTRL_BYTES, "ctrl");
DEV u32 foff(u32 idx) { return W_CTRL + idx * 128u; }

constexpr u32 OOB = 0x80000000u;
constexpr u32 PSENT = 0x7F817F81u;
constexpr int SPIN_CAP = 40000;
constexpr int LDS_BYTES = 160 * 1024;

#define LDUMP(cond, id) do {} while (0)
#define LMX(cond, k, s_, v) do {} while (0)
#define LS3(cond, slot, idx) do {} while (0)
#define LST(idx, v) do {} while (0)
#define LS2(cond, idx) do {} while (0)
struct Params {
    const u16 *x, *mem0, *Wq, *Wk, *Wv, *Wo, *bk, *bv, *shift, *ssmk, *ssmD;
    u16* out;
    unsigned char* ws;
};

#define INAUX 2
#define INNT "nt"
typedef __amdgpu_buffer_rsrc_t rsrc_t;
DEV rsrc_t mk_rsrc(const void* p, u32 bytes) { return __builtin_amdgcn_make_buffer_rsrc((void*)p, 0, bytes, 0x00020000); }
DEV v4u ld16s(rsrc_t r, u32 off, u32 so) { return __builtin_amdgcn_raw_buffer_load_b128(r, off, so, 16); }
DEV v2u ld8s(rsrc_t r, u32 off, u32 so) { return __builtin_amdgcn_raw_buffer_load_b64(r, off, so, 16); }
DEV v4u ldin16(rsrc_t r, u32 off, u32 so) { return __builtin_amdgcn_raw_buffer_load_b128(r, off, so, INAUX); }
DEV v2u ldin8(rsrc_t r, u32 off, u32 so) { return __builtin_amdgcn_raw_buffer_load_b64(r, off, so, INAUX); }
DEV u32 ld4(rsrc_t r, u32 off) { return __builtin_amdgcn_raw_buffer_load_b32(r, off, 0, 16); }
DEV void st16(rsrc_t r, u32 off, v4u v) { __builtin_amdgcn_raw_buffer_store_b128(v, r, off, 0, 16); }
DEV void st8s(rsrc_t r, u32 off, u32 so, v2u v) { __builtin_amdgcn_raw_buffer_store_b64(v, r, off, so, 16); }
DEV void st4s(rsrc_t r, u32 off, u32 so, u32 v) { __builtin_amdgcn_raw_buffer_store_b32(v, r, off, so, 16); }
#define DRAIN() asm volatile("s_waitcnt vmcnt(0)" ::: "memory")
#define VMW(n) asm volatile("s_waitcnt vmcnt(%0)" :: "i"(n) : "memory")
#define BAR() asm volatile("s_waitcnt lgkmcnt(0)\n\ts_barrier" ::: "memory")
#define SCHED() __builtin_amdgcn_sched_barrier(0)
#define CFENCE() asm volatile("" ::: "memory")
#define NOPS(n) asm volatile("s_nop %0" :: "i"(n))
#define PIN(x) asm volatile("" : "+v"(x))
#define PINS(x) asm volatile("" : "+s"(x))

template <int V> struct IC { static constexpr int value = V; };
template <int I, int N, class F> DEV void static_for(F&& f) { if constexpr (I < N) { f(IC<I>{}); static_for<I + 1, N>(f); } }
#define SFOR(var, n) static_for<0, n>([&](auto ic_) __attribute__((always_inline)) { constexpr int var = decltype(ic_)::value;
#define SEND });

DEV u32 pk(float a, float b) { bf16x2 v = {(__bf16)a, (__bf16)b}; return __builtin_bit_cast(u32, v); }
DEV float lo_f(u32 u) { return __builtin_bit_cast(float, u << 16); }
DEV float hi_f(u32 u) { return __builtin_bit_cast(float, u & 0xffff0000u); }
DEV f32x2 up2(u32 u) { return (f32x2){lo_f(u), hi_f(u)}; }
DEV u32 pk2(f32x2 v) { return pk(v.x, v.y); }
DEV float rbf(float a) { return (float)(__bf16)a; }
DEV v4u lds_r16(lptr p) { return *(LAS v4u*)p; }
DEV void lds_w16(lptr p, v4u v) { *(LAS v4u*)p = v; }
DEV v2u lds_tr(lptr p) { return __builtin_bit_cast(v2u, __builtin_amdgcn_ds_read_tr16_b64_v4i16((LAS s16x4*)p)); }
DEV v4u tr2(lptr p0, lptr p1) { const v2u a = lds_tr(p0), b = lds_tr(p1); return (v4u){a.x, a.y, b.x, b.y}; }
DEV v8u cat8(v4u a, v4u b) { return __builtin_shufflevector(a, b, 0, 1, 2, 3, 4, 5, 6, 7); }
DEV f32x4 mfma16(v4u a, v4u b, f32x4 c) { return __builtin_amdgcn_mfma_f32_16x16x32_bf16(__builtin_bit_cast(bf16x8, a), __builtin_bit_cast(bf16x8, b), c, 0, 0, 0); }
DEV f32x16 mfma32(v4u a, v4u b, f32x16 c) { return __builtin_amdgcn_mfma_f32_32x32x16_bf16(__builtin_bit_cast(bf16x8, a), __builtin_bit_cast(bf16x8, b), c, 0, 0, 0); }

DEV float f_(u32 u) { return __builtin_bit_cast(float, u); }
DEV u32 u_(float f) { return __builtin_bit_cast(u32, f); }
template <int CTRL> DEV float dpp(float v) { return f_((u32)__builtin_amdgcn_update_dpp(0, (int)u_(v), CTRL, 0xf, 0xf, false)); }
DEV float max16(float v) { auto r = __builtin_amdgcn_permlane16_swap(u_(v), u_(v), false, false); return fmaxf(f_(r[0]), f_(r[1])); }
DEV float max32(float v) { auto r = __builtin_amdgcn_permlane32_swap(u_(v), u_(v), false, false); return fmaxf(f_(r[0]), f_(r[1])); }
DEV float add16(float v) { auto r = __builtin_amdgcn_permlane16_swap(u_(v), u_(v), false, false); return f_(r[0]) + f_(r[1]); }
DEV float add32(float v) { auto r = __builtin_amdgcn_permlane32_swap(u_(v), u_(v), false, false); return f_(r[0]) + f_(r[1]); }
DEV float wave_sum(float v) {
    v += dpp<0xB1>(v);
    v += dpp<0x4E>(v);
    v += dpp<0x124>(v);
    v += dpp<0x128>(v);
    return add32(add16(v));
}
DEV bool all_(bool c) { return __builtin_amdgcn_ballot_w64(!c) == 0ull; }
DEV int uni(int v) { return __builtin_amdgcn_readfirstlane(v); }
DEV u32 uu(u32 v) { return (u32)__builtin_amdgcn_readfirstlane((int)v); }
DEV u32 rdl(u32 v, int l) { return (u32)__builtin_amdgcn_readlane((int)v, l); }
DEV u32 rt32() { return (u32)__builtin_amdgcn_s_memrealtime(); }

#define WSL 2
DEV void wait_flags(rsrc_t rs, u32 off, u32 want, bool& dead) {
    if (dead) return;
#pragma unroll 1
    for (int n = 0;; ++n) {
        CFENCE();
        const u32 v = ld4(rs, off);
        if (all_(off >= OOB || v >= want)) break;
        __builtin_amdgcn_s_sleep(WSL);
        if ((n & 63) == 63) { if (uni((int)ld4(rs, foff(F_ERR))) != 0) { dead = true; break; } }
        if (n > SPIN_CAP) { st4s(rs, foff(F_ERR), 0, 1u); dead = true; break; }
    }
    CFENCE();
}

DEV void pre_post(rsrc_t rs, int idx, u32 stage, v2u ep) { st8s(rs, threadIdx.x == 0 ? foff(F_PRE + idx) : OOB, 0, (v2u){ep.x + stage, ep.y}); }
DEV void pre_poll(rsrc_t rs, int first, int n, u32 stage, v2u ep) {
    const int lane = threadIdx.x & 63;
#pragma unroll 1
    for (int it = 0; it < SPIN_CAP; ++it) {
        CFENCE();
        const v2u a = ld8s(rs, foff(F_PRE + first + min(lane, n - 1)), 0), b = ld8s(rs, foff(F_PRE + first + min(lane + 64, n - 1)), 0);
        if (all_(a.y == ep.y && b.y == ep.y && max(a.x - ep.x - stage, b.x - ep.x - stage) < 8u - stage)) break;
        __builtin_amdgcn_s_sleep(2);
    }
    CFENCE();
}
DEV void pre_wait(rsrc_t rs, int first, int n, u32 stage, v2u ep) {
    if (uni(threadIdx.x >> 6) == 0) pre_poll(rs, first, n, stage, ep);
    BAR();
}
#define POLX 0u
DEV u32 pol_wait(rsrc_t rs, v2u ep) {
    const int lane = threadIdx.x & 63;
    v4u a;
#pragma unroll 1
    for (int it = 0; it < SPIN_CAP; ++it) {
        CFENCE();
        a = ld16s(rs, foff(F_PRE + (lane < NPRE || lane >= 48 ? lane : 0)), 0);
        if (all_(a.y == ep.y && a.x - ep.x - 2u < 6u)) break;
        __builtin_amdgcn_s_sleep(2);
    }
    CFENCE();
    return uu((wave_sum(lane >= 48 ? (float)(int)a.z : 0.f) >= 0.f ? 8192u : 0u) + (wave_sum(lane >= 48 ? (float)(int)a.w : 0.f) >= 0.f ? 16384u : 0u)) ^ POLX;
}
#define PROBE_C 7
#define PROBE_T 620
DEV v2u pol_probe(rsrc_t rs, int id) {
    const int lane = threadIdx.x & 63;
    const u32 off0 = (u32)id * 32768u, tend = rt32() + PROBE_T;
    st4s(rs, lane < 32 ? off0 + (u32)lane * 1024u : OOB, 0, PSENT); DRAIN();
    int d = 1 << 16, calm = 0;
#pragma unroll 1
    for (int q = 0; q < 32 && ((q & 3) || (calm < PROBE_C && (int)(rt32() - tend) < 0)); ++q) {
        const int kind = (q ^ (q >> 2) ^ id) & 3;
        const unsigned long long t0 = __builtin_amdgcn_s_memtime();
        const u32 v = ld4(rs, lane == 0 ? off0 + (u32)(kind * 8192 + (q >> 2) * 1024) : OOB);
        asm volatile("s_waitcnt vmcnt(0)" :: "v"(v) : "memory");
        const int dt = (int)(__builtin_amdgcn_s_memtime() - t0);
        d = lane == kind ? min(d, dt) : d;
        calm += dt < 1100;
    }
    const int d0 = (int)rdl(d, 0), d1 = (int)rdl(d, 1), d2 = (int)rdl(d, 2), d3 = (int)rdl(d, 3), a = min(d0, d2) - min(d1, d3);
    const int b0 = min(max(d0 - d2, -80), 80), b1 = min(max(d1 - d3, -80), 80);
    const u32 xcc = (u32)__builtin_amdgcn_s_getreg(20 | (3 << 11));
    return (v2u){(u32)min(max((xcc & 4u) ? -a : a, -250), 250), (u32)((xcc & 1u) ? b0 + b1 : a > 0 ? b0 - b1 : b1 - b0)};
}
DEV f32x2 ln_stats(const f32x2 (&x)[5]) {
    const float c = f_(uu(u_(x[0].x)));
    f32x2 d[5];
#pragma unroll
    for (int i = 0; i < 5; ++i) d[i] = x[i] - c;
    const f32x2 s2 = (d[0] + d[1]) + (d[2] + d[3]) + d[4], q2 = (d[0] * d[0] + d[1] * d[1]) + (d[2] * d[2] + d[3] * d[3] + d[4] * d[4]);
    float a = s2.x + s2.y, b = q2.x + q2.y, a2, b2;
#define LN_DPP(m) "v_add_f32_dpp %0, %0, %0 " m " row_mask:0xf bank_mask:0xf\n\tv_add_f32_dpp %1, %1, %1 " m " row_mask:0xf bank_mask:0xf\n\ts_nop 0\n\t"
    asm("s_nop 1\n\t" LN_DPP("quad_perm:[1,0,3,2]") LN_DPP("quad_perm:[2,3,0,1]") LN_DPP("row_ror:4")
        "v_add_f32_dpp %0, %0, %0 row_ror:8 row_mask:0xf bank_mask:0xf\n\tv_add_f32_dpp %1, %1, %1 row_ror:8 row_mask:0xf bank_mask:0xf\n\ts_nop 1\n\t"
        "v_permlane16_swap_b32 %0, %1\n\tv_add_f32 %0, %0, %1\n\tv_mov_b32 %1, %0\n\ts_nop 1\n\tv_permlane32_swap_b32 %0, %1\n\tv_add_f32 %0, %0, %1\n\ts_nop 0\n\t"
        "v_readlane_b32 %2, %0, 0\n\tv_readlane_b32 %3, %0, 16\n\ts_nop 1"
        : "+v"(a), "+v"(b), "=s"(a2), "=s"(b2));
    const float m = a2 * (1.0f / DM), var = fmaxf(b2 * (1.0f / DM) - m * m, 0.f);
    const float sd = rbf(__builtin_amdgcn_sqrtf(rbf(rbf(var) + 1e-5f)));
    return (f32x2){rbf(c + m), __builtin_amdgcn_rcpf(sd)};
}
DEV u32 pkv(f32x2 v) { u32 d; asm("v_cvt_pk_bf16_f32 %0, %1, %2" : "=v"(d) : "v"(v.x), "v"(v.y)); return d; }
DEV void store_h(rsrc_t rs, u32 base, int row, const u32 (&hp)[5]) {
    const u32 lane = threadIdx.x & 63;
    const u32 sw = (u32)(row & 15) * 16u, o8 = (lane >> 5) * 8192u + (((lane & 31u) * 8u) ^ sw), hb = base + (u32)(row >> 5) * (32 * DM * 2) + (u32)(row & 31) * 256u;
    st8s(rs, o8, hb, (v2u){hp[0], hp[1]});
    st8s(rs, o8, hb + 16384, (v2u){hp[2], hp[3]});
    st4s(rs, (lane * 4) ^ sw, hb + 32768, hp[4]);
}

#define DMA16(ldsb, vo, so) asm volatile("s_mov_b32 m0, %0\n\ts_nop 0\n\tbuffer_load_dwordx4 %1, %2, %3 offen sc1 lds" :: "s"(ldsb), "v"(vo), "s"(srd), "s"(so) : "memory")
#define DMA16I(ldsb, lo, vo, so, go) do { u32 so_; asm volatile("s_add_u32 m0, %1, %5\n\ts_add_u32 %0, %4, %6\n\tbuffer_load_dwordx4 %2, %3, %0 offen sc1 lds" \
                         : "=&s"(so_) : "s"(ldsb), "v"(vo), "s"(srd), "s"(so), "i"(lo), "i"(go) : "memory", "scc"); } while (0)
#define DMA32(ldsb, vo, so) asm volatile("s_mov_b32 m0, %0\n\ts_nop 0\n\tbuffer_load_dwordx4 %1, %2, %3 offen sc1 lds\n\tbuffer_load_dwordx4 %1, %2, %3 offen offset:1024 sc1 lds" \
                         :: "s"(ldsb), "v"(vo), "s"(srd), "s"(so) : "memory")
#define DMAIN(sr, ldsb, vo, so) asm volatile("s_mov_b32 m0, %0\n\ts_nop 0\n\tbuffer_load_dwordx4 %1, %2, %3 offen lds" :: "s"(ldsb), "v"(vo), "s"(sr), "s"(so) : "memory")
DEV v4u mk_srd(const void* q, u32 bytes) { return (v4u){uu((u32)(size_t)q), uu((u32)((size_t)q >> 32) & 0xffffu), bytes, 0x00020000u}; }
#define DMANT(sr, ldsb, vo, so) asm volatile("s_mov_b32 m0, %0\n\ts_nop 0\n\tbuffer_load_dwordx4 %1, %2, %3 offen " INNT " lds" :: "s"(ldsb), "v"(vo), "s"(sr), "s"(so) : "memory")
#define AHEAD(sr, vo, so) asm volatile("buffer_load_dwordx4 %0, %1, %2, %3 offen" : "+v"(junk) : "v"(vo), "s"(sr), "s"(so) : "memory")
#define MK_SRD() const v4u srd = mk_srd(p.ws, W_END)

constexpr int CV_W = 40960;
template <bool B> struct CvL {
    static constexpr int NF = B ? 5 : 3, NK = B ? 4 : 2, KP = 544 + NK * 256, CH = NF * 1024 + 4 * KP;
};
constexpr int CV_Y = 4 * CvL<false>::CH, CV_YP = 272;
static_assert(CV_Y + 32 * CV_YP <= CV_W && 2 * CvL<true>::CH <= CV_Y, "conv lds");
DEV int cv_chan(int u, int i) { return 16 * (u % 40) + 4 * i + u / 40; }
template <bool B> DEV void cv_filt(const Params& p, u32 dst, int d, int i) {
    const u32 lane = threadIdx.x & 63;
    const v4u sf = mk_srd(p.ssmk + d * SEQ, SEQ * 2);
    if (i == 0) DMANT(sf, dst, lane < 4u ? OOB : lane * 16u - 64u, 0);
    else DMANT(sf, uu(dst + i * 1024), i == CvL<B>::NF - 1 && lane >= 4u ? OOB : lane * 16u, i * 1024 - 64);
}
DEV v4u cv_kvld(rsrc_t rs, int d, int i) { return ld16s(rs, (threadIdx.x & 63) * 16u, uu(W_KVT + (u32)d * (SEQ * 2) + i * 1024)); }
template <bool B> DEV void cv_kvput(lptr F, const v4u* k) {
    const int lane = threadIdx.x & 63;
#pragma unroll
    for (int i = 0; i < CvL<B>::NK; ++i) lds_w16(F + CvL<B>::NF * 1024 + (lane & 3) * CvL<B>::KP + 512 + i * 256 + (lane >> 2) * 16, k[i]);
}
template <bool B> DEV void cv_zero(lptr F) {
    const int lane = threadIdx.x & 63;
#pragma unroll
    for (int i = 0; i < 4; ++i) lds_w16(F + (lane >> 5) * CvL<B>::CH + CvL<B>::NF * 1024 + i * CvL<B>::KP + (lane & 31) * 16, (v4u){0u, 0u, 0u, 0u});
}
#define M32V(d, a, b) asm volatile("v_mfma_f32_32x32x16_bf16 %0, %1, %2, %0" : "+v"(d) : "v"(a), "v"(b))
template <bool B, class G> DEV void cv_run(const Params& p, rsrc_t rs, lptr F, int d, u32 D, f32x4 sp, float (&y)[16], G&& gap) {
    constexpr int KP = CvL<B>::KP, FO = B ? 2048 : 0;
    const int lane = threadIdx.x & 63, n = lane & 31, hh = lane >> 5, c = lane & 3;
    lptr fa = F + 64 + (32 * hh + 16 * (c & 1) + 8 * ((lane >> 4) & 1) + 4 * (c >> 1) - (lane & 12)) * 2;
    lptr kl = F + CvL<B>::NF * 1024 + (32 + n - hh) * 16;
    v4u dg;
#pragma unroll
    for (int q = 0; q < 4; ++q) { const int k = (lane & 35) ? -1 : (n & 4) + (n >> 3); dg[q] = k == 2 * q ? D : k == 2 * q + 1 ? D << 16 : 0u; }
    v4u l[4], h[4]; v2u fc[4], fh[4];
    f32x16 acc[4];
#pragma unroll
    for (int j = 0; j < 4; ++j) { l[j] = lds_r16(kl + j * KP); if constexpr (B) h[j] = lds_r16(kl + j * KP + 512); }
#pragma unroll
    for (int j = 0; j < 2; ++j) { fc[j] = lds_tr(fa + FO - 32 * j); if constexpr (B) fh[j] = lds_tr(fa - 32 * j); }
    asm volatile("s_nop 1" : "+v"(dg));
#pragma unroll
    for (int j = 0; j < 4; ++j) asm volatile("v_mfma_f32_32x32x16_bf16 %0, %1, %2, 0" : "=&v"(acc[j]) : "v"(dg), "v"(B ? h[j] : l[j]));
    constexpr int U = B ? 2 : 4;
    kl -= 32 * U; fa -= 32;
    auto body = [&](bool first) __attribute__((always_inline)) {
#pragma unroll
        for (int z = 0; z < U; ++z) {
            const int c = 2 * (z & 1);
            const v4u f = __builtin_shufflevector(fc[c], fc[c + 1], 0, 1, 2, 3), g = __builtin_shufflevector(fh[c], fh[c + 1], 0, 1, 2, 3);
#pragma unroll
            for (int j = 0; j < 4; ++j) {
                M32V(acc[j], f, l[j]); l[j] = lds_r16(kl + j * KP + 32 * (U - z - 1));
                if (j < 2) fc[(c ^ 2) + j] = lds_tr(fa + FO + (z + 1) * 128 + 32 - 32 * j);
                if (first && j > 0) gap(5 * z + j - 1);
                SCHED();
            }
            if constexpr (B) {
#pragma unroll
                for (int j = 0; j < 4; ++j) {
                    M32V(acc[j], g, h[j]); h[j] = lds_r16(kl + j * KP + 512 + 32 * (U - z - 1));
                    if (j < 2) fh[(c ^ 2) + j] = lds_tr(fa + (z + 1) * 128 + 32 - 32 * j);
                    if (first && j > 1) gap(5 * z + j + 1);
                    SCHED();
                }
            }
        }
        kl -= 32 * U; fa += 128 * U;
    };
    if constexpr (B) body(true);
#pragma unroll 1
    for (int m = B ? 2 * U : 0; m < 32; m += 2 * U) body(false);
    asm volatile("s_nop 11" : "+v"(acc[0]), "+v"(acc[1]), "+v"(acc[2]), "+v"(acc[3]));
    const float o[3] = {acc[1][15] + acc[2][14] + acc[3][13], acc[2][15] + acc[3][14], acc[3][15]};
    if constexpr (!B) st16(rs, lane == 63 ? W_SP + (u32)d * 16u : OOB, (v4u){u_(o[0]), u_(o[1]), u_(o[2]), 0u});
#pragma unroll
    for (int i = 0; i < 16; ++i) {
        float v = acc[0][i];
#pragma unroll
        for (int j = 1; j < 4; ++j) if (j <= i) v += acc[j][i - j];
        if (i < 3) { const float e = f_((u32)__builtin_amdgcn_ds_bpermute((hh ? lane - 32 : lane + 31) * 4, (int)u_(o[i]))); v += lane == 0 ? sp[i] : e; }
        y[i] = v;
    }
}
DEV void cv_put(lptr F, int pr, const float (&y0)[16], const float (&y1)[16]) {
    const int lane = threadIdx.x & 63;
#pragma unroll
    for (int i = 0; i < 16; ++i) *(LAS u32*)(F + CV_Y + (lane & 31) * CV_YP + (lane >> 5) * 128 + i * 8 + pr * 4) = pk(y0[i], y1[i]);
}
DEV void cv_out(rsrc_t rs, lptr F, int u, int blk0) {
    const int lane = threadIdx.x & 63;
    lptr q = F + CV_Y + ((lane >> 2) & 3) * CV_YP + ((lane >> 4) * 4 + (lane & 3)) * 8;
    const u32 yo = W_Y + (u32)(((lane >> 2) * 8 + blk0) * DM + 4 * u + (lane & 3)) * 16u;
#pragma unroll
    for (int i = 0; i < 8; ++i) { lptr a = q + (i >> 1) * (8 * CV_YP) + (i & 1) * 128; st16(rs, yo + (u32)((i & 1) * 128 + (i >> 1)) * (DM * 16), tr2(a, a + 4 * CV_YP)); }
}
struct KvIn { v2u k[19], v[16]; v4u s[2]; };
template <class F> DEV void kvt_load(const Params& p, int tile, KvIn& in, F&& mid) {
    const int lane = threadIdx.x & 63;
    const rsrc_t rk = mk_rsrc(p.bk, ACT), rv = mk_rsrc(p.bv, ACT), rh = mk_rsrc(p.shift, DM * 8);
    const int row = (tile / 10) * 64 + (lane >> 4) * 16, col = (tile % 10) * 64 + (lane & 15) * 4;
    const u32 vo = (u32)(row * DM + col) * 2u, vp = row > 0 ? vo - 3 * (DM * 2) : OOB;
#pragma unroll
    for (int i = 0; i < 2; ++i) in.s[i] = ldin16(rh, (u32)col * 8u + i * 16, 0);
#pragma unroll
    for (int i = 0; i < 19; ++i) { if (i == 7) mid(); in.k[i] = i < 3 ? ldin8(rk, vp, i * (DM * 2)) : ldin8(rk, vo, (i - 3) * (DM * 2)); if (i >= 3) in.v[i - 3] = ldin8(rv, vo, (i - 3) * (DM * 2)); }
}
DEV void kvt_make(rsrc_t rs, lptr xt, int tile, const KvIn& in) {
    const int lane = threadIdx.x & 63;
#pragma unroll
    for (int c = 0; c < 2; ++c) {
        const v4u sv = in.s[c];
        const f32x2 sh[4] = {{lo_f(sv.x), lo_f(sv.z)}, {hi_f(sv.x), hi_f(sv.z)}, {lo_f(sv.y), lo_f(sv.w)}, {hi_f(sv.y), hi_f(sv.w)}};
        f32x2 kf[19];
#pragma unroll
        for (int i = 0; i < 19; ++i) kf[i] = up2(in.k[i][c]);
        u32 kv[16];
#pragma unroll
        for (int i = 0; i < 16; ++i) kv[i] = pk2(((kf[i] * sh[3] + kf[i + 1] * sh[2]) + (kf[i + 2] * sh[1] + kf[i + 3] * sh[0])) * up2(in.v[i][c]));
#pragma unroll
        for (int q = 0; q < 8; ++q) {
            const u32 sel = (q & 1) ? 0x07060302u : 0x05040100u; const u32* z = kv + (q >> 1);
            *(LAS v2u*)(xt + ((2 * c + (q & 1)) * 16 + (lane & 15)) * 144 + (lane >> 5) * 64 + (q >> 1) * 16 + ((lane >> 4) & 1) * 8) =
                (v2u){__builtin_amdgcn_perm(z[4], z[0], sel), __builtin_amdgcn_perm(z[12], z[8], sel)};
        }
    }
    const u32 o = W_KVT + (u32)(((tile % 10) * 64 + 4 * (lane >> 3)) * SEQ + (tile / 10) * 64 + (lane & 7) * 8) * 2u;
#pragma unroll
    for (int i = 0; i < 8; ++i) st16(rs, o + (u32)(32 * (i & 1) + (i >> 1)) * (SEQ * 2), lds_r16(xt + (8 * i + (lane >> 3)) * 144 + (lane & 7) * 16));
}
struct RowX { v2u a, b; u32 c; };
DEV void ln_row(rsrc_t rs, int row, const RowX& r) {
    const f32x2 x[5] = {up2(r.a.x), up2(r.a.y), up2(r.b.x), up2(r.b.y), up2(r.c)};
    const f32x2 st = ln_stats(x);
    u32 hp[5];
#pragma unroll
    for (int q = 0; q < 5; ++q) hp[q] = pk2(up2(pkv(x[q] - st.x)) * st.y);
    store_h(rs, W_H1, row, hp);
}
DEV v4u mem0_load(const Params& p, int j) {
    const u32 n = (u32)(j * 256 + threadIdx.x);
    return ldin16(mk_rsrc(p.mem0, 8 * DM * 2), n < 640u ? ((2 * ((n >> 2) & 3) + (n & 1)) * DM + (n >> 4) * 16 + (n & 2) * 4) * 2u : OOB, 0);
}
DEV void pre_flags(rsrc_t rs, int j, v4u m) {
    const u32 n = (u32)(j * 256 + threadIdx.x);
#pragma unroll
    for (int i = 0; i < 4; ++i) m[i] = m[i] == PSENT ? 0x7FC17FC1u : m[i];
    st4s(rs, n - 36u * 256u < F_CLR ? foff(n - 36u * 256u) : OOB, 0, 0u);
    st16(rs, n < 640u ? W_MEM0 + (n >> 9) * 32768u + (n & 511u) * 16u : OOB, m);
}
constexpr int KT0 = NPRE * 4, LN0 = NPRE * 8;
struct Early { RowX r[2]; v4u m; };
DEV void conv_early(const Params& p, rsrc_t rs, int j, Early& e) {
    const int lane = threadIdx.x & 63, w = uni(threadIdx.x >> 6), u = j * 4 + w;
    if (j < NPRE) {
        const rsrc_t rx = mk_rsrc(p.x, ACT);
#pragma unroll
        for (int i = 0; i < 2; ++i) {
            const u32 so = uu((u + i * KT0) * (DM * 2));
            e.r[i].a = __builtin_amdgcn_raw_buffer_load_b64(rx, lane * 8u, so, 0); e.r[i].b = __builtin_amdgcn_raw_buffer_load_b64(rx, lane * 8u + 512u, so, 0);
            e.r[i].c = __builtin_amdgcn_raw_buffer_load_b32(rx, lane * 4u + 1024u, so, 0);
        }
        e.m = mem0_load(p, j);
    }
}
DEV void conv_stage(const Params& p, rsrc_t rs, lptr lds, int j, int g0, int ge, v2u ep, Early& e) {
    const int tid = threadIdx.x, lane = tid & 63, w = uni(tid >> 6), u = j * 4 + w;
    lptr F = lds + w * CV_W;
    const u32 fb = uu(w * CV_W);
    const rsrc_t rd = mk_rsrc(p.ssmD, DM * 2);
    float y0[16], y1[16];
    KvIn in;
    if (j < NPRE) {
        constexpr int CH = CvL<false>::CH;
        if (w == 1) LST(1500 + j, rt32());
        pre_flags(rs, j, e.m);
        ln_row(rs, u, e.r[0]); ln_row(rs, u + KT0, e.r[1]);
        if (w == 1) LST(1550 + j, rt32());
        CFENCE();
        kvt_load(p, u, in, [&] { VMW(13); BAR(); pre_post(rs, j, 1u, ep); if (w == 0) LST(1450 + j, rt32()); });
        if (j == 20) LST(260, rt32());
#pragma unroll 1
        for (u32 o = (u32)(j * 256 + tid) * 16u; o < 2 * MB; o += NPRE * 4096) st16(rs, o, (v4u){PSENT, PSENT, PSENT, PSENT});
        const u32 D = __builtin_amdgcn_raw_buffer_load_b16(rd, (u32)cv_chan(u, lane & 3) * 2u, 0, 0);
#pragma unroll 1
        for (int i = 0; i < 4; ++i) { const int d = cv_chan(u, i); cv_filt<false>(p, fb + i * CH, d, 0); cv_filt<false>(p, fb + i * CH, d, 1); cv_filt<false>(p, fb + i * CH, d, 2); }
        kvt_make(rs, F + CV_Y, u, in);
        cv_zero<false>(F); cv_zero<false>(F + 2 * CH);
        DRAIN(); BAR();
        pre_post(rs, j, 2u, ep);
        if (j == 20) LST(262, rt32());
        pre_wait(rs, 0, NPRE, 2u, ep);
        if (j == 20) LST(281, rt32());
        {
            v4u k[8];
#pragma unroll
            for (int i = 0; i < 8; ++i) k[i] = cv_kvld(rs, cv_chan(u, i >> 1), i & 1);
#pragma unroll
            for (int i = 0; i < 4; ++i) cv_kvput<false>(F + i * CH, k + 2 * i);
        }
        if (j == 20) LST(286, rt32());
#pragma unroll 1
        for (int i = 0; i < 4; i += 2) {
            cv_run<false>(p, rs, F + i * CH, cv_chan(u, i), rdl(D, i), f32x4{}, y0, [](int) {});
            cv_run<false>(p, rs, F + (i + 1) * CH, cv_chan(u, i + 1), rdl(D, i + 1), f32x4{}, y1, [](int) {});
            cv_put(F, i >> 1, y0, y1);
        }
        if (j == 20) LST(287, rt32());
        cv_out(rs, F, u, 0);
        if (j == 20) LST(263, rt32());
    } else {
        const v2u vote = pol_probe(rs, u - NPRE * 4);
        st16(rs, lane == 0 ? foff(F_PRE + 48 + u - NPRE * 4) : OOB, (v4u){ep.x + 2u, ep.y, vote.x, vote.y});
#pragma unroll 1
        for (int t = u + 128; t < 320; t += 16) { kvt_load(p, t, in, [] {}); kvt_make(rs, F, t, in); }
    }
    DRAIN(); BAR();
    if (g0 < ge) {
        constexpr int CH = CvL<true>::CH;
        pre_post(rs, j, 3u, ep);
        cv_zero<true>(F);
        if (w == 0) { pre_poll(rs, 64, 32, 3u, ep); pre_poll(rs, 0, NCV, 3u, ep); }
        BAR();
        v4u k[4];
        auto fetch = [&](u32 dst, int d, int i) { if (i < 5) cv_filt<true>(p, dst, d, i); else if (i < 9) k[i - 5] = cv_kvld(rs, d, i - 5); };
#pragma unroll
        for (int i = 0; i < 9; ++i) fetch(fb, cv_chan(g0 * 4 + w, 0), i);
        const u32 ch = (u32)cv_chan(min((g0 + 8 * (lane >> 2)) * 4 + w, 159), lane & 3);
        u32 D = __builtin_amdgcn_raw_buffer_load_b16(rd, ch * 2u, 0, 0);
        v4u sp = ld16s(rs, W_SP + ch * 16u, 0);
        asm volatile("" : "+v"(D), "+v"(sp));
#pragma unroll 1
        for (int g = g0, n = 0; g < ge; g += 8) {
            const int un = g * 4 + w;
            if (g == 16) { LST(282, rt32()); LST(288, __builtin_amdgcn_s_memtime()); }
#pragma unroll 1
            for (int i = 0; i < 4; i += 2, n += 2) {
                const int d1 = (int)rdl(ch, n + 1), d2 = (int)rdl(ch, n + 2);
                cv_kvput<true>(F, k);
                cv_run<true>(p, rs, F, 0, rdl(D, n), (f32x4){f_(rdl(sp.x, n)), f_(rdl(sp.y, n)), f_(rdl(sp.z, n)), 0.f}, y0, [&](int q) { fetch(fb + CH, d1, q); });
                cv_kvput<true>(F + CH, k);
                cv_run<true>(p, rs, F + CH, 326, rdl(D, n + 1), (f32x4){f_(rdl(sp.x, n + 1)), f_(rdl(sp.y, n + 1)), f_(rdl(sp.z, n + 1)), 0.f}, y1, [&](int q) { fetch(fb, d2, q); });
                cv_put(F, i >> 1, y0, y1);
            }
            cv_out(rs, F, un, 4);
            if (g == 16) { LST(283, rt32()); LST(289, __builtin_amdgcn_s_memtime()); }
        }
        DRAIN(); BAR();
    }
    pre_post(rs, j, 5u, ep);
    if (j == 20) LST(284, rt32());
    if (j == 3) LST(285, rt32());
}

#define M32A(first, d, a, b) do { if (first) asm volatile("v_mfma_f32_32x32x16_bf16 %0, %1, %2, 0" : "=&v"(d) : "a"(a), "v"(b)); \
                                  else asm volatile("v_mfma_f32_32x32x16_bf16 %0, %1, %2, %0" : "+v"(d) : "a"(a), "v"(b)); } while (0)
#define M32B(first, d, a, b) do { if (first) asm volatile("v_mfma_f32_32x32x16_bf16 %0, %1, %2, 0" : "=&v"(d) : "v"(a), "a"(b)); \
                                  else asm volatile("v_mfma_f32_32x32x16_bf16 %0, %1, %2, %0" : "+v"(d) : "v"(a), "a"(b)); } while (0)
#define VCVT(d, a, b) asm volatile("v_cvt_pk_bf16_f32 %0, %1, %2" : "=v"(d) : "v"(a), "v"(b))
#define VADD(d, a, b) asm volatile("v_add_f32 %0, %1, %2" : "=v"(d) : "v"(a), "v"(b))
#define LD4A(d, vo, so) asm volatile("buffer_load_dword %0, %1, %2, %3 offen sc1" : "=v"(d) : "v"(vo), "s"(srd), "s"(so) : "memory")
DEV u32 wst_src(int lane, int ld) { return (u32)((lane >> 4) * ld + 8 * ((lane & 15) ^ ((lane >> 4) << 2))) * 2u; }
DEV u32 wst_rd(int lane, int w) { const int q4 = (lane >> 2) & 3; return (u32)(q4 * 256 + ((w ^ q4) * 64) + ((lane >> 4) & 1) * 32 + (lane & 3) * 8); }

constexpr int QK_SEL = 81920;
#define QK_LEAD 12
#define QK_SMP 29
#define QK_YB 26
#define QK_W0 2
DEV void role_qkv(const Params& p, lptr lds, int c, v2u ep) {
    const int tid = threadIdx.x, lane = tid & 63, w = uni(tid >> 6), hh = lane >> 5, r32 = lane & 31;
    const int m = c >> 5, h = c & 31;
    const rsrc_t rs = mk_rsrc(p.ws, W_END);
    v4u wt[40];
    {
        const v4u sw = mk_srd(m == 0 ? p.Wq : m == 1 ? p.Wk : p.Wv, DM * NQ * 2), sx = mk_srd(p.x, ACT);
        const u32 vo = wst_src(lane, NQ) + (u32)h * (DH * 2), x8 = (u32)lane * 8u, x4 = (u32)lane * 4u;
        RowX xr[5];
        auto wfill = [&](int i0, int n) {
#pragma unroll 1
            for (int i = i0; i < i0 + n; ++i) DMAIN(sw, uu((4 * i + w) * 1024), vo, uu((16 * i + 4 * w) * (NQ * 2)));
        };
        wfill(0, QK_W0);
#pragma unroll
        for (int i = 0; i < 5; ++i)
            asm volatile("buffer_load_dwordx2 %0, %3, %5, %6 offen\n\tbuffer_load_dwordx2 %1, %3, %5, %6 offen offset:512\n\tbuffer_load_dword %2, %4, %5, %6 offen offset:1024"
                         : "=&v"(xr[i].a), "=&v"(xr[i].b), "=&v"(xr[i].c) : "v"(x8), "v"(x4), "s"(sx), "s"(uu((LN0 + 4 * c + w + 384 * i) * (DM * 2))) : "memory");
        wfill(QK_W0, 10 - QK_W0);
        if (c == 3) LST(275, rt32());
        VMW(10 - QK_W0);
        if (c == 3) LST(276, rt32());
#pragma unroll
        for (int i = 0; i < 5; ++i) {
            asm volatile("" : "+v"(xr[i].a), "+v"(xr[i].b), "+v"(xr[i].c));
            const int row = LN0 + 4 * c + w + 384 * i;
            if (i < 4 || row < SEQ) ln_row(rs, row, xr[i]);
            wfill(10 + 6 * i, 6);
        }
        if (c == 3) LST(264, rt32());
        lptr tb = lds + hh * 2048 + wst_rd(lane, w);
        SFOR(k, 40)
            if constexpr (k == 0) { VMW(15); BAR(); }
            if constexpr (k == 28) {
                if (w == 0) pre_poll(rs, 0, NPRE, 1u, ep);
                DRAIN(); BAR();
                pre_post(rs, 96 + c, 1u, ep);
            }
            lptr q = tb + (k >> 3) * 32768 + (k & 7) * 4096; wt[k] = tr2(q, q + 1024); asm volatile("" : "+a"(wt[k]));
        SEND
    }
    if (c == 3) LST(268, rt32());
    if (w == 0) LST(1350 + c, rt32());
    bool dead = false;
    int n0, n1;
    const u32 t16 = (u32)(w * 2048 + lane * 16), wb = uu(w * 2048);
    u32 ar[16];
#pragma unroll
    for (int j = 0; j < 16; ++j) ar[j] = (u32)(r32 * 256 + ((2 * (j & 7) + hh) ^ (r32 & 15)) * 16 + (j >> 3) * 49152);
    MK_SRD();
#define XRD(y, ks) lds_r16(lds + ar[((ks) & 7) + ((ks) >= 24 ? 8 : 0)] + ((ks) >> 3) % 3 * 16384 + (y) * 8192)
#define QM0(y, b) asm volatile("s_add_u32 m0, %0, %1" :: "s"(wb), "i"((b) * 16384 + (y) * 8192) : "scc", "memory")
#define QDMA(i, vo) asm volatile("buffer_load_dwordx4 %0, %1, %2 offen offset:%3 sc1 lds" :: "v"(vo), "s"(srd), "s"(((i) >> 1) * 8192), "i"(((i) & 1) * 1024) : "memory")
    const u32 h2flag = (lane < 16 && w == 0) ? foff(F_H2 + lane) : OOB;
    const u32 ol = (m == 2 ? (u32)(2 * w + (r32 >> 4)) * 1024u : (u32)((r32 >> 4) * 4 + w) * 1024u) + (u32)(hh * 16 + (r32 & 15)) * 16u
                 + (m == 2 ? W_VO : m == 1 ? W_KQ : W_KQ + 8192u) + (u32)h * 16384u;
    const u32 fo = tid < 2 ? foff(F_QKV + tid * 96 + c) : OOB;
    lptr selw = lds + QK_SEL + (w == 0 ? 0 : 64), selr = lds + QK_SEL;
    PIN(selw);
    static_assert(QK_LEAD > 8 && QK_LEAD % 2 == 0 && LN0 == 10 * 32, "X = 8 comes up, before any item of layer 1");
    static_assert(W_H2 - W_H1 == 2 * 64 * (32 * DM * 2), "h");
    auto run = [&](auto isv) __attribute__((always_inline)) {
        constexpr bool V = decltype(isv)::value;
        constexpr int YB = QK_YB;
        f32x16 ax, ay = {};
        v4u x[8], pp, pq;
        u32 xv = t16 + W_H1, ox = ol, oy = OOB, nn = 1, lt = 0, fcnt = 0, fa = OOB;
        asm volatile("" : "+v"(lt), "+v"(nn));
#pragma unroll
        for (int z = 0; z < 5; ++z) x[z] = XRD(1, 40 - YB + z);
#define QPK(r_, a1, a_, q_) do { if constexpr (V) VCVT(r_[q_], a1[4 * (a_) + 8 * ((q_) >> 1) + 2 * ((q_) & 1)], a1[4 * (a_) + 8 * ((q_) >> 1) + 2 * ((q_) & 1) + 1]); \
                                 else VCVT(r_[q_], a1[4 * (a_) + (q_)], a1[4 * (a_) + (q_) + 8]); } while (0)
#define QPK4(r_, a1, a_) do { QPK(r_, a1, a_, 0); QPK(r_, a1, a_, 1); QPK(r_, a1, a_, 2); QPK(r_, a1, a_, 3); } while (0)
        for (;;) {
            u32 ls = uu(lt), yv, fv, nx, need;
            if (ls != 0u && uu(nn) < 256u) pre_wait(rs, 96, 96, 1u, ep);
            else if (ls != 0u) {
                n1 = (int)(uu(nn) >> 8);
                CFENCE();
                const u32 hf = h2flag + (u32)(~n1 & 1) * 2048u;
                u32 f2 = ld4(rs, hf);
                asm volatile("s_waitcnt vmcnt(0)" : "+v"(f2) :: "memory");
                *(LAS u32*)(selw + 16) = (u32)__builtin_amdgcn_ballot_w64(f2 < (u32)n1) & 0xFFFFu;
                BAR();
                st4s(rs, fa, 0, fcnt);
                fa = OOB;
                if (n1 > NSEG) { LDUMP(w == 0, 64 + c); __builtin_amdgcn_endpgm(); }
                if (uu(*(LAS u32*)(selr + 16)) != 0u) {
                    if (w == 0) wait_flags(rs, hf, (u32)n1, dead);
                    BAR();
                }
            }
#define QT(k) do {} while (0)
            QT(0);
            SCHED();
            QM0(0, 0); NOPS(0); QDMA(0, xv); QDMA(1, xv); QM0(0, 1);
            SFOR(z, 80)
                constexpr int ks = (z + 40 - YB) % 40, zx = z - YB;
                constexpr auto upto = [](int q) { const int d = (q - YB + 49) % 40; return q + 6 + (d == 0 || d == 2 ? 1 : d == 1 ? 2 : 0); };
                if constexpr (zx >= 0 && zx < 40) { if constexpr (V) M32B(ks == 0, ax, x[z & 7], wt[ks]); else M32A(ks == 0, ax, wt[ks], x[z & 7]); }
                else { if constexpr (V) M32B(ks == 0, ay, x[z & 7], wt[ks]); else M32A(ks == 0, ay, wt[ks], x[z & 7]); }
                if constexpr (z == 78) lt = *(LAS u32*)selr;
#pragma unroll
                for (int q = upto(z - 1); q < upto(z); ++q) x[q & 7] = XRD(q % 80 < YB || q % 80 >= YB + 40, (q + 40 - YB) % 40);
                SCHED();
                (void)&srd; (void)&wb;
                if constexpr (z < 2) QDMA(z + 2, xv);
                if constexpr (z >= 2 && z < 14 && (z & 3) == 2) QM0(0, (z + 6) >> 2);
                if constexpr (z >= 3 && z < 15 && (z & 1)) QDMA((z + 5) >> 1, xv);
                if constexpr (z == 4) { n0 = uni((int)nn); n1 = n0 >> 8; n0 &= 255; PINS(n0); PINS(n1); }
                if constexpr (z == 8) { nx = n0 < NSEG ? W_H1 + (u32)n0 * (32 * DM * 2) : OOB; PINS(nx); }
                if constexpr (z == 12) { yv = t16 + nx; PIN(yv); }
                if constexpr (zx == -6) { QT(1); asm volatile("s_waitcnt vmcnt(6) lgkmcnt(0)\n\ts_barrier" ::: "memory"); QT(2); }
                if constexpr (zx == -5) st4s(rs, fa, 0, fcnt);
                if constexpr (zx == -4 || zx == 0 || zx == 4) QM0(1, (zx + 4) >> 2);
                if constexpr (zx >= -3 && zx < 8 && (zx & 1)) QDMA((zx + 3) >> 1, yv);
                if constexpr (zx == 2) QPK4(pp, ay, 0);
                if constexpr (zx == 6) QPK4(pq, ay, 1);
                if constexpr (zx == 8) st16(rs, oy, pp);
                if constexpr (zx == 9) st16(rs, oy + 512, pq);
                if constexpr (zx == 10) { QT(3); asm volatile("s_waitcnt vmcnt(9)\n\ts_barrier" ::: "memory"); QT(4); }
                if constexpr (zx == 11 || zx == 15) QM0(1, (zx + 1) >> 2);
                if constexpr (zx >= 12 && zx < 20 && (zx & 1) == 0) QDMA(zx >> 1, yv);
                if constexpr (zx == 21) { fcnt = lane == 0 ? (u32)n0 : (u32)n1; fa = fo; asm volatile("" : "+v"(fcnt), "+v"(fa)); }
                if constexpr (zx == 22) { oy = (ol + (u32)n0 * (NH * 16384u)) | (yv & OOB); PIN(oy); }
                if constexpr (zx == 23) { n0 = min(n0 + 1, NSEG); PINS(n0); nx = (u32)min(NSEG, n1 + QK_LEAD); PINS(nx); }
                if constexpr (zx == 24)
                    asm volatile("s_cmp_ge_u32 %1, %0\n\ts_cselect_b32 %0, %4, %1\n\ts_addc_u32 %2, %2, 0\n\ts_cmp_lt_u32 %0, 64\n\ts_cselect_b32 %3, 0, %2"
                                 : "+s"(nx), "+s"(n0), "+s"(n1), "=&s"(need) : "s"(n1 + 64) : "scc");
                if constexpr (zx == 25) { n0 += 1 - (int)min(nx >> 6, 1u); PINS(n0); }
                if constexpr (zx == 26) { nn = (u32)(n0 | n1 << 8); PIN(nn); }
                if constexpr (zx == 27) { ls = W_H1 + (nx + (nx & 64u)) * (32 * DM * 2); PINS(ls); }
                if constexpr (zx == 28) { xv = t16 + ls; PIN(xv); need = nx == 8u ? 255u : need; PINS(need); }
                if constexpr (zx == 34) { asm volatile("s_waitcnt vmcnt(%0) lgkmcnt(0)\n\ts_barrier" :: "i"(QK_SMP < 34 ? 7 : 6) : "memory"); QT(5); }
                if constexpr (zx == QK_SMP) { const u32 po = (nx & 1u) * 2048u; LD4A(fv, h2flag, po); }
                if constexpr (zx == 42) QPK4(pp, ax, 0);
                if constexpr (zx == 43) st16(rs, ox, pp);
                if constexpr (zx == 44) QPK4(pq, ax, 1);
                if constexpr (zx == 45) st16(rs, ox + 512, pq);
                if constexpr (zx == 47) { ox = ol + nx * (NH * 16384u); PIN(ox); }
                if constexpr (z == 74) { asm volatile("s_waitcnt vmcnt(2)" : "+v"(fv) :: "memory"); QT(6); ls = (u32)__builtin_amdgcn_ballot_w64(fv < need) & 0xFFFFu; PINS(ls); }
                if constexpr (z == 75) { lt = ls; PIN(lt); *(LAS u32*)selw = lt; }
                if constexpr (z == 77) asm volatile("s_waitcnt lgkmcnt(2)\n\ts_barrier" ::: "memory");
                SCHED();
            SEND
        }
    };
    if (m == 2) run(IC<1>{}); else run(IC<0>{});
}

DEV void role_out(const Params& p, lptr lds, int L, int kq, int ng, v2u ep) {
    const int tid = threadIdx.x, lane = tid & 63, w = uni(tid >> 6), hh = lane >> 5, r32 = lane & 31;
    const rsrc_t rs = mk_rsrc(p.ws, W_END);
    v4u wo[64];
    bool dead = false;
    {
        const v4u sw = mk_srd(p.Wo, DM * NQ * 2);
        const u32 vo = wst_src(lane, DM) + (u32)ng * 256u;
        auto fill = [&](int b) {
#pragma unroll 1
            for (int i = 0; i < 8; ++i) DMAIN(sw, uu((b % 5) * 32768 + (4 * i + w) * 1024), vo, uu((kq * 1024 + b * 128 + 16 * i + 4 * w) * (DM * 2)));
        };
#pragma unroll 1
        for (int b = 0; b < 5; ++b) fill(b);
        lptr tb = lds + hh * 1024 + wst_rd(lane, w);
        SFOR(b, 8)
            VMW(b < 4 ? 32 : 56 - 8 * b); BAR();
            lptr q = tb + (b % 5) * 32768;
#pragma unroll
            for (int k = 0; k < 8; ++k) { lptr a = q + (k >> 1) * 8192 + (k & 1) * 2048; wo[8 * b + k] = tr2(a, a + 4096); if (b < 7) asm volatile("" : "+a"(wo[8 * b + k])); else PIN(wo[8 * b + k]); }
            if constexpr (b < 3) { BAR(); fill(b + 5); }
        SEND
    }
    pre_wait(rs, 0, NPRE, 1u, ep);
    if (kq == 0 && ng == 0) LST(272 + L, rt32());
    MK_SRD();
    const u32 ofl = lane < 8 ? foff(F_O + L * 32 + kq * 8 + lane) : OOB, l16 = (u32)lane * 16u;
    const u32 fo = (tid == 0) ? foff(F_P + L * 32 + kq * 5 + ng) : OOB;
#define OM0(dst, b) asm volatile("s_add_u32 m0, %0, %1" :: "s"(dst), "i"((b) * 8192) : "scc", "memory")
#define ODMA(i, vo) asm volatile("buffer_load_dwordx4 %0, %1, %2 offen offset:%3 sc1 lds" :: "v"(vo), "s"(srd), "s"(((i) >> 1) * 16384), "i"(((i) & 1) * 1024) : "memory")
    auto first = [&](u32 dst, u32 vo) {
#pragma unroll
        for (int b = 0; b < 5; ++b) { OM0(dst, b); NOPS(0); ODMA(2 * b, vo); ODMA(2 * b + 1, vo); }
    };
    u32 pa = W_P + (u32)(L * NSEG) * P_ITEM + (u32)(2 * hh * 4 + kq) * (DM * 4) + (u32)(ng * 128 + w * 32 + r32) * 4u, fv = 0;
    u32 ov = W_VO + 8192u + (u32)(L * NSEG * NH + kq * 8) * 16384u + (u32)w * 2048u + l16;
    bool early = false;
    for (int s = 0; s < NSEG; ++s) {
        const u32 dst = uu((s & 1) * 65536 + w * 2048), ndst = dst ^ 65536u, need = s + 1 < NSEG ? (u32)s + 2u : 255u;
        lptr A = lds + (s & 1) * 65536 + l16;
        if (!early) {
            if (!all_(ofl == OOB || fv >= (u32)s + 1)) wait_flags(rs, ofl, (u32)s + 1, dead);
            LS3(L == 0 && w == 1, 1, kq * 5 + ng);
            first(dst, ov);
        }
#define OT(k) do {} while (0)
        OM0(dst, 5);
        asm volatile("s_waitcnt vmcnt(8)\n\ts_barrier" ::: "memory");
        OT(0);
        v4u o[64];
#pragma unroll
        for (int k = 0; k < 4; ++k) o[k] = lds_r16(A + k * 1024);
        f32x16 acc;
        u32 far, nv;
        SCHED();
        SFOR(k, 64)
            if constexpr (k < 56) M32B(k == 0, acc, o[k], wo[k]); else M32V(acc, o[k], wo[k]);
            if constexpr (k < 60) o[k + 4] = lds_r16(A + (k + 4) * 1024);
            SCHED();
            (void)&srd; (void)&dst; (void)&ndst;
            if constexpr (k < 12 && (k & 1) == 0) ODMA(10 + k / 2, ov);
            if constexpr (k == 3) asm volatile("s_waitcnt vmcnt(6)\n\ts_barrier" ::: "memory");
            if constexpr (k == 3 || k == 7) OM0(dst, (k + 21) >> 2);
            if constexpr (k == 12) { ov += NH * 16384u; PIN(ov); }
            if constexpr (k == 19) asm volatile("s_waitcnt vmcnt(6)\n\ts_barrier" ::: "memory");
            if constexpr (k == 20) LD4A(fv, ofl, 0);
            if constexpr (k == 35) asm volatile("s_waitcnt vmcnt(1)\n\ts_barrier" ::: "memory");
            if constexpr (k == 40) { asm volatile("s_waitcnt vmcnt(0)" : "+v"(fv) :: "memory"); far = (u32)__builtin_amdgcn_ballot_w64(fv < need) & 255u; PINS(far); }
            if constexpr (k == 41) { nv = far ? OOB : ov; PIN(nv); }
            if constexpr (k >= 42 && k < 62 && (k & 3) == 2) OM0(ndst, (k - 42) >> 2);
            if constexpr (k >= 43 && k < 63 && (k & 1)) ODMA((k - 43) >> 1, nv);
            if constexpr (k == 48) LD4A(fv, ofl, 0);
            if constexpr (k == 4) OT(1);
            if constexpr (k == 20) OT(2);
            if constexpr (k == 36) OT(3);
            if constexpr (k == 41) OT(4);
            SCHED();
        SEND
        OT(5);
        asm volatile("s_nop 11" : "+v"(acc), "+v"(pa));
#pragma unroll
        for (int i = 0; i < 8; ++i) st4s(rs, pa, (u32)((4 * (i >> 1) + (i & 1)) * (4 * DM * 4)), pk(acc[2 * i], acc[2 * i + 1]));
        asm volatile("s_waitcnt vmcnt(15)" : "+v"(fv) :: "memory");
        OT(6);
        LS3(L == 0 && w == 1, 2, kq * 5 + ng);
        early = far == 0u || all_(ofl == OOB || fv >= need);
        if (far != 0u && early) { first(ndst, ov); VMW(10); }
        else { fv = ld4(rs, ofl); asm volatile("s_waitcnt vmcnt(0)" : "+v"(fv) :: "memory"); }
        BAR();
        st4s(rs, fo, 0, (u32)s + 1);
        pa += P_ITEM;
        LS2(L == 0 && w == 1, 160 + kq * 5 + ng);
    }
    LDUMP(L == 0 && w == 1, kq * 5 + ng);
}

DEV void ln_stats2(const f32x2 (&x)[10], f32x2& mean, f32x2& rstd) {
    const f32x2 c = {f_(uu(u_(x[0].x))), f_(uu(u_(x[0].y)))};
    f32x2 d[10];
#pragma unroll
    for (int i = 0; i < 10; ++i) d[i] = x[i] - c;
    f32x2 a = ((d[0] + d[1]) + (d[2] + d[3])) + ((d[4] + d[5]) + (d[6] + d[7])) + (d[8] + d[9]);
    f32x2 b = (d[0] * d[0] + d[2] * d[2] + d[4] * d[4] + d[6] * d[6] + d[8] * d[8]) + (d[1] * d[1] + d[3] * d[3] + d[5] * d[5] + d[7] * d[7] + d[9] * d[9]);
    float a0, a1, b0, b1;
#define LN_DP4(m) "v_add_f32_dpp %0, %0, %0 " m " row_mask:0xf bank_mask:0xf\n\tv_add_f32_dpp %1, %1, %1 " m " row_mask:0xf bank_mask:0xf\n\t" \
                  "v_add_f32_dpp %2, %2, %2 " m " row_mask:0xf bank_mask:0xf\n\tv_add_f32_dpp %3, %3, %3 " m " row_mask:0xf bank_mask:0xf\n\t"
    asm("s_nop 1\n\t" LN_DP4("quad_perm:[1,0,3,2]") LN_DP4("quad_perm:[2,3,0,1]") LN_DP4("row_ror:4") LN_DP4("row_ror:8")
        "v_permlane16_swap_b32 %0, %1\n\tv_add_f32 %0, %0, %1\n\tv_permlane16_swap_b32 %2, %3\n\tv_add_f32 %2, %2, %3\n\ts_nop 1\n\t"
        "v_permlane32_swap_b32 %0, %2\n\tv_add_f32 %0, %0, %2\n\ts_nop 0\n\t"
        "v_readlane_b32 %4, %0, 0\n\tv_readlane_b32 %5, %0, 16\n\tv_readlane_b32 %6, %0, 32\n\tv_readlane_b32 %7, %0, 48\n\ts_nop 1"
        : "+v"(a.x), "+v"(b.x), "+v"(a.y), "+v"(b.y), "=s"(a0), "=s"(b0), "=s"(a1), "=s"(b1));
    const f32x2 m = (f32x2){a0, a1} * (1.0f / DM), v = (f32x2){b0, b1} * (1.0f / DM) - m * m;
    const f32x2 q = up2(pkv(up2(pkv((f32x2){fmaxf(v.x, 0.f), fmaxf(v.y, 0.f)})) + 1e-5f));
    const f32x2 sd = up2(pk(__builtin_amdgcn_sqrtf(q.x), __builtin_amdgcn_sqrtf(q.y)));
    mean = up2(pkv(c + m)); rstd = (f32x2){__builtin_amdgcn_rcpf(sd.x), __builtin_amdgcn_rcpf(sd.y)};
}
DEV void role_row(const Params& p, lptr lds, int id, v2u ep) {
    const int lane = threadIdx.x & 63, w = uni(threadIdx.x >> 6);
    const int pr = id >> 2, L = id & 1, par = (id >> 1) & 1;
    const rsrc_t rs = mk_rsrc(p.ws, W_END), rx = mk_rsrc(p.x, ACT), ro_ = mk_rsrc(p.out, ACT);
    bool dead = false;
    const u32 fp = lane < 20 ? foff(F_P + L * 32 + lane) : OOB, l16 = (u32)lane * 16u, l8 = (u32)lane * 8u, l4 = (u32)lane * 4u, fb = uu(w * CV_W);
    lptr F = lds + w * CV_W;
    MK_SRD();
    for (int s = par; s < NSEG; s += 2) {
        const u32 ro = (u32)(s * 32 + 2 * pr) * (DM * 2);
        if ((s & 6) == 0) {
            if ((s & 30) == 0) pre_poll(rs, 0, s < 32 ? NPRE : NCV, s < 32 ? 3u : 5u, ep);
            if (id == 0 && s == 0) LST(274, rt32());
            if (s == 48) st8s(rs, (id == 1 && lane == 0) ? foff(F_EPOCH) : OOB, 0, (v2u){ep.x + 8u, ep.y});
#pragma unroll 1
            for (int i = 0; i < 5; ++i) {
                const u32 d = fb + i * 2048, o = W_Y + (u32)(16 * pr + (s >> 3)) * 10240u + i * 2048;
                DMA32(d, l16, o); DMA32(d + 10240u, l16, o + 81920u);
            }
            DRAIN();
        }
        RowX xr[2];
        auto xload = [&](rsrc_t r, u32 o, auto aux) {
#pragma unroll
            for (int i = 0; i < 2; ++i) {
                xr[i].a = __builtin_amdgcn_raw_buffer_load_b64(r, l8, o + i * (DM * 2), aux.value); xr[i].b = __builtin_amdgcn_raw_buffer_load_b64(r, l8 + 512u, o + i * (DM * 2), aux.value);
                xr[i].c = __builtin_amdgcn_raw_buffer_load_b32(r, l4 + 1024u, o + i * (DM * 2), aux.value);
            }
        };
        if (L == 0) xload(rx, ro, IC<0>{});
        f32x2 y[10], x[10];
        {
            lptr ya = F + (s & 7) * 2 + l16, yc = F + (s & 7) * 2 + 2048 + (lane >> 1) * 16 + (lane & 1) * 5120;
#pragma unroll
            for (int i = 0; i < 10; ++i) {
                lptr q = i < 8 ? ya + (i & 3) * 2560 + (i >> 2) * 1024 : yc + (i & 1) * 2560;
                y[i] = (f32x2){lo_f(*(LAS u16*)q), lo_f(*(LAS u16*)(q + 10240))}; PIN(y[i]);
            }
        }
        wait_flags(rs, fp, (u32)s + 1, dead);
        LS3(L == 0, 3, ri);
        LMX(L == 0, 0, s, rt32());
        {
            const u32 o = uu(W_P + (u32)(L * NSEG + s) * P_ITEM + (u32)pr * (4 * DM * 4));
            if (L == 1) xload(rs, W_X2 + ro, IC<16>{});
            v4u a[4], b[4]; v2u c[4];
#pragma unroll
            for (int kq = 0; kq < 4; ++kq) { a[kq] = ld16s(rs, l16, o + kq * (DM * 4)); SCHED(); b[kq] = ld16s(rs, l16 + 1024u, o + kq * (DM * 4)); SCHED(); c[kq] = ld8s(rs, l8 + 2048u, o + kq * (DM * 4)); SCHED(); }
            const u32 u0[5] = {xr[0].a.x, xr[0].a.y, xr[0].b.x, xr[0].b.y, xr[0].c}, u1[5] = {xr[1].a.x, xr[1].a.y, xr[1].b.x, xr[1].b.y, xr[1].c};
#pragma unroll
            for (int i = 0; i < 5; ++i) { x[2 * i] = (f32x2){lo_f(u0[i]), lo_f(u1[i])}; x[2 * i + 1] = (f32x2){hi_f(u0[i]), hi_f(u1[i])}; }
#pragma unroll
            for (int kq = 0; kq < 4; ++kq)
#pragma unroll
                for (int i = 0; i < 10; ++i)
                    asm volatile("v_dot2_f32_bf16 %0, %2, %3, %0\n\tv_dot2_f32_bf16 %1, %2, %4, %1" : "+v"(x[i].x), "+v"(x[i].y)
                                 : "v"(i < 4 ? a[kq][i & 3] : i < 8 ? b[kq][i & 3] : c[kq][i & 1]), "s"(0x3F80u), "s"(0x3F800000u));
            asm volatile("s_nop 2" : "+v"(x[8]), "+v"(x[9]));
        }
        LS3(L == 0, 4, ri);
        f32x2 mean, rstd;
        ln_stats2(x, mean, rstd);
#pragma unroll
        for (int i = 0; i < 10; ++i) x[i] += (x[i] - mean) * (y[i] * rstd);
        u32 xp[2][5];
#pragma unroll
        for (int i = 0; i < 5; ++i) { xp[0][i] = pkv((f32x2){x[2 * i].x, x[2 * i + 1].x}); xp[1][i] = pkv((f32x2){x[2 * i].y, x[2 * i + 1].y}); }
        if (L == 0) {
#pragma unroll
            for (int i = 0; i < 5; ++i) { x[2 * i] = (f32x2){lo_f(xp[0][i]), lo_f(xp[1][i])}; x[2 * i + 1] = (f32x2){hi_f(xp[0][i]), hi_f(xp[1][i])}; }
            ln_stats2(x, mean, rstd);
#pragma unroll
            for (int i = 0; i < 10; ++i) x[i] = (x[i] - mean) * rstd;
#pragma unroll
            for (int r = 0; r < 2; ++r) {
                u32 hp[5];
#pragma unroll
                for (int i = 0; i < 5; ++i) hp[i] = pkv((f32x2){x[2 * i][r], x[2 * i + 1][r]});
                store_h(rs, W_H2, s * 32 + 2 * pr + r, hp);
            }
#pragma unroll
            for (int r = 0; r < 2; ++r) {
                st8s(rs, l8, W_X2 + ro + r * (DM * 2), (v2u){xp[r][0], xp[r][1]});
                st8s(rs, l8 + 512u, W_X2 + ro + r * (DM * 2), (v2u){xp[r][2], xp[r][3]});
                st4s(rs, l4 + 1024u, W_X2 + ro + r * (DM * 2), xp[r][4]);
            }
            LS3(L == 0, 5, ri);
            DRAIN();
            st4s(rs, lane == 0 ? foff(F_H2 + par * 16 + pr) : OOB, 0, (u32)s + 1);
        } else {
#pragma unroll
            for (int r = 0; r < 2; ++r) {
                st8s(ro_, l8, ro + r * (DM * 2), (v2u){xp[r][0], xp[r][1]});
                st8s(ro_, l8 + 512u, ro + r * (DM * 2), (v2u){xp[r][2], xp[r][3]});
                st4s(ro_, l4 + 1024u, ro + r * (DM * 2), xp[r][4]);
            }
        }
        if (pr == 0) LST(320 + L * 64 + s, rt32());
        LS2(L == 0, 224 + ri);
        LMX(L == 0, 1, s, rt32());
    }
    LDUMP(L == 0, 32 + ri);
}

constexpr float SCALE = 0.08838834764831845f, LOG2E = 1.4426950408889634f;
constexpr int C_K = 0, C_V = 8192, C_QX = 16384, C_MEM = 24576, C_Q = C_MEM + 10240, C_KM = C_Q + 2304, C_VM = C_KM + 2304, C_DUMP = C_VM + 2048,
              C_ZERO = C_DUMP + 1024, C_WL = C_ZERO + 128;
constexpr int NLF = 14;
static_assert(C_WL % 16 == 0 && C_WL + 4 * NLF * 2048 <= LDS_BYTES, "head lds");
#define CH_VIS 8
#define CH_H0 104
#define CH_W0 82
#define CH_MH 3
#define CH_MW 3
#define CH_REC 28
#define CH_RTY 32u
#define CH_RISE 20u
#define CH_FALL 3u
#define CH_PRE 9
#define CH_PRW 2
#define CH_T0 190
#define CH_UP 2
#define CH_WX 6
#define WAIT_TXT(tmp, t4) "s_memrealtime vcc\n\t.p2align 5\n1:\n\ts_waitcnt lgkmcnt(0)\n\ts_sub_u32 " tmp ", " t4 ", vcc_lo\n\ts_memrealtime vcc\n\ts_cmpk_lt_u32 " tmp ", 400\n\ts_cbranch_scc1 1b\n\t" \
                          "s_add_u32 " tmp ", " tmp ", 4\n\ts_cselect_b32 " tmp ", " tmp ", 0\n2:\n\ts_sub_u32 " tmp ", " tmp ", 1\n\ts_cbranch_scc0 2b\n\t"
DEV void wait_until(u32 t) { u32 tmp; asm volatile(WAIT_TXT("%0", "%1") "s_waitcnt lgkmcnt(0)" : "=&s"(tmp) : "s"(uu(t - 4u)) : "vcc", "scc", "memory"); }
#define SMF_A(acc, a, b) asm volatile("v_smfmac_f32_16x16x64_bf16 %0, %1, %2, %3" : "+v"(acc) : "v"(a), "a"(b), "v"(idx))
#define SMF_V(acc, a, b) asm volatile("v_smfmac_f32_16x16x64_bf16 %0, %1, %2, %3" : "+v"(acc) : "v"(a), "v"(b), "v"(idx))
#define MF0(d, a, b) asm volatile("v_mfma_f32_16x16x32_bf16 %0, %1, %2, 0" : "=&v"(d) : "v"(a), "v"(b))
#define MF1(d, a, b) asm volatile("v_mfma_f32_16x16x32_bf16 %0, %1, %2, %0" : "+v"(d) : "v"(a), "v"(b))
#define MF0H(d, a, b) asm volatile("v_mfma_f32_16x16x16_bf16 %0, %1, %2, 0" : "=&v"(d) : "v"(a), "v"(b))
#define MF1H(d, a, b) asm volatile("v_mfma_f32_16x16x16_bf16 %0, %1, %2, %0" : "+v"(d) : "v"(a), "v"(b))
DEV v8u tr_frag(lptr p0, lptr p1, lptr p2, lptr p3) { return cat8(tr2(p0, p1), tr2(p2, p3)); }
constexpr int qslot(int ks) { return (ks & 1) && ks < 8 ? ks >> 1 : -1; }
constexpr int kslot(int ks) { return ks < 4 ? 4 + ks : -1; }
constexpr int vslot(int ks) { return ks >= 4 ? 4 + ks : -1; }
DEV u32 ring_map(u32 off) { return ((off >> 13) << 15) + (off & 8191u); }
DEV u32 oslot(int s) { return (u32)(s & 3) << 18 | (u32)(s & 2) << 13; }
#define ZRD(x, i) x = __builtin_bit_cast(f32x4, lds_r16(ZP + 16 * (i)))
#define VMAX(d, a, b) asm volatile("v_max_f32 %0, %1, %2" : "=v"(d) : "v"(a), "v"(b))
#define VMAX3(d, a, b, c_) asm volatile("v_max3_f32 %0, %1, %2, %3" : "=v"(d) : "v"(a), "v"(b), "v"(c_))
#define VEXP(d, a) asm volatile("v_exp_f32 %0, %1" : "=v"(d) : "v"(a))
#define VMULC(d, a) asm volatile("v_mul_f32 %0, 0x3e0293ee, %1" : "=v"(d) : "v"(a))
#define CADD(d, a, b) do { d = (a) + (b); PIN(d); } while (0)
#define CMULC(d, a) do { d = (a) * CEXP; PIN(d); } while (0)
#define CCVT(d, a, b) do { d = pk(a, b); PIN(d); } while (0)
#define LGK(n) __builtin_amdgcn_s_waitcnt(0xC07F | (n) << 8)
#define SEL(d, a, b) asm volatile("v_cndmask_b32 %0, %1, %2, %3" : "=v"(d) : "v"(a), "v"(b), "s"(himask))
#define QPERM(d, a, q) asm volatile("v_mov_b32_dpp %0, %1 quad_perm:" q " row_mask:0xf bank_mask:0xf" : "=v"(d) : "v"(a))

#define AL_H 3
#define HD_SMP 56
DEV void role_head(const Params& p, lptr lds, int L, int h, v2u ep) {
    const int tid = threadIdx.x, lane = tid & 63, w = uni(tid >> 6), g = lane >> 4, r = lane & 15;
    const rsrc_t rs = mk_rsrc(p.ws, W_END);
    v8u wq0[10], wq1[10], wk0[10], wk1[10], wv0[10], wv1[10];
    lptr WL = lds + C_WL + w * (NLF * 2048) + lane * 16;
    {
        constexpr int GS = 1056, CS = 48 * GS;
        const v4u sq = mk_srd(p.Wq, DM * NQ * 2), sk = mk_srd(p.Wk, DM * NQ * 2), sv = mk_srd(p.Wv, DM * NQ * 2);
        const u32 vo = (u32)(g * NQ + h * DH + 8 * (r ^ ((g & 1) << 3))) * 2u;
        auto fill = [&](int ch) {
#pragma unroll 1
            for (int i = 0; i < 4; ++i) {
                const u32 dst = uu((ch % 3) * CS + (4 * i + w) * GS), so = uu((ch * 64 + 16 * i + 4 * w) * (NQ * 2));
                DMAIN(sq, dst, vo, so); DMAIN(sk, dst + 16 * GS, vo, so); DMAIN(sv, dst + 32 * GS, vo, so);
            }
        };
        fill(0); fill(1); fill(2);
        const int q4 = r >> 2;
        lptr tb = lds + (4 * (g >> 1) + 2 * (q4 >> 1) + (g & 1)) * GS + (q4 & 1) * 256 + ((w * 64 + (r & 3) * 8) ^ ((q4 & 1) << 7));
        SFOR(ch, 10)
            VMW(ch < 8 ? 24 : 108 - 12 * ch);
            BAR();
            auto frag = [&](int m, int t) { lptr b = tb + (ch % 3) * CS + m * 16 * GS + t * 32; return tr_frag(b, b + 512, b + 8 * GS, b + 8 * GS + 512); };
            wq0[ch] = frag(0, 0); wq1[ch] = frag(0, 1); wk0[ch] = frag(1, 0); wk1[ch] = frag(1, 1); wv0[ch] = frag(2, 0); wv1[ch] = frag(2, 1);
            asm volatile("" : "+a"(wq0[ch]), "+a"(wk0[ch]), "+v"(wv0[ch]), "+v"(wv1[ch]));
            if constexpr (qslot(ch) < 0) asm volatile("" : "+a"(wq1[ch])); else asm volatile("" : "+v"(wq1[ch]));
            if constexpr (kslot(ch) < 0) asm volatile("" : "+a"(wk1[ch])); else asm volatile("" : "+v"(wk1[ch]));
            BAR();
            if constexpr (ch < 7) fill(ch + 3);
        SEND
#pragma unroll
        for (int ch = 0; ch < 10; ++ch) {
            if (qslot(ch) >= 0) { lds_w16(WL + qslot(ch) * 2048, wq1[ch].lo); lds_w16(WL + qslot(ch) * 2048 + 1024, wq1[ch].hi); }
            if (kslot(ch) >= 0) { lds_w16(WL + kslot(ch) * 2048, wk1[ch].lo); lds_w16(WL + kslot(ch) * 2048 + 1024, wk1[ch].hi); }
            if (vslot(ch) >= 0) { lds_w16(WL + vslot(ch) * 2048, wv1[ch].lo); lds_w16(WL + vslot(ch) * 2048 + 1024, wv1[ch].hi); }
        }
        CFENCE();
    }
    const u32 idx = (r & 2) ? 0xEEEEu : 0x4444u;
    unsigned long long himask = 0xCCCCCCCCCCCCCCCCull;
    PINS(himask);
    bool dead = false;
    if (h == 3) LST(266 + L, rt32());
    if (w == 0) LST(1240 + L * 32 + h, rt32());
    if (L == 1) {
        KvIn in;
        kvt_load(p, KT0 + 4 * h + w, in, [] {}); kvt_make(rs, lds + w * 9216, KT0 + 4 * h + w, in);
        DRAIN(); BAR();
        pre_post(rs, 64 + h, 3u, ep);
    }
    const u32 pol0 = pol_wait(rs, ep);
    if (h == 3 && L == 0) LST(277, rt32());
    const u32 l16 = (u32)lane * 16u;
    const u32 mring = (u32)L * MB + (pol0 ^ 16384u), hint = mring + 4 * 65536u;
    const u32 fkh = lane < 3 ? foff(F_QKV + L * 96 + lane * 32 + h) : lane < 7 ? hint + (u32)(lane - 3) * 4u : OOB;
    u32 tm = 0;
    MK_SRD();
    u32 ksrc = uu(W_KQ + (u32)(L * (NSEG - 1) * NH + h) * 16384u + w * 2048), vsrc = ksrc + (W_VO - W_KQ);
    u32 dk = uu(C_K + w * 2048), dv = dk + (C_V - C_K), dq = dk + (C_QX - C_K), dm = uu(C_MEM + w * 2048), dm2 = uu(w < 2 ? C_MEM + 8192 + w * 1024 : C_DUMP);
    asm volatile("" : "+s"(dk), "+s"(dv), "+s"(dq), "+s"(dm), "+s"(dm2));
    auto keys_wait = [&](u32 need) {
#pragma unroll 1
        for (int n = 0; !dead; ++n) {
            const bool beat = need + 2 * L > 2u;
            if (beat) wait_until(tm);
            const u32 hv = ld4(rs, lane == 7 ? foff(F_ERR) : fkh);
            if (beat) { tm = rdl(hv, 5); wait_until(rdl(hv, 3) - 30u); }
            if (((u32)__builtin_amdgcn_ballot_w64(ld4(rs, fkh) < need) & 7u) == 0u) break;
            dead = n > SPIN_CAP || rdl(hv, 7) != 0u;
            st4s(rs, dead ? foff(F_ERR) : OOB, 0, 1u);
        }
        CFENCE();
        DMA32(dk, l16, ksrc); DMA32(dv, l16, vsrc); DMA32(dq, l16, ksrc ^ 8192u);
    };
    if (L == 0) { st4s(rs, tid == 0 ? hint + 4096u + (u32)h * 128u : OOB, 0, 0u); keys_wait(1u); }
    lptr qw = lds + C_Q + (2 * g) * 288 + (32 * w + 2 * r) * 2;
    lptr kw = qw + (C_KM - C_Q); PIN(kw);
    lptr qr = lds + C_Q + (2 * (r >> 2) + (r & 1)) * 288 + g * 16;
    lptr LB = lds + l16, VX = lds + C_V + w * 2048 + l16;
    PIN(VX);
    if (tid >= 64 && tid < 70) { const u32 z = tid == 68 ? 0x3F803F80u : tid == 69 ? PSENT : 0u; lds_w16(lds + C_ZERO + (tid - 64) * 16, (v4u){z, z, z, z}); }
    u32 zaddr = 0; PIN(zaddr);
    lptr ZP = lds + zaddr + C_ZERO;
    const float CEXP = SCALE * LOG2E;
#define LF_RD(slot, hf) lf[slot][hf] = lds_r16(WL + (slot) * 2048 + (hf) * 1024)
    BAR();
    const u32 ofl = tid == 0 ? foff(F_O + L * 32 + h) : OOB;
    u32 oso = (((g | L) & 1) ? OOB : 0u) + (u32)L * MB + (pol0 ^ oslot(-L)) + ring_map((u32)((r >> 3) * 32768 + h * 1024 + w * 256 + (g >> 1) * 128 + (r & 7) * 16));
    unsigned long long vis[8];
#pragma unroll
    for (int i = 0; i < 8; ++i) vis[i] = __builtin_amdgcn_ballot_w64(16 * (i >> 2) + 4 * g + (i & 3) <= 16 * (w & 1) + r);
    u32 missed = 0;
    for (int s = -L; s < NSEG;) {
        u32 sv = (u32)max(s, 0), ms = s > 0 ? mring + (sv & 3u) * 65536u : W_MEM0, ms2 = ms + 32768u + (w & 1) * 1024;
        ms += w * 2048;
        asm volatile("" : "+s"(ms), "+s"(ms2)); PIN(sv);
        f32x4 aq0, aq1, ak0, ak1;
        v4u lf[NLF][2];
        ZRD(aq0, 0); ZRD(aq1, 1); ZRD(ak0, 2); ZRD(ak1, 3);
#pragma unroll
        for (int i = 0; i < 4; ++i) { LF_RD(i, 0); LF_RD(i, 1); }
        SCHED();
        v4u A[10];
#define HT(k) do {} while (0)
        v4u kx[8], qf[4], kmf[4], vx[2], ones, P;
        float fa, fb, fc, fd, inv, sx[4], t[4], tm0, tm1; u32 pa, pb, fl, e01, e23, pmm;
        f32x4 av0, av1, s0, s1, sm, oa, ob, rsum;
        v2u vm, pm;
#define LW(sl) cat8(lf[sl][0], lf[sl][1])
#define PQ(ks, tl) do { if constexpr (!(tl)) SMF_A(aq0, A[ks], wq0[ks]); else if constexpr (qslot(ks) < 0) SMF_A(aq1, A[ks], wq1[ks]); else { const v8u lw_ = LW(qslot(ks)); SMF_V(aq1, A[ks], lw_); } } while (0)
#define PK(ks, tl) do { if constexpr (!(tl)) SMF_A(ak0, A[ks], wk0[ks]); else if constexpr (kslot(ks) < 0) SMF_A(ak1, A[ks], wk1[ks]); else { const v8u lw_ = LW(kslot(ks)); SMF_V(ak1, A[ks], lw_); } } while (0)
#define PV(ks, tl) do { if constexpr (!(tl)) SMF_V(av0, A[ks], wv0[ks]); else if constexpr (vslot(ks) < 0) SMF_V(av1, A[ks], wv1[ks]); else { const v8u lw_ = LW(vslot(ks)); SMF_V(av1, A[ks], lw_); } } while (0)
#define KXR(i) kx[i] = lds_r16(LB + C_K + ((i) & 1) * 4096 + ((i) >> 1) * 1024)
#define XW(q_) do { *(LAS u32*)(q_) = pa; *(LAS u32*)((q_) + 288) = pb; } while (0)
        auto slot = [&](auto zc) __attribute__((always_inline)) {
            constexpr int z = decltype(zc)::value;
            if constexpr (z < 14) PQ(z >> 1, z & 1);
            else if constexpr (z < 17) PQ(z - 7, 0);
            else if constexpr (z < 20) PQ(z - 10, 1);
            else if constexpr (z < 34) PK((z - 20) >> 1, z & 1);
            else if constexpr (z < 37) PK(z - 27, 0);
            else if constexpr (z < 40) PK(z - 30, 1);
            else if constexpr (z < 42) { if constexpr (z == 40) MF0(s0, kx[0], qf[0]); else MF0(s1, kx[1], qf[0]); }
            else if constexpr (z < 48) { if constexpr (z & 1) MF1(s1, kx[z - 40], qf[(z - 40) >> 1]); else MF1(s0, kx[z - 40], qf[(z - 40) >> 1]); }
            else if constexpr (z < 54) PV(z - 48, 0);
            else if constexpr (z == 54) MF0(sm, kmf[0], qf[0]);
            else if constexpr (z < 58) MF1(sm, kmf[z - 54], qf[z - 54]);
            else if constexpr (z < 62) PV(z - 52, 0);
            else if constexpr (z < 64) PV(z - 62, 1);
            else if constexpr (z == 64) MF0(oa, vx[0], P);
            else if constexpr (z == 65) MF0(ob, vx[1], P);
            else if constexpr (z == 66) MF0(rsum, ones, P);
            else if constexpr (z < 75) PV(z < 70 ? z - 63 : z == 70 ? 2 : z == 71 ? 7 : z == 72 ? 3 : z - 65, 1);
            else { const v2u ones2 = ones.lo; MF1H(rsum, ones2, pm); }
            if constexpr (z < 6) A[4 + z] = lds_r16(LB + C_MEM + (4 + z) * 1024);
            if constexpr (z >= 6 && z < 10) LF_RD(4 + ((z - 6) >> 1), z & 1);
            if constexpr (z >= 14 && z < 18) LF_RD(6 + ((z - 14) >> 1), z & 1);
            if constexpr (z >= 22 && z < 28) KXR(z - 22);
            if constexpr (z == 28) XW(qw);
            if constexpr (z == 29 || z == 30) KXR(z - 23);
            if constexpr (z == 32 || z == 47) asm volatile("s_barrier" ::: "memory");
            if constexpr (z >= 33 && z < 37) qf[z - 33] = lds_r16(qr + (z - 33) * 64);
            if constexpr (z == 20) ZRD(av0, 0);
            if constexpr (z == 43) XW(kw);
            if constexpr (z == 44 || z == 45) vx[z - 44] = lds_r16(VX + (z - 44) * 1024);
            if constexpr (z >= 48 && z < 52) kmf[z - 48] = lds_r16(qr + (C_KM - C_Q) + (z - 48) * 64);
            if constexpr (z == 52) ZRD(av1, 1);
            if constexpr (z == 37) ones = lds_r16(ZP + 64);
            if constexpr (z >= 57 && z < 64) LF_RD(8 + ((z - 57) >> 1), (z - 57) & 1);
            if constexpr (z >= 65 && z < 70) LF_RD(8 + ((z - 58) >> 1), (z - 58) & 1);
            if constexpr (z == HD_SMP) LD4A(fl, fkh, 0);
            SCHED();
            if constexpr (z == 19) HT(2);
            if constexpr (z == 32) HT(3);
            if constexpr (z == 47) HT(4);
            if constexpr (z == 64) HT(5);
            if constexpr (z == 19) LGK(4);
            if constexpr (z == 21 || z == 41) LGK(1);
            if constexpr (z == 56 || z == 73) LGK(0);
            if constexpr (z == 31 || z == 46) LGK(2);
            if constexpr (z == 39) LGK(4);
            if constexpr (z >= 53 && z < 56) LGK(57 - z);
            if constexpr (z == 64) LGK(3);
            if constexpr (z == 68) LGK(5);
            if constexpr (z >= 70 && z < 73) LGK(74 - z);
            SCHED();
            if constexpr (z == 18) CADD(fa, aq0[0], aq0[2]);
            if constexpr (z == 19) CADD(fb, aq0[1], aq0[3]);
            if constexpr (z == 20) CMULC(fa, fa);
            if constexpr (z == 21) CADD(fc, aq1[0], aq1[2]);
            if constexpr (z == 22) CADD(fd, aq1[1], aq1[3]);
            if constexpr (z == 23) CMULC(fc, fc);
            if constexpr (z == 24) CMULC(fb, fb);
            if constexpr (z == 25) CMULC(fd, fd);
            if constexpr (z == 26) CCVT(pa, fa, fc);
            if constexpr (z == 27) CCVT(pb, fb, fd);
            if constexpr (z == 38) CADD(fa, ak0[0], ak0[2]);
            if constexpr (z == 39) CADD(fb, ak0[1], ak0[3]);
            if constexpr (z == 41) { CADD(fc, ak1[0], ak1[2]); CADD(fd, ak1[1], ak1[3]); }
            if constexpr (z == 42) { CCVT(pa, fa, fc); CCVT(pb, fb, fd); }
            if constexpr (z >= 49 && z < 53) SEL(sx[z - 49], s0[z - 49], s1[z - 49]);
            if constexpr (z >= 53 && z < 57) VEXP(t[z - 53], sx[z - 53]);
            if constexpr (z == 57) CCVT(e01, t[0], t[1]);
            if constexpr (z == 58) CCVT(e23, t[2], t[3]);
            if constexpr (z == 59) QPERM(P.x, e01, "[0,1,0,1]");
            if constexpr (z == 60) QPERM(P.z, e01, "[2,3,2,3]");
            if constexpr (z == 61) QPERM(P.y, e23, "[0,1,0,1]");
            if constexpr (z == 62) QPERM(P.w, e23, "[2,3,2,3]");
            if constexpr (z == 63) CADD(fa, av0[0], av0[2]);
            if constexpr (z == 64) { asm volatile("" :: "v"(sm)); VEXP(tm0, sm[0]); }
            if constexpr (z == 65) VEXP(tm1, sm[1]);
            if constexpr (z == 66) CADD(fb, av0[1], av0[3]);
            if constexpr (z == 67) CCVT(pmm, tm0, tm1);
            if constexpr (z >= 68 && z < 72) SEL(oa[z - 68], oa[z - 68], ob[z - 68]);
            if constexpr (z == 72) asm volatile("v_cndmask_b32 %0, %1, 0, %2" : "=v"(pm.x) : "v"(pmm), "s"(himask));
            if constexpr (z == 73) asm volatile("v_cndmask_b32 %0, 0, %1, %2" : "=v"(pm.y) : "v"(pmm), "s"(himask));
            if constexpr (z == 74) LGK(15);
            if constexpr (z == 75) CCVT(vm.x, fa, fb);
            SCHED();
        };
        {
            u32 tmp;
            asm volatile("s_mov_b32 m0, %2\n\t" WAIT_TXT("%0", "%1") "buffer_load_dwordx4 %4, %5, %6 offen sc1 lds\n\tbuffer_load_dwordx4 %4, %5, %6 offen offset:1024 sc1 lds\n\t"
                         "s_mov_b32 m0, %3\n\ts_nop 0\n\tbuffer_load_dwordx4 %4, %5, %7 offen sc1 lds\n\t.p2align 6\n\t.rept %8\n\ts_nop 0\n\t.endr\n\ts_waitcnt vmcnt(0) lgkmcnt(0)\n\ts_barrier"
                         : "=&s"(tmp) : "s"(uu(tm - 4u)), "s"(dm), "s"(dm2), "v"(l16), "s"(srd), "s"(ms), "s"(ms2), "i"(AL_H) : "vcc", "scc", "memory");
        }
        HT(1);
#pragma unroll
        for (int ks = 0; ks < 4; ++ks) A[ks] = lds_r16(LB + C_MEM + ks * 1024);
        SCHED();
        st4s(rs, ofl, 0, sv);
        SCHED();
        static_for<0, 76>(slot);
        asm volatile("s_nop 2\n\tv_add_f32 %1, %3, %5\n\tv_add_f32 %2, %4, %6\n\tv_cvt_pk_bf16_f32 %0, %1, %2"
                     : "=v"(vm.y), "=&v"(fc), "=&v"(fd) : "v"(av1[0]), "v"(av1[1]), "v"(av1[2]), "v"(av1[3]));
        __builtin_amdgcn_s_waitcnt(0xCF7F);
        asm volatile("s_nop 0\n\tv_mfma_f32_16x16x16_bf16 %0, %2, %3, %0\n\tv_rcp_f32 %1, %4" : "+v"(oa), "=&v"(inv) : "v"(vm), "v"(pm), "v"(rsum[0]), "v"(rsum));
        SCHED();
        if (__builtin_expect(__builtin_amdgcn_ballot_w64(u_(rsum[0]) - 0x21800000u > 0x3C000000u) != 0ull, 0)) {
            DRAIN(); SCHED();
            bool miss = false;
#pragma unroll 1
            for (int i = 0; i < 10; ++i) { const v4u a = lds_r16(LB + C_MEM + i * 1024); miss |= a.x == PSENT || a.y == PSENT || a.z == PSENT || a.w == PSENT; }
            if (missed < SPIN_CAP && !all_(!miss)) {
                *(LAS u32*)(lds + C_DUMP + (w == 0 ? 0 : 64)) = ld4(rs, foff(F_ERR));
                asm volatile("" : "+v"(fl) :: "memory");
                tm = rdl(fl, 5);
                BAR();
                missed = uu(*(LAS u32*)(lds + C_DUMP)) != 0u ? SPIN_CAP : missed + 1;
                st4s(rs, missed == SPIN_CAP ? foff(F_ERR) : OOB, 0, 1u);
                BAR();
                continue;
            }
            float mx = fmaxf(fmaxf(fmaxf(sx[0], sx[1]), fmaxf(sx[2], sx[3])), fmaxf(sm[0], sm[1]));
            mx = max32(max16(fmaxf(mx, dpp<0x4E>(mx))));
#pragma unroll
            for (int i = 0; i < 4; ++i) t[i] = __builtin_amdgcn_exp2f(sx[i] - mx);
            e01 = pk(t[0], t[1]); e23 = pk(t[2], t[3]);
            pmm = pk(__builtin_amdgcn_exp2f(sm[0] - mx), __builtin_amdgcn_exp2f(sm[1] - mx));
            asm volatile("v_cndmask_b32 %0, %2, 0, %3\n\tv_cndmask_b32 %1, 0, %2, %3" : "=&v"(pm.x), "=&v"(pm.y) : "v"(pmm), "s"(himask));
            asm volatile("s_nop 1" : "+v"(e01), "+v"(e23), "+v"(pm));
            QPERM(P.x, e01, "[0,1,0,1]"); QPERM(P.z, e01, "[2,3,2,3]"); QPERM(P.y, e23, "[0,1,0,1]"); QPERM(P.w, e23, "[2,3,2,3]");
            NOPS(1);
            MF0(rsum, ones, P); MF0(oa, vx[0], P); MF0(ob, vx[1], P);
            NOPS(7);
            { const v2u ones2 = ones.lo; MF1H(rsum, ones2, pm); }
#pragma unroll
            for (int i = 0; i < 4; ++i) SEL(oa[i], oa[i], ob[i]);
            NOPS(1);
            MF1H(oa, vm, pm);
            asm volatile("s_nop 7\n\tv_rcp_f32 %0, %1" : "=v"(inv) : "v"(rsum[0]), "v"(rsum));
        }
        {
            v4u oc; f32x2 u0 = oa.lo, u1 = oa.hi; const f32x2 i2 = {inv, inv};
            asm volatile("s_nop 0\n\tv_pk_mul_f32 %0, %0, %2\n\tv_pk_mul_f32 %1, %1, %2" : "+v"(u0), "+v"(u1) : "v"(i2));
            __builtin_amdgcn_s_waitcnt(0xCF7F);
            asm volatile("v_cvt_pk_bf16_f32 %0, %4, %5\n\tv_cvt_pk_bf16_f32 %2, %4, %5\n\tv_cvt_pk_bf16_f32 %1, %6, %7\n\tv_cvt_pk_bf16_f32 %3, %6, %7\n\t"
                         "v_permlane16_swap_b32 %0, %2\n\ts_nop 0\n\tv_permlane16_swap_b32 %1, %3"
                         : "=&v"(oc.x), "=&v"(oc.y), "=&v"(oc.z), "=&v"(oc.w) : "v"(u0.x), "v"(u0.y), "v"(u1.x), "v"(u1.y));
            st16(rs, oso, oc);
        }
        SCHED();
        unsigned long long clk;
        asm volatile("s_memrealtime %0" : "=s"(clk) :: "memory");
        HT(6);
        v4u tq[4], tk[8], tv[2];
        f32x4 c0, c1, cm;
        u32 oo, kvo; bool kvok;
        u32 ln = l16; PIN(ln);
        lptr QX = lds + C_QX + (w & 1) * 4096 + ln, VO = lds + C_V + (w ^ 1) * 2048 + ln, VMX = lds + C_VM + w * 512 + (ln >> 1);
#pragma unroll
        for (int i = 0; i < 2; ++i) { tq[i] = lds_r16(QX + i * 1024); kmf[i] = lds_r16(qr + (C_KM - C_Q) + i * 64); tk[2 * i] = lds_r16(LB + C_K + i * 1024); tk[2 * i + 1] = lds_r16(LB + C_K + 4096 + i * 1024); }
        *(LAS v2u*)VMX = vm;
        SCHED();
        SFOR(i, 12)
            if constexpr (i == 0) MF0(cm, kmf[0], tq[0]); else if constexpr (i == 1) MF0(c0, tk[0], tq[0]); else if constexpr (i == 2) MF0(c1, tk[1], tq[0]);
            else if constexpr (i % 3 == 0) MF1(cm, kmf[i / 3], tq[i / 3]); else if constexpr (i % 3 == 1) MF1(c0, tk[2 * (i / 3)], tq[i / 3]); else MF1(c1, tk[2 * (i / 3) + 1], tq[i / 3]);
            if constexpr (i < 2) kmf[2 + i] = lds_r16(qr + (C_KM - C_Q) + (2 + i) * 64);
            if constexpr (i == 0) { tq[2] = lds_r16(QX + 2048); tk[4] = lds_r16(LB + C_K + 2048); }
            if constexpr (i == 1) { tk[5] = lds_r16(LB + C_K + 6144); tq[3] = lds_r16(QX + 3072); }
            if constexpr (i == 2) { tk[6] = lds_r16(LB + C_K + 3072); tk[7] = lds_r16(LB + C_K + 7168); }
            if constexpr (i == 3) { tv[0] = lds_r16(VO); tv[1] = lds_r16(VO + 1024); }
            if constexpr (i == 4) { oo = ln + (ln & 0x300u) + ((vsrc ^ 8192u) + (u32)((w & 1) * 256) + ((u32)s & OOB)); PIN(oo); }
            if constexpr (i == 5) { ksrc += NH * 16384; vsrc += NH * 16384; PINS(ksrc); PINS(vsrc); }
            if constexpr (i == 9) { asm volatile("s_waitcnt vmcnt(1)" : "+v"(fl) :: "memory"); kvok = ((u32)__builtin_amdgcn_ballot_w64(fl < (u32)s + 2) & 7u) == 0u; }
            if constexpr (i == 10) { kvo = l16 | (kvok ? 0u : OOB); PIN(kvo); }
            SCHED();
        SEND
        asm volatile("s_waitcnt lgkmcnt(0)\n\ts_barrier" : "+s"(clk) :: "memory");
        const v2u tvm = *(LAS v2u*)(VMX + (512 - (w & 1) * 1024));
        const v4u pv = lds_r16(ZP + 80);
        DMA32(dk, kvo, ksrc);
        SCHED(); asm volatile("" :: "v"(cm));
        float u[10], m, m2;
        {
            float NEG = -1000000000.0f / SCALE; PIN(NEG);
#define MSK(d, a, i) asm volatile("v_cndmask_b32 %0, %1, %2, %3" : "=v"(d) : "v"(NEG), "v"(a), "s"(vis[i]))
#pragma unroll
            for (int i = 0; i < 4; ++i) MSK(u[i], c0[i], i);
#pragma unroll
            for (int i = 0; i < 4; ++i) MSK(u[4 + i], c1[i], 4 + i);
            asm volatile("v_max3_f32 %0, %2, %3, %10\n\tv_max3_f32 %1, %4, %5, %11\n\tv_max3_f32 %0, %0, %6, %7\n\tv_max3_f32 %1, %1, %8, %9\n\t"
                         "v_max_f32 %0, %0, %1\n\tv_mov_b32 %1, %0\n\ts_nop 1\n\tv_permlane16_swap_b32 %0, %1\n\tv_max_f32 %0, %0, %1\n\tv_mov_b32 %1, %0\n\ts_nop 1\n\t"
                         "v_permlane32_swap_b32 %0, %1\n\tv_max_f32 %0, %0, %1\n\tv_mul_f32 %0, 0xbe0293ee, %0"
                         : "=&v"(m), "=&v"(m2) : "v"(u[0]), "v"(u[1]), "v"(u[2]), "v"(u[3]), "v"(u[4]), "v"(u[5]), "v"(u[6]), "v"(u[7]), "v"(cm[0]), "v"(cm[1]));
        }
        DMA32(dv, kvo, vsrc);
        {
            v4u px; v2u p0, p1;
#define EX2(d, a, b) asm volatile("v_fmamk_f32 %0, %2, 0x3e0293ee, %4\n\tv_fmamk_f32 %1, %3, 0x3e0293ee, %4\n\tv_exp_f32 %0, %0\n\tv_exp_f32 %1, %1" : "=&v"(d.x), "=&v"(d.y) : "v"(a), "v"(b), "v"(m))
            f32x2 ex[5];
            EX2(ex[0], u[0], u[1]); EX2(ex[1], u[2], u[3]); EX2(ex[2], u[4], u[5]); EX2(ex[3], u[6], u[7]); EX2(ex[4], cm[0], cm[1]);
            asm volatile("v_cvt_pk_bf16_f32 %0, %8, %9\n\tv_cvt_pk_bf16_f32 %1, %10, %11\n\tv_cvt_pk_bf16_f32 %2, %12, %13\n\tv_cvt_pk_bf16_f32 %3, %14, %15\n\t"
                         "v_mov_b32 %5, 0\n\tv_mov_b32 %6, 0\n\tv_cvt_pk_bf16_f32 %4, %16, %17\n\tv_cvt_pk_bf16_f32 %7, %16, %17"
                         : "=&v"(px.x), "=&v"(px.y), "=&v"(px.z), "=&v"(px.w), "=&v"(p0.x), "=&v"(p0.y), "=&v"(p1.x), "=&v"(p1.y)
                         : "v"(ex[0].x), "v"(ex[0].y), "v"(ex[1].x), "v"(ex[1].y), "v"(ex[2].x), "v"(ex[2].y), "v"(ex[3].x), "v"(ex[3].y), "v"(ex[4].x), "v"(ex[4].y));
            f32x4 o[4], sum;
            const v2u ones2 = ones.lo;
            const u32 now = (u32)clk;
            u32 oz;
            SCHED();
            MF0(sum, ones, px); { const u32 q = ksrc ^ 8192u; DMA32(dq, kvo, q); } SCHED();
            MF0(o[0], vx[0], px);
            MF0(o[1], vx[1], px);
            MF0(o[2], tv[0], px); oz = oso ^ (oslot(s) ^ oslot(s + 3)); PIN(oz); SCHED();
            MF0(o[3], tv[1], px); oso ^= oslot(s) ^ oslot(s + 1); PIN(oso); SCHED();
            MF1H(sum, ones2, p0);
            MF1H(o[0], vm, p0);
            MF1H(o[1], vm, p1); st16(rs, oz, pv); SCHED();
            MF1H(o[2], tvm, p0);
            MF1H(o[3], tvm, p1);
            asm volatile("s_nop 2\n\tv_rcp_f32 %0, %1\n\ts_nop 0" : "=v"(inv) : "v"(sum[0]), "v"(sum));
            asm volatile("" : "+v"(o[0]), "+v"(o[1]));
            st16(rs, oo, (v4u){pk2(o[0].lo * inv), pk2(o[0].hi * inv), pk2(o[1].lo * inv), pk2(o[1].hi * inv)});
            asm volatile("" : "+v"(o[2]), "+v"(o[3]));
            st16(rs, oo ^ 2048u, (v4u){pk2(o[2].lo * inv), pk2(o[2].hi * inv), pk2(o[3].lo * inv), pk2(o[3].hi * inv)});
            SCHED();
            LS2(L == 0 && w == 0, h);
            st4s(rs, ((h & 15) == 0 && tid == 192) ? hint + 128u + (u32)h * 8u : OOB, 0, (u32)s << 16 | min(missed, 1u) << 14 | min(now - tm, 0x3FFFu));
            tm = rdl(fl, 5);
            missed = missed == SPIN_CAP ? missed : 0u;
            if (__builtin_expect(s < 1 - L, 0)) {
                if (s < 0) oso -= (ln & 256u) ? 0u : OOB;
#pragma unroll 1
                for (int n = 0; n < (s < 0 ? 0 : 200); ++n) {
                    CFENCE();
                    tm = rdl(ld4(rs, fkh), 6);
                    if (tm != PSENT) break;
                }
            }
        }
        if (__builtin_expect(!kvok, 0) && s + 1 < NSEG) {
            st4s(rs, ln == 0 ? hint + 4096u + (u32)h * 128u : OOB, 0, (u32)s + 1u);
            keys_wait((u32)s + 2u);
        }
        ++s;
    }
    DRAIN(); BAR();
    st4s(rs, ofl, 0, NSEG);
    LDUMP(w == 0, 160 + L * 32 + h);
}

#define WO_NL 3
#define WO_ROT w
#define WO_AH 1
DEV void role_wo(const Params& p, lptr lds, int j, v2u ep) {
    const int tid = threadIdx.x, lane = tid & 63, w = uni(tid >> 6), g = lane >> 4, r = lane & 15;
    const int rg = j & 1, cg = j >> 1, rot = WO_ROT;
    const rsrc_t rs = mk_rsrc(p.ws, W_END);
    v8u wo[8][5];
    {
        const v4u so_ = mk_srd(p.Wo, DM * NQ * 2);
        u32 vo[5];
#pragma unroll
        for (int i = 0; i < 5; ++i) { const int n = 64 * i + lane; vo[i] = (u32)((n / 5) * DM + cg * 40 + (n % 5) * 8) * 2u; }
        v4u junk = {};
        const u32 av = (u32)(j * 5120 + lane * 16);
        auto ahead = [&](int b) {
#pragma unroll
            for (int i = 0; i < 5; ++i) AHEAD(so_, av, uu((8 * w + ((b + rot) & 7)) * (DH * DM * 2) + i * 1024));
        };
        auto fill = [&](int b) {
            if (b + WO_AH < 8) ahead(b + WO_AH);
            const u32 dst = uu(w * 40960 + (b & 3) * 10240), src = uu((8 * w + ((b + rot) & 7)) * (DH * DM * 2));
#pragma unroll
            for (int i = 0; i < 10; ++i) DMAIN(so_, dst + i * 1024, vo[i % 5], src + (i / 5) * (64 * DM * 2));
        };
#pragma unroll 1
        for (int b = 0; b < WO_AH; ++b) ahead(b);
#pragma unroll 1
        for (int b = 0; b < 4; ++b) fill(b);
        const int q4 = r >> 2;
        lptr tb = lds + w * 40960 + (32 * (g >> 1) + 16 * (q4 >> 1) + 8 * ((r >> 1) & 1) + 4 * (g & 1) + (q4 & 1)) * 80 + (r & 1) * 8;
        SFOR(b, 8)
            VMW((b < 5 ? 30 : 70 - 10 * b) + (b + 1 + WO_AH < 8 ? 5 : 0) + (b + 2 + WO_AH < 8 ? 5 : 0) + (b + 3 + WO_AH < 8 ? 5 : 0));
            if constexpr (b + WO_AH == 7) PIN(junk);
#pragma unroll
            for (int t = 0; t < 5; ++t) {
                lptr q = tb + (b & 3) * 10240 + t * 16;
                wo[b][t] = tr_frag(q, q + 160, q + 64 * 80, q + 64 * 80 + 160);
                if (b * 5 + t < 32) asm volatile("" : "+a"(wo[b][t])); else asm volatile("" : "+v"(wo[b][t]));
            }
            CFENCE();
            if constexpr (b < 4) fill(b + 4);
        SEND
    }
    if (j == 0) LST(265, rt32());
    LST(1100 + j * 4 + w, rt32());
    const u32 idx = (r & 2) ? 0xEEEEu : 0x4444u;
    const u32 pol0 = pol_wait(rs, ep);
    BAR();
    const u32 l16 = (u32)lane * 16u;
    u32 oo[8];
#pragma unroll
    for (int i = 0; i < 8; ++i) oo[i] = uu(ring_map((u32)(rg * 32768 + (8 * w + ((i + rot) & 7)) * 1024)));
    const u32 hint = (pol0 ^ 16384u) + 4 * 65536u;
    u32 hvo[2];
#pragma unroll
    for (int l = 0; l < 2; ++l) hvo[l] = lane < 2 ? hint + (u32)(l ^ 1) * MB + (u32)lane * 4u : lane == 2 ? hint + (u32)l * MB + 128u : lane == 3 ? foff(F_ERR) : lane == 4 ? hint + (u32)l * MB + 256u :
                 (lane >= 32 && j == 0 && w == 3) ? hint + (u32)l * MB + (u32)lane * 128u : OOB;
    lptr xw = r < 8 ? lds + w * 640 + (2 * (g & 1) + (g >> 1)) * 32 + r * 4 : lds + 8192 + lane * 4;
    const int G = ((5 * cg) >> 1) + w, tt = 2 * G + ((lane >> 3) & 1) - 5 * cg;
    const bool pub = w < 3 && lane < 32 && tt >= 0 && tt < 5;
    lptr xr = lds + (pub ? tt * 128 + (2 * (lane >> 4) + ((lane >> 2) & 1)) * 32 + (lane & 3) * 8 : 0);
    const u32 mpo = pub ? (pol0 ^ 16384u) + ring_map((u32)(G * 256 + rg * 128 + lane * 4)) : OOB;
    constexpr u32 PER0 = CH_W0 + CH_MW + CH_H0 + CH_MH + 2 * CH_VIS, DW0 = CH_W0 + CH_VIS + CH_MW + CH_REC;
    const bool lead = j == 0 && w == 3;
    const u32 hso = (lead && lane == 0) ? hint : OOB;
    MK_SRD();
    u32 t = 0, tn = 0;
    int sn[2] = {0, 0};
    u32 hav = 8 * CH_H0, wav = 8 * CH_W0, mh = 8 * CH_MH, mw = 8 * CH_MW, ok[2] = {0u, 0u}, dw[2] = {DW0, DW0};
    u32 vo = l16 + pol0, tp = 0, fin, left, cnt = 0;
    unsigned long long clk = 0;
    {
        if (w == 0) LST(1310 + j, rt32());
        if (lead) {
#pragma unroll 1
            for (int n = 0; n < SPIN_CAP; ++n) {
                CFENCE();
                const u32 a = ld4(rs, foff(F_QKV + lane)), b = ld4(rs, lane < 32 ? foff(F_QKV + 64 + lane) : hvo[0]);
                if (all_(a != 0u && b != (lane < 32 ? 0u : PSENT))) break;
                __builtin_amdgcn_s_sleep(1);
            }
        }
        if (lead) {
            t = rt32() + CH_T0; tn = t + PER0 / 2 + CH_REC;
            st16(rs, lane < 2 ? hint + (u32)lane * MB : OOB, (v4u){lane == 0 ? t : tn, PER0, (lane == 0 ? t : tn) + DW0, t + DW0});
        } else {
#pragma unroll 1
            for (int n = 0; n < SPIN_CAP; ++n) {
                CFENCE();
                const u32 hv = ld4(rs, hvo[1]);
                t = rdl(hv, 0);
                if (rdl(hv, 1) < 400u) break;
                __builtin_amdgcn_s_sleep(1);
            }
        }
        clk = t - 100u;
    }
    const u32 tm0 = t + DW0;
    u32 zero = 0, hv = 0xFFFFu, pst = 1, pmp = OOB;
    const u32 psent = PSENT;
    lptr sw = lds + 2560 + w * 4;
    asm volatile("" : "+v"(zero), "+v"(sw));
    auto turn = [&](auto lc) __attribute__((always_inline)) {
        constexpr int L = decltype(lc)::value, M = L ^ 1;
        u32 done, rep, rd, lost, dh, dn, nt, za, pdur, mp; v4u hw;
        const int sc = sn[L];
        auto books = [&](auto ic) __attribute__((always_inline)) {
            constexpr int i = decltype(ic)::value;
            if constexpr (i == 0) { rep = max(rdl(hv, 2), rdl(hv, 4)); PINS(rep); }
            if constexpr (i == 1) { lost = (u32)(__builtin_amdgcn_ballot_w64(hv == (u32)sn[M]) >> 32); PINS(lost); }
            if constexpr (i == 2) { lost |= (rep >> 16) ^ (u32)sn[M]; PINS(lost); }
            if constexpr (i == 3) { lost |= ok[M] ^ 2u; rd = rep & 0xFFFFu; PINS(lost); PINS(rd); }
            if constexpr (i == 4) { lost = lost == 0u ? CH_UP * 8u : 0u; PINS(lost); }
            if constexpr (i == 5) { pdur = ((u32)clk - tp) * 8u; PINS(pdur); pdur = min(pdur, wav + CH_RISE); PINS(pdur); }
            if constexpr (i == 6) { pdur = (done & ok[M]) ? pdur : wav; PINS(pdur); }
            if constexpr (i == 7) { wav = max(pdur, wav - CH_FALL); PINS(wav); }
            if constexpr (i == 8) { nt = min(rd * 8u, hav + CH_RISE); PINS(nt); }
            if constexpr (i == 9) { nt = rd < 0x3FFFu ? nt : hav; PINS(nt); }
            if constexpr (i == 10) { hav = max(nt, hav - CH_FALL); PINS(hav); }
            if constexpr (i == 11) { mw = max(mw - 1u, CH_MW * 8u); PINS(mw); }
            if constexpr (i == 12) { mh = max(mh - 1u, CH_MH * 8u); PINS(mh); }
            if constexpr (i == 13) { rd = rd >> 14 == 1u ? lost : 0u; PINS(rd); }
            if constexpr (i == 14) { mw += rd; mh += lost - rd; PINS(mw); PINS(mh); }
            if constexpr (i == 15) { ok[M] = done ? 1u : (ok[M] & 1u) << 1; PINS(ok[M]); }
            if constexpr (i == 16) { dh = (hav + mh) / 8u + (sn[1] < 3 ? CH_VIS + CH_PRE : CH_VIS); PINS(dh); }
            if constexpr (i == 17) { dn = (wav + mw) / 8u + (sn[1] < 3 ? CH_VIS + CH_PRW : CH_VIS); PINS(dn); }
            if constexpr (i == 18) { nt = dw[L] + dh + (u32)max((int)((u32)clk + CH_WX - t), 0); PINS(nt); }
            if constexpr (i == 19) { dh += dn; dw[L] = dn; sn[M] += (int)done; PINS(dh); PINS(sn[M]); }
            if constexpr (i == 20) { rd = tn - t + (dh >> 1); PINS(rd); }
            if constexpr (i == 21) { lost = (u32)sc - 1u < NSEG - 2 ? CH_RTY : 0u; PINS(lost); nt += ok[L] != 1u ? lost : 0u; PINS(nt); }
            if constexpr (i == 22) { nt = t + (u32)max((int)nt, (int)rd); PINS(nt); }
            if constexpr (i == 23) { hw.x = nt; hw.y = dh; PIN(hw.x); PIN(hw.y); }
            if constexpr (i == 24) { hw.z = nt + dn; hw.w = tm0; PIN(hw); }
            if constexpr (i == 25) st16(rs, hso + L * MB, hw);
        };
        {
#define WT(k) do {} while (0)
            v4u d[8];
            f32x4 acc[5] = {};
            done = uu(pst) ^ 1u; PINS(done);
            za = pmp | pst << 31;
            *(LAS u32*)(sw + L * 4096) = vo >> 31;
            mp = mpo + (L * MB + (u32)((sc + 1) & 3) * 65536u); pmp = mpo + (L * MB + (u32)(sc & 3) * 65536u);
            asm volatile("" : "+v"(acc[0]), "+v"(acc[1]), "+v"(acc[2]), "+v"(acc[3]), "+v"(acc[4]), "+v"(mp), "+v"(pmp), "+v"(za));
            {
                u32 tmp;
                asm volatile(WAIT_TXT("%1", "%2") "buffer_load_dwordx4 %0, %3, %4, %5 offen sc1\n\ts_waitcnt lgkmcnt(0)"
                             : "=&v"(d[0]), "=&s"(tmp) : "s"(uu(t - 4u)), "v"(vo), "s"(srd), "s"(oo[0]) : "vcc", "scc", "memory");
                PINS(clk);
                WT(0);
            }
            SFOR(i, 7)
                asm volatile("buffer_load_dwordx4 %0, %1, %2, %3 offen sc1" : "=v"(d[i + 1]) : "v"(vo), "s"(srd), "s"(oo[i + 1]) : "memory");
                SCHED();
                static_for<(25 * i) / 7, (25 * (i + 1)) / 7>(books);
                SCHED();
            SEND
            WT(1);
            float f0[5], f1[5]; unsigned long long bad;
#define WSW(o_) asm volatile("v_permlane32_swap_b32 %0, %1" : "+v"(f0[o_]), "+v"(f1[o_]))
#define WAD(o_) asm volatile("v_add_f32_dpp %0, %1, %0 row_ror:8 row_mask:0xf bank_mask:0xf" : "+v"(f0[o_]) : "v"(f1[o_]))
#define WFO(o_) do { VADD(f0[o_], acc[o_][0], acc[o_][2]); VADD(f1[o_], acc[o_][1], acc[o_][3]); } while (0)
#define WWR(o_) *(LAS float*)(xw + L * 4096 + (o_) * 128) = f0[o_]
            u32 th, per, so;
            auto next = [&](auto ic_) __attribute__((always_inline)) {
                constexpr int i = decltype(ic_)::value;
                if constexpr (i == 0) { so = (u32)(sn[M] & 3) * 0x42000u; PINS(so); }
                if constexpr (i == 1) { so = (so & 0xC4000u) ^ (pol0 + M * MB); PINS(so); }
                if constexpr (i == 2) { so = sn[M] + 1 < NSEG ? so : OOB; PINS(so); }
                if constexpr (i == 4) { vo = l16 + so; PIN(vo); }
                if constexpr (i == 5 && L) { fin = (u32)(sn[0] + sn[1] + 2); PINS(fin); }
                if constexpr (i == 6 && L) { fin = (fin | uu(left)) >> 7; PIN(fin); }
                if constexpr (i == 6 && !L) { cnt += 1u; PIN(cnt); left = cnt >> 9; PIN(left); }
                if constexpr (i == 7) { PIN(hv); th = rdl(hv, 0); PINS(th); }
                if constexpr (i == 8) { per = rdl(hv, 1); PINS(per); so = th - t; PINS(so); }
                if constexpr (i == 9) { per = (int)so < 20 ? per : 0u; PINS(per); }
                if constexpr (i == 10) { th += per; PINS(th); if constexpr (L) { per = rdl(hv, 3); PINS(per); } }
                if constexpr (i == 11) { tp = t; t = lead ? tn : th; PINS(t); }
                if constexpr (i == 12) { if constexpr (L) { fin |= per; PIN(fin); } tn = nt; }
            };
            auto slot = [&](auto ic_) __attribute__((always_inline)) {
                constexpr int i = decltype(ic_)::value;
                constexpr int NL = WO_NL, I0 = 40 - 5 * NL, pc = i < I0 ? i / 5 : 8 - NL + (i - I0) % NL, oc = i < I0 ? i % 5 : (i - I0) / NL;
                f32x4& ac = acc[oc]; v4u& dp = d[pc]; v8u& wp = wo[pc][oc];
                if constexpr (oc == 0) {
                    if constexpr (pc * 5 < 32) asm volatile("s_waitcnt vmcnt(%4)\n\tv_smfmac_f32_16x16x64_bf16 %0, %1, %2, %3" : "+v"(ac), "+v"(dp) : "a"(wp), "v"(idx), "i"(pc < 2 ? 7 - pc : 9 - pc));
                    else asm volatile("s_waitcnt vmcnt(%4)\n\tv_smfmac_f32_16x16x64_bf16 %0, %1, %2, %3" : "+v"(ac), "+v"(dp) : "v"(wp), "v"(idx), "i"(9 - pc));
                } else if constexpr (pc * 5 + oc < 32) SMF_A(ac, dp, wp); else SMF_V(ac, dp, wp);
                SCHED();
                if constexpr (i == 0) WT(2);
                if constexpr (i < 25 && oc == 0) LGK(15);
                if constexpr (i == 6) { u32& h_ = hv; (void)&srd; LD4A(h_, hvo[L], 0); }
                if constexpr (i == 8) books(IC<25>{});
                if constexpr (i >= 11 && i < 19 && i != 15) next(IC<i - 11 - (i > 15)>{});
                if constexpr (i == 20) WT(4);
                constexpr int e = i - (I0 + NL + 1), o = e < 0 ? 0 : e / 3;
                if constexpr (e >= 0 && e % 3 == 0) { CADD(f0[o], acc[o][0], acc[o][2]); if constexpr (o > 0) WAD(o - 1); }
                if constexpr (e >= 0 && e % 3 == 1) { if constexpr (o > 0) { WWR(o - 1); SCHED(); } CADD(f1[o], acc[o][1], acc[o][3]); }
                if constexpr (e >= 0 && e % 3 == 2) {
                    if constexpr (o == 0) asm volatile("v_cmp_u_f32 %0, %1, %2" : "=s"(bad) : "v"(f0[0]), "v"(f1[0]));
                    else LGK(15);
                    WSW(o);
                }
                SCHED();
            };
            WT(3);
            static_for<0, 40>(slot);
            WT(5);
            static_assert(WO_NL == 3, "tail");
            if (__builtin_expect(bad != 0ull, 0)) {
                bool miss = false;
#pragma unroll
                for (int i = 0; i < 8; ++i) miss |= d[i].x == PSENT;
                if (all_(!miss)) {
#pragma unroll
                    for (int i = 0; i < 8; ++i) miss |= d[i].y == PSENT || d[i].z == PSENT || d[i].w == PSENT;
                }
                if (miss) *(LAS u32*)(sw + L * 4096) = 1u;
            }
            asm volatile("v_permlane32_swap_b32 %0, %1\n\ts_nop 1\n\tv_add_f32_dpp %0, %1, %0 row_ror:8 row_mask:0xf bank_mask:0xf\n\tv_add_f32 %2, %4, %6\n\tv_add_f32 %3, %5, %7"
                         : "+v"(f0[3]), "+v"(f1[3]), "=&v"(f0[4]), "=&v"(f1[4]) : "v"(acc[4][0]), "v"(acc[4][1]), "v"(acc[4][2]), "v"(acc[4][3]));
            WWR(3); SCHED();
            asm volatile("s_nop 0\n\tv_permlane32_swap_b32 %0, %1\n\ts_nop 1\n\tv_add_f32_dpp %0, %1, %0 row_ror:8 row_mask:0xf bank_mask:0xf" : "+v"(f0[4]), "+v"(f1[4]));
            WWR(4);
            WT(6);
            f32x2 a, b, c, e; u32 mq; v4u sv;
            asm volatile("s_waitcnt lgkmcnt(0)\n\ts_barrier\n\tds_read_b128 %4, %6 offset:%7+2560\n\t"
                         "ds_read_b64 %0, %5 offset:%7\n\tds_read_b64 %1, %5 offset:%7+640\n\tds_read_b64 %2, %5 offset:%7+1280\n\tds_read_b64 %3, %5 offset:%7+1920"
                         : "=&v"(a), "=&v"(b), "=&v"(c), "=&v"(e), "=&v"(sv) : "v"(xr), "v"(zero), "i"(L * 4096) : "memory");
            asm volatile("s_waitcnt lgkmcnt(4)\n\tv_or_b32 %4, %6, %7\n\tv_or3_b32 %4, %4, %8, %9\n\tv_lshl_or_b32 %5, %4, 31, %10\n\t"
                         "s_waitcnt lgkmcnt(2)\n\tv_pk_add_f32 %0, %0, %1\n\ts_waitcnt lgkmcnt(0)\n\tv_pk_add_f32 %2, %2, %3\n\ts_nop 0\n\tv_pk_add_f32 %0, %0, %2"
                         : "+v"(a), "+v"(b), "+v"(c), "+v"(e), "=&v"(pst), "=&v"(mq) : "v"(sv.x), "v"(sv.y), "v"(sv.z), "v"(sv.w), "v"(mp) : "memory");
            st4s(rs, mq, 0, pkv(a));
            SCHED();
            asm volatile("s_memrealtime %0" : "=s"(clk));
            WT(7);
            st4s(rs, za, 0, psent);
            VMW(3); SCHED();
            static_for<7, 13>(next);
        }
    };
    do {
        turn(IC<0>{});
        turn(IC<1>{});
    } while (uu(fin) == 0u);
    if (sn[0] + 1 < NSEG || sn[1] + 1 < NSEG) st4s(rs, tid == 0 ? foff(F_ERR) : OOB, 0, 1u);
    if (w == 0) LST(1900 + j, rt32());
    LDUMP(w == 0, 224 + j);
}

__global__ void __launch_bounds__(256, 1) k_main(Params p) {
    lptr lds = (lptr)(u32)16 - 16;
    if (blockIdx.x == 0) LST(256, rt32());
    unsigned long long pc; int len;
    asm volatile("s_getpc_b64 %0\n.Lk_main_here:\n\ts_mov_b32 %1, .Lk_end_here-.Lk_main_here" : "=s"(pc), "=s"(len));
    const rsrc_t rs = mk_rsrc(p.ws, W_END);
    const v2u e0 = ld8s(rs, threadIdx.x == 0 ? foff(F_EPOCH) : OOB, 0);
    const v4u code = __builtin_amdgcn_raw_buffer_load_b128(mk_rsrc((const void*)(pc & ~15ull), (u32)max(len, 0)), (((blockIdx.x >> 3) & 31u) * 256u + threadIdx.x) * 16u, 0, 0);
    CFENCE();
    auto conv_id = [](int role) {
        const int x = role >> 5, i = role & 31, no = x < 5 ? 5 : 10, og = x < 5 ? x - 3 : 2 * (x - 4) + (i >= 5), r = (x - 3) * 5 + i - no, q = r / 3;
        return x < 3 || i >= no + 5 || r >= 24 ? NCV : i < no ? ((og & 1) ? (og >> 1) * 5 + i % 5 : NCV) : r % 3 == 0 ? 20 + (q & 1) * 20 + (q >> 1) : 16 + (r % 3) * 8 + q;
    };
    int role = (int)(((u32)__builtin_amdgcn_s_getreg(20 | (3 << 11)) & 7u) * 32u + (blockIdx.x >> 3));
    Early e;
    v4u rv;
#pragma unroll 1
    for (int n = 0;; ++n) {
        conv_early(p, rs, conv_id(role), e);
        if (n) break;
        CFENCE();
        if (threadIdx.x == 0) {
            GAS unsigned long long* slots = (GAS unsigned long long*)(p.ws + W_CTRL + F_SLOT * 128);
            const unsigned long long tag = (unsigned long long)e0.y << 32 | (e0.x + 1u);
            u32 mine = (u32)role;
#pragma unroll 1
            for (int k = 0; k < 256 && __hip_atomic_exchange(slots + mine * 16, tag, __ATOMIC_RELAXED, __HIP_MEMORY_SCOPE_AGENT) == tag; ++k) mine = (mine + 1u) & 255u;
            lds_w16(lds, (v4u){mine, e0.x, e0.y, 0u});
        }
        BAR();
        rv = lds_r16(lds);
        BAR();
        if (uni((int)rv.x) == role) break;
        role = uni((int)rv.x);
    }
    const int x = role >> 5, i = role & 31, w = uni(threadIdx.x >> 6), no = x < 5 ? 5 : 10, nr = x < 7 ? 5 : 4, j = conv_id(role);
    const v2u ep = {uu(rv.y), uu(rv.z)};
    asm volatile("" :: "v"(code));
    if (x == 1) { role_wo(p, lds, i, ep); __builtin_amdgcn_endpgm(); }
    if (x < 3) { role_head(p, lds, i >> 4, x * 8 + (i & 15), ep); __builtin_amdgcn_endpgm(); }
    if (i >= no + nr) { role_qkv(p, lds, (x < 5 ? (x - 3) * 22 : 44 + (x - 5) * 17) + i - no - nr, ep); __builtin_amdgcn_endpgm(); }
    const bool cv = j >= NPRE || (j >= 20 && j < 24);
    if (j < NCV) conv_stage(p, rs, lds, j, (j & 3) * 2 + (j >= NPRE), cv ? 40 : 0, ep, e);
    if (i >= no) {
        if (!cv) role_row(p, lds, (j - 24) * 4 + w, ep);
        __builtin_amdgcn_endpgm();
    }
    const int og = x < 5 ? x - 3 : 2 * (x - 4) + (i >= 5);
    role_out(p, lds, og & 1, og >> 1, i % 5, ep);
}
__global__ void k_end() { asm volatile(".Lk_end_here:"); }

extern "C" void kernel_launch(void* const* d_in, const int* in_sizes, int n_in, void* d_out, int out_size, void* d_ws, size_t ws_size,
                              hipStream_t stream) {
    Params p;
    p.x = (const u16*)d_in[0]; p.mem0 = (const u16*)d_in[1]; p.Wq = (const u16*)d_in[2]; p.Wk = (const u16*)d_in[3];
    p.Wv = (const u16*)d_in[4]; p.Wo = (const u16*)d_in[5];
    p.bk = (const u16*)d_in[8]; p.bv = (const u16*)d_in[9]; p.shift = (const u16*)d_in[10]; p.ssmk = (const u16*)d_in[11]; p.ssmD = (const u16*)d_in[12];
    p.out = (u16*)d_out;
    p.ws = (unsigned char*)(((size_t)d_ws + 2 * MB - 1) & ~(size_t)(2 * MB - 1));
    (void)hipFuncSetAttribute((const void*)k_main, hipFuncAttributeMaxDynamicSharedMemorySize, LDS_BYTES);
    hipLaunchKernelGGL(k_main, dim3(256), dim3(256), LDS_BYTES, stream, p);
}
```

```cpp
#include <hip/hip_runtime.h>
#include <hip/hip_bf16.h>

typedef unsigned short u16;
typedef unsigned int u32;
typedef u32 v2u __attribute__((ext_vector_type(2)));
typedef u32 v4u __attribute__((ext_vector_type(4)));
typedef u32 v8u __attribute__((ext_vector_type(8)));
typedef float f32x2 __attribute__((ext_vector_type(2)));
typedef float f32x4 __attribute__((ext_vector_type(4)));
typedef float f32x16 __attribute__((ext_vector_type(16)));
typedef __bf16 bf16x2 __attribute__((ext_vector_type(2)));
typedef __bf16 bf16x8 __attribute__((ext_vector_type(8)));
typedef short s16x4 __attribute__((ext_vector_type(4)));
#define LAS __attribute__((address_space(3)))
#define GAS __attribute__((address_space(1)))
typedef LAS unsigned char* lptr;
#define DEV __device__ __forceinline__

constexpr int SEQ = 2048, DM = 640, NH = 32, DH = 128, NSEG = 64, NQ = NH * DH;

constexpr u32 MB = 1u << 20;
constexpr u32 W_MEM0 = 2 * MB;
constexpr u32 W_CTRL = 4 * MB;
constexpr u32 CTRL_BYTES = 128 * 1024;
constexpr u32 ACT = SEQ * DM * 2;
constexpr u32 W_H1 = 5 * MB, W_Y = W_H1 + ACT, W_H2 = W_Y + ACT, W_X2 = W_H2 + ACT;
constexpr u32 W_KQ = 16 * MB;
constexpr u32 W_VO = W_KQ + 64 * MB;
constexpr u32 W_P = W_VO + 64 * MB;
constexpr u32 P_ITEM = 16 * 4 * DM * 4;
constexpr u32 W_KVT = W_P + 4 * NSEG * P_ITEM;
constexpr u32 W_SP = W_KVT + ACT;
constexpr u32 W_END = W_KVT + 3 * MB;
static_assert((size_t)W_END + 2 * MB <= 256ull * MB, "workspace");
constexpr u32 F_ERR = 0, F_QKV = 16, F_O = F_QKV + 192, F_P = F_O + 64, F_H2 = F_P + 64, F_CLR = F_H2 + 32,
              F_EPOCH = F_CLR + 32, F_SLOT = F_EPOCH + 1, F_PRE = F_SLOT + 256, F_N = F_PRE + 256;
constexpr int NCV = 44, NPRE = 40;
static_assert(F_N * 128 <= CTRL_BYTES, "ctrl");
DEV u32 foff(u32 idx) { return W_CTRL + idx * 128u; }

constexpr u32 OOB = 0x80000000u;
constexpr u32 PSENT = 0x7F817F81u;
constexpr int SPIN_CAP = 40000;
constexpr int LDS_BYTES = 160 * 1024;

#define LDUMP(cond, id) do {} while (0)
#define LMX(cond, k, s_, v) do {} while (0)
#define LS3(cond, slot, idx) do {} while (0)
#define LST(idx, v) do {} while (0)
#define LS2(cond, idx) do {} while (0)
struct Params {
    const u16 *x, *mem0, *Wq, *Wk, *Wv, *Wo, *bk, *bv, *shift, *ssmk, *ssmD;
    u16* out;
    unsigned char* ws;
};

#define INAUX 2
#define INNT "nt"
typedef __amdgpu_buffer_rsrc_t rsrc_t;
DEV rsrc_t mk_rsrc(const void* p, u32 bytes) { return __builtin_amdgcn_make_buffer_rsrc((void*)p, 0, bytes, 0x00020000); }
DEV v4u ld16s(rsrc_t r, u32 off, u32 so) { return __builtin_amdgcn_raw_buffer_load_b128(r, off, so, 16); }
DEV v2u ld8s(rsrc_t r, u32 off, u32 so) { return __builtin_amdgcn_raw_buffer_load_b64(r, off, so, 16); }
DEV v4u ldin16(rsrc_t r, u32 off, u32 so) { return __builtin_amdgcn_raw_buffer_load_b128(r, off, so, INAUX); }
DEV v2u ldin8(rsrc_t r, u32 off, u32 so) { return __builtin_amdgcn_raw_buffer_load_b64(r, off, so, INAUX); }
DEV u32 ld4(rsrc_t r, u32 off) { return __builtin_amdgcn_raw_buffer_load_b32(r, off, 0, 16); }
DEV void st16(rsrc_t r, u32 off, v4u v) { __builtin_amdgcn_raw_buffer_store_b128(v, r, off, 0, 16); }
DEV void st8s(rsrc_t r, u32 off, u32 so, v2u v) { __builtin_amdgcn_raw_buffer_store_b64(v, r, off, so, 16); }
DEV void st4s(rsrc_t r, u32 off, u32 so, u32 v) { __builtin_amdgcn_raw_buffer_store_b32(v, r, off, so, 16); }
#define DRAIN() asm volatile("s_waitcnt vmcnt(0)" ::: "memory")
#define VMW(n) asm volatile("s_waitcnt vmcnt(%0)" :: "i"(n) : "memory")
#define BAR() asm volatile("s_waitcnt lgkmcnt(0)\n\ts_barrier" ::: "memory")
#define SCHED() __builtin_amdgcn_sched_barrier(0)
#define CFENCE() asm volatile("" ::: "memory")
#define NOPS(n) asm volatile("s_nop %0" :: "i"(n))
#define PIN(x) asm volatile("" : "+v"(x))
#define PINS(x) asm volatile("" : "+s"(x))

template <int V> struct IC { static constexpr int value = V; };
template <int I, int N, class F> DEV void static_for(F&& f) { if constexpr (I < N) { f(IC<I>{}); static_for<I + 1, N>(f); } }
#define SFOR(var, n) static_for<0, n>([&](auto ic_) __attribute__((always_inline)) { constexpr int var = decltype(ic_)::value;
#define SEND });

DEV u32 pk(float a, float b) { bf16x2 v = {(__bf16)a, (__bf16)b}; return __builtin_bit_cast(u32, v); }
DEV float lo_f(u32 u) { return __builtin_bit_cast(float, u << 16); }
DEV float hi_f(u32 u) { return __builtin_bit_cast(float, u & 0xffff0000u); }
DEV f32x2 up2(u32 u) { return (f32x2){lo_f(u), hi_f(u)}; }
DEV u32 pk2(f32x2 v) { return pk(v.x, v.y); }
DEV float rbf(float a) { return (float)(__bf16)a; }
DEV v4u lds_r16(lptr p) { return *(LAS v4u*)p; }
DEV void lds_w16(lptr p, v4u v) { *(LAS v4u*)p = v; }
DEV v2u lds_tr(lptr p) { return __builtin_bit_cast(v2u, __builtin_amdgcn_ds_read_tr16_b64_v4i16((LAS s16x4*)p)); }
DEV v4u tr2(lptr p0, lptr p1) { const v2u a = lds_tr(p0), b = lds_tr(p1); return (v4u){a.x, a.y, b.x, b.y}; }
DEV v8u cat8(v4u a, v4u b) { return __builtin_shufflevector(a, b, 0, 1, 2, 3, 4, 5, 6, 7); }
DEV f32x4 mfma16(v4u a, v4u b, f32x4 c) { return __builtin_amdgcn_mfma_f32_16x16x32_bf16(__builtin_bit_cast(bf16x8, a), __builtin_bit_cast(bf16x8, b), c, 0, 0, 0); }
DEV f32x16 mfma32(v4u a, v4u b, f32x16 c) { return __builtin_amdgcn_mfma_f32_32x32x16_bf16(__builtin_bit_cast(bf16x8, a), __builtin_bit_cast(bf16x8, b), c, 0, 0, 0); }

DEV float f_(u32 u) { return __builtin_bit_cast(float, u); }
DEV u32 u_(float f) { return __builtin_bit_cast(u32, f); }
template <int CTRL> DEV float dpp(float v) { return f_((u32)__builtin_amdgcn_update_dpp(0, (int)u_(v), CTRL, 0xf, 0xf, false)); }
DEV float max16(float v) { auto r = __builtin_amdgcn_permlane16_swap(u_(v), u_(v), false, false); return fmaxf(f_(r[0]), f_(r[1])); }
DEV float max32(float v) { auto r = __builtin_amdgcn_permlane32_swap(u_(v), u_(v), false, false); return fmaxf(f_(r[0]), f_(r[1])); }
DEV float add16(float v) { auto r = __builtin_amdgcn_permlane16_swap(u_(v), u_(v), false, false); return f_(r[0]) + f_(r[1]); }
DEV float add32(float v) { auto r = __builtin_amdgcn_permlane32_swap(u_(v), u_(v), false, false); return f_(r[0]) + f_(r[1]); }
DEV float wave_sum(float v) {
    v += dpp<0xB1>(v);
    v += dpp<0x4E>(v);
    v += dpp<0x124>(v);
    v += dpp<0x128>(v);
    return add32(add16(v));
}
DEV bool all_(bool c) { return __builtin_amdgcn_ballot_w64(!c) == 0ull; }
DEV int uni(int v) { return __builtin_amdgcn_readfirstlane(v); }
DEV u32 uu(u32 v) { return (u32)__builtin_amdgcn_readfirstlane((int)v); }
DEV u32 rdl(u32 v, int l) { return (u32)__builtin_amdgcn_readlane((int)v, l); }
DEV u32 rt32() { return (u32)__builtin_amdgcn_s_memrealtime(); }

#define WSL 2
DEV void wait_flags(rsrc_t rs, u32 off, u32 want, bool& dead) {
    if (dead) return;
#pragma unroll 1
    for (int n = 0;; ++n) {
        CFENCE();
        const u32 v = ld4(rs, off);
        if (all_(off >= OOB || v >= want)) break;
        __builtin_amdgcn_s_sleep(WSL);
        if ((n & 63) == 63) { if (uni((int)ld4(rs, foff(F_ERR))) != 0) { dead = true; break; } }
        if (n > SPIN_CAP) { st4s(rs, foff(F_ERR), 0, 1u); dead = true; break; }
    }
    CFENCE();
}

DEV void pre_post(rsrc_t rs, int idx, u32 stage, v2u ep) { st8s(rs, threadIdx.x == 0 ? foff(F_PRE + idx) : OOB, 0, (v2u){ep.x + stage, ep.y}); }
DEV void pre_poll(rsrc_t rs, int first, int n, u32 stage, v2u ep) {
    const int lane = threadIdx.x & 63;
#pragma unroll 1
    for (int it = 0; it < SPIN_CAP; ++it) {
        CFENCE();
        const v2u a = ld8s(rs, foff(F_PRE + first + min(lane, n - 1)), 0), b = ld8s(rs, foff(F_PRE + first + min(lane + 64, n - 1)), 0);
        if (all_(a.y == ep.y && b.y == ep.y && max(a.x - ep.x - stage, b.x - ep.x - stage) < 8u - stage)) break;
        __builtin_amdgcn_s_sleep(2);
    }
    CFENCE();
}
DEV void pre_wait(rsrc_t rs, int first, int n, u32 stage, v2u ep) {
    if (uni(threadIdx.x >> 6) == 0) pre_poll(rs, first, n, stage, ep);
    BAR();
}
#define POLX 0u
DEV u32 pol_wait(rsrc_t rs, v2u ep) {
    const int lane = threadIdx.x & 63;
    v4u a;
#pragma unroll 1
    for (int it = 0; it < SPIN_CAP; ++it) {
        CFENCE();
        a = ld16s(rs, foff(F_PRE + (lane < NPRE || lane >= 48 ? lane : 0)), 0);
        if (all_(a.y == ep.y && a.x - ep.x - 2u < 6u)) break;
        __builtin_amdgcn_s_sleep(2);
    }
    CFENCE();
    return uu((wave_sum(lane >= 48 ? (float)(int)a.z : 0.f) >= 0.f ? 8192u : 0u) + (wave_sum(lane >= 48 ? (float)(int)a.w : 0.f) >= 0.f ? 16384u : 0u)) ^ POLX;
}
#define PROBE_C 7
#define PROBE_T 620
DEV v2u pol_probe(rsrc_t rs, int id) {
    const int lane = threadIdx.x & 63;
    const u32 off0 = (u32)id * 32768u, tend = rt32() + PROBE_T;
    st4s(rs, lane < 32 ? off0 + (u32)lane * 1024u : OOB, 0, PSENT); DRAIN();
    int d = 1 << 16, calm = 0;
#pragma unroll 1
    for (int q = 0; q < 32 && ((q & 3) || (calm < PROBE_C && (int)(rt32() - tend) < 0)); ++q) {
        const int kind = (q ^ (q >> 2) ^ id) & 3;
        const unsigned long long t0 = __builtin_amdgcn_s_memtime();
        const u32 v = ld4(rs, lane == 0 ? off0 + (u32)(kind * 8192 + (q >> 2) * 1024) : OOB);
        asm volatile("s_waitcnt vmcnt(0)" :: "v"(v) : "memory");
        const int dt = (int)(__builtin_amdgcn_s_memtime() - t0);
        d = lane == kind ? min(d, dt) : d;
        calm += dt < 1100;
    }
    const int d0 = (int)rdl(d, 0), d1 = (int)rdl(d, 1), d2 = (int)rdl(d, 2), d3 = (int)rdl(d, 3), a = min(d0, d2) - min(d1, d3);
    const int b0 = min(max(d0 - d2, -80), 80), b1 = min(max(d1 - d3, -80), 80);
    const u32 xcc = (u32)__builtin_amdgcn_s_getreg(20 | (3 << 11));
    return (v2u){(u32)min(max((xcc & 4u) ? -a : a, -250), 250), (u32)((xcc & 1u) ? b0 + b1 : a > 0 ? b0 - b1 : b1 - b0)};
}
DEV f32x2 ln_stats(const f32x2 (&x)[5]) {
    const float c = f_(uu(u_(x[0].x)));
    f32x2 d[5];
#pragma unroll
    for (int i = 0; i < 5; ++i) d[i] = x[i] - c;
    const f32x2 s2 = (d[0] + d[1]) + (d[2] + d[3]) + d[4], q2 = (d[0] * d[0] + d[1] * d[1]) + (d[2] * d[2] + d[3] * d[3] + d[4] * d[4]);
    float a = s2.x + s2.y, b = q2.x + q2.y, a2, b2;
#define LN_DPP(m) "v_add_f32_dpp %0, %0, %0 " m " row_mask:0xf bank_mask:0xf\n\tv_add_f32_dpp %1, %1, %1 " m " row_mask:0xf bank_mask:0xf\n\ts_nop 0\n\t"
    asm("s_nop 1\n\t" LN_DPP("quad_perm:[1,0,3,2]") LN_DPP("quad_perm:[2,3,0,1]") LN_DPP("row_ror:4")
        "v_add_f32_dpp %0, %0, %0 row_ror:8 row_mask:0xf bank_mask:0xf\n\tv_add_f32_dpp %1, %1, %1 row_ror:8 row_mask:0xf bank_mask:0xf\n\ts_nop 1\n\t"
        "v_permlane16_swap_b32 %0, %1\n\tv_add_f32 %0, %0, %1\n\tv_mov_b32 %1, %0\n\ts_nop 1\n\tv_permlane32_swap_b32 %0, %1\n\tv_add_f32 %0, %0, %1\n\ts_nop 0\n\t"
        "v_readlane_b32 %2, %0, 0\n\tv_readlane_b32 %3, %0, 16\n\ts_nop 1"
        : "+v"(a), "+v"(b), "=s"(a2), "=s"(b2));
    const float m = a2 * (1.0f / DM), var = fmaxf(b2 * (1.0f / DM) - m * m, 0.f);
    const float sd = rbf(__builtin_amdgcn_sqrtf(rbf(rbf(var) + 1e-5f)));
    return (f32x2){rbf(c + m), __builtin_amdgcn_rcpf(sd)};
}
DEV u32 pkv(f32x2 v) { u32 d; asm("v_cvt_pk_bf16_f32 %0, %1, %2" : "=v"(d) : "v"(v.x), "v"(v.y)); return d; }
DEV void store_h(rsrc_t rs, u32 base, int row, const u32 (&hp)[5]) {
    const u32 lane = threadIdx.x & 63;
    const u32 sw = (u32)(row & 15) * 16u, o8 = (lane >> 5) * 8192u + (((lane & 31u) * 8u) ^ sw), hb = base + (u32)(row >> 5) * (32 * DM * 2) + (u32)(row & 31) * 256u;
    st8s(rs, o8, hb, (v2u){hp[0], hp[1]});
    st8s(rs, o8, hb + 16384, (v2u){hp[2], hp[3]});
    st4s(rs, (lane * 4) ^ sw, hb + 32768, hp[4]);
}

#define DMA16(ldsb, vo, so) asm volatile("s_mov_b32 m0, %0\n\ts_nop 0\n\tbuffer_load_dwordx4 %1, %2, %3 offen sc1 lds" :: "s"(ldsb), "v"(vo), "s"(srd), "s"(so) : "memory")
#define DMA16I(ldsb, lo, vo, so, go) do { u32 so_; asm volatile("s_add_u32 m0, %1, %5\n\ts_add_u32 %0, %4, %6\n\tbuffer_load_dwordx4 %2, %3, %0 offen sc1 lds" \
                         : "=&s"(so_) : "s"(ldsb), "v"(vo), "s"(srd), "s"(so), "i"(lo), "i"(go) : "memory", "scc"); } while (0)
#define DMA32(ldsb, vo, so) asm volatile("s_mov_b32 m0, %0\n\ts_nop 0\n\tbuffer_load_dwordx4 %1, %2, %3 offen sc1 lds\n\tbuffer_load_dwordx4 %1, %2, %3 offen offset:1024 sc1 lds" \
                         :: "s"(ldsb), "v"(vo), "s"(srd), "s"(so) : "memory")
#define DMAIN(sr, ldsb, vo, so) asm volatile("s_mov_b32 m0, %0\n\ts_nop 0\n\tbuffer_load_dwordx4 %1, %2, %3 offen lds" :: "s"(ldsb), "v"(vo), "s"(sr), "s"(so) : "memory")
DEV v4u mk_srd(const void* q, u32 bytes) { return (v4u){uu((u32)(size_t)q), uu((u32)((size_t)q >> 32) & 0xffffu), bytes, 0x00020000u}; }
#define DMANT(sr, ldsb, vo, so) asm volatile("s_mov_b32 m0, %0\n\ts_nop 0\n\tbuffer_load_dwordx4 %1, %2, %3 offen " INNT " lds" :: "s"(ldsb), "v"(vo), "s"(sr), "s"(so) : "memory")
#define AHEAD(sr, vo, so) asm volatile("buffer_load_dwordx4 %0, %1, %2, %3 offen" : "+v"(junk) : "v"(vo), "s"(sr), "s"(so) : "memory")
#define MK_SRD() const v4u srd = mk_srd(p.ws, W_END)

constexpr int CV_W = 40960;
template <bool B> struct CvL {
    static constexpr int NF = B ? 5 : 3, NK = B ? 4 : 2, KP = 544 + NK * 256, CH = NF * 1024 + 4 * KP;
};
constexpr int CV_Y = 4 * CvL<false>::CH, CV_YP = 272;
static_assert(CV_Y + 32 * CV_YP <= CV_W && 2 * CvL<true>::CH <= CV_Y, "conv lds");
DEV int cv_chan(int u, int i) { return 16 * (u % 40) + 4 * i + u / 40; }
template <bool B> DEV void cv_filt(const Params& p, u32 dst, int d, int i) {
    const u32 lane = threadIdx.x & 63;
    const v4u sf = mk_srd(p.ssmk + d * SEQ, SEQ * 2);
    if (i == 0) DMANT(sf, dst, lane < 4u ? OOB : lane * 16u - 64u, 0);
    else DMANT(sf, uu(dst + i * 1024), i == CvL<B>::NF - 1 && lane >= 4u ? OOB : lane * 16u, i * 1024 - 64);
}
DEV v4u cv_kvld(rsrc_t rs, int d, int i) { return ld16s(rs, (threadIdx.x & 63) * 16u, uu(W_KVT + (u32)d * (SEQ * 2) + i * 1024)); }
template <bool B> DEV void cv_kvput(lptr F, const v4u* k) {
    const int lane = threadIdx.x & 63;
#pragma unroll
    for (int i = 0; i < CvL<B>::NK; ++i) lds_w16(F + CvL<B>::NF * 1024 + (lane & 3) * CvL<B>::KP + 512 + i * 256 + (lane >> 2) * 16, k[i]);
}
template <bool B> DEV void cv_zero(lptr F) {
    const int lane = threadIdx.x & 63;
#pragma unroll
    for (int i = 0; i < 4; ++i) lds_w16(F + (lane >> 5) * CvL<B>::CH + CvL<B>::NF * 1024 + i * CvL<B>::KP + (lane & 31) * 16, (v4u){0u, 0u, 0u, 0u});
}
#define M32V(d, a, b) asm volatile("v_mfma_f32_32x32x16_bf16 %0, %1, %2, %0" : "+v"(d) : "v"(a), "v"(b))
template <bool B, class G> DEV void cv_run(const Params& p, rsrc_t rs, lptr F, int d, u32 D, f32x4 sp, float (&y)[16], G&& gap) {
    constexpr int KP = CvL<B>::KP, FO = B ? 2048 : 0;
    const int lane = threadIdx.x & 63, n = lane & 31, hh = lane >> 5, c = lane & 3;
    lptr fa = F + 64 + (32 * hh + 16 * (c & 1) + 8 * ((lane >> 4) & 1) + 4 * (c >> 1) - (lane & 12)) * 2;
    lptr kl = F + CvL<B>::NF * 1024 + (32 + n - hh) * 16;
    v4u dg;
#pragma unroll
    for (int q = 0; q < 4; ++q) { const int k = (lane & 35) ? -1 : (n & 4) + (n >> 3); dg[q] = k == 2 * q ? D : k == 2 * q + 1 ? D << 16 : 0u; }
    v4u l[4], h[4]; v2u fc[4], fh[4];
    f32x16 acc[4];
#pragma unroll
    for (int j = 0; j < 4; ++j) { l[j] = lds_r16(kl + j * KP); if constexpr (B) h[j] = lds_r16(kl + j * KP + 512); }
#pragma unroll
    for (int j = 0; j < 2; ++j) { fc[j] = lds_tr(fa + FO - 32 * j); if constexpr (B) fh[j] = lds_tr(fa - 32 * j); }
    asm volatile("s_nop 1" : "+v"(dg));
#pragma unroll
    for (int j = 0; j < 4; ++j) asm volatile("v_mfma_f32_32x32x16_bf16 %0, %1, %2, 0" : "=&v"(acc[j]) : "v"(dg), "v"(B ? h[j] : l[j]));
    constexpr int U = B ? 2 : 4;
    kl -= 32 * U; fa -= 32;
    auto body = [&](bool first) __attribute__((always_inline)) {
#pragma unroll
        for (int z = 0; z < U; ++z) {
            const int c = 2 * (z & 1);
            const v4u f = __builtin_shufflevector(fc[c], fc[c + 1], 0, 1, 2, 3), g = __builtin_shufflevector(fh[c], fh[c + 1], 0, 1, 2, 3);
#pragma unroll
            for (int j = 0; j < 4; ++j) {
                M32V(acc[j], f, l[j]); l[j] = lds_r16(kl + j * KP + 32 * (U - z - 1));
                if (j < 2) fc[(c ^ 2) + j] = lds_tr(fa + FO + (z + 1) * 128 + 32 - 32 * j);
                if (first && j > 0) gap(5 * z + j - 1);
                SCHED();
            }
            if constexpr (B) {
#pragma unroll
                for (int j = 0; j < 4; ++j) {
                    M32V(acc[j], g, h[j]); h[j] = lds_r16(kl + j * KP + 512 + 32 * (U - z - 1));
                    if (j < 2) fh[(c ^ 2) + j] = lds_tr(fa + (z + 1) * 128 + 32 - 32 * j);
                    if (first && j > 1) gap(5 * z + j + 1);
                    SCHED();
                }
            }
        }
        kl -= 32 * U; fa += 128 * U;
    };
    if constexpr (B) body(true);
#pragma unroll 1
    for (int m = B ? 2 * U : 0; m < 32; m += 2 * U) body(false);
    asm volatile("s_nop 11" : "+v"(acc[0]), "+v"(acc[1]), "+v"(acc[2]), "+v"(acc[3]));
    const float o[3] = {acc[1][15] + acc[2][14] + acc[3][13], acc[2][15] + acc[3][14], acc[3][15]};
    if constexpr (!B) st16(rs, lane == 63 ? W_SP + (u32)d * 16u : OOB, (v4u){u_(o[0]), u_(o[1]), u_(o[2]), 0u});
#pragma unroll
    for (int i = 0; i < 16; ++i) {
        float v = acc[0][i];
#pragma unroll
        for (int j = 1; j < 4; ++j) if (j <= i) v += acc[j][i - j];
        if (i < 3) { const float e = f_((u32)__builtin_amdgcn_ds_bpermute((hh ? lane - 32 : lane + 31) * 4, (int)u_(o[i]))); v += lane == 0 ? sp[i] : e; }
        y[i] = v;
    }
}
DEV void cv_put(lptr F, int pr, const float (&y0)[16], const float (&y1)[16]) {
    const int lane = threadIdx.x & 63;
#pragma unroll
    for (int i = 0; i < 16; ++i) *(LAS u32*)(F + CV_Y + (lane & 31) * CV_YP + (lane >> 5) * 128 + i * 8 + pr * 4) = pk(y0[i], y1[i]);
}
DEV void cv_out(rsrc_t rs, lptr F, int u, int blk0) {
    const int lane = threadIdx.x & 63;
    lptr q = F + CV_Y + ((lane >> 2) & 3) * CV_YP + ((lane >> 4) * 4 + (lane & 3)) * 8;
    const u32 yo = W_Y + (u32)(((lane >> 2) * 8 + blk0) * DM + 4 * u + (lane & 3)) * 16u;
#pragma unroll
    for (int i = 0; i < 8; ++i) { lptr a = q + (i >> 1) * (8 * CV_YP) + (i & 1) * 128; st16(rs, yo + (u32)((i & 1) * 128 + (i >> 1)) * (DM * 16), tr2(a, a + 4 * CV_YP)); }
}
struct KvIn { v2u k[19], v[16]; v4u s[2]; };
template <class F> DEV void kvt_load(const Params& p, int tile, KvIn& in, F&& mid) {
    const int lane = threadIdx.x & 63;
    const rsrc_t rk = mk_rsrc(p.bk, ACT), rv = mk_rsrc(p.bv, ACT), rh = mk_rsrc(p.shift, DM * 8);
    const int row = (tile / 10) * 64 + (lane >> 4) * 16, col = (tile % 10) * 64 + (lane & 15) * 4;
    const u32 vo = (u32)(row * DM + col) * 2u, vp = row > 0 ? vo - 3 * (DM * 2) : OOB;
#pragma unroll
    for (int i = 0; i < 2; ++i) in.s[i] = ldin16(rh, (u32)col * 8u + i * 16, 0);
#pragma unroll
    for (int i = 0; i < 19; ++i) { if (i == 7) mid(); in.k[i] = i < 3 ? ldin8(rk, vp, i * (DM * 2)) : ldin8(rk, vo, (i - 3) * (DM * 2)); if (i >= 3) in.v[i - 3] = ldin8(rv, vo, (i - 3) * (DM * 2)); }
}
DEV void kvt_make(rsrc_t rs, lptr xt, int tile, const KvIn& in) {
    const int lane = threadIdx.x & 63;
#pragma unroll
    for (int c = 0; c < 2; ++c) {
        const v4u sv = in.s[c];
        const f32x2 sh[4] = {{lo_f(sv.x), lo_f(sv.z)}, {hi_f(sv.x), hi_f(sv.z)}, {lo_f(sv.y), lo_f(sv.w)}, {hi_f(sv.y), hi_f(sv.w)}};
        f32x2 kf[19];
#pragma unroll
        for (int i = 0; i < 19; ++i) kf[i] = up2(in.k[i][c]);
        u32 kv[16];
#pragma unroll
        for (int i = 0; i < 16; ++i) kv[i] = pk2(((kf[i] * sh[3] + kf[i + 1] * sh[2]) + (kf[i + 2] * sh[1] + kf[i + 3] * sh[0])) * up2(in.v[i][c]));
#pragma unroll
        for (int q = 0; q < 8; ++q) {
            const u32 sel = (q & 1) ? 0x07060302u : 0x05040100u; const u32* z = kv + (q >> 1);
            *(LAS v2u*)(xt + ((2 * c + (q & 1)) * 16 + (lane & 15)) * 144 + (lane >> 5) * 64 + (q >> 1) * 16 + ((lane >> 4) & 1) * 8) =
                (v2u){__builtin_amdgcn_perm(z[4], z[0], sel), __builtin_amdgcn_perm(z[12], z[8], sel)};
        }
    }
    const u32 o = W_KVT + (u32)(((tile % 10) * 64 + 4 * (lane >> 3)) * SEQ + (tile / 10) * 64 + (lane & 7) * 8) * 2u;
#pragma unroll
    for (int i = 0; i < 8; ++i) st16(rs, o + (u32)(32 * (i & 1) + (i >> 1)) * (SEQ * 2), lds_r16(xt + (8 * i + (lane >> 3)) * 144 + (lane & 7) * 16));
}
struct RowX { v2u a, b; u32 c; };
DEV void ln_row(rsrc_t rs, int row, const RowX& r) {
    const f32x2 x[5] = {up2(r.a.x), up2(r.a.y), up2(r.b.x), up2(r.b.y), up2(r.c)};
    const f32x2 st = ln_stats(x);
    u32 hp[5];
#pragma unroll
    for (int q = 0; q < 5; ++q) hp[q] = pk2(up2(pkv(x[q] - st.x)) * st.y);
    store_h(rs, W_H1, row, hp);
}
DEV v4u mem0_load(const Params& p, int j) {
    const u32 n = (u32)(j * 256 + threadIdx.x);
    return ldin16(mk_rsrc(p.mem0, 8 * DM * 2), n < 640u ? ((2 * ((n >> 2) & 3) + (n & 1)) * DM + (n >> 4) * 16 + (n & 2) * 4) * 2u : OOB, 0);
}
DEV void pre_flags(rsrc_t rs, int j, v4u m) {
    const u32 n = (u32)(j * 256 + threadIdx.x);
#pragma unroll
    for (int i = 0; i < 4; ++i) m[i] = m[i] == PSENT ? 0x7FC17FC1u : m[i];
    st4s(rs, n - 36u * 256u < F_CLR ? foff(n - 36u * 256u) : OOB, 0, 0u);
    st16(rs, n < 640u ? W_MEM0 + (n >> 9) * 32768u + (n & 511u) * 16u : OOB, m);
}
constexpr int KT0 = NPRE * 4, LN0 = NPRE * 8;
struct Early { RowX r[2]; v4u m; };
DEV void conv_early(const Params& p, rsrc_t rs, int j, Early& e) {
    const int lane = threadIdx.x & 63, w = uni(threadIdx.x >> 6), u = j * 4 + w;
    if (j < NPRE) {
        const rsrc_t rx = mk_rsrc(p.x, ACT);
#pragma unroll
        for (int i = 0; i < 2; ++i) {
            const u32 so = uu((u + i * KT0) * (DM * 2));
            e.r[i].a = __builtin_amdgcn_raw_buffer_load_b64(rx, lane * 8u, so, 0); e.r[i].b = __builtin_amdgcn_raw_buffer_load_b64(rx, lane * 8u + 512u, so, 0);
            e.r[i].c = __builtin_amdgcn_raw_buffer_load_b32(rx, lane * 4u + 1024u, so, 0);
        }
        e.m = mem0_load(p, j);
    }
}
DEV void conv_stage(const Params& p, rsrc_t rs, lptr lds, int j, v2u ep, Early& e) {
    const int tid = threadIdx.x, lane = tid & 63, w = uni(tid >> 6), u = j * 4 + w;
    lptr F = lds + w * CV_W;
    const u32 fb = uu(w * CV_W);
    const rsrc_t rd = mk_rsrc(p.ssmD, DM * 2);
    float y0[16], y1[16];
    KvIn in;
    if (j < NPRE) {
        constexpr int CH = CvL<false>::CH;
        if (w == 1) LST(1500 + j, rt32());
        pre_flags(rs, j, e.m);
        ln_row(rs, u, e.r[0]); ln_row(rs, u + KT0, e.r[1]);
        if (w == 1) LST(1550 + j, rt32());
        CFENCE();
        kvt_load(p, u, in, [&] { VMW(13); BAR(); pre_post(rs, j, 1u, ep); if (w == 0) LST(1450 + j, rt32()); });
        if (j == 20) LST(260, rt32());
#pragma unroll 1
        for (u32 o = (u32)(j * 256 + tid) * 16u; o < 2 * MB; o += NPRE * 4096) st16(rs, o, (v4u){PSENT, PSENT, PSENT, PSENT});
        const u32 D = __builtin_amdgcn_raw_buffer_load_b16(rd, (u32)cv_chan(u, lane & 3) * 2u, 0, 0);
#pragma unroll 1
        for (int i = 0; i < 4; ++i) { const int d = cv_chan(u, i); cv_filt<false>(p, fb + i * CH, d, 0); cv_filt<false>(p, fb + i * CH, d, 1); cv_filt<false>(p, fb + i * CH, d, 2); }
        kvt_make(rs, F + CV_Y, u, in);
        cv_zero<false>(F); cv_zero<false>(F + 2 * CH);
        DRAIN(); BAR();
        pre_post(rs, j, 2u, ep);
        if (j == 20) LST(262, rt32());
        pre_wait(rs, 0, NPRE, 2u, ep);
        if (j == 20) LST(281, rt32());
        {
            v4u k[8];
#pragma unroll
            for (int i = 0; i < 8; ++i) k[i] = cv_kvld(rs, cv_chan(u, i >> 1), i & 1);
#pragma unroll
            for (int i = 0; i < 4; ++i) cv_kvput<false>(F + i * CH, k + 2 * i);
        }
        if (j == 20) LST(286, rt32());
#pragma unroll 1
        for (int i = 0; i < 4; i += 2) {
            cv_run<false>(p, rs, F + i * CH, cv_chan(u, i), rdl(D, i), f32x4{}, y0, [](int) {});
            cv_run<false>(p, rs, F + (i + 1) * CH, cv_chan(u, i + 1), rdl(D, i + 1), f32x4{}, y1, [](int) {});
            cv_put(F, i >> 1, y0, y1);
        }
        if (j == 20) LST(287, rt32());
        cv_out(rs, F, u, 0);
        if (j == 20) LST(263, rt32());
    } else {
        const v2u vote = pol_probe(rs, u - NPRE * 4);
        st16(rs, lane == 0 ? foff(F_PRE + 48 + u - NPRE * 4) : OOB, (v4u){ep.x + 2u, ep.y, vote.x, vote.y});
#pragma unroll 1
        for (int t = u + 128; t < 320; t += 16) { kvt_load(p, t, in, [] {}); kvt_make(rs, F, t, in); }
    }
    DRAIN(); BAR();
    pre_post(rs, j, 3u, ep);
    const int b = j < 20 ? -1 : j < 24 ? u - 80 : j >= NPRE ? u - 144 : (w & 1) ? (u >> 1) - 16 : -1, u0 = b < 32 ? b : b + 96, ue = b < 32 ? 128 : 160;
    if (b >= 0) {
        constexpr int CH = CvL<true>::CH;
        cv_zero<true>(F);
        pre_poll(rs, 64, 32, 3u, ep); pre_poll(rs, 0, NCV, 3u, ep);
        v4u k[4];
        auto fetch = [&](u32 dst, int d, int i) { if (i < 5) cv_filt<true>(p, dst, d, i); else if (i < 9) k[i - 5] = cv_kvld(rs, d, i - 5); };
#pragma unroll
        for (int i = 0; i < 9; ++i) fetch(fb, cv_chan(u0, 0), i);
        const u32 ch = (u32)cv_chan(min(u0 + 32 * (lane >> 2), 159), lane & 3);
        u32 D = __builtin_amdgcn_raw_buffer_load_b16(rd, ch * 2u, 0, 0);
        v4u sp = ld16s(rs, W_SP + ch * 16u, 0);
        asm volatile("" : "+v"(D), "+v"(sp));
#pragma unroll 1
        for (int un = u0, n = 0; un < ue; un += 32) {
            if (un == 64) { LST(282, rt32()); LST(288, __builtin_amdgcn_s_memtime()); }
#pragma unroll 1
            for (int i = 0; i < 4; i += 2, n += 2) {
                const int d1 = (int)rdl(ch, n + 1), d2 = (int)rdl(ch, n + 2);
                cv_kvput<true>(F, k);
                cv_run<true>(p, rs, F, 0, rdl(D, n), (f32x4){f_(rdl(sp.x, n)), f_(rdl(sp.y, n)), f_(rdl(sp.z, n)), 0.f}, y0, [&](int q) { fetch(fb + CH, d1, q); });
                cv_kvput<true>(F + CH, k);
                cv_run<true>(p, rs, F + CH, 326, rdl(D, n + 1), (f32x4){f_(rdl(sp.x, n + 1)), f_(rdl(sp.y, n + 1)), f_(rdl(sp.z, n + 1)), 0.f}, y1, [&](int q) { fetch(fb, d2, q); });
                cv_put(F, i >> 1, y0, y1);
            }
            cv_out(rs, F, un, 4);
            if (un == 64) { LST(283, rt32()); LST(289, __builtin_amdgcn_s_memtime()); }
        }
        DRAIN();
        st8s(rs, lane == 0 ? foff(F_PRE + 192 + b) : OOB, 0, (v2u){ep.x + 3u, ep.y});
    }
    if (j == 20) LST(284, rt32());
    if (b == 62) LST(285, rt32());
}

#define M32A(first, d, a, b) do { if (first) asm volatile("v_mfma_f32_32x32x16_bf16 %0, %1, %2, 0" : "=&v"(d) : "a"(a), "v"(b)); \
                                  else asm volatile("v_mfma_f32_32x32x16_bf16 %0, %1, %2, %0" : "+v"(d) : "a"(a), "v"(b)); } while (0)
#define M32B(first, d, a, b) do { if (first) asm volatile("v_mfma_f32_32x32x16_bf16 %0, %1, %2, 0" : "=&v"(d) : "v"(a), "a"(b)); \
                                  else asm volatile("v_mfma_f32_32x32x16_bf16 %0, %1, %2, %0" : "+v"(d) : "v"(a), "a"(b)); } while (0)
#define VCVT(d, a, b) asm volatile("v_cvt_pk_bf16_f32 %0, %1, %2" : "=v"(d) : "v"(a), "v"(b))
#define VADD(d, a, b) asm volatile("v_add_f32 %0, %1, %2" : "=v"(d) : "v"(a), "v"(b))
#define LD4A(d, vo, so) asm volatile("buffer_load_dword %0, %1, %2, %3 offen sc1" : "=v"(d) : "v"(vo), "s"(srd), "s"(so) : "memory")
DEV u32 wst_src(int lane, int ld) { return (u32)((lane >> 4) * ld + 8 * ((lane & 15) ^ ((lane >> 4) << 2))) * 2u; }
DEV u32 wst_rd(int lane, int w) { const int q4 = (lane >> 2) & 3; return (u32)(q4 * 256 + ((w ^ q4) * 64) + ((lane >> 4) & 1) * 32 + (lane & 3) * 8); }

constexpr int QK_SEL = 81920;
#define QK_LEAD 12
#define QK_SMP 29
#define QK_YB 26
#define QK_W0 2
DEV void role_qkv(const Params& p, lptr lds, int c, v2u ep) {
    const int tid = threadIdx.x, lane = tid & 63, w = uni(tid >> 6), hh = lane >> 5, r32 = lane & 31;
    const int m = c >> 5, h = c & 31;
    const rsrc_t rs = mk_rsrc(p.ws, W_END);
    v4u wt[40];
    {
        const v4u sw = mk_srd(m == 0 ? p.Wq : m == 1 ? p.Wk : p.Wv, DM * NQ * 2), sx = mk_srd(p.x, ACT);
        const u32 vo = wst_src(lane, NQ) + (u32)h * (DH * 2), x8 = (u32)lane * 8u, x4 = (u32)lane * 4u;
        RowX xr[5];
        auto wfill = [&](int i0, int n) {
#pragma unroll 1
            for (int i = i0; i < i0 + n; ++i) DMAIN(sw, uu((4 * i + w) * 1024), vo, uu((16 * i + 4 * w) * (NQ * 2)));
        };
        wfill(0, QK_W0);
#pragma unroll
        for (int i = 0; i < 5; ++i)
            asm volatile("buffer_load_dwordx2 %0, %3, %5, %6 offen\n\tbuffer_load_dwordx2 %1, %3, %5, %6 offen offset:512\n\tbuffer_load_dword %2, %4, %5, %6 offen offset:1024"
                         : "=&v"(xr[i].a), "=&v"(xr[i].b), "=&v"(xr[i].c) : "v"(x8), "v"(x4), "s"(sx), "s"(uu((LN0 + 4 * c + w + 384 * i) * (DM * 2))) : "memory");
        wfill(QK_W0, 10 - QK_W0);
        if (c == 3) LST(275, rt32());
        VMW(10 - QK_W0);
        if (c == 3) LST(276, rt32());
#pragma unroll
        for (int i = 0; i < 5; ++i) {
            asm volatile("" : "+v"(xr[i].a), "+v"(xr[i].b), "+v"(xr[i].c));
            const int row = LN0 + 4 * c + w + 384 * i;
            if (i < 4 || row < SEQ) ln_row(rs, row, xr[i]);
            wfill(10 + 6 * i, 6);
        }
        if (c == 3) LST(264, rt32());
        lptr tb = lds + hh * 2048 + wst_rd(lane, w);
        SFOR(k, 40)
            if constexpr (k == 0) { VMW(15); BAR(); }
            if constexpr (k == 28) {
                if (w == 0) pre_poll(rs, 0, NPRE, 1u, ep);
                DRAIN(); BAR();
                pre_post(rs, 96 + c, 1u, ep);
            }
            lptr q = tb + (k >> 3) * 32768 + (k & 7) * 4096; wt[k] = tr2(q, q + 1024); asm volatile("" : "+a"(wt[k]));
        SEND
    }
    if (c == 3) LST(268, rt32());
    if (w == 0) LST(1350 + c, rt32());
    bool dead = false;
    int n0, n1;
    const u32 t16 = (u32)(w * 2048 + lane * 16), wb = uu(w * 2048);
    u32 ar[16];
#pragma unroll
    for (int j = 0; j < 16; ++j) ar[j] = (u32)(r32 * 256 + ((2 * (j & 7) + hh) ^ (r32 & 15)) * 16 + (j >> 3) * 49152);
    MK_SRD();
#define XRD(y, ks) lds_r16(lds + ar[((ks) & 7) + ((ks) >= 24 ? 8 : 0)] + ((ks) >> 3) % 3 * 16384 + (y) * 8192)
#define QM0(y, b) asm volatile("s_add_u32 m0, %0, %1" :: "s"(wb), "i"((b) * 16384 + (y) * 8192) : "scc", "memory")
#define QDMA(i, vo) asm volatile("buffer_load_dwordx4 %0, %1, %2 offen offset:%3 sc1 lds" :: "v"(vo), "s"(srd), "s"(((i) >> 1) * 8192), "i"(((i) & 1) * 1024) : "memory")
    const u32 h2flag = (lane < 16 && w == 0) ? foff(F_H2 + lane) : OOB;
    const u32 ol = (m == 2 ? (u32)(2 * w + (r32 >> 4)) * 1024u : (u32)((r32 >> 4) * 4 + w) * 1024u) + (u32)(hh * 16 + (r32 & 15)) * 16u
                 + (m == 2 ? W_VO : m == 1 ? W_KQ : W_KQ + 8192u) + (u32)h * 16384u;
    const u32 fo = tid < 2 ? foff(F_QKV + tid * 96 + c) : OOB;
    lptr selw = lds + QK_SEL + (w == 0 ? 0 : 64), selr = lds + QK_SEL;
    PIN(selw);
    static_assert(QK_LEAD > 8 && QK_LEAD % 2 == 0 && LN0 == 10 * 32, "X = 8 comes up, before any item of layer 1");
    static_assert(W_H2 - W_H1 == 2 * 64 * (32 * DM * 2), "h");
    auto run = [&](auto isv) __attribute__((always_inline)) {
        constexpr bool V = decltype(isv)::value;
        constexpr int YB = QK_YB;
        f32x16 ax, ay = {};
        v4u x[8], pp, pq;
        u32 xv = t16 + W_H1, ox = ol, oy = OOB, nn = 1, lt = 0, fcnt = 0, fa = OOB;
        asm volatile("" : "+v"(lt), "+v"(nn));
#pragma unroll
        for (int z = 0; z < 5; ++z) x[z] = XRD(1, 40 - YB + z);
#define QPK(r_, a1, a_, q_) do { if constexpr (V) VCVT(r_[q_], a1[4 * (a_) + 8 * ((q_) >> 1) + 2 * ((q_) & 1)], a1[4 * (a_) + 8 * ((q_) >> 1) + 2 * ((q_) & 1) + 1]); \
                                 else VCVT(r_[q_], a1[4 * (a_) + (q_)], a1[4 * (a_) + (q_) + 8]); } while (0)
#define QPK4(r_, a1, a_) do { QPK(r_, a1, a_, 0); QPK(r_, a1, a_, 1); QPK(r_, a1, a_, 2); QPK(r_, a1, a_, 3); } while (0)
        for (;;) {
            u32 ls = uu(lt), yv, fv, nx, need;
            if (ls != 0u && uu(nn) < 256u) pre_wait(rs, 96, 96, 1u, ep);
            else if (ls != 0u) {
                n1 = (int)(uu(nn) >> 8);
                CFENCE();
                const u32 hf = h2flag + (u32)(~n1 & 1) * 2048u;
                u32 f2 = ld4(rs, hf);
                asm volatile("s_waitcnt vmcnt(0)" : "+v"(f2) :: "memory");
                *(LAS u32*)(selw + 16) = (u32)__builtin_amdgcn_ballot_w64(f2 < (u32)n1) & 0xFFFFu;
                BAR();
                st4s(rs, fa, 0, fcnt);
                fa = OOB;
                if (n1 > NSEG) { LDUMP(w == 0, 64 + c); __builtin_amdgcn_endpgm(); }
                if (uu(*(LAS u32*)(selr + 16)) != 0u) {
                    if (w == 0) wait_flags(rs, hf, (u32)n1, dead);
                    BAR();
                }
            }
#define QT(k) do {} while (0)
            QT(0);
            SCHED();
            QM0(0, 0); NOPS(0); QDMA(0, xv); QDMA(1, xv); QM0(0, 1);
            SFOR(z, 80)
                constexpr int ks = (z + 40 - YB) % 40, zx = z - YB;
                constexpr auto upto = [](int q) { const int d = (q - YB + 49) % 40; return q + 6 + (d == 0 || d == 2 ? 1 : d == 1 ? 2 : 0); };
                if constexpr (zx >= 0 && zx < 40) { if constexpr (V) M32B(ks == 0, ax, x[z & 7], wt[ks]); else M32A(ks == 0, ax, wt[ks], x[z & 7]); }
                else { if constexpr (V) M32B(ks == 0, ay, x[z & 7], wt[ks]); else M32A(ks == 0, ay, wt[ks], x[z & 7]); }
                if constexpr (z == 78) lt = *(LAS u32*)selr;
#pragma unroll
                for (int q = upto(z - 1); q < upto(z); ++q) x[q & 7] = XRD(q % 80 < YB || q % 80 >= YB + 40, (q + 40 - YB) % 40);
                SCHED();
                (void)&srd; (void)&wb;
                if constexpr (z < 2) QDMA(z + 2, xv);
                if constexpr (z >= 2 && z < 14 && (z & 3) == 2) QM0(0, (z + 6) >> 2);
                if constexpr (z >= 3 && z < 15 && (z & 1)) QDMA((z + 5) >> 1, xv);
                if constexpr (z == 4) { n0 = uni((int)nn); n1 = n0 >> 8; n0 &= 255; PINS(n0); PINS(n1); }
                if constexpr (z == 8) { nx = n0 < NSEG ? W_H1 + (u32)n0 * (32 * DM * 2) : OOB; PINS(nx); }
                if constexpr (z == 12) { yv = t16 + nx; PIN(yv); }
                if constexpr (zx == -6) { QT(1); asm volatile("s_waitcnt vmcnt(6) lgkmcnt(0)\n\ts_barrier" ::: "memory"); QT(2); }
                if constexpr (zx == -5) st4s(rs, fa, 0, fcnt);
                if constexpr (zx == -4 || zx == 0 || zx == 4) QM0(1, (zx + 4) >> 2);
                if constexpr (zx >= -3 && zx < 8 && (zx & 1)) QDMA((zx + 3) >> 1, yv);
                if constexpr (zx == 2) QPK4(pp, ay, 0);
                if constexpr (zx == 6) QPK4(pq, ay, 1);
                if constexpr (zx == 8) st16(rs, oy, pp);
                if constexpr (zx == 9) st16(rs, oy + 512, pq);
                if constexpr (zx == 10) { QT(3); asm volatile("s_waitcnt vmcnt(9)\n\ts_barrier" ::: "memory"); QT(4); }
                if constexpr (zx == 11 || zx == 15) QM0(1, (zx + 1) >> 2);
                if constexpr (zx >= 12 && zx < 20 && (zx & 1) == 0) QDMA(zx >> 1, yv);
                if constexpr (zx == 21) { fcnt = lane == 0 ? (u32)n0 : (u32)n1; fa = fo; asm volatile("" : "+v"(fcnt), "+v"(fa)); }
                if constexpr (zx == 22) { oy = (ol + (u32)n0 * (NH * 16384u)) | (yv & OOB); PIN(oy); }
                if constexpr (zx == 23) { n0 = min(n0 + 1, NSEG); PINS(n0); nx = (u32)min(NSEG, n1 + QK_LEAD); PINS(nx); }
                if constexpr (zx == 24)
                    asm volatile("s_cmp_ge_u32 %1, %0\n\ts_cselect_b32 %0, %4, %1\n\ts_addc_u32 %2, %2, 0\n\ts_cmp_lt_u32 %0, 64\n\ts_cselect_b32 %3, 0, %2"
                                 : "+s"(nx), "+s"(n0), "+s"(n1), "=&s"(need) : "s"(n1 + 64) : "scc");
                if constexpr (zx == 25) { n0 += 1 - (int)min(nx >> 6, 1u); PINS(n0); }
                if constexpr (zx == 26) { nn = (u32)(n0 | n1 << 8); PIN(nn); }
                if constexpr (zx == 27) { ls = W_H1 + (nx + (nx & 64u)) * (32 * DM * 2); PINS(ls); }
                if constexpr (zx == 28) { xv = t16 + ls; PIN(xv); need = nx == 8u ? 255u : need; PINS(need); }
                if constexpr (zx == 34) { asm volatile("s_waitcnt vmcnt(%0) lgkmcnt(0)\n\ts_barrier" :: "i"(QK_SMP < 34 ? 7 : 6) : "memory"); QT(5); }
                if constexpr (zx == QK_SMP) { const u32 po = (nx & 1u) * 2048u; LD4A(fv, h2flag, po); }
                if constexpr (zx == 42) QPK4(pp, ax, 0);
                if constexpr (zx == 43) st16(rs, ox, pp);
                if constexpr (zx == 44) QPK4(pq, ax, 1);
                if constexpr (zx == 45) st16(rs, ox + 512, pq);
                if constexpr (zx == 47) { ox = ol + nx * (NH * 16384u); PIN(ox); }
                if constexpr (z == 74) { asm volatile("s_waitcnt vmcnt(2)" : "+v"(fv) :: "memory"); QT(6); ls = (u32)__builtin_amdgcn_ballot_w64(fv < need) & 0xFFFFu; PINS(ls); }
                if constexpr (z == 75) { lt = ls; PIN(lt); *(LAS u32*)selw = lt; }
                if constexpr (z == 77) asm volatile("s_waitcnt lgkmcnt(2)\n\ts_barrier" ::: "memory");
                SCHED();
            SEND
        }
    };
    if (m == 2) run(IC<1>{}); else run(IC<0>{});
}

#define OU_FLG 25
#define OU_SMP 17
DEV void role_out(const Params& p, lptr lds, int L, int kq, int ng, v2u ep) {
    const int tid = threadIdx.x, lane = tid & 63, w = uni(tid >> 6), hh = lane >> 5, r32 = lane & 31;
    const rsrc_t rs = mk_rsrc(p.ws, W_END);
    v4u wo[64];
    bool dead = false;
    {
        const v4u sw = mk_srd(p.Wo, DM * NQ * 2);
        const u32 vo = wst_src(lane, DM) + (u32)ng * 256u;
        auto fill = [&](int b) {
#pragma unroll 1
            for (int i = 0; i < 8; ++i) DMAIN(sw, uu((b % 5) * 32768 + (4 * i + w) * 1024), vo, uu((kq * 1024 + b * 128 + 16 * i + 4 * w) * (DM * 2)));
        };
#pragma unroll 1
        for (int b = 0; b < 5; ++b) fill(b);
        lptr tb = lds + hh * 1024 + wst_rd(lane, w);
        SFOR(b, 8)
            VMW(b < 4 ? 32 : 56 - 8 * b); BAR();
            lptr q = tb + (b % 5) * 32768;
#pragma unroll
            for (int k = 0; k < 8; ++k) { lptr a = q + (k >> 1) * 8192 + (k & 1) * 2048; wo[8 * b + k] = tr2(a, a + 4096); if (b < 7) asm volatile("" : "+a"(wo[8 * b + k])); else PIN(wo[8 * b + k]); }
            if constexpr (b < 3) { BAR(); fill(b + 5); }
        SEND
    }
    pre_wait(rs, 0, NPRE, 1u, ep);
    if (kq == 0 && ng == 0) LST(272 + L, rt32());
    MK_SRD();
    const u32 ofl = lane < 8 ? foff(F_O + L * 32 + kq * 8 + lane) : OOB, l16 = (u32)lane * 16u;
    const u32 fo = (tid == 0) ? foff(F_P + L * 32 + kq * 5 + ng) : OOB;
#define OM0(dst, b) asm volatile("s_add_u32 m0, %0, %1" :: "s"(dst), "i"((b) * 8192) : "scc", "memory")
#define ODMA(i, vo) asm volatile("buffer_load_dwordx4 %0, %1, %2 offen offset:%3 sc1 lds" :: "v"(vo), "s"(srd), "s"(((i) >> 1) * 16384), "i"(((i) & 1) * 1024) : "memory")
    auto first = [&](u32 dst, u32 vo) {
#pragma unroll
        for (int b = 0; b < 5; ++b) { OM0(dst, b); NOPS(0); ODMA(2 * b, vo); ODMA(2 * b + 1, vo); }
    };
    u32 pa = W_P + (u32)(L * NSEG) * P_ITEM + (u32)(2 * hh * 4 + kq) * (DM * 4) + (u32)(ng * 128 + w * 32 + r32) * 4u, fv = 0;
    u32 ov = W_VO + 8192u + (u32)(L * NSEG * NH + kq * 8) * 16384u + (u32)w * 2048u + l16;
    bool early = false;
    u32 farv = 1, owed = OOB;
    lptr selw = lds + 131072 + (w == 0 ? 0 : 64), selr = lds + 131072;
    asm volatile("" : "+v"(farv), "+v"(selw), "+v"(selr));
#define OWAIT(strict, loose) asm volatile("s_cmp_eq_u32 %0, 0\n\ts_cbranch_scc1 1f\n\ts_waitcnt vmcnt(%1)\n1:\n\ts_waitcnt vmcnt(%2)\n\ts_barrier" :: "s"(far), "i"(strict), "i"(loose) : "scc", "memory")
    for (int s = 0; s < NSEG; ++s) {
        const u32 dst = uu((s & 1) * 65536 + w * 2048), ndst = dst ^ 65536u, need = s + 1 < NSEG ? (u32)s + 2u : 255u;
        lptr A = lds + (s & 1) * 65536 + l16;
        if (!early) {
            if (!all_(ofl == OOB || fv >= (u32)s + 1)) wait_flags(rs, ofl, (u32)s + 1, dead);
            LS3(L == 0 && w == 1, 1, kq * 5 + ng);
            first(dst, ov);
        }
#define OT(k) do {} while (0)
        u32 far = uu(farv), nv, sv = (u32)s;
        OM0(dst, 5);
        OWAIT(8, 17);
        OT(0);
        v4u o[64];
#pragma unroll
        for (int k = 0; k < 4; ++k) o[k] = lds_r16(A + k * 1024);
        f32x16 acc;
        SCHED();
        SFOR(k, 64)
            if constexpr (k < 56) M32B(k == 0, acc, o[k], wo[k]); else M32V(acc, o[k], wo[k]);
            if constexpr (k < 60) o[k + 4] = lds_r16(A + (k + 4) * 1024);
            SCHED();
            (void)&srd; (void)&dst; (void)&ndst;
            if constexpr (k < 12 && (k & 1) == 0) ODMA(10 + k / 2, ov);
            if constexpr (k == 3) OWAIT(6, 14);
            if constexpr (k == 3 || k == 7) OM0(dst, (k + 21) >> 2);
            if constexpr (k == 12) { ov += NH * 16384u; PIN(ov); }
            if constexpr (k == OU_SMP) LD4A(fv, ofl, 0);
            if constexpr (k == 19) OWAIT(7, 15);
            if constexpr (k == 33) { asm volatile("s_waitcnt vmcnt(1)" : "+v"(fv) :: "memory"); far = (u32)__builtin_amdgcn_ballot_w64(fv < need) & 255u; PINS(far); }
            if constexpr (k == 34) { farv = far; PIN(farv); *(LAS u32*)selw = farv; }
            if constexpr (k == 35) asm volatile("s_waitcnt lgkmcnt(1)\n\ts_barrier" ::: "memory");
            if constexpr (k == OU_FLG) asm volatile("s_waitcnt vmcnt(7)\n\ts_barrier" ::: "memory");
            if constexpr (k == OU_FLG + 1) st4s(rs, owed, 0, sv);
            if constexpr (k == 36) farv = *(LAS u32*)selr;
            if constexpr (k == 41) { nv = farv ? OOB : ov; PIN(nv); }
            if constexpr (k >= 42 && k < 62 && (k & 3) == 2) OM0(ndst, (k - 42) >> 2);
            if constexpr (k >= 43 && k < 63 && (k & 1)) ODMA((k - 43) >> 1, nv);
            if constexpr (k == 48) LD4A(fv, ofl, 0);
            if constexpr (k == 4) OT(1);
            if constexpr (k == 20) OT(2);
            if constexpr (k == 36) OT(3);
            if constexpr (k == 41) OT(4);
            SCHED();
        SEND
        OT(5);
        asm volatile("s_nop 11" : "+v"(acc), "+v"(pa));
#pragma unroll
        for (int i = 0; i < 8; ++i) st4s(rs, pa, (u32)((4 * (i >> 1) + (i & 1)) * (4 * DM * 4)), pk(acc[2 * i], acc[2 * i + 1]));
        asm volatile("s_waitcnt vmcnt(15)" : "+v"(fv) :: "memory");
        OT(6);
        LS3(L == 0 && w == 1, 2, kq * 5 + ng);
        far = uu(farv);
        early = far == 0u || all_(ofl == OOB || fv >= need);
        owed = fo;
        if (far != 0u) {
            if (early) { first(ndst, ov); VMW(10); }
            else { fv = ld4(rs, ofl); asm volatile("s_waitcnt vmcnt(0)" : "+v"(fv) :: "memory"); }
            BAR();
            st4s(rs, fo, 0, (u32)s + 1);
            owed = OOB;
        }
        pa += P_ITEM;
        LS2(L == 0 && w == 1, 160 + kq * 5 + ng);
    }
    LDUMP(L == 0 && w == 1, kq * 5 + ng);
}

DEV void ln_stats2(const f32x2 (&x)[10], f32x2& mean, f32x2& rstd) {
    const f32x2 c = {f_(uu(u_(x[0].x))), f_(uu(u_(x[0].y)))};
    f32x2 d[10];
#pragma unroll
    for (int i = 0; i < 10; ++i) d[i] = x[i] - c;
    f32x2 a = ((d[0] + d[1]) + (d[2] + d[3])) + ((d[4] + d[5]) + (d[6] + d[7])) + (d[8] + d[9]);
    f32x2 b = (d[0] * d[0] + d[2] * d[2] + d[4] * d[4] + d[6] * d[6] + d[8] * d[8]) + (d[1] * d[1] + d[3] * d[3] + d[5] * d[5] + d[7] * d[7] + d[9] * d[9]);
    float a0, a1, b0, b1;
#define LN_DP4(m) "v_add_f32_dpp %0, %0, %0 " m " row_mask:0xf bank_mask:0xf\n\tv_add_f32_dpp %1, %1, %1 " m " row_mask:0xf bank_mask:0xf\n\t" \
                  "v_add_f32_dpp %2, %2, %2 " m " row_mask:0xf bank_mask:0xf\n\tv_add_f32_dpp %3, %3, %3 " m " row_mask:0xf bank_mask:0xf\n\t"
    asm("s_nop 1\n\t" LN_DP4("quad_perm:[1,0,3,2]") LN_DP4("quad_perm:[2,3,0,1]") LN_DP4("row_ror:4") LN_DP4("row_ror:8")
        "v_permlane16_swap_b32 %0, %1\n\tv_add_f32 %0, %0, %1\n\tv_permlane16_swap_b32 %2, %3\n\tv_add_f32 %2, %2, %3\n\ts_nop 1\n\t"
        "v_permlane32_swap_b32 %0, %2\n\tv_add_f32 %0, %0, %2\n\ts_nop 0\n\t"
        "v_readlane_b32 %4, %0, 0\n\tv_readlane_b32 %5, %0, 16\n\tv_readlane_b32 %6, %0, 32\n\tv_readlane_b32 %7, %0, 48\n\ts_nop 1"
        : "+v"(a.x), "+v"(b.x), "+v"(a.y), "+v"(b.y), "=s"(a0), "=s"(b0), "=s"(a1), "=s"(b1));
    const f32x2 m = (f32x2){a0, a1} * (1.0f / DM), v = (f32x2){b0, b1} * (1.0f / DM) - m * m;
    const f32x2 q = up2(pkv(up2(pkv((f32x2){fmaxf(v.x, 0.f), fmaxf(v.y, 0.f)})) + 1e-5f));
    const f32x2 sd = up2(pk(__builtin_amdgcn_sqrtf(q.x), __builtin_amdgcn_sqrtf(q.y)));
    mean = up2(pkv(c + m)); rstd = (f32x2){__builtin_amdgcn_rcpf(sd.x), __builtin_amdgcn_rcpf(sd.y)};
}
DEV void role_row(const Params& p, lptr lds, int id, v2u ep) {
    const int lane = threadIdx.x & 63, w = uni(threadIdx.x >> 6);
    const int pr = id >> 2, L = id & 1, par = (id >> 1) & 1;
    const rsrc_t rs = mk_rsrc(p.ws, W_END), rx = mk_rsrc(p.x, ACT), ro_ = mk_rsrc(p.out, ACT);
    bool dead = false;
    const u32 fp = lane < 20 ? foff(F_P + L * 32 + lane) : OOB, l16 = (u32)lane * 16u, l8 = (u32)lane * 8u, l4 = (u32)lane * 4u, fb = uu(w * CV_W);
    lptr F = lds + w * CV_W;
    MK_SRD();
    for (int s = par; s < NSEG; s += 2) {
        const u32 ro = (u32)(s * 32 + 2 * pr) * (DM * 2);
        if ((s & 6) == 0) {
            if ((s & 30) == 0) pre_poll(rs, s < 32 ? 0 : 192, s < 32 ? NPRE : 64, 3u, ep);
            if (id == 0 && s == 0) LST(274, rt32());
            if (s == 48) st8s(rs, (id == 1 && lane == 0) ? foff(F_EPOCH) : OOB, 0, (v2u){ep.x + 8u, ep.y});
#pragma unroll 1
            for (int i = 0; i < 5; ++i) {
                const u32 d = fb + i * 2048, o = W_Y + (u32)(16 * pr + (s >> 3)) * 10240u + i * 2048;
                DMA32(d, l16, o); DMA32(d + 10240u, l16, o + 81920u);
            }
            DRAIN();
        }
        RowX xr[2];
        auto xload = [&](rsrc_t r, u32 o, auto aux) {
#pragma unroll
            for (int i = 0; i < 2; ++i) {
                xr[i].a = __builtin_amdgcn_raw_buffer_load_b64(r, l8, o + i * (DM * 2), aux.value); xr[i].b = __builtin_amdgcn_raw_buffer_load_b64(r, l8 + 512u, o + i * (DM * 2), aux.value);
                xr[i].c = __builtin_amdgcn_raw_buffer_load_b32(r, l4 + 1024u, o + i * (DM * 2), aux.value);
            }
        };
        if (L == 0) xload(rx, ro, IC<0>{});
        f32x2 y[10], x[10];
        {
            lptr ya = F + (s & 7) * 2 + l16, yc = F + (s & 7) * 2 + 2048 + (lane >> 1) * 16 + (lane & 1) * 5120;
#pragma unroll
            for (int i = 0; i < 10; ++i) {
                lptr q = i < 8 ? ya + (i & 3) * 2560 + (i >> 2) * 1024 : yc + (i & 1) * 2560;
                y[i] = (f32x2){lo_f(*(LAS u16*)q), lo_f(*(LAS u16*)(q + 10240))}; PIN(y[i]);
            }
        }
        wait_flags(rs, fp, (u32)s + 1, dead);
        LS3(L == 0, 3, ri);
        LMX(L == 0, 0, s, rt32());
        {
            const u32 o = uu(W_P + (u32)(L * NSEG + s) * P_ITEM + (u32)pr * (4 * DM * 4));
            if (L == 1) xload(rs, W_X2 + ro, IC<16>{});
            v4u a[4], b[4]; v2u c[4];
#pragma unroll
            for (int kq = 0; kq < 4; ++kq) { a[kq] = ld16s(rs, l16, o + kq * (DM * 4)); SCHED(); b[kq] = ld16s(rs, l16 + 1024u, o + kq * (DM * 4)); SCHED(); c[kq] = ld8s(rs, l8 + 2048u, o + kq * (DM * 4)); SCHED(); }
            const u32 u0[5] = {xr[0].a.x, xr[0].a.y, xr[0].b.x, xr[0].b.y, xr[0].c}, u1[5] = {xr[1].a.x, xr[1].a.y, xr[1].b.x, xr[1].b.y, xr[1].c};
#pragma unroll
            for (int i = 0; i < 5; ++i) { x[2 * i] = (f32x2){lo_f(u0[i]), lo_f(u1[i])}; x[2 * i + 1] = (f32x2){hi_f(u0[i]), hi_f(u1[i])}; }
#pragma unroll
            for (int kq = 0; kq < 4; ++kq)
#pragma unroll
                for (int i = 0; i < 10; ++i)
                    asm volatile("v_dot2_f32_bf16 %0, %2, %3, %0\n\tv_dot2_f32_bf16 %1, %2, %4, %1" : "+v"(x[i].x), "+v"(x[i].y)
                                 : "v"(i < 4 ? a[kq][i & 3] : i < 8 ? b[kq][i & 3] : c[kq][i & 1]), "s"(0x3F80u), "s"(0x3F800000u));
            asm volatile("s_nop 2" : "+v"(x[8]), "+v"(x[9]));
        }
        LS3(L == 0, 4, ri);
        f32x2 mean, rstd;
        ln_stats2(x, mean, rstd);
#pragma unroll
        for (int i = 0; i < 10; ++i) x[i] += (x[i] - mean) * (y[i] * rstd);
        u32 xp[2][5];
#pragma unroll
        for (int i = 0; i < 5; ++i) { xp[0][i] = pkv((f32x2){x[2 * i].x, x[2 * i + 1].x}); xp[1][i] = pkv((f32x2){x[2 * i].y, x[2 * i + 1].y}); }
        if (L == 0) {
#pragma unroll
            for (int i = 0; i < 5; ++i) { x[2 * i] = (f32x2){lo_f(xp[0][i]), lo_f(xp[1][i])}; x[2 * i + 1] = (f32x2){hi_f(xp[0][i]), hi_f(xp[1][i])}; }
            ln_stats2(x, mean, rstd);
#pragma unroll
            for (int i = 0; i < 10; ++i) x[i] = (x[i] - mean) * rstd;
#pragma unroll
            for (int r = 0; r < 2; ++r) {
                u32 hp[5];
#pragma unroll
                for (int i = 0; i < 5; ++i) hp[i] = pkv((f32x2){x[2 * i][r], x[2 * i + 1][r]});
                store_h(rs, W_H2, s * 32 + 2 * pr + r, hp);
            }
#pragma unroll
            for (int r = 0; r < 2; ++r) {
                st8s(rs, l8, W_X2 + ro + r * (DM * 2), (v2u){xp[r][0], xp[r][1]});
                st8s(rs, l8 + 512u, W_X2 + ro + r * (DM * 2), (v2u){xp[r][2], xp[r][3]});
                st4s(rs, l4 + 1024u, W_X2 + ro + r * (DM * 2), xp[r][4]);
            }
            LS3(L == 0, 5, ri);
            DRAIN();
            st4s(rs, lane == 0 ? foff(F_H2 + par * 16 + pr) : OOB, 0, (u32)s + 1);
        } else {
#pragma unroll
            for (int r = 0; r < 2; ++r) {
                st8s(ro_, l8, ro + r * (DM * 2), (v2u){xp[r][0], xp[r][1]});
                st8s(ro_, l8 + 512u, ro + r * (DM * 2), (v2u){xp[r][2], xp[r][3]});
                st4s(ro_, l4 + 1024u, ro + r * (DM * 2), xp[r][4]);
            }
        }
        if (pr == 0) LST(320 + L * 64 + s, rt32());
        LS2(L == 0, 224 + ri);
        LMX(L == 0, 1, s, rt32());
    }
    LDUMP(L == 0, 32 + ri);
}

constexpr float SCALE = 0.08838834764831845f, LOG2E = 1.4426950408889634f;
constexpr int C_K = 0, C_V = 8192, C_QX = 16384, C_MEM = 24576, C_Q = C_MEM + 10240, C_KM = C_Q + 2304, C_VM = C_KM + 2304, C_DUMP = C_VM + 2048,
              C_ZERO = C_DUMP + 1024, C_WL = C_ZERO + 128;
constexpr int NLF = 14;
static_assert(C_WL % 16 == 0 && C_WL + 4 * NLF * 2048 <= LDS_BYTES, "head lds");
#define CH_VIS 8
#define CH_H0 104
#define CH_W0 80
#define CH_MH 3
#define CH_MW 3
#define CH_REC 28
#define CH_RTY 32u
#define CH_RISE 20u
#define CH_FALL 3u
#define CH_PRE 7
#define CH_PRW 2
#define CH_T0 190
#define CH_UP 2
#define CH_WX 6
#define WAIT_TXT(tmp, t4) "s_memrealtime vcc\n\t.p2align 5\n1:\n\ts_waitcnt lgkmcnt(0)\n\ts_sub_u32 " tmp ", " t4 ", vcc_lo\n\ts_memrealtime vcc\n\ts_cmpk_lt_u32 " tmp ", 400\n\ts_cbranch_scc1 1b\n\t" \
                          "s_add_u32 " tmp ", " tmp ", 4\n\ts_cselect_b32 " tmp ", " tmp ", 0\n2:\n\ts_sub_u32 " tmp ", " tmp ", 1\n\ts_cbranch_scc0 2b\n\t"
DEV void wait_until(u32 t) { u32 tmp; asm volatile(WAIT_TXT("%0", "%1") "s_waitcnt lgkmcnt(0)" : "=&s"(tmp) : "s"(uu(t - 4u)) : "vcc", "scc", "memory"); }
#define SMF_A(acc, a, b) asm volatile("v_smfmac_f32_16x16x64_bf16 %0, %1, %2, %3" : "+v"(acc) : "v"(a), "a"(b), "v"(idx))
#define SMF_V(acc, a, b) asm volatile("v_smfmac_f32_16x16x64_bf16 %0, %1, %2, %3" : "+v"(acc) : "v"(a), "v"(b), "v"(idx))
#define MF0(d, a, b) asm volatile("v_mfma_f32_16x16x32_bf16 %0, %1, %2, 0" : "=&v"(d) : "v"(a), "v"(b))
#define MF1(d, a, b) asm volatile("v_mfma_f32_16x16x32_bf16 %0, %1, %2, %0" : "+v"(d) : "v"(a), "v"(b))
#define MF0H(d, a, b) asm volatile("v_mfma_f32_16x16x16_bf16 %0, %1, %2, 0" : "=&v"(d) : "v"(a), "v"(b))
#define MF1H(d, a, b) asm volatile("v_mfma_f32_16x16x16_bf16 %0, %1, %2, %0" : "+v"(d) : "v"(a), "v"(b))
DEV v8u tr_frag(lptr p0, lptr p1, lptr p2, lptr p3) { return cat8(tr2(p0, p1), tr2(p2, p3)); }
constexpr int qslot(int ks) { return (ks & 1) && ks < 8 ? ks >> 1 : -1; }
constexpr int kslot(int ks) { return ks < 4 ? 4 + ks : -1; }
constexpr int vslot(int ks) { return ks >= 4 ? 4 + ks : -1; }
DEV u32 ring_map(u32 off) { return ((off >> 13) << 15) + (off & 8191u); }
DEV u32 oslot(int s) { return (u32)(s & 3) << 18 | (u32)(s & 2) << 13; }
#define ZRD(x, i) x = __builtin_bit_cast(f32x4, lds_r16(ZP + 16 * (i)))
#define VMAX(d, a, b) asm volatile("v_max_f32 %0, %1, %2" : "=v"(d) : "v"(a), "v"(b))
#define VMAX3(d, a, b, c_) asm volatile("v_max3_f32 %0, %1, %2, %3" : "=v"(d) : "v"(a), "v"(b), "v"(c_))
#define VEXP(d, a) asm volatile("v_exp_f32 %0, %1" : "=v"(d) : "v"(a))
#define VMULC(d, a) asm volatile("v_mul_f32 %0, 0x3e0293ee, %1" : "=v"(d) : "v"(a))
#define CADD(d, a, b) do { d = (a) + (b); PIN(d); } while (0)
#define CMULC(d, a) do { d = (a) * CEXP; PIN(d); } while (0)
#define CCVT(d, a, b) do { d = pk(a, b); PIN(d); } while (0)
#define LGK(n) __builtin_amdgcn_s_waitcnt(0xC07F | (n) << 8)
#define SEL(d, a, b) asm volatile("v_cndmask_b32 %0, %1, %2, %3" : "=v"(d) : "v"(a), "v"(b), "s"(himask))
#define QPERM(d, a, q) asm volatile("v_mov_b32_dpp %0, %1 quad_perm:" q " row_mask:0xf bank_mask:0xf" : "=v"(d) : "v"(a))

#define AL_H 3
#define HD_SMP 70
DEV void role_head(const Params& p, lptr lds, int L, int h, v2u ep) {
    const int tid = threadIdx.x, lane = tid & 63, w = uni(tid >> 6), g = lane >> 4, r = lane & 15;
    const rsrc_t rs = mk_rsrc(p.ws, W_END);
    v8u wq0[10], wq1[10], wk0[10], wk1[10], wv0[10], wv1[10];
    lptr WL = lds + C_WL + w * (NLF * 2048) + lane * 16;
    {
        constexpr int GS = 1056, CS = 48 * GS;
        const v4u sq = mk_srd(p.Wq, DM * NQ * 2), sk = mk_srd(p.Wk, DM * NQ * 2), sv = mk_srd(p.Wv, DM * NQ * 2);
        const u32 vo = (u32)(g * NQ + h * DH + 8 * (r ^ ((g & 1) << 3))) * 2u;
        auto fill = [&](int ch) {
#pragma unroll 1
            for (int i = 0; i < 4; ++i) {
                const u32 dst = uu((ch % 3) * CS + (4 * i + w) * GS), so = uu((ch * 64 + 16 * i + 4 * w) * (NQ * 2));
                DMAIN(sq, dst, vo, so); DMAIN(sk, dst + 16 * GS, vo, so); DMAIN(sv, dst + 32 * GS, vo, so);
            }
        };
        fill(0); fill(1); fill(2);
        const int q4 = r >> 2;
        lptr tb = lds + (4 * (g >> 1) + 2 * (q4 >> 1) + (g & 1)) * GS + (q4 & 1) * 256 + ((w * 64 + (r & 3) * 8) ^ ((q4 & 1) << 7));
        SFOR(ch, 10)
            VMW(ch < 8 ? 24 : 108 - 12 * ch);
            BAR();
            auto frag = [&](int m, int t) { lptr b = tb + (ch % 3) * CS + m * 16 * GS + t * 32; return tr_frag(b, b + 512, b + 8 * GS, b + 8 * GS + 512); };
            wq0[ch] = frag(0, 0); wq1[ch] = frag(0, 1); wk0[ch] = frag(1, 0); wk1[ch] = frag(1, 1); wv0[ch] = frag(2, 0); wv1[ch] = frag(2, 1);
            asm volatile("" : "+a"(wq0[ch]), "+a"(wk0[ch]), "+v"(wv0[ch]), "+v"(wv1[ch]));
            if constexpr (qslot(ch) < 0) asm volatile("" : "+a"(wq1[ch])); else asm volatile("" : "+v"(wq1[ch]));
            if constexpr (kslot(ch) < 0) asm volatile("" : "+a"(wk1[ch])); else asm volatile("" : "+v"(wk1[ch]));
            BAR();
            if constexpr (ch < 7) fill(ch + 3);
        SEND
#pragma unroll
        for (int ch = 0; ch < 10; ++ch) {
            if (qslot(ch) >= 0) { lds_w16(WL + qslot(ch) * 2048, wq1[ch].lo); lds_w16(WL + qslot(ch) * 2048 + 1024, wq1[ch].hi); }
            if (kslot(ch) >= 0) { lds_w16(WL + kslot(ch) * 2048, wk1[ch].lo); lds_w16(WL + kslot(ch) * 2048 + 1024, wk1[ch].hi); }
            if (vslot(ch) >= 0) { lds_w16(WL + vslot(ch) * 2048, wv1[ch].lo); lds_w16(WL + vslot(ch) * 2048 + 1024, wv1[ch].hi); }
        }
        CFENCE();
    }
    const u32 idx = (r & 2) ? 0xEEEEu : 0x4444u;
    unsigned long long himask = 0xCCCCCCCCCCCCCCCCull;
    PINS(himask);
    bool dead = false;
    if (h == 3) LST(266 + L, rt32());
    if (w == 0) LST(1240 + L * 32 + h, rt32());
    if (L == 1) {
        KvIn in;
        kvt_load(p, KT0 + 4 * h + w, in, [] {}); kvt_make(rs, lds + w * 9216, KT0 + 4 * h + w, in);
        DRAIN(); BAR();
        pre_post(rs, 64 + h, 3u, ep);
    }
    const u32 pol0 = pol_wait(rs, ep);
    if (h == 3 && L == 0) LST(277, rt32());
    const u32 l16 = (u32)lane * 16u;
    const u32 mring = (u32)L * MB + (pol0 ^ 16384u), hint = mring + 4 * 65536u;
    const u32 fkh = lane < 3 ? foff(F_QKV + L * 96 + lane * 32 + h) : lane < 7 ? hint + (u32)(lane - 3) * 4u : OOB;
    u32 tm = 0;
    MK_SRD();
    u32 ksrc = uu(W_KQ + (u32)(L * (NSEG - 1) * NH + h) * 16384u + w * 2048), vsrc = ksrc + (W_VO - W_KQ);
    u32 dk = uu(C_K + w * 2048), dv = dk + (C_V - C_K), dq = dk + (C_QX - C_K), dm = uu(C_MEM + w * 2048), dm2 = uu(w < 2 ? C_MEM + 8192 + w * 1024 : C_DUMP);
    asm volatile("" : "+s"(dk), "+s"(dv), "+s"(dq), "+s"(dm), "+s"(dm2));
    auto keys_wait = [&](u32 need) {
#pragma unroll 1
        for (int n = 0; !dead; ++n) {
            const bool beat = need + 2 * L > 2u;
            if (beat) wait_until(tm);
            const u32 hv = ld4(rs, lane == 7 ? foff(F_ERR) : fkh);
            if (beat) { tm = rdl(hv, 5); wait_until(rdl(hv, 3) - 30u); }
            if (((u32)__builtin_amdgcn_ballot_w64(ld4(rs, fkh) < need) & 7u) == 0u) break;
            dead = n > SPIN_CAP || rdl(hv, 7) != 0u;
            st4s(rs, dead ? foff(F_ERR) : OOB, 0, 1u);
        }
        CFENCE();
        DMA32(dk, l16, ksrc); DMA32(dv, l16, vsrc); DMA32(dq, l16, ksrc ^ 8192u);
    };
    if (L == 0) { st4s(rs, tid == 0 ? hint + 4096u + (u32)h * 128u : OOB, 0, 0u); keys_wait(1u); }
    lptr qw = lds + C_Q + (2 * g) * 288 + (32 * w + 2 * r) * 2;
    lptr kw = qw + (C_KM - C_Q); PIN(kw);
    lptr qr = lds + C_Q + (2 * (r >> 2) + (r & 1)) * 288 + g * 16;
    lptr LB = lds + l16, VX = lds + C_V + w * 2048 + l16;
    PIN(VX);
    if (tid >= 64 && tid < 70) { const u32 z = tid == 68 ? 0x3F803F80u : tid == 69 ? PSENT : 0u; lds_w16(lds + C_ZERO + (tid - 64) * 16, (v4u){z, z, z, z}); }
    u32 zaddr = 0; PIN(zaddr);
    lptr ZP = lds + zaddr + C_ZERO;
    const float CEXP = SCALE * LOG2E;
#define LF_RD(slot, hf) lf[slot][hf] = lds_r16(WL + (slot) * 2048 + (hf) * 1024)
    BAR();
    const u32 ofl = tid == 0 ? foff(F_O + L * 32 + h) : OOB;
    u32 oso = (((g | L) & 1) ? OOB : 0u) + (u32)L * MB + (pol0 ^ oslot(-L)) + ring_map((u32)((r >> 3) * 32768 + h * 1024 + w * 256 + (g >> 1) * 128 + (r & 7) * 16));
    unsigned long long vis[8];
#pragma unroll
    for (int i = 0; i < 8; ++i) vis[i] = __builtin_amdgcn_ballot_w64(16 * (i >> 2) + 4 * g + (i & 3) <= 16 * (w & 1) + r);
    u32 missed = 0;
    for (int s = -L; s < NSEG;) {
        u32 sv = (u32)max(s, 0), ms = s > 0 ? mring + (sv & 3u) * 65536u : W_MEM0, ms2 = ms + 32768u + (w & 1) * 1024;
        ms += w * 2048;
        asm volatile("" : "+s"(ms), "+s"(ms2)); PIN(sv);
        f32x4 aq0, aq1, ak0, ak1;
        v4u lf[NLF][2];
        ZRD(aq0, 0); ZRD(aq1, 1); ZRD(ak0, 2); ZRD(ak1, 3);
#pragma unroll
        for (int i = 0; i < 4; ++i) { LF_RD(i, 0); LF_RD(i, 1); }
        SCHED();
        v4u A[10];
#define HT(k) do {} while (0)
        v4u kx[8], qf[4], kmf[4], vx[2], ones, P;
        float fa, fb, fc, fd, inv, sx[4], t[4], tm0, tm1; u32 pa, pb, fl, e01, e23, pmm;
        f32x4 av0, av1, s0, s1, sm, oa, ob, rsum;
        v2u vm, pm;
#define LW(sl) cat8(lf[sl][0], lf[sl][1])
#define PQ(ks, tl) do { if constexpr (!(tl)) SMF_A(aq0, A[ks], wq0[ks]); else if constexpr (qslot(ks) < 0) SMF_A(aq1, A[ks], wq1[ks]); else { const v8u lw_ = LW(qslot(ks)); SMF_V(aq1, A[ks], lw_); } } while (0)
#define PK(ks, tl) do { if constexpr (!(tl)) SMF_A(ak0, A[ks], wk0[ks]); else if constexpr (kslot(ks) < 0) SMF_A(ak1, A[ks], wk1[ks]); else { const v8u lw_ = LW(kslot(ks)); SMF_V(ak1, A[ks], lw_); } } while (0)
#define PV(ks, tl) do { if constexpr (!(tl)) SMF_V(av0, A[ks], wv0[ks]); else if constexpr (vslot(ks) < 0) SMF_V(av1, A[ks], wv1[ks]); else { const v8u lw_ = LW(vslot(ks)); SMF_V(av1, A[ks], lw_); } } while (0)
#define KXR(i) kx[i] = lds_r16(LB + C_K + ((i) & 1) * 4096 + ((i) >> 1) * 1024)
#define XW(q_) do { *(LAS u32*)(q_) = pa; *(LAS u32*)((q_) + 288) = pb; } while (0)
        auto slot = [&](auto zc) __attribute__((always_inline)) {
            constexpr int z = decltype(zc)::value;
            if constexpr (z < 14) PQ(z >> 1, z & 1);
            else if constexpr (z < 17) PQ(z - 7, 0);
            else if constexpr (z < 20) PQ(z - 10, 1);
            else if constexpr (z < 34) PK((z - 20) >> 1, z & 1);
            else if constexpr (z < 37) PK(z - 27, 0);
            else if constexpr (z < 40) PK(z - 30, 1);
            else if constexpr (z < 42) { if constexpr (z == 40) MF0(s0, kx[0], qf[0]); else MF0(s1, kx[1], qf[0]); }
            else if constexpr (z < 48) { if constexpr (z & 1) MF1(s1, kx[z - 40], qf[(z - 40) >> 1]); else MF1(s0, kx[z - 40], qf[(z - 40) >> 1]); }
            else if constexpr (z < 54) PV(z - 48, 0);
            else if constexpr (z == 54) MF0(sm, kmf[0], qf[0]);
            else if constexpr (z < 58) MF1(sm, kmf[z - 54], qf[z - 54]);
            else if constexpr (z < 62) PV(z - 52, 0);
            else if constexpr (z < 64) PV(z - 62, 1);
            else if constexpr (z == 64) MF0(oa, vx[0], P);
            else if constexpr (z == 65) MF0(ob, vx[1], P);
            else if constexpr (z == 66) MF0(rsum, ones, P);
            else if constexpr (z < 75) PV(z < 70 ? z - 63 : z == 70 ? 2 : z == 71 ? 7 : z == 72 ? 3 : z - 65, 1);
            else { const v2u ones2 = ones.lo; MF1H(rsum, ones2, pm); }
            if constexpr (z < 6) A[4 + z] = lds_r16(LB + C_MEM + (4 + z) * 1024);
            if constexpr (z >= 6 && z < 10) LF_RD(4 + ((z - 6) >> 1), z & 1);
            if constexpr (z >= 14 && z < 18) LF_RD(6 + ((z - 14) >> 1), z & 1);
            if constexpr (z >= 22 && z < 28) KXR(z - 22);
            if constexpr (z == 28) XW(qw);
            if constexpr (z == 29 || z == 30) KXR(z - 23);
            if constexpr (z == 32 || z == 47) asm volatile("s_barrier" ::: "memory");
            if constexpr (z >= 33 && z < 37) qf[z - 33] = lds_r16(qr + (z - 33) * 64);
            if constexpr (z == 20) ZRD(av0, 0);
            if constexpr (z == 43) XW(kw);
            if constexpr (z == 44 || z == 45) vx[z - 44] = lds_r16(VX + (z - 44) * 1024);
            if constexpr (z >= 48 && z < 52) kmf[z - 48] = lds_r16(qr + (C_KM - C_Q) + (z - 48) * 64);
            if constexpr (z == 52) ZRD(av1, 1);
            if constexpr (z == 37) ones = lds_r16(ZP + 64);
            if constexpr (z >= 57 && z < 64) LF_RD(8 + ((z - 57) >> 1), (z - 57) & 1);
            if constexpr (z >= 65 && z < 70) LF_RD(8 + ((z - 58) >> 1), (z - 58) & 1);
            if constexpr (z == HD_SMP) LD4A(fl, fkh, 0);
            SCHED();
            if constexpr (z == 19) HT(2);
            if constexpr (z == 32) HT(3);
            if constexpr (z == 47) HT(4);
            if constexpr (z == 64) HT(5);
            if constexpr (z == 19) LGK(4);
            if constexpr (z == 21 || z == 41) LGK(1);
            if constexpr (z == 56 || z == 73) LGK(0);
            if constexpr (z == 31 || z == 46) LGK(2);
            if constexpr (z == 39) LGK(4);
            if constexpr (z >= 53 && z < 56) LGK(57 - z);
            if constexpr (z == 64) LGK(3);
            if constexpr (z == 68) LGK(5);
            if constexpr (z >= 70 && z < 73) LGK(74 - z);
            SCHED();
            if constexpr (z == 18) CADD(fa, aq0[0], aq0[2]);
            if constexpr (z == 19) CADD(fb, aq0[1], aq0[3]);
            if constexpr (z == 20) CMULC(fa, fa);
            if constexpr (z == 21) CADD(fc, aq1[0], aq1[2]);
            if constexpr (z == 22) CADD(fd, aq1[1], aq1[3]);
            if constexpr (z == 23) CMULC(fc, fc);
            if constexpr (z == 24) CMULC(fb, fb);
            if constexpr (z == 25) CMULC(fd, fd);
            if constexpr (z == 26) CCVT(pa, fa, fc);
            if constexpr (z == 27) CCVT(pb, fb, fd);
            if constexpr (z == 38) CADD(fa, ak0[0], ak0[2]);
            if constexpr (z == 39) CADD(fb, ak0[1], ak0[3]);
            if constexpr (z == 41) { CADD(fc, ak1[0], ak1[2]); CADD(fd, ak1[1], ak1[3]); }
            if constexpr (z == 42) { CCVT(pa, fa, fc); CCVT(pb, fb, fd); }
            if constexpr (z >= 49 && z < 53) SEL(sx[z - 49], s0[z - 49], s1[z - 49]);
            if constexpr (z >= 53 && z < 57) VEXP(t[z - 53], sx[z - 53]);
            if constexpr (z == 57) CCVT(e01, t[0], t[1]);
            if constexpr (z == 58) CCVT(e23, t[2], t[3]);
            if constexpr (z == 59) QPERM(P.x, e01, "[0,1,0,1]");
            if constexpr (z == 60) QPERM(P.z, e01, "[2,3,2,3]");
            if constexpr (z == 61) QPERM(P.y, e23, "[0,1,0,1]");
            if constexpr (z == 62) QPERM(P.w, e23, "[2,3,2,3]");
            if constexpr (z == 63) CADD(fa, av0[0], av0[2]);
            if constexpr (z == 64) { asm volatile("" :: "v"(sm)); VEXP(tm0, sm[0]); }
            if constexpr (z == 65) VEXP(tm1, sm[1]);
            if constexpr (z == 66) CADD(fb, av0[1], av0[3]);
            if constexpr (z == 67) CCVT(pmm, tm0, tm1);
            if constexpr (z >= 68 && z < 72) SEL(oa[z - 68], oa[z - 68], ob[z - 68]);
            if constexpr (z == 72) asm volatile("v_cndmask_b32 %0, %1, 0, %2" : "=v"(pm.x) : "v"(pmm), "s"(himask));
            if constexpr (z == 73) asm volatile("v_cndmask_b32 %0, 0, %1, %2" : "=v"(pm.y) : "v"(pmm), "s"(himask));
            if constexpr (z == 74) LGK(15);
            if constexpr (z == 75) CCVT(vm.x, fa, fb);
            SCHED();
        };
        {
            u32 tmp;
            asm volatile("s_mov_b32 m0, %2\n\t" WAIT_TXT("%0", "%1") "buffer_load_dwordx4 %4, %5, %6 offen sc1 lds\n\tbuffer_load_dwordx4 %4, %5, %6 offen offset:1024 sc1 lds\n\t"
                         "s_mov_b32 m0, %3\n\ts_nop 0\n\tbuffer_load_dwordx4 %4, %5, %7 offen sc1 lds\n\t.p2align 6\n\t.rept %8\n\ts_nop 0\n\t.endr\n\ts_waitcnt vmcnt(0) lgkmcnt(0)\n\ts_barrier"
                         : "=&s"(tmp) : "s"(uu(tm - 4u)), "s"(dm), "s"(dm2), "v"(l16), "s"(srd), "s"(ms), "s"(ms2), "i"(AL_H) : "vcc", "scc", "memory");
        }
        HT(1);
#pragma unroll
        for (int ks = 0; ks < 4; ++ks) A[ks] = lds_r16(LB + C_MEM + ks * 1024);
        SCHED();
        st4s(rs, ofl, 0, sv);
        SCHED();
        static_for<0, 76>(slot);
        asm volatile("s_nop 2\n\tv_add_f32 %1, %3, %5\n\tv_add_f32 %2, %4, %6\n\tv_cvt_pk_bf16_f32 %0, %1, %2"
                     : "=v"(vm.y), "=&v"(fc), "=&v"(fd) : "v"(av1[0]), "v"(av1[1]), "v"(av1[2]), "v"(av1[3]));
        __builtin_amdgcn_s_waitcnt(0xCF7F);
        asm volatile("s_nop 0\n\tv_mfma_f32_16x16x16_bf16 %0, %2, %3, %0\n\tv_rcp_f32 %1, %4" : "+v"(oa), "=&v"(inv) : "v"(vm), "v"(pm), "v"(rsum[0]), "v"(rsum));
        SCHED();
        if (__builtin_expect(__builtin_amdgcn_ballot_w64(u_(rsum[0]) - 0x21800000u > 0x3C000000u) != 0ull, 0)) {
            DRAIN(); SCHED();
            bool miss = false;
#pragma unroll 1
            for (int i = 0; i < 10; ++i) { const v4u a = lds_r16(LB + C_MEM + i * 1024); miss |= a.x == PSENT || a.y == PSENT || a.z == PSENT || a.w == PSENT; }
            if (missed < SPIN_CAP && !all_(!miss)) {
                *(LAS u32*)(lds + C_DUMP + (w == 0 ? 0 : 64)) = ld4(rs, foff(F_ERR));
                asm volatile("" : "+v"(fl) :: "memory");
                tm = rdl(fl, 5);
                BAR();
                missed = uu(*(LAS u32*)(lds + C_DUMP)) != 0u ? SPIN_CAP : missed + 1;
                st4s(rs, missed == SPIN_CAP ? foff(F_ERR) : OOB, 0, 1u);
                BAR();
                continue;
            }
            float mx = fmaxf(fmaxf(fmaxf(sx[0], sx[1]), fmaxf(sx[2], sx[3])), fmaxf(sm[0], sm[1]));
            mx = max32(max16(fmaxf(mx, dpp<0x4E>(mx))));
#pragma unroll
            for (int i = 0; i < 4; ++i) t[i] = __builtin_amdgcn_exp2f(sx[i] - mx);
            e01 = pk(t[0], t[1]); e23 = pk(t[2], t[3]);
            pmm = pk(__builtin_amdgcn_exp2f(sm[0] - mx), __builtin_amdgcn_exp2f(sm[1] - mx));
            asm volatile("v_cndmask_b32 %0, %2, 0, %3\n\tv_cndmask_b32 %1, 0, %2, %3" : "=&v"(pm.x), "=&v"(pm.y) : "v"(pmm), "s"(himask));
            asm volatile("s_nop 1" : "+v"(e01), "+v"(e23), "+v"(pm));
            QPERM(P.x, e01, "[0,1,0,1]"); QPERM(P.z, e01, "[2,3,2,3]"); QPERM(P.y, e23, "[0,1,0,1]"); QPERM(P.w, e23, "[2,3,2,3]");
            NOPS(1);
            MF0(rsum, ones, P); MF0(oa, vx[0], P); MF0(ob, vx[1], P);
            NOPS(7);
            { const v2u ones2 = ones.lo; MF1H(rsum, ones2, pm); }
#pragma unroll
            for (int i = 0; i < 4; ++i) SEL(oa[i], oa[i], ob[i]);
            NOPS(1);
            MF1H(oa, vm, pm);
            asm volatile("s_nop 7\n\tv_rcp_f32 %0, %1" : "=v"(inv) : "v"(rsum[0]), "v"(rsum));
        }
        {
            v4u oc; f32x2 u0 = oa.lo, u1 = oa.hi; const f32x2 i2 = {inv, inv};
            asm volatile("s_nop 0\n\tv_pk_mul_f32 %0, %0, %2\n\tv_pk_mul_f32 %1, %1, %2" : "+v"(u0), "+v"(u1) : "v"(i2));
            __builtin_amdgcn_s_waitcnt(0xCF7F);
            asm volatile("v_cvt_pk_bf16_f32 %0, %4, %5\n\tv_cvt_pk_bf16_f32 %2, %4, %5\n\tv_cvt_pk_bf16_f32 %1, %6, %7\n\tv_cvt_pk_bf16_f32 %3, %6, %7\n\t"
                         "v_permlane16_swap_b32 %0, %2\n\ts_nop 0\n\tv_permlane16_swap_b32 %1, %3"
                         : "=&v"(oc.x), "=&v"(oc.y), "=&v"(oc.z), "=&v"(oc.w) : "v"(u0.x), "v"(u0.y), "v"(u1.x), "v"(u1.y));
            st16(rs, oso, oc);
        }
        SCHED();
        unsigned long long clk;
        asm volatile("s_memrealtime %0" : "=s"(clk) :: "memory");
        HT(6);
        v4u tq[4], tk[8], tv[2];
        f32x4 c0, c1, cm;
        u32 oo, kvo; bool kvok;
        u32 ln = l16; PIN(ln);
        lptr QX = lds + C_QX + (w & 1) * 4096 + ln, VO = lds + C_V + (w ^ 1) * 2048 + ln, VMX = lds + C_VM + w * 512 + (ln >> 1);
#pragma unroll
        for (int i = 0; i < 2; ++i) { tq[i] = lds_r16(QX + i * 1024); kmf[i] = lds_r16(qr + (C_KM - C_Q) + i * 64); tk[2 * i] = lds_r16(LB + C_K + i * 1024); tk[2 * i + 1] = lds_r16(LB + C_K + 4096 + i * 1024); }
        *(LAS v2u*)VMX = vm;
        SCHED();
        SFOR(i, 12)
            if constexpr (i == 0) MF0(cm, kmf[0], tq[0]); else if constexpr (i == 1) MF0(c0, tk[0], tq[0]); else if constexpr (i == 2) MF0(c1, tk[1], tq[0]);
            else if constexpr (i % 3 == 0) MF1(cm, kmf[i / 3], tq[i / 3]); else if constexpr (i % 3 == 1) MF1(c0, tk[2 * (i / 3)], tq[i / 3]); else MF1(c1, tk[2 * (i / 3) + 1], tq[i / 3]);
            if constexpr (i < 2) kmf[2 + i] = lds_r16(qr + (C_KM - C_Q) + (2 + i) * 64);
            if constexpr (i == 0) { tq[2] = lds_r16(QX + 2048); tk[4] = lds_r16(LB + C_K + 2048); }
            if constexpr (i == 1) { tk[5] = lds_r16(LB + C_K + 6144); tq[3] = lds_r16(QX + 3072); }
            if constexpr (i == 2) { tk[6] = lds_r16(LB + C_K + 3072); tk[7] = lds_r16(LB + C_K + 7168); }
            if constexpr (i == 3) { tv[0] = lds_r16(VO); tv[1] = lds_r16(VO + 1024); }
            if constexpr (i == 4) { oo = ln + (ln & 0x300u) + ((vsrc ^ 8192u) + (u32)((w & 1) * 256) + ((u32)s & OOB)); PIN(oo); }
            if constexpr (i == 5) { ksrc += NH * 16384; vsrc += NH * 16384; PINS(ksrc); PINS(vsrc); }
            if constexpr (i == 9) { asm volatile("s_waitcnt vmcnt(1)" : "+v"(fl) :: "memory"); kvok = ((u32)__builtin_amdgcn_ballot_w64(fl < (u32)s + 2) & 7u) == 0u; }
            if constexpr (i == 10) { kvo = l16 | (kvok ? 0u : OOB); PIN(kvo); }
            SCHED();
        SEND
        asm volatile("s_waitcnt lgkmcnt(0)\n\ts_barrier" : "+s"(clk) :: "memory");
        const v2u tvm = *(LAS v2u*)(VMX + (512 - (w & 1) * 1024));
        const v4u pv = lds_r16(ZP + 80);
        DMA32(dk, kvo, ksrc);
        SCHED(); asm volatile("" :: "v"(cm));
        float u[10], m, m2;
        {
            float NEG = -1000000000.0f / SCALE; PIN(NEG);
#define MSK(d, a, i) asm volatile("v_cndmask_b32 %0, %1, %2, %3" : "=v"(d) : "v"(NEG), "v"(a), "s"(vis[i]))
#pragma unroll
            for (int i = 0; i < 4; ++i) MSK(u[i], c0[i], i);
#pragma unroll
            for (int i = 0; i < 4; ++i) MSK(u[4 + i], c1[i], 4 + i);
            asm volatile("v_max3_f32 %0, %2, %3, %10\n\tv_max3_f32 %1, %4, %5, %11\n\tv_max3_f32 %0, %0, %6, %7\n\tv_max3_f32 %1, %1, %8, %9\n\t"
                         "v_max_f32 %0, %0, %1\n\tv_mov_b32 %1, %0\n\ts_nop 1\n\tv_permlane16_swap_b32 %0, %1\n\tv_max_f32 %0, %0, %1\n\tv_mov_b32 %1, %0\n\ts_nop 1\n\t"
                         "v_permlane32_swap_b32 %0, %1\n\tv_max_f32 %0, %0, %1\n\tv_mul_f32 %0, 0xbe0293ee, %0"
                         : "=&v"(m), "=&v"(m2) : "v"(u[0]), "v"(u[1]), "v"(u[2]), "v"(u[3]), "v"(u[4]), "v"(u[5]), "v"(u[6]), "v"(u[7]), "v"(cm[0]), "v"(cm[1]));
        }
        DMA32(dv, kvo, vsrc);
        {
            v4u px; v2u p0, p1;
#define EX2(d, a, b) asm volatile("v_fmamk_f32 %0, %2, 0x3e0293ee, %4\n\tv_fmamk_f32 %1, %3, 0x3e0293ee, %4\n\tv_exp_f32 %0, %0\n\tv_exp_f32 %1, %1" : "=&v"(d.x), "=&v"(d.y) : "v"(a), "v"(b), "v"(m))
            f32x2 ex[5];
            EX2(ex[0], u[0], u[1]); EX2(ex[1], u[2], u[3]); EX2(ex[2], u[4], u[5]); EX2(ex[3], u[6], u[7]); EX2(ex[4], cm[0], cm[1]);
            asm volatile("v_cvt_pk_bf16_f32 %0, %8, %9\n\tv_cvt_pk_bf16_f32 %1, %10, %11\n\tv_cvt_pk_bf16_f32 %2, %12, %13\n\tv_cvt_pk_bf16_f32 %3, %14, %15\n\t"
                         "v_mov_b32 %5, 0\n\tv_mov_b32 %6, 0\n\tv_cvt_pk_bf16_f32 %4, %16, %17\n\tv_cvt_pk_bf16_f32 %7, %16, %17"
                         : "=&v"(px.x), "=&v"(px.y), "=&v"(px.z), "=&v"(px.w), "=&v"(p0.x), "=&v"(p0.y), "=&v"(p1.x), "=&v"(p1.y)
                         : "v"(ex[0].x), "v"(ex[0].y), "v"(ex[1].x), "v"(ex[1].y), "v"(ex[2].x), "v"(ex[2].y), "v"(ex[3].x), "v"(ex[3].y), "v"(ex[4].x), "v"(ex[4].y));
            f32x4 o[4], sum;
            const v2u ones2 = ones.lo;
            const u32 now = (u32)clk;
            u32 oz;
            SCHED();
            MF0(sum, ones, px); { const u32 q = ksrc ^ 8192u; DMA32(dq, kvo, q); } SCHED();
            MF0(o[0], vx[0], px);
            MF0(o[1], vx[1], px);
            MF0(o[2], tv[0], px); oz = oso ^ (oslot(s) ^ oslot(s + 3)); PIN(oz); SCHED();
            MF0(o[3], tv[1], px); oso ^= oslot(s) ^ oslot(s + 1); PIN(oso); SCHED();
            MF1H(sum, ones2, p0);
            MF1H(o[0], vm, p0);
            MF1H(o[1], vm, p1); st16(rs, oz, pv); SCHED();
            MF1H(o[2], tvm, p0);
            MF1H(o[3], tvm, p1);
            asm volatile("s_nop 2\n\tv_rcp_f32 %0, %1\n\ts_nop 0" : "=v"(inv) : "v"(sum[0]), "v"(sum));
            asm volatile("" : "+v"(o[0]), "+v"(o[1]));
            st16(rs, oo, (v4u){pk2(o[0].lo * inv), pk2(o[0].hi * inv), pk2(o[1].lo * inv), pk2(o[1].hi * inv)});
            asm volatile("" : "+v"(o[2]), "+v"(o[3]));
            st16(rs, oo ^ 2048u, (v4u){pk2(o[2].lo * inv), pk2(o[2].hi * inv), pk2(o[3].lo * inv), pk2(o[3].hi * inv)});
            SCHED();
            LS2(L == 0 && w == 0, h);
            st4s(rs, ((h & 15) == 0 && tid == 192) ? hint + 128u + (u32)h * 8u : OOB, 0, (u32)s << 16 | min(missed, 1u) << 14 | min(now - tm, 0x3FFFu));
            tm = rdl(fl, 5);
            missed = missed == SPIN_CAP ? missed : 0u;
            if (__builtin_expect(s < 1 - L, 0)) {
                if (s < 0) oso -= (ln & 256u) ? 0u : OOB;
#pragma unroll 1
                for (int n = 0; n < (s < 0 ? 0 : 200); ++n) {
                    CFENCE();
                    tm = rdl(ld4(rs, fkh), 6);
                    if (tm != PSENT) break;
                }
            }
        }
        if (__builtin_expect(!kvok, 0) && s + 1 < NSEG) {
            st4s(rs, ln == 0 ? hint + 4096u + (u32)h * 128u : OOB, 0, (u32)s + 1u);
            keys_wait((u32)s + 2u);
        }
        ++s;
    }
    DRAIN(); BAR();
    st4s(rs, ofl, 0, NSEG);
    LDUMP(w == 0, 160 + L * 32 + h);
}

#define WO_NL 3
#define WO_ROT w
#define WO_AH 1
DEV void role_wo(const Params& p, lptr lds, int j, v2u ep) {
    const int tid = threadIdx.x, lane = tid & 63, w = uni(tid >> 6), g = lane >> 4, r = lane & 15;
    const int rg = j & 1, cg = j >> 1, rot = WO_ROT;
    const rsrc_t rs = mk_rsrc(p.ws, W_END);
    v8u wo[8][5];
    {
        const v4u so_ = mk_srd(p.Wo, DM * NQ * 2);
        u32 vo[5];
#pragma unroll
        for (int i = 0; i < 5; ++i) { const int n = 64 * i + lane; vo[i] = (u32)((n / 5) * DM + cg * 40 + (n % 5) * 8) * 2u; }
        v4u junk = {};
        const u32 av = (u32)(j * 5120 + lane * 16);
        auto ahead = [&](int b) {
#pragma unroll
            for (int i = 0; i < 5; ++i) AHEAD(so_, av, uu((8 * w + ((b + rot) & 7)) * (DH * DM * 2) + i * 1024));
        };
        auto fill = [&](int b) {
            if (b + WO_AH < 8) ahead(b + WO_AH);
            const u32 dst = uu(w * 40960 + (b & 3) * 10240), src = uu((8 * w + ((b + rot) & 7)) * (DH * DM * 2));
#pragma unroll
            for (int i = 0; i < 10; ++i) DMAIN(so_, dst + i * 1024, vo[i % 5], src + (i / 5) * (64 * DM * 2));
        };
#pragma unroll 1
        for (int b = 0; b < WO_AH; ++b) ahead(b);
#pragma unroll 1
        for (int b = 0; b < 4; ++b) fill(b);
        const int q4 = r >> 2;
        lptr tb = lds + w * 40960 + (32 * (g >> 1) + 16 * (q4 >> 1) + 8 * ((r >> 1) & 1) + 4 * (g & 1) + (q4 & 1)) * 80 + (r & 1) * 8;
        SFOR(b, 8)
            VMW((b < 5 ? 30 : 70 - 10 * b) + (b + 1 + WO_AH < 8 ? 5 : 0) + (b + 2 + WO_AH < 8 ? 5 : 0) + (b + 3 + WO_AH < 8 ? 5 : 0));
            if constexpr (b + WO_AH == 7) PIN(junk);
#pragma unroll
            for (int t = 0; t < 5; ++t) {
                lptr q = tb + (b & 3) * 10240 + t * 16;
                wo[b][t] = tr_frag(q, q + 160, q + 64 * 80, q + 64 * 80 + 160);
                if (b * 5 + t < 32) asm volatile("" : "+a"(wo[b][t])); else asm volatile("" : "+v"(wo[b][t]));
            }
            CFENCE();
            if constexpr (b < 4) fill(b + 4);
        SEND
    }
    if (j == 0) LST(265, rt32());
    LST(1100 + j * 4 + w, rt32());
    const u32 idx = (r & 2) ? 0xEEEEu : 0x4444u;
    const u32 pol0 = pol_wait(rs, ep);
    BAR();
    const u32 l16 = (u32)lane * 16u;
    u32 oo[8];
#pragma unroll
    for (int i = 0; i < 8; ++i) oo[i] = uu(ring_map((u32)(rg * 32768 + (8 * w + ((i + rot) & 7)) * 1024)));
    const u32 hint = (pol0 ^ 16384u) + 4 * 65536u;
    u32 hvo[2];
#pragma unroll
    for (int l = 0; l < 2; ++l) hvo[l] = lane < 2 ? hint + (u32)(l ^ 1) * MB + (u32)lane * 4u : lane == 2 ? hint + (u32)l * MB + 128u : lane == 3 ? foff(F_ERR) : lane == 4 ? hint + (u32)l * MB + 256u :
                 (lane >= 32 && j == 0 && w == 3) ? hint + (u32)l * MB + (u32)lane * 128u : OOB;
    lptr xw = r < 8 ? lds + w * 640 + (2 * (g & 1) + (g >> 1)) * 32 + r * 4 : lds + 8192 + lane * 4;
    const int G = ((5 * cg) >> 1) + w, tt = 2 * G + ((lane >> 3) & 1) - 5 * cg;
    const bool pub = w < 3 && lane < 32 && tt >= 0 && tt < 5;
    lptr xr = lds + (pub ? tt * 128 + (2 * (lane >> 4) + ((lane >> 2) & 1)) * 32 + (lane & 3) * 8 : 0);
    const u32 mpo = pub ? (pol0 ^ 16384u) + ring_map((u32)(G * 256 + rg * 128 + lane * 4)) : OOB;
    constexpr u32 PER0 = CH_W0 + CH_MW + CH_H0 + CH_MH + 2 * CH_VIS, DW0 = CH_W0 + CH_VIS + CH_MW + CH_REC;
    const bool lead = j == 0 && w == 3;
    const u32 hso = (lead && lane == 0) ? hint : OOB;
    MK_SRD();
    u32 t = 0, tn = 0;
    int sn[2] = {0, 0};
    u32 hav = 8 * CH_H0, wav = 8 * CH_W0, mh = 8 * CH_MH, mw = 8 * CH_MW, ok[2] = {0u, 0u}, dw[2] = {DW0, CH_T0 + CH_REC - 8};
    u32 vo = l16 + pol0 + MB, tp = 0, fin, left, cnt = 0;
    unsigned long long clk = 0;
    {
        if (w == 0) LST(1310 + j, rt32());
        if (lead) {
#pragma unroll 1
            for (int n = 0; n < SPIN_CAP; ++n) {
                CFENCE();
                const u32 a = ld4(rs, foff(F_QKV + lane)), b = ld4(rs, lane < 32 ? foff(F_QKV + 64 + lane) : hvo[0]);
                if (all_(a != 0u && b != (lane < 32 ? 0u : PSENT))) break;
                __builtin_amdgcn_s_sleep(1);
            }
        }
        if (lead) {
            t = rt32() + CH_T0;
            st16(rs, hso, (v4u){t, PER0, t + DW0, t + DW0});
        } else {
#pragma unroll 1
            for (int n = 0; n < SPIN_CAP; ++n) {
                CFENCE();
                const u32 hv = ld4(rs, hvo[1]);
                t = rdl(hv, 0);
                if (rdl(hv, 1) < 400u) break;
                __builtin_amdgcn_s_sleep(1);
            }
        }
    }
    const u32 tm0 = t + DW0;
    tn = t; t -= CH_T0; clk = t - 100u;
    u32 zero = 0, hv = 0xFFFFu, pst = 1, pmp = OOB;
    const u32 psent = PSENT;
    lptr sw = lds + 2560 + w * 4;
    asm volatile("" : "+v"(zero), "+v"(sw));
    auto turn = [&](auto lc) __attribute__((always_inline)) {
        constexpr int L = decltype(lc)::value, M = L ^ 1;
        u32 done, rep, rd, lost, dh, dn, nt, za, pdur, mp; v4u hw;
        const int sc = sn[L];
        auto books = [&](auto ic) __attribute__((always_inline)) {
            constexpr int i = decltype(ic)::value;
            if constexpr (i == 0) { rep = max(rdl(hv, 2), rdl(hv, 4)); PINS(rep); }
            if constexpr (i == 1) { lost = (u32)(__builtin_amdgcn_ballot_w64(hv == (u32)sn[M]) >> 32); PINS(lost); }
            if constexpr (i == 2) { lost |= (rep >> 16) ^ (u32)sn[M]; PINS(lost); }
            if constexpr (i == 3) { lost |= ok[M] ^ 2u; rd = rep & 0xFFFFu; PINS(lost); PINS(rd); }
            if constexpr (i == 4) { lost = lost == 0u ? CH_UP * 8u : 0u; PINS(lost); }
            if constexpr (i == 5) { pdur = ((u32)clk - tp) * 8u; PINS(pdur); pdur = min(pdur, wav + CH_RISE); PINS(pdur); }
            if constexpr (i == 6) { pdur = (done & ok[M]) ? pdur : wav; PINS(pdur); }
            if constexpr (i == 7) { wav = max(pdur, wav - CH_FALL); PINS(wav); }
            if constexpr (i == 8) { nt = min(rd * 8u, hav + CH_RISE); PINS(nt); }
            if constexpr (i == 9) { nt = rd < 0x3FFFu ? nt : hav; PINS(nt); }
            if constexpr (i == 10) { hav = max(nt, hav - CH_FALL); PINS(hav); }
            if constexpr (i == 11) { mw = max(mw - 1u, CH_MW * 8u); PINS(mw); }
            if constexpr (i == 12) { mh = max(mh - 1u, CH_MH * 8u); PINS(mh); }
            if constexpr (i == 13) { rd = rd >> 14 == 1u ? lost : 0u; PINS(rd); }
            if constexpr (i == 14) { mw += rd; mh += lost - rd; PINS(mw); PINS(mh); }
            if constexpr (i == 15) { ok[M] = done ? 1u : (ok[M] & 1u) << 1; PINS(ok[M]); }
            if constexpr (i == 16) { dh = (hav + mh) / 8u + (sn[1] < 3 ? CH_VIS + CH_PRE : CH_VIS); PINS(dh); }
            if constexpr (i == 17) { dn = (wav + mw) / 8u + (sn[1] < 3 ? CH_VIS + CH_PRW : CH_VIS); PINS(dn); }
            if constexpr (i == 18) { nt = dw[L] + dh + (u32)max((int)((u32)clk + CH_WX - t), 0); PINS(nt); }
            if constexpr (i == 19) { dh += dn; dw[L] = dn; sn[M] += (int)done; PINS(dh); PINS(sn[M]); }
            if constexpr (i == 20) { rd = tn - t + (dh >> 1); PINS(rd); }
            if constexpr (i == 21) { lost = (u32)sc - 1u < NSEG - 2 ? CH_RTY : 0u; PINS(lost); nt += ok[L] != 1u ? lost : 0u; PINS(nt); }
            if constexpr (i == 22) { nt = t + (u32)max((int)nt, (int)rd); PINS(nt); }
            if constexpr (i == 23) { hw.x = nt; hw.y = dh; PIN(hw.x); PIN(hw.y); }
            if constexpr (i == 24) { hw.z = nt + dn; hw.w = tm0; PIN(hw); }
            if constexpr (i == 25) st16(rs, hso + L * MB, hw);
        };
        {
#define WT(k) do {} while (0)
            v4u d[8];
            f32x4 acc[5] = {};
            done = uu(pst) ^ 1u; PINS(done);
            za = pmp | pst << 31;
            *(LAS u32*)(sw + L * 4096) = vo >> 31;
            mp = mpo + (L * MB + (u32)((sc + 1) & 3) * 65536u); pmp = mpo + (L * MB + (u32)(sc & 3) * 65536u);
            asm volatile("" : "+v"(acc[0]), "+v"(acc[1]), "+v"(acc[2]), "+v"(acc[3]), "+v"(acc[4]), "+v"(mp), "+v"(pmp), "+v"(za));
            {
                u32 tmp;
                asm volatile(WAIT_TXT("%1", "%2") "buffer_load_dwordx4 %0, %3, %4, %5 offen sc1\n\ts_waitcnt lgkmcnt(0)"
                             : "=&v"(d[0]), "=&s"(tmp) : "s"(uu(t - 4u)), "v"(vo), "s"(srd), "s"(oo[0]) : "vcc", "scc", "memory");
                PINS(clk);
                WT(0);
            }
            SFOR(i, 7)
                asm volatile("buffer_load_dwordx4 %0, %1, %2, %3 offen sc1" : "=v"(d[i + 1]) : "v"(vo), "s"(srd), "s"(oo[i + 1]) : "memory");
                SCHED();
                static_for<(25 * i) / 7, (25 * (i + 1)) / 7>(books);
                SCHED();
            SEND
            WT(1);
            float f0[5], f1[5]; unsigned long long bad;
#define WSW(o_) asm volatile("v_permlane32_swap_b32 %0, %1" : "+v"(f0[o_]), "+v"(f1[o_]))
#define WAD(o_) asm volatile("v_add_f32_dpp %0, %1, %0 row_ror:8 row_mask:0xf bank_mask:0xf" : "+v"(f0[o_]) : "v"(f1[o_]))
#define WFO(o_) do { VADD(f0[o_], acc[o_][0], acc[o_][2]); VADD(f1[o_], acc[o_][1], acc[o_][3]); } while (0)
#define WWR(o_) *(LAS float*)(xw + L * 4096 + (o_) * 128) = f0[o_]
            u32 th, per, so;
            auto next = [&](auto ic_) __attribute__((always_inline)) {
                constexpr int i = decltype(ic_)::value;
                if constexpr (i == 0) { so = (u32)(sn[M] & 3) * 0x42000u; PINS(so); }
                if constexpr (i == 1) { so = (so & 0xC4000u) ^ (pol0 + M * MB); PINS(so); }
                if constexpr (i == 2) { so = sn[M] + 1 < NSEG ? so : OOB; PINS(so); }
                if constexpr (i == 4) { vo = l16 + so; PIN(vo); }
                if constexpr (i == 5 && !L) { fin = (u32)(sn[0] + sn[1] + 2); PINS(fin); }
                if constexpr (i == 6 && L == 0) { fin = (fin | uu(left)) >> 7; PIN(fin); }
                if constexpr (i == 6 && L == 1) { cnt += 1u; PIN(cnt); left = cnt >> 9; PIN(left); }
                if constexpr (i == 7) { PIN(hv); th = rdl(hv, 0); PINS(th); }
                if constexpr (i == 8) { per = rdl(hv, 1); PINS(per); so = th - t; PINS(so); }
                if constexpr (i == 9) { per = (int)so < 20 ? per : 0u; PINS(per); }
                if constexpr (i == 10) { th += per; PINS(th); if constexpr (!L) { per = rdl(hv, 3); PINS(per); } }
                if constexpr (i == 11) { tp = t; t = lead ? tn : th; PINS(t); }
                if constexpr (i == 12) { if constexpr (!L) { fin |= per; PIN(fin); } tn = nt; }
            };
            auto slot = [&](auto ic_) __attribute__((always_inline)) {
                constexpr int i = decltype(ic_)::value;
                constexpr int NL = WO_NL, I0 = 40 - 5 * NL, pc = i < I0 ? i / 5 : 8 - NL + (i - I0) % NL, oc = i < I0 ? i % 5 : (i - I0) / NL;
                f32x4& ac = acc[oc]; v4u& dp = d[pc]; v8u& wp = wo[pc][oc];
                if constexpr (oc == 0) {
                    if constexpr (pc * 5 < 32) asm volatile("s_waitcnt vmcnt(%4)\n\tv_smfmac_f32_16x16x64_bf16 %0, %1, %2, %3" : "+v"(ac), "+v"(dp) : "a"(wp), "v"(idx), "i"(pc < 2 ? 7 - pc : 9 - pc));
                    else asm volatile("s_waitcnt vmcnt(%4)\n\tv_smfmac_f32_16x16x64_bf16 %0, %1, %2, %3" : "+v"(ac), "+v"(dp) : "v"(wp), "v"(idx), "i"(9 - pc));
                } else if constexpr (pc * 5 + oc < 32) SMF_A(ac, dp, wp); else SMF_V(ac, dp, wp);
                SCHED();
                if constexpr (i == 0) WT(2);
                if constexpr (i < 25 && oc == 0) LGK(15);
                if constexpr (i == 6) { u32& h_ = hv; (void)&srd; LD4A(h_, hvo[L], 0); }
                if constexpr (i == 8) books(IC<25>{});
                if constexpr (i >= 11 && i < 19 && i != 15) next(IC<i - 11 - (i > 15)>{});
                if constexpr (i == 20) WT(4);
                constexpr int e = i - (I0 + NL + 1), o = e < 0 ? 0 : e / 3;
                if constexpr (e >= 0 && e % 3 == 0) { CADD(f0[o], acc[o][0], acc[o][2]); if constexpr (o > 0) WAD(o - 1); }
                if constexpr (e >= 0 && e % 3 == 1) { if constexpr (o > 0) { WWR(o - 1); SCHED(); } CADD(f1[o], acc[o][1], acc[o][3]); }
                if constexpr (e >= 0 && e % 3 == 2) {
                    if constexpr (o == 0) asm volatile("v_cmp_u_f32 %0, %1, %2" : "=s"(bad) : "v"(f0[0]), "v"(f1[0]));
                    else LGK(15);
                    WSW(o);
                }
                SCHED();
            };
            WT(3);
            static_for<0, 40>(slot);
            WT(5);
            static_assert(WO_NL == 3, "tail");
            if (__builtin_expect(bad != 0ull, 0)) {
                bool miss = false;
#pragma unroll
                for (int i = 0; i < 8; ++i) miss |= d[i].x == PSENT;
                if (all_(!miss)) {
#pragma unroll
                    for (int i = 0; i < 8; ++i) miss |= d[i].y == PSENT || d[i].z == PSENT || d[i].w == PSENT;
                }
                if (miss) *(LAS u32*)(sw + L * 4096) = 1u;
            }
            asm volatile("v_permlane32_swap_b32 %0, %1\n\ts_nop 1\n\tv_add_f32_dpp %0, %1, %0 row_ror:8 row_mask:0xf bank_mask:0xf\n\tv_add_f32 %2, %4, %6\n\tv_add_f32 %3, %5, %7"
                         : "+v"(f0[3]), "+v"(f1[3]), "=&v"(f0[4]), "=&v"(f1[4]) : "v"(acc[4][0]), "v"(acc[4][1]), "v"(acc[4][2]), "v"(acc[4][3]));
            WWR(3); SCHED();
            asm volatile("s_nop 0\n\tv_permlane32_swap_b32 %0, %1\n\ts_nop 1\n\tv_add_f32_dpp %0, %1, %0 row_ror:8 row_mask:0xf bank_mask:0xf" : "+v"(f0[4]), "+v"(f1[4]));
            WWR(4);
            WT(6);
            f32x2 a, b, c, e; u32 mq; v4u sv;
            asm volatile("s_waitcnt lgkmcnt(0)\n\ts_barrier\n\tds_read_b128 %4, %6 offset:%7+2560\n\t"
                         "ds_read_b64 %0, %5 offset:%7\n\tds_read_b64 %1, %5 offset:%7+640\n\tds_read_b64 %2, %5 offset:%7+1280\n\tds_read_b64 %3, %5 offset:%7+1920"
                         : "=&v"(a), "=&v"(b), "=&v"(c), "=&v"(e), "=&v"(sv) : "v"(xr), "v"(zero), "i"(L * 4096) : "memory");
            asm volatile("s_waitcnt lgkmcnt(4)\n\tv_or_b32 %4, %6, %7\n\tv_or3_b32 %4, %4, %8, %9\n\tv_lshl_or_b32 %5, %4, 31, %10\n\t"
                         "s_waitcnt lgkmcnt(2)\n\tv_pk_add_f32 %0, %0, %1\n\ts_waitcnt lgkmcnt(0)\n\tv_pk_add_f32 %2, %2, %3\n\ts_nop 0\n\tv_pk_add_f32 %0, %0, %2"
                         : "+v"(a), "+v"(b), "+v"(c), "+v"(e), "=&v"(pst), "=&v"(mq) : "v"(sv.x), "v"(sv.y), "v"(sv.z), "v"(sv.w), "v"(mp) : "memory");
            st4s(rs, mq, 0, pkv(a));
            SCHED();
            asm volatile("s_memrealtime %0" : "=s"(clk));
            WT(7);
            st4s(rs, za, 0, psent);
            VMW(3); SCHED();
            static_for<7, 13>(next);
        }
    };
    do {
        turn(IC<1>{});
        turn(IC<0>{});
    } while (uu(fin) == 0u);
    if (sn[0] + 1 < NSEG || sn[1] + 1 < NSEG) st4s(rs, tid == 0 ? foff(F_ERR) : OOB, 0, 1u);
    if (w == 0) LST(1900 + j, rt32());
    LDUMP(w == 0, 224 + j);
}

__global__ void __launch_bounds__(256, 1) k_main(Params p) {
    lptr lds = (lptr)(u32)16 - 16;
    if (blockIdx.x == 0) LST(256, rt32());
    unsigned long long pc; int len;
    asm volatile("s_getpc_b64 %0\n.Lk_main_here:\n\ts_mov_b32 %1, .Lk_end_here-.Lk_main_here" : "=s"(pc), "=s"(len));
    const rsrc_t rs = mk_rsrc(p.ws, W_END);
    const v2u e0 = ld8s(rs, threadIdx.x == 0 ? foff(F_EPOCH) : OOB, 0);
    const v4u code = __builtin_amdgcn_raw_buffer_load_b128(mk_rsrc((const void*)(pc & ~15ull), (u32)max(len, 0)), (((blockIdx.x >> 3) & 31u) * 256u + threadIdx.x) * 16u, 0, 0);
    CFENCE();
    auto conv_id = [](int role) {
        const int x = role >> 5, i = role & 31, no = x < 5 ? 5 : 10, og = x < 5 ? x - 3 : 2 * (x - 4) + (i >= 5), r = (x - 3) * 5 + i - no, q = r / 3;
        return x < 3 || i >= no + 5 || r >= 24 ? NCV : i < no ? ((og & 1) ? (og >> 1) * 5 + i % 5 : NCV) : r % 3 == 0 ? 20 + (q & 1) * 20 + (q >> 1) : 16 + (r % 3) * 8 + q;
    };
    int role = (int)(((u32)__builtin_amdgcn_s_getreg(20 | (3 << 11)) & 7u) * 32u + (blockIdx.x >> 3));
    Early e;
    v4u rv;
#pragma unroll 1
    for (int n = 0;; ++n) {
        conv_early(p, rs, conv_id(role), e);
        if (n) break;
        CFENCE();
        if (threadIdx.x == 0) {
            GAS unsigned long long* slots = (GAS unsigned long long*)(p.ws + W_CTRL + F_SLOT * 128);
            const unsigned long long tag = (unsigned long long)e0.y << 32 | (e0.x + 1u);
            u32 mine = (u32)role;
#pragma unroll 1
            for (int k = 0; k < 256 && __hip_atomic_exchange(slots + mine * 16, tag, __ATOMIC_RELAXED, __HIP_MEMORY_SCOPE_AGENT) == tag; ++k) mine = (mine + 1u) & 255u;
            lds_w16(lds, (v4u){mine, e0.x, e0.y, 0u});
        }
        BAR();
        rv = lds_r16(lds);
        BAR();
        if (uni((int)rv.x) == role) break;
        role = uni((int)rv.x);
    }
    const int x = role >> 5, i = role & 31, w = uni(threadIdx.x >> 6), no = x < 5 ? 5 : 10, nr = x < 7 ? 5 : 4, j = conv_id(role);
    const v2u ep = {uu(rv.y), uu(rv.z)};
    asm volatile("" :: "v"(code));
    if (x == 1) { role_wo(p, lds, i, ep); __builtin_amdgcn_endpgm(); }
    if (x < 3) { role_head(p, lds, i >> 4, x * 8 + (i & 15), ep); __builtin_amdgcn_endpgm(); }
    if (i >= no + nr) { role_qkv(p, lds, (x < 5 ? (x - 3) * 22 : 44 + (x - 5) * 17) + i - no - nr, ep); __builtin_amdgcn_endpgm(); }
    if (j < NCV) conv_stage(p, rs, lds, j, ep, e);
    if (i >= no) {
        if (j >= 24 && j < NPRE) role_row(p, lds, (j - 24) * 4 + w, ep);
        __builtin_amdgcn_endpgm();
    }
    const int og = x < 5 ? x - 3 : 2 * (x - 4) + (i >= 5);
    role_out(p, lds, og & 1, og >> 1, i % 5, ep);
}
__global__ void k_end() { asm volatile(".Lk_end_here:"); }

extern "C" void kernel_launch(void* const* d_in, const int* in_sizes, int n_in, void* d_out, int out_size, void* d_ws, size_t ws_size,
                              hipStream_t stream) {
    Params p;
    p.x = (const u16*)d_in[0]; p.mem0 = (const u16*)d_in[1]; p.Wq = (const u16*)d_in[2]; p.Wk = (const u16*)d_in[3];
    p.Wv = (const u16*)d_in[4]; p.Wo = (const u16*)d_in[5];
    p.bk = (const u16*)d_in[8]; p.bv = (const u16*)d_in[9]; p.shift = (const u16*)d_in[10]; p.ssmk = (const u16*)d_in[11]; p.ssmD = (const u16*)d_in[12];
    p.out = (u16*)d_out;
    p.ws = (unsigned char*)(((size_t)d_ws + 2 * MB - 1) & ~(size_t)(2 * MB - 1));
    (void)hipFuncSetAttribute((const void*)k_main, hipFuncAttributeMaxDynamicSharedMemorySize, LDS_BYTES);
    hipLaunchKernelGGL(k_main, dim3(256), dim3(256), LDS_BYTES, stream, p);
}
```

```cpp
#include <hip/hip_runtime.h>
#include <hip/hip_bf16.h>

typedef unsigned short u16;
typedef unsigned int u32;
typedef u32 v2u __attribute__((ext_vector_type(2)));
typedef u32 v4u __attribute__((ext_vector_type(4)));
typedef u32 v8u __attribute__((ext_vector_type(8)));
typedef float f32x2 __attribute__((ext_vector_type(2)));
typedef float f32x4 __attribute__((ext_vector_type(4)));
typedef float f32x16 __attribute__((ext_vector_type(16)));
typedef __bf16 bf16x2 __attribute__((ext_vector_type(2)));
typedef __bf16 bf16x8 __attribute__((ext_vector_type(8)));
typedef short s16x4 __attribute__((ext_vector_type(4)));
#define LAS __attribute__((address_space(3)))
#define GAS __attribute__((address_space(1)))
typedef LAS unsigned char* lptr;
#define DEV __device__ __forceinline__

constexpr int SEQ = 2048, DM = 640, NH = 32, DH = 128, NSEG = 64, NQ = NH * DH;

constexpr u32 MB = 1u << 20;
constexpr u32 W_MEM0 = 2 * MB;
constexpr u32 W_CTRL = 4 * MB;
constexpr u32 CTRL_BYTES = 128 * 1024;
constexpr u32 ACT = SEQ * DM * 2;
constexpr u32 W_H1 = 5 * MB, W_Y = W_H1 + ACT, W_H2 = W_Y + ACT, W_X2 = W_H2 + ACT;
constexpr u32 W_KQ = 16 * MB;
constexpr u32 W_VO = W_KQ + 64 * MB;
constexpr u32 W_P = W_VO + 64 * MB;
constexpr u32 P_ITEM = 16 * 4 * DM * 4;
constexpr u32 W_KVT = W_P + 4 * NSEG * P_ITEM;
constexpr u32 W_SP = W_KVT + ACT;
constexpr u32 W_END = W_KVT + 3 * MB;
static_assert((size_t)W_END + 2 * MB <= 256ull * MB, "workspace");
constexpr u32 F_ERR = 0, F_QKV = 16, F_O = F_QKV + 192, F_P = F_O + 64, F_H2 = F_P + 64, F_CLR = F_H2 + 32,
              F_EPOCH = F_CLR + 32, F_SLOT = F_EPOCH + 1, F_PRE = F_SLOT + 256, F_N = F_PRE + 256;
constexpr int NCV = 44, NPRE = 40;
static_assert(F_N * 128 <= CTRL_BYTES, "ctrl");
DEV u32 foff(u32 idx) { return W_CTRL + idx * 128u; }

constexpr u32 OOB = 0x80000000u;
constexpr u32 PSENT = 0x7F817F81u;
constexpr int SPIN_CAP = 40000;
constexpr int LDS_BYTES = 160 * 1024;

#define LDUMP(cond, id) do {} while (0)
#define LMX(cond, k, s_, v) do {} while (0)
#define LS3(cond, slot, idx) do {} while (0)
#define LST(idx, v) do {} while (0)
#define LS2(cond, idx) do {} while (0)
struct Params {
    const u16 *x, *mem0, *Wq, *Wk, *Wv, *Wo, *bk, *bv, *shift, *ssmk, *ssmD;
    u16* out;
    unsigned char* ws;
};

#define INAUX 2
#define INNT "nt"
typedef __amdgpu_buffer_rsrc_t rsrc_t;
DEV rsrc_t mk_rsrc(const void* p, u32 bytes) { return __builtin_amdgcn_make_buffer_rsrc((void*)p, 0, bytes, 0x00020000); }
DEV v4u ld16s(rsrc_t r, u32 off, u32 so) { return __builtin_amdgcn_raw_buffer_load_b128(r, off, so, 16); }
DEV v2u ld8s(rsrc_t r, u32 off, u32 so) { return __builtin_amdgcn_raw_buffer_load_b64(r, off, so, 16); }
DEV v4u ldin16(rsrc_t r, u32 off, u32 so) { return __builtin_amdgcn_raw_buffer_load_b128(r, off, so, INAUX); }
DEV v2u ldin8(rsrc_t r, u32 off, u32 so) { return __builtin_amdgcn_raw_buffer_load_b64(r, off, so, INAUX); }
DEV u32 ld4(rsrc_t r, u32 off) { return __builtin_amdgcn_raw_buffer_load_b32(r, off, 0, 16); }
DEV void st16(rsrc_t r, u32 off, v4u v) { __builtin_amdgcn_raw_buffer_store_b128(v, r, off, 0, 16); }
DEV void st8s(rsrc_t r, u32 off, u32 so, v2u v) { __builtin_amdgcn_raw_buffer_store_b64(v, r, off, so, 16); }
DEV void st4s(rsrc_t r, u32 off, u32 so, u32 v) { __builtin_amdgcn_raw_buffer_store_b32(v, r, off, so, 16); }
#define DRAIN() asm volatile("s_waitcnt vmcnt(0)" ::: "memory")
#define VMW(n) asm volatile("s_waitcnt vmcnt(%0)" :: "i"(n) : "memory")
#define BAR() asm volatile("s_waitcnt lgkmcnt(0)\n\ts_barrier" ::: "memory")
#define SCHED() __builtin_amdgcn_sched_barrier(0)
#define CFENCE() asm volatile("" ::: "memory")
#define NOPS(n) asm volatile("s_nop %0" :: "i"(n))
#define PIN(x) asm volatile("" : "+v"(x))
#define PINS(x) asm volatile("" : "+s"(x))

template <int V> struct IC { static constexpr int value = V; };
template <int I, int N, class F> DEV void static_for(F&& f) { if constexpr (I < N) { f(IC<I>{}); static_for<I + 1, N>(f); } }
#define SFOR(var, n) static_for<0, n>([&](auto ic_) __attribute__((always_inline)) { constexpr int var = decltype(ic_)::value;
#define SEND });

DEV u32 pk(float a, float b) { bf16x2 v = {(__bf16)a, (__bf16)b}; return __builtin_bit_cast(u32, v); }
DEV float lo_f(u32 u) { return __builtin_bit_cast(float, u << 16); }
DEV float hi_f(u32 u) { return __builtin_bit_cast(float, u & 0xffff0000u); }
DEV f32x2 up2(u32 u) { return (f32x2){lo_f(u), hi_f(u)}; }
DEV u32 pk2(f32x2 v) { return pk(v.x, v.y); }
DEV float rbf(float a) { return (float)(__bf16)a; }
DEV v4u lds_r16(lptr p) { return *(LAS v4u*)p; }
DEV void lds_w16(lptr p, v4u v) { *(LAS v4u*)p = v; }
DEV v2u lds_tr(lptr p) { return __builtin_bit_cast(v2u, __builtin_amdgcn_ds_read_tr16_b64_v4i16((LAS s16x4*)p)); }
DEV v4u tr2(lptr p0, lptr p1) { const v2u a = lds_tr(p0), b = lds_tr(p1); return (v4u){a.x, a.y, b.x, b.y}; }
DEV v8u cat8(v4u a, v4u b) { return __builtin_shufflevector(a, b, 0, 1, 2, 3, 4, 5, 6, 7); }
DEV f32x4 mfma16(v4u a, v4u b, f32x4 c) { return __builtin_amdgcn_mfma_f32_16x16x32_bf16(__builtin_bit_cast(bf16x8, a), __builtin_bit_cast(bf16x8, b), c, 0, 0, 0); }
DEV f32x16 mfma32(v4u a, v4u b, f32x16 c) { return __builtin_amdgcn_mfma_f32_32x32x16_bf16(__builtin_bit_cast(bf16x8, a), __builtin_bit_cast(bf16x8, b), c, 0, 0, 0); }

DEV float f_(u32 u) { return __builtin_bit_cast(float, u); }
DEV u32 u_(float f) { return __builtin_bit_cast(u32, f); }
template <int CTRL> DEV float dpp(float v) { return f_((u32)__builtin_amdgcn_update_dpp(0, (int)u_(v), CTRL, 0xf, 0xf, false)); }
DEV float max16(float v) { auto r = __builtin_amdgcn_permlane16_swap(u_(v), u_(v), false, false); return fmaxf(f_(r[0]), f_(r[1])); }
DEV float max32(float v) { auto r = __builtin_amdgcn_permlane32_swap(u_(v), u_(v), false, false); return fmaxf(f_(r[0]), f_(r[1])); }
DEV float add16(float v) { auto r = __builtin_amdgcn_permlane16_swap(u_(v), u_(v), false, false); return f_(r[0]) + f_(r[1]); }
DEV float add32(float v) { auto r = __builtin_amdgcn_permlane32_swap(u_(v), u_(v), false, false); return f_(r[0]) + f_(r[1]); }
DEV float wave_sum(float v) {
    v += dpp<0xB1>(v);
    v += dpp<0x4E>(v);
    v += dpp<0x124>(v);
    v += dpp<0x128>(v);
    return add32(add16(v));
}
DEV bool all_(bool c) { return __builtin_amdgcn_ballot_w64(!c) == 0ull; }
DEV int uni(int v) { return __builtin_amdgcn_readfirstlane(v); }
DEV u32 uu(u32 v) { return (u32)__builtin_amdgcn_readfirstlane((int)v); }
DEV u32 rdl(u32 v, int l) { return (u32)__builtin_amdgcn_readlane((int)v, l); }
DEV u32 rt32() { return (u32)__builtin_amdgcn_s_memrealtime(); }

#define WSL 2
DEV void wait_flags(rsrc_t rs, u32 off, u32 want, bool& dead) {
    if (dead) return;
#pragma unroll 1
    for (int n = 0;; ++n) {
        CFENCE();
        const u32 v = ld4(rs, off);
        if (all_(off >= OOB || v >= want)) break;
        __builtin_amdgcn_s_sleep(WSL);
        if ((n & 63) == 63) { if (uni((int)ld4(rs, foff(F_ERR))) != 0) { dead = true; break; } }
        if (n > SPIN_CAP) { st4s(rs, foff(F_ERR), 0, 1u); dead = true; break; }
    }
    CFENCE();
}

DEV void pre_post(rsrc_t rs, int idx, u32 stage, v2u ep) { st8s(rs, threadIdx.x == 0 ? foff(F_PRE + idx) : OOB, 0, (v2u){ep.x + stage, ep.y}); }
DEV void pre_poll(rsrc_t rs, int first, int n, u32 stage, v2u ep) {
    const int lane = threadIdx.x & 63;
#pragma unroll 1
    for (int it = 0; it < SPIN_CAP; ++it) {
        CFENCE();
        const v2u a = ld8s(rs, foff(F_PRE + first + min(lane, n - 1)), 0), b = ld8s(rs, foff(F_PRE + first + min(lane + 64, n - 1)), 0);
        if (all_(a.y == ep.y && b.y == ep.y && max(a.x - ep.x - stage, b.x - ep.x - stage) < 8u - stage)) break;
        __builtin_amdgcn_s_sleep(2);
    }
    CFENCE();
}
DEV void pre_wait(rsrc_t rs, int first, int n, u32 stage, v2u ep) {
    if (uni(threadIdx.x >> 6) == 0) pre_poll(rs, first, n, stage, ep);
    BAR();
}
#define POLX 0u
DEV u32 pol_wait(rsrc_t rs, v2u ep) {
    const int lane = threadIdx.x & 63;
    v4u a;
#pragma unroll 1
    for (int it = 0; it < SPIN_CAP; ++it) {
        CFENCE();
        a = ld16s(rs, foff(F_PRE + (lane < NPRE || lane >= 48 ? lane : 0)), 0);
        if (all_(a.y == ep.y && a.x - ep.x - 2u < 6u)) break;
        __builtin_amdgcn_s_sleep(2);
    }
    CFENCE();
    return uu((wave_sum(lane >= 48 ? (float)(int)a.z : 0.f) >= 0.f ? 8192u : 0u) + (wave_sum(lane >= 48 ? (float)(int)a.w : 0.f) >= 0.f ? 16384u : 0u)) ^ POLX;
}
#define PROBE_C 7
#define PROBE_T 620
DEV v2u pol_probe(rsrc_t rs, int id) {
    const int lane = threadIdx.x & 63;
    const u32 off0 = (u32)id * 32768u, tend = rt32() + PROBE_T;
    st4s(rs, lane < 32 ? off0 + (u32)lane * 1024u : OOB, 0, PSENT); DRAIN();
    int d = 1 << 16, calm = 0;
#pragma unroll 1
    for (int q = 0; q < 32 && ((q & 3) || (calm < PROBE_C && (int)(rt32() - tend) < 0)); ++q) {
        const int kind = (q ^ (q >> 2) ^ id) & 3;
        const unsigned long long t0 = __builtin_amdgcn_s_memtime();
        const u32 v = ld4(rs, lane == 0 ? off0 + (u32)(kind * 8192 + (q >> 2) * 1024) : OOB);
        asm volatile("s_waitcnt vmcnt(0)" :: "v"(v) : "memory");
        const int dt = (int)(__builtin_amdgcn_s_memtime() - t0);
        d = lane == kind ? min(d, dt) : d;
        calm += dt < 1100;
    }
    const int d0 = (int)rdl(d, 0), d1 = (int)rdl(d, 1), d2 = (int)rdl(d, 2), d3 = (int)rdl(d, 3), a = min(d0, d2) - min(d1, d3);
    const int b0 = min(max(d0 - d2, -80), 80), b1 = min(max(d1 - d3, -80), 80);
    const u32 xcc = (u32)__builtin_amdgcn_s_getreg(20 | (3 << 11));
    return (v2u){(u32)min(max((xcc & 4u) ? -a : a, -250), 250), (u32)((xcc & 1u) ? b0 + b1 : a > 0 ? b0 - b1 : b1 - b0)};
}
DEV f32x2 ln_stats(const f32x2 (&x)[5]) {
    const float c = f_(uu(u_(x[0].x)));
    f32x2 d[5];
#pragma unroll
    for (int i = 0; i < 5; ++i) d[i] = x[i] - c;
    const f32x2 s2 = (d[0] + d[1]) + (d[2] + d[3]) + d[4], q2 = (d[0] * d[0] + d[1] * d[1]) + (d[2] * d[2] + d[3] * d[3] + d[4] * d[4]);
    float a = s2.x + s2.y, b = q2.x + q2.y, a2, b2;
#define LN_DPP(m) "v_add_f32_dpp %0, %0, %0 " m " row_mask:0xf bank_mask:0xf\n\tv_add_f32_dpp %1, %1, %1 " m " row_mask:0xf bank_mask:0xf\n\ts_nop 0\n\t"
    asm("s_nop 1\n\t" LN_DPP("quad_perm:[1,0,3,2]") LN_DPP("quad_perm:[2,3,0,1]") LN_DPP("row_ror:4")
        "v_add_f32_dpp %0, %0, %0 row_ror:8 row_mask:0xf bank_mask:0xf\n\tv_add_f32_dpp %1, %1, %1 row_ror:8 row_mask:0xf bank_mask:0xf\n\ts_nop 1\n\t"
        "v_permlane16_swap_b32 %0, %1\n\tv_add_f32 %0, %0, %1\n\tv_mov_b32 %1, %0\n\ts_nop 1\n\tv_permlane32_swap_b32 %0, %1\n\tv_add_f32 %0, %0, %1\n\ts_nop 0\n\t"
        "v_readlane_b32 %2, %0, 0\n\tv_readlane_b32 %3, %0, 16\n\ts_nop 1"
        : "+v"(a), "+v"(b), "=s"(a2), "=s"(b2));
    const float m = a2 * (1.0f / DM), var = fmaxf(b2 * (1.0f / DM) - m * m, 0.f);
    const float sd = rbf(__builtin_amdgcn_sqrtf(rbf(rbf(var) + 1e-5f)));
    return (f32x2){rbf(c + m), __builtin_amdgcn_rcpf(sd)};
}
DEV u32 pkv(f32x2 v) { u32 d; asm("v_cvt_pk_bf16_f32 %0, %1, %2" : "=v"(d) : "v"(v.x), "v"(v.y)); return d; }
DEV void store_h(rsrc_t rs, u32 base, int row, const u32 (&hp)[5]) {
    const u32 lane = threadIdx.x & 63;
    const u32 sw = (u32)(row & 15) * 16u, o8 = (lane >> 5) * 8192u + (((lane & 31u) * 8u) ^ sw), hb = base + (u32)(row >> 5) * (32 * DM * 2) + (u32)(row & 31) * 256u;
    st8s(rs, o8, hb, (v2u){hp[0], hp[1]});
    st8s(rs, o8, hb + 16384, (v2u){hp[2], hp[3]});
    st4s(rs, (lane * 4) ^ sw, hb + 32768, hp[4]);
}

#define DMA16(ldsb, vo, so) asm volatile("s_mov_b32 m0, %0\n\ts_nop 0\n\tbuffer_load_dwordx4 %1, %2, %3 offen sc1 lds" :: "s"(ldsb), "v"(vo), "s"(srd), "s"(so) : "memory")
#define DMA16I(ldsb, lo, vo, so, go) do { u32 so_; asm volatile("s_add_u32 m0, %1, %5\n\ts_add_u32 %0, %4, %6\n\tbuffer_load_dwordx4 %2, %3, %0 offen sc1 lds" \
                         : "=&s"(so_) : "s"(ldsb), "v"(vo), "s"(srd), "s"(so), "i"(lo), "i"(go) : "memory", "scc"); } while (0)
#define DMA32(ldsb, vo, so) asm volatile("s_mov_b32 m0, %0\n\ts_nop 0\n\tbuffer_load_dwordx4 %1, %2, %3 offen sc1 lds\n\tbuffer_load_dwordx4 %1, %2, %3 offen offset:1024 sc1 lds" \
                         :: "s"(ldsb), "v"(vo), "s"(srd), "s"(so) : "memory")
#define DMAIN(sr, ldsb, vo, so) asm volatile("s_mov_b32 m0, %0\n\ts_nop 0\n\tbuffer_load_dwordx4 %1, %2, %3 offen lds" :: "s"(ldsb), "v"(vo), "s"(sr), "s"(so) : "memory")
DEV v4u mk_srd(const void* q, u32 bytes) { return (v4u){uu((u32)(size_t)q), uu((u32)((size_t)q >> 32) & 0xffffu), bytes, 0x00020000u}; }
#define DMANT(sr, ldsb, vo, so) asm volatile("s_mov_b32 m0, %0\n\ts_nop 0\n\tbuffer_load_dwordx4 %1, %2, %3 offen " INNT " lds" :: "s"(ldsb), "v"(vo), "s"(sr), "s"(so) : "memory")
#define AHEAD(sr, vo, so) asm volatile("buffer_load_dwordx4 %0, %1, %2, %3 offen" : "+v"(junk) : "v"(vo), "s"(sr), "s"(so) : "memory")
#define MK_SRD() const v4u srd = mk_srd(p.ws, W_END)

constexpr int CV_W = 40960;
template <bool B> struct CvL {
    static constexpr int NF = B ? 5 : 3, NK = B ? 4 : 2, KP = 544 + NK * 256, CH = NF * 1024 + 4 * KP;
};
constexpr int CV_Y = 4 * CvL<false>::CH, CV_YP = 272;
static_assert(CV_Y + 32 * CV_YP <= CV_W && 2 * CvL<true>::CH <= CV_Y, "conv lds");
DEV int cv_chan(int u, int i) { return 16 * (u % 40) + 4 * i + u / 40; }
template <bool B> DEV void cv_filt(const Params& p, u32 dst, int d, int i) {
    const u32 lane = threadIdx.x & 63;
    const v4u sf = mk_srd(p.ssmk + d * SEQ, SEQ * 2);
    if (i == 0) DMANT(sf, dst, lane < 4u ? OOB : lane * 16u - 64u, 0);
    else DMANT(sf, uu(dst + i * 1024), i == CvL<B>::NF - 1 && lane >= 4u ? OOB : lane * 16u, i * 1024 - 64);
}
DEV v4u cv_kvld(rsrc_t rs, int d, int i) { return ld16s(rs, (threadIdx.x & 63) * 16u, uu(W_KVT + (u32)d * (SEQ * 2) + i * 1024)); }
template <bool B> DEV void cv_kvput(lptr F, const v4u* k) {
    const int lane = threadIdx.x & 63;
#pragma unroll
    for (int i = 0; i < CvL<B>::NK; ++i) lds_w16(F + CvL<B>::NF * 1024 + (lane & 3) * CvL<B>::KP + 512 + i * 256 + (lane >> 2) * 16, k[i]);
}
template <bool B> DEV void cv_zero(lptr F) {
    const int lane = threadIdx.x & 63;
#pragma unroll
    for (int i = 0; i < 4; ++i) lds_w16(F + (lane >> 5) * CvL<B>::CH + CvL<B>::NF * 1024 + i * CvL<B>::KP + (lane & 31) * 16, (v4u){0u, 0u, 0u, 0u});
}
#define M32V(d, a, b) asm volatile("v_mfma_f32_32x32x16_bf16 %0, %1, %2, %0" : "+v"(d) : "v"(a), "v"(b))
template <bool B, class G> DEV void cv_run(const Params& p, rsrc_t rs, lptr F, int d, u32 D, f32x4 sp, float (&y)[16], G&& gap) {
    constexpr int KP = CvL<B>::KP, FO = B ? 2048 : 0;
    const int lane = threadIdx.x & 63, n = lane & 31, hh = lane >> 5, c = lane & 3;
    lptr fa = F + 64 + (32 * hh + 16 * (c & 1) + 8 * ((lane >> 4) & 1) + 4 * (c >> 1) - (lane & 12)) * 2;
    lptr kl = F + CvL<B>::NF * 1024 + (32 + n - hh) * 16;
    v4u dg;
#pragma unroll
    for (int q = 0; q < 4; ++q) { const int k = (lane & 35) ? -1 : (n & 4) + (n >> 3); dg[q] = k == 2 * q ? D : k == 2 * q + 1 ? D << 16 : 0u; }
    v4u l[4], h[4]; v2u fc[4], fh[4];
    f32x16 acc[4];
#pragma unroll
    for (int j = 0; j < 4; ++j) { l[j] = lds_r16(kl + j * KP); if constexpr (B) h[j] = lds_r16(kl + j * KP + 512); }
#pragma unroll
    for (int j = 0; j < 2; ++j) { fc[j] = lds_tr(fa + FO - 32 * j); if constexpr (B) fh[j] = lds_tr(fa - 32 * j); }
    asm volatile("s_nop 1" : "+v"(dg));
#pragma unroll
    for (int j = 0; j < 4; ++j) asm volatile("v_mfma_f32_32x32x16_bf16 %0, %1, %2, 0" : "=&v"(acc[j]) : "v"(dg), "v"(B ? h[j] : l[j]));
    constexpr int U = B ? 2 : 4;
    kl -= 32 * U; fa -= 32;
    auto body = [&](bool first) __attribute__((always_inline)) {
#pragma unroll
        for (int z = 0; z < U; ++z) {
            const int c = 2 * (z & 1);
            const v4u f = __builtin_shufflevector(fc[c], fc[c + 1], 0, 1, 2, 3), g = __builtin_shufflevector(fh[c], fh[c + 1], 0, 1, 2, 3);
#pragma unroll
            for (int j = 0; j < 4; ++j) {
                M32V(acc[j], f, l[j]); l[j] = lds_r16(kl + j * KP + 32 * (U - z - 1));
                if (j < 2) fc[(c ^ 2) + j] = lds_tr(fa + FO + (z + 1) * 128 + 32 - 32 * j);
                if (first && j > 0) gap(5 * z + j - 1);
                SCHED();
            }
            if constexpr (B) {
#pragma unroll
                for (int j = 0; j < 4; ++j) {
                    M32V(acc[j], g, h[j]); h[j] = lds_r16(kl + j * KP + 512 + 32 * (U - z - 1));
                    if (j < 2) fh[(c ^ 2) + j] = lds_tr(fa + (z + 1) * 128 + 32 - 32 * j);
                    if (first && j > 1) gap(5 * z + j + 1);
                    SCHED();
                }
            }
        }
        kl -= 32 * U; fa += 128 * U;
    };
    if constexpr (B) body(true);
#pragma unroll 1
    for (int m = B ? 2 * U : 0; m < 32; m += 2 * U) body(false);
    asm volatile("s_nop 11" : "+v"(acc[0]), "+v"(acc[1]), "+v"(acc[2]), "+v"(acc[3]));
    const float o[3] = {acc[1][15] + acc[2][14] + acc[3][13], acc[2][15] + acc[3][14], acc[3][15]};
    if constexpr (!B) st16(rs, lane == 63 ? W_SP + (u32)d * 16u : OOB, (v4u){u_(o[0]), u_(o[1]), u_(o[2]), 0u});
#pragma unroll
    for (int i = 0; i < 16; ++i) {
        float v = acc[0][i];
#pragma unroll
        for (int j = 1; j < 4; ++j) if (j <= i) v += acc[j][i - j];
        if (i < 3) { const float e = f_((u32)__builtin_amdgcn_ds_bpermute((hh ? lane - 32 : lane + 31) * 4, (int)u_(o[i]))); v += lane == 0 ? sp[i] : e; }
        y[i] = v;
    }
}
DEV void cv_put(lptr F, int pr, const float (&y0)[16], const float (&y1)[16]) {
    const int lane = threadIdx.x & 63;
#pragma unroll
    for (int i = 0; i < 16; ++i) *(LAS u32*)(F + CV_Y + (lane & 31) * CV_YP + (lane >> 5) * 128 + i * 8 + pr * 4) = pk(y0[i], y1[i]);
}
DEV void cv_out(rsrc_t rs, lptr F, int u, int blk0) {
    const int lane = threadIdx.x & 63;
    lptr q = F + CV_Y + ((lane >> 2) & 3) * CV_YP + ((lane >> 4) * 4 + (lane & 3)) * 8;
    const u32 yo = W_Y + (u32)(((lane >> 2) * 8 + blk0) * DM + 4 * u + (lane & 3)) * 16u;
#pragma unroll
    for (int i = 0; i < 8; ++i) { lptr a = q + (i >> 1) * (8 * CV_YP) + (i & 1) * 128; st16(rs, yo + (u32)((i & 1) * 128 + (i >> 1)) * (DM * 16), tr2(a, a + 4 * CV_YP)); }
}
struct KvIn { v2u k[19], v[16]; v4u s[2]; };
template <class F> DEV void kvt_load(const Params& p, int tile, KvIn& in, F&& mid) {
    const int lane = threadIdx.x & 63;
    const rsrc_t rk = mk_rsrc(p.bk, ACT), rv = mk_rsrc(p.bv, ACT), rh = mk_rsrc(p.shift, DM * 8);
    const int row = (tile / 10) * 64 + (lane >> 4) * 16, col = (tile % 10) * 64 + (lane & 15) * 4;
    const u32 vo = (u32)(row * DM + col) * 2u, vp = row > 0 ? vo - 3 * (DM * 2) : OOB;
#pragma unroll
    for (int i = 0; i < 2; ++i) in.s[i] = ldin16(rh, (u32)col * 8u + i * 16, 0);
#pragma unroll
    for (int i = 0; i < 19; ++i) { if (i == 7) mid(); in.k[i] = i < 3 ? ldin8(rk, vp, i * (DM * 2)) : ldin8(rk, vo, (i - 3) * (DM * 2)); if (i >= 3) in.v[i - 3] = ldin8(rv, vo, (i - 3) * (DM * 2)); }
}
DEV void kvt_make(rsrc_t rs, lptr xt, int tile, const KvIn& in) {
    const int lane = threadIdx.x & 63;
#pragma unroll
    for (int c = 0; c < 2; ++c) {
        const v4u sv = in.s[c];
        const f32x2 sh[4] = {{lo_f(sv.x), lo_f(sv.z)}, {hi_f(sv.x), hi_f(sv.z)}, {lo_f(sv.y), lo_f(sv.w)}, {hi_f(sv.y), hi_f(sv.w)}};
        f32x2 kf[19];
#pragma unroll
        for (int i = 0; i < 19; ++i) kf[i] = up2(in.k[i][c]);
        u32 kv[16];
#pragma unroll
        for (int i = 0; i < 16; ++i) kv[i] = pk2(((kf[i] * sh[3] + kf[i + 1] * sh[2]) + (kf[i + 2] * sh[1] + kf[i + 3] * sh[0])) * up2(in.v[i][c]));
#pragma unroll
        for (int q = 0; q < 8; ++q) {
            const u32 sel = (q & 1) ? 0x07060302u : 0x05040100u; const u32* z = kv + (q >> 1);
            *(LAS v2u*)(xt + ((2 * c + (q & 1)) * 16 + (lane & 15)) * 144 + (lane >> 5) * 64 + (q >> 1) * 16 + ((lane >> 4) & 1) * 8) =
                (v2u){__builtin_amdgcn_perm(z[4], z[0], sel), __builtin_amdgcn_perm(z[12], z[8], sel)};
        }
    }
    const u32 o = W_KVT + (u32)(((tile % 10) * 64 + 4 * (lane >> 3)) * SEQ + (tile / 10) * 64 + (lane & 7) * 8) * 2u;
#pragma unroll
    for (int i = 0; i < 8; ++i) st16(rs, o + (u32)(32 * (i & 1) + (i >> 1)) * (SEQ * 2), lds_r16(xt + (8 * i + (lane >> 3)) * 144 + (lane & 7) * 16));
}
struct RowX { v2u a, b; u32 c; };
DEV void ln_row(rsrc_t rs, int row, const RowX& r) {
    const f32x2 x[5] = {up2(r.a.x), up2(r.a.y), up2(r.b.x), up2(r.b.y), up2(r.c)};
    const f32x2 st = ln_stats(x);
    u32 hp[5];
#pragma unroll
    for (int q = 0; q < 5; ++q) hp[q] = pk2(up2(pkv(x[q] - st.x)) * st.y);
    store_h(rs, W_H1, row, hp);
}
DEV v4u mem0_load(const Params& p, int j) {
    const u32 n = (u32)(j * 256 + threadIdx.x);
    return ldin16(mk_rsrc(p.mem0, 8 * DM * 2), n < 640u ? ((2 * ((n >> 2) & 3) + (n & 1)) * DM + (n >> 4) * 16 + (n & 2) * 4) * 2u : OOB, 0);
}
DEV void pre_flags(rsrc_t rs, int j, v4u m) {
    const u32 n = (u32)(j * 256 + threadIdx.x);
#pragma unroll
    for (int i = 0; i < 4; ++i) m[i] = m[i] == PSENT ? 0x7FC17FC1u : m[i];
    st4s(rs, n - 36u * 256u < F_CLR ? foff(n - 36u * 256u) : OOB, 0, 0u);
    st16(rs, n < 640u ? W_MEM0 + (n >> 9) * 32768u + (n & 511u) * 16u : OOB, m);
}
constexpr int KT0 = NPRE * 4, LN0 = NPRE * 8;
struct Early { RowX r[2]; v4u m; };
DEV void conv_early(const Params& p, rsrc_t rs, int j, Early& e) {
    const int lane = threadIdx.x & 63, w = uni(threadIdx.x >> 6), u = j * 4 + w;
    if (j < NPRE) {
        const rsrc_t rx = mk_rsrc(p.x, ACT);
#pragma unroll
        for (int i = 0; i < 2; ++i) {
            const u32 so = uu((u + i * KT0) * (DM * 2));
            e.r[i].a = __builtin_amdgcn_raw_buffer_load_b64(rx, lane * 8u, so, 0); e.r[i].b = __builtin_amdgcn_raw_buffer_load_b64(rx, lane * 8u + 512u, so, 0);
            e.r[i].c = __builtin_amdgcn_raw_buffer_load_b32(rx, lane * 4u + 1024u, so, 0);
        }
        e.m = mem0_load(p, j);
    }
}
DEV void conv_stage(const Params& p, rsrc_t rs, lptr lds, int j, v2u ep, Early& e) {
    const int tid = threadIdx.x, lane = tid & 63, w = uni(tid >> 6), u = j * 4 + w;
    lptr F = lds + w * CV_W;
    const u32 fb = uu(w * CV_W);
    const rsrc_t rd = mk_rsrc(p.ssmD, DM * 2);
    float y0[16], y1[16];
    KvIn in;
    if (j < NPRE) {
        constexpr int CH = CvL<false>::CH;
        if (w == 1) LST(1500 + j, rt32());
        pre_flags(rs, j, e.m);
        ln_row(rs, u, e.r[0]); ln_row(rs, u + KT0, e.r[1]);
        if (w == 1) LST(1550 + j, rt32());
        CFENCE();
        kvt_load(p, u, in, [&] { VMW(13); BAR(); pre_post(rs, j, 1u, ep); if (w == 0) LST(1450 + j, rt32()); });
        if (j == 20) LST(260, rt32());
#pragma unroll 1
        for (u32 o = (u32)(j * 256 + tid) * 16u; o < 2 * MB; o += NPRE * 4096) st16(rs, o, (v4u){PSENT, PSENT, PSENT, PSENT});
        const u32 D = __builtin_amdgcn_raw_buffer_load_b16(rd, (u32)cv_chan(u, lane & 3) * 2u, 0, 0);
#pragma unroll 1
        for (int i = 0; i < 4; ++i) { const int d = cv_chan(u, i); cv_filt<false>(p, fb + i * CH, d, 0); cv_filt<false>(p, fb + i * CH, d, 1); cv_filt<false>(p, fb + i * CH, d, 2); }
        kvt_make(rs, F + CV_Y, u, in);
        cv_zero<false>(F); cv_zero<false>(F + 2 * CH);
        DRAIN(); BAR();
        pre_post(rs, j, 2u, ep);
        if (j == 20) LST(262, rt32());
        pre_wait(rs, 0, NPRE, 2u, ep);
        if (j == 20) LST(281, rt32());
        {
            v4u k[8];
#pragma unroll
            for (int i = 0; i < 8; ++i) k[i] = cv_kvld(rs, cv_chan(u, i >> 1), i & 1);
#pragma unroll
            for (int i = 0; i < 4; ++i) cv_kvput<false>(F + i * CH, k + 2 * i);
        }
        if (j == 20) LST(286, rt32());
#pragma unroll 1
        for (int i = 0; i < 4; i += 2) {
            cv_run<false>(p, rs, F + i * CH, cv_chan(u, i), rdl(D, i), f32x4{}, y0, [](int) {});
            cv_run<false>(p, rs, F + (i + 1) * CH, cv_chan(u, i + 1), rdl(D, i + 1), f32x4{}, y1, [](int) {});
            cv_put(F, i >> 1, y0, y1);
        }
        if (j == 20) LST(287, rt32());
        cv_out(rs, F, u, 0);
        if (j == 20) LST(263, rt32());
    } else {
        const v2u vote = pol_probe(rs, u - NPRE * 4);
        st16(rs, lane == 0 ? foff(F_PRE + 48 + u - NPRE * 4) : OOB, (v4u){ep.x + 2u, ep.y, vote.x, vote.y});
#pragma unroll 1
        for (int t = u + 128; t < 320; t += 16) { kvt_load(p, t, in, [] {}); kvt_make(rs, F, t, in); }
    }
    DRAIN(); BAR();
    pre_post(rs, j, 3u, ep);
    const int b = j < 20 ? -1 : j < 24 ? u - 80 : j >= NPRE ? u - 144 : (w & 1) ? (u >> 1) - 16 : -1, u0 = b < 32 ? b : b + 96, ue = b < 32 ? 128 : 160;
    if (b >= 0) {
        constexpr int CH = CvL<true>::CH;
        cv_zero<true>(F);
        pre_poll(rs, 64, 32, 3u, ep); pre_poll(rs, 0, NCV, 3u, ep);
        v4u k[4];
        auto fetch = [&](u32 dst, int d, int i) { if (i < 5) cv_filt<true>(p, dst, d, i); else if (i < 9) k[i - 5] = cv_kvld(rs, d, i - 5); };
#pragma unroll
        for (int i = 0; i < 9; ++i) fetch(fb, cv_chan(u0, 0), i);
        const u32 ch = (u32)cv_chan(min(u0 + 32 * (lane >> 2), 159), lane & 3);
        u32 D = __builtin_amdgcn_raw_buffer_load_b16(rd, ch * 2u, 0, 0);
        v4u sp = ld16s(rs, W_SP + ch * 16u, 0);
        asm volatile("" : "+v"(D), "+v"(sp));
#pragma unroll 1
        for (int un = u0, n = 0; un < ue; un += 32) {
            if (un == 64) { LST(282, rt32()); LST(288, __builtin_amdgcn_s_memtime()); }
#pragma unroll 1
            for (int i = 0; i < 4; i += 2, n += 2) {
                const int d1 = (int)rdl(ch, n + 1), d2 = (int)rdl(ch, n + 2);
                cv_kvput<true>(F, k);
                cv_run<true>(p, rs, F, 0, rdl(D, n), (f32x4){f_(rdl(sp.x, n)), f_(rdl(sp.y, n)), f_(rdl(sp.z, n)), 0.f}, y0, [&](int q) { fetch(fb + CH, d1, q); });
                cv_kvput<true>(F + CH, k);
                cv_run<true>(p, rs, F + CH, 326, rdl(D, n + 1), (f32x4){f_(rdl(sp.x, n + 1)), f_(rdl(sp.y, n + 1)), f_(rdl(sp.z, n + 1)), 0.f}, y1, [&](int q) { fetch(fb, d2, q); });
                cv_put(F, i >> 1, y0, y1);
            }
            cv_out(rs, F, un, 4);
            if (un == 64) { LST(283, rt32()); LST(289, __builtin_amdgcn_s_memtime()); }
        }
        DRAIN();
        st8s(rs, lane == 0 ? foff(F_PRE + 192 + b) : OOB, 0, (v2u){ep.x + 3u, ep.y});
    }
    if (j == 20) LST(284, rt32());
    if (b == 62) LST(285, rt32());
}

#define M32A(first, d, a, b) do { if (first) asm volatile("v_mfma_f32_32x32x16_bf16 %0, %1, %2, 0" : "=&v"(d) : "a"(a), "v"(b)); \
                                  else asm volatile("v_mfma_f32_32x32x16_bf16 %0, %1, %2, %0" : "+v"(d) : "a"(a), "v"(b)); } while (0)
#define M32B(first, d, a, b) do { if (first) asm volatile("v_mfma_f32_32x32x16_bf16 %0, %1, %2, 0" : "=&v"(d) : "v"(a), "a"(b)); \
                                  else asm volatile("v_mfma_f32_32x32x16_bf16 %0, %1, %2, %0" : "+v"(d) : "v"(a), "a"(b)); } while (0)
#define VCVT(d, a, b) asm volatile("v_cvt_pk_bf16_f32 %0, %1, %2" : "=v"(d) : "v"(a), "v"(b))
#define VADD(d, a, b) asm volatile("v_add_f32 %0, %1, %2" : "=v"(d) : "v"(a), "v"(b))
#define LD4A(d, vo, so) asm volatile("buffer_load_dword %0, %1, %2, %3 offen sc1" : "=v"(d) : "v"(vo), "s"(srd), "s"(so) : "memory")
DEV u32 wst_src(int lane, int ld) { return (u32)((lane >> 4) * ld + 8 * ((lane & 15) ^ ((lane >> 4) << 2))) * 2u; }
DEV u32 wst_rd(int lane, int w) { const int q4 = (lane >> 2) & 3; return (u32)(q4 * 256 + ((w ^ q4) * 64) + ((lane >> 4) & 1) * 32 + (lane & 3) * 8); }

constexpr int QK_SEL = 81920;
#define QK_LEAD 12
#define QK_SMP 29
#define QK_YB 26
#define QK_W0 2
DEV void role_qkv(const Params& p, lptr lds, int c, v2u ep) {
    const int tid = threadIdx.x, lane = tid & 63, w = uni(tid >> 6), hh = lane >> 5, r32 = lane & 31;
    const int m = c >> 5, h = c & 31;
    const rsrc_t rs = mk_rsrc(p.ws, W_END);
    v4u wt[40];
    {
        const v4u sw = mk_srd(m == 0 ? p.Wq : m == 1 ? p.Wk : p.Wv, DM * NQ * 2), sx = mk_srd(p.x, ACT);
        const u32 vo = wst_src(lane, NQ) + (u32)h * (DH * 2), x8 = (u32)lane * 8u, x4 = (u32)lane * 4u;
        RowX xr[5];
        auto wfill = [&](int i0, int n) {
#pragma unroll 1
            for (int i = i0; i < i0 + n; ++i) DMAIN(sw, uu((4 * i + w) * 1024), vo, uu((16 * i + 4 * w) * (NQ * 2)));
        };
        wfill(0, QK_W0);
#pragma unroll
        for (int i = 0; i < 5; ++i)
            asm volatile("buffer_load_dwordx2 %0, %3, %5, %6 offen\n\tbuffer_load_dwordx2 %1, %3, %5, %6 offen offset:512\n\tbuffer_load_dword %2, %4, %5, %6 offen offset:1024"
                         : "=&v"(xr[i].a), "=&v"(xr[i].b), "=&v"(xr[i].c) : "v"(x8), "v"(x4), "s"(sx), "s"(uu((LN0 + 4 * c + w + 384 * i) * (DM * 2))) : "memory");
        wfill(QK_W0, 10 - QK_W0);
        if (c == 3) LST(275, rt32());
        VMW(10 - QK_W0);
        if (c == 3) LST(276, rt32());
#pragma unroll
        for (int i = 0; i < 5; ++i) {
            asm volatile("" : "+v"(xr[i].a), "+v"(xr[i].b), "+v"(xr[i].c));
            const int row = LN0 + 4 * c + w + 384 * i;
            if (i < 4 || row < SEQ) ln_row(rs, row, xr[i]);
            wfill(10 + 6 * i, 6);
        }
        if (c == 3) LST(264, rt32());
        lptr tb = lds + hh * 2048 + wst_rd(lane, w);
        SFOR(k, 40)
            if constexpr (k == 0) { VMW(15); BAR(); }
            if constexpr (k == 28) {
                if (w == 0) pre_poll(rs, 0, NPRE, 1u, ep);
                DRAIN(); BAR();
                pre_post(rs, 96 + c, 1u, ep);
            }
            lptr q = tb + (k >> 3) * 32768 + (k & 7) * 4096; wt[k] = tr2(q, q + 1024); asm volatile("" : "+a"(wt[k]));
        SEND
    }
    if (c == 3) LST(268, rt32());
    if (w == 0) LST(1350 + c, rt32());
    bool dead = false;
    int n0, n1;
    const u32 t16 = (u32)(w * 2048 + lane * 16), wb = uu(w * 2048);
    u32 ar[16];
#pragma unroll
    for (int j = 0; j < 16; ++j) ar[j] = (u32)(r32 * 256 + ((2 * (j & 7) + hh) ^ (r32 & 15)) * 16 + (j >> 3) * 49152);
    MK_SRD();
#define XRD(y, ks) lds_r16(lds + ar[((ks) & 7) + ((ks) >= 24 ? 8 : 0)] + ((ks) >> 3) % 3 * 16384 + (y) * 8192)
#define QM0(y, b) asm volatile("s_add_u32 m0, %0, %1" :: "s"(wb), "i"((b) * 16384 + (y) * 8192) : "scc", "memory")
#define QDMA(i, vo) asm volatile("buffer_load_dwordx4 %0, %1, %2 offen offset:%3 sc1 lds" :: "v"(vo), "s"(srd), "s"(((i) >> 1) * 8192), "i"(((i) & 1) * 1024) : "memory")
    const u32 h2flag = (lane < 16 && w == 0) ? foff(F_H2 + lane) : OOB;
    const u32 ol = (m == 2 ? (u32)(2 * w + (r32 >> 4)) * 1024u : (u32)((r32 >> 4) * 4 + w) * 1024u) + (u32)(hh * 16 + (r32 & 15)) * 16u
                 + (m == 2 ? W_VO : m == 1 ? W_KQ : W_KQ + 8192u) + (u32)h * 16384u;
    const u32 fo = tid < 2 ? foff(F_QKV + tid * 96 + c) : OOB;
    lptr selw = lds + QK_SEL + (w == 0 ? 0 : 64), selr = lds + QK_SEL;
    PIN(selw);
    static_assert(QK_LEAD > 8 && QK_LEAD % 2 == 0 && LN0 == 10 * 32, "X = 8 comes up, before any item of layer 1");
    static_assert(W_H2 - W_H1 == 2 * 64 * (32 * DM * 2), "h");
    auto run = [&](auto isv) __attribute__((always_inline)) {
        constexpr bool V = decltype(isv)::value;
        constexpr int YB = QK_YB;
        f32x16 ax, ay = {};
        v4u x[8], pp, pq;
        u32 xv = t16 + W_H1, ox = ol, oy = OOB, nn = 1, lt = 0, fcnt = 0, fa = OOB;
        asm volatile("" : "+v"(lt), "+v"(nn));
#pragma unroll
        for (int z = 0; z < 5; ++z) x[z] = XRD(1, 40 - YB + z);
#define QPK(r_, a1, a_, q_) do { if constexpr (V) VCVT(r_[q_], a1[4 * (a_) + 8 * ((q_) >> 1) + 2 * ((q_) & 1)], a1[4 * (a_) + 8 * ((q_) >> 1) + 2 * ((q_) & 1) + 1]); \
                                 else VCVT(r_[q_], a1[4 * (a_) + (q_)], a1[4 * (a_) + (q_) + 8]); } while (0)
#define QPK4(r_, a1, a_) do { QPK(r_, a1, a_, 0); QPK(r_, a1, a_, 1); QPK(r_, a1, a_, 2); QPK(r_, a1, a_, 3); } while (0)
        for (;;) {
            u32 ls = uu(lt), yv, fv, nx, need;
            if (ls != 0u && uu(nn) < 256u) pre_wait(rs, 96, 96, 1u, ep);
            else if (ls != 0u) {
                n1 = (int)(uu(nn) >> 8);
                CFENCE();
                const u32 hf = h2flag + (u32)(~n1 & 1) * 2048u;
                u32 f2 = ld4(rs, hf);
                asm volatile("s_waitcnt vmcnt(0)" : "+v"(f2) :: "memory");
                *(LAS u32*)(selw + 16) = (u32)__builtin_amdgcn_ballot_w64(f2 < (u32)n1) & 0xFFFFu;
                BAR();
                st4s(rs, fa, 0, fcnt);
                fa = OOB;
                if (n1 > NSEG) { LDUMP(w == 0, 64 + c); __builtin_amdgcn_endpgm(); }
                if (uu(*(LAS u32*)(selr + 16)) != 0u) {
                    if (w == 0) wait_flags(rs, hf, (u32)n1, dead);
                    BAR();
                }
            }
#define QT(k) do {} while (0)
            QT(0);
            SCHED();
            QM0(0, 0); NOPS(0); QDMA(0, xv); QDMA(1, xv); QM0(0, 1);
            SFOR(z, 80)
                constexpr int ks = (z + 40 - YB) % 40, zx = z - YB;
                constexpr auto upto = [](int q) { const int d = (q - YB + 49) % 40; return q + 6 + (d == 0 || d == 2 ? 1 : d == 1 ? 2 : 0); };
                if constexpr (zx >= 0 && zx < 40) { if constexpr (V) M32B(ks == 0, ax, x[z & 7], wt[ks]); else M32A(ks == 0, ax, wt[ks], x[z & 7]); }
                else { if constexpr (V) M32B(ks == 0, ay, x[z & 7], wt[ks]); else M32A(ks == 0, ay, wt[ks], x[z & 7]); }
                if constexpr (z == 78) lt = *(LAS u32*)selr;
#pragma unroll
                for (int q = upto(z - 1); q < upto(z); ++q) x[q & 7] = XRD(q % 80 < YB || q % 80 >= YB + 40, (q + 40 - YB) % 40);
                SCHED();
                (void)&srd; (void)&wb;
                if constexpr (z < 2) QDMA(z + 2, xv);
                if constexpr (z >= 2 && z < 14 && (z & 3) == 2) QM0(0, (z + 6) >> 2);
                if constexpr (z >= 3 && z < 15 && (z & 1)) QDMA((z + 5) >> 1, xv);
                if constexpr (z == 4) { n0 = uni((int)nn); n1 = n0 >> 8; n0 &= 255; PINS(n0); PINS(n1); }
                if constexpr (z == 8) { nx = n0 < NSEG ? W_H1 + (u32)n0 * (32 * DM * 2) : OOB; PINS(nx); }
                if constexpr (z == 12) { yv = t16 + nx; PIN(yv); }
                if constexpr (zx == -6) { QT(1); asm volatile("s_waitcnt vmcnt(6) lgkmcnt(0)\n\ts_barrier" ::: "memory"); QT(2); }
                if constexpr (zx == -5) st4s(rs, fa, 0, fcnt);
                if constexpr (zx == -4 || zx == 0 || zx == 4) QM0(1, (zx + 4) >> 2);
                if constexpr (zx >= -3 && zx < 8 && (zx & 1)) QDMA((zx + 3) >> 1, yv);
                if constexpr (zx == 2) QPK4(pp, ay, 0);
                if constexpr (zx == 6) QPK4(pq, ay, 1);
                if constexpr (zx == 8) st16(rs, oy, pp);
                if constexpr (zx == 9) st16(rs, oy + 512, pq);
                if constexpr (zx == 10) { QT(3); asm volatile("s_waitcnt vmcnt(9)\n\ts_barrier" ::: "memory"); QT(4); }
                if constexpr (zx == 11 || zx == 15) QM0(1, (zx + 1) >> 2);
                if constexpr (zx >= 12 && zx < 20 && (zx & 1) == 0) QDMA(zx >> 1, yv);
                if constexpr (zx == 21) { fcnt = lane == 0 ? (u32)n0 : (u32)n1; fa = fo; asm volatile("" : "+v"(fcnt), "+v"(fa)); }
                if constexpr (zx == 22) { oy = (ol + (u32)n0 * (NH * 16384u)) | (yv & OOB); PIN(oy); }
                if constexpr (zx == 23) { n0 = min(n0 + 1, NSEG); PINS(n0); nx = (u32)min(NSEG, n1 + QK_LEAD); PINS(nx); }
                if constexpr (zx == 24)
                    asm volatile("s_cmp_ge_u32 %1, %0\n\ts_cselect_b32 %0, %4, %1\n\ts_addc_u32 %2, %2, 0\n\ts_cmp_lt_u32 %0, 64\n\ts_cselect_b32 %3, 0, %2"
                                 : "+s"(nx), "+s"(n0), "+s"(n1), "=&s"(need) : "s"(n1 + 64) : "scc");
                if constexpr (zx == 25) { n0 += 1 - (int)min(nx >> 6, 1u); PINS(n0); }
                if constexpr (zx == 26) { nn = (u32)(n0 | n1 << 8); PIN(nn); }
                if constexpr (zx == 27) { ls = W_H1 + (nx + (nx & 64u)) * (32 * DM * 2); PINS(ls); }
                if constexpr (zx == 28) { xv = t16 + ls; PIN(xv); need = nx == 8u ? 255u : need; PINS(need); }
                if constexpr (zx == 34) { asm volatile("s_waitcnt vmcnt(%0) lgkmcnt(0)\n\ts_barrier" :: "i"(QK_SMP < 34 ? 7 : 6) : "memory"); QT(5); }
                if constexpr (zx == QK_SMP) { const u32 po = (nx & 1u) * 2048u; LD4A(fv, h2flag, po); }
                if constexpr (zx == 42) QPK4(pp, ax, 0);
                if constexpr (zx == 43) st16(rs, ox, pp);
                if constexpr (zx == 44) QPK4(pq, ax, 1);
                if constexpr (zx == 45) st16(rs, ox + 512, pq);
                if constexpr (zx == 47) { ox = ol + nx * (NH * 16384u); PIN(ox); }
                if constexpr (z == 74) { asm volatile("s_waitcnt vmcnt(2)" : "+v"(fv) :: "memory"); QT(6); ls = (u32)__builtin_amdgcn_ballot_w64(fv < need) & 0xFFFFu; PINS(ls); }
                if constexpr (z == 75) { lt = ls; PIN(lt); *(LAS u32*)selw = lt; }
                if constexpr (z == 77) asm volatile("s_waitcnt lgkmcnt(2)\n\ts_barrier" ::: "memory");
                SCHED();
            SEND
        }
    };
    if (m == 2) run(IC<1>{}); else run(IC<0>{});
}

#define OU_FLG 25
#define OU_SMP 17
DEV void role_out(const Params& p, lptr lds, int L, int kq, int ng, v2u ep) {
    const int tid = threadIdx.x, lane = tid & 63, w = uni(tid >> 6), hh = lane >> 5, r32 = lane & 31;
    const rsrc_t rs = mk_rsrc(p.ws, W_END);
    v4u wo[64];
    bool dead = false;
    {
        const v4u sw = mk_srd(p.Wo, DM * NQ * 2);
        const u32 vo = wst_src(lane, DM) + (u32)ng * 256u;
        auto fill = [&](int b) {
#pragma unroll 1
            for (int i = 0; i < 8; ++i) DMAIN(sw, uu((b % 5) * 32768 + (4 * i + w) * 1024), vo, uu((kq * 1024 + b * 128 + 16 * i + 4 * w) * (DM * 2)));
        };
#pragma unroll 1
        for (int b = 0; b < 5; ++b) fill(b);
        lptr tb = lds + hh * 1024 + wst_rd(lane, w);
        SFOR(b, 8)
            VMW(b < 4 ? 32 : 56 - 8 * b); BAR();
            lptr q = tb + (b % 5) * 32768;
#pragma unroll
            for (int k = 0; k < 8; ++k) { lptr a = q + (k >> 1) * 8192 + (k & 1) * 2048; wo[8 * b + k] = tr2(a, a + 4096); if (b < 7) asm volatile("" : "+a"(wo[8 * b + k])); else PIN(wo[8 * b + k]); }
            if constexpr (b < 3) { BAR(); fill(b + 5); }
        SEND
    }
    pre_wait(rs, 0, NPRE, 1u, ep);
    if (kq == 0 && ng == 0) LST(272 + L, rt32());
    MK_SRD();
    const u32 ofl = lane < 8 ? foff(F_O + L * 32 + kq * 8 + lane) : OOB, l16 = (u32)lane * 16u;
    const u32 fo = (tid == 0) ? foff(F_P + L * 32 + kq * 5 + ng) : OOB;
#define OM0(dst, b) asm volatile("s_add_u32 m0, %0, %1" :: "s"(dst), "i"((b) * 8192) : "scc", "memory")
#define ODMA(i, vo) asm volatile("buffer_load_dwordx4 %0, %1, %2 offen offset:%3 sc1 lds" :: "v"(vo), "s"(srd), "s"(((i) >> 1) * 16384), "i"(((i) & 1) * 1024) : "memory")
    auto first = [&](u32 dst, u32 vo) {
#pragma unroll
        for (int b = 0; b < 5; ++b) { OM0(dst, b); NOPS(0); ODMA(2 * b, vo); ODMA(2 * b + 1, vo); }
    };
    u32 pa = W_P + (u32)(L * NSEG) * P_ITEM + (u32)(2 * hh * 4 + kq) * (DM * 4) + (u32)(ng * 128 + w * 32 + r32) * 4u, fv = 0;
    u32 ov = W_VO + 8192u + (u32)(L * NSEG * NH + kq * 8) * 16384u + (u32)w * 2048u + l16;
    bool early = false;
    u32 farv = 1, owed = OOB;
    lptr selw = lds + 131072 + (w == 0 ? 0 : 64), selr = lds + 131072;
    asm volatile("" : "+v"(farv), "+v"(selw), "+v"(selr));
#define OWAIT(strict, loose) asm volatile("s_cmp_eq_u32 %0, 0\n\ts_cbranch_scc1 1f\n\ts_waitcnt vmcnt(%1)\n1:\n\ts_waitcnt vmcnt(%2)\n\ts_barrier" :: "s"(far), "i"(strict), "i"(loose) : "scc", "memory")
    for (int s = 0; s < NSEG; ++s) {
        const u32 dst = uu((s & 1) * 65536 + w * 2048), ndst = dst ^ 65536u, need = s + 1 < NSEG ? (u32)s + 2u : 255u;
        lptr A = lds + (s & 1) * 65536 + l16;
        if (!early) {
            if (!all_(ofl == OOB || fv >= (u32)s + 1)) wait_flags(rs, ofl, (u32)s + 1, dead);
            LS3(L == 0 && w == 1, 1, kq * 5 + ng);
            first(dst, ov);
        }
#define OT(k) do {} while (0)
        u32 far = uu(farv), nv, sv = (u32)s;
        OM0(dst, 5);
        OWAIT(8, 17);
        OT(0);
        v4u o[64];
#pragma unroll
        for (int k = 0; k < 4; ++k) o[k] = lds_r16(A + k * 1024);
        f32x16 acc;
        SCHED();
        SFOR(k, 64)
            if constexpr (k < 56) M32B(k == 0, acc, o[k], wo[k]); else M32V(acc, o[k], wo[k]);
            if constexpr (k < 60) o[k + 4] = lds_r16(A + (k + 4) * 1024);
            SCHED();
            (void)&srd; (void)&dst; (void)&ndst;
            if constexpr (k < 12 && (k & 1) == 0) ODMA(10 + k / 2, ov);
            if constexpr (k == 3) OWAIT(6, 14);
            if constexpr (k == 3 || k == 7) OM0(dst, (k + 21) >> 2);
            if constexpr (k == 12) { ov += NH * 16384u; PIN(ov); }
            if constexpr (k == OU_SMP) LD4A(fv, ofl, 0);
            if constexpr (k == 19) OWAIT(7, 15);
            if constexpr (k == 33) { asm volatile("s_waitcnt vmcnt(1)" : "+v"(fv) :: "memory"); far = (u32)__builtin_amdgcn_ballot_w64(fv < need) & 255u; PINS(far); }
            if constexpr (k == 34) { farv = far; PIN(farv); *(LAS u32*)selw = farv; }
            if constexpr (k == 35) asm volatile("s_waitcnt lgkmcnt(1)\n\ts_barrier" ::: "memory");
            if constexpr (k == OU_FLG) asm volatile("s_waitcnt vmcnt(7)\n\ts_barrier" ::: "memory");
            if constexpr (k == OU_FLG + 1) st4s(rs, owed, 0, sv);
            if constexpr (k == 36) farv = *(LAS u32*)selr;
            if constexpr (k == 41) { nv = farv ? OOB : ov; PIN(nv); }
            if constexpr (k >= 42 && k < 62 && (k & 3) == 2) OM0(ndst, (k - 42) >> 2);
            if constexpr (k >= 43 && k < 63 && (k & 1)) ODMA((k - 43) >> 1, nv);
            if constexpr (k == 48) LD4A(fv, ofl, 0);
            if constexpr (k == 4) OT(1);
            if constexpr (k == 20) OT(2);
            if constexpr (k == 36) OT(3);
            if constexpr (k == 41) OT(4);
            SCHED();
        SEND
        OT(5);
        asm volatile("s_nop 11" : "+v"(acc), "+v"(pa));
#pragma unroll
        for (int i = 0; i < 8; ++i) st4s(rs, pa, (u32)((4 * (i >> 1) + (i & 1)) * (4 * DM * 4)), pk(acc[2 * i], acc[2 * i + 1]));
        asm volatile("s_waitcnt vmcnt(15)" : "+v"(fv) :: "memory");
        OT(6);
        LS3(L == 0 && w == 1, 2, kq * 5 + ng);
        far = uu(farv);
        early = far == 0u || all_(ofl == OOB || fv >= need);
        owed = fo;
        if (far != 0u) {
            if (early) { first(ndst, ov); VMW(10); }
            else { fv = ld4(rs, ofl); asm volatile("s_waitcnt vmcnt(0)" : "+v"(fv) :: "memory"); }
            BAR();
            st4s(rs, fo, 0, (u32)s + 1);
            owed = OOB;
        }
        pa += P_ITEM;
        LS2(L == 0 && w == 1, 160 + kq * 5 + ng);
    }
    LDUMP(L == 0 && w == 1, kq * 5 + ng);
}

DEV void ln_stats2(const f32x2 (&x)[10], f32x2& mean, f32x2& rstd) {
    const f32x2 c = {f_(uu(u_(x[0].x))), f_(uu(u_(x[0].y)))};
    f32x2 d[10];
#pragma unroll
    for (int i = 0; i < 10; ++i) d[i] = x[i] - c;
    f32x2 a = ((d[0] + d[1]) + (d[2] + d[3])) + ((d[4] + d[5]) + (d[6] + d[7])) + (d[8] + d[9]);
    f32x2 b = (d[0] * d[0] + d[2] * d[2] + d[4] * d[4] + d[6] * d[6] + d[8] * d[8]) + (d[1] * d[1] + d[3] * d[3] + d[5] * d[5] + d[7] * d[7] + d[9] * d[9]);
    float a0, a1, b0, b1;
#define LN_DP4(m) "v_add_f32_dpp %0, %0, %0 " m " row_mask:0xf bank_mask:0xf\n\tv_add_f32_dpp %1, %1, %1 " m " row_mask:0xf bank_mask:0xf\n\t" \
                  "v_add_f32_dpp %2, %2, %2 " m " row_mask:0xf bank_mask:0xf\n\tv_add_f32_dpp %3, %3, %3 " m " row_mask:0xf bank_mask:0xf\n\t"
    asm("s_nop 1\n\t" LN_DP4("quad_perm:[1,0,3,2]") LN_DP4("quad_perm:[2,3,0,1]") LN_DP4("row_ror:4") LN_DP4("row_ror:8")
        "v_permlane16_swap_b32 %0, %1\n\tv_add_f32 %0, %0, %1\n\tv_permlane16_swap_b32 %2, %3\n\tv_add_f32 %2, %2, %3\n\ts_nop 1\n\t"
        "v_permlane32_swap_b32 %0, %2\n\tv_add_f32 %0, %0, %2\n\ts_nop 0\n\t"
        "v_readlane_b32 %4, %0, 0\n\tv_readlane_b32 %5, %0, 16\n\tv_readlane_b32 %6, %0, 32\n\tv_readlane_b32 %7, %0, 48\n\ts_nop 1"
        : "+v"(a.x), "+v"(b.x), "+v"(a.y), "+v"(b.y), "=s"(a0), "=s"(b0), "=s"(a1), "=s"(b1));
    const f32x2 m = (f32x2){a0, a1} * (1.0f / DM), v = (f32x2){b0, b1} * (1.0f / DM) - m * m;
    const f32x2 q = up2(pkv(up2(pkv((f32x2){fmaxf(v.x, 0.f), fmaxf(v.y, 0.f)})) + 1e-5f));
    const f32x2 sd = up2(pk(__builtin_amdgcn_sqrtf(q.x), __builtin_amdgcn_sqrtf(q.y)));
    mean = up2(pkv(c + m)); rstd = (f32x2){__builtin_amdgcn_rcpf(sd.x), __builtin_amdgcn_rcpf(sd.y)};
}
DEV void role_row(const Params& p, lptr lds, int id, v2u ep) {
    const int lane = threadIdx.x & 63, w = uni(threadIdx.x >> 6);
    const int pr = id >> 2, L = id & 1, par = (id >> 1) & 1;
    const rsrc_t rs = mk_rsrc(p.ws, W_END), rx = mk_rsrc(p.x, ACT), ro_ = mk_rsrc(p.out, ACT);
    bool dead = false;
    const u32 fp = lane < 20 ? foff(F_P + L * 32 + lane) : OOB, l16 = (u32)lane * 16u, l8 = (u32)lane * 8u, l4 = (u32)lane * 4u, fb = uu(w * CV_W);
    lptr F = lds + w * CV_W;
    MK_SRD();
    for (int s = par; s < NSEG; s += 2) {
        const u32 ro = (u32)(s * 32 + 2 * pr) * (DM * 2);
        if ((s & 6) == 0) {
            if ((s & 30) == 0) pre_poll(rs, s < 32 ? 0 : 192, s < 32 ? NPRE : 64, 3u, ep);
            if (id == 0 && s == 0) LST(274, rt32());
            if (s == 48) st8s(rs, (id == 1 && lane == 0) ? foff(F_EPOCH) : OOB, 0, (v2u){ep.x + 8u, ep.y});
#pragma unroll 1
            for (int i = 0; i < 5; ++i) {
                const u32 d = fb + i * 2048, o = W_Y + (u32)(16 * pr + (s >> 3)) * 10240u + i * 2048;
                DMA32(d, l16, o); DMA32(d + 10240u, l16, o + 81920u);
            }
            DRAIN();
        }
        RowX xr[2];
        auto xload = [&](rsrc_t r, u32 o, auto aux) {
#pragma unroll
            for (int i = 0; i < 2; ++i) {
                xr[i].a = __builtin_amdgcn_raw_buffer_load_b64(r, l8, o + i * (DM * 2), aux.value); xr[i].b = __builtin_amdgcn_raw_buffer_load_b64(r, l8 + 512u, o + i * (DM * 2), aux.value);
                xr[i].c = __builtin_amdgcn_raw_buffer_load_b32(r, l4 + 1024u, o + i * (DM * 2), aux.value);
            }
        };
        if (L == 0) xload(rx, ro, IC<0>{});
        f32x2 y[10], x[10];
        {
            lptr ya = F + (s & 7) * 2 + l16, yc = F + (s & 7) * 2 + 2048 + (lane >> 1) * 16 + (lane & 1) * 5120;
#pragma unroll
            for (int i = 0; i < 10; ++i) {
                lptr q = i < 8 ? ya + (i & 3) * 2560 + (i >> 2) * 1024 : yc + (i & 1) * 2560;
                y[i] = (f32x2){lo_f(*(LAS u16*)q), lo_f(*(LAS u16*)(q + 10240))}; PIN(y[i]);
            }
        }
        wait_flags(rs, fp, (u32)s + 1, dead);
        LS3(L == 0, 3, ri);
        LMX(L == 0, 0, s, rt32());
        {
            const u32 o = uu(W_P + (u32)(L * NSEG + s) * P_ITEM + (u32)pr * (4 * DM * 4));
            if (L == 1) xload(rs, W_X2 + ro, IC<16>{});
            v4u a[4], b[4]; v2u c[4];
#pragma unroll
            for (int kq = 0; kq < 4; ++kq) { a[kq] = ld16s(rs, l16, o + kq * (DM * 4)); SCHED(); b[kq] = ld16s(rs, l16 + 1024u, o + kq * (DM * 4)); SCHED(); c[kq] = ld8s(rs, l8 + 2048u, o + kq * (DM * 4)); SCHED(); }
            const u32 u0[5] = {xr[0].a.x, xr[0].a.y, xr[0].b.x, xr[0].b.y, xr[0].c}, u1[5] = {xr[1].a.x, xr[1].a.y, xr[1].b.x, xr[1].b.y, xr[1].c};
#pragma unroll
            for (int i = 0; i < 5; ++i) { x[2 * i] = (f32x2){lo_f(u0[i]), lo_f(u1[i])}; x[2 * i + 1] = (f32x2){hi_f(u0[i]), hi_f(u1[i])}; }
#pragma unroll
            for (int kq = 0; kq < 4; ++kq)
#pragma unroll
                for (int i = 0; i < 10; ++i)
                    asm volatile("v_dot2_f32_bf16 %0, %2, %3, %0\n\tv_dot2_f32_bf16 %1, %2, %4, %1" : "+v"(x[i].x), "+v"(x[i].y)
                                 : "v"(i < 4 ? a[kq][i & 3] : i < 8 ? b[kq][i & 3] : c[kq][i & 1]), "s"(0x3F80u), "s"(0x3F800000u));
            asm volatile("s_nop 2" : "+v"(x[8]), "+v"(x[9]));
        }
        LS3(L == 0, 4, ri);
        f32x2 mean, rstd;
        ln_stats2(x, mean, rstd);
#pragma unroll
        for (int i = 0; i < 10; ++i) x[i] += (x[i] - mean) * (y[i] * rstd);
        u32 xp[2][5];
#pragma unroll
        for (int i = 0; i < 5; ++i) { xp[0][i] = pkv((f32x2){x[2 * i].x, x[2 * i + 1].x}); xp[1][i] = pkv((f32x2){x[2 * i].y, x[2 * i + 1].y}); }
        if (L == 0) {
#pragma unroll
            for (int i = 0; i < 5; ++i) { x[2 * i] = (f32x2){lo_f(xp[0][i]), lo_f(xp[1][i])}; x[2 * i + 1] = (f32x2){hi_f(xp[0][i]), hi_f(xp[1][i])}; }
            ln_stats2(x, mean, rstd);
#pragma unroll
            for (int i = 0; i < 10; ++i) x[i] = (x[i] - mean) * rstd;
#pragma unroll
            for (int r = 0; r < 2; ++r) {
                u32 hp[5];
#pragma unroll
                for (int i = 0; i < 5; ++i) hp[i] = pkv((f32x2){x[2 * i][r], x[2 * i + 1][r]});
                store_h(rs, W_H2, s * 32 + 2 * pr + r, hp);
            }
#pragma unroll
            for (int r = 0; r < 2; ++r) {
                st8s(rs, l8, W_X2 + ro + r * (DM * 2), (v2u){xp[r][0], xp[r][1]});
                st8s(rs, l8 + 512u, W_X2 + ro + r * (DM * 2), (v2u){xp[r][2], xp[r][3]});
                st4s(rs, l4 + 1024u, W_X2 + ro + r * (DM * 2), xp[r][4]);
            }
            LS3(L == 0, 5, ri);
            DRAIN();
            st4s(rs, lane == 0 ? foff(F_H2 + par * 16 + pr) : OOB, 0, (u32)s + 1);
        } else {
#pragma unroll
            for (int r = 0; r < 2; ++r) {
                st8s(ro_, l8, ro + r * (DM * 2), (v2u){xp[r][0], xp[r][1]});
                st8s(ro_, l8 + 512u, ro + r * (DM * 2), (v2u){xp[r][2], xp[r][3]});
                st4s(ro_, l4 + 1024u, ro + r * (DM * 2), xp[r][4]);
            }
        }
        if (pr == 0) LST(320 + L * 64 + s, rt32());
        LS2(L == 0, 224 + ri);
        LMX(L == 0, 1, s, rt32());
    }
    LDUMP(L == 0, 32 + ri);
}

constexpr float SCALE = 0.08838834764831845f, LOG2E = 1.4426950408889634f;
constexpr int C_K = 0, C_V = 8192, C_QX = 16384, C_MEM = 24576, C_Q = C_MEM + 10240, C_KM = C_Q + 2304, C_VM = C_KM + 2304, C_DUMP = C_VM + 2048,
              C_ZERO = C_DUMP + 1024, C_WL = C_ZERO + 128;
constexpr int NLF = 14;
static_assert(C_WL % 16 == 0 && C_WL + 4 * NLF * 2048 <= LDS_BYTES, "head lds");
#define CH_VIS 8
#define CH_H0 104
#define CH_W0 80
#define CH_MH 3
#define CH_MW 3
#define CH_REC 28
#define CH_RTY 32u
#define CH_RISE 20u
#define CH_FALL 3u
#define CH_PRE 7
#define CH_PRW 2
#define CH_T0 190
#define CH_UP 2
#define CH_WX 6
#define WAIT_TXT(tmp, t4) "s_memrealtime vcc\n\t.p2align 5\n1:\n\ts_waitcnt lgkmcnt(0)\n\ts_sub_u32 " tmp ", " t4 ", vcc_lo\n\ts_memrealtime vcc\n\ts_cmpk_lt_u32 " tmp ", 400\n\ts_cbranch_scc1 1b\n\t" \
                          "s_add_u32 " tmp ", " tmp ", 4\n\ts_cselect_b32 " tmp ", " tmp ", 0\n2:\n\ts_sub_u32 " tmp ", " tmp ", 1\n\ts_cbranch_scc0 2b\n\t"
DEV void wait_until(u32 t) { u32 tmp; asm volatile(WAIT_TXT("%0", "%1") "s_waitcnt lgkmcnt(0)" : "=&s"(tmp) : "s"(uu(t - 4u)) : "vcc", "scc", "memory"); }
#define SMF_A(acc, a, b) asm volatile("v_smfmac_f32_16x16x64_bf16 %0, %1, %2, %3" : "+v"(acc) : "v"(a), "a"(b), "v"(idx))
#define SMF_V(acc, a, b) asm volatile("v_smfmac_f32_16x16x64_bf16 %0, %1, %2, %3" : "+v"(acc) : "v"(a), "v"(b), "v"(idx))
#define MF0(d, a, b) asm volatile("v_mfma_f32_16x16x32_bf16 %0, %1, %2, 0" : "=&v"(d) : "v"(a), "v"(b))
#define MF1(d, a, b) asm volatile("v_mfma_f32_16x16x32_bf16 %0, %1, %2, %0" : "+v"(d) : "v"(a), "v"(b))
#define MF0H(d, a, b) asm volatile("v_mfma_f32_16x16x16_bf16 %0, %1, %2, 0" : "=&v"(d) : "v"(a), "v"(b))
#define MF1H(d, a, b) asm volatile("v_mfma_f32_16x16x16_bf16 %0, %1, %2, %0" : "+v"(d) : "v"(a), "v"(b))
DEV v8u tr_frag(lptr p0, lptr p1, lptr p2, lptr p3) { return cat8(tr2(p0, p1), tr2(p2, p3)); }
constexpr int qslot(int ks) { return (ks & 1) && ks < 8 ? ks >> 1 : -1; }
constexpr int kslot(int ks) { return ks < 4 ? 4 + ks : -1; }
constexpr int vslot(int ks) { return ks >= 4 ? 4 + ks : -1; }
DEV u32 ring_map(u32 off) { return ((off >> 13) << 15) + (off & 8191u); }
DEV u32 oslot(int s) { return (u32)(s & 3) << 18 | (u32)(s & 2) << 13; }
#define ZRD(x, i) x = __builtin_bit_cast(f32x4, lds_r16(ZP + 16 * (i)))
#define VMAX(d, a, b) asm volatile("v_max_f32 %0, %1, %2" : "=v"(d) : "v"(a), "v"(b))
#define VMAX3(d, a, b, c_) asm volatile("v_max3_f32 %0, %1, %2, %3" : "=v"(d) : "v"(a), "v"(b), "v"(c_))
#define VEXP(d, a) asm volatile("v_exp_f32 %0, %1" : "=v"(d) : "v"(a))
#define VMULC(d, a) asm volatile("v_mul_f32 %0, 0x3e0293ee, %1" : "=v"(d) : "v"(a))
#define CADD(d, a, b) do { d = (a) + (b); PIN(d); } while (0)
#define CMULC(d, a) do { d = (a) * CEXP; PIN(d); } while (0)
#define CCVT(d, a, b) do { d = pk(a, b); PIN(d); } while (0)
#define LGK(n) __builtin_amdgcn_s_waitcnt(0xC07F | (n) << 8)
#define SEL(d, a, b) asm volatile("v_cndmask_b32 %0, %1, %2, %3" : "=v"(d) : "v"(a), "v"(b), "s"(himask))
#define QPERM(d, a, q) asm volatile("v_mov_b32_dpp %0, %1 quad_perm:" q " row_mask:0xf bank_mask:0xf" : "=v"(d) : "v"(a))

#define AL_H 3
#define HD_SMP 70
DEV void role_head(const Params& p, lptr lds, int L, int h, v2u ep) {
    const int tid = threadIdx.x, lane = tid & 63, w = uni(tid >> 6), g = lane >> 4, r = lane & 15;
    const rsrc_t rs = mk_rsrc(p.ws, W_END);
    v8u wq0[10], wq1[10], wk0[10], wk1[10], wv0[10], wv1[10];
    lptr WL = lds + C_WL + w * (NLF * 2048) + lane * 16;
    {
        constexpr int GS = 1056, CS = 48 * GS;
        const v4u sq = mk_srd(p.Wq, DM * NQ * 2), sk = mk_srd(p.Wk, DM * NQ * 2), sv = mk_srd(p.Wv, DM * NQ * 2);
        const u32 vo = (u32)(g * NQ + h * DH + 8 * (r ^ ((g & 1) << 3))) * 2u;
        auto fill = [&](int ch) {
#pragma unroll 1
            for (int i = 0; i < 4; ++i) {
                const u32 dst = uu((ch % 3) * CS + (4 * i + w) * GS), so = uu((ch * 64 + 16 * i + 4 * w) * (NQ * 2));
                DMAIN(sq, dst, vo, so); DMAIN(sk, dst + 16 * GS, vo, so); DMAIN(sv, dst + 32 * GS, vo, so);
            }
        };
        fill(0); fill(1); fill(2);
        const int q4 = r >> 2;
        lptr tb = lds + (4 * (g >> 1) + 2 * (q4 >> 1) + (g & 1)) * GS + (q4 & 1) * 256 + ((w * 64 + (r & 3) * 8) ^ ((q4 & 1) << 7));
        SFOR(ch, 10)
            VMW(ch < 8 ? 24 : 108 - 12 * ch);
            BAR();
            auto frag = [&](int m, int t) { lptr b = tb + (ch % 3) * CS + m * 16 * GS + t * 32; return tr_frag(b, b + 512, b + 8 * GS, b + 8 * GS + 512); };
            wq0[ch] = frag(0, 0); wq1[ch] = frag(0, 1); wk0[ch] = frag(1, 0); wk1[ch] = frag(1, 1); wv0[ch] = frag(2, 0); wv1[ch] = frag(2, 1);
            asm volatile("" : "+a"(wq0[ch]), "+a"(wk0[ch]), "+v"(wv0[ch]), "+v"(wv1[ch]));
            if constexpr (qslot(ch) < 0) asm volatile("" : "+a"(wq1[ch])); else asm volatile("" : "+v"(wq1[ch]));
            if constexpr (kslot(ch) < 0) asm volatile("" : "+a"(wk1[ch])); else asm volatile("" : "+v"(wk1[ch]));
            BAR();
            if constexpr (ch < 7) fill(ch + 3);
        SEND
#pragma unroll
        for (int ch = 0; ch < 10; ++ch) {
            if (qslot(ch) >= 0) { lds_w16(WL + qslot(ch) * 2048, wq1[ch].lo); lds_w16(WL + qslot(ch) * 2048 + 1024, wq1[ch].hi); }
            if (kslot(ch) >= 0) { lds_w16(WL + kslot(ch) * 2048, wk1[ch].lo); lds_w16(WL + kslot(ch) * 2048 + 1024, wk1[ch].hi); }
            if (vslot(ch) >= 0) { lds_w16(WL + vslot(ch) * 2048, wv1[ch].lo); lds_w16(WL + vslot(ch) * 2048 + 1024, wv1[ch].hi); }
        }
        CFENCE();
    }
    const u32 idx = (r & 2) ? 0xEEEEu : 0x4444u;
    unsigned long long himask = 0xCCCCCCCCCCCCCCCCull;
    PINS(himask);
    bool dead = false;
    if (h == 3) LST(266 + L, rt32());
    if (w == 0) LST(1240 + L * 32 + h, rt32());
    if (L == 1) {
        KvIn in;
        kvt_load(p, KT0 + 4 * h + w, in, [] {}); kvt_make(rs, lds + w * 9216, KT0 + 4 * h + w, in);
        DRAIN(); BAR();
        pre_post(rs, 64 + h, 3u, ep);
    }
    const u32 pol0 = pol_wait(rs, ep);
    if (h == 3 && L == 0) LST(277, rt32());
    const u32 l16 = (u32)lane * 16u;
    const u32 mring = (u32)L * MB + (pol0 ^ 16384u), hint = mring + 4 * 65536u;
    const u32 fkh = lane < 3 ? foff(F_QKV + L * 96 + lane * 32 + h) : lane < 7 ? hint + (u32)(lane - 3) * 4u : OOB;
    u32 tm = 0;
    MK_SRD();
    u32 ksrc = uu(W_KQ + (u32)(L * (NSEG - 1) * NH + h) * 16384u + w * 2048), vsrc = ksrc + (W_VO - W_KQ);
    u32 dk = uu(C_K + w * 2048), dv = dk + (C_V - C_K), dq = dk + (C_QX - C_K), dm = uu(C_MEM + w * 2048), dm2 = uu(w < 2 ? C_MEM + 8192 + w * 1024 : C_DUMP);
    asm volatile("" : "+s"(dk), "+s"(dv), "+s"(dq), "+s"(dm), "+s"(dm2));
    auto keys_wait = [&](u32 need) {
#pragma unroll 1
        for (int n = 0; !dead; ++n) {
            const bool beat = need + 2 * L > 2u;
            if (beat) wait_until(tm);
            const u32 hv = ld4(rs, lane == 7 ? foff(F_ERR) : fkh);
            if (beat) { tm = rdl(hv, 5); wait_until(rdl(hv, 3) - 30u); }
            if (((u32)__builtin_amdgcn_ballot_w64(ld4(rs, fkh) < need) & 7u) == 0u) break;
            dead = n > SPIN_CAP || rdl(hv, 7) != 0u;
            st4s(rs, dead ? foff(F_ERR) : OOB, 0, 1u);
        }
        CFENCE();
        DMA32(dk, l16, ksrc); DMA32(dv, l16, vsrc); DMA32(dq, l16, ksrc ^ 8192u);
    };
    if (L == 0) { st4s(rs, tid == 0 ? hint + 4096u + (u32)h * 128u : OOB, 0, 0u); keys_wait(1u); }
    lptr qw = lds + C_Q + (2 * g) * 288 + (32 * w + 2 * r) * 2;
    lptr kw = qw + (C_KM - C_Q); PIN(kw);
    lptr qr = lds + C_Q + (2 * (r >> 2) + (r & 1)) * 288 + g * 16;
    lptr LB = lds + l16, VX = lds + C_V + w * 2048 + l16;
    PIN(VX);
    if (tid >= 64 && tid < 70) { const u32 z = tid == 68 ? 0x3F803F80u : tid == 69 ? PSENT : 0u; lds_w16(lds + C_ZERO + (tid - 64) * 16, (v4u){z, z, z, z}); }
    u32 zaddr = 0; PIN(zaddr);
    lptr ZP = lds + zaddr + C_ZERO;
    const float CEXP = SCALE * LOG2E;
#define LF_RD(slot, hf) lf[slot][hf] = lds_r16(WL + (slot) * 2048 + (hf) * 1024)
    BAR();
    const u32 ofl = tid == 0 ? foff(F_O + L * 32 + h) : OOB;
    u32 oso = (((g | L) & 1) ? OOB : 0u) + (u32)L * MB + (pol0 ^ oslot(-L)) + ring_map((u32)((r >> 3) * 32768 + h * 1024 + w * 256 + (g >> 1) * 128 + (r & 7) * 16));
    unsigned long long vis[8];
#pragma unroll
    for (int i = 0; i < 8; ++i) vis[i] = __builtin_amdgcn_ballot_w64(16 * (i >> 2) + 4 * g + (i & 3) <= 16 * (w & 1) + r);
    u32 missed = 0;
    for (int s = -L; s < NSEG;) {
        u32 sv = (u32)max(s, 0), ms = s > 0 ? mring + (sv & 3u) * 65536u : W_MEM0, ms2 = ms + 32768u + (w & 1) * 1024;
        ms += w * 2048;
        asm volatile("" : "+s"(ms), "+s"(ms2)); PIN(sv);
        f32x4 aq0, aq1, ak0, ak1;
        v4u lf[NLF][2];
        ZRD(aq0, 0); ZRD(aq1, 1); ZRD(ak0, 2); ZRD(ak1, 3);
#pragma unroll
        for (int i = 0; i < 4; ++i) { LF_RD(i, 0); LF_RD(i, 1); }
        SCHED();
        v4u A[10];
#define HT(k) do {} while (0)
        v4u kx[8], qf[4], kmf[4], vx[2], ones, P;
        float fa, fb, fc, fd, inv, sx[4], t[4], tm0, tm1; u32 pa, pb, fl, e01, e23, pmm;
        f32x4 av0, av1, s0, s1, sm, oa, ob, rsum;
        v2u vm, pm;
#define LW(sl) cat8(lf[sl][0], lf[sl][1])
#define PQ(ks, tl) do { if constexpr (!(tl)) SMF_A(aq0, A[ks], wq0[ks]); else if constexpr (qslot(ks) < 0) SMF_A(aq1, A[ks], wq1[ks]); else { const v8u lw_ = LW(qslot(ks)); SMF_V(aq1, A[ks], lw_); } } while (0)
#define PK(ks, tl) do { if constexpr (!(tl)) SMF_A(ak0, A[ks], wk0[ks]); else if constexpr (kslot(ks) < 0) SMF_A(ak1, A[ks], wk1[ks]); else { const v8u lw_ = LW(kslot(ks)); SMF_V(ak1, A[ks], lw_); } } while (0)
#define PV(ks, tl) do { if constexpr (!(tl)) SMF_V(av0, A[ks], wv0[ks]); else if constexpr (vslot(ks) < 0) SMF_V(av1, A[ks], wv1[ks]); else { const v8u lw_ = LW(vslot(ks)); SMF_V(av1, A[ks], lw_); } } while (0)
#define KXR(i) kx[i] = lds_r16(LB + C_K + ((i) & 1) * 4096 + ((i) >> 1) * 1024)
#define XW(q_) do { *(LAS u32*)(q_) = pa; *(LAS u32*)((q_) + 288) = pb; } while (0)
        auto slot = [&](auto zc) __attribute__((always_inline)) {
            constexpr int z = decltype(zc)::value;
            if constexpr (z < 14) PQ(z >> 1, z & 1);
            else if constexpr (z < 17) PQ(z - 7, 0);
            else if constexpr (z < 20) PQ(z - 10, 1);
            else if constexpr (z < 34) PK((z - 20) >> 1, z & 1);
            else if constexpr (z < 37) PK(z - 27, 0);
            else if constexpr (z < 40) PK(z - 30, 1);
            else if constexpr (z < 42) { if constexpr (z == 40) MF0(s0, kx[0], qf[0]); else MF0(s1, kx[1], qf[0]); }
            else if constexpr (z < 48) { if constexpr (z & 1) MF1(s1, kx[z - 40], qf[(z - 40) >> 1]); else MF1(s0, kx[z - 40], qf[(z - 40) >> 1]); }
            else if constexpr (z < 54) PV(z - 48, 0);
            else if constexpr (z == 54) MF0(sm, kmf[0], qf[0]);
            else if constexpr (z < 58) MF1(sm, kmf[z - 54], qf[z - 54]);
            else if constexpr (z < 62) PV(z - 52, 0);
            else if constexpr (z < 64) PV(z - 62, 1);
            else if constexpr (z == 64) MF0(oa, vx[0], P);
            else if constexpr (z == 65) MF0(ob, vx[1], P);
            else if constexpr (z == 66) MF0(rsum, ones, P);
            else if constexpr (z < 75) PV(z < 70 ? z - 63 : z == 70 ? 2 : z == 71 ? 7 : z == 72 ? 3 : z - 65, 1);
            else { const v2u ones2 = ones.lo; MF1H(rsum, ones2, pm); }
            if constexpr (z < 6) A[4 + z] = lds_r16(LB + C_MEM + (4 + z) * 1024);
            if constexpr (z >= 6 && z < 10) LF_RD(4 + ((z - 6) >> 1), z & 1);
            if constexpr (z >= 14 && z < 18) LF_RD(6 + ((z - 14) >> 1), z & 1);
            if constexpr (z >= 22 && z < 28) KXR(z - 22);
            if constexpr (z == 28) XW(qw);
            if constexpr (z == 29 || z == 30) KXR(z - 23);
            if constexpr (z == 32 || z == 47) asm volatile("s_barrier" ::: "memory");
            if constexpr (z >= 33 && z < 37) qf[z - 33] = lds_r16(qr + (z - 33) * 64);
            if constexpr (z == 20) ZRD(av0, 0);
            if constexpr (z == 43) XW(kw);
            if constexpr (z == 44 || z == 45) vx[z - 44] = lds_r16(VX + (z - 44) * 1024);
            if constexpr (z >= 48 && z < 52) kmf[z - 48] = lds_r16(qr + (C_KM - C_Q) + (z - 48) * 64);
            if constexpr (z == 52) ZRD(av1, 1);
            if constexpr (z == 37) ones = lds_r16(ZP + 64);
            if constexpr (z >= 57 && z < 64) LF_RD(8 + ((z - 57) >> 1), (z - 57) & 1);
            if constexpr (z >= 65 && z < 70) LF_RD(8 + ((z - 58) >> 1), (z - 58) & 1);
            if constexpr (z == HD_SMP) LD4A(fl, fkh, 0);
            SCHED();
            if constexpr (z == 19) HT(2);
            if constexpr (z == 32) HT(3);
            if constexpr (z == 47) HT(4);
            if constexpr (z == 64) HT(5);
            if constexpr (z == 19) LGK(4);
            if constexpr (z == 21 || z == 41) LGK(1);
            if constexpr (z == 56 || z == 73) LGK(0);
            if constexpr (z == 31 || z == 46) LGK(2);
            if constexpr (z == 39) LGK(4);
            if constexpr (z >= 53 && z < 56) LGK(57 - z);
            if constexpr (z == 64) LGK(3);
            if constexpr (z == 68) LGK(5);
            if constexpr (z >= 70 && z < 73) LGK(74 - z);
            SCHED();
            if constexpr (z == 18) CADD(fa, aq0[0], aq0[2]);
            if constexpr (z == 19) CADD(fb, aq0[1], aq0[3]);
            if constexpr (z == 20) CMULC(fa, fa);
            if constexpr (z == 21) CADD(fc, aq1[0], aq1[2]);
            if constexpr (z == 22) CADD(fd, aq1[1], aq1[3]);
            if constexpr (z == 23) CMULC(fc, fc);
            if constexpr (z == 24) CMULC(fb, fb);
            if constexpr (z == 25) CMULC(fd, fd);
            if constexpr (z == 26) CCVT(pa, fa, fc);
            if constexpr (z == 27) CCVT(pb, fb, fd);
            if constexpr (z == 38) CADD(fa, ak0[0], ak0[2]);
            if constexpr (z == 39) CADD(fb, ak0[1], ak0[3]);
            if constexpr (z == 41) { CADD(fc, ak1[0], ak1[2]); CADD(fd, ak1[1], ak1[3]); }
            if constexpr (z == 42) { CCVT(pa, fa, fc); CCVT(pb, fb, fd); }
            if constexpr (z >= 49 && z < 53) SEL(sx[z - 49], s0[z - 49], s1[z - 49]);
            if constexpr (z >= 53 && z < 57) VEXP(t[z - 53], sx[z - 53]);
            if constexpr (z == 57) CCVT(e01, t[0], t[1]);
            if constexpr (z == 58) CCVT(e23, t[2], t[3]);
            if constexpr (z == 59) QPERM(P.x, e01, "[0,1,0,1]");
            if constexpr (z == 60) QPERM(P.z, e01, "[2,3,2,3]");
            if constexpr (z == 61) QPERM(P.y, e23, "[0,1,0,1]");
            if constexpr (z == 62) QPERM(P.w, e23, "[2,3,2,3]");
            if constexpr (z == 63) CADD(fa, av0[0], av0[2]);
            if constexpr (z == 64) { asm volatile("" :: "v"(sm)); VEXP(tm0, sm[0]); }
            if constexpr (z == 65) VEXP(tm1, sm[1]);
            if constexpr (z == 66) CADD(fb, av0[1], av0[3]);
            if constexpr (z == 67) CCVT(pmm, tm0, tm1);
            if constexpr (z >= 68 && z < 72) SEL(oa[z - 68], oa[z - 68], ob[z - 68]);
            if constexpr (z == 72) asm volatile("v_cndmask_b32 %0, %1, 0, %2" : "=v"(pm.x) : "v"(pmm), "s"(himask));
            if constexpr (z == 73) asm volatile("v_cndmask_b32 %0, 0, %1, %2" : "=v"(pm.y) : "v"(pmm), "s"(himask));
            if constexpr (z == 74) LGK(15);
            if constexpr (z == 75) CCVT(vm.x, fa, fb);
            SCHED();
        };
#define HT0 ""
#define HT0O
        {
            u32 tmp;
            asm volatile("s_mov_b32 m0, %2\n\t" WAIT_TXT("%0", "%1") HT0 "buffer_load_dwordx4 %4, %5, %6 offen sc1 lds\n\tbuffer_load_dwordx4 %4, %5, %6 offen offset:1024 sc1 lds\n\t"
                         "s_mov_b32 m0, %3\n\ts_nop 0\n\tbuffer_load_dwordx4 %4, %5, %7 offen sc1 lds\n\t.p2align 6\n\t.rept %8\n\ts_nop 0\n\t.endr\n\ts_waitcnt vmcnt(0) lgkmcnt(0)\n\ts_barrier"
                         : "=&s"(tmp) : "s"(uu(tm - 4u)), "s"(dm), "s"(dm2), "v"(l16), "s"(srd), "s"(ms), "s"(ms2), "i"(AL_H) HT0O : "vcc", "scc", "memory");
        }
        HT(1);
#pragma unroll
        for (int ks = 0; ks < 4; ++ks) A[ks] = lds_r16(LB + C_MEM + ks * 1024);
        SCHED();
        st4s(rs, ofl, 0, sv);
        SCHED();
        static_for<0, 76>(slot);
        asm volatile("s_nop 2\n\tv_add_f32 %1, %3, %5\n\tv_add_f32 %2, %4, %6\n\tv_cvt_pk_bf16_f32 %0, %1, %2"
                     : "=v"(vm.y), "=&v"(fc), "=&v"(fd) : "v"(av1[0]), "v"(av1[1]), "v"(av1[2]), "v"(av1[3]));
        __builtin_amdgcn_s_waitcnt(0xCF7F);
        asm volatile("s_nop 0\n\tv_mfma_f32_16x16x16_bf16 %0, %2, %3, %0\n\tv_rcp_f32 %1, %4" : "+v"(oa), "=&v"(inv) : "v"(vm), "v"(pm), "v"(rsum[0]), "v"(rsum));
        SCHED();
        if (__builtin_expect(__builtin_amdgcn_ballot_w64(u_(rsum[0]) - 0x21800000u > 0x3C000000u) != 0ull, 0)) {
            DRAIN(); SCHED();
            bool miss = false;
#pragma unroll 1
            for (int i = 0; i < 10; ++i) { const v4u a = lds_r16(LB + C_MEM + i * 1024); miss |= a.x == PSENT || a.y == PSENT || a.z == PSENT || a.w == PSENT; }
            if (missed < SPIN_CAP && !all_(!miss)) {
                *(LAS u32*)(lds + C_DUMP + (w == 0 ? 0 : 64)) = ld4(rs, foff(F_ERR));
                asm volatile("" : "+v"(fl) :: "memory");
                tm = rdl(fl, 5);
                BAR();
                missed = uu(*(LAS u32*)(lds + C_DUMP)) != 0u ? SPIN_CAP : missed + 1;
                st4s(rs, missed == SPIN_CAP ? foff(F_ERR) : OOB, 0, 1u);
                BAR();
                continue;
            }
            float mx = fmaxf(fmaxf(fmaxf(sx[0], sx[1]), fmaxf(sx[2], sx[3])), fmaxf(sm[0], sm[1]));
            mx = max32(max16(fmaxf(mx, dpp<0x4E>(mx))));
#pragma unroll
            for (int i = 0; i < 4; ++i) t[i] = __builtin_amdgcn_exp2f(sx[i] - mx);
            e01 = pk(t[0], t[1]); e23 = pk(t[2], t[3]);
            pmm = pk(__builtin_amdgcn_exp2f(sm[0] - mx), __builtin_amdgcn_exp2f(sm[1] - mx));
            asm volatile("v_cndmask_b32 %0, %2, 0, %3\n\tv_cndmask_b32 %1, 0, %2, %3" : "=&v"(pm.x), "=&v"(pm.y) : "v"(pmm), "s"(himask));
            asm volatile("s_nop 1" : "+v"(e01), "+v"(e23), "+v"(pm));
            QPERM(P.x, e01, "[0,1,0,1]"); QPERM(P.z, e01, "[2,3,2,3]"); QPERM(P.y, e23, "[0,1,0,1]"); QPERM(P.w, e23, "[2,3,2,3]");
            NOPS(1);
            MF0(rsum, ones, P); MF0(oa, vx[0], P); MF0(ob, vx[1], P);
            NOPS(7);
            { const v2u ones2 = ones.lo; MF1H(rsum, ones2, pm); }
#pragma unroll
            for (int i = 0; i < 4; ++i) SEL(oa[i], oa[i], ob[i]);
            NOPS(1);
            MF1H(oa, vm, pm);
            asm volatile("s_nop 7\n\tv_rcp_f32 %0, %1" : "=v"(inv) : "v"(rsum[0]), "v"(rsum));
        }
        {
            v4u oc; f32x2 u0 = oa.lo, u1 = oa.hi; const f32x2 i2 = {inv, inv};
            asm volatile("s_nop 0\n\tv_pk_mul_f32 %0, %0, %2\n\tv_pk_mul_f32 %1, %1, %2" : "+v"(u0), "+v"(u1) : "v"(i2));
            __builtin_amdgcn_s_waitcnt(0xCF7F);
            asm volatile("v_cvt_pk_bf16_f32 %0, %4, %5\n\tv_cvt_pk_bf16_f32 %2, %4, %5\n\tv_cvt_pk_bf16_f32 %1, %6, %7\n\tv_cvt_pk_bf16_f32 %3, %6, %7\n\t"
                         "v_permlane16_swap_b32 %0, %2\n\ts_nop 0\n\tv_permlane16_swap_b32 %1, %3"
                         : "=&v"(oc.x), "=&v"(oc.y), "=&v"(oc.z), "=&v"(oc.w) : "v"(u0.x), "v"(u0.y), "v"(u1.x), "v"(u1.y));
            st16(rs, oso, oc);
        }
        SCHED();
        unsigned long long clk;
        asm volatile("s_memrealtime %0" : "=s"(clk) :: "memory");
        HT(6);
        v4u tq[4], tk[8], tv[2];
        f32x4 c0, c1, cm;
        u32 oo, kvo; bool kvok;
        u32 ln = l16; PIN(ln);
        lptr QX = lds + C_QX + (w & 1) * 4096 + ln, VO = lds + C_V + (w ^ 1) * 2048 + ln, VMX = lds + C_VM + w * 512 + (ln >> 1);
#pragma unroll
        for (int i = 0; i < 2; ++i) { tq[i] = lds_r16(QX + i * 1024); kmf[i] = lds_r16(qr + (C_KM - C_Q) + i * 64); tk[2 * i] = lds_r16(LB + C_K + i * 1024); tk[2 * i + 1] = lds_r16(LB + C_K + 4096 + i * 1024); }
        *(LAS v2u*)VMX = vm;
        SCHED();
        SFOR(i, 12)
            if constexpr (i == 0) MF0(cm, kmf[0], tq[0]); else if constexpr (i == 1) MF0(c0, tk[0], tq[0]); else if constexpr (i == 2) MF0(c1, tk[1], tq[0]);
            else if constexpr (i % 3 == 0) MF1(cm, kmf[i / 3], tq[i / 3]); else if constexpr (i % 3 == 1) MF1(c0, tk[2 * (i / 3)], tq[i / 3]); else MF1(c1, tk[2 * (i / 3) + 1], tq[i / 3]);
            if constexpr (i < 2) kmf[2 + i] = lds_r16(qr + (C_KM - C_Q) + (2 + i) * 64);
            if constexpr (i == 0) { tq[2] = lds_r16(QX + 2048); tk[4] = lds_r16(LB + C_K + 2048); }
            if constexpr (i == 1) { tk[5] = lds_r16(LB + C_K + 6144); tq[3] = lds_r16(QX + 3072); }
            if constexpr (i == 2) { tk[6] = lds_r16(LB + C_K + 3072); tk[7] = lds_r16(LB + C_K + 7168); }
            if constexpr (i == 3) { tv[0] = lds_r16(VO); tv[1] = lds_r16(VO + 1024); }
            if constexpr (i == 4) { oo = ln + (ln & 0x300u) + ((vsrc ^ 8192u) + (u32)((w & 1) * 256) + ((u32)s & OOB)); PIN(oo); }
            if constexpr (i == 5) { ksrc += NH * 16384; vsrc += NH * 16384; PINS(ksrc); PINS(vsrc); }
            if constexpr (i == 9) { asm volatile("s_waitcnt vmcnt(1)" : "+v"(fl) :: "memory"); kvok = ((u32)__builtin_amdgcn_ballot_w64(fl < (u32)s + 2) & 7u) == 0u; }
            if constexpr (i == 10) { kvo = l16 | (kvok ? 0u : OOB); PIN(kvo); }
            SCHED();
        SEND
        asm volatile("s_waitcnt lgkmcnt(0)\n\ts_barrier" : "+s"(clk) :: "memory");
        const v2u tvm = *(LAS v2u*)(VMX + (512 - (w & 1) * 1024));
        const v4u pv = lds_r16(ZP + 80);
        DMA32(dk, kvo, ksrc);
        SCHED(); asm volatile("" :: "v"(cm));
        float u[10], m, m2;
        {
            float NEG = -1000000000.0f / SCALE; PIN(NEG);
#define MSK(d, a, i) asm volatile("v_cndmask_b32 %0, %1, %2, %3" : "=v"(d) : "v"(NEG), "v"(a), "s"(vis[i]))
#pragma unroll
            for (int i = 0; i < 4; ++i) MSK(u[i], c0[i], i);
#pragma unroll
            for (int i = 0; i < 4; ++i) MSK(u[4 + i], c1[i], 4 + i);
            asm volatile("v_max3_f32 %0, %2, %3, %10\n\tv_max3_f32 %1, %4, %5, %11\n\tv_max3_f32 %0, %0, %6, %7\n\tv_max3_f32 %1, %1, %8, %9\n\t"
                         "v_max_f32 %0, %0, %1\n\tv_mov_b32 %1, %0\n\ts_nop 1\n\tv_permlane16_swap_b32 %0, %1\n\tv_max_f32 %0, %0, %1\n\tv_mov_b32 %1, %0\n\ts_nop 1\n\t"
                         "v_permlane32_swap_b32 %0, %1\n\tv_max_f32 %0, %0, %1\n\tv_mul_f32 %0, 0xbe0293ee, %0"
                         : "=&v"(m), "=&v"(m2) : "v"(u[0]), "v"(u[1]), "v"(u[2]), "v"(u[3]), "v"(u[4]), "v"(u[5]), "v"(u[6]), "v"(u[7]), "v"(cm[0]), "v"(cm[1]));
        }
        DMA32(dv, kvo, vsrc);
        {
            v4u px; v2u p0, p1;
#define EX2(d, a, b) asm volatile("v_fmamk_f32 %0, %2, 0x3e0293ee, %4\n\tv_fmamk_f32 %1, %3, 0x3e0293ee, %4\n\tv_exp_f32 %0, %0\n\tv_exp_f32 %1, %1" : "=&v"(d.x), "=&v"(d.y) : "v"(a), "v"(b), "v"(m))
            f32x2 ex[5];
            EX2(ex[0], u[0], u[1]); EX2(ex[1], u[2], u[3]); EX2(ex[2], u[4], u[5]); EX2(ex[3], u[6], u[7]); EX2(ex[4], cm[0], cm[1]);
            asm volatile("v_cvt_pk_bf16_f32 %0, %8, %9\n\tv_cvt_pk_bf16_f32 %1, %10, %11\n\tv_cvt_pk_bf16_f32 %2, %12, %13\n\tv_cvt_pk_bf16_f32 %3, %14, %15\n\t"
                         "v_mov_b32 %5, 0\n\tv_mov_b32 %6, 0\n\tv_cvt_pk_bf16_f32 %4, %16, %17\n\tv_cvt_pk_bf16_f32 %7, %16, %17"
                         : "=&v"(px.x), "=&v"(px.y), "=&v"(px.z), "=&v"(px.w), "=&v"(p0.x), "=&v"(p0.y), "=&v"(p1.x), "=&v"(p1.y)
                         : "v"(ex[0].x), "v"(ex[0].y), "v"(ex[1].x), "v"(ex[1].y), "v"(ex[2].x), "v"(ex[2].y), "v"(ex[3].x), "v"(ex[3].y), "v"(ex[4].x), "v"(ex[4].y));
            f32x4 o[4], sum;
            const v2u ones2 = ones.lo;
            const u32 now = (u32)clk;
            u32 oz;
            SCHED();
            MF0(sum, ones, px); { const u32 q = ksrc ^ 8192u; DMA32(dq, kvo, q); } SCHED();
            MF0(o[0], vx[0], px);
            MF0(o[1], vx[1], px);
            MF0(o[2], tv[0], px); oz = oso ^ (oslot(s) ^ oslot(s + 3)); PIN(oz); SCHED();
            MF0(o[3], tv[1], px); oso ^= oslot(s) ^ oslot(s + 1); PIN(oso); SCHED();
            MF1H(sum, ones2, p0);
            MF1H(o[0], vm, p0);
            MF1H(o[1], vm, p1); st16(rs, oz, pv); SCHED();
            MF1H(o[2], tvm, p0);
            MF1H(o[3], tvm, p1);
            asm volatile("s_nop 2\n\tv_rcp_f32 %0, %1\n\ts_nop 0" : "=v"(inv) : "v"(sum[0]), "v"(sum));
            asm volatile("" : "+v"(o[0]), "+v"(o[1]));
            st16(rs, oo, (v4u){pk2(o[0].lo * inv), pk2(o[0].hi * inv), pk2(o[1].lo * inv), pk2(o[1].hi * inv)});
            asm volatile("" : "+v"(o[2]), "+v"(o[3]));
            st16(rs, oo ^ 2048u, (v4u){pk2(o[2].lo * inv), pk2(o[2].hi * inv), pk2(o[3].lo * inv), pk2(o[3].hi * inv)});
            SCHED();
            LS2(L == 0 && w == 0, h);
            st4s(rs, ((h & 15) == 0 && tid == 192) ? hint + 128u + (u32)h * 8u : OOB, 0, (u32)s << 16 | min(missed, 1u) << 14 | min(now - tm, 0x3FFFu));
            tm = rdl(fl, 5);
            missed = missed == SPIN_CAP ? missed : 0u;
            if (__builtin_expect(s < 1 - L, 0)) {
                if (s < 0) oso -= (ln & 256u) ? 0u : OOB;
#pragma unroll 1
                for (int n = 0; n < (s < 0 ? 0 : 200); ++n) {
                    CFENCE();
                    tm = rdl(ld4(rs, fkh), 6);
                    if (tm != PSENT) break;
                }
            }
        }
        if (__builtin_expect(!kvok, 0) && s + 1 < NSEG) {
            st4s(rs, ln == 0 ? hint + 4096u + (u32)h * 128u : OOB, 0, (u32)s + 1u);
            keys_wait((u32)s + 2u);
        }
        ++s;
    }
    DRAIN(); BAR();
    st4s(rs, ofl, 0, NSEG);
    LDUMP(w == 0, 160 + L * 32 + h);
}

#define WO_NL 3
#define WO_ROT w
#define WO_AH 1
DEV void role_wo(const Params& p, lptr lds, int j, v2u ep) {
    const int tid = threadIdx.x, lane = tid & 63, w = uni(tid >> 6), g = lane >> 4, r = lane & 15;
    const int rg = j & 1, cg = j >> 1, rot = WO_ROT;
    const rsrc_t rs = mk_rsrc(p.ws, W_END);
    v8u wo[8][5];
    {
        const v4u so_ = mk_srd(p.Wo, DM * NQ * 2);
        u32 vo[5];
#pragma unroll
        for (int i = 0; i < 5; ++i) { const int n = 64 * i + lane; vo[i] = (u32)((n / 5) * DM + cg * 40 + (n % 5) * 8) * 2u; }
        v4u junk = {};
        const u32 av = (u32)(j * 5120 + lane * 16);
        auto ahead = [&](int b) {
#pragma unroll
            for (int i = 0; i < 5; ++i) AHEAD(so_, av, uu((8 * w + ((b + rot) & 7)) * (DH * DM * 2) + i * 1024));
        };
        auto fill = [&](int b) {
            if (b + WO_AH < 8) ahead(b + WO_AH);
            const u32 dst = uu(w * 40960 + (b & 3) * 10240), src = uu((8 * w + ((b + rot) & 7)) * (DH * DM * 2));
#pragma unroll
            for (int i = 0; i < 10; ++i) DMAIN(so_, dst + i * 1024, vo[i % 5], src + (i / 5) * (64 * DM * 2));
        };
#pragma unroll 1
        for (int b = 0; b < WO_AH; ++b) ahead(b);
#pragma unroll 1
        for (int b = 0; b < 4; ++b) fill(b);
        const int q4 = r >> 2;
        lptr tb = lds + w * 40960 + (32 * (g >> 1) + 16 * (q4 >> 1) + 8 * ((r >> 1) & 1) + 4 * (g & 1) + (q4 & 1)) * 80 + (r & 1) * 8;
        SFOR(b, 8)
            VMW((b < 5 ? 30 : 70 - 10 * b) + (b + 1 + WO_AH < 8 ? 5 : 0) + (b + 2 + WO_AH < 8 ? 5 : 0) + (b + 3 + WO_AH < 8 ? 5 : 0));
            if constexpr (b + WO_AH == 7) PIN(junk);
#pragma unroll
            for (int t = 0; t < 5; ++t) {
                lptr q = tb + (b & 3) * 10240 + t * 16;
                wo[b][t] = tr_frag(q, q + 160, q + 64 * 80, q + 64 * 80 + 160);
                if (b * 5 + t < 32) asm volatile("" : "+a"(wo[b][t])); else asm volatile("" : "+v"(wo[b][t]));
            }
            CFENCE();
            if constexpr (b < 4) fill(b + 4);
        SEND
    }
    if (j == 0) LST(265, rt32());
    LST(1100 + j * 4 + w, rt32());
    const u32 idx = (r & 2) ? 0xEEEEu : 0x4444u;
    const u32 pol0 = pol_wait(rs, ep);
    BAR();
    const u32 l16 = (u32)lane * 16u;
    u32 oo[8];
#pragma unroll
    for (int i = 0; i < 8; ++i) oo[i] = uu(ring_map((u32)(rg * 32768 + (8 * w + ((i + rot) & 7)) * 1024)));
    const u32 hint = (pol0 ^ 16384u) + 4 * 65536u;
    u32 hvo[2];
#pragma unroll
    for (int l = 0; l < 2; ++l) hvo[l] = lane < 2 ? hint + (u32)(l ^ 1) * MB + (u32)lane * 4u : lane == 2 ? hint + (u32)l * MB + 128u : lane == 3 ? foff(F_ERR) : lane == 4 ? hint + (u32)l * MB + 256u :
                 (lane >= 32 && j == 0 && w == 3) ? hint + (u32)l * MB + (u32)lane * 128u : OOB;
    lptr xw = r < 8 ? lds + w * 640 + (2 * (g & 1) + (g >> 1)) * 32 + r * 4 : lds + 8192 + lane * 4;
    const int G = ((5 * cg) >> 1) + w, tt = 2 * G + ((lane >> 3) & 1) - 5 * cg;
    const bool pub = w < 3 && lane < 32 && tt >= 0 && tt < 5;
    lptr xr = lds + (pub ? tt * 128 + (2 * (lane >> 4) + ((lane >> 2) & 1)) * 32 + (lane & 3) * 8 : 0);
    const u32 mpo = pub ? (pol0 ^ 16384u) + ring_map((u32)(G * 256 + rg * 128 + lane * 4)) : OOB;
    constexpr u32 PER0 = CH_W0 + CH_MW + CH_H0 + CH_MH + 2 * CH_VIS, DW0 = CH_W0 + CH_VIS + CH_MW + CH_REC;
    const bool lead = j == 0 && w == 3;
    const u32 hso = (lead && lane == 0) ? hint : OOB;
    MK_SRD();
    u32 t = 0, tn = 0;
    int sn[2] = {0, 0};
    u32 hav = 8 * CH_H0, wav = 8 * CH_W0, mh = 8 * CH_MH, mw = 8 * CH_MW, ok[2] = {0u, 0u}, dw[2] = {DW0, CH_T0 + CH_REC - 8};
    u32 vo = l16 + pol0 + MB, tp = 0, fin, left, cnt = 0;
    unsigned long long clk = 0;
    {
        if (w == 0) LST(1310 + j, rt32());
        if (lead) {
#pragma unroll 1
            for (int n = 0; n < SPIN_CAP; ++n) {
                CFENCE();
                const u32 a = ld4(rs, foff(F_QKV + lane)), b = ld4(rs, lane < 32 ? foff(F_QKV + 64 + lane) : hvo[0]);
                if (all_(a != 0u && b != (lane < 32 ? 0u : PSENT))) break;
                __builtin_amdgcn_s_sleep(1);
            }
        }
        if (lead) {
            t = rt32() + CH_T0;
            st16(rs, hso, (v4u){t, PER0, t + DW0, t + DW0});
        } else {
#pragma unroll 1
            for (int n = 0; n < SPIN_CAP; ++n) {
                CFENCE();
                const u32 hv = ld4(rs, hvo[1]);
                t = rdl(hv, 0);
                if (rdl(hv, 1) < 400u) break;
                __builtin_amdgcn_s_sleep(1);
            }
        }
    }
    const u32 tm0 = t + DW0;
    tn = t; t -= CH_T0; clk = t - 100u;
    u32 zero = 0, hv = 0xFFFFu, pst = 1, pmp = OOB;
    const u32 psent = PSENT;
    lptr sw = lds + 2560 + w * 4;
    asm volatile("" : "+v"(zero), "+v"(sw));
    auto turn = [&](auto lc) __attribute__((always_inline)) {
        constexpr int L = decltype(lc)::value, M = L ^ 1;
        u32 done, rep, rd, lost, dh, dn, nt, za, pdur, mp; v4u hw;
        const int sc = sn[L];
        auto books = [&](auto ic) __attribute__((always_inline)) {
            constexpr int i = decltype(ic)::value;
            if constexpr (i == 0) { rep = max(rdl(hv, 2), rdl(hv, 4)); PINS(rep); }
            if constexpr (i == 1) { lost = (u32)(__builtin_amdgcn_ballot_w64(hv == (u32)sn[M]) >> 32); PINS(lost); }
            if constexpr (i == 2) { lost |= (rep >> 16) ^ (u32)sn[M]; PINS(lost); }
            if constexpr (i == 3) { lost |= ok[M] ^ 2u; rd = rep & 0xFFFFu; PINS(lost); PINS(rd); }
            if constexpr (i == 4) { lost = lost == 0u ? CH_UP * 8u : 0u; PINS(lost); }
            if constexpr (i == 5) { pdur = ((u32)clk - tp) * 8u; PINS(pdur); pdur = min(pdur, wav + CH_RISE); PINS(pdur); }
            if constexpr (i == 6) { pdur = (done & ok[M]) ? pdur : wav; PINS(pdur); }
            if constexpr (i == 7) { wav = max(pdur, wav - CH_FALL); PINS(wav); }
            if constexpr (i == 8) { nt = min(rd * 8u, hav + CH_RISE); PINS(nt); }
            if constexpr (i == 9) { nt = rd < 0x3FFFu ? nt : hav; PINS(nt); }
            if constexpr (i == 10) { hav = max(nt, hav - CH_FALL); PINS(hav); }
            if constexpr (i == 11) { mw = max(mw - 1u, CH_MW * 8u); PINS(mw); }
            if constexpr (i == 12) { mh = max(mh - 1u, CH_MH * 8u); PINS(mh); }
            if constexpr (i == 13) { rd = rd >> 14 == 1u ? lost : 0u; PINS(rd); }
            if constexpr (i == 14) { mw += rd; mh += lost - rd; PINS(mw); PINS(mh); }
            if constexpr (i == 15) { ok[M] = done ? 1u : (ok[M] & 1u) << 1; PINS(ok[M]); }
            if constexpr (i == 16) { dh = (hav + mh) / 8u + (sn[1] < 3 ? CH_VIS + CH_PRE : CH_VIS); PINS(dh); }
            if constexpr (i == 17) { dn = (wav + mw) / 8u + (sn[1] < 3 ? CH_VIS + CH_PRW : CH_VIS); PINS(dn); }
            if constexpr (i == 18) { nt = dw[L] + dh + (u32)max((int)((u32)clk + CH_WX - t), 0); PINS(nt); }
            if constexpr (i == 19) { dh += dn; dw[L] = dn; sn[M] += (int)done; PINS(dh); PINS(sn[M]); }
            if constexpr (i == 20) { rd = tn - t + (dh >> 1); PINS(rd); }
            if constexpr (i == 21) { lost = (u32)sc - 1u < NSEG - 2 ? CH_RTY : 0u; PINS(lost); nt += ok[L] != 1u ? lost : 0u; PINS(nt); }
            if constexpr (i == 22) { nt = t + (u32)max((int)nt, (int)rd); PINS(nt); }
            if constexpr (i == 23) { hw.x = nt; hw.y = dh; PIN(hw.x); PIN(hw.y); }
            if constexpr (i == 24) { hw.z = nt + dn; hw.w = tm0; PIN(hw); }
            if constexpr (i == 25) st16(rs, hso + L * MB, hw);
        };
        {
#define WT(k) do {} while (0)
            v4u d[8];
            f32x4 acc[5] = {};
            done = uu(pst) ^ 1u; PINS(done);
            za = pmp | pst << 31;
            *(LAS u32*)(sw + L * 4096) = vo >> 31;
            mp = mpo + (L * MB + (u32)((sc + 1) & 3) * 65536u); pmp = mpo + (L * MB + (u32)(sc & 3) * 65536u);
            asm volatile("" : "+v"(acc[0]), "+v"(acc[1]), "+v"(acc[2]), "+v"(acc[3]), "+v"(acc[4]), "+v"(mp), "+v"(pmp), "+v"(za));
            {
                u32 tmp;
                asm volatile(WAIT_TXT("%1", "%2") "buffer_load_dwordx4 %0, %3, %4, %5 offen sc1\n\ts_waitcnt lgkmcnt(0)"
                             : "=&v"(d[0]), "=&s"(tmp) : "s"(uu(t - 4u)), "v"(vo), "s"(srd), "s"(oo[0]) : "vcc", "scc", "memory");
                PINS(clk);
                WT(0);
            }
            SFOR(i, 7)
                asm volatile("buffer_load_dwordx4 %0, %1, %2, %3 offen sc1" : "=v"(d[i + 1]) : "v"(vo), "s"(srd), "s"(oo[i + 1]) : "memory");
                SCHED();
                static_for<(25 * i) / 7, (25 * (i + 1)) / 7>(books);
                SCHED();
            SEND
            WT(1);
            float f0[5], f1[5]; unsigned long long bad;
#define WSW(o_) asm volatile("v_permlane32_swap_b32 %0, %1" : "+v"(f0[o_]), "+v"(f1[o_]))
#define WAD(o_) asm volatile("v_add_f32_dpp %0, %1, %0 row_ror:8 row_mask:0xf bank_mask:0xf" : "+v"(f0[o_]) : "v"(f1[o_]))
#define WFO(o_) do { VADD(f0[o_], acc[o_][0], acc[o_][2]); VADD(f1[o_], acc[o_][1], acc[o_][3]); } while (0)
#define WWR(o_) *(LAS float*)(xw + L * 4096 + (o_) * 128) = f0[o_]
            u32 th, per, so;
            auto next = [&](auto ic_) __attribute__((always_inline)) {
                constexpr int i = decltype(ic_)::value;
                if constexpr (i == 0) { so = (u32)(sn[M] & 3) * 0x42000u; PINS(so); }
                if constexpr (i == 1) { so = (so & 0xC4000u) ^ (pol0 + M * MB); PINS(so); }
                if constexpr (i == 2) { so = sn[M] + 1 < NSEG ? so : OOB; PINS(so); }
                if constexpr (i == 4) { vo = l16 + so; PIN(vo); }
                if constexpr (i == 5 && !L) { fin = (u32)(sn[0] + sn[1] + 2); PINS(fin); }
                if constexpr (i == 6 && L == 0) { fin = (fin | uu(left)) >> 7; PIN(fin); }
                if constexpr (i == 6 && L == 1) { cnt += 1u; PIN(cnt); left = cnt >> 9; PIN(left); }
                if constexpr (i == 7) { PIN(hv); th = rdl(hv, 0); PINS(th); }
                if constexpr (i == 8) { per = rdl(hv, 1); PINS(per); so = th - t; PINS(so); }
                if constexpr (i == 9) { per = (int)so < 20 ? per : 0u; PINS(per); }
                if constexpr (i == 10) { th += per; PINS(th); if constexpr (!L) { per = rdl(hv, 3); PINS(per); } }
                if constexpr (i == 11) { tp = t; t = lead ? tn : th; PINS(t); }
                if constexpr (i == 12) { if constexpr (!L) { fin |= per; PIN(fin); } tn = nt; }
            };
            auto slot = [&](auto ic_) __attribute__((always_inline)) {
                constexpr int i = decltype(ic_)::value;
                constexpr int NL = WO_NL, I0 = 40 - 5 * NL, pc = i < I0 ? i / 5 : 8 - NL + (i - I0) % NL, oc = i < I0 ? i % 5 : (i - I0) / NL;
                f32x4& ac = acc[oc]; v4u& dp = d[pc]; v8u& wp = wo[pc][oc];
                if constexpr (oc == 0) {
                    if constexpr (pc * 5 < 32) asm volatile("s_waitcnt vmcnt(%4)\n\tv_smfmac_f32_16x16x64_bf16 %0, %1, %2, %3" : "+v"(ac), "+v"(dp) : "a"(wp), "v"(idx), "i"(pc < 2 ? 7 - pc : 9 - pc));
                    else asm volatile("s_waitcnt vmcnt(%4)\n\tv_smfmac_f32_16x16x64_bf16 %0, %1, %2, %3" : "+v"(ac), "+v"(dp) : "v"(wp), "v"(idx), "i"(9 - pc));
                } else if constexpr (pc * 5 + oc < 32) SMF_A(ac, dp, wp); else SMF_V(ac, dp, wp);
                SCHED();
                if constexpr (i == 0) WT(2);
                if constexpr (i < 25 && oc == 0) LGK(15);
                if constexpr (i == 6) { u32& h_ = hv; (void)&srd; LD4A(h_, hvo[L], 0); }
                if constexpr (i == 8) books(IC<25>{});
                if constexpr (i >= 11 && i < 19 && i != 15) next(IC<i - 11 - (i > 15)>{});
                if constexpr (i == 20) WT(4);
                constexpr int e = i - (I0 + NL + 1), o = e < 0 ? 0 : e / 3;
                if constexpr (e >= 0 && e % 3 == 0) { CADD(f0[o], acc[o][0], acc[o][2]); if constexpr (o > 0) WAD(o - 1); }
                if constexpr (e >= 0 && e % 3 == 1) { if constexpr (o > 0) { WWR(o - 1); SCHED(); } CADD(f1[o], acc[o][1], acc[o][3]); }
                if constexpr (e >= 0 && e % 3 == 2) {
                    if constexpr (o == 0) asm volatile("v_cmp_u_f32 %0, %1, %2" : "=s"(bad) : "v"(f0[0]), "v"(f1[0]));
                    else LGK(15);
                    WSW(o);
                }
                SCHED();
            };
            WT(3);
            static_for<0, 40>(slot);
            WT(5);
            static_assert(WO_NL == 3, "tail");
            if (__builtin_expect(bad != 0ull, 0)) {
                bool miss = false;
#pragma unroll
                for (int i = 0; i < 8; ++i) miss |= d[i].x == PSENT;
                if (all_(!miss)) {
#pragma unroll
                    for (int i = 0; i < 8; ++i) miss |= d[i].y == PSENT || d[i].z == PSENT || d[i].w == PSENT;
                }
                if (miss) *(LAS u32*)(sw + L * 4096) = 1u;
            }
            asm volatile("v_permlane32_swap_b32 %0, %1\n\ts_nop 1\n\tv_add_f32_dpp %0, %1, %0 row_ror:8 row_mask:0xf bank_mask:0xf\n\tv_add_f32 %2, %4, %6\n\tv_add_f32 %3, %5, %7"
                         : "+v"(f0[3]), "+v"(f1[3]), "=&v"(f0[4]), "=&v"(f1[4]) : "v"(acc[4][0]), "v"(acc[4][1]), "v"(acc[4][2]), "v"(acc[4][3]));
            WWR(3); SCHED();
            asm volatile("s_nop 0\n\tv_permlane32_swap_b32 %0, %1\n\ts_nop 1\n\tv_add_f32_dpp %0, %1, %0 row_ror:8 row_mask:0xf bank_mask:0xf" : "+v"(f0[4]), "+v"(f1[4]));
            WWR(4);
            WT(6);
            f32x2 a, b, c, e; u32 mq; v4u sv;
            asm volatile("s_waitcnt lgkmcnt(0)\n\ts_barrier\n\tds_read_b128 %4, %6 offset:%7+2560\n\t"
                         "ds_read_b64 %0, %5 offset:%7\n\tds_read_b64 %1, %5 offset:%7+640\n\tds_read_b64 %2, %5 offset:%7+1280\n\tds_read_b64 %3, %5 offset:%7+1920"
                         : "=&v"(a), "=&v"(b), "=&v"(c), "=&v"(e), "=&v"(sv) : "v"(xr), "v"(zero), "i"(L * 4096) : "memory");
            asm volatile("s_waitcnt lgkmcnt(4)\n\tv_or_b32 %4, %6, %7\n\tv_or3_b32 %4, %4, %8, %9\n\tv_lshl_or_b32 %5, %4, 31, %10\n\t"
                         "s_waitcnt lgkmcnt(2)\n\tv_pk_add_f32 %0, %0, %1\n\ts_waitcnt lgkmcnt(0)\n\tv_pk_add_f32 %2, %2, %3\n\ts_nop 0\n\tv_pk_add_f32 %0, %0, %2"
                         : "+v"(a), "+v"(b), "+v"(c), "+v"(e), "=&v"(pst), "=&v"(mq) : "v"(sv.x), "v"(sv.y), "v"(sv.z), "v"(sv.w), "v"(mp) : "memory");
            st4s(rs, mq, 0, pkv(a));
            SCHED();
            asm volatile("s_memrealtime %0" : "=s"(clk));
            WT(7);
            st4s(rs, za, 0, psent);
            VMW(3); SCHED();
            static_for<7, 13>(next);
        }
    };
    do {
        turn(IC<1>{});
        turn(IC<0>{});
    } while (uu(fin) == 0u);
    if (sn[0] + 1 < NSEG || sn[1] + 1 < NSEG) st4s(rs, tid == 0 ? foff(F_ERR) : OOB, 0, 1u);
    if (w == 0) LST(1900 + j, rt32());
    LDUMP(w == 0, 224 + j);
}

__global__ void __launch_bounds__(256, 1) k_main(Params p) {
    lptr lds = (lptr)(u32)16 - 16;
    if (blockIdx.x == 0) LST(256, rt32());
    unsigned long long pc; int len;
    asm volatile("s_getpc_b64 %0\n.Lk_main_here:\n\ts_mov_b32 %1, .Lk_end_here-.Lk_main_here" : "=s"(pc), "=s"(len));
    const rsrc_t rs = mk_rsrc(p.ws, W_END);
    const v2u e0 = ld8s(rs, threadIdx.x == 0 ? foff(F_EPOCH) : OOB, 0);
    const v4u code = __builtin_amdgcn_raw_buffer_load_b128(mk_rsrc((const void*)(pc & ~15ull), (u32)max(len, 0)), (((blockIdx.x >> 3) & 31u) * 256u + threadIdx.x) * 16u, 0, 0);
    CFENCE();
    auto conv_id = [](int role) {
        const int x = role >> 5, i = role & 31, no = x < 5 ? 5 : 10, og = x < 5 ? x - 3 : 2 * (x - 4) + (i >= 5), r = (x - 3) * 5 + i - no, q = r / 3;
        return x < 3 || i >= no + 5 || r >= 24 ? NCV : i < no ? ((og & 1) ? (og >> 1) * 5 + i % 5 : NCV) : r % 3 == 0 ? 20 + (q & 1) * 20 + (q >> 1) : 16 + (r % 3) * 8 + q;
    };
    int role = (int)(((u32)__builtin_amdgcn_s_getreg(20 | (3 << 11)) & 7u) * 32u + (blockIdx.x >> 3));
    Early e;
    v4u rv;
#pragma unroll 1
    for (int n = 0;; ++n) {
        conv_early(p, rs, conv_id(role), e);
        if (n) break;
        CFENCE();
        if (threadIdx.x == 0) {
            GAS unsigned long long* slots = (GAS unsigned long long*)(p.ws + W_CTRL + F_SLOT * 128);
            const unsigned long long tag = (unsigned long long)e0.y << 32 | (e0.x + 1u);
            u32 mine = (u32)role;
#pragma unroll 1
            for (int k = 0; k < 256 && __hip_atomic_exchange(slots + mine * 16, tag, __ATOMIC_RELAXED, __HIP_MEMORY_SCOPE_AGENT) == tag; ++k) mine = (mine + 1u) & 255u;
            lds_w16(lds, (v4u){mine, e0.x, e0.y, 0u});
        }
        BAR();
        rv = lds_r16(lds);
        BAR();
        if (uni((int)rv.x) == role) break;
        role = uni((int)rv.x);
    }
    const int x = role >> 5, i = role & 31, w = uni(threadIdx.x >> 6), no = x < 5 ? 5 : 10, nr = x < 7 ? 5 : 4, j = conv_id(role);
    const v2u ep = {uu(rv.y), uu(rv.z)};
    asm volatile("" :: "v"(code));
    if (x == 1) { role_wo(p, lds, i, ep); __builtin_amdgcn_endpgm(); }
    if (x < 3) { role_head(p, lds, i >> 4, x * 8 + (i & 15), ep); __builtin_amdgcn_endpgm(); }
    if (i >= no + nr) { role_qkv(p, lds, (x < 5 ? (x - 3) * 22 : 44 + (x - 5) * 17) + i - no - nr, ep); __builtin_amdgcn_endpgm(); }
    if (j < NCV) conv_stage(p, rs, lds, j, ep, e);
    if (i >= no) {
        if (j >= 24 && j < NPRE) role_row(p, lds, (j - 24) * 4 + w, ep);
        __builtin_amdgcn_endpgm();
    }
    const int og = x < 5 ? x - 3 : 2 * (x - 4) + (i >= 5);
    role_out(p, lds, og & 1, og >> 1, i % 5, ep);
}
__global__ void k_end() { asm volatile(".Lk_end_here:"); }

extern "C" void kernel_launch(void* const* d_in, const int* in_sizes, int n_in, void* d_out, int out_size, void* d_ws, size_t ws_size,
                              hipStream_t stream) {
    Params p;
    p.x = (const u16*)d_in[0]; p.mem0 = (const u16*)d_in[1]; p.Wq = (const u16*)d_in[2]; p.Wk = (const u16*)d_in[3];
    p.Wv = (const u16*)d_in[4]; p.Wo = (const u16*)d_in[5];
    p.bk = (const u16*)d_in[8]; p.bv = (const u16*)d_in[9]; p.shift = (const u16*)d_in[10]; p.ssmk = (const u16*)d_in[11]; p.ssmD = (const u16*)d_in[12];
    p.out = (u16*)d_out;
    p.ws = (unsigned char*)(((size_t)d_ws + 2 * MB - 1) & ~(size_t)(2 * MB - 1));
    (void)hipFuncSetAttribute((const void*)k_main, hipFuncAttributeMaxDynamicSharedMemorySize, LDS_BYTES);
    hipLaunchKernelGGL(k_main, dim3(256), dim3(256), LDS_BYTES, stream, p);
}
```

```cpp
#include <hip/hip_runtime.h>
#include <hip/hip_bf16.h>

typedef unsigned short u16;
typedef unsigned int u32;
typedef u32 v2u __attribute__((ext_vector_type(2)));
typedef u32 v4u __attribute__((ext_vector_type(4)));
typedef u32 v8u __attribute__((ext_vector_type(8)));
typedef float f32x2 __attribute__((ext_vector_type(2)));
typedef float f32x4 __attribute__((ext_vector_type(4)));
typedef float f32x16 __attribute__((ext_vector_type(16)));
typedef __bf16 bf16x2 __attribute__((ext_vector_type(2)));
typedef __bf16 bf16x8 __attribute__((ext_vector_type(8)));
typedef short s16x4 __attribute__((ext_vector_type(4)));
#define LAS __attribute__((address_space(3)))
#define GAS __attribute__((address_space(1)))
typedef LAS unsigned char* lptr;
#define DEV __device__ __forceinline__

constexpr int SEQ = 2048, DM = 640, NH = 32, DH = 128, NSEG = 64, NQ = NH * DH;

constexpr u32 MB = 1u << 20;
constexpr u32 W_MEM0 = 2 * MB;
constexpr u32 W_CTRL = 4 * MB;
constexpr u32 CTRL_BYTES = 128 * 1024;
constexpr u32 ACT = SEQ * DM * 2;
constexpr u32 W_H1 = 5 * MB, W_Y = W_H1 + ACT, W_H2 = W_Y + ACT, W_X2 = W_H2 + ACT;
constexpr u32 W_KQ = 16 * MB;
constexpr u32 W_VO = W_KQ + 64 * MB;
constexpr u32 W_P = W_VO + 64 * MB;
constexpr u32 P_ITEM = 16 * 4 * DM * 4;
constexpr u32 W_KVT = W_P + 4 * NSEG * P_ITEM;
constexpr u32 W_SP = W_KVT + ACT;
constexpr u32 W_END = W_KVT + 3 * MB;
static_assert((size_t)W_END + 2 * MB <= 256ull * MB, "workspace");
constexpr u32 F_ERR = 0, F_QKV = 16, F_O = F_QKV + 192, F_P = F_O + 64, F_H2 = F_P + 64, F_CLR = F_H2 + 32,
              F_EPOCH = F_CLR + 32, F_SLOT = F_EPOCH + 1, F_PRE = F_SLOT + 256, F_N = F_PRE + 256;
constexpr int NCV = 44, NPRE = 40;
static_assert(F_N * 128 <= CTRL_BYTES, "ctrl");
DEV u32 foff(u32 idx) { return W_CTRL + idx * 128u; }

constexpr u32 OOB = 0x80000000u;
constexpr u32 PSENT = 0x7F817F81u;
constexpr int SPIN_CAP = 40000;
constexpr int LDS_BYTES = 160 * 1024;

#define LDUMP(cond, id) do {} while (0)
#define LMX(cond, k, s_, v) do {} while (0)
#define LS3(cond, slot, idx) do {} while (0)
#define LST(idx, v) do {} while (0)
#define LS2(cond, idx) do {} while (0)
struct Params {
    const u16 *x, *mem0, *Wq, *Wk, *Wv, *Wo, *bk, *bv, *shift, *ssmk, *ssmD;
    u16* out;
    unsigned char* ws;
};

#define INAUX 2
#define INNT "nt"
typedef __amdgpu_buffer_rsrc_t rsrc_t;
DEV rsrc_t mk_rsrc(const void* p, u32 bytes) { return __builtin_amdgcn_make_buffer_rsrc((void*)p, 0, bytes, 0x00020000); }
DEV v4u ld16s(rsrc_t r, u32 off, u32 so) { return __builtin_amdgcn_raw_buffer_load_b128(r, off, so, 16); }
DEV v2u ld8s(rsrc_t r, u32 off, u32 so) { return __builtin_amdgcn_raw_buffer_load_b64(r, off, so, 16); }
DEV v4u ldin16(rsrc_t r, u32 off, u32 so) { return __builtin_amdgcn_raw_buffer_load_b128(r, off, so, INAUX); }
DEV v2u ldin8(rsrc_t r, u32 off, u32 so) { return __builtin_amdgcn_raw_buffer_load_b64(r, off, so, INAUX); }
DEV u32 ld4(rsrc_t r, u32 off) { return __builtin_amdgcn_raw_buffer_load_b32(r, off, 0, 16); }
DEV void st16(rsrc_t r, u32 off, v4u v) { __builtin_amdgcn_raw_buffer_store_b128(v, r, off, 0, 16); }
DEV void st8s(rsrc_t r, u32 off, u32 so, v2u v) { __builtin_amdgcn_raw_buffer_store_b64(v, r, off, so, 16); }
DEV void st4s(rsrc_t r, u32 off, u32 so, u32 v) { __builtin_amdgcn_raw_buffer_store_b32(v, r, off, so, 16); }
#define DRAIN() asm volatile("s_waitcnt vmcnt(0)" ::: "memory")
#define VMW(n) asm volatile("s_waitcnt vmcnt(%0)" :: "i"(n) : "memory")
#define BAR() asm volatile("s_waitcnt lgkmcnt(0)\n\ts_barrier" ::: "memory")
#define SCHED() __builtin_amdgcn_sched_barrier(0)
#define CFENCE() asm volatile("" ::: "memory")
#define NOPS(n) asm volatile("s_nop %0" :: "i"(n))
#define PIN(x) asm volatile("" : "+v"(x))
#define PINS(x) asm volatile("" : "+s"(x))

template <int V> struct IC { static constexpr int value = V; };
template <int I, int N, class F> DEV void static_for(F&& f) { if constexpr (I < N) { f(IC<I>{}); static_for<I + 1, N>(f); } }
#define SFOR(var, n) static_for<0, n>([&](auto ic_) __attribute__((always_inline)) { constexpr int var = decltype(ic_)::value;
#define SEND });

DEV u32 pk(float a, float b) { bf16x2 v = {(__bf16)a, (__bf16)b}; return __builtin_bit_cast(u32, v); }
DEV float lo_f(u32 u) { return __builtin_bit_cast(float, u << 16); }
DEV float hi_f(u32 u) { return __builtin_bit_cast(float, u & 0xffff0000u); }
DEV f32x2 up2(u32 u) { return (f32x2){lo_f(u), hi_f(u)}; }
DEV u32 pk2(f32x2 v) { return pk(v.x, v.y); }
DEV float rbf(float a) { return (float)(__bf16)a; }
DEV v4u lds_r16(lptr p) { return *(LAS v4u*)p; }
DEV void lds_w16(lptr p, v4u v) { *(LAS v4u*)p = v; }
DEV v2u lds_tr(lptr p) { return __builtin_bit_cast(v2u, __builtin_amdgcn_ds_read_tr16_b64_v4i16((LAS s16x4*)p)); }
DEV v4u tr2(lptr p0, lptr p1) { const v2u a = lds_tr(p0), b = lds_tr(p1); return (v4u){a.x, a.y, b.x, b.y}; }
DEV v8u cat8(v4u a, v4u b) { return __builtin_shufflevector(a, b, 0, 1, 2, 3, 4, 5, 6, 7); }
DEV f32x4 mfma16(v4u a, v4u b, f32x4 c) { return __builtin_amdgcn_mfma_f32_16x16x32_bf16(__builtin_bit_cast(bf16x8, a), __builtin_bit_cast(bf16x8, b), c, 0, 0, 0); }
DEV f32x16 mfma32(v4u a, v4u b, f32x16 c) { return __builtin_amdgcn_mfma_f32_32x32x16_bf16(__builtin_bit_cast(bf16x8, a), __builtin_bit_cast(bf16x8, b), c, 0, 0, 0); }

DEV float f_(u32 u) { return __builtin_bit_cast(float, u); }
DEV u32 u_(float f) { return __builtin_bit_cast(u32, f); }
template <int CTRL> DEV float dpp(float v) { return f_((u32)__builtin_amdgcn_update_dpp(0, (int)u_(v), CTRL, 0xf, 0xf, false)); }
DEV float max16(float v) { auto r = __builtin_amdgcn_permlane16_swap(u_(v), u_(v), false, false); return fmaxf(f_(r[0]), f_(r[1])); }
DEV float max32(float v) { auto r = __builtin_amdgcn_permlane32_swap(u_(v), u_(v), false, false); return fmaxf(f_(r[0]), f_(r[1])); }
DEV float add16(float v) { auto r = __builtin_amdgcn_permlane16_swap(u_(v), u_(v), false, false); return f_(r[0]) + f_(r[1]); }
DEV float add32(float v) { auto r = __builtin_amdgcn_permlane32_swap(u_(v), u_(v), false, false); return f_(r[0]) + f_(r[1]); }
DEV float wave_sum(float v) {
    v += dpp<0xB1>(v);
    v += dpp<0x4E>(v);
    v += dpp<0x124>(v);
    v += dpp<0x128>(v);
    return add32(add16(v));
}
DEV bool all_(bool c) { return __builtin_amdgcn_ballot_w64(!c) == 0ull; }
DEV int uni(int v) { return __builtin_amdgcn_readfirstlane(v); }
DEV u32 uu(u32 v) { return (u32)__builtin_amdgcn_readfirstlane((int)v); }
DEV u32 rdl(u32 v, int l) { return (u32)__builtin_amdgcn_readlane((int)v, l); }
DEV u32 rt32() { return (u32)__builtin_amdgcn_s_memrealtime(); }

#define WSL 2
DEV void wait_flags(rsrc_t rs, u32 off, u32 want, bool& dead) {
    if (dead) return;
#pragma unroll 1
    for (int n = 0;; ++n) {
        CFENCE();
        const u32 v = ld4(rs, off);
        if (all_(off >= OOB || v >= want)) break;
        __builtin_amdgcn_s_sleep(WSL);
        if ((n & 63) == 63) { if (uni((int)ld4(rs, foff(F_ERR))) != 0) { dead = true; break; } }
        if (n > SPIN_CAP) { st4s(rs, foff(F_ERR), 0, 1u); dead = true; break; }
    }
    CFENCE();
}

DEV void pre_post(rsrc_t rs, int idx, u32 stage, v2u ep) { st8s(rs, threadIdx.x == 0 ? foff(F_PRE + idx) : OOB, 0, (v2u){ep.x + stage, ep.y}); }
DEV void pre_poll(rsrc_t rs, int first, int n, u32 stage, v2u ep) {
    const int lane = threadIdx.x & 63;
#pragma unroll 1
    for (int it = 0; it < SPIN_CAP; ++it) {
        CFENCE();
        const v2u a = ld8s(rs, foff(F_PRE + first + min(lane, n - 1)), 0), b = ld8s(rs, foff(F_PRE + first + min(lane + 64, n - 1)), 0);
        if (all_(a.y == ep.y && b.y == ep.y && max(a.x - ep.x - stage, b.x - ep.x - stage) < 8u - stage)) break;
        __builtin_amdgcn_s_sleep(2);
    }
    CFENCE();
}
DEV void pre_wait(rsrc_t rs, int first, int n, u32 stage, v2u ep) {
    if (uni(threadIdx.x >> 6) == 0) pre_poll(rs, first, n, stage, ep);
    BAR();
}
#define POLX 0u
DEV u32 pol_wait(rsrc_t rs, v2u ep) {
    const int lane = threadIdx.x & 63;
    v4u a;
#pragma unroll 1
    for (int it = 0; it < SPIN_CAP; ++it) {
        CFENCE();
        a = ld16s(rs, foff(F_PRE + (lane < NPRE || lane >= 48 ? lane : 0)), 0);
        if (all_(a.y == ep.y && a.x - ep.x - 2u < 6u)) break;
        __builtin_amdgcn_s_sleep(2);
    }
    CFENCE();
    return uu((wave_sum(lane >= 48 ? (float)(int)a.z : 0.f) >= 0.f ? 8192u : 0u) + (wave_sum(lane >= 48 ? (float)(int)a.w : 0.f) >= 0.f ? 16384u : 0u)) ^ POLX;
}
#define PROBE_C 7
#define PROBE_T 620
DEV v2u pol_probe(rsrc_t rs, int id) {
    const int lane = threadIdx.x & 63;
    const u32 off0 = (u32)id * 32768u, tend = rt32() + PROBE_T;
    st4s(rs, lane < 32 ? off0 + (u32)lane * 1024u : OOB, 0, PSENT); DRAIN();
    int d = 1 << 16, calm = 0;
#pragma unroll 1
    for (int q = 0; q < 32 && ((q & 3) || (calm < PROBE_C && (int)(rt32() - tend) < 0)); ++q) {
        const int kind = (q ^ (q >> 2) ^ id) & 3;
        const unsigned long long t0 = __builtin_amdgcn_s_memtime();
        const u32 v = ld4(rs, lane == 0 ? off0 + (u32)(kind * 8192 + (q >> 2) * 1024) : OOB);
        asm volatile("s_waitcnt vmcnt(0)" :: "v"(v) : "memory");
        const int dt = (int)(__builtin_amdgcn_s_memtime() - t0);
        d = lane == kind ? min(d, dt) : d;
        calm += dt < 1100;
    }
    const int d0 = (int)rdl(d, 0), d1 = (int)rdl(d, 1), d2 = (int)rdl(d, 2), d3 = (int)rdl(d, 3), a = min(d0, d2) - min(d1, d3);
    const int b0 = min(max(d0 - d2, -80), 80), b1 = min(max(d1 - d3, -80), 80);
    const u32 xcc = (u32)__builtin_amdgcn_s_getreg(20 | (3 << 11));
    return (v2u){(u32)min(max((xcc & 4u) ? -a : a, -250), 250), (u32)((xcc & 1u) ? b0 + b1 : a > 0 ? b0 - b1 : b1 - b0)};
}
DEV f32x2 ln_stats(const f32x2 (&x)[5]) {
    const float c = f_(uu(u_(x[0].x)));
    f32x2 d[5];
#pragma unroll
    for (int i = 0; i < 5; ++i) d[i] = x[i] - c;
    const f32x2 s2 = (d[0] + d[1]) + (d[2] + d[3]) + d[4], q2 = (d[0] * d[0] + d[1] * d[1]) + (d[2] * d[2] + d[3] * d[3] + d[4] * d[4]);
    float a = s2.x + s2.y, b = q2.x + q2.y, a2, b2;
#define LN_DPP(m) "v_add_f32_dpp %0, %0, %0 " m " row_mask:0xf bank_mask:0xf\n\tv_add_f32_dpp %1, %1, %1 " m " row_mask:0xf bank_mask:0xf\n\ts_nop 0\n\t"
    asm("s_nop 1\n\t" LN_DPP("quad_perm:[1,0,3,2]") LN_DPP("quad_perm:[2,3,0,1]") LN_DPP("row_ror:4")
        "v_add_f32_dpp %0, %0, %0 row_ror:8 row_mask:0xf bank_mask:0xf\n\tv_add_f32_dpp %1, %1, %1 row_ror:8 row_mask:0xf bank_mask:0xf\n\ts_nop 1\n\t"
        "v_permlane16_swap_b32 %0, %1\n\tv_add_f32 %0, %0, %1\n\tv_mov_b32 %1, %0\n\ts_nop 1\n\tv_permlane32_swap_b32 %0, %1\n\tv_add_f32 %0, %0, %1\n\ts_nop 0\n\t"
        "v_readlane_b32 %2, %0, 0\n\tv_readlane_b32 %3, %0, 16\n\ts_nop 1"
        : "+v"(a), "+v"(b), "=s"(a2), "=s"(b2));
    const float m = a2 * (1.0f / DM), var = fmaxf(b2 * (1.0f / DM) - m * m, 0.f);
    const float sd = rbf(__builtin_amdgcn_sqrtf(rbf(rbf(var) + 1e-5f)));
    return (f32x2){rbf(c + m), __builtin_amdgcn_rcpf(sd)};
}
DEV u32 pkv(f32x2 v) { u32 d; asm("v_cvt_pk_bf16_f32 %0, %1, %2" : "=v"(d) : "v"(v.x), "v"(v.y)); return d; }
DEV void store_h(rsrc_t rs, u32 base, int row, const u32 (&hp)[5]) {
    const u32 lane = threadIdx.x & 63;
    const u32 sw = (u32)(row & 15) * 16u, o8 = (lane >> 5) * 8192u + (((lane & 31u) * 8u) ^ sw), hb = base + (u32)(row >> 5) * (32 * DM * 2) + (u32)(row & 31) * 256u;
    st8s(rs, o8, hb, (v2u){hp[0], hp[1]});
    st8s(rs, o8, hb + 16384, (v2u){hp[2], hp[3]});
    st4s(rs, (lane * 4) ^ sw, hb + 32768, hp[4]);
}

#define DMA16(ldsb, vo, so) asm volatile("s_mov_b32 m0, %0\n\ts_nop 0\n\tbuffer_load_dwordx4 %1, %2, %3 offen sc1 lds" :: "s"(ldsb), "v"(vo), "s"(srd), "s"(so) : "memory")
#define DMA16I(ldsb, lo, vo, so, go) do { u32 so_; asm volatile("s_add_u32 m0, %1, %5\n\ts_add_u32 %0, %4, %6\n\tbuffer_load_dwordx4 %2, %3, %0 offen sc1 lds" \
                         : "=&s"(so_) : "s"(ldsb), "v"(vo), "s"(srd), "s"(so), "i"(lo), "i"(go) : "memory", "scc"); } while (0)
#define DMA32(ldsb, vo, so) asm volatile("s_mov_b32 m0, %0\n\ts_nop 0\n\tbuffer_load_dwordx4 %1, %2, %3 offen sc1 lds\n\tbuffer_load_dwordx4 %1, %2, %3 offen offset:1024 sc1 lds" \
                         :: "s"(ldsb), "v"(vo), "s"(srd), "s"(so) : "memory")
#define DMAIN(sr, ldsb, vo, so) asm volatile("s_mov_b32 m0, %0\n\ts_nop 0\n\tbuffer_load_dwordx4 %1, %2, %3 offen lds" :: "s"(ldsb), "v"(vo), "s"(sr), "s"(so) : "memory")
DEV v4u mk_srd(const void* q, u32 bytes) { return (v4u){uu((u32)(size_t)q), uu((u32)((size_t)q >> 32) & 0xffffu), bytes, 0x00020000u}; }
#define DMANT(sr, ldsb, vo, so) asm volatile("s_mov_b32 m0, %0\n\ts_nop 0\n\tbuffer_load_dwordx4 %1, %2, %3 offen " INNT " lds" :: "s"(ldsb), "v"(vo), "s"(sr), "s"(so) : "memory")
#define AHEAD(sr, vo, so) asm volatile("buffer_load_dwordx4 %0, %1, %2, %3 offen" : "+v"(junk) : "v"(vo), "s"(sr), "s"(so) : "memory")
#define MK_SRD() const v4u srd = mk_srd(p.ws, W_END)

constexpr int CV_W = 40960;
template <bool B> struct CvL {
    static constexpr int NF = B ? 5 : 3, NK = B ? 4 : 2, KP = 544 + NK * 256, CH = NF * 1024 + 4 * KP;
};
constexpr int CV_Y = 4 * CvL<false>::CH, CV_YP = 272;
static_assert(CV_Y + 32 * CV_YP <= CV_W && 2 * CvL<true>::CH <= CV_Y, "conv lds");
DEV int cv_chan(int u, int i) { return 16 * (u % 40) + 4 * i + u / 40; }
template <bool B> DEV void cv_filt(const Params& p, u32 dst, int d, int i) {
    const u32 lane = threadIdx.x & 63;
    const v4u sf = mk_srd(p.ssmk + d * SEQ, SEQ * 2);
    if (i == 0) DMANT(sf, dst, lane < 4u ? OOB : lane * 16u - 64u, 0);
    else DMANT(sf, uu(dst + i * 1024), i == CvL<B>::NF - 1 && lane >= 4u ? OOB : lane * 16u, i * 1024 - 64);
}
DEV v4u cv_kvld(rsrc_t rs, int d, int i) { return ld16s(rs, (threadIdx.x & 63) * 16u, uu(W_KVT + (u32)d * (SEQ * 2) + i * 1024)); }
template <bool B> DEV void cv_kvput(lptr F, const v4u* k) {
    const int lane = threadIdx.x & 63;
#pragma unroll
    for (int i = 0; i < CvL<B>::NK; ++i) lds_w16(F + CvL<B>::NF * 1024 + (lane & 3) * CvL<B>::KP + 512 + i * 256 + (lane >> 2) * 16, k[i]);
}
template <bool B> DEV void cv_zero(lptr F) {
    const int lane = threadIdx.x & 63;
#pragma unroll
    for (int i = 0; i < 4; ++i) lds_w16(F + (lane >> 5) * CvL<B>::CH + CvL<B>::NF * 1024 + i * CvL<B>::KP + (lane & 31) * 16, (v4u){0u, 0u, 0u, 0u});
}
#define M32V(d, a, b) asm volatile("v_mfma_f32_32x32x16_bf16 %0, %1, %2, %0" : "+v"(d) : "v"(a), "v"(b))
template <bool B, class G> DEV void cv_run(const Params& p, rsrc_t rs, lptr F, int d, u32 D, f32x4 sp, float (&y)[16], G&& gap) {
    constexpr int KP = CvL<B>::KP, FO = B ? 2048 : 0;
    const int lane = threadIdx.x & 63, n = lane & 31, hh = lane >> 5, c = lane & 3;
    lptr fa = F + 64 + (32 * hh + 16 * (c & 1) + 8 * ((lane >> 4) & 1) + 4 * (c >> 1) - (lane & 12)) * 2;
    lptr kl = F + CvL<B>::NF * 1024 + (32 + n - hh) * 16;
    v4u dg;
#pragma unroll
    for (int q = 0; q < 4; ++q) { const int k = (lane & 35) ? -1 : (n & 4) + (n >> 3); dg[q] = k == 2 * q ? D : k == 2 * q + 1 ? D << 16 : 0u; }
    v4u l[4], h[4]; v2u fc[4], fh[4];
    f32x16 acc[4];
#pragma unroll
    for (int j = 0; j < 4; ++j) { l[j] = lds_r16(kl + j * KP); if constexpr (B) h[j] = lds_r16(kl + j * KP + 512); }
#pragma unroll
    for (int j = 0; j < 2; ++j) { fc[j] = lds_tr(fa + FO - 32 * j); if constexpr (B) fh[j] = lds_tr(fa - 32 * j); }
    asm volatile("s_nop 1" : "+v"(dg));
#pragma unroll
    for (int j = 0; j < 4; ++j) asm volatile("v_mfma_f32_32x32x16_bf16 %0, %1, %2, 0" : "=&v"(acc[j]) : "v"(dg), "v"(B ? h[j] : l[j]));
    constexpr int U = B ? 2 : 4;
    kl -= 32 * U; fa -= 32;
    auto body = [&](bool first) __attribute__((always_inline)) {
#pragma unroll
        for (int z = 0; z < U; ++z) {
            const int c = 2 * (z & 1);
            const v4u f = __builtin_shufflevector(fc[c], fc[c + 1], 0, 1, 2, 3), g = __builtin_shufflevector(fh[c], fh[c + 1], 0, 1, 2, 3);
#pragma unroll
            for (int j = 0; j < 4; ++j) {
                M32V(acc[j], f, l[j]); l[j] = lds_r16(kl + j * KP + 32 * (U - z - 1));
                if (j < 2) fc[(c ^ 2) + j] = lds_tr(fa + FO + (z + 1) * 128 + 32 - 32 * j);
                if (first && j > 0) gap(5 * z + j - 1);
                SCHED();
            }
            if constexpr (B) {
#pragma unroll
                for (int j = 0; j < 4; ++j) {
                    M32V(acc[j], g, h[j]); h[j] = lds_r16(kl + j * KP + 512 + 32 * (U - z - 1));
                    if (j < 2) fh[(c ^ 2) + j] = lds_tr(fa + (z + 1) * 128 + 32 - 32 * j);
                    if (first && j > 1) gap(5 * z + j + 1);
                    SCHED();
                }
            }
        }
        kl -= 32 * U; fa += 128 * U;
    };
    if constexpr (B) body(true);
#pragma unroll 1
    for (int m = B ? 2 * U : 0; m < 32; m += 2 * U) body(false);
    asm volatile("s_nop 11" : "+v"(acc[0]), "+v"(acc[1]), "+v"(acc[2]), "+v"(acc[3]));
    const float o[3] = {acc[1][15] + acc[2][14] + acc[3][13], acc[2][15] + acc[3][14], acc[3][15]};
    if constexpr (!B) st16(rs, lane == 63 ? W_SP + (u32)d * 16u : OOB, (v4u){u_(o[0]), u_(o[1]), u_(o[2]), 0u});
#pragma unroll
    for (int i = 0; i < 16; ++i) {
        float v = acc[0][i];
#pragma unroll
        for (int j = 1; j < 4; ++j) if (j <= i) v += acc[j][i - j];
        if (i < 3) { const float e = f_((u32)__builtin_amdgcn_ds_bpermute((hh ? lane - 32 : lane + 31) * 4, (int)u_(o[i]))); v += lane == 0 ? sp[i] : e; }
        y[i] = v;
    }
}
DEV void cv_put(lptr F, int pr, const float (&y0)[16], const float (&y1)[16]) {
    const int lane = threadIdx.x & 63;
#pragma unroll
    for (int i = 0; i < 16; ++i) *(LAS u32*)(F + CV_Y + (lane & 31) * CV_YP + (lane >> 5) * 128 + i * 8 + pr * 4) = pk(y0[i], y1[i]);
}
DEV void cv_out(rsrc_t rs, lptr F, int u, int blk0) {
    const int lane = threadIdx.x & 63;
    lptr q = F + CV_Y + ((lane >> 2) & 3) * CV_YP + ((lane >> 4) * 4 + (lane & 3)) * 8;
    const u32 yo = W_Y + (u32)(((lane >> 2) * 8 + blk0) * DM + 4 * u + (lane & 3)) * 16u;
#pragma unroll
    for (int i = 0; i < 8; ++i) { lptr a = q + (i >> 1) * (8 * CV_YP) + (i & 1) * 128; st16(rs, yo + (u32)((i & 1) * 128 + (i >> 1)) * (DM * 16), tr2(a, a + 4 * CV_YP)); }
}
struct KvIn { v2u k[19], v[16]; v4u s[2]; };
template <class F> DEV void kvt_load(const Params& p, int tile, KvIn& in, F&& mid) {
    const int lane = threadIdx.x & 63;
    const rsrc_t rk = mk_rsrc(p.bk, ACT), rv = mk_rsrc(p.bv, ACT), rh = mk_rsrc(p.shift, DM * 8);
    const int row = (tile / 10) * 64 + (lane >> 4) * 16, col = (tile % 10) * 64 + (lane & 15) * 4;
    const u32 vo = (u32)(row * DM + col) * 2u, vp = row > 0 ? vo - 3 * (DM * 2) : OOB;
#pragma unroll
    for (int i = 0; i < 2; ++i) in.s[i] = ldin16(rh, (u32)col * 8u + i * 16, 0);
#pragma unroll
    for (int i = 0; i < 19; ++i) { if (i == 7) mid(); in.k[i] = i < 3 ? ldin8(rk, vp, i * (DM * 2)) : ldin8(rk, vo, (i - 3) * (DM * 2)); if (i >= 3) in.v[i - 3] = ldin8(rv, vo, (i - 3) * (DM * 2)); }
}
DEV void kvt_make(rsrc_t rs, lptr xt, int tile, const KvIn& in) {
    const int lane = threadIdx.x & 63;
#pragma unroll
    for (int c = 0; c < 2; ++c) {
        const v4u sv = in.s[c];
        const f32x2 sh[4] = {{lo_f(sv.x), lo_f(sv.z)}, {hi_f(sv.x), hi_f(sv.z)}, {lo_f(sv.y), lo_f(sv.w)}, {hi_f(sv.y), hi_f(sv.w)}};
        f32x2 kf[19];
#pragma unroll
        for (int i = 0; i < 19; ++i) kf[i] = up2(in.k[i][c]);
        u32 kv[16];
#pragma unroll
        for (int i = 0; i < 16; ++i) kv[i] = pk2(((kf[i] * sh[3] + kf[i + 1] * sh[2]) + (kf[i + 2] * sh[1] + kf[i + 3] * sh[0])) * up2(in.v[i][c]));
#pragma unroll
        for (int q = 0; q < 8; ++q) {
            const u32 sel = (q & 1) ? 0x07060302u : 0x05040100u; const u32* z = kv + (q >> 1);
            *(LAS v2u*)(xt + ((2 * c + (q & 1)) * 16 + (lane & 15)) * 144 + (lane >> 5) * 64 + (q >> 1) * 16 + ((lane >> 4) & 1) * 8) =
                (v2u){__builtin_amdgcn_perm(z[4], z[0], sel), __builtin_amdgcn_perm(z[12], z[8], sel)};
        }
    }
    const u32 o = W_KVT + (u32)(((tile % 10) * 64 + 4 * (lane >> 3)) * SEQ + (tile / 10) * 64 + (lane & 7) * 8) * 2u;
#pragma unroll
    for (int i = 0; i < 8; ++i) st16(rs, o + (u32)(32 * (i & 1) + (i >> 1)) * (SEQ * 2), lds_r16(xt + (8 * i + (lane >> 3)) * 144 + (lane & 7) * 16));
}
struct RowX { v2u a, b; u32 c; };
DEV void ln_row(rsrc_t rs, int row, const RowX& r) {
    const f32x2 x[5] = {up2(r.a.x), up2(r.a.y), up2(r.b.x), up2(r.b.y), up2(r.c)};
    const f32x2 st = ln_stats(x);
    u32 hp[5];
#pragma unroll
    for (int q = 0; q < 5; ++q) hp[q] = pk2(up2(pkv(x[q] - st.x)) * st.y);
    store_h(rs, W_H1, row, hp);
}
DEV v4u mem0_load(const Params& p, int j) {
    const u32 n = (u32)(j * 256 + threadIdx.x);
    return ldin16(mk_rsrc(p.mem0, 8 * DM * 2), n < 640u ? ((2 * ((n >> 2) & 3) + (n & 1)) * DM + (n >> 4) * 16 + (n & 2) * 4) * 2u : OOB, 0);
}
DEV void pre_flags(rsrc_t rs, int j, v4u m) {
    const u32 n = (u32)(j * 256 + threadIdx.x);
#pragma unroll
    for (int i = 0; i < 4; ++i) m[i] = m[i] == PSENT ? 0x7FC17FC1u : m[i];
    st4s(rs, n - 36u * 256u < F_CLR ? foff(n - 36u * 256u) : OOB, 0, 0u);
    st16(rs, n < 640u ? W_MEM0 + (n >> 9) * 32768u + (n & 511u) * 16u : OOB, m);
}
constexpr int KT0 = NPRE * 4, LN0 = NPRE * 8;
struct Early { RowX r[2]; v4u m; };
DEV void conv_early(const Params& p, rsrc_t rs, int j, Early& e) {
    const int lane = threadIdx.x & 63, w = uni(threadIdx.x >> 6), u = j * 4 + w;
    if (j < NPRE) {
        const rsrc_t rx = mk_rsrc(p.x, ACT);
#pragma unroll
        for (int i = 0; i < 2; ++i) {
            const u32 so = uu((u + i * KT0) * (DM * 2));
            e.r[i].a = __builtin_amdgcn_raw_buffer_load_b64(rx, lane * 8u, so, 0); e.r[i].b = __builtin_amdgcn_raw_buffer_load_b64(rx, lane * 8u + 512u, so, 0);
            e.r[i].c = __builtin_amdgcn_raw_buffer_load_b32(rx, lane * 4u + 1024u, so, 0);
        }
        e.m = mem0_load(p, j);
    }
}
DEV void conv_stage(const Params& p, rsrc_t rs, lptr lds, int j, v2u ep, Early& e) {
    const int tid = threadIdx.x, lane = tid & 63, w = uni(tid >> 6), u = j * 4 + w;
    lptr F = lds + w * CV_W;
    const u32 fb = uu(w * CV_W);
    const rsrc_t rd = mk_rsrc(p.ssmD, DM * 2);
    float y0[16], y1[16];
    KvIn in;
    if (j < NPRE) {
        constexpr int CH = CvL<false>::CH;
        if (w == 1) LST(1500 + j, rt32());
        pre_flags(rs, j, e.m);
        ln_row(rs, u, e.r[0]); ln_row(rs, u + KT0, e.r[1]);
        if (w == 1) LST(1550 + j, rt32());
        CFENCE();
        kvt_load(p, u, in, [&] { VMW(13); BAR(); pre_post(rs, j, 1u, ep); if (w == 0) LST(1450 + j, rt32()); });
        if (j == 20) LST(260, rt32());
#pragma unroll 1
        for (u32 o = (u32)(j * 256 + tid) * 16u; o < 2 * MB; o += NPRE * 4096) st16(rs, o, (v4u){PSENT, PSENT, PSENT, PSENT});
        const u32 D = __builtin_amdgcn_raw_buffer_load_b16(rd, (u32)cv_chan(u, lane & 3) * 2u, 0, 0);
#pragma unroll 1
        for (int i = 0; i < 4; ++i) { const int d = cv_chan(u, i); cv_filt<false>(p, fb + i * CH, d, 0); cv_filt<false>(p, fb + i * CH, d, 1); cv_filt<false>(p, fb + i * CH, d, 2); }
        kvt_make(rs, F + CV_Y, u, in);
        cv_zero<false>(F); cv_zero<false>(F + 2 * CH);
        DRAIN(); BAR();
        pre_post(rs, j, 2u, ep);
        if (j == 20) LST(262, rt32());
        pre_wait(rs, 0, NPRE, 2u, ep);
        if (j == 20) LST(281, rt32());
        {
            v4u k[8];
#pragma unroll
            for (int i = 0; i < 8; ++i) k[i] = cv_kvld(rs, cv_chan(u, i >> 1), i & 1);
#pragma unroll
            for (int i = 0; i < 4; ++i) cv_kvput<false>(F + i * CH, k + 2 * i);
        }
        if (j == 20) LST(286, rt32());
#pragma unroll 1
        for (int i = 0; i < 4; i += 2) {
            cv_run<false>(p, rs, F + i * CH, cv_chan(u, i), rdl(D, i), f32x4{}, y0, [](int) {});
            cv_run<false>(p, rs, F + (i + 1) * CH, cv_chan(u, i + 1), rdl(D, i + 1), f32x4{}, y1, [](int) {});
            cv_put(F, i >> 1, y0, y1);
        }
        if (j == 20) LST(287, rt32());
        cv_out(rs, F, u, 0);
        if (j == 20) LST(263, rt32());
    } else {
        const v2u vote = pol_probe(rs, u - NPRE * 4);
        st16(rs, lane == 0 ? foff(F_PRE + 48 + u - NPRE * 4) : OOB, (v4u){ep.x + 2u, ep.y, vote.x, vote.y});
#pragma unroll 1
        for (int t = u + 128; t < 320; t += 16) { kvt_load(p, t, in, [] {}); kvt_make(rs, F, t, in); }
    }
    DRAIN(); BAR();
    pre_post(rs, j, 3u, ep);
    const int b = j < 20 ? -1 : j < 24 ? u - 80 : j >= NPRE ? u - 144 : (w & 1) ? (u >> 1) - 16 : -1, u0 = b < 32 ? b : b + 96, ue = b < 32 ? 128 : 160;
    if (b >= 0) {
        constexpr int CH = CvL<true>::CH;
        cv_zero<true>(F);
        pre_poll(rs, 64, 32, 3u, ep); pre_poll(rs, 0, NCV, 3u, ep);
        v4u k[4];
        auto fetch = [&](u32 dst, int d, int i) { if (i < 5) cv_filt<true>(p, dst, d, i); else if (i < 9) k[i - 5] = cv_kvld(rs, d, i - 5); };
#pragma unroll
        for (int i = 0; i < 9; ++i) fetch(fb, cv_chan(u0, 0), i);
        const u32 ch = (u32)cv_chan(min(u0 + 32 * (lane >> 2), 159), lane & 3);
        u32 D = __builtin_amdgcn_raw_buffer_load_b16(rd, ch * 2u, 0, 0);
        v4u sp = ld16s(rs, W_SP + ch * 16u, 0);
        asm volatile("" : "+v"(D), "+v"(sp));
#pragma unroll 1
        for (int un = u0, n = 0; un < ue; un += 32) {
            if (un == 64) { LST(282, rt32()); LST(288, __builtin_amdgcn_s_memtime()); }
#pragma unroll 1
            for (int i = 0; i < 4; i += 2, n += 2) {
                const int d1 = (int)rdl(ch, n + 1), d2 = (int)rdl(ch, n + 2);
                cv_kvput<true>(F, k);
                cv_run<true>(p, rs, F, 0, rdl(D, n), (f32x4){f_(rdl(sp.x, n)), f_(rdl(sp.y, n)), f_(rdl(sp.z, n)), 0.f}, y0, [&](int q) { fetch(fb + CH, d1, q); });
                cv_kvput<true>(F + CH, k);
                cv_run<true>(p, rs, F + CH, 326, rdl(D, n + 1), (f32x4){f_(rdl(sp.x, n + 1)), f_(rdl(sp.y, n + 1)), f_(rdl(sp.z, n + 1)), 0.f}, y1, [&](int q) { fetch(fb, d2, q); });
                cv_put(F, i >> 1, y0, y1);
            }
            cv_out(rs, F, un, 4);
            if (un == 64) { LST(283, rt32()); LST(289, __builtin_amdgcn_s_memtime()); }
        }
        DRAIN();
        st8s(rs, lane == 0 ? foff(F_PRE + 192 + b) : OOB, 0, (v2u){ep.x + 3u, ep.y});
    }
    if (j == 20) LST(284, rt32());
    if (b == 62) LST(285, rt32());
}

#define M32A(first, d, a, b) do { if (first) asm volatile("v_mfma_f32_32x32x16_bf16 %0, %1, %2, 0" : "=&v"(d) : "a"(a), "v"(b)); \
                                  else asm volatile("v_mfma_f32_32x32x16_bf16 %0, %1, %2, %0" : "+v"(d) : "a"(a), "v"(b)); } while (0)
#define M32B(first, d, a, b) do { if (first) asm volatile("v_mfma_f32_32x32x16_bf16 %0, %1, %2, 0" : "=&v"(d) : "v"(a), "a"(b)); \
                                  else asm volatile("v_mfma_f32_32x32x16_bf16 %0, %1, %2, %0" : "+v"(d) : "v"(a), "a"(b)); } while (0)
#define VCVT(d, a, b) asm volatile("v_cvt_pk_bf16_f32 %0, %1, %2" : "=v"(d) : "v"(a), "v"(b))
#define VADD(d, a, b) asm volatile("v_add_f32 %0, %1, %2" : "=v"(d) : "v"(a), "v"(b))
#define LD4A(d, vo, so) asm volatile("buffer_load_dword %0, %1, %2, %3 offen sc1" : "=v"(d) : "v"(vo), "s"(srd), "s"(so) : "memory")
DEV u32 wst_src(int lane, int ld) { return (u32)((lane >> 4) * ld + 8 * ((lane & 15) ^ ((lane >> 4) << 2))) * 2u; }
DEV u32 wst_rd(int lane, int w) { const int q4 = (lane >> 2) & 3; return (u32)(q4 * 256 + ((w ^ q4) * 64) + ((lane >> 4) & 1) * 32 + (lane & 3) * 8); }

constexpr int QK_SEL = 81920;
#define QK_LEAD 12
#define QK_SMP 29
#define QK_YB 26
#define QK_W0 2
DEV void role_qkv(const Params& p, lptr lds, int c, v2u ep) {
    const int tid = threadIdx.x, lane = tid & 63, w = uni(tid >> 6), hh = lane >> 5, r32 = lane & 31;
    const int m = c >> 5, h = c & 31;
    const rsrc_t rs = mk_rsrc(p.ws, W_END);
    v4u wt[40];
    {
        const v4u sw = mk_srd(m == 0 ? p.Wq : m == 1 ? p.Wk : p.Wv, DM * NQ * 2), sx = mk_srd(p.x, ACT);
        const u32 vo = wst_src(lane, NQ) + (u32)h * (DH * 2), x8 = (u32)lane * 8u, x4 = (u32)lane * 4u;
        RowX xr[5];
        auto wfill = [&](int i0, int n) {
#pragma unroll 1
            for (int i = i0; i < i0 + n; ++i) DMAIN(sw, uu((4 * i + w) * 1024), vo, uu((16 * i + 4 * w) * (NQ * 2)));
        };
        wfill(0, QK_W0);
#pragma unroll
        for (int i = 0; i < 5; ++i)
            asm volatile("buffer_load_dwordx2 %0, %3, %5, %6 offen\n\tbuffer_load_dwordx2 %1, %3, %5, %6 offen offset:512\n\tbuffer_load_dword %2, %4, %5, %6 offen offset:1024"
                         : "=&v"(xr[i].a), "=&v"(xr[i].b), "=&v"(xr[i].c) : "v"(x8), "v"(x4), "s"(sx), "s"(uu((LN0 + 4 * c + w + 384 * i) * (DM * 2))) : "memory");
        wfill(QK_W0, 10 - QK_W0);
        if (c == 3) LST(275, rt32());
        VMW(10 - QK_W0);
        if (c == 3) LST(276, rt32());
#pragma unroll
        for (int i = 0; i < 5; ++i) {
            asm volatile("" : "+v"(xr[i].a), "+v"(xr[i].b), "+v"(xr[i].c));
            const int row = LN0 + 4 * c + w + 384 * i;
            if (i < 4 || row < SEQ) ln_row(rs, row, xr[i]);
            wfill(10 + 6 * i, 6);
        }
        if (c == 3) LST(264, rt32());
        lptr tb = lds + hh * 2048 + wst_rd(lane, w);
        SFOR(k, 40)
            if constexpr (k == 0) { VMW(15); BAR(); }
            if constexpr (k == 28) {
                if (w == 0) pre_poll(rs, 0, NPRE, 1u, ep);
                DRAIN(); BAR();
                pre_post(rs, 96 + c, 1u, ep);
            }
            lptr q = tb + (k >> 3) * 32768 + (k & 7) * 4096; wt[k] = tr2(q, q + 1024); asm volatile("" : "+a"(wt[k]));
        SEND
    }
    if (c == 3) LST(268, rt32());
    if (w == 0) LST(1350 + c, rt32());
    bool dead = false;
    int n0, n1;
    const u32 t16 = (u32)(w * 2048 + lane * 16), wb = uu(w * 2048);
    u32 ar[16];
#pragma unroll
    for (int j = 0; j < 16; ++j) ar[j] = (u32)(r32 * 256 + ((2 * (j & 7) + hh) ^ (r32 & 15)) * 16 + (j >> 3) * 49152);
    MK_SRD();
#define XRD(y, ks) lds_r16(lds + ar[((ks) & 7) + ((ks) >= 24 ? 8 : 0)] + ((ks) >> 3) % 3 * 16384 + (y) * 8192)
#define QM0(y, b) asm volatile("s_add_u32 m0, %0, %1" :: "s"(wb), "i"((b) * 16384 + (y) * 8192) : "scc", "memory")
#define QDMA(i, vo) asm volatile("buffer_load_dwordx4 %0, %1, %2 offen offset:%3 sc1 lds" :: "v"(vo), "s"(srd), "s"(((i) >> 1) * 8192), "i"(((i) & 1) * 1024) : "memory")
    const u32 h2flag = (lane < 16 && w == 0) ? foff(F_H2 + lane) : OOB;
    const u32 ol = (m == 2 ? (u32)(2 * w + (r32 >> 4)) * 1024u : (u32)((r32 >> 4) * 4 + w) * 1024u) + (u32)(hh * 16 + (r32 & 15)) * 16u
                 + (m == 2 ? W_VO : m == 1 ? W_KQ : W_KQ + 8192u) + (u32)h * 16384u;
    const u32 fo = tid < 2 ? foff(F_QKV + tid * 96 + c) : OOB;
    lptr selw = lds + QK_SEL + (w == 0 ? 0 : 64), selr = lds + QK_SEL;
    PIN(selw);
    static_assert(QK_LEAD > 8 && QK_LEAD % 2 == 0 && LN0 == 10 * 32, "X = 8 comes up, before any item of layer 1");
    static_assert(W_H2 - W_H1 == 2 * 64 * (32 * DM * 2), "h");
    auto run = [&](auto isv) __attribute__((always_inline)) {
        constexpr bool V = decltype(isv)::value;
        constexpr int YB = QK_YB;
        f32x16 ax, ay = {};
        v4u x[8], pp, pq;
        u32 xv = t16 + W_H1, ox = ol, oy = OOB, nn = 1, lt = 0, fcnt = 0, fa = OOB;
        asm volatile("" : "+v"(lt), "+v"(nn));
#pragma unroll
        for (int z = 0; z < 5; ++z) x[z] = XRD(1, 40 - YB + z);
#define QPK(r_, a1, a_, q_) do { if constexpr (V) VCVT(r_[q_], a1[4 * (a_) + 8 * ((q_) >> 1) + 2 * ((q_) & 1)], a1[4 * (a_) + 8 * ((q_) >> 1) + 2 * ((q_) & 1) + 1]); \
                                 else VCVT(r_[q_], a1[4 * (a_) + (q_)], a1[4 * (a_) + (q_) + 8]); } while (0)
#define QPK4(r_, a1, a_) do { QPK(r_, a1, a_, 0); QPK(r_, a1, a_, 1); QPK(r_, a1, a_, 2); QPK(r_, a1, a_, 3); } while (0)
        for (;;) {
            u32 ls = uu(lt), yv, fv, nx, need;
            if (ls != 0u && uu(nn) < 256u) pre_wait(rs, 96, 96, 1u, ep);
            else if (ls != 0u) {
                n1 = (int)(uu(nn) >> 8);
                CFENCE();
                const u32 hf = h2flag + (u32)(~n1 & 1) * 2048u;
                u32 f2 = ld4(rs, hf);
                asm volatile("s_waitcnt vmcnt(0)" : "+v"(f2) :: "memory");
                *(LAS u32*)(selw + 16) = (u32)__builtin_amdgcn_ballot_w64(f2 < (u32)n1) & 0xFFFFu;
                BAR();
                st4s(rs, fa, 0, fcnt);
                fa = OOB;
                if (n1 > NSEG) { LDUMP(w == 0, 64 + c); __builtin_amdgcn_endpgm(); }
                if (uu(*(LAS u32*)(selr + 16)) != 0u) {
                    if (w == 0) wait_flags(rs, hf, (u32)n1, dead);
                    BAR();
                }
            }
#define QT(k) do {} while (0)
            QT(0);
            SCHED();
            QM0(0, 0); NOPS(0); QDMA(0, xv); QDMA(1, xv); QM0(0, 1);
            SFOR(z, 80)
                constexpr int ks = (z + 40 - YB) % 40, zx = z - YB;
                constexpr auto upto = [](int q) { const int d = (q - YB + 49) % 40; return q + 6 + (d == 0 || d == 2 ? 1 : d == 1 ? 2 : 0); };
                if constexpr (zx >= 0 && zx < 40) { if constexpr (V) M32B(ks == 0, ax, x[z & 7], wt[ks]); else M32A(ks == 0, ax, wt[ks], x[z & 7]); }
                else { if constexpr (V) M32B(ks == 0, ay, x[z & 7], wt[ks]); else M32A(ks == 0, ay, wt[ks], x[z & 7]); }
                if constexpr (z == 78) lt = *(LAS u32*)selr;
#pragma unroll
                for (int q = upto(z - 1); q < upto(z); ++q) x[q & 7] = XRD(q % 80 < YB || q % 80 >= YB + 40, (q + 40 - YB) % 40);
                SCHED();
                (void)&srd; (void)&wb;
                if constexpr (z < 2) QDMA(z + 2, xv);
                if constexpr (z >= 2 && z < 14 && (z & 3) == 2) QM0(0, (z + 6) >> 2);
                if constexpr (z >= 3 && z < 15 && (z & 1)) QDMA((z + 5) >> 1, xv);
                if constexpr (z == 4) { n0 = uni((int)nn); n1 = n0 >> 8; n0 &= 255; PINS(n0); PINS(n1); }
                if constexpr (z == 8) { nx = n0 < NSEG ? W_H1 + (u32)n0 * (32 * DM * 2) : OOB; PINS(nx); }
                if constexpr (z == 12) { yv = t16 + nx; PIN(yv); }
                if constexpr (zx == -6) { QT(1); asm volatile("s_waitcnt vmcnt(6) lgkmcnt(0)\n\ts_barrier" ::: "memory"); QT(2); }
                if constexpr (zx == -5) st4s(rs, fa, 0, fcnt);
                if constexpr (zx == -4 || zx == 0 || zx == 4) QM0(1, (zx + 4) >> 2);
                if constexpr (zx >= -3 && zx < 8 && (zx & 1)) QDMA((zx + 3) >> 1, yv);
                if constexpr (zx == 2) QPK4(pp, ay, 0);
                if constexpr (zx == 6) QPK4(pq, ay, 1);
                if constexpr (zx == 8) st16(rs, oy, pp);
                if constexpr (zx == 9) st16(rs, oy + 512, pq);
                if constexpr (zx == 10) { QT(3); asm volatile("s_waitcnt vmcnt(9)\n\ts_barrier" ::: "memory"); QT(4); }
                if constexpr (zx == 11 || zx == 15) QM0(1, (zx + 1) >> 2);
                if constexpr (zx >= 12 && zx < 20 && (zx & 1) == 0) QDMA(zx >> 1, yv);
                if constexpr (zx == 21) { fcnt = lane == 0 ? (u32)n0 : (u32)n1; fa = fo; asm volatile("" : "+v"(fcnt), "+v"(fa)); }
                if constexpr (zx == 22) { oy = (ol + (u32)n0 * (NH * 16384u)) | (yv & OOB); PIN(oy); }
                if constexpr (zx == 23) { n0 = min(n0 + 1, NSEG); PINS(n0); nx = (u32)min(NSEG, n1 + QK_LEAD); PINS(nx); }
                if constexpr (zx == 24)
                    asm volatile("s_cmp_ge_u32 %1, %0\n\ts_cselect_b32 %0, %4, %1\n\ts_addc_u32 %2, %2, 0\n\ts_cmp_lt_u32 %0, 64\n\ts_cselect_b32 %3, 0, %2"
                                 : "+s"(nx), "+s"(n0), "+s"(n1), "=&s"(need) : "s"(n1 + 64) : "scc");
                if constexpr (zx == 25) { n0 += 1 - (int)min(nx >> 6, 1u); PINS(n0); }
                if constexpr (zx == 26) { nn = (u32)(n0 | n1 << 8); PIN(nn); }
                if constexpr (zx == 27) { ls = W_H1 + (nx + (nx & 64u)) * (32 * DM * 2); PINS(ls); }
                if constexpr (zx == 28) { xv = t16 + ls; PIN(xv); need = nx == 8u ? 255u : need; PINS(need); }
                if constexpr (zx == 34) { asm volatile("s_waitcnt vmcnt(%0) lgkmcnt(0)\n\ts_barrier" :: "i"(QK_SMP < 34 ? 7 : 6) : "memory"); QT(5); }
                if constexpr (zx == QK_SMP) { const u32 po = (nx & 1u) * 2048u; LD4A(fv, h2flag, po); }
                if constexpr (zx == 42) QPK4(pp, ax, 0);
                if constexpr (zx == 43) st16(rs, ox, pp);
                if constexpr (zx == 44) QPK4(pq, ax, 1);
                if constexpr (zx == 45) st16(rs, ox + 512, pq);
                if constexpr (zx == 47) { ox = ol + nx * (NH * 16384u); PIN(ox); }
                if constexpr (z == 74) { asm volatile("s_waitcnt vmcnt(2)" : "+v"(fv) :: "memory"); QT(6); ls = (u32)__builtin_amdgcn_ballot_w64(fv < need) & 0xFFFFu; PINS(ls); }
                if constexpr (z == 75) { lt = ls; PIN(lt); *(LAS u32*)selw = lt; }
                if constexpr (z == 77) asm volatile("s_waitcnt lgkmcnt(2)\n\ts_barrier" ::: "memory");
                SCHED();
            SEND
        }
    };
    if (m == 2) run(IC<1>{}); else run(IC<0>{});
}

#define OU_FLG 25
#define OU_SMP 17
DEV void role_out(const Params& p, lptr lds, int L, int kq, int ng, v2u ep) {
    const int tid = threadIdx.x, lane = tid & 63, w = uni(tid >> 6), hh = lane >> 5, r32 = lane & 31;
    const rsrc_t rs = mk_rsrc(p.ws, W_END);
    v4u wo[64];
    bool dead = false;
    {
        const v4u sw = mk_srd(p.Wo, DM * NQ * 2);
        const u32 vo = wst_src(lane, DM) + (u32)ng * 256u;
        auto fill = [&](int b) {
#pragma unroll 1
            for (int i = 0; i < 8; ++i) DMAIN(sw, uu((b % 5) * 32768 + (4 * i + w) * 1024), vo, uu((kq * 1024 + b * 128 + 16 * i + 4 * w) * (DM * 2)));
        };
#pragma unroll 1
        for (int b = 0; b < 5; ++b) fill(b);
        lptr tb = lds + hh * 1024 + wst_rd(lane, w);
        SFOR(b, 8)
            VMW(b < 4 ? 32 : 56 - 8 * b); BAR();
            lptr q = tb + (b % 5) * 32768;
#pragma unroll
            for (int k = 0; k < 8; ++k) { lptr a = q + (k >> 1) * 8192 + (k & 1) * 2048; wo[8 * b + k] = tr2(a, a + 4096); if (b < 7) asm volatile("" : "+a"(wo[8 * b + k])); else PIN(wo[8 * b + k]); }
            if constexpr (b < 3) { BAR(); fill(b + 5); }
        SEND
    }
    pre_wait(rs, 0, NPRE, 1u, ep);
    if (kq == 0 && ng == 0) LST(272 + L, rt32());
    MK_SRD();
    const u32 ofl = lane < 8 ? foff(F_O + L * 32 + kq * 8 + lane) : OOB, l16 = (u32)lane * 16u;
    const u32 fo = (tid == 0) ? foff(F_P + L * 32 + kq * 5 + ng) : OOB;
#define OM0(dst, b) asm volatile("s_add_u32 m0, %0, %1" :: "s"(dst), "i"((b) * 8192) : "scc", "memory")
#define ODMA(i, vo) asm volatile("buffer_load_dwordx4 %0, %1, %2 offen offset:%3 sc1 lds" :: "v"(vo), "s"(srd), "s"(((i) >> 1) * 16384), "i"(((i) & 1) * 1024) : "memory")
    auto first = [&](u32 dst, u32 vo) {
#pragma unroll
        for (int b = 0; b < 5; ++b) { OM0(dst, b); NOPS(0); ODMA(2 * b, vo); ODMA(2 * b + 1, vo); }
    };
    u32 pa = W_P + (u32)(L * NSEG) * P_ITEM + (u32)(2 * hh * 4 + kq) * (DM * 4) + (u32)(ng * 128 + w * 32 + r32) * 4u, fv = 0;
    u32 ov = W_VO + 8192u + (u32)(L * NSEG * NH + kq * 8) * 16384u + (u32)w * 2048u + l16;
    bool early = false;
    u32 farv = 1, owed = OOB;
    lptr selw = lds + 131072 + (w == 0 ? 0 : 64), selr = lds + 131072;
    asm volatile("" : "+v"(farv), "+v"(selw), "+v"(selr));
#define OWAIT(strict, loose) asm volatile("s_cmp_eq_u32 %0, 0\n\ts_cbranch_scc1 1f\n\ts_waitcnt vmcnt(%1)\n1:\n\ts_waitcnt vmcnt(%2)\n\ts_barrier" :: "s"(far), "i"(strict), "i"(loose) : "scc", "memory")
    for (int s = 0; s < NSEG; ++s) {
        const u32 dst = uu((s & 1) * 65536 + w * 2048), ndst = dst ^ 65536u, need = s + 1 < NSEG ? (u32)s + 2u : 255u;
        lptr A = lds + (s & 1) * 65536 + l16;
        if (!early) {
            if (!all_(ofl == OOB || fv >= (u32)s + 1)) wait_flags(rs, ofl, (u32)s + 1, dead);
            LS3(L == 0 && w == 1, 1, kq * 5 + ng);
            first(dst, ov);
        }
#define OT(k) do {} while (0)
        u32 far = uu(farv), nv, sv = (u32)s;
        OM0(dst, 5);
        OWAIT(8, 17);
        OT(0);
        v4u o[64];
#pragma unroll
        for (int k = 0; k < 4; ++k) o[k] = lds_r16(A + k * 1024);
        f32x16 acc;
        SCHED();
        SFOR(k, 64)
            if constexpr (k < 56) M32B(k == 0, acc, o[k], wo[k]); else M32V(acc, o[k], wo[k]);
            if constexpr (k < 60) o[k + 4] = lds_r16(A + (k + 4) * 1024);
            SCHED();
            (void)&srd; (void)&dst; (void)&ndst;
            if constexpr (k < 12 && (k & 1) == 0) ODMA(10 + k / 2, ov);
            if constexpr (k == 3) OWAIT(6, 14);
            if constexpr (k == 3 || k == 7) OM0(dst, (k + 21) >> 2);
            if constexpr (k == 12) { ov += NH * 16384u; PIN(ov); }
            if constexpr (k == OU_SMP) LD4A(fv, ofl, 0);
            if constexpr (k == 19) OWAIT(7, 15);
            if constexpr (k == 33) { asm volatile("s_waitcnt vmcnt(1)" : "+v"(fv) :: "memory"); far = (u32)__builtin_amdgcn_ballot_w64(fv < need) & 255u; PINS(far); }
            if constexpr (k == 34) { farv = far; PIN(farv); *(LAS u32*)selw = farv; }
            if constexpr (k == 35) asm volatile("s_waitcnt lgkmcnt(1)\n\ts_barrier" ::: "memory");
            if constexpr (k == OU_FLG) asm volatile("s_waitcnt vmcnt(7)\n\ts_barrier" ::: "memory");
            if constexpr (k == OU_FLG + 1) st4s(rs, owed, 0, sv);
            if constexpr (k == 36) farv = *(LAS u32*)selr;
            if constexpr (k == 41) { nv = farv ? OOB : ov; PIN(nv); }
            if constexpr (k >= 42 && k < 62 && (k & 3) == 2) OM0(ndst, (k - 42) >> 2);
            if constexpr (k >= 43 && k < 63 && (k & 1)) ODMA((k - 43) >> 1, nv);
            if constexpr (k == 48) LD4A(fv, ofl, 0);
            if constexpr (k == 4) OT(1);
            if constexpr (k == 20) OT(2);
            if constexpr (k == 36) OT(3);
            if constexpr (k == 41) OT(4);
            SCHED();
        SEND
        OT(5);
        asm volatile("s_nop 11" : "+v"(acc), "+v"(pa));
#pragma unroll
        for (int i = 0; i < 8; ++i) st4s(rs, pa, (u32)((4 * (i >> 1) + (i & 1)) * (4 * DM * 4)), pk(acc[2 * i], acc[2 * i + 1]));
        asm volatile("s_waitcnt vmcnt(15)" : "+v"(fv) :: "memory");
        OT(6);
        LS3(L == 0 && w == 1, 2, kq * 5 + ng);
        far = uu(farv);
        early = far == 0u || all_(ofl == OOB || fv >= need);
        owed = fo;
        if (far != 0u) {
            if (early) { first(ndst, ov); VMW(10); }
            else { fv = ld4(rs, ofl); asm volatile("s_waitcnt vmcnt(0)" : "+v"(fv) :: "memory"); }
            BAR();
            st4s(rs, fo, 0, (u32)s + 1);
            owed = OOB;
        }
        pa += P_ITEM;
        LS2(L == 0 && w == 1, 160 + kq * 5 + ng);
    }
    LDUMP(L == 0 && w == 1, kq * 5 + ng);
}

DEV void ln_stats2(const f32x2 (&x)[10], f32x2& mean, f32x2& rstd) {
    const f32x2 c = {f_(uu(u_(x[0].x))), f_(uu(u_(x[0].y)))};
    f32x2 d[10];
#pragma unroll
    for (int i = 0; i < 10; ++i) d[i] = x[i] - c;
    f32x2 a = ((d[0] + d[1]) + (d[2] + d[3])) + ((d[4] + d[5]) + (d[6] + d[7])) + (d[8] + d[9]);
    f32x2 b = (d[0] * d[0] + d[2] * d[2] + d[4] * d[4] + d[6] * d[6] + d[8] * d[8]) + (d[1] * d[1] + d[3] * d[3] + d[5] * d[5] + d[7] * d[7] + d[9] * d[9]);
    float a0, a1, b0, b1;
#define LN_DP4(m) "v_add_f32_dpp %0, %0, %0 " m " row_mask:0xf bank_mask:0xf\n\tv_add_f32_dpp %1, %1, %1 " m " row_mask:0xf bank_mask:0xf\n\t" \
                  "v_add_f32_dpp %2, %2, %2 " m " row_mask:0xf bank_mask:0xf\n\tv_add_f32_dpp %3, %3, %3 " m " row_mask:0xf bank_mask:0xf\n\t"
    asm("s_nop 1\n\t" LN_DP4("quad_perm:[1,0,3,2]") LN_DP4("quad_perm:[2,3,0,1]") LN_DP4("row_ror:4") LN_DP4("row_ror:8")
        "v_permlane16_swap_b32 %0, %1\n\tv_add_f32 %0, %0, %1\n\tv_permlane16_swap_b32 %2, %3\n\tv_add_f32 %2, %2, %3\n\ts_nop 1\n\t"
        "v_permlane32_swap_b32 %0, %2\n\tv_add_f32 %0, %0, %2\n\ts_nop 0\n\t"
        "v_readlane_b32 %4, %0, 0\n\tv_readlane_b32 %5, %0, 16\n\tv_readlane_b32 %6, %0, 32\n\tv_readlane_b32 %7, %0, 48\n\ts_nop 1"
        : "+v"(a.x), "+v"(b.x), "+v"(a.y), "+v"(b.y), "=s"(a0), "=s"(b0), "=s"(a1), "=s"(b1));
    const f32x2 m = (f32x2){a0, a1} * (1.0f / DM), v = (f32x2){b0, b1} * (1.0f / DM) - m * m;
    const f32x2 q = up2(pkv(up2(pkv((f32x2){fmaxf(v.x, 0.f), fmaxf(v.y, 0.f)})) + 1e-5f));
    const f32x2 sd = up2(pk(__builtin_amdgcn_sqrtf(q.x), __builtin_amdgcn_sqrtf(q.y)));
    mean = up2(pkv(c + m)); rstd = (f32x2){__builtin_amdgcn_rcpf(sd.x), __builtin_amdgcn_rcpf(sd.y)};
}
DEV void role_row(const Params& p, lptr lds, int id, v2u ep) {
    const int lane = threadIdx.x & 63, w = uni(threadIdx.x >> 6);
    const int pr = id >> 2, L = id & 1, par = (id >> 1) & 1;
    const rsrc_t rs = mk_rsrc(p.ws, W_END), rx = mk_rsrc(p.x, ACT), ro_ = mk_rsrc(p.out, ACT);
    bool dead = false;
    const u32 fp = lane < 20 ? foff(F_P + L * 32 + lane) : OOB, l16 = (u32)lane * 16u, l8 = (u32)lane * 8u, l4 = (u32)lane * 4u, fb = uu(w * CV_W);
    lptr F = lds + w * CV_W;
    MK_SRD();
    for (int s = par; s < NSEG; s += 2) {
        const u32 ro = (u32)(s * 32 + 2 * pr) * (DM * 2);
        if ((s & 6) == 0) {
            if ((s & 30) == 0) pre_poll(rs, s < 32 ? 0 : 192, s < 32 ? NPRE : 64, 3u, ep);
            if (id == 0 && s == 0) LST(274, rt32());
            if (s == 48) st8s(rs, (id == 1 && lane == 0) ? foff(F_EPOCH) : OOB, 0, (v2u){ep.x + 8u, ep.y});
#pragma unroll 1
            for (int i = 0; i < 5; ++i) {
                const u32 d = fb + i * 2048, o = W_Y + (u32)(16 * pr + (s >> 3)) * 10240u + i * 2048;
                DMA32(d, l16, o); DMA32(d + 10240u, l16, o + 81920u);
            }
            DRAIN();
        }
        RowX xr[2];
        auto xload = [&](rsrc_t r, u32 o, auto aux) {
#pragma unroll
            for (int i = 0; i < 2; ++i) {
                xr[i].a = __builtin_amdgcn_raw_buffer_load_b64(r, l8, o + i * (DM * 2), aux.value); xr[i].b = __builtin_amdgcn_raw_buffer_load_b64(r, l8 + 512u, o + i * (DM * 2), aux.value);
                xr[i].c = __builtin_amdgcn_raw_buffer_load_b32(r, l4 + 1024u, o + i * (DM * 2), aux.value);
            }
        };
        if (L == 0) xload(rx, ro, IC<0>{});
        f32x2 y[10], x[10];
        {
            lptr ya = F + (s & 7) * 2 + l16, yc = F + (s & 7) * 2 + 2048 + (lane >> 1) * 16 + (lane & 1) * 5120;
#pragma unroll
            for (int i = 0; i < 10; ++i) {
                lptr q = i < 8 ? ya + (i & 3) * 2560 + (i >> 2) * 1024 : yc + (i & 1) * 2560;
                y[i] = (f32x2){lo_f(*(LAS u16*)q), lo_f(*(LAS u16*)(q + 10240))}; PIN(y[i]);
            }
        }
        wait_flags(rs, fp, (u32)s + 1, dead);
        LS3(L == 0, 3, ri);
        LMX(L == 0, 0, s, rt32());
        {
            const u32 o = uu(W_P + (u32)(L * NSEG + s) * P_ITEM + (u32)pr * (4 * DM * 4));
            if (L == 1) xload(rs, W_X2 + ro, IC<16>{});
            v4u a[4], b[4]; v2u c[4];
#pragma unroll
            for (int kq = 0; kq < 4; ++kq) { a[kq] = ld16s(rs, l16, o + kq * (DM * 4)); SCHED(); b[kq] = ld16s(rs, l16 + 1024u, o + kq * (DM * 4)); SCHED(); c[kq] = ld8s(rs, l8 + 2048u, o + kq * (DM * 4)); SCHED(); }
            const u32 u0[5] = {xr[0].a.x, xr[0].a.y, xr[0].b.x, xr[0].b.y, xr[0].c}, u1[5] = {xr[1].a.x, xr[1].a.y, xr[1].b.x, xr[1].b.y, xr[1].c};
#pragma unroll
            for (int i = 0; i < 5; ++i) { x[2 * i] = (f32x2){lo_f(u0[i]), lo_f(u1[i])}; x[2 * i + 1] = (f32x2){hi_f(u0[i]), hi_f(u1[i])}; }
#pragma unroll
            for (int kq = 0; kq < 4; ++kq)
#pragma unroll
                for (int i = 0; i < 10; ++i)
                    asm volatile("v_dot2_f32_bf16 %0, %2, %3, %0\n\tv_dot2_f32_bf16 %1, %2, %4, %1" : "+v"(x[i].x), "+v"(x[i].y)
                                 : "v"(i < 4 ? a[kq][i & 3] : i < 8 ? b[kq][i & 3] : c[kq][i & 1]), "s"(0x3F80u), "s"(0x3F800000u));
            asm volatile("s_nop 2" : "+v"(x[8]), "+v"(x[9]));
        }
        LS3(L == 0, 4, ri);
        f32x2 mean, rstd;
        ln_stats2(x, mean, rstd);
#pragma unroll
        for (int i = 0; i < 10; ++i) x[i] += (x[i] - mean) * (y[i] * rstd);
        u32 xp[2][5];
#pragma unroll
        for (int i = 0; i < 5; ++i) { xp[0][i] = pkv((f32x2){x[2 * i].x, x[2 * i + 1].x}); xp[1][i] = pkv((f32x2){x[2 * i].y, x[2 * i + 1].y}); }
        if (L == 0) {
#pragma unroll
            for (int i = 0; i < 5; ++i) { x[2 * i] = (f32x2){lo_f(xp[0][i]), lo_f(xp[1][i])}; x[2 * i + 1] = (f32x2){hi_f(xp[0][i]), hi_f(xp[1][i])}; }
            ln_stats2(x, mean, rstd);
#pragma unroll
            for (int i = 0; i < 10; ++i) x[i] = (x[i] - mean) * rstd;
#pragma unroll
            for (int r = 0; r < 2; ++r) {
                u32 hp[5];
#pragma unroll
                for (int i = 0; i < 5; ++i) hp[i] = pkv((f32x2){x[2 * i][r], x[2 * i + 1][r]});
                store_h(rs, W_H2, s * 32 + 2 * pr + r, hp);
            }
#pragma unroll
            for (int r = 0; r < 2; ++r) {
                st8s(rs, l8, W_X2 + ro + r * (DM * 2), (v2u){xp[r][0], xp[r][1]});
                st8s(rs, l8 + 512u, W_X2 + ro + r * (DM * 2), (v2u){xp[r][2], xp[r][3]});
                st4s(rs, l4 + 1024u, W_X2 + ro + r * (DM * 2), xp[r][4]);
            }
            LS3(L == 0, 5, ri);
            DRAIN();
            st4s(rs, lane == 0 ? foff(F_H2 + par * 16 + pr) : OOB, 0, (u32)s + 1);
        } else {
#pragma unroll
            for (int r = 0; r < 2; ++r) {
                st8s(ro_, l8, ro + r * (DM * 2), (v2u){xp[r][0], xp[r][1]});
                st8s(ro_, l8 + 512u, ro + r * (DM * 2), (v2u){xp[r][2], xp[r][3]});
                st4s(ro_, l4 + 1024u, ro + r * (DM * 2), xp[r][4]);
            }
        }
        if (pr == 0) LST(320 + L * 64 + s, rt32());
        LS2(L == 0, 224 + ri);
        LMX(L == 0, 1, s, rt32());
    }
    LDUMP(L == 0, 32 + ri);
}

constexpr float SCALE = 0.08838834764831845f, LOG2E = 1.4426950408889634f;
constexpr int C_K = 0, C_V = 8192, C_QX = 16384, C_MEM = 24576, C_Q = C_MEM + 10240, C_KM = C_Q + 2304, C_VM = C_KM + 2304, C_DUMP = C_VM + 2048,
              C_ZERO = C_DUMP + 1024, C_WL = C_ZERO + 128;
constexpr int NLF = 14;
static_assert(C_WL % 16 == 0 && C_WL + 4 * NLF * 2048 <= LDS_BYTES, "head lds");
#define CH_VIS 8
#define CH_H0 104
#define CH_W0 80
#define CH_MH 3
#define CH_MW 3
#define CH_REC 28
#define CH_RTY 32u
#define CH_RISE 20u
#define CH_FALL 3u
#define CH_PRE 7
#define CH_PRW 2
#define CH_T0 190
#define CH_UP 2
#define CH_WX 6
#define WAIT_TXT(tmp, t4) "s_memrealtime vcc\n\t.p2align 5\n1:\n\ts_waitcnt lgkmcnt(0)\n\ts_sub_u32 " tmp ", " t4 ", vcc_lo\n\ts_memrealtime vcc\n\ts_cmpk_lt_u32 " tmp ", 400\n\ts_cbranch_scc1 1b\n\t" \
                          "s_add_u32 " tmp ", " tmp ", 4\n\ts_cselect_b32 " tmp ", " tmp ", 0\n2:\n\ts_sub_u32 " tmp ", " tmp ", 1\n\ts_cbranch_scc0 2b\n\t"
DEV void wait_until(u32 t) { u32 tmp; asm volatile(WAIT_TXT("%0", "%1") "s_waitcnt lgkmcnt(0)" : "=&s"(tmp) : "s"(uu(t - 4u)) : "vcc", "scc", "memory"); }
#define SMF_A(acc, a, b) asm volatile("v_smfmac_f32_16x16x64_bf16 %0, %1, %2, %3" : "+v"(acc) : "v"(a), "a"(b), "v"(idx))
#define SMF_V(acc, a, b) asm volatile("v_smfmac_f32_16x16x64_bf16 %0, %1, %2, %3" : "+v"(acc) : "v"(a), "v"(b), "v"(idx))
#define MF0(d, a, b) asm volatile("v_mfma_f32_16x16x32_bf16 %0, %1, %2, 0" : "=&v"(d) : "v"(a), "v"(b))
#define MF1(d, a, b) asm volatile("v_mfma_f32_16x16x32_bf16 %0, %1, %2, %0" : "+v"(d) : "v"(a), "v"(b))
#define MF0H(d, a, b) asm volatile("v_mfma_f32_16x16x16_bf16 %0, %1, %2, 0" : "=&v"(d) : "v"(a), "v"(b))
#define MF1H(d, a, b) asm volatile("v_mfma_f32_16x16x16_bf16 %0, %1, %2, %0" : "+v"(d) : "v"(a), "v"(b))
DEV v8u tr_frag(lptr p0, lptr p1, lptr p2, lptr p3) { return cat8(tr2(p0, p1), tr2(p2, p3)); }
constexpr int qslot(int ks) { return (ks & 1) && ks < 8 ? ks >> 1 : -1; }
constexpr int kslot(int ks) { return ks < 4 ? 4 + ks : -1; }
constexpr int vslot(int ks) { return ks >= 4 ? 4 + ks : -1; }
DEV u32 ring_map(u32 off) { return ((off >> 13) << 15) + (off & 8191u); }
DEV u32 oslot(int s) { return (u32)(s & 3) << 18 | (u32)(s & 2) << 13; }
#define ZRD(x, i) x = __builtin_bit_cast(f32x4, lds_r16(ZP + 16 * (i)))
#define VMAX(d, a, b) asm volatile("v_max_f32 %0, %1, %2" : "=v"(d) : "v"(a), "v"(b))
#define VMAX3(d, a, b, c_) asm volatile("v_max3_f32 %0, %1, %2, %3" : "=v"(d) : "v"(a), "v"(b), "v"(c_))
#define VEXP(d, a) asm volatile("v_exp_f32 %0, %1" : "=v"(d) : "v"(a))
#define VMULC(d, a) asm volatile("v_mul_f32 %0, 0x3e0293ee, %1" : "=v"(d) : "v"(a))
#define CADD(d, a, b) do { d = (a) + (b); PIN(d); } while (0)
#define CMULC(d, a) do { d = (a) * CEXP; PIN(d); } while (0)
#define CCVT(d, a, b) do { d = pk(a, b); PIN(d); } while (0)
#define LGK(n) __builtin_amdgcn_s_waitcnt(0xC07F | (n) << 8)
#define SEL(d, a, b) asm volatile("v_cndmask_b32 %0, %1, %2, %3" : "=v"(d) : "v"(a), "v"(b), "s"(himask))
#define QPERM(d, a, q) asm volatile("v_mov_b32_dpp %0, %1 quad_perm:" q " row_mask:0xf bank_mask:0xf" : "=v"(d) : "v"(a))

#define AL_H 3
#define HD_SMP 70
DEV void role_head(const Params& p, lptr lds, int L, int h, v2u ep) {
    const int tid = threadIdx.x, lane = tid & 63, w = uni(tid >> 6), g = lane >> 4, r = lane & 15;
    const rsrc_t rs = mk_rsrc(p.ws, W_END);
    v8u wq0[10], wq1[10], wk0[10], wk1[10], wv0[10], wv1[10];
    lptr WL = lds + C_WL + w * (NLF * 2048) + lane * 16;
    {
        constexpr int GS = 1056, CS = 48 * GS;
        const v4u sq = mk_srd(p.Wq, DM * NQ * 2), sk = mk_srd(p.Wk, DM * NQ * 2), sv = mk_srd(p.Wv, DM * NQ * 2);
        const u32 vo = (u32)(g * NQ + h * DH + 8 * (r ^ ((g & 1) << 3))) * 2u;
        auto fill = [&](int ch) {
#pragma unroll 1
            for (int i = 0; i < 4; ++i) {
                const u32 dst = uu((ch % 3) * CS + (4 * i + w) * GS), so = uu((ch * 64 + 16 * i + 4 * w) * (NQ * 2));
                DMAIN(sq, dst, vo, so); DMAIN(sk, dst + 16 * GS, vo, so); DMAIN(sv, dst + 32 * GS, vo, so);
            }
        };
        fill(0); fill(1); fill(2);
        const int q4 = r >> 2;
        lptr tb = lds + (4 * (g >> 1) + 2 * (q4 >> 1) + (g & 1)) * GS + (q4 & 1) * 256 + ((w * 64 + (r & 3) * 8) ^ ((q4 & 1) << 7));
        SFOR(ch, 10)
            VMW(ch < 8 ? 24 : 108 - 12 * ch);
            BAR();
            auto frag = [&](int m, int t) { lptr b = tb + (ch % 3) * CS + m * 16 * GS + t * 32; return tr_frag(b, b + 512, b + 8 * GS, b + 8 * GS + 512); };
            wq0[ch] = frag(0, 0); wq1[ch] = frag(0, 1); wk0[ch] = frag(1, 0); wk1[ch] = frag(1, 1); wv0[ch] = frag(2, 0); wv1[ch] = frag(2, 1);
            asm volatile("" : "+a"(wq0[ch]), "+a"(wk0[ch]), "+v"(wv0[ch]), "+v"(wv1[ch]));
            if constexpr (qslot(ch) < 0) asm volatile("" : "+a"(wq1[ch])); else asm volatile("" : "+v"(wq1[ch]));
            if constexpr (kslot(ch) < 0) asm volatile("" : "+a"(wk1[ch])); else asm volatile("" : "+v"(wk1[ch]));
            BAR();
            if constexpr (ch < 7) fill(ch + 3);
        SEND
#pragma unroll
        for (int ch = 0; ch < 10; ++ch) {
            if (qslot(ch) >= 0) { lds_w16(WL + qslot(ch) * 2048, wq1[ch].lo); lds_w16(WL + qslot(ch) * 2048 + 1024, wq1[ch].hi); }
            if (kslot(ch) >= 0) { lds_w16(WL + kslot(ch) * 2048, wk1[ch].lo); lds_w16(WL + kslot(ch) * 2048 + 1024, wk1[ch].hi); }
            if (vslot(ch) >= 0) { lds_w16(WL + vslot(ch) * 2048, wv1[ch].lo); lds_w16(WL + vslot(ch) * 2048 + 1024, wv1[ch].hi); }
        }
        CFENCE();
    }
    const u32 idx = (r & 2) ? 0xEEEEu : 0x4444u;
    unsigned long long himask = 0xCCCCCCCCCCCCCCCCull;
    PINS(himask);
    bool dead = false;
    if (h == 3) LST(266 + L, rt32());
    if (w == 0) LST(1240 + L * 32 + h, rt32());
    if (L == 1) {
        KvIn in;
        kvt_load(p, KT0 + 4 * h + w, in, [] {}); kvt_make(rs, lds + w * 9216, KT0 + 4 * h + w, in);
        DRAIN(); BAR();
        pre_post(rs, 64 + h, 3u, ep);
    }
    const u32 pol0 = pol_wait(rs, ep);
    if (h == 3 && L == 0) LST(277, rt32());
    const u32 l16 = (u32)lane * 16u;
    const u32 mring = (u32)L * MB + (pol0 ^ 16384u), hint = mring + 4 * 65536u;
    const u32 fkh = lane < 3 ? foff(F_QKV + L * 96 + lane * 32 + h) : lane < 7 ? hint + (u32)(lane - 3) * 4u : OOB;
    u32 tm = 0;
    MK_SRD();
    u32 ksrc = uu(W_KQ + (u32)(L * (NSEG - 1) * NH + h) * 16384u + w * 2048), vsrc = ksrc + (W_VO - W_KQ);
    u32 dk = uu(C_K + w * 2048), dv = dk + (C_V - C_K), dq = dk + (C_QX - C_K), dm = uu(C_MEM + w * 2048), dm2 = uu(w < 2 ? C_MEM + 8192 + w * 1024 : C_DUMP);
    asm volatile("" : "+s"(dk), "+s"(dv), "+s"(dq), "+s"(dm), "+s"(dm2));
    auto keys_wait = [&](u32 need) {
#pragma unroll 1
        for (int n = 0; !dead; ++n) {
            const bool beat = need + 2 * L > 2u;
            if (beat) wait_until(tm);
            const u32 hv = ld4(rs, lane == 7 ? foff(F_ERR) : fkh);
            if (beat) { tm = rdl(hv, 5); wait_until(rdl(hv, 3) - 30u); }
            if (((u32)__builtin_amdgcn_ballot_w64(ld4(rs, fkh) < need) & 7u) == 0u) break;
            dead = n > SPIN_CAP || rdl(hv, 7) != 0u;
            st4s(rs, dead ? foff(F_ERR) : OOB, 0, 1u);
        }
        CFENCE();
        DMA32(dk, l16, ksrc); DMA32(dv, l16, vsrc); DMA32(dq, l16, ksrc ^ 8192u);
    };
    if (L == 0) { st4s(rs, tid == 0 ? hint + 4096u + (u32)h * 128u : OOB, 0, 0u); keys_wait(1u); }
    lptr qw = lds + C_Q + (2 * g) * 288 + (32 * w + 2 * r) * 2;
    lptr kw = qw + (C_KM - C_Q); PIN(kw);
    lptr qr = lds + C_Q + (2 * (r >> 2) + (r & 1)) * 288 + g * 16;
    lptr LB = lds + l16, VX = lds + C_V + w * 2048 + l16;
    PIN(VX);
    if (tid >= 64 && tid < 70) { const u32 z = tid == 68 ? 0x3F803F80u : tid == 69 ? PSENT : 0u; lds_w16(lds + C_ZERO + (tid - 64) * 16, (v4u){z, z, z, z}); }
    u32 zaddr = 0; PIN(zaddr);
    lptr ZP = lds + zaddr + C_ZERO;
    const float CEXP = SCALE * LOG2E;
#define LF_RD(slot, hf) lf[slot][hf] = lds_r16(WL + (slot) * 2048 + (hf) * 1024)
    BAR();
    const u32 ofl = tid == 0 ? foff(F_O + L * 32 + h) : OOB;
    u32 oso = (((g | L) & 1) ? OOB : 0u) + (u32)L * MB + (pol0 ^ oslot(-L)) + ring_map((u32)((r >> 3) * 32768 + h * 1024 + w * 256 + (g >> 1) * 128 + (r & 7) * 16));
    unsigned long long vis[8];
#pragma unroll
    for (int i = 0; i < 8; ++i) vis[i] = __builtin_amdgcn_ballot_w64(16 * (i >> 2) + 4 * g + (i & 3) <= 16 * (w & 1) + r);
    u32 missed = 0;
    for (int s = -L; s < NSEG;) {
        u32 sv = (u32)max(s, 0), ms = s > 0 ? mring + (sv & 3u) * 65536u : W_MEM0, ms2 = ms + 32768u + (w & 1) * 1024;
        ms += w * 2048;
        asm volatile("" : "+s"(ms), "+s"(ms2)); PIN(sv);
        f32x4 aq0, aq1, ak0, ak1;
        v4u lf[NLF][2];
        ZRD(aq0, 0); ZRD(aq1, 1); ZRD(ak0, 2); ZRD(ak1, 3);
#pragma unroll
        for (int i = 0; i < 4; ++i) { LF_RD(i, 0); LF_RD(i, 1); }
        SCHED();
        v4u A[10];
#define HT(k) do {} while (0)
        v4u kx[8], qf[4], kmf[4], vx[2], ones, P;
        float fa, fb, fc, fd, inv, sx[4], t[4], tm0, tm1; u32 pa, pb, fl, e01, e23, pmm;
        f32x4 av0, av1, s0, s1, sm, oa, ob, rsum;
        v2u vm, pm;
#define LW(sl) cat8(lf[sl][0], lf[sl][1])
#define PQ(ks, tl) do { if constexpr (!(tl)) SMF_A(aq0, A[ks], wq0[ks]); else if constexpr (qslot(ks) < 0) SMF_A(aq1, A[ks], wq1[ks]); else { const v8u lw_ = LW(qslot(ks)); SMF_V(aq1, A[ks], lw_); } } while (0)
#define PK(ks, tl) do { if constexpr (!(tl)) SMF_A(ak0, A[ks], wk0[ks]); else if constexpr (kslot(ks) < 0) SMF_A(ak1, A[ks], wk1[ks]); else { const v8u lw_ = LW(kslot(ks)); SMF_V(ak1, A[ks], lw_); } } while (0)
#define PV(ks, tl) do { if constexpr (!(tl)) SMF_V(av0, A[ks], wv0[ks]); else if constexpr (vslot(ks) < 0) SMF_V(av1, A[ks], wv1[ks]); else { const v8u lw_ = LW(vslot(ks)); SMF_V(av1, A[ks], lw_); } } while (0)
#define KXR(i) kx[i] = lds_r16(LB + C_K + ((i) & 1) * 4096 + ((i) >> 1) * 1024)
#define XW(q_) do { *(LAS u32*)(q_) = pa; *(LAS u32*)((q_) + 288) = pb; } while (0)
        auto slot = [&](auto zc) __attribute__((always_inline)) {
            constexpr int z = decltype(zc)::value;
            if constexpr (z < 14) PQ(z >> 1, z & 1);
            else if constexpr (z < 17) PQ(z - 7, 0);
            else if constexpr (z < 20) PQ(z - 10, 1);
            else if constexpr (z < 34) PK((z - 20) >> 1, z & 1);
            else if constexpr (z < 37) PK(z - 27, 0);
            else if constexpr (z < 40) PK(z - 30, 1);
            else if constexpr (z < 42) { if constexpr (z == 40) MF0(s0, kx[0], qf[0]); else MF0(s1, kx[1], qf[0]); }
            else if constexpr (z < 48) { if constexpr (z & 1) MF1(s1, kx[z - 40], qf[(z - 40) >> 1]); else MF1(s0, kx[z - 40], qf[(z - 40) >> 1]); }
            else if constexpr (z < 54) PV(z - 48, 0);
            else if constexpr (z == 54) MF0(sm, kmf[0], qf[0]);
            else if constexpr (z < 58) MF1(sm, kmf[z - 54], qf[z - 54]);
            else if constexpr (z < 62) PV(z - 52, 0);
            else if constexpr (z < 64) PV(z - 62, 1);
            else if constexpr (z == 64) MF0(oa, vx[0], P);
            else if constexpr (z == 65) MF0(ob, vx[1], P);
            else if constexpr (z == 66) MF0(rsum, ones, P);
            else if constexpr (z < 75) PV(z < 70 ? z - 63 : z == 70 ? 2 : z == 71 ? 7 : z == 72 ? 3 : z - 65, 1);
            else { const v2u ones2 = ones.lo; MF1H(rsum, ones2, pm); }
            if constexpr (z < 6) A[4 + z] = lds_r16(LB + C_MEM + (4 + z) * 1024);
            if constexpr (z >= 6 && z < 10) LF_RD(4 + ((z - 6) >> 1), z & 1);
            if constexpr (z >= 14 && z < 18) LF_RD(6 + ((z - 14) >> 1), z & 1);
            if constexpr (z >= 22 && z < 28) KXR(z - 22);
            if constexpr (z == 28) XW(qw);
            if constexpr (z == 29 || z == 30) KXR(z - 23);
            if constexpr (z == 32 || z == 47) asm volatile("s_barrier" ::: "memory");
            if constexpr (z >= 33 && z < 37) qf[z - 33] = lds_r16(qr + (z - 33) * 64);
            if constexpr (z == 20) ZRD(av0, 0);
            if constexpr (z == 43) XW(kw);
            if constexpr (z == 44 || z == 45) vx[z - 44] = lds_r16(VX + (z - 44) * 1024);
            if constexpr (z >= 48 && z < 52) kmf[z - 48] = lds_r16(qr + (C_KM - C_Q) + (z - 48) * 64);
            if constexpr (z == 52) ZRD(av1, 1);
            if constexpr (z == 37) ones = lds_r16(ZP + 64);
            if constexpr (z >= 57 && z < 64) LF_RD(8 + ((z - 57) >> 1), (z - 57) & 1);
            if constexpr (z >= 65 && z < 70) LF_RD(8 + ((z - 58) >> 1), (z - 58) & 1);
            if constexpr (z == HD_SMP) LD4A(fl, fkh, 0);
            SCHED();
            if constexpr (z == 19) HT(2);
            if constexpr (z == 32) HT(3);
            if constexpr (z == 47) HT(4);
            if constexpr (z == 64) HT(5);
            if constexpr (z == 19) LGK(4);
            if constexpr (z == 21 || z == 41) LGK(1);
            if constexpr (z == 56 || z == 73) LGK(0);
            if constexpr (z == 31 || z == 46) LGK(2);
            if constexpr (z == 39) LGK(4);
            if constexpr (z >= 53 && z < 56) LGK(57 - z);
            if constexpr (z == 64) LGK(3);
            if constexpr (z == 68) LGK(5);
            if constexpr (z >= 70 && z < 73) LGK(74 - z);
            SCHED();
            if constexpr (z == 18) CADD(fa, aq0[0], aq0[2]);
            if constexpr (z == 19) CADD(fb, aq0[1], aq0[3]);
            if constexpr (z == 20) CMULC(fa, fa);
            if constexpr (z == 21) CADD(fc, aq1[0], aq1[2]);
            if constexpr (z == 22) CADD(fd, aq1[1], aq1[3]);
            if constexpr (z == 23) CMULC(fc, fc);
            if constexpr (z == 24) CMULC(fb, fb);
            if constexpr (z == 25) CMULC(fd, fd);
            if constexpr (z == 26) CCVT(pa, fa, fc);
            if constexpr (z == 27) CCVT(pb, fb, fd);
            if constexpr (z == 38) CADD(fa, ak0[0], ak0[2]);
            if constexpr (z == 39) CADD(fb, ak0[1], ak0[3]);
            if constexpr (z == 41) { CADD(fc, ak1[0], ak1[2]); CADD(fd, ak1[1], ak1[3]); }
            if constexpr (z == 42) { CCVT(pa, fa, fc); CCVT(pb, fb, fd); }
            if constexpr (z >= 49 && z < 53) SEL(sx[z - 49], s0[z - 49], s1[z - 49]);
            if constexpr (z >= 53 && z < 57) VEXP(t[z - 53], sx[z - 53]);
            if constexpr (z == 57) CCVT(e01, t[0], t[1]);
            if constexpr (z == 58) CCVT(e23, t[2], t[3]);
            if constexpr (z == 59) QPERM(P.x, e01, "[0,1,0,1]");
            if constexpr (z == 60) QPERM(P.z, e01, "[2,3,2,3]");
            if constexpr (z == 61) QPERM(P.y, e23, "[0,1,0,1]");
            if constexpr (z == 62) QPERM(P.w, e23, "[2,3,2,3]");
            if constexpr (z == 63) CADD(fa, av0[0], av0[2]);
            if constexpr (z == 64) { asm volatile("" :: "v"(sm)); VEXP(tm0, sm[0]); }
            if constexpr (z == 65) VEXP(tm1, sm[1]);
            if constexpr (z == 66) CADD(fb, av0[1], av0[3]);
            if constexpr (z == 67) CCVT(pmm, tm0, tm1);
            if constexpr (z >= 68 && z < 72) SEL(oa[z - 68], oa[z - 68], ob[z - 68]);
            if constexpr (z == 72) asm volatile("v_cndmask_b32 %0, %1, 0, %2" : "=v"(pm.x) : "v"(pmm), "s"(himask));
            if constexpr (z == 73) asm volatile("v_cndmask_b32 %0, 0, %1, %2" : "=v"(pm.y) : "v"(pmm), "s"(himask));
            if constexpr (z == 74) LGK(15);
            if constexpr (z == 75) CCVT(vm.x, fa, fb);
            SCHED();
        };
#define HT0 ""
#define HT0O
        {
            u32 tmp;
            asm volatile("s_mov_b32 m0, %2\n\t" WAIT_TXT("%0", "%1") HT0 "buffer_load_dwordx4 %4, %5, %6 offen sc1 lds\n\tbuffer_load_dwordx4 %4, %5, %6 offen offset:1024 sc1 lds\n\t"
                         "s_mov_b32 m0, %3\n\ts_nop 0\n\tbuffer_load_dwordx4 %4, %5, %7 offen sc1 lds\n\t.p2align 6\n\t.rept %8\n\ts_nop 0\n\t.endr\n\ts_waitcnt vmcnt(0) lgkmcnt(0)\n\ts_barrier"
                         : "=&s"(tmp) : "s"(uu(tm - 4u)), "s"(dm), "s"(dm2), "v"(l16), "s"(srd), "s"(ms), "s"(ms2), "i"(AL_H) HT0O : "vcc", "scc", "memory");
        }
        HT(1);
#pragma unroll
        for (int ks = 0; ks < 4; ++ks) A[ks] = lds_r16(LB + C_MEM + ks * 1024);
        SCHED();
        st4s(rs, ofl, 0, sv);
        SCHED();
        static_for<0, 76>(slot);
        asm volatile("s_nop 2\n\tv_add_f32 %1, %3, %5\n\tv_add_f32 %2, %4, %6\n\tv_cvt_pk_bf16_f32 %0, %1, %2"
                     : "=v"(vm.y), "=&v"(fc), "=&v"(fd) : "v"(av1[0]), "v"(av1[1]), "v"(av1[2]), "v"(av1[3]));
        __builtin_amdgcn_s_waitcnt(0xCF7F);
        f32x2 i2; u32 oad; unsigned long long far;
        auto publish = [&] {
            v4u oc; f32x2 u0 = oa.lo, u1 = oa.hi;
            asm volatile("v_pk_mul_f32 %0, %0, %2\n\tv_pk_mul_f32 %1, %1, %2" : "+v"(u0), "+v"(u1) : "v"(i2));
            __builtin_amdgcn_s_waitcnt(0xCF7F);
            asm volatile("v_cvt_pk_bf16_f32 %0, %4, %5\n\tv_cvt_pk_bf16_f32 %2, %4, %5\n\tv_cvt_pk_bf16_f32 %1, %6, %7\n\tv_cvt_pk_bf16_f32 %3, %6, %7\n\t"
                         "v_permlane16_swap_b32 %0, %2\n\ts_nop 0\n\tv_permlane16_swap_b32 %1, %3"
                         : "=&v"(oc.x), "=&v"(oc.y), "=&v"(oc.z), "=&v"(oc.w) : "v"(u0.x), "v"(u0.y), "v"(u1.x), "v"(u1.y));
            st16(rs, oad, oc);
        };
        asm volatile("s_nop 0\n\tv_mfma_f32_16x16x16_bf16 %0, %5, %6, %0\n\tv_rcp_f32 %1, %7\n\tv_add_u32 %3, 0xde800000, %7\n\tv_cmp_lt_u32 %4, %9, %3\n\tv_mov_b32 %2, %1"
                     : "+v"(oa), "=&v"(i2.x), "=&v"(i2.y), "=&v"(oad), "=&s"(far) : "v"(vm), "v"(pm), "v"(rsum[0]), "v"(rsum), "s"(0x3C000000u));
        if constexpr (HD_SMP > 75) LD4A(fl, fkh, 0); else NOPS(0);
        asm volatile("v_cndmask_b32 %0, %1, -16, %2" : "=v"(oad) : "v"(oso), "s"(far));
        __builtin_amdgcn_s_waitcnt(0xCF7F);
        publish();
        SCHED();
        if (__builtin_expect(far != 0ull, 0)) {
            DRAIN(); SCHED();
            bool miss = false;
#pragma unroll 1
            for (int i = 0; i < 10; ++i) { const v4u a = lds_r16(LB + C_MEM + i * 1024); miss |= a.x == PSENT || a.y == PSENT || a.z == PSENT || a.w == PSENT; }
            if (missed < SPIN_CAP && !all_(!miss)) {
                *(LAS u32*)(lds + C_DUMP + (w == 0 ? 0 : 64)) = ld4(rs, foff(F_ERR));
                asm volatile("" : "+v"(fl) :: "memory");
                tm = rdl(fl, 5);
                BAR();
                missed = uu(*(LAS u32*)(lds + C_DUMP)) != 0u ? SPIN_CAP : missed + 1;
                st4s(rs, missed == SPIN_CAP ? foff(F_ERR) : OOB, 0, 1u);
                BAR();
                continue;
            }
            float mx = fmaxf(fmaxf(fmaxf(sx[0], sx[1]), fmaxf(sx[2], sx[3])), fmaxf(sm[0], sm[1]));
            mx = max32(max16(fmaxf(mx, dpp<0x4E>(mx))));
#pragma unroll
            for (int i = 0; i < 4; ++i) t[i] = __builtin_amdgcn_exp2f(sx[i] - mx);
            e01 = pk(t[0], t[1]); e23 = pk(t[2], t[3]);
            pmm = pk(__builtin_amdgcn_exp2f(sm[0] - mx), __builtin_amdgcn_exp2f(sm[1] - mx));
            asm volatile("v_cndmask_b32 %0, %2, 0, %3\n\tv_cndmask_b32 %1, 0, %2, %3" : "=&v"(pm.x), "=&v"(pm.y) : "v"(pmm), "s"(himask));
            asm volatile("s_nop 1" : "+v"(e01), "+v"(e23), "+v"(pm));
            QPERM(P.x, e01, "[0,1,0,1]"); QPERM(P.z, e01, "[2,3,2,3]"); QPERM(P.y, e23, "[0,1,0,1]"); QPERM(P.w, e23, "[2,3,2,3]");
            NOPS(1);
            MF0(rsum, ones, P); MF0(oa, vx[0], P); MF0(ob, vx[1], P);
            NOPS(7);
            { const v2u ones2 = ones.lo; MF1H(rsum, ones2, pm); }
#pragma unroll
            for (int i = 0; i < 4; ++i) SEL(oa[i], oa[i], ob[i]);
            NOPS(1);
            MF1H(oa, vm, pm);
            asm volatile("s_nop 7\n\tv_rcp_f32 %0, %3\n\tv_cndmask_b32 %2, -16, %5, %6\n\tv_mov_b32 %1, %0\n\ts_nop 0"
                         : "=&v"(i2.x), "=&v"(i2.y), "=&v"(oad) : "v"(rsum[0]), "v"(rsum), "v"(oso), "s"(far));
            publish();
        }
        SCHED();
        unsigned long long clk;
        asm volatile("s_memrealtime %0" : "=s"(clk) :: "memory");
        HT(6);
        v4u tq[4], tk[8], tv[2];
        f32x4 c0, c1, cm;
        u32 oo, kvo; bool kvok;
        u32 ln = l16; PIN(ln);
        lptr QX = lds + C_QX + (w & 1) * 4096 + ln, VO = lds + C_V + (w ^ 1) * 2048 + ln, VMX = lds + C_VM + w * 512 + (ln >> 1);
#pragma unroll
        for (int i = 0; i < 2; ++i) { tq[i] = lds_r16(QX + i * 1024); kmf[i] = lds_r16(qr + (C_KM - C_Q) + i * 64); tk[2 * i] = lds_r16(LB + C_K + i * 1024); tk[2 * i + 1] = lds_r16(LB + C_K + 4096 + i * 1024); }
        *(LAS v2u*)VMX = vm;
        SCHED();
        SFOR(i, 12)
            if constexpr (i == 0) MF0(cm, kmf[0], tq[0]); else if constexpr (i == 1) MF0(c0, tk[0], tq[0]); else if constexpr (i == 2) MF0(c1, tk[1], tq[0]);
            else if constexpr (i % 3 == 0) MF1(cm, kmf[i / 3], tq[i / 3]); else if constexpr (i % 3 == 1) MF1(c0, tk[2 * (i / 3)], tq[i / 3]); else MF1(c1, tk[2 * (i / 3) + 1], tq[i / 3]);
            if constexpr (i < 2) kmf[2 + i] = lds_r16(qr + (C_KM - C_Q) + (2 + i) * 64);
            if constexpr (i == 0) { tq[2] = lds_r16(QX + 2048); tk[4] = lds_r16(LB + C_K + 2048); }
            if constexpr (i == 1) { tk[5] = lds_r16(LB + C_K + 6144); tq[3] = lds_r16(QX + 3072); }
            if constexpr (i == 2) { tk[6] = lds_r16(LB + C_K + 3072); tk[7] = lds_r16(LB + C_K + 7168); }
            if constexpr (i == 3) { tv[0] = lds_r16(VO); tv[1] = lds_r16(VO + 1024); }
            if constexpr (i == 4) { oo = ln + (ln & 0x300u) + ((vsrc ^ 8192u) + (u32)((w & 1) * 256) + ((u32)s & OOB)); PIN(oo); }
            if constexpr (i == 5) { ksrc += NH * 16384; vsrc += NH * 16384; PINS(ksrc); PINS(vsrc); }
            if constexpr (i == 9) { asm volatile("s_waitcnt vmcnt(1)" : "+v"(fl) :: "memory"); kvok = ((u32)__builtin_amdgcn_ballot_w64(fl < (u32)s + 2) & 7u) == 0u; }
            if constexpr (i == 10) { kvo = l16 | (kvok ? 0u : OOB); PIN(kvo); }
            SCHED();
        SEND
        asm volatile("s_waitcnt lgkmcnt(0)\n\ts_barrier" : "+s"(clk) :: "memory");
        const v2u tvm = *(LAS v2u*)(VMX + (512 - (w & 1) * 1024));
        const v4u pv = lds_r16(ZP + 80);
        DMA32(dk, kvo, ksrc);
        SCHED(); asm volatile("" :: "v"(cm));
        float u[10], m, m2;
        {
            float NEG = -1000000000.0f / SCALE; PIN(NEG);
#define MSK(d, a, i) asm volatile("v_cndmask_b32 %0, %1, %2, %3" : "=v"(d) : "v"(NEG), "v"(a), "s"(vis[i]))
#pragma unroll
            for (int i = 0; i < 4; ++i) MSK(u[i], c0[i], i);
#pragma unroll
            for (int i = 0; i < 4; ++i) MSK(u[4 + i], c1[i], 4 + i);
            asm volatile("v_max3_f32 %0, %2, %3, %10\n\tv_max3_f32 %1, %4, %5, %11\n\tv_max3_f32 %0, %0, %6, %7\n\tv_max3_f32 %1, %1, %8, %9\n\t"
                         "v_max_f32 %0, %0, %1\n\tv_mov_b32 %1, %0\n\ts_nop 1\n\tv_permlane16_swap_b32 %0, %1\n\tv_max_f32 %0, %0, %1\n\tv_mov_b32 %1, %0\n\ts_nop 1\n\t"
                         "v_permlane32_swap_b32 %0, %1\n\tv_max_f32 %0, %0, %1\n\tv_mul_f32 %0, 0xbe0293ee, %0"
                         : "=&v"(m), "=&v"(m2) : "v"(u[0]), "v"(u[1]), "v"(u[2]), "v"(u[3]), "v"(u[4]), "v"(u[5]), "v"(u[6]), "v"(u[7]), "v"(cm[0]), "v"(cm[1]));
        }
        DMA32(dv, kvo, vsrc);
        {
            v4u px; v2u p0, p1;
#define EX2(d, a, b) asm volatile("v_fmamk_f32 %0, %2, 0x3e0293ee, %4\n\tv_fmamk_f32 %1, %3, 0x3e0293ee, %4\n\tv_exp_f32 %0, %0\n\tv_exp_f32 %1, %1" : "=&v"(d.x), "=&v"(d.y) : "v"(a), "v"(b), "v"(m))
            f32x2 ex[5];
            EX2(ex[0], u[0], u[1]); EX2(ex[1], u[2], u[3]); EX2(ex[2], u[4], u[5]); EX2(ex[3], u[6], u[7]); EX2(ex[4], cm[0], cm[1]);
            asm volatile("v_cvt_pk_bf16_f32 %0, %8, %9\n\tv_cvt_pk_bf16_f32 %1, %10, %11\n\tv_cvt_pk_bf16_f32 %2, %12, %13\n\tv_cvt_pk_bf16_f32 %3, %14, %15\n\t"
                         "v_mov_b32 %5, 0\n\tv_mov_b32 %6, 0\n\tv_cvt_pk_bf16_f32 %4, %16, %17\n\tv_cvt_pk_bf16_f32 %7, %16, %17"
                         : "=&v"(px.x), "=&v"(px.y), "=&v"(px.z), "=&v"(px.w), "=&v"(p0.x), "=&v"(p0.y), "=&v"(p1.x), "=&v"(p1.y)
                         : "v"(ex[0].x), "v"(ex[0].y), "v"(ex[1].x), "v"(ex[1].y), "v"(ex[2].x), "v"(ex[2].y), "v"(ex[3].x), "v"(ex[3].y), "v"(ex[4].x), "v"(ex[4].y));
            f32x4 o[4], sum;
            const v2u ones2 = ones.lo;
            const u32 now = (u32)clk;
            u32 oz;
            SCHED();
            MF0(sum, ones, px); { const u32 q = ksrc ^ 8192u; DMA32(dq, kvo, q); } SCHED();
            MF0(o[0], vx[0], px);
            MF0(o[1], vx[1], px);
            MF0(o[2], tv[0], px); oz = oso ^ (oslot(s) ^ oslot(s + 3)); PIN(oz); SCHED();
            MF0(o[3], tv[1], px); oso ^= oslot(s) ^ oslot(s + 1); PIN(oso); SCHED();
            MF1H(sum, ones2, p0);
            MF1H(o[0], vm, p0);
            MF1H(o[1], vm, p1); st16(rs, oz, pv); SCHED();
            MF1H(o[2], tvm, p0);
            MF1H(o[3], tvm, p1);
            asm volatile("s_nop 2\n\tv_rcp_f32 %0, %1\n\ts_nop 0" : "=v"(inv) : "v"(sum[0]), "v"(sum));
            asm volatile("" : "+v"(o[0]), "+v"(o[1]));
            st16(rs, oo, (v4u){pk2(o[0].lo * inv), pk2(o[0].hi * inv), pk2(o[1].lo * inv), pk2(o[1].hi * inv)});
            asm volatile("" : "+v"(o[2]), "+v"(o[3]));
            st16(rs, oo ^ 2048u, (v4u){pk2(o[2].lo * inv), pk2(o[2].hi * inv), pk2(o[3].lo * inv), pk2(o[3].hi * inv)});
            SCHED();
            LS2(L == 0 && w == 0, h);
            st4s(rs, ((h & 15) == 0 && tid == 192) ? hint + 128u + (u32)h * 8u : OOB, 0, (u32)s << 16 | min(missed, 1u) << 14 | min(now - tm, 0x3FFFu));
            tm = rdl(fl, 5);
            missed = missed == SPIN_CAP ? missed : 0u;
            if (__builtin_expect(s < 1 - L, 0)) {
                if (s < 0) oso -= (ln & 256u) ? 0u : OOB;
#pragma unroll 1
                for (int n = 0; n < (s < 0 ? 0 : 200); ++n) {
                    CFENCE();
                    tm = rdl(ld4(rs, fkh), 6);
                    if (tm != PSENT) break;
                }
            }
        }
        if (__builtin_expect(!kvok, 0) && s + 1 < NSEG) {
            st4s(rs, ln == 0 ? hint + 4096u + (u32)h * 128u : OOB, 0, (u32)s + 1u);
            keys_wait((u32)s + 2u);
        }
        ++s;
    }
    DRAIN(); BAR();
    st4s(rs, ofl, 0, NSEG);
    LDUMP(w == 0, 160 + L * 32 + h);
}

#define WO_NL 3
#define WO_ROT w
#define WO_AH 1
DEV void role_wo(const Params& p, lptr lds, int j, v2u ep) {
    const int tid = threadIdx.x, lane = tid & 63, w = uni(tid >> 6), g = lane >> 4, r = lane & 15;
    const int rg = j & 1, cg = j >> 1, rot = WO_ROT;
    const rsrc_t rs = mk_rsrc(p.ws, W_END);
    v8u wo[8][5];
    {
        const v4u so_ = mk_srd(p.Wo, DM * NQ * 2);
        u32 vo[5];
#pragma unroll
        for (int i = 0; i < 5; ++i) { const int n = 64 * i + lane; vo[i] = (u32)((n / 5) * DM + cg * 40 + (n % 5) * 8) * 2u; }
        v4u junk = {};
        const u32 av = (u32)(j * 5120 + lane * 16);
        auto ahead = [&](int b) {
#pragma unroll
            for (int i = 0; i < 5; ++i) AHEAD(so_, av, uu((8 * w + ((b + rot) & 7)) * (DH * DM * 2) + i * 1024));
        };
        auto fill = [&](int b) {
            if (b + WO_AH < 8) ahead(b + WO_AH);
            const u32 dst = uu(w * 40960 + (b & 3) * 10240), src = uu((8 * w + ((b + rot) & 7)) * (DH * DM * 2));
#pragma unroll
            for (int i = 0; i < 10; ++i) DMAIN(so_, dst + i * 1024, vo[i % 5], src + (i / 5) * (64 * DM * 2));
        };
#pragma unroll 1
        for (int b = 0; b < WO_AH; ++b) ahead(b);
#pragma unroll 1
        for (int b = 0; b < 4; ++b) fill(b);
        const int q4 = r >> 2;
        lptr tb = lds + w * 40960 + (32 * (g >> 1) + 16 * (q4 >> 1) + 8 * ((r >> 1) & 1) + 4 * (g & 1) + (q4 & 1)) * 80 + (r & 1) * 8;
        SFOR(b, 8)
            VMW((b < 5 ? 30 : 70 - 10 * b) + (b + 1 + WO_AH < 8 ? 5 : 0) + (b + 2 + WO_AH < 8 ? 5 : 0) + (b + 3 + WO_AH < 8 ? 5 : 0));
            if constexpr (b + WO_AH == 7) PIN(junk);
#pragma unroll
            for (int t = 0; t < 5; ++t) {
                lptr q = tb + (b & 3) * 10240 + t * 16;
                wo[b][t] = tr_frag(q, q + 160, q + 64 * 80, q + 64 * 80 + 160);
                if (b * 5 + t < 32) asm volatile("" : "+a"(wo[b][t])); else asm volatile("" : "+v"(wo[b][t]));
            }
            CFENCE();
            if constexpr (b < 4) fill(b + 4);
        SEND
    }
    if (j == 0) LST(265, rt32());
    LST(1100 + j * 4 + w, rt32());
    const u32 idx = (r & 2) ? 0xEEEEu : 0x4444u;
    const u32 pol0 = pol_wait(rs, ep);
    BAR();
    const u32 l16 = (u32)lane * 16u;
    u32 oo[8];
#pragma unroll
    for (int i = 0; i < 8; ++i) oo[i] = uu(ring_map((u32)(rg * 32768 + (8 * w + ((i + rot) & 7)) * 1024)));
    const u32 hint = (pol0 ^ 16384u) + 4 * 65536u;
    u32 hvo[2];
#pragma unroll
    for (int l = 0; l < 2; ++l) hvo[l] = lane < 2 ? hint + (u32)(l ^ 1) * MB + (u32)lane * 4u : lane == 2 ? hint + (u32)l * MB + 128u : lane == 3 ? foff(F_ERR) : lane == 4 ? hint + (u32)l * MB + 256u :
                 (lane >= 32 && j == 0 && w == 3) ? hint + (u32)l * MB + (u32)lane * 128u : OOB;
    lptr xw = r < 8 ? lds + w * 640 + (2 * (g & 1) + (g >> 1)) * 32 + r * 4 : lds + 8192 + lane * 4;
    const int G = ((5 * cg) >> 1) + w, tt = 2 * G + ((lane >> 3) & 1) - 5 * cg;
    const bool pub = w < 3 && lane < 32 && tt >= 0 && tt < 5;
    lptr xr = lds + (pub ? tt * 128 + (2 * (lane >> 4) + ((lane >> 2) & 1)) * 32 + (lane & 3) * 8 : 0);
    const u32 mpo = pub ? (pol0 ^ 16384u) + ring_map((u32)(G * 256 + rg * 128 + lane * 4)) : OOB;
    constexpr u32 PER0 = CH_W0 + CH_MW + CH_H0 + CH_MH + 2 * CH_VIS, DW0 = CH_W0 + CH_VIS + CH_MW + CH_REC;
    const bool lead = j == 0 && w == 3;
    const u32 hso = (lead && lane == 0) ? hint : OOB;
    MK_SRD();
    u32 t = 0, tn = 0;
    int sn[2] = {0, 0};
    u32 hav = 8 * CH_H0, wav = 8 * CH_W0, mh = 8 * CH_MH, mw = 8 * CH_MW, ok[2] = {0u, 0u}, dw[2] = {DW0, CH_T0 + CH_REC - 8};
    u32 vo = l16 + pol0 + MB, tp = 0, fin, left, cnt = 0;
    unsigned long long clk = 0;
    {
        if (w == 0) LST(1310 + j, rt32());
        if (lead) {
#pragma unroll 1
            for (int n = 0; n < SPIN_CAP; ++n) {
                CFENCE();
                const u32 a = ld4(rs, foff(F_QKV + lane)), b = ld4(rs, lane < 32 ? foff(F_QKV + 64 + lane) : hvo[0]);
                if (all_(a != 0u && b != (lane < 32 ? 0u : PSENT))) break;
                __builtin_amdgcn_s_sleep(1);
            }
        }
        if (lead) {
            t = rt32() + CH_T0;
            st16(rs, hso, (v4u){t, PER0, t + DW0, t + DW0});
        } else {
#pragma unroll 1
            for (int n = 0; n < SPIN_CAP; ++n) {
                CFENCE();
                const u32 hv = ld4(rs, hvo[1]);
                t = rdl(hv, 0);
                if (rdl(hv, 1) < 400u) break;
                __builtin_amdgcn_s_sleep(1);
            }
        }
    }
    const u32 tm0 = t + DW0;
    tn = t; t -= CH_T0; clk = t - 100u;
    u32 zero = 0, hv = 0xFFFFu, pst = 1, pmp = OOB;
    const u32 psent = PSENT;
    lptr sw = lds + 2560 + w * 4;
    asm volatile("" : "+v"(zero), "+v"(sw));
    auto turn = [&](auto lc) __attribute__((always_inline)) {
        constexpr int L = decltype(lc)::value, M = L ^ 1;
        u32 done, rep, rd, lost, dh, dn, nt, za, pdur, mp; v4u hw;
        const int sc = sn[L];
        auto books = [&](auto ic) __attribute__((always_inline)) {
            constexpr int i = decltype(ic)::value;
            if constexpr (i == 0) { rep = max(rdl(hv, 2), rdl(hv, 4)); PINS(rep); }
            if constexpr (i == 1) { lost = (u32)(__builtin_amdgcn_ballot_w64(hv == (u32)sn[M]) >> 32); PINS(lost); }
            if constexpr (i == 2) { lost |= (rep >> 16) ^ (u32)sn[M]; PINS(lost); }
            if constexpr (i == 3) { lost |= ok[M] ^ 2u; rd = rep & 0xFFFFu; PINS(lost); PINS(rd); }
            if constexpr (i == 4) { lost = lost == 0u ? CH_UP * 8u : 0u; PINS(lost); }
            if constexpr (i == 5) { pdur = ((u32)clk - tp) * 8u; PINS(pdur); pdur = min(pdur, wav + CH_RISE); PINS(pdur); }
            if constexpr (i == 6) { pdur = (done & ok[M]) ? pdur : wav; PINS(pdur); }
            if constexpr (i == 7) { wav = max(pdur, wav - CH_FALL); PINS(wav); }
            if constexpr (i == 8) { nt = min(rd * 8u, hav + CH_RISE); PINS(nt); }
            if constexpr (i == 9) { nt = rd < 0x3FFFu ? nt : hav; PINS(nt); }
            if constexpr (i == 10) { hav = max(nt, hav - CH_FALL); PINS(hav); }
            if constexpr (i == 11) { mw = max(mw - 1u, CH_MW * 8u); PINS(mw); }
            if constexpr (i == 12) { mh = max(mh - 1u, CH_MH * 8u); PINS(mh); }
            if constexpr (i == 13) { rd = rd >> 14 == 1u ? lost : 0u; PINS(rd); }
            if constexpr (i == 14) { mw += rd; mh += lost - rd; PINS(mw); PINS(mh); }
            if constexpr (i == 15) { ok[M] = done ? 1u : (ok[M] & 1u) << 1; PINS(ok[M]); }
            if constexpr (i == 16) { dh = (hav + mh) / 8u + (sn[1] < 3 ? CH_VIS + CH_PRE : CH_VIS); PINS(dh); }
            if constexpr (i == 17) { dn = (wav + mw) / 8u + (sn[1] < 3 ? CH_VIS + CH_PRW : CH_VIS); PINS(dn); }
            if constexpr (i == 18) { nt = dw[L] + dh + (u32)max((int)((u32)clk + CH_WX - t), 0); PINS(nt); }
            if constexpr (i == 19) { dh += dn; dw[L] = dn; sn[M] += (int)done; PINS(dh); PINS(sn[M]); }
            if constexpr (i == 20) { rd = tn - t + (dh >> 1); PINS(rd); }
            if constexpr (i == 21) { lost = (u32)sc - 1u < NSEG - 2 ? CH_RTY : 0u; PINS(lost); nt += ok[L] != 1u ? lost : 0u; PINS(nt); }
            if constexpr (i == 22) { nt = t + (u32)max((int)nt, (int)rd); PINS(nt); }
            if constexpr (i == 23) { hw.x = nt; hw.y = dh; PIN(hw.x); PIN(hw.y); }
            if constexpr (i == 24) { hw.z = nt + dn; hw.w = tm0; PIN(hw); }
            if constexpr (i == 25) st16(rs, hso + L * MB, hw);
        };
        {
#define WT(k) do {} while (0)
            v4u d[8];
            f32x4 acc[5] = {};
            done = uu(pst) ^ 1u; PINS(done);
            za = pmp | pst << 31;
            *(LAS u32*)(sw + L * 4096) = vo >> 31;
            mp = mpo + (L * MB + (u32)((sc + 1) & 3) * 65536u); pmp = mpo + (L * MB + (u32)(sc & 3) * 65536u);
            asm volatile("" : "+v"(acc[0]), "+v"(acc[1]), "+v"(acc[2]), "+v"(acc[3]), "+v"(acc[4]), "+v"(mp), "+v"(pmp), "+v"(za));
            {
                u32 tmp;
                asm volatile(WAIT_TXT("%1", "%2") "buffer_load_dwordx4 %0, %3, %4, %5 offen sc1\n\ts_waitcnt lgkmcnt(0)"
                             : "=&v"(d[0]), "=&s"(tmp) : "s"(uu(t - 4u)), "v"(vo), "s"(srd), "s"(oo[0]) : "vcc", "scc", "memory");
                PINS(clk);
                WT(0);
            }
            SFOR(i, 7)
                asm volatile("buffer_load_dwordx4 %0, %1, %2, %3 offen sc1" : "=v"(d[i + 1]) : "v"(vo), "s"(srd), "s"(oo[i + 1]) : "memory");
                SCHED();
                static_for<(25 * i) / 7, (25 * (i + 1)) / 7>(books);
                SCHED();
            SEND
            WT(1);
            float f0[5], f1[5]; unsigned long long bad;
#define WSW(o_) asm volatile("v_permlane32_swap_b32 %0, %1" : "+v"(f0[o_]), "+v"(f1[o_]))
#define WAD(o_) asm volatile("v_add_f32_dpp %0, %1, %0 row_ror:8 row_mask:0xf bank_mask:0xf" : "+v"(f0[o_]) : "v"(f1[o_]))
#define WFO(o_) do { VADD(f0[o_], acc[o_][0], acc[o_][2]); VADD(f1[o_], acc[o_][1], acc[o_][3]); } while (0)
#define WWR(o_) *(LAS float*)(xw + L * 4096 + (o_) * 128) = f0[o_]
            u32 th, per, so;
            auto next = [&](auto ic_) __attribute__((always_inline)) {
                constexpr int i = decltype(ic_)::value;
                if constexpr (i == 0) { so = (u32)(sn[M] & 3) * 0x42000u; PINS(so); }
                if constexpr (i == 1) { so = (so & 0xC4000u) ^ (pol0 + M * MB); PINS(so); }
                if constexpr (i == 2) { so = sn[M] + 1 < NSEG ? so : OOB; PINS(so); }
                if constexpr (i == 4) { vo = l16 + so; PIN(vo); }
                if constexpr (i == 5 && !L) { fin = (u32)(sn[0] + sn[1] + 2); PINS(fin); }
                if constexpr (i == 6 && L == 0) { fin = (fin | uu(left)) >> 7; PIN(fin); }
                if constexpr (i == 6 && L == 1) { cnt += 1u; PIN(cnt); left = cnt >> 9; PIN(left); }
                if constexpr (i == 7) { PIN(hv); th = rdl(hv, 0); PINS(th); }
                if constexpr (i == 8) { per = rdl(hv, 1); PINS(per); so = th - t; PINS(so); }
                if constexpr (i == 9) { per = (int)so < 20 ? per : 0u; PINS(per); }
                if constexpr (i == 10) { th += per; PINS(th); if constexpr (!L) { per = rdl(hv, 3); PINS(per); } }
                if constexpr (i == 11) { tp = t; t = lead ? tn : th; PINS(t); }
                if constexpr (i == 12) { if constexpr (!L) { fin |= per; PIN(fin); } tn = nt; }
            };
            auto slot = [&](auto ic_) __attribute__((always_inline)) {
                constexpr int i = decltype(ic_)::value;
                constexpr int NL = WO_NL, I0 = 40 - 5 * NL, pc = i < I0 ? i / 5 : 8 - NL + (i - I0) % NL, oc = i < I0 ? i % 5 : (i - I0) / NL;
                f32x4& ac = acc[oc]; v4u& dp = d[pc]; v8u& wp = wo[pc][oc];
                if constexpr (oc == 0) {
                    if constexpr (pc * 5 < 32) asm volatile("s_waitcnt vmcnt(%4)\n\tv_smfmac_f32_16x16x64_bf16 %0, %1, %2, %3" : "+v"(ac), "+v"(dp) : "a"(wp), "v"(idx), "i"(pc < 2 ? 7 - pc : 9 - pc));
                    else asm volatile("s_waitcnt vmcnt(%4)\n\tv_smfmac_f32_16x16x64_bf16 %0, %1, %2, %3" : "+v"(ac), "+v"(dp) : "v"(wp), "v"(idx), "i"(9 - pc));
                } else if constexpr (pc * 5 + oc < 32) SMF_A(ac, dp, wp); else SMF_V(ac, dp, wp);
                SCHED();
                if constexpr (i == 0) WT(2);
                if constexpr (i < 25 && oc == 0) LGK(15);
                if constexpr (i == 6) { u32& h_ = hv; (void)&srd; LD4A(h_, hvo[L], 0); }
                if constexpr (i == 8) books(IC<25>{});
                if constexpr (i >= 11 && i < 19 && i != 15) next(IC<i - 11 - (i > 15)>{});
                if constexpr (i == 20) WT(4);
                constexpr int e = i - (I0 + NL + 1), o = e < 0 ? 0 : e / 3;
                if constexpr (e >= 0 && e % 3 == 0) { CADD(f0[o], acc[o][0], acc[o][2]); if constexpr (o > 0) WAD(o - 1); }
                if constexpr (e >= 0 && e % 3 == 1) { if constexpr (o > 0) { WWR(o - 1); SCHED(); } CADD(f1[o], acc[o][1], acc[o][3]); }
                if constexpr (e >= 0 && e % 3 == 2) {
                    if constexpr (o == 0) asm volatile("v_cmp_u_f32 %0, %1, %2" : "=s"(bad) : "v"(f0[0]), "v"(f1[0]));
                    else LGK(15);
                    WSW(o);
                }
                SCHED();
            };
            WT(3);
            static_for<0, 40>(slot);
            WT(5);
            static_assert(WO_NL == 3, "tail");
            if (__builtin_expect(bad != 0ull, 0)) {
                bool miss = false;
#pragma unroll
                for (int i = 0; i < 8; ++i) miss |= d[i].x == PSENT;
                if (all_(!miss)) {
#pragma unroll
                    for (int i = 0; i < 8; ++i) miss |= d[i].y == PSENT || d[i].z == PSENT || d[i].w == PSENT;
                }
                if (miss) *(LAS u32*)(sw + L * 4096) = 1u;
            }
            asm volatile("v_permlane32_swap_b32 %0, %1\n\ts_nop 1\n\tv_add_f32_dpp %0, %1, %0 row_ror:8 row_mask:0xf bank_mask:0xf\n\tv_add_f32 %2, %4, %6\n\tv_add_f32 %3, %5, %7"
                         : "+v"(f0[3]), "+v"(f1[3]), "=&v"(f0[4]), "=&v"(f1[4]) : "v"(acc[4][0]), "v"(acc[4][1]), "v"(acc[4][2]), "v"(acc[4][3]));
            WWR(3); SCHED();
            asm volatile("s_nop 0\n\tv_permlane32_swap_b32 %0, %1\n\ts_nop 1\n\tv_add_f32_dpp %0, %1, %0 row_ror:8 row_mask:0xf bank_mask:0xf" : "+v"(f0[4]), "+v"(f1[4]));
            WWR(4);
            WT(6);
            f32x2 a, b, c, e; u32 mq; v4u sv;
            asm volatile("s_waitcnt lgkmcnt(0)\n\ts_barrier\n\tds_read_b128 %4, %6 offset:%7+2560\n\t"
                         "ds_read_b64 %0, %5 offset:%7\n\tds_read_b64 %1, %5 offset:%7+640\n\tds_read_b64 %2, %5 offset:%7+1280\n\tds_read_b64 %3, %5 offset:%7+1920"
                         : "=&v"(a), "=&v"(b), "=&v"(c), "=&v"(e), "=&v"(sv) : "v"(xr), "v"(zero), "i"(L * 4096) : "memory");
            asm volatile("s_waitcnt lgkmcnt(4)\n\tv_or_b32 %4, %6, %7\n\tv_or3_b32 %4, %4, %8, %9\n\tv_lshl_or_b32 %5, %4, 31, %10\n\t"
                         "s_waitcnt lgkmcnt(2)\n\tv_pk_add_f32 %0, %0, %1\n\ts_waitcnt lgkmcnt(0)\n\tv_pk_add_f32 %2, %2, %3\n\ts_nop 0\n\tv_pk_add_f32 %0, %0, %2"
                         : "+v"(a), "+v"(b), "+v"(c), "+v"(e), "=&v"(pst), "=&v"(mq) : "v"(sv.x), "v"(sv.y), "v"(sv.z), "v"(sv.w), "v"(mp) : "memory");
            st4s(rs, mq, 0, pkv(a));
            SCHED();
            asm volatile("s_memrealtime %0" : "=s"(clk));
            WT(7);
            st4s(rs, za, 0, psent);
            VMW(3); SCHED();
            static_for<7, 13>(next);
        }
    };
    do {
        turn(IC<1>{});
        turn(IC<0>{});
    } while (uu(fin) == 0u);
    if (sn[0] + 1 < NSEG || sn[1] + 1 < NSEG) st4s(rs, tid == 0 ? foff(F_ERR) : OOB, 0, 1u);
    if (w == 0) LST(1900 + j, rt32());
    LDUMP(w == 0, 224 + j);
}

__global__ void __launch_bounds__(256, 1) k_main(Params p) {
    lptr lds = (lptr)(u32)16 - 16;
    if (blockIdx.x == 0) LST(256, rt32());
    unsigned long long pc; int len;
    asm volatile("s_getpc_b64 %0\n.Lk_main_here:\n\ts_mov_b32 %1, .Lk_end_here-.Lk_main_here" : "=s"(pc), "=s"(len));
    const rsrc_t rs = mk_rsrc(p.ws, W_END);
    const v2u e0 = ld8s(rs, threadIdx.x == 0 ? foff(F_EPOCH) : OOB, 0);
    const v4u code = __builtin_amdgcn_raw_buffer_load_b128(mk_rsrc((const void*)(pc & ~15ull), (u32)max(len, 0)), (((blockIdx.x >> 3) & 31u) * 256u + threadIdx.x) * 16u, 0, 0);
    CFENCE();
    auto conv_id = [](int role) {
        const int x = role >> 5, i = role & 31, no = x < 5 ? 5 : 10, og = x < 5 ? x - 3 : 2 * (x - 4) + (i >= 5), r = (x - 3) * 5 + i - no, q = r / 3;
        return x < 3 || i >= no + 5 || r >= 24 ? NCV : i < no ? ((og & 1) ? (og >> 1) * 5 + i % 5 : NCV) : r % 3 == 0 ? 20 + (q & 1) * 20 + (q >> 1) : 16 + (r % 3) * 8 + q;
    };
    int role = (int)(((u32)__builtin_amdgcn_s_getreg(20 | (3 << 11)) & 7u) * 32u + (blockIdx.x >> 3));
    Early e;
    v4u rv;
#pragma unroll 1
    for (int n = 0;; ++n) {
        conv_early(p, rs, conv_id(role), e);
        if (n) break;
        CFENCE();
        if (threadIdx.x == 0) {
            GAS unsigned long long* slots = (GAS unsigned long long*)(p.ws + W_CTRL + F_SLOT * 128);
            const unsigned long long tag = (unsigned long long)e0.y << 32 | (e0.x + 1u);
            u32 mine = (u32)role;
#pragma unroll 1
            for (int k = 0; k < 256 && __hip_atomic_exchange(slots + mine * 16, tag, __ATOMIC_RELAXED, __HIP_MEMORY_SCOPE_AGENT) == tag; ++k) mine = (mine + 1u) & 255u;
            lds_w16(lds, (v4u){mine, e0.x, e0.y, 0u});
        }
        BAR();
        rv = lds_r16(lds);
        BAR();
        if (uni((int)rv.x) == role) break;
        role = uni((int)rv.x);
    }
    const int x = role >> 5, i = role & 31, w = uni(threadIdx.x >> 6), no = x < 5 ? 5 : 10, nr = x < 7 ? 5 : 4, j = conv_id(role);
    const v2u ep = {uu(rv.y), uu(rv.z)};
    asm volatile("" :: "v"(code));
    if (x == 1) { role_wo(p, lds, i, ep); __builtin_amdgcn_endpgm(); }
    if (x < 3) { role_head(p, lds, i >> 4, x * 8 + (i & 15), ep); __builtin_amdgcn_endpgm(); }
    if (i >= no + nr) { role_qkv(p, lds, (x < 5 ? (x - 3) * 22 : 44 + (x - 5) * 17) + i - no - nr, ep); __builtin_amdgcn_endpgm(); }
    if (j < NCV) conv_stage(p, rs, lds, j, ep, e);
    if (i >= no) {
        if (j >= 24 && j < NPRE) role_row(p, lds, (j - 24) * 4 + w, ep);
        __builtin_amdgcn_endpgm();
    }
    const int og = x < 5 ? x - 3 : 2 * (x - 4) + (i >= 5);
    role_out(p, lds, og & 1, og >> 1, i % 5, ep);
}
__global__ void k_end() { asm volatile(".Lk_end_here:"); }

extern "C" void kernel_launch(void* const* d_in, const int* in_sizes, int n_in, void* d_out, int out_size, void* d_ws, size_t ws_size,
                              hipStream_t stream) {
    Params p;
    p.x = (const u16*)d_in[0]; p.mem0 = (const u16*)d_in[1]; p.Wq = (const u16*)d_in[2]; p.Wk = (const u16*)d_in[3];
    p.Wv = (const u16*)d_in[4]; p.Wo = (const u16*)d_in[5];
    p.bk = (const u16*)d_in[8]; p.bv = (const u16*)d_in[9]; p.shift = (const u16*)d_in[10]; p.ssmk = (const u16*)d_in[11]; p.ssmD = (const u16*)d_in[12];
    p.out = (u16*)d_out;
    p.ws = (unsigned char*)(((size_t)d_ws + 2 * MB - 1) & ~(size_t)(2 * MB - 1));
    (void)hipFuncSetAttribute((const void*)k_main, hipFuncAttributeMaxDynamicSharedMemorySize, LDS_BYTES);
    hipLaunchKernelGGL(k_main, dim3(256), dim3(256), LDS_BYTES, stream, p);
}
```

```cpp
#include <hip/hip_runtime.h>
#include <hip/hip_bf16.h>

typedef unsigned short u16;
typedef unsigned int u32;
typedef u32 v2u __attribute__((ext_vector_type(2)));
typedef u32 v4u __attribute__((ext_vector_type(4)));
typedef u32 v8u __attribute__((ext_vector_type(8)));
typedef float f32x2 __attribute__((ext_vector_type(2)));
typedef float f32x4 __attribute__((ext_vector_type(4)));
typedef float f32x16 __attribute__((ext_vector_type(16)));
typedef __bf16 bf16x2 __attribute__((ext_vector_type(2)));
typedef __bf16 bf16x8 __attribute__((ext_vector_type(8)));
typedef short s16x4 __attribute__((ext_vector_type(4)));
#define LAS __attribute__((address_space(3)))
#define GAS __attribute__((address_space(1)))
typedef LAS unsigned char* lptr;
#define DEV __device__ __forceinline__

constexpr int SEQ = 2048, DM = 640, NH = 32, DH = 128, NSEG = 64, NQ = NH * DH;

constexpr u32 MB = 1u << 20;
constexpr u32 W_MEM0 = 2 * MB;
constexpr u32 W_CTRL = 4 * MB;
constexpr u32 CTRL_BYTES = 128 * 1024;
constexpr u32 ACT = SEQ * DM * 2;
constexpr u32 W_H1 = 5 * MB, W_Y = W_H1 + ACT, W_H2 = W_Y + ACT, W_X2 = W_H2 + ACT;
constexpr u32 W_KQ = 16 * MB;
constexpr u32 W_VO = W_KQ + 64 * MB;
constexpr u32 W_P = W_VO + 64 * MB;
constexpr u32 P_ITEM = 16 * 4 * DM * 4;
constexpr u32 W_KVT = W_P + 4 * NSEG * P_ITEM;
constexpr u32 W_SP = W_KVT + ACT;
constexpr u32 W_END = W_KVT + 3 * MB;
static_assert((size_t)W_END + 2 * MB <= 256ull * MB, "workspace");
constexpr u32 F_ERR = 0, F_QKV = 16, F_O = F_QKV + 192, F_P = F_O + 64, F_H2 = F_P + 64, F_CLR = F_H2 + 32,
              F_EPOCH = F_CLR + 32, F_SLOT = F_EPOCH + 1, F_PRE = F_SLOT + 256, F_N = F_PRE + 288;
constexpr int NCV = 44, NPRE = 40;
static_assert(F_N * 128 <= CTRL_BYTES, "ctrl");
DEV u32 foff(u32 idx) { return W_CTRL + idx * 128u; }

constexpr u32 OOB = 0x80000000u;
constexpr u32 PSENT = 0x7F817F81u;
constexpr int SPIN_CAP = 40000;
constexpr int LDS_BYTES = 160 * 1024;

#define LDUMP(cond, id) do {} while (0)
#define LMX(cond, k, s_, v) do {} while (0)
#define LS3(cond, slot, idx) do {} while (0)
#define LST(idx, v) do {} while (0)
#define LS2(cond, idx) do {} while (0)
struct Params {
    const u16 *x, *mem0, *Wq, *Wk, *Wv, *Wo, *bk, *bv, *shift, *ssmk, *ssmD;
    u16* out;
    unsigned char* ws;
};

#define INAUX 2
#define INNT "nt"
typedef __amdgpu_buffer_rsrc_t rsrc_t;
DEV rsrc_t mk_rsrc(const void* p, u32 bytes) { return __builtin_amdgcn_make_buffer_rsrc((void*)p, 0, bytes, 0x00020000); }
DEV v4u ld16s(rsrc_t r, u32 off, u32 so) { return __builtin_amdgcn_raw_buffer_load_b128(r, off, so, 16); }
DEV v2u ld8s(rsrc_t r, u32 off, u32 so) { return __builtin_amdgcn_raw_buffer_load_b64(r, off, so, 16); }
DEV v4u ldin16(rsrc_t r, u32 off, u32 so) { return __builtin_amdgcn_raw_buffer_load_b128(r, off, so, INAUX); }
DEV v2u ldin8(rsrc_t r, u32 off, u32 so) { return __builtin_amdgcn_raw_buffer_load_b64(r, off, so, INAUX); }
DEV u32 ld4(rsrc_t r, u32 off) { return __builtin_amdgcn_raw_buffer_load_b32(r, off, 0, 16); }
DEV void st16(rsrc_t r, u32 off, v4u v) { __builtin_amdgcn_raw_buffer_store_b128(v, r, off, 0, 16); }
DEV void st8s(rsrc_t r, u32 off, u32 so, v2u v) { __builtin_amdgcn_raw_buffer_store_b64(v, r, off, so, 16); }
DEV void st4s(rsrc_t r, u32 off, u32 so, u32 v) { __builtin_amdgcn_raw_buffer_store_b32(v, r, off, so, 16); }
#define DRAIN() asm volatile("s_waitcnt vmcnt(0)" ::: "memory")
#define VMW(n) asm volatile("s_waitcnt vmcnt(%0)" :: "i"(n) : "memory")
#define BAR() asm volatile("s_waitcnt lgkmcnt(0)\n\ts_barrier" ::: "memory")
#define SCHED() __builtin_amdgcn_sched_barrier(0)
#define CFENCE() asm volatile("" ::: "memory")
#define NOPS(n) asm volatile("s_nop %0" :: "i"(n))
#define PIN(x) asm volatile("" : "+v"(x))
#define PINS(x) asm volatile("" : "+s"(x))

template <int V> struct IC { static constexpr int value = V; };
template <int I, int N, class F> DEV void static_for(F&& f) { if constexpr (I < N) { f(IC<I>{}); static_for<I + 1, N>(f); } }
#define SFOR(var, n) static_for<0, n>([&](auto ic_) __attribute__((always_inline)) { constexpr int var = decltype(ic_)::value;
#define SEND });

DEV u32 pk(float a, float b) { bf16x2 v = {(__bf16)a, (__bf16)b}; return __builtin_bit_cast(u32, v); }
DEV float lo_f(u32 u) { return __builtin_bit_cast(float, u << 16); }
DEV float hi_f(u32 u) { return __builtin_bit_cast(float, u & 0xffff0000u); }
DEV f32x2 up2(u32 u) { return (f32x2){lo_f(u), hi_f(u)}; }
DEV u32 pk2(f32x2 v) { return pk(v.x, v.y); }
DEV float rbf(float a) { return (float)(__bf16)a; }
DEV v4u lds_r16(lptr p) { return *(LAS v4u*)p; }
DEV void lds_w16(lptr p, v4u v) { *(LAS v4u*)p = v; }
DEV v2u lds_tr(lptr p) { return __builtin_bit_cast(v2u, __builtin_amdgcn_ds_read_tr16_b64_v4i16((LAS s16x4*)p)); }
DEV v4u tr2(lptr p0, lptr p1) { const v2u a = lds_tr(p0), b = lds_tr(p1); return (v4u){a.x, a.y, b.x, b.y}; }
DEV v8u cat8(v4u a, v4u b) { return __builtin_shufflevector(a, b, 0, 1, 2, 3, 4, 5, 6, 7); }
DEV f32x4 mfma16(v4u a, v4u b, f32x4 c) { return __builtin_amdgcn_mfma_f32_16x16x32_bf16(__builtin_bit_cast(bf16x8, a), __builtin_bit_cast(bf16x8, b), c, 0, 0, 0); }
DEV f32x16 mfma32(v4u a, v4u b, f32x16 c) { return __builtin_amdgcn_mfma_f32_32x32x16_bf16(__builtin_bit_cast(bf16x8, a), __builtin_bit_cast(bf16x8, b), c, 0, 0, 0); }

DEV float f_(u32 u) { return __builtin_bit_cast(float, u); }
DEV u32 u_(float f) { return __builtin_bit_cast(u32, f); }
template <int CTRL> DEV float dpp(float v) { return f_((u32)__builtin_amdgcn_update_dpp(0, (int)u_(v), CTRL, 0xf, 0xf, false)); }
DEV float max16(float v) { auto r = __builtin_amdgcn_permlane16_swap(u_(v), u_(v), false, false); return fmaxf(f_(r[0]), f_(r[1])); }
DEV float max32(float v) { auto r = __builtin_amdgcn_permlane32_swap(u_(v), u_(v), false, false); return fmaxf(f_(r[0]), f_(r[1])); }
DEV float add16(float v) { auto r = __builtin_amdgcn_permlane16_swap(u_(v), u_(v), false, false); return f_(r[0]) + f_(r[1]); }
DEV float add32(float v) { auto r = __builtin_amdgcn_permlane32_swap(u_(v), u_(v), false, false); return f_(r[0]) + f_(r[1]); }
DEV float wave_sum(float v) {
    v += dpp<0xB1>(v);
    v += dpp<0x4E>(v);
    v += dpp<0x124>(v);
    v += dpp<0x128>(v);
    return add32(add16(v));
}
DEV bool all_(bool c) { return __builtin_amdgcn_ballot_w64(!c) == 0ull; }
DEV int uni(int v) { return __builtin_amdgcn_readfirstlane(v); }
DEV u32 uu(u32 v) { return (u32)__builtin_amdgcn_readfirstlane((int)v); }
DEV u32 rdl(u32 v, int l) { return (u32)__builtin_amdgcn_readlane((int)v, l); }
DEV u32 rt32() { return (u32)__builtin_amdgcn_s_memrealtime(); }

#define WSL 2
DEV void wait_flags(rsrc_t rs, u32 off, u32 want, bool& dead) {
    if (dead) return;
#pragma unroll 1
    for (int n = 0;; ++n) {
        CFENCE();
        const u32 v = ld4(rs, off);
        if (all_(off >= OOB || v >= want)) break;
        __builtin_amdgcn_s_sleep(WSL);
        if ((n & 63) == 63) { if (uni((int)ld4(rs, foff(F_ERR))) != 0) { dead = true; break; } }
        if (n > SPIN_CAP) { st4s(rs, foff(F_ERR), 0, 1u); dead = true; break; }
    }
    CFENCE();
}

DEV void pre_post(rsrc_t rs, int idx, u32 stage, v2u ep) { st8s(rs, threadIdx.x == 0 ? foff(F_PRE + idx) : OOB, 0, (v2u){ep.x + stage, ep.y}); }
DEV void pre_poll(rsrc_t rs, int first, int n, u32 stage, v2u ep) {
    const int lane = threadIdx.x & 63;
#pragma unroll 1
    for (int it = 0; it < SPIN_CAP; ++it) {
        CFENCE();
        const v2u a = ld8s(rs, foff(F_PRE + first + min(lane, n - 1)), 0), b = ld8s(rs, foff(F_PRE + first + min(lane + 64, n - 1)), 0);
        if (all_(a.y == ep.y && b.y == ep.y && max(a.x - ep.x - stage, b.x - ep.x - stage) < 8u - stage)) break;
        __builtin_amdgcn_s_sleep(2);
    }
    CFENCE();
}
DEV void pre_wait(rsrc_t rs, int first, int n, u32 stage, v2u ep) {
    if (uni(threadIdx.x >> 6) == 0) pre_poll(rs, first, n, stage, ep);
    BAR();
}
#define POLX 0u
DEV u32 pol_wait(rsrc_t rs, v2u ep) {
    const int lane = threadIdx.x & 63;
    v4u a;
#pragma unroll 1
    for (int it = 0; it < SPIN_CAP; ++it) {
        CFENCE();
        a = ld16s(rs, foff(F_PRE + (lane < NPRE || lane >= 48 ? lane : 0)), 0);
        const v2u b = ld8s(rs, foff(F_PRE + 256 + min(lane, 19)), 0);
        if (all_(a.y == ep.y && b.y == ep.y && max(a.x - ep.x - 1u, b.x - ep.x - 1u) < 7u)) break;
        __builtin_amdgcn_s_sleep(2);
    }
    CFENCE();
    return uu((wave_sum(lane >= 48 ? (float)(int)a.z : 0.f) >= 0.f ? 8192u : 0u) + (wave_sum(lane >= 48 ? (float)(int)a.w : 0.f) >= 0.f ? 16384u : 0u)) ^ POLX;
}
#define PROBE_C 7
#define PROBE_T 620
DEV v2u pol_probe(rsrc_t rs, int id) {
    const int lane = threadIdx.x & 63;
    const u32 off0 = (u32)id * 32768u, tend = rt32() + PROBE_T;
    st4s(rs, lane < 32 ? off0 + (u32)((lane >> 3) * 8192 + (lane & 7) * 256) : OOB, 0, PSENT); DRAIN();
    int d = 1 << 16, calm = 0;
#pragma unroll 1
    for (int q = 0; q < 32 && ((q & 3) || (calm < PROBE_C && (int)(rt32() - tend) < 0)); ++q) {
        const int kind = (q ^ (q >> 2) ^ id) & 3;
        const unsigned long long t0 = __builtin_amdgcn_s_memtime();
        const u32 v = ld4(rs, lane == 0 ? off0 + (u32)(kind * 8192 + (q >> 2) * 256) : OOB);
        asm volatile("s_waitcnt vmcnt(0)" :: "v"(v) : "memory");
        const int dt = (int)(__builtin_amdgcn_s_memtime() - t0);
        d = lane == kind ? min(d, dt) : d;
        calm += dt < 1100;
    }
    const int d0 = (int)rdl(d, 0), d1 = (int)rdl(d, 1), d2 = (int)rdl(d, 2), d3 = (int)rdl(d, 3), a = min(d0, d2) - min(d1, d3);
    const int b0 = min(max(d0 - d2, -80), 80), b1 = min(max(d1 - d3, -80), 80);
    const u32 xcc = (u32)__builtin_amdgcn_s_getreg(20 | (3 << 11));
    return (v2u){(u32)min(max((xcc & 4u) ? -a : a, -250), 250), (u32)((xcc & 1u) ? b0 + b1 : a > 0 ? b0 - b1 : b1 - b0)};
}
DEV f32x2 ln_stats(const f32x2 (&x)[5]) {
    const float c = f_(uu(u_(x[0].x)));
    f32x2 d[5];
#pragma unroll
    for (int i = 0; i < 5; ++i) d[i] = x[i] - c;
    const f32x2 s2 = (d[0] + d[1]) + (d[2] + d[3]) + d[4], q2 = (d[0] * d[0] + d[1] * d[1]) + (d[2] * d[2] + d[3] * d[3] + d[4] * d[4]);
    float a = s2.x + s2.y, b = q2.x + q2.y, a2, b2;
#define LN_DPP(m) "v_add_f32_dpp %0, %0, %0 " m " row_mask:0xf bank_mask:0xf\n\tv_add_f32_dpp %1, %1, %1 " m " row_mask:0xf bank_mask:0xf\n\ts_nop 0\n\t"
    asm("s_nop 1\n\t" LN_DPP("quad_perm:[1,0,3,2]") LN_DPP("quad_perm:[2,3,0,1]") LN_DPP("row_ror:4")
        "v_add_f32_dpp %0, %0, %0 row_ror:8 row_mask:0xf bank_mask:0xf\n\tv_add_f32_dpp %1, %1, %1 row_ror:8 row_mask:0xf bank_mask:0xf\n\ts_nop 1\n\t"
        "v_permlane16_swap_b32 %0, %1\n\tv_add_f32 %0, %0, %1\n\tv_mov_b32 %1, %0\n\ts_nop 1\n\tv_permlane32_swap_b32 %0, %1\n\tv_add_f32 %0, %0, %1\n\ts_nop 0\n\t"
        "v_readlane_b32 %2, %0, 0\n\tv_readlane_b32 %3, %0, 16\n\ts_nop 1"
        : "+v"(a), "+v"(b), "=s"(a2), "=s"(b2));
    const float m = a2 * (1.0f / DM), var = fmaxf(b2 * (1.0f / DM) - m * m, 0.f);
    const float sd = rbf(__builtin_amdgcn_sqrtf(rbf(rbf(var) + 1e-5f)));
    return (f32x2){rbf(c + m), __builtin_amdgcn_rcpf(sd)};
}
DEV u32 pkv(f32x2 v) { u32 d; asm("v_cvt_pk_bf16_f32 %0, %1, %2" : "=v"(d) : "v"(v.x), "v"(v.y)); return d; }
DEV void store_h(rsrc_t rs, u32 base, int row, const u32 (&hp)[5]) {
    const u32 lane = threadIdx.x & 63;
    const u32 sw = (u32)(row & 15) * 16u, o8 = (lane >> 5) * 8192u + (((lane & 31u) * 8u) ^ sw), hb = base + (u32)(row >> 5) * (32 * DM * 2) + (u32)(row & 31) * 256u;
    st8s(rs, o8, hb, (v2u){hp[0], hp[1]});
    st8s(rs, o8, hb + 16384, (v2u){hp[2], hp[3]});
    st4s(rs, (lane * 4) ^ sw, hb + 32768, hp[4]);
}

#define DMA16(ldsb, vo, so) asm volatile("s_mov_b32 m0, %0\n\ts_nop 0\n\tbuffer_load_dwordx4 %1, %2, %3 offen sc1 lds" :: "s"(ldsb), "v"(vo), "s"(srd), "s"(so) : "memory")
#define DMA16I(ldsb, lo, vo, so, go) do { u32 so_; asm volatile("s_add_u32 m0, %1, %5\n\ts_add_u32 %0, %4, %6\n\tbuffer_load_dwordx4 %2, %3, %0 offen sc1 lds" \
                         : "=&s"(so_) : "s"(ldsb), "v"(vo), "s"(srd), "s"(so), "i"(lo), "i"(go) : "memory", "scc"); } while (0)
#define DMA32(ldsb, vo, so) asm volatile("s_mov_b32 m0, %0\n\ts_nop 0\n\tbuffer_load_dwordx4 %1, %2, %3 offen sc1 lds\n\tbuffer_load_dwordx4 %1, %2, %3 offen offset:1024 sc1 lds" \
                         :: "s"(ldsb), "v"(vo), "s"(srd), "s"(so) : "memory")
#define DMAIN(sr, ldsb, vo, so) asm volatile("s_mov_b32 m0, %0\n\ts_nop 0\n\tbuffer_load_dwordx4 %1, %2, %3 offen lds" :: "s"(ldsb), "v"(vo), "s"(sr), "s"(so) : "memory")
DEV v4u mk_srd(const void* q, u32 bytes) { return (v4u){uu((u32)(size_t)q), uu((u32)((size_t)q >> 32) & 0xffffu), bytes, 0x00020000u}; }
#define DMANT(sr, ldsb, vo, so) asm volatile("s_mov_b32 m0, %0\n\ts_nop 0\n\tbuffer_load_dwordx4 %1, %2, %3 offen " INNT " lds" :: "s"(ldsb), "v"(vo), "s"(sr), "s"(so) : "memory")
#define AHEAD(sr, vo, so) asm volatile("buffer_load_dwordx4 %0, %1, %2, %3 offen" : "+v"(junk) : "v"(vo), "s"(sr), "s"(so) : "memory")
#define MK_SRD() const v4u srd = mk_srd(p.ws, W_END)

constexpr int CV_W = 40960;
template <bool B> struct CvL {
    static constexpr int NF = B ? 5 : 3, NK = B ? 4 : 2, KP = 544 + NK * 256, CH = NF * 1024 + 4 * KP;
};
constexpr int CV_Y = 4 * CvL<false>::CH, CV_YP = 272;
static_assert(CV_Y + 32 * CV_YP <= CV_W && 2 * CvL<true>::CH <= CV_Y, "conv lds");
DEV int cv_chan(int u, int i) { return 16 * (u % 40) + 4 * i + u / 40; }
template <bool B> DEV void cv_filt(const Params& p, u32 dst, int d, int i) {
    const u32 lane = threadIdx.x & 63;
    const v4u sf = mk_srd(p.ssmk + d * SEQ, SEQ * 2);
    if (i == 0) DMANT(sf, dst, lane < 4u ? OOB : lane * 16u - 64u, 0);
    else DMANT(sf, uu(dst + i * 1024), i == CvL<B>::NF - 1 && lane >= 4u ? OOB : lane * 16u, i * 1024 - 64);
}
DEV v4u cv_kvld(rsrc_t rs, int d, int i) { return ld16s(rs, (threadIdx.x & 63) * 16u, uu(W_KVT + (u32)d * (SEQ * 2) + i * 1024)); }
template <bool B> DEV void cv_kvput(lptr F, const v4u* k) {
    const int lane = threadIdx.x & 63;
#pragma unroll
    for (int i = 0; i < CvL<B>::NK; ++i) lds_w16(F + CvL<B>::NF * 1024 + (lane & 3) * CvL<B>::KP + 512 + i * 256 + (lane >> 2) * 16, k[i]);
}
template <bool B> DEV void cv_zero(lptr F) {
    const int lane = threadIdx.x & 63;
#pragma unroll
    for (int i = 0; i < 4; ++i) lds_w16(F + (lane >> 5) * CvL<B>::CH + CvL<B>::NF * 1024 + i * CvL<B>::KP + (lane & 31) * 16, (v4u){0u, 0u, 0u, 0u});
}
#define M32V(d, a, b) asm volatile("v_mfma_f32_32x32x16_bf16 %0, %1, %2, %0" : "+v"(d) : "v"(a), "v"(b))
template <bool B, class G> DEV void cv_run(const Params& p, rsrc_t rs, lptr F, int d, u32 D, f32x4 sp, float (&y)[16], G&& gap) {
    constexpr int KP = CvL<B>::KP, FO = B ? 2048 : 0;
    const int lane = threadIdx.x & 63, n = lane & 31, hh = lane >> 5, c = lane & 3;
    lptr fa = F + 64 + (32 * hh + 16 * (c & 1) + 8 * ((lane >> 4) & 1) + 4 * (c >> 1) - (lane & 12)) * 2;
    lptr kl = F + CvL<B>::NF * 1024 + (32 + n - hh) * 16;
    v4u dg;
#pragma unroll
    for (int q = 0; q < 4; ++q) { const int k = (lane & 35) ? -1 : (n & 4) + (n >> 3); dg[q] = k == 2 * q ? D : k == 2 * q + 1 ? D << 16 : 0u; }
    v4u l[4], h[4]; v2u fc[4], fh[4];
    f32x16 acc[4];
#pragma unroll
    for (int j = 0; j < 4; ++j) { l[j] = lds_r16(kl + j * KP); if constexpr (B) h[j] = lds_r16(kl + j * KP + 512); }
#pragma unroll
    for (int j = 0; j < 2; ++j) { fc[j] = lds_tr(fa + FO - 32 * j); if constexpr (B) fh[j] = lds_tr(fa - 32 * j); }
    asm volatile("s_nop 1" : "+v"(dg));
#pragma unroll
    for (int j = 0; j < 4; ++j) asm volatile("v_mfma_f32_32x32x16_bf16 %0, %1, %2, 0" : "=&v"(acc[j]) : "v"(dg), "v"(B ? h[j] : l[j]));
    constexpr int U = B ? 2 : 4;
    kl -= 32 * U; fa -= 32;
    auto body = [&](bool first) __attribute__((always_inline)) {
#pragma unroll
        for (int z = 0; z < U; ++z) {
            const int c = 2 * (z & 1);
            const v4u f = __builtin_shufflevector(fc[c], fc[c + 1], 0, 1, 2, 3), g = __builtin_shufflevector(fh[c], fh[c + 1], 0, 1, 2, 3);
#pragma unroll
            for (int j = 0; j < 4; ++j) {
                M32V(acc[j], f, l[j]); l[j] = lds_r16(kl + j * KP + 32 * (U - z - 1));
                if (j < 2) fc[(c ^ 2) + j] = lds_tr(fa + FO + (z + 1) * 128 + 32 - 32 * j);
                if (first && j > 0) gap(5 * z + j - 1);
                SCHED();
            }
            if constexpr (B) {
#pragma unroll
                for (int j = 0; j < 4; ++j) {
                    M32V(acc[j], g, h[j]); h[j] = lds_r16(kl + j * KP + 512 + 32 * (U - z - 1));
                    if (j < 2) fh[(c ^ 2) + j] = lds_tr(fa + (z + 1) * 128 + 32 - 32 * j);
                    if (first && j > 1) gap(5 * z + j + 1);
                    SCHED();
                }
            }
        }
        kl -= 32 * U; fa += 128 * U;
    };
    if constexpr (B) body(true);
#pragma unroll 1
    for (int m = B ? 2 * U : 0; m < 32; m += 2 * U) body(false);
    asm volatile("s_nop 11" : "+v"(acc[0]), "+v"(acc[1]), "+v"(acc[2]), "+v"(acc[3]));
    const float o[3] = {acc[1][15] + acc[2][14] + acc[3][13], acc[2][15] + acc[3][14], acc[3][15]};
    if constexpr (!B) st16(rs, lane == 63 ? W_SP + (u32)d * 16u : OOB, (v4u){u_(o[0]), u_(o[1]), u_(o[2]), 0u});
#pragma unroll
    for (int i = 0; i < 16; ++i) {
        float v = acc[0][i];
#pragma unroll
        for (int j = 1; j < 4; ++j) if (j <= i) v += acc[j][i - j];
        if (i < 3) { const float e = f_((u32)__builtin_amdgcn_ds_bpermute((hh ? lane - 32 : lane + 31) * 4, (int)u_(o[i]))); v += lane == 0 ? sp[i] : e; }
        y[i] = v;
    }
}
DEV void cv_put(lptr F, int pr, const float (&y0)[16], const float (&y1)[16]) {
    const int lane = threadIdx.x & 63;
#pragma unroll
    for (int i = 0; i < 16; ++i) *(LAS u32*)(F + CV_Y + (lane & 31) * CV_YP + (lane >> 5) * 128 + i * 8 + pr * 4) = pk(y0[i], y1[i]);
}
DEV void cv_out(rsrc_t rs, lptr F, int u, int blk0) {
    const int lane = threadIdx.x & 63;
    lptr q = F + CV_Y + ((lane >> 2) & 3) * CV_YP + ((lane >> 4) * 4 + (lane & 3)) * 8;
    const u32 yo = W_Y + (u32)(((lane >> 2) * 8 + blk0) * DM + 4 * u + (lane & 3)) * 16u;
#pragma unroll
    for (int i = 0; i < 8; ++i) { lptr a = q + (i >> 1) * (8 * CV_YP) + (i & 1) * 128; st16(rs, yo + (u32)((i & 1) * 128 + (i >> 1)) * (DM * 16), tr2(a, a + 4 * CV_YP)); }
}
struct KvIn { v2u k[19], v[16]; v4u s[2]; };
template <class F> DEV void kvt_load(const Params& p, int tile, KvIn& in, F&& mid) {
    const int lane = threadIdx.x & 63;
    const rsrc_t rk = mk_rsrc(p.bk, ACT), rv = mk_rsrc(p.bv, ACT), rh = mk_rsrc(p.shift, DM * 8);
    const int row = (tile / 10) * 64 + (lane >> 4) * 16, col = (tile % 10) * 64 + (lane & 15) * 4;
    const u32 vo = (u32)(row * DM + col) * 2u, vp = row > 0 ? vo - 3 * (DM * 2) : OOB;
#pragma unroll
    for (int i = 0; i < 2; ++i) in.s[i] = ldin16(rh, (u32)col * 8u + i * 16, 0);
#pragma unroll
    for (int i = 0; i < 19; ++i) { if (i == 7) mid(); in.k[i] = i < 3 ? ldin8(rk, vp, i * (DM * 2)) : ldin8(rk, vo, (i - 3) * (DM * 2)); if (i >= 3) in.v[i - 3] = ldin8(rv, vo, (i - 3) * (DM * 2)); }
}
DEV void kvt_make(rsrc_t rs, lptr xt, int tile, const KvIn& in) {
    const int lane = threadIdx.x & 63;
#pragma unroll
    for (int c = 0; c < 2; ++c) {
        const v4u sv = in.s[c];
        const f32x2 sh[4] = {{lo_f(sv.x), lo_f(sv.z)}, {hi_f(sv.x), hi_f(sv.z)}, {lo_f(sv.y), lo_f(sv.w)}, {hi_f(sv.y), hi_f(sv.w)}};
        f32x2 kf[19];
#pragma unroll
        for (int i = 0; i < 19; ++i) kf[i] = up2(in.k[i][c]);
        u32 kv[16];
#pragma unroll
        for (int i = 0; i < 16; ++i) kv[i] = pk2(((kf[i] * sh[3] + kf[i + 1] * sh[2]) + (kf[i + 2] * sh[1] + kf[i + 3] * sh[0])) * up2(in.v[i][c]));
#pragma unroll
        for (int q = 0; q < 8; ++q) {
            const u32 sel = (q & 1) ? 0x07060302u : 0x05040100u; const u32* z = kv + (q >> 1);
            *(LAS v2u*)(xt + ((2 * c + (q & 1)) * 16 + (lane & 15)) * 144 + (lane >> 5) * 64 + (q >> 1) * 16 + ((lane >> 4) & 1) * 8) =
                (v2u){__builtin_amdgcn_perm(z[4], z[0], sel), __builtin_amdgcn_perm(z[12], z[8], sel)};
        }
    }
    const u32 o = W_KVT + (u32)(((tile % 10) * 64 + 4 * (lane >> 3)) * SEQ + (tile / 10) * 64 + (lane & 7) * 8) * 2u;
#pragma unroll
    for (int i = 0; i < 8; ++i) st16(rs, o + (u32)(32 * (i & 1) + (i >> 1)) * (SEQ * 2), lds_r16(xt + (8 * i + (lane >> 3)) * 144 + (lane & 7) * 16));
}
struct RowX { v2u a, b; u32 c; };
DEV void ln_row(rsrc_t rs, int row, const RowX& r) {
    const f32x2 x[5] = {up2(r.a.x), up2(r.a.y), up2(r.b.x), up2(r.b.y), up2(r.c)};
    const f32x2 st = ln_stats(x);
    u32 hp[5];
#pragma unroll
    for (int q = 0; q < 5; ++q) hp[q] = pk2(up2(pkv(x[q] - st.x)) * st.y);
    store_h(rs, W_H1, row, hp);
}
DEV v4u mem0_load(const Params& p, int j) {
    const u32 n = (u32)(j * 256 + threadIdx.x);
    return ldin16(mk_rsrc(p.mem0, 8 * DM * 2), n < 640u ? ((2 * ((n >> 2) & 3) + (n & 1)) * DM + (n >> 4) * 16 + (n & 2) * 4) * 2u : OOB, 0);
}
DEV void pre_flags(rsrc_t rs, int j, v4u m) {
    const u32 n = (u32)(j * 256 + threadIdx.x);
#pragma unroll
    for (int i = 0; i < 4; ++i) m[i] = m[i] == PSENT ? 0x7FC17FC1u : m[i];
    st4s(rs, n - 36u * 256u < F_CLR ? foff(n - 36u * 256u) : OOB, 0, 0u);
    st16(rs, n < 640u ? W_MEM0 + (n >> 9) * 32768u + (n & 511u) * 16u : OOB, m);
}
constexpr int KT0 = NPRE * 4, LN0 = NPRE * 8;
struct Early { RowX r[2]; v4u m; };
DEV void conv_early(const Params& p, rsrc_t rs, int j, Early& e) {
    const int lane = threadIdx.x & 63, w = uni(threadIdx.x >> 6), u = j * 4 + w;
    if (j < NPRE) {
        const rsrc_t rx = mk_rsrc(p.x, ACT);
#pragma unroll
        for (int i = 0; i < 2; ++i) {
            const u32 so = uu((u + i * KT0) * (DM * 2));
            e.r[i].a = __builtin_amdgcn_raw_buffer_load_b64(rx, lane * 8u, so, 0); e.r[i].b = __builtin_amdgcn_raw_buffer_load_b64(rx, lane * 8u + 512u, so, 0);
            e.r[i].c = __builtin_amdgcn_raw_buffer_load_b32(rx, lane * 4u + 1024u, so, 0);
        }
        e.m = mem0_load(p, j);
    }
}
DEV void conv_stage(const Params& p, rsrc_t rs, lptr lds, int j, v2u ep, Early& e) {
    const int tid = threadIdx.x, lane = tid & 63, w = uni(tid >> 6), u = j * 4 + w;
    lptr F = lds + w * CV_W;
    const u32 fb = uu(w * CV_W);
    const rsrc_t rd = mk_rsrc(p.ssmD, DM * 2);
    float y0[16], y1[16];
    KvIn in;
    if (j < NPRE) {
        constexpr int CH = CvL<false>::CH;
        if (w == 1) LST(1500 + j, rt32());
        pre_flags(rs, j, e.m);
        ln_row(rs, u, e.r[0]); ln_row(rs, u + KT0, e.r[1]);
        if (w == 1) LST(1550 + j, rt32());
        CFENCE();
        kvt_load(p, u, in, [&] { VMW(13); BAR(); pre_post(rs, j, 1u, ep); if (w == 0) LST(1450 + j, rt32()); });
        if (j == 20) LST(260, rt32());
        const u32 D = __builtin_amdgcn_raw_buffer_load_b16(rd, (u32)cv_chan(u, lane & 3) * 2u, 0, 0);
#pragma unroll 1
        for (int i = 0; i < 4; ++i) { const int d = cv_chan(u, i); cv_filt<false>(p, fb + i * CH, d, 0); cv_filt<false>(p, fb + i * CH, d, 1); cv_filt<false>(p, fb + i * CH, d, 2); }
        kvt_make(rs, F + CV_Y, u, in);
        cv_zero<false>(F); cv_zero<false>(F + 2 * CH);
        DRAIN(); BAR();
        pre_post(rs, j, 2u, ep);
        if (j == 20) LST(262, rt32());
        pre_wait(rs, 0, NPRE, 2u, ep);
        if (j == 20) LST(281, rt32());
        {
            v4u k[8];
#pragma unroll
            for (int i = 0; i < 8; ++i) k[i] = cv_kvld(rs, cv_chan(u, i >> 1), i & 1);
#pragma unroll
            for (int i = 0; i < 4; ++i) cv_kvput<false>(F + i * CH, k + 2 * i);
        }
        if (j == 20) LST(286, rt32());
#pragma unroll 1
        for (int i = 0; i < 4; i += 2) {
            cv_run<false>(p, rs, F + i * CH, cv_chan(u, i), rdl(D, i), f32x4{}, y0, [](int) {});
            cv_run<false>(p, rs, F + (i + 1) * CH, cv_chan(u, i + 1), rdl(D, i + 1), f32x4{}, y1, [](int) {});
            cv_put(F, i >> 1, y0, y1);
        }
        if (j == 20) LST(287, rt32());
        cv_out(rs, F, u, 0);
        if (j == 20) LST(263, rt32());
    } else {
        const v2u vote = pol_probe(rs, u - NPRE * 4);
        st16(rs, lane == 0 ? foff(F_PRE + 48 + u - NPRE * 4) : OOB, (v4u){ep.x + 1u, ep.y, vote.x, vote.y});
#pragma unroll 1
        for (int t = u + 128; t < 320; t += 16) { kvt_load(p, t, in, [] {}); kvt_make(rs, F, t, in); }
    }
    DRAIN(); BAR();
    pre_post(rs, j, 4u, ep);
    const int b = j < 20 ? -1 : j < 24 ? u - 80 : j >= NPRE ? u - 144 : (w & 1) ? (u >> 1) - 16 : -1, u0 = b < 32 ? b : b + 96, ue = b < 32 ? 128 : 160;
    if (b >= 0) {
        constexpr int CH = CvL<true>::CH;
        cv_zero<true>(F);
        pre_poll(rs, 64, 32, 4u, ep); pre_poll(rs, 0, NCV, 4u, ep);
        v4u k[4];
        auto fetch = [&](u32 dst, int d, int i) { if (i < 5) cv_filt<true>(p, dst, d, i); else if (i < 9) k[i - 5] = cv_kvld(rs, d, i - 5); };
#pragma unroll
        for (int i = 0; i < 9; ++i) fetch(fb, cv_chan(u0, 0), i);
        const u32 ch = (u32)cv_chan(min(u0 + 32 * (lane >> 2), 159), lane & 3);
        u32 D = __builtin_amdgcn_raw_buffer_load_b16(rd, ch * 2u, 0, 0);
        v4u sp = ld16s(rs, W_SP + ch * 16u, 0);
        asm volatile("" : "+v"(D), "+v"(sp));
#pragma unroll 1
        for (int un = u0, n = 0; un < ue; un += 32) {
            if (un == 64) { LST(282, rt32()); LST(288, __builtin_amdgcn_s_memtime()); }
#pragma unroll 1
            for (int i = 0; i < 4; i += 2, n += 2) {
                const int d1 = (int)rdl(ch, n + 1), d2 = (int)rdl(ch, n + 2);
                cv_kvput<true>(F, k);
                cv_run<true>(p, rs, F, 0, rdl(D, n), (f32x4){f_(rdl(sp.x, n)), f_(rdl(sp.y, n)), f_(rdl(sp.z, n)), 0.f}, y0, [&](int q) { fetch(fb + CH, d1, q); });
                cv_kvput<true>(F + CH, k);
                cv_run<true>(p, rs, F + CH, 326, rdl(D, n + 1), (f32x4){f_(rdl(sp.x, n + 1)), f_(rdl(sp.y, n + 1)), f_(rdl(sp.z, n + 1)), 0.f}, y1, [&](int q) { fetch(fb, d2, q); });
                cv_put(F, i >> 1, y0, y1);
            }
            cv_out(rs, F, un, 4);
            if (un == 64) { LST(283, rt32()); LST(289, __builtin_amdgcn_s_memtime()); }
        }
        DRAIN();
        st8s(rs, lane == 0 ? foff(F_PRE + 192 + b) : OOB, 0, (v2u){ep.x + 4u, ep.y});
    }
    if (j == 20) LST(284, rt32());
    if (b == 62) LST(285, rt32());
}

#define M32A(first, d, a, b) do { if (first) asm volatile("v_mfma_f32_32x32x16_bf16 %0, %1, %2, 0" : "=&v"(d) : "a"(a), "v"(b)); \
                                  else asm volatile("v_mfma_f32_32x32x16_bf16 %0, %1, %2, %0" : "+v"(d) : "a"(a), "v"(b)); } while (0)
#define M32B(first, d, a, b) do { if (first) asm volatile("v_mfma_f32_32x32x16_bf16 %0, %1, %2, 0" : "=&v"(d) : "v"(a), "a"(b)); \
                                  else asm volatile("v_mfma_f32_32x32x16_bf16 %0, %1, %2, %0" : "+v"(d) : "v"(a), "a"(b)); } while (0)
#define VCVT(d, a, b) asm volatile("v_cvt_pk_bf16_f32 %0, %1, %2" : "=v"(d) : "v"(a), "v"(b))
#define VADD(d, a, b) asm volatile("v_add_f32 %0, %1, %2" : "=v"(d) : "v"(a), "v"(b))
#define LD4A(d, vo, so) asm volatile("buffer_load_dword %0, %1, %2, %3 offen sc1" : "=v"(d) : "v"(vo), "s"(srd), "s"(so) : "memory")
DEV u32 wst_src(int lane, int ld) { return (u32)((lane >> 4) * ld + 8 * ((lane & 15) ^ ((lane >> 4) << 2))) * 2u; }
DEV u32 wst_rd(int lane, int w) { const int q4 = (lane >> 2) & 3; return (u32)(q4 * 256 + ((w ^ q4) * 64) + ((lane >> 4) & 1) * 32 + (lane & 3) * 8); }

constexpr int QK_SEL = 81920;
#define QK_LEAD 12
#define QK_SMP 29
#define QK_YB 26
#define QK_W0 2
DEV void role_qkv(const Params& p, lptr lds, int c, v2u ep) {
    const int tid = threadIdx.x, lane = tid & 63, w = uni(tid >> 6), hh = lane >> 5, r32 = lane & 31;
    const int m = c >> 5, h = c & 31;
    const rsrc_t rs = mk_rsrc(p.ws, W_END);
    v4u wt[40];
    {
        const v4u sw = mk_srd(m == 0 ? p.Wq : m == 1 ? p.Wk : p.Wv, DM * NQ * 2), sx = mk_srd(p.x, ACT);
        const u32 vo = wst_src(lane, NQ) + (u32)h * (DH * 2), x8 = (u32)lane * 8u, x4 = (u32)lane * 4u;
        RowX xr[5];
        auto wfill = [&](int i0, int n) {
#pragma unroll 1
            for (int i = i0; i < i0 + n; ++i) DMAIN(sw, uu((4 * i + w) * 1024), vo, uu((16 * i + 4 * w) * (NQ * 2)));
        };
        wfill(0, QK_W0);
#pragma unroll
        for (int i = 0; i < 5; ++i)
            asm volatile("buffer_load_dwordx2 %0, %3, %5, %6 offen\n\tbuffer_load_dwordx2 %1, %3, %5, %6 offen offset:512\n\tbuffer_load_dword %2, %4, %5, %6 offen offset:1024"
                         : "=&v"(xr[i].a), "=&v"(xr[i].b), "=&v"(xr[i].c) : "v"(x8), "v"(x4), "s"(sx), "s"(uu((LN0 + 4 * c + w + 384 * i) * (DM * 2))) : "memory");
        wfill(QK_W0, 10 - QK_W0);
        if (c == 3) LST(275, rt32());
        VMW(10 - QK_W0);
        if (c == 3) LST(276, rt32());
#pragma unroll
        for (int i = 0; i < 5; ++i) {
            asm volatile("" : "+v"(xr[i].a), "+v"(xr[i].b), "+v"(xr[i].c));
            const int row = LN0 + 4 * c + w + 384 * i;
            if (i < 4 || row < SEQ) ln_row(rs, row, xr[i]);
            wfill(10 + 6 * i, 6);
        }
        if (c == 3) LST(264, rt32());
        lptr tb = lds + hh * 2048 + wst_rd(lane, w);
        SFOR(k, 40)
            if constexpr (k == 0) { VMW(15); BAR(); }
            if constexpr (k == 28) {
                if (w == 0) pre_poll(rs, 0, NPRE, 1u, ep);
                DRAIN(); BAR();
                pre_post(rs, 96 + c, 1u, ep);
            }
            lptr q = tb + (k >> 3) * 32768 + (k & 7) * 4096; wt[k] = tr2(q, q + 1024); asm volatile("" : "+a"(wt[k]));
        SEND
    }
    if (c == 3) LST(268, rt32());
    if (w == 0) LST(1350 + c, rt32());
    bool dead = false;
    int n0, n1;
    const u32 t16 = (u32)(w * 2048 + lane * 16), wb = uu(w * 2048);
    u32 ar[16];
#pragma unroll
    for (int j = 0; j < 16; ++j) ar[j] = (u32)(r32 * 256 + ((2 * (j & 7) + hh) ^ (r32 & 15)) * 16 + (j >> 3) * 49152);
    MK_SRD();
#define XRD(y, ks) lds_r16(lds + ar[((ks) & 7) + ((ks) >= 24 ? 8 : 0)] + ((ks) >> 3) % 3 * 16384 + (y) * 8192)
#define QM0(y, b) asm volatile("s_add_u32 m0, %0, %1" :: "s"(wb), "i"((b) * 16384 + (y) * 8192) : "scc", "memory")
#define QDMA(i, vo) asm volatile("buffer_load_dwordx4 %0, %1, %2 offen offset:%3 sc1 lds" :: "v"(vo), "s"(srd), "s"(((i) >> 1) * 8192), "i"(((i) & 1) * 1024) : "memory")
    const u32 h2flag = (lane < 16 && w == 0) ? foff(F_H2 + lane) : OOB;
    const u32 ol = (m == 2 ? (u32)(2 * w + (r32 >> 4)) * 1024u : (u32)((r32 >> 4) * 4 + w) * 1024u) + (u32)(hh * 16 + (r32 & 15)) * 16u
                 + (m == 2 ? W_VO : m == 1 ? W_KQ : W_KQ + 8192u) + (u32)h * 16384u;
    const u32 fo = tid < 2 ? foff(F_QKV + tid * 96 + c) : OOB;
    lptr selw = lds + QK_SEL + (w == 0 ? 0 : 64), selr = lds + QK_SEL;
    PIN(selw);
    static_assert(QK_LEAD > 8 && QK_LEAD % 2 == 0 && LN0 == 10 * 32, "X = 8 comes up, before any item of layer 1");
    static_assert(W_H2 - W_H1 == 2 * 64 * (32 * DM * 2), "h");
    auto run = [&](auto isv) __attribute__((always_inline)) {
        constexpr bool V = decltype(isv)::value;
        constexpr int YB = QK_YB;
        f32x16 ax, ay = {};
        v4u x[8], pp, pq;
        u32 xv = t16 + W_H1, ox = ol, oy = OOB, nn = 1, lt = 0, fcnt = 0, fa = OOB;
        asm volatile("" : "+v"(lt), "+v"(nn));
#pragma unroll
        for (int z = 0; z < 5; ++z) x[z] = XRD(1, 40 - YB + z);
#define QPK(r_, a1, a_, q_) do { if constexpr (V) VCVT(r_[q_], a1[4 * (a_) + 8 * ((q_) >> 1) + 2 * ((q_) & 1)], a1[4 * (a_) + 8 * ((q_) >> 1) + 2 * ((q_) & 1) + 1]); \
                                 else VCVT(r_[q_], a1[4 * (a_) + (q_)], a1[4 * (a_) + (q_) + 8]); } while (0)
#define QPK4(r_, a1, a_) do { QPK(r_, a1, a_, 0); QPK(r_, a1, a_, 1); QPK(r_, a1, a_, 2); QPK(r_, a1, a_, 3); } while (0)
        for (;;) {
            u32 ls = uu(lt), yv, fv, nx, need;
            if (ls != 0u && uu(nn) < 256u) pre_wait(rs, 96, 96, 1u, ep);
            else if (ls != 0u) {
                n1 = (int)(uu(nn) >> 8);
                CFENCE();
                const u32 hf = h2flag + (u32)(~n1 & 1) * 2048u;
                u32 f2 = ld4(rs, hf);
                asm volatile("s_waitcnt vmcnt(0)" : "+v"(f2) :: "memory");
                *(LAS u32*)(selw + 16) = (u32)__builtin_amdgcn_ballot_w64(f2 < (u32)n1) & 0xFFFFu;
                BAR();
                st4s(rs, fa, 0, fcnt);
                fa = OOB;
                if (n1 > NSEG) { LDUMP(w == 0, 64 + c); __builtin_amdgcn_endpgm(); }
                if (uu(*(LAS u32*)(selr + 16)) != 0u) {
                    if (w == 0) wait_flags(rs, hf, (u32)n1, dead);
                    BAR();
                }
            }
#define QT(k) do {} while (0)
            QT(0);
            SCHED();
            QM0(0, 0); NOPS(0); QDMA(0, xv); QDMA(1, xv); QM0(0, 1);
            SFOR(z, 80)
                constexpr int ks = (z + 40 - YB) % 40, zx = z - YB;
                constexpr auto upto = [](int q) { const int d = (q - YB + 49) % 40; return q + 6 + (d == 0 || d == 2 ? 1 : d == 1 ? 2 : 0); };
                if constexpr (zx >= 0 && zx < 40) { if constexpr (V) M32B(ks == 0, ax, x[z & 7], wt[ks]); else M32A(ks == 0, ax, wt[ks], x[z & 7]); }
                else { if constexpr (V) M32B(ks == 0, ay, x[z & 7], wt[ks]); else M32A(ks == 0, ay, wt[ks], x[z & 7]); }
                if constexpr (z == 78) lt = *(LAS u32*)selr;
#pragma unroll
                for (int q = upto(z - 1); q < upto(z); ++q) x[q & 7] = XRD(q % 80 < YB || q % 80 >= YB + 40, (q + 40 - YB) % 40);
                SCHED();
                (void)&srd; (void)&wb;
                if constexpr (z < 2) QDMA(z + 2, xv);
                if constexpr (z >= 2 && z < 14 && (z & 3) == 2) QM0(0, (z + 6) >> 2);
                if constexpr (z >= 3 && z < 15 && (z & 1)) QDMA((z + 5) >> 1, xv);
                if constexpr (z == 4) { n0 = uni((int)nn); n1 = n0 >> 8; n0 &= 255; PINS(n0); PINS(n1); }
                if constexpr (z == 8) { nx = n0 < NSEG ? W_H1 + (u32)n0 * (32 * DM * 2) : OOB; PINS(nx); }
                if constexpr (z == 12) { yv = t16 + nx; PIN(yv); }
                if constexpr (zx == -6) { QT(1); asm volatile("s_waitcnt vmcnt(6) lgkmcnt(0)\n\ts_barrier" ::: "memory"); QT(2); }
                if constexpr (zx == -5) st4s(rs, fa, 0, fcnt);
                if constexpr (zx == -4 || zx == 0 || zx == 4) QM0(1, (zx + 4) >> 2);
                if constexpr (zx >= -3 && zx < 8 && (zx & 1)) QDMA((zx + 3) >> 1, yv);
                if constexpr (zx == 2 || zx == 6) { QPK(pp, ay, 0, (zx - 2) >> 1); QPK(pp, ay, 0, zx >> 1); }
                if constexpr (zx == 8 || zx == 11) { QPK(pq, ay, 1, zx & 2); QPK(pq, ay, 1, (zx & 2) + 1); }
                if constexpr (zx == 9) st16(rs, oy, pp);
                if constexpr (zx == 10) { QT(3); asm volatile("s_waitcnt vmcnt(8)\n\ts_barrier" ::: "memory"); QT(4); }
                if constexpr (zx == 12) st16(rs, oy + 512, pq);
                if constexpr (zx == 13 || zx == 17) QM0(1, (zx - 1) >> 2);
                if constexpr (zx >= 14 && zx < 22 && (zx & 1) == 0) QDMA((zx - 2) >> 1, yv);
                if constexpr (zx == 21) { fcnt = lane == 0 ? (u32)n0 : (u32)n1; fa = fo; asm volatile("" : "+v"(fcnt), "+v"(fa)); }
                if constexpr (zx == 22) { oy = (ol + (u32)n0 * (NH * 16384u)) | (yv & OOB); PIN(oy); }
                if constexpr (zx == 23) { n0 = min(n0 + 1, NSEG); PINS(n0); nx = (u32)min(NSEG, n1 + QK_LEAD); PINS(nx); }
                if constexpr (zx == 24)
                    asm volatile("s_cmp_ge_u32 %1, %0\n\ts_cselect_b32 %0, %4, %1\n\ts_addc_u32 %2, %2, 0\n\ts_cmp_lt_u32 %0, 64\n\ts_cselect_b32 %3, 0, %2"
                                 : "+s"(nx), "+s"(n0), "+s"(n1), "=&s"(need) : "s"(n1 + 64) : "scc");
                if constexpr (zx == 25) { n0 += 1 - (int)min(nx >> 6, 1u); PINS(n0); }
                if constexpr (zx == 26) { nn = (u32)(n0 | n1 << 8); PIN(nn); }
                if constexpr (zx == 27) { ls = W_H1 + (nx + (nx & 64u)) * (32 * DM * 2); PINS(ls); }
                if constexpr (zx == 28) { xv = t16 + ls; PIN(xv); need = nx == 8u ? 255u : need; PINS(need); }
                if constexpr (zx == 34) { asm volatile("s_waitcnt vmcnt(%0) lgkmcnt(0)\n\ts_barrier" :: "i"(QK_SMP < 34 ? 7 : 6) : "memory"); QT(5); }
                if constexpr (zx == QK_SMP) { const u32 po = (nx & 1u) * 2048u; LD4A(fv, h2flag, po); }
                if constexpr (zx == 42) QPK4(pp, ax, 0);
                if constexpr (zx == 43) st16(rs, ox, pp);
                if constexpr (zx == 44) QPK4(pq, ax, 1);
                if constexpr (zx == 45) st16(rs, ox + 512, pq);
                if constexpr (zx == 47) { ox = ol + nx * (NH * 16384u); PIN(ox); }
                if constexpr (z == 74) { asm volatile("s_waitcnt vmcnt(2)" : "+v"(fv) :: "memory"); QT(6); ls = (u32)__builtin_amdgcn_ballot_w64(fv < need) & 0xFFFFu; PINS(ls); }
                if constexpr (z == 75) { lt = ls; PIN(lt); *(LAS u32*)selw = lt; }
                if constexpr (z == 77) asm volatile("s_waitcnt lgkmcnt(2)\n\ts_barrier" ::: "memory");
                SCHED();
            SEND
        }
    };
    if (m == 2) run(IC<1>{}); else run(IC<0>{});
}

#define OU_FLG 25
#define OU_SMP 17
DEV void role_out(const Params& p, lptr lds, int L, int kq, int ng, v2u ep) {
    const int tid = threadIdx.x, lane = tid & 63, w = uni(tid >> 6), hh = lane >> 5, r32 = lane & 31;
    const rsrc_t rs = mk_rsrc(p.ws, W_END);
    v4u wo[64];
    bool dead = false;
    {
        const v4u sw = mk_srd(p.Wo, DM * NQ * 2);
        const u32 vo = wst_src(lane, DM) + (u32)ng * 256u;
        auto fill = [&](int b) {
#pragma unroll 1
            for (int i = 0; i < 8; ++i) DMAIN(sw, uu((b % 5) * 32768 + (4 * i + w) * 1024), vo, uu((kq * 1024 + b * 128 + 16 * i + 4 * w) * (DM * 2)));
        };
#pragma unroll 1
        for (u32 o = (u32)((kq * 5 + ng) * 256 + tid) * 16u + (u32)L * (2 * MB); o < 2 * MB; o += 20 * 4096) st16(rs, o, (v4u){PSENT, PSENT, PSENT, PSENT});
        fill(0);
        VMW(8); BAR();
        if (L == 0) pre_post(rs, 256 + kq * 5 + ng, 1u, ep);
#pragma unroll 1
        for (int b = 1; b < 5; ++b) fill(b);
        lptr tb = lds + hh * 1024 + wst_rd(lane, w);
        SFOR(b, 8)
            VMW(b < 4 ? 32 : 56 - 8 * b); BAR();
            lptr q = tb + (b % 5) * 32768;
#pragma unroll
            for (int k = 0; k < 8; ++k) { lptr a = q + (k >> 1) * 8192 + (k & 1) * 2048; wo[8 * b + k] = tr2(a, a + 4096); if (b < 7) asm volatile("" : "+a"(wo[8 * b + k])); else PIN(wo[8 * b + k]); }
            if constexpr (b < 3) { BAR(); fill(b + 5); }
        SEND
    }
    pre_wait(rs, 0, NPRE, 1u, ep);
    if (kq == 0 && ng == 0) LST(272 + L, rt32());
    MK_SRD();
    const u32 ofl = lane < 8 ? foff(F_O + L * 32 + kq * 8 + lane) : OOB, l16 = (u32)lane * 16u;
    const u32 fo = (tid == 0) ? foff(F_P + L * 32 + kq * 5 + ng) : OOB;
#define OM0(dst, b) asm volatile("s_add_u32 m0, %0, %1" :: "s"(dst), "i"((b) * 8192) : "scc", "memory")
#define ODMA(i, vo) asm volatile("buffer_load_dwordx4 %0, %1, %2 offen offset:%3 sc1 lds" :: "v"(vo), "s"(srd), "s"(((i) >> 1) * 16384), "i"(((i) & 1) * 1024) : "memory")
    auto first = [&](u32 dst, u32 vo) {
#pragma unroll
        for (int b = 0; b < 5; ++b) { OM0(dst, b); NOPS(0); ODMA(2 * b, vo); ODMA(2 * b + 1, vo); }
    };
    u32 pa = W_P + (u32)(L * NSEG) * P_ITEM + (u32)(2 * hh * 4 + kq) * (DM * 4) + (u32)(ng * 128 + w * 32 + r32) * 4u, fv = 0;
    u32 ov = W_VO + 8192u + (u32)(L * NSEG * NH + kq * 8) * 16384u + (u32)w * 2048u + l16;
    bool early = false;
    u32 farv = 1, owed = OOB;
    lptr selw = lds + 131072 + (w == 0 ? 0 : 64), selr = lds + 131072;
    asm volatile("" : "+v"(farv), "+v"(selw), "+v"(selr));
#define OWAIT(strict, loose) asm volatile("s_cmp_eq_u32 %0, 0\n\ts_cbranch_scc1 1f\n\ts_waitcnt vmcnt(%1)\n1:\n\ts_waitcnt vmcnt(%2)\n\ts_barrier" :: "s"(far), "i"(strict), "i"(loose) : "scc", "memory")
    for (int s = 0; s < NSEG; ++s) {
        const u32 dst = uu((s & 1) * 65536 + w * 2048), ndst = dst ^ 65536u, need = s + 1 < NSEG ? (u32)s + 2u : 255u;
        lptr A = lds + (s & 1) * 65536 + l16;
        if (!early) {
            if (!all_(ofl == OOB || fv >= (u32)s + 1)) wait_flags(rs, ofl, (u32)s + 1, dead);
            LS3(L == 0 && w == 1, 1, kq * 5 + ng);
            first(dst, ov);
        }
#define OT(k) do {} while (0)
        u32 far = uu(farv), nv, sv = (u32)s;
        OM0(dst, 5);
        OWAIT(8, 17);
        OT(0);
        v4u o[64];
#pragma unroll
        for (int k = 0; k < 4; ++k) o[k] = lds_r16(A + k * 1024);
        f32x16 acc;
        SCHED();
        SFOR(k, 64)
            if constexpr (k < 56) M32B(k == 0, acc, o[k], wo[k]); else M32V(acc, o[k], wo[k]);
            if constexpr (k < 60) o[k + 4] = lds_r16(A + (k + 4) * 1024);
            SCHED();
            (void)&srd; (void)&dst; (void)&ndst;
            if constexpr (k < 12 && (k & 1) == 0) ODMA(10 + k / 2, ov);
            if constexpr (k == 3) OWAIT(6, 14);
            if constexpr (k == 3 || k == 7) OM0(dst, (k + 21) >> 2);
            if constexpr (k == 12) { ov += NH * 16384u; PIN(ov); }
            if constexpr (k == OU_SMP) LD4A(fv, ofl, 0);
            if constexpr (k == 19) OWAIT(7, 15);
            if constexpr (k == 33) { asm volatile("s_waitcnt vmcnt(1)" : "+v"(fv) :: "memory"); far = (u32)__builtin_amdgcn_ballot_w64(fv < need) & 255u; PINS(far); }
            if constexpr (k == 34) { farv = far; PIN(farv); *(LAS u32*)selw = farv; }
            if constexpr (k == 35) asm volatile("s_waitcnt lgkmcnt(1)\n\ts_barrier" ::: "memory");
            if constexpr (k == OU_FLG) asm volatile("s_waitcnt vmcnt(7)\n\ts_barrier" ::: "memory");
            if constexpr (k == OU_FLG + 1) st4s(rs, owed, 0, sv);
            if constexpr (k == 36) farv = *(LAS u32*)selr;
            if constexpr (k == 41) { nv = farv ? OOB : ov; PIN(nv); }
            if constexpr (k >= 42 && k < 62 && (k & 3) == 2) OM0(ndst, (k - 42) >> 2);
            if constexpr (k >= 43 && k < 63 && (k & 1)) ODMA((k - 43) >> 1, nv);
            if constexpr (k == 48) LD4A(fv, ofl, 0);
            if constexpr (k == 4) OT(1);
            if constexpr (k == 20) OT(2);
            if constexpr (k == 36) OT(3);
            if constexpr (k == 41) OT(4);
            SCHED();
        SEND
        OT(5);
        asm volatile("s_nop 11" : "+v"(acc), "+v"(pa));
#pragma unroll
        for (int i = 0; i < 8; ++i) st4s(rs, pa, (u32)((4 * (i >> 1) + (i & 1)) * (4 * DM * 4)), pk(acc[2 * i], acc[2 * i + 1]));
        asm volatile("s_waitcnt vmcnt(15)" : "+v"(fv) :: "memory");
        OT(6);
        LS3(L == 0 && w == 1, 2, kq * 5 + ng);
        far = uu(farv);
        early = far == 0u || all_(ofl == OOB || fv >= need);
        owed = fo;
        if (far != 0u) {
            if (early) { first(ndst, ov); VMW(10); }
            else { fv = ld4(rs, ofl); asm volatile("s_waitcnt vmcnt(0)" : "+v"(fv) :: "memory"); }
            BAR();
            st4s(rs, fo, 0, (u32)s + 1);
            owed = OOB;
        }
        pa += P_ITEM;
        LS2(L == 0 && w == 1, 160 + kq * 5 + ng);
    }
    LDUMP(L == 0 && w == 1, kq * 5 + ng);
}

DEV void ln_stats2(const f32x2 (&x)[10], f32x2& mean, f32x2& rstd) {
    const f32x2 c = {f_(uu(u_(x[0].x))), f_(uu(u_(x[0].y)))};
    f32x2 d[10];
#pragma unroll
    for (int i = 0; i < 10; ++i) d[i] = x[i] - c;
    f32x2 a = ((d[0] + d[1]) + (d[2] + d[3])) + ((d[4] + d[5]) + (d[6] + d[7])) + (d[8] + d[9]);
    f32x2 b = (d[0] * d[0] + d[2] * d[2] + d[4] * d[4] + d[6] * d[6] + d[8] * d[8]) + (d[1] * d[1] + d[3] * d[3] + d[5] * d[5] + d[7] * d[7] + d[9] * d[9]);
    float a0, a1, b0, b1;
#define LN_DP4(m) "v_add_f32_dpp %0, %0, %0 " m " row_mask:0xf bank_mask:0xf\n\tv_add_f32_dpp %1, %1, %1 " m " row_mask:0xf bank_mask:0xf\n\t" \
                  "v_add_f32_dpp %2, %2, %2 " m " row_mask:0xf bank_mask:0xf\n\tv_add_f32_dpp %3, %3, %3 " m " row_mask:0xf bank_mask:0xf\n\t"
    asm("s_nop 1\n\t" LN_DP4("quad_perm:[1,0,3,2]") LN_DP4("quad_perm:[2,3,0,1]") LN_DP4("row_ror:4") LN_DP4("row_ror:8")
        "v_permlane16_swap_b32 %0, %1\n\tv_add_f32 %0, %0, %1\n\tv_permlane16_swap_b32 %2, %3\n\tv_add_f32 %2, %2, %3\n\ts_nop 1\n\t"
        "v_permlane32_swap_b32 %0, %2\n\tv_add_f32 %0, %0, %2\n\ts_nop 0\n\t"
        "v_readlane_b32 %4, %0, 0\n\tv_readlane_b32 %5, %0, 16\n\tv_readlane_b32 %6, %0, 32\n\tv_readlane_b32 %7, %0, 48\n\ts_nop 1"
        : "+v"(a.x), "+v"(b.x), "+v"(a.y), "+v"(b.y), "=s"(a0), "=s"(b0), "=s"(a1), "=s"(b1));
    const f32x2 m = (f32x2){a0, a1} * (1.0f / DM), v = (f32x2){b0, b1} * (1.0f / DM) - m * m;
    const f32x2 q = up2(pkv(up2(pkv((f32x2){fmaxf(v.x, 0.f), fmaxf(v.y, 0.f)})) + 1e-5f));
    const f32x2 sd = up2(pk(__builtin_amdgcn_sqrtf(q.x), __builtin_amdgcn_sqrtf(q.y)));
    mean = up2(pkv(c + m)); rstd = (f32x2){__builtin_amdgcn_rcpf(sd.x), __builtin_amdgcn_rcpf(sd.y)};
}
DEV void role_row(const Params& p, lptr lds, int id, v2u ep) {
    const int lane = threadIdx.x & 63, w = uni(threadIdx.x >> 6);
    const int pr = id >> 2, L = id & 1, par = (id >> 1) & 1;
    const rsrc_t rs = mk_rsrc(p.ws, W_END), rx = mk_rsrc(p.x, ACT), ro_ = mk_rsrc(p.out, ACT);
    bool dead = false;
    const u32 fp = lane < 20 ? foff(F_P + L * 32 + lane) : OOB, l16 = (u32)lane * 16u, l8 = (u32)lane * 8u, l4 = (u32)lane * 4u, fb = uu(w * CV_W);
    lptr F = lds + w * CV_W;
    MK_SRD();
    for (int s = par; s < NSEG; s += 2) {
        const u32 ro = (u32)(s * 32 + 2 * pr) * (DM * 2);
        if ((s & 6) == 0) {
            if ((s & 30) == 0) pre_poll(rs, s < 32 ? 0 : 192, s < 32 ? NPRE : 64, 4u, ep);
            if (id == 0 && s == 0) LST(274, rt32());
            if (s == 48) st8s(rs, (id == 1 && lane == 0) ? foff(F_EPOCH) : OOB, 0, (v2u){ep.x + 8u, ep.y});
#pragma unroll 1
            for (int i = 0; i < 5; ++i) {
                const u32 d = fb + i * 2048, o = W_Y + (u32)(16 * pr + (s >> 3)) * 10240u + i * 2048;
                DMA32(d, l16, o); DMA32(d + 10240u, l16, o + 81920u);
            }
            DRAIN();
        }
        RowX xr[2];
        auto xload = [&](rsrc_t r, u32 o, auto aux) {
#pragma unroll
            for (int i = 0; i < 2; ++i) {
                xr[i].a = __builtin_amdgcn_raw_buffer_load_b64(r, l8, o + i * (DM * 2), aux.value); xr[i].b = __builtin_amdgcn_raw_buffer_load_b64(r, l8 + 512u, o + i * (DM * 2), aux.value);
                xr[i].c = __builtin_amdgcn_raw_buffer_load_b32(r, l4 + 1024u, o + i * (DM * 2), aux.value);
            }
        };
        if (L == 0) xload(rx, ro, IC<0>{});
        f32x2 y[10], x[10];
        {
            lptr ya = F + (s & 7) * 2 + l16, yc = F + (s & 7) * 2 + 2048 + (lane >> 1) * 16 + (lane & 1) * 5120;
#pragma unroll
            for (int i = 0; i < 10; ++i) {
                lptr q = i < 8 ? ya + (i & 3) * 2560 + (i >> 2) * 1024 : yc + (i & 1) * 2560;
                y[i] = (f32x2){lo_f(*(LAS u16*)q), lo_f(*(LAS u16*)(q + 10240))}; PIN(y[i]);
            }
        }
        wait_flags(rs, fp, (u32)s + 1, dead);
        LS3(L == 0, 3, ri);
        LMX(L == 0, 0, s, rt32());
        {
            const u32 o = uu(W_P + (u32)(L * NSEG + s) * P_ITEM + (u32)pr * (4 * DM * 4));
            if (L == 1) xload(rs, W_X2 + ro, IC<16>{});
            v4u a[4], b[4]; v2u c[4];
#pragma unroll
            for (int kq = 0; kq < 4; ++kq) { a[kq] = ld16s(rs, l16, o + kq * (DM * 4)); SCHED(); b[kq] = ld16s(rs, l16 + 1024u, o + kq * (DM * 4)); SCHED(); c[kq] = ld8s(rs, l8 + 2048u, o + kq * (DM * 4)); SCHED(); }
            const u32 u0[5] = {xr[0].a.x, xr[0].a.y, xr[0].b.x, xr[0].b.y, xr[0].c}, u1[5] = {xr[1].a.x, xr[1].a.y, xr[1].b.x, xr[1].b.y, xr[1].c};
#pragma unroll
            for (int i = 0; i < 5; ++i) { x[2 * i] = (f32x2){lo_f(u0[i]), lo_f(u1[i])}; x[2 * i + 1] = (f32x2){hi_f(u0[i]), hi_f(u1[i])}; }
#pragma unroll
            for (int kq = 0; kq < 4; ++kq)
#pragma unroll
                for (int i = 0; i < 10; ++i)
                    asm volatile("v_dot2_f32_bf16 %0, %2, %3, %0\n\tv_dot2_f32_bf16 %1, %2, %4, %1" : "+v"(x[i].x), "+v"(x[i].y)
                                 : "v"(i < 4 ? a[kq][i & 3] : i < 8 ? b[kq][i & 3] : c[kq][i & 1]), "s"(0x3F80u), "s"(0x3F800000u));
            asm volatile("s_nop 2" : "+v"(x[8]), "+v"(x[9]));
        }
        LS3(L == 0, 4, ri);
        f32x2 mean, rstd;
        ln_stats2(x, mean, rstd);
#pragma unroll
        for (int i = 0; i < 10; ++i) x[i] += (x[i] - mean) * (y[i] * rstd);
        u32 xp[2][5];
#pragma unroll
        for (int i = 0; i < 5; ++i) { xp[0][i] = pkv((f32x2){x[2 * i].x, x[2 * i + 1].x}); xp[1][i] = pkv((f32x2){x[2 * i].y, x[2 * i + 1].y}); }
        if (L == 0) {
#pragma unroll
            for (int i = 0; i < 5; ++i) { x[2 * i] = (f32x2){lo_f(xp[0][i]), lo_f(xp[1][i])}; x[2 * i + 1] = (f32x2){hi_f(xp[0][i]), hi_f(xp[1][i])}; }
            ln_stats2(x, mean, rstd);
#pragma unroll
            for (int i = 0; i < 10; ++i) x[i] = (x[i] - mean) * rstd;
#pragma unroll
            for (int r = 0; r < 2; ++r) {
                u32 hp[5];
#pragma unroll
                for (int i = 0; i < 5; ++i) hp[i] = pkv((f32x2){x[2 * i][r], x[2 * i + 1][r]});
                store_h(rs, W_H2, s * 32 + 2 * pr + r, hp);
            }
#pragma unroll
            for (int r = 0; r < 2; ++r) {
                st8s(rs, l8, W_X2 + ro + r * (DM * 2), (v2u){xp[r][0], xp[r][1]});
                st8s(rs, l8 + 512u, W_X2 + ro + r * (DM * 2), (v2u){xp[r][2], xp[r][3]});
                st4s(rs, l4 + 1024u, W_X2 + ro + r * (DM * 2), xp[r][4]);
            }
            LS3(L == 0, 5, ri);
            DRAIN();
            st4s(rs, lane == 0 ? foff(F_H2 + par * 16 + pr) : OOB, 0, (u32)s + 1);
        } else {
#pragma unroll
            for (int r = 0; r < 2; ++r) {
                st8s(ro_, l8, ro + r * (DM * 2), (v2u){xp[r][0], xp[r][1]});
                st8s(ro_, l8 + 512u, ro + r * (DM * 2), (v2u){xp[r][2], xp[r][3]});
                st4s(ro_, l4 + 1024u, ro + r * (DM * 2), xp[r][4]);
            }
        }
        if (pr == 0) LST(320 + L * 64 + s, rt32());
        LS2(L == 0, 224 + ri);
        LMX(L == 0, 1, s, rt32());
    }
    LDUMP(L == 0, 32 + ri);
}

constexpr float SCALE = 0.08838834764831845f, LOG2E = 1.4426950408889634f;
constexpr int C_K = 0, C_V = 8192, C_QX = 16384, C_MEM = 24576, C_Q = C_MEM + 10240, C_KM = C_Q + 2304, C_VM = C_KM + 2304, C_DUMP = C_VM + 2048,
              C_ZERO = C_DUMP + 1024, C_WL = C_ZERO + 128;
constexpr int NLF = 14;
constexpr int C_MSK = C_WL + 4 * NLF * 2048;
static_assert(C_WL % 16 == 0 && C_MSK + 4096 <= LDS_BYTES, "head lds");
#define CH_VIS 8
#define CH_H0 104
#define CH_W0 80
#define CH_MH 3
#define CH_MW 3
#define CH_REC 28
#define CH_RTY 54u
#define CH_RISE 20u
#define CH_FALL 3u
#define CH_PRE 7
#define CH_PRW 2
#define CH_T0 205
#define CH_UP 2
#define CH_WX 6
#define WAIT_TXT(tmp, t4) "s_memrealtime vcc\n\t.p2align 5\n1:\n\ts_waitcnt lgkmcnt(0)\n\ts_sub_u32 " tmp ", " t4 ", vcc_lo\n\ts_memrealtime vcc\n\ts_cmpk_lt_u32 " tmp ", 400\n\ts_cbranch_scc1 1b\n\t" \
                          "s_add_u32 " tmp ", " tmp ", 4\n\ts_cselect_b32 " tmp ", " tmp ", 0\n2:\n\ts_sub_u32 " tmp ", " tmp ", 1\n\ts_cbranch_scc0 2b\n\t"
DEV void wait_until(u32 t) { u32 tmp; asm volatile(WAIT_TXT("%0", "%1") "s_waitcnt lgkmcnt(0)" : "=&s"(tmp) : "s"(uu(t - 4u)) : "vcc", "scc", "memory"); }
#define SMF_A(acc, a, b) asm volatile("v_smfmac_f32_16x16x64_bf16 %0, %1, %2, %3" : "+v"(acc) : "v"(a), "a"(b), "v"(idx))
#define SMF_V(acc, a, b) asm volatile("v_smfmac_f32_16x16x64_bf16 %0, %1, %2, %3" : "+v"(acc) : "v"(a), "v"(b), "v"(idx))
#define MF0(d, a, b) asm volatile("v_mfma_f32_16x16x32_bf16 %0, %1, %2, 0" : "=&v"(d) : "v"(a), "v"(b))
#define MF1(d, a, b) asm volatile("v_mfma_f32_16x16x32_bf16 %0, %1, %2, %0" : "+v"(d) : "v"(a), "v"(b))
#define MF0H(d, a, b) asm volatile("v_mfma_f32_16x16x16_bf16 %0, %1, %2, 0" : "=&v"(d) : "v"(a), "v"(b))
#define MF1H(d, a, b) asm volatile("v_mfma_f32_16x16x16_bf16 %0, %1, %2, %0" : "+v"(d) : "v"(a), "v"(b))
DEV v8u tr_frag(lptr p0, lptr p1, lptr p2, lptr p3) { return cat8(tr2(p0, p1), tr2(p2, p3)); }
constexpr int qslot(int ks) { return (ks & 1) && ks < 8 ? ks >> 1 : -1; }
constexpr int kslot(int ks) { return ks < 4 ? 4 + ks : -1; }
constexpr int vslot(int ks) { return ks >= 4 ? 4 + ks : -1; }
DEV u32 ring_map(u32 off) { return ((off >> 13) << 15) + (off & 8191u); }
DEV u32 oslot(int s) { return (u32)(s & 3) << 18 | (u32)(s & 2) << 13; }
#define ZRD(x, i) x = __builtin_bit_cast(f32x4, lds_r16(ZP + 16 * (i)))
#define VMAX(d, a, b) asm volatile("v_max_f32 %0, %1, %2" : "=v"(d) : "v"(a), "v"(b))
#define VMAX3(d, a, b, c_) asm volatile("v_max3_f32 %0, %1, %2, %3" : "=v"(d) : "v"(a), "v"(b), "v"(c_))
#define VEXP(d, a) asm volatile("v_exp_f32 %0, %1" : "=v"(d) : "v"(a))
#define VMULC(d, a) asm volatile("v_mul_f32 %0, 0x3e0293ee, %1" : "=v"(d) : "v"(a))
#define CADD(d, a, b) do { d = (a) + (b); PIN(d); } while (0)
#define CMULC(d, a) do { d = (a) * CEXP; PIN(d); } while (0)
#define CCVT(d, a, b) do { d = pk(a, b); PIN(d); } while (0)
#define LGK(n) __builtin_amdgcn_s_waitcnt(0xC07F | (n) << 8)
#define SEL(d, a, b) asm volatile("v_cndmask_b32 %0, %1, %2, %3" : "=v"(d) : "v"(a), "v"(b), "s"(himask))
#define QPERM(d, a, q) asm volatile("v_mov_b32_dpp %0, %1 quad_perm:" q " row_mask:0xf bank_mask:0xf" : "=v"(d) : "v"(a))

#define AL_H 3
#define HD_SMP 70
DEV void role_head(const Params& p, lptr lds, int L, int h, v2u ep) {
    const int tid = threadIdx.x, lane = tid & 63, w = uni(tid >> 6), g = lane >> 4, r = lane & 15;
    const rsrc_t rs = mk_rsrc(p.ws, W_END);
    v8u wq0[10], wq1[10], wk0[10], wk1[10], wv0[10], wv1[10];
    lptr WL = lds + C_WL + w * (NLF * 2048) + lane * 16;
    {
        constexpr int GS = 1056, CS = 48 * GS;
        const v4u sq = mk_srd(p.Wq, DM * NQ * 2), sk = mk_srd(p.Wk, DM * NQ * 2), sv = mk_srd(p.Wv, DM * NQ * 2);
        const u32 vo = (u32)(g * NQ + h * DH + 8 * (r ^ ((g & 1) << 3))) * 2u;
        auto fill = [&](int ch) {
#pragma unroll 1
            for (int i = 0; i < 4; ++i) {
                const u32 dst = uu((ch % 3) * CS + (4 * i + w) * GS), so = uu((ch * 64 + 16 * i + 4 * w) * (NQ * 2));
                DMAIN(sq, dst, vo, so); DMAIN(sk, dst + 16 * GS, vo, so); DMAIN(sv, dst + 32 * GS, vo, so);
            }
        };
        fill(0); fill(1); fill(2);
        const int q4 = r >> 2;
        lptr tb = lds + (4 * (g >> 1) + 2 * (q4 >> 1) + (g & 1)) * GS + (q4 & 1) * 256 + ((w * 64 + (r & 3) * 8) ^ ((q4 & 1) << 7));
        SFOR(ch, 10)
            VMW(ch < 8 ? 24 : 108 - 12 * ch);
            BAR();
            auto frag = [&](int m, int t) { lptr b = tb + (ch % 3) * CS + m * 16 * GS + t * 32; return tr_frag(b, b + 512, b + 8 * GS, b + 8 * GS + 512); };
            wq0[ch] = frag(0, 0); wq1[ch] = frag(0, 1); wk0[ch] = frag(1, 0); wk1[ch] = frag(1, 1); wv0[ch] = frag(2, 0); wv1[ch] = frag(2, 1);
            asm volatile("" : "+a"(wq0[ch]), "+a"(wk0[ch]), "+v"(wv0[ch]), "+v"(wv1[ch]));
            if constexpr (qslot(ch) < 0) asm volatile("" : "+a"(wq1[ch])); else asm volatile("" : "+v"(wq1[ch]));
            if constexpr (kslot(ch) < 0) asm volatile("" : "+a"(wk1[ch])); else asm volatile("" : "+v"(wk1[ch]));
            BAR();
            if constexpr (ch < 7) fill(ch + 3);
        SEND
#pragma unroll
        for (int ch = 0; ch < 10; ++ch) {
            if (qslot(ch) >= 0) { lds_w16(WL + qslot(ch) * 2048, wq1[ch].lo); lds_w16(WL + qslot(ch) * 2048 + 1024, wq1[ch].hi); }
            if (kslot(ch) >= 0) { lds_w16(WL + kslot(ch) * 2048, wk1[ch].lo); lds_w16(WL + kslot(ch) * 2048 + 1024, wk1[ch].hi); }
            if (vslot(ch) >= 0) { lds_w16(WL + vslot(ch) * 2048, wv1[ch].lo); lds_w16(WL + vslot(ch) * 2048 + 1024, wv1[ch].hi); }
        }
        CFENCE();
    }
    const u32 idx = (r & 2) ? 0xEEEEu : 0x4444u;
    unsigned long long himask = 0xCCCCCCCCCCCCCCCCull;
    PINS(himask);
    bool dead = false;
    if (h == 3) LST(266 + L, rt32());
    if (w == 0) LST(1240 + L * 32 + h, rt32());
    if (L == 1) {
        KvIn in;
        kvt_load(p, KT0 + 4 * h + w, in, [] {}); kvt_make(rs, lds + w * 9216, KT0 + 4 * h + w, in);
        DRAIN(); BAR();
        pre_post(rs, 64 + h, 4u, ep);
    }
    const u32 pol0 = pol_wait(rs, ep);
    if (h == 3 && L == 0) LST(277, rt32());
    const u32 l16 = (u32)lane * 16u;
    const u32 mring = (u32)L * MB + (pol0 ^ 16384u), hint = mring + 4 * 65536u;
    const u32 fkh = lane < 3 ? foff(F_QKV + L * 96 + lane * 32 + h) : lane < 7 ? hint + (u32)(lane - 3) * 4u : OOB;
    u32 tm = 0;
    MK_SRD();
    u32 ksrc = uu(W_KQ + (u32)(L * (NSEG - 1) * NH + h) * 16384u + w * 2048), vsrc = ksrc + (W_VO - W_KQ);
    u32 dk = uu(C_K + w * 2048), dv = dk + (C_V - C_K), dq = dk + (C_QX - C_K), dm = uu(C_MEM + w * 2048), dm2 = uu(w < 2 ? C_MEM + 8192 + w * 1024 : 0);
    asm volatile("" : "+s"(dk), "+s"(dv), "+s"(dq), "+s"(dm), "+s"(dm2));
    auto keys_wait = [&](u32 need) {
#pragma unroll 1
        for (int n = 0; !dead; ++n) {
            const bool beat = need + 2 * L > 2u;
            if (beat) wait_until(tm);
            const u32 hv = ld4(rs, lane == 7 ? foff(F_ERR) : fkh);
            if (beat) { tm = rdl(hv, 5); wait_until(rdl(hv, 3) - 30u); }
            const bool any = need == (u32)L;
            const u32 f0 = ld4(rs, any ? foff(F_QKV + 96 + lane) : fkh), f1 = ld4(rs, any ? foff(F_QKV + 128 + lane) : fkh);
            if (all_(min(f0, f1) >= need || (!any && lane >= 3))) break;
            dead = n > SPIN_CAP || rdl(hv, 7) != 0u;
            st4s(rs, dead ? foff(F_ERR) : OOB, 0, 1u);
        }
        CFENCE();
        DMA32(dk, l16, ksrc); DMA32(dv, l16, vsrc); DMA32(dq, l16, ksrc ^ 8192u);
    };
    if (L == 0) { st4s(rs, tid == 0 ? hint + 4096u + (u32)h * 128u : OOB, 0, 0u); keys_wait(1u); }
    lptr qw = lds + C_Q + (2 * g) * 288 + (32 * w + 2 * r) * 2;
    lptr kw = qw + (C_KM - C_Q); PIN(kw);
    lptr qr = lds + C_Q + (2 * (r >> 2) + (r & 1)) * 288 + g * 16;
    lptr LB = lds + l16, VX = lds + C_V + w * 2048 + l16;
    PIN(VX);
    if (tid >= 64 && tid < 70) { const u32 z = tid == 68 ? 0x3F803F80u : tid == 69 ? PSENT : 0u; lds_w16(lds + C_ZERO + (tid - 64) * 16, (v4u){z, z, z, z}); }
    {
        f32x4 mk;
#pragma unroll
        for (int i = 0; i < 4; ++i) mk[i] = 16 * (w & 1) + 4 * g + i <= 16 * (w >> 1) + r ? 0.f : -1000000000.0f / SCALE;
        *(LAS f32x4*)(lds + C_MSK + tid * 16) = mk;
    }
    u32 zaddr = 0; PIN(zaddr);
    lptr ZP = lds + zaddr + C_ZERO;
    const float CEXP = SCALE * LOG2E;
#define LF_RD(slot, hf) lf[slot][hf] = lds_r16(WL + (slot) * 2048 + (hf) * 1024)
    BAR();
    const u32 ofl = tid == 0 ? foff(F_O + L * 32 + h) : OOB;
    u32 oso = (((g | L) & 1) ? OOB : 0u) + (u32)L * MB + (pol0 ^ oslot(-L)) + ring_map((u32)((r >> 3) * 32768 + h * 1024 + w * 256 + (g >> 1) * 128 + (r & 7) * 16));
    u32 missed = 0;
    for (int s = -L; s < NSEG;) {
        u32 sv = (u32)max(s, 0), ms = s > 0 ? mring + (sv & 3u) * 65536u : W_MEM0, ms2 = ms + 32768u + (w & 1) * 1024;
        ms += w * 2048;
        asm volatile("" : "+s"(ms), "+s"(ms2)); PIN(sv);
        f32x4 aq0, aq1, ak0, ak1;
        v4u lf[NLF][2];
        ZRD(aq0, 0); ZRD(aq1, 1); ZRD(ak0, 2); ZRD(ak1, 3);
#pragma unroll
        for (int i = 0; i < 4; ++i) { LF_RD(i, 0); LF_RD(i, 1); }
        SCHED();
        v4u A[10];
#define HT(k) do {} while (0)
        v4u kx[8], qf[4], kmf[4], vx[2], ones, P;
        float fa, fb, fc, fd, inv, sx[4], t[4], tm0, tm1; u32 pa, pb, fl, e01, e23, pmm;
        f32x4 av0, av1, s0, s1, sm, oa, ob, rsum;
        v2u vm, pm;
#define LW(sl) cat8(lf[sl][0], lf[sl][1])
#define PQ(ks, tl) do { if constexpr (!(tl)) SMF_A(aq0, A[ks], wq0[ks]); else if constexpr (qslot(ks) < 0) SMF_A(aq1, A[ks], wq1[ks]); else { const v8u lw_ = LW(qslot(ks)); SMF_V(aq1, A[ks], lw_); } } while (0)
#define PK(ks, tl) do { if constexpr (!(tl)) SMF_A(ak0, A[ks], wk0[ks]); else if constexpr (kslot(ks) < 0) SMF_A(ak1, A[ks], wk1[ks]); else { const v8u lw_ = LW(kslot(ks)); SMF_V(ak1, A[ks], lw_); } } while (0)
#define PV(ks, tl) do { if constexpr (!(tl)) SMF_V(av0, A[ks], wv0[ks]); else if constexpr (vslot(ks) < 0) SMF_V(av1, A[ks], wv1[ks]); else { const v8u lw_ = LW(vslot(ks)); SMF_V(av1, A[ks], lw_); } } while (0)
#define KXR(i) kx[i] = lds_r16(LB + C_K + ((i) & 1) * 4096 + ((i) >> 1) * 1024)
#define XW(q_) do { *(LAS u32*)(q_) = pa; *(LAS u32*)((q_) + 288) = pb; } while (0)
        auto slot = [&](auto zc) __attribute__((always_inline)) {
            constexpr int z = decltype(zc)::value;
            if constexpr (z < 14) PQ(z >> 1, z & 1);
            else if constexpr (z < 17) PQ(z - 7, 0);
            else if constexpr (z < 20) PQ(z - 10, 1);
            else if constexpr (z < 34) PK((z - 20) >> 1, z & 1);
            else if constexpr (z < 37) PK(z - 27, 0);
            else if constexpr (z < 40) PK(z - 30, 1);
            else if constexpr (z < 42) { if constexpr (z == 40) MF0(s0, kx[0], qf[0]); else MF0(s1, kx[1], qf[0]); }
            else if constexpr (z < 48) { if constexpr (z & 1) MF1(s1, kx[z - 40], qf[(z - 40) >> 1]); else MF1(s0, kx[z - 40], qf[(z - 40) >> 1]); }
            else if constexpr (z < 54) PV(z - 48, 0);
            else if constexpr (z == 54) MF0(sm, kmf[0], qf[0]);
            else if constexpr (z < 58) MF1(sm, kmf[z - 54], qf[z - 54]);
            else if constexpr (z < 62) PV(z - 52, 0);
            else if constexpr (z < 64) PV(z - 62, 1);
            else if constexpr (z == 64) MF0(oa, vx[0], P);
            else if constexpr (z == 65) MF0(ob, vx[1], P);
            else if constexpr (z == 66) MF0(rsum, ones, P);
            else if constexpr (z < 75) PV(z < 70 ? z - 63 : z == 70 ? 2 : z == 71 ? 7 : z == 72 ? 3 : z - 65, 1);
            else { const v2u ones2 = ones.lo; MF1H(rsum, ones2, pm); }
            if constexpr (z < 6) A[4 + z] = lds_r16(LB + C_MEM + (4 + z) * 1024);
            if constexpr (z >= 6 && z < 10) LF_RD(4 + ((z - 6) >> 1), z & 1);
            if constexpr (z >= 14 && z < 18) LF_RD(6 + ((z - 14) >> 1), z & 1);
            if constexpr (z >= 22 && z < 28) KXR(z - 22);
            if constexpr (z == 28) XW(qw);
            if constexpr (z == 29 || z == 30) KXR(z - 23);
            if constexpr (z == 32 || z == 47) asm volatile("s_barrier" ::: "memory");
            if constexpr (z >= 33 && z < 37) qf[z - 33] = lds_r16(qr + (z - 33) * 64);
            if constexpr (z == 20) ZRD(av0, 0);
            if constexpr (z == 43) XW(kw);
            if constexpr (z == 44 || z == 45) vx[z - 44] = lds_r16(VX + (z - 44) * 1024);
            if constexpr (z >= 48 && z < 52) kmf[z - 48] = lds_r16(qr + (C_KM - C_Q) + (z - 48) * 64);
            if constexpr (z == 52) ZRD(av1, 1);
            if constexpr (z == 37) ones = lds_r16(ZP + 64);
            if constexpr (z >= 57 && z < 64) LF_RD(8 + ((z - 57) >> 1), (z - 57) & 1);
            if constexpr (z >= 65 && z < 70) LF_RD(8 + ((z - 58) >> 1), (z - 58) & 1);
            if constexpr (z == HD_SMP) LD4A(fl, fkh, 0);
            SCHED();
            if constexpr (z == 19) HT(2);
            if constexpr (z == 32) HT(3);
            if constexpr (z == 47) HT(4);
            if constexpr (z == 64) HT(5);
            if constexpr (z == 19) LGK(4);
            if constexpr (z == 21 || z == 41) LGK(1);
            if constexpr (z == 56 || z == 73) LGK(0);
            if constexpr (z == 31 || z == 46) LGK(2);
            if constexpr (z == 39) LGK(4);
            if constexpr (z >= 53 && z < 56) LGK(57 - z);
            if constexpr (z == 64) LGK(3);
            if constexpr (z == 68) LGK(5);
            if constexpr (z >= 70 && z < 73) LGK(74 - z);
            SCHED();
            if constexpr (z == 18) CADD(fa, aq0[0], aq0[2]);
            if constexpr (z == 19) CADD(fb, aq0[1], aq0[3]);
            if constexpr (z == 20) CMULC(fa, fa);
            if constexpr (z == 21) CADD(fc, aq1[0], aq1[2]);
            if constexpr (z == 22) CADD(fd, aq1[1], aq1[3]);
            if constexpr (z == 23) CMULC(fc, fc);
            if constexpr (z == 24) CMULC(fb, fb);
            if constexpr (z == 25) CMULC(fd, fd);
            if constexpr (z == 26) CCVT(pa, fa, fc);
            if constexpr (z == 27) CCVT(pb, fb, fd);
            if constexpr (z == 38) CADD(fa, ak0[0], ak0[2]);
            if constexpr (z == 39) CADD(fb, ak0[1], ak0[3]);
            if constexpr (z == 41) { CADD(fc, ak1[0], ak1[2]); CADD(fd, ak1[1], ak1[3]); }
            if constexpr (z == 42) { CCVT(pa, fa, fc); CCVT(pb, fb, fd); }
            if constexpr (z >= 49 && z < 53) SEL(sx[z - 49], s0[z - 49], s1[z - 49]);
            if constexpr (z >= 53 && z < 57) VEXP(t[z - 53], sx[z - 53]);
            if constexpr (z == 57) CCVT(e01, t[0], t[1]);
            if constexpr (z == 58) CCVT(e23, t[2], t[3]);
            if constexpr (z == 59) QPERM(P.x, e01, "[0,1,0,1]");
            if constexpr (z == 60) QPERM(P.z, e01, "[2,3,2,3]");
            if constexpr (z == 61) QPERM(P.y, e23, "[0,1,0,1]");
            if constexpr (z == 62) QPERM(P.w, e23, "[2,3,2,3]");
            if constexpr (z == 63) CADD(fa, av0[0], av0[2]);
            if constexpr (z == 64) { asm volatile("" :: "v"(sm)); VEXP(tm0, sm[0]); }
            if constexpr (z == 65) VEXP(tm1, sm[1]);
            if constexpr (z == 66) CADD(fb, av0[1], av0[3]);
            if constexpr (z == 67) CCVT(pmm, tm0, tm1);
            if constexpr (z >= 68 && z < 72) SEL(oa[z - 68], oa[z - 68], ob[z - 68]);
            if constexpr (z == 72) asm volatile("v_cndmask_b32 %0, %1, 0, %2" : "=v"(pm.x) : "v"(pmm), "s"(himask));
            if constexpr (z == 73) asm volatile("v_cndmask_b32 %0, 0, %1, %2" : "=v"(pm.y) : "v"(pmm), "s"(himask));
            if constexpr (z == 74) LGK(15);
            if constexpr (z == 75) CCVT(vm.x, fa, fb);
            SCHED();
        };
#define HT0 ""
#define HT0O
        {
            u32 tmp;
            asm volatile("s_mov_b32 m0, %2\n\t" WAIT_TXT("%0", "%1") HT0 "buffer_load_dwordx4 %4, %5, %6 offen sc1 lds\n\tbuffer_load_dwordx4 %4, %5, %6 offen offset:1024 sc1 lds\n\t"
                         "s_and_b32 m0, %3, %3\n\ts_cbranch_scc0 3f\n\tbuffer_load_dwordx4 %4, %5, %7 offen sc1 lds\n3:\n\t.p2align 6\n\t.rept %8\n\ts_nop 0\n\t.endr\n\ts_waitcnt vmcnt(0) lgkmcnt(0)\n\ts_barrier"
                         : "=&s"(tmp) : "s"(uu(tm - 4u)), "s"(dm), "s"(dm2), "v"(l16), "s"(srd), "s"(ms), "s"(ms2), "i"(AL_H) HT0O : "vcc", "scc", "memory");
        }
        HT(1);
#pragma unroll
        for (int ks = 0; ks < 4; ++ks) A[ks] = lds_r16(LB + C_MEM + ks * 1024);
        SCHED();
        st4s(rs, ofl, 0, sv);
        SCHED();
        static_for<0, 76>(slot);
        asm volatile("s_nop 2\n\tv_add_f32 %1, %3, %5\n\tv_add_f32 %2, %4, %6\n\tv_cvt_pk_bf16_f32 %0, %1, %2"
                     : "=v"(vm.y), "=&v"(fc), "=&v"(fd) : "v"(av1[0]), "v"(av1[1]), "v"(av1[2]), "v"(av1[3]));
        __builtin_amdgcn_s_waitcnt(0xCF7F);
        f32x2 i2; u32 oad; unsigned long long far;
        auto publish = [&] {
            v4u oc; f32x2 u0 = oa.lo, u1 = oa.hi;
            asm volatile("v_pk_mul_f32 %0, %0, %2\n\tv_pk_mul_f32 %1, %1, %2" : "+v"(u0), "+v"(u1) : "v"(i2));
            __builtin_amdgcn_s_waitcnt(0xCF7F);
            asm volatile("v_cvt_pk_bf16_f32 %0, %4, %5\n\tv_cvt_pk_bf16_f32 %2, %4, %5\n\tv_cvt_pk_bf16_f32 %1, %6, %7\n\tv_cvt_pk_bf16_f32 %3, %6, %7\n\t"
                         "v_permlane16_swap_b32 %0, %2\n\ts_nop 0\n\tv_permlane16_swap_b32 %1, %3"
                         : "=&v"(oc.x), "=&v"(oc.y), "=&v"(oc.z), "=&v"(oc.w) : "v"(u0.x), "v"(u0.y), "v"(u1.x), "v"(u1.y));
            st16(rs, oad, oc);
        };
        asm volatile("s_nop 0\n\tv_mfma_f32_16x16x16_bf16 %0, %5, %6, %0\n\tv_rcp_f32 %1, %7\n\tv_add_u32 %3, 0xde800000, %7\n\tv_cmp_lt_u32 %4, %9, %3\n\tv_mov_b32 %2, %1"
                     : "+v"(oa), "=&v"(i2.x), "=&v"(i2.y), "=&v"(oad), "=&s"(far) : "v"(vm), "v"(pm), "v"(rsum[0]), "v"(rsum), "s"(0x3C000000u));
        if constexpr (HD_SMP > 75) LD4A(fl, fkh, 0); else NOPS(0);
        asm volatile("v_cndmask_b32 %0, %1, -16, %2" : "=v"(oad) : "v"(oso), "s"(far));
        __builtin_amdgcn_s_waitcnt(0xCF7F);
        publish();
        SCHED();
        if (__builtin_expect(far != 0ull, 0)) {
            DRAIN(); SCHED();
            bool miss = false;
#pragma unroll 1
            for (int i = 0; i < 10; ++i) { const v4u a = lds_r16(LB + C_MEM + i * 1024); miss |= a.x == PSENT || a.y == PSENT || a.z == PSENT || a.w == PSENT; }
            if (missed < SPIN_CAP && !all_(!miss)) {
                *(LAS u32*)(lds + C_DUMP + (w == 0 ? 0 : 64)) = ld4(rs, foff(F_ERR));
                asm volatile("" : "+v"(fl) :: "memory");
                tm = rdl(fl, 5);
                BAR();
                missed = uu(*(LAS u32*)(lds + C_DUMP)) != 0u ? SPIN_CAP : missed + 1;
                st4s(rs, missed == SPIN_CAP ? foff(F_ERR) : OOB, 0, 1u);
                BAR();
                continue;
            }
            float mx = fmaxf(fmaxf(fmaxf(sx[0], sx[1]), fmaxf(sx[2], sx[3])), fmaxf(sm[0], sm[1]));
            mx = max32(max16(fmaxf(mx, dpp<0x4E>(mx))));
#pragma unroll
            for (int i = 0; i < 4; ++i) t[i] = __builtin_amdgcn_exp2f(sx[i] - mx);
            e01 = pk(t[0], t[1]); e23 = pk(t[2], t[3]);
            pmm = pk(__builtin_amdgcn_exp2f(sm[0] - mx), __builtin_amdgcn_exp2f(sm[1] - mx));
            asm volatile("v_cndmask_b32 %0, %2, 0, %3\n\tv_cndmask_b32 %1, 0, %2, %3" : "=&v"(pm.x), "=&v"(pm.y) : "v"(pmm), "s"(himask));
            asm volatile("s_nop 1" : "+v"(e01), "+v"(e23), "+v"(pm));
            QPERM(P.x, e01, "[0,1,0,1]"); QPERM(P.z, e01, "[2,3,2,3]"); QPERM(P.y, e23, "[0,1,0,1]"); QPERM(P.w, e23, "[2,3,2,3]");
            NOPS(1);
            MF0(rsum, ones, P); MF0(oa, vx[0], P); MF0(ob, vx[1], P);
            NOPS(7);
            { const v2u ones2 = ones.lo; MF1H(rsum, ones2, pm); }
#pragma unroll
            for (int i = 0; i < 4; ++i) SEL(oa[i], oa[i], ob[i]);
            NOPS(1);
            MF1H(oa, vm, pm);
            asm volatile("s_nop 7\n\tv_rcp_f32 %0, %3\n\tv_cndmask_b32 %2, -16, %5, %6\n\tv_mov_b32 %1, %0\n\ts_nop 0"
                         : "=&v"(i2.x), "=&v"(i2.y), "=&v"(oad) : "v"(rsum[0]), "v"(rsum), "v"(oso), "s"(far));
            publish();
        }
        SCHED();
        unsigned long long clk;
        asm volatile("s_memrealtime %0" : "=s"(clk) :: "memory");
        HT(6);
        v4u tq[4], tk[8], tv[2];
        f32x4 c0, c1, cm;
        u32 oo, oz, kvo; bool kvok;
        u32 ln = l16; PIN(ln);
        lptr QX = lds + C_QX + (w & 1) * 4096 + ln, VO = lds + C_V + (w ^ 1) * 2048 + ln, VMX = lds + C_VM + w * 512 + (ln >> 1);
#pragma unroll
        for (int i = 0; i < 2; ++i) { tq[i] = lds_r16(QX + i * 1024); kmf[i] = lds_r16(qr + (C_KM - C_Q) + i * 64); tk[2 * i] = lds_r16(LB + C_K + i * 1024); tk[2 * i + 1] = lds_r16(LB + C_K + 4096 + i * 1024); }
        { lptr MK = lds + C_MSK + (w & 1) * 2048 + ln; c0 = *(LAS f32x4*)MK; c1 = *(LAS f32x4*)(MK + 1024); }
        *(LAS v2u*)VMX = vm;
        SCHED();
        SFOR(i, 12)
            if constexpr (i == 0) MF0(cm, kmf[0], tq[0]);
            else if constexpr (i % 3 == 0) MF1(cm, kmf[i / 3], tq[i / 3]); else if constexpr (i % 3 == 1) MF1(c0, tk[2 * (i / 3)], tq[i / 3]); else MF1(c1, tk[2 * (i / 3) + 1], tq[i / 3]);
            if constexpr (i < 2) kmf[2 + i] = lds_r16(qr + (C_KM - C_Q) + (2 + i) * 64);
            if constexpr (i == 0) { tq[2] = lds_r16(QX + 2048); tk[4] = lds_r16(LB + C_K + 2048); }
            if constexpr (i == 1) { tk[5] = lds_r16(LB + C_K + 6144); tq[3] = lds_r16(QX + 3072); }
            if constexpr (i == 2) { tk[6] = lds_r16(LB + C_K + 3072); tk[7] = lds_r16(LB + C_K + 7168); }
            if constexpr (i == 3) { tv[0] = lds_r16(VO); tv[1] = lds_r16(VO + 1024); }
            if constexpr (i == 4) { oo = ln + (ln & 0x300u) + ((vsrc ^ 8192u) + (u32)((w & 1) * 256) + ((u32)s & OOB)); PIN(oo); }
            if constexpr (i == 5) { ksrc += NH * 16384; vsrc += NH * 16384; PINS(ksrc); PINS(vsrc); }
            if constexpr (i == 6) { oso ^= (s & 1) ? oslot(3) : oslot(1); PIN(oso); }
            if constexpr (i == 7) { oz = oso ^ oslot(2); PIN(oz); }
            if constexpr (i == 9) { asm volatile("s_waitcnt vmcnt(1)" : "+v"(fl) :: "memory"); kvok = ((u32)__builtin_amdgcn_ballot_w64(fl < (u32)s + 2) & 7u) == 0u; }
            if constexpr (i == 10) { kvo = l16 | (kvok ? 0u : OOB); PIN(kvo); }
            SCHED();
        SEND
        asm volatile("s_waitcnt lgkmcnt(0)\n\ts_barrier" : "+s"(clk) :: "memory");
        const v2u tvm = *(LAS v2u*)(VMX + (512 - (w & 1) * 1024));
        const v4u pv = lds_r16(ZP + 80);
        DMA32(dk, kvo, ksrc);
        SCHED(); asm volatile("" :: "v"(cm));
        const float u[8] = {c0[0], c0[1], c0[2], c0[3], c1[0], c1[1], c1[2], c1[3]};
        float m, m2;
        {
            asm volatile("v_max3_f32 %0, %2, %3, %10\n\tv_max3_f32 %1, %4, %5, %11\n\tv_max3_f32 %0, %0, %6, %7\n\tv_max3_f32 %1, %1, %8, %9\n\t"
                         "v_max_f32 %0, %0, %1\n\tv_mov_b32 %1, %0\n\ts_nop 1\n\tv_permlane16_swap_b32 %0, %1\n\tv_max_f32 %0, %0, %1\n\tv_mov_b32 %1, %0\n\ts_nop 1\n\t"
                         "v_permlane32_swap_b32 %0, %1\n\tv_max_f32 %0, %0, %1\n\tv_mul_f32 %0, 0xbe0293ee, %0"
                         : "=&v"(m), "=&v"(m2) : "v"(u[0]), "v"(u[1]), "v"(u[2]), "v"(u[3]), "v"(u[4]), "v"(u[5]), "v"(u[6]), "v"(u[7]), "v"(cm[0]), "v"(cm[1]));
        }
        DMA32(dv, kvo, vsrc);
        {
            v4u px; v2u p0, p1;
#define EX2(d, a, b) asm volatile("v_fmamk_f32 %0, %2, 0x3e0293ee, %4\n\tv_fmamk_f32 %1, %3, 0x3e0293ee, %4\n\tv_exp_f32 %0, %0\n\tv_exp_f32 %1, %1" : "=&v"(d.x), "=&v"(d.y) : "v"(a), "v"(b), "v"(m))
            f32x2 ex[5];
            EX2(ex[0], u[0], u[1]); EX2(ex[1], u[2], u[3]); EX2(ex[2], u[4], u[5]); EX2(ex[3], u[6], u[7]); EX2(ex[4], cm[0], cm[1]);
            asm volatile("v_cvt_pk_bf16_f32 %0, %8, %9\n\tv_cvt_pk_bf16_f32 %1, %10, %11\n\tv_cvt_pk_bf16_f32 %2, %12, %13\n\tv_cvt_pk_bf16_f32 %3, %14, %15\n\t"
                         "v_mov_b32 %5, 0\n\tv_mov_b32 %6, 0\n\tv_cvt_pk_bf16_f32 %4, %16, %17\n\tv_cvt_pk_bf16_f32 %7, %16, %17"
                         : "=&v"(px.x), "=&v"(px.y), "=&v"(px.z), "=&v"(px.w), "=&v"(p0.x), "=&v"(p0.y), "=&v"(p1.x), "=&v"(p1.y)
                         : "v"(ex[0].x), "v"(ex[0].y), "v"(ex[1].x), "v"(ex[1].y), "v"(ex[2].x), "v"(ex[2].y), "v"(ex[3].x), "v"(ex[3].y), "v"(ex[4].x), "v"(ex[4].y));
            f32x4 o[4], sum;
            const v2u ones2 = ones.lo;
            const u32 now = (u32)clk;
            SCHED();
            MF0(sum, ones, px); { const u32 q = ksrc ^ 8192u; DMA32(dq, kvo, q); } SCHED();
            MF0(o[0], vx[0], px);
            MF0(o[1], vx[1], px);
            MF0(o[2], tv[0], px);
            MF0(o[3], tv[1], px);
            MF1H(sum, ones2, p0);
            MF1H(o[0], vm, p0);
            MF1H(o[1], vm, p1); st16(rs, oz, pv); SCHED();
            MF1H(o[2], tvm, p0);
            MF1H(o[3], tvm, p1);
            asm volatile("s_nop 2\n\tv_rcp_f32 %0, %1\n\ts_nop 0" : "=v"(inv) : "v"(sum[0]), "v"(sum));
            asm volatile("" : "+v"(o[0]), "+v"(o[1]));
            st16(rs, oo, (v4u){pk2(o[0].lo * inv), pk2(o[0].hi * inv), pk2(o[1].lo * inv), pk2(o[1].hi * inv)});
            asm volatile("" : "+v"(o[2]), "+v"(o[3]));
            st16(rs, oo ^ 2048u, (v4u){pk2(o[2].lo * inv), pk2(o[2].hi * inv), pk2(o[3].lo * inv), pk2(o[3].hi * inv)});
            SCHED();
            LS2(L == 0 && w == 0, h);
            st4s(rs, ((h & 15) == 0 && tid == 192) ? hint + 128u + (u32)h * 8u : OOB, 0, (u32)s << 16 | min(missed, 1u) << 14 | min(now - tm, 0x3FFFu));
            tm = rdl(fl, 5);
            missed = missed == SPIN_CAP ? missed : 0u;
            if (__builtin_expect(s < 1 - L, 0)) {
                if (s < 0) oso -= (ln & 256u) ? 0u : OOB;
#pragma unroll 1
                for (int n = 0; n < (s < 0 ? 0 : 200); ++n) {
                    CFENCE();
                    tm = rdl(ld4(rs, fkh), 6);
                    if (tm != PSENT) break;
                }
            }
        }
        if (__builtin_expect(!kvok, 0) && s + 1 < NSEG) {
            st4s(rs, ln == 0 ? hint + 4096u + (u32)h * 128u : OOB, 0, (u32)s + 1u);
            keys_wait((u32)s + 2u);
        }
        ++s;
    }
    DRAIN(); BAR();
    st4s(rs, ofl, 0, NSEG);
    LDUMP(w == 0, 160 + L * 32 + h);
}

#define WO_NL 3
#define WO_ROT w
#define WO_AH 1
DEV void role_wo(const Params& p, lptr lds, int j, v2u ep) {
    const int tid = threadIdx.x, lane = tid & 63, w = uni(tid >> 6), g = lane >> 4, r = lane & 15;
    const int rg = j & 1, cg = j >> 1, rot = WO_ROT;
    const rsrc_t rs = mk_rsrc(p.ws, W_END);
    v8u wo[8][5];
    {
        const v4u so_ = mk_srd(p.Wo, DM * NQ * 2);
        u32 vo[5];
#pragma unroll
        for (int i = 0; i < 5; ++i) { const int n = 64 * i + lane; vo[i] = (u32)((n / 5) * DM + cg * 40 + (n % 5) * 8) * 2u; }
        v4u junk = {};
        const u32 av = (u32)(j * 5120 + lane * 16);
        auto ahead = [&](int b) {
#pragma unroll
            for (int i = 0; i < 5; ++i) AHEAD(so_, av, uu((8 * w + ((b + rot) & 7)) * (DH * DM * 2) + i * 1024));
        };
        auto fill = [&](int b) {
            if (b + WO_AH < 8) ahead(b + WO_AH);
            const u32 dst = uu(w * 40960 + (b & 3) * 10240), src = uu((8 * w + ((b + rot) & 7)) * (DH * DM * 2));
#pragma unroll
            for (int i = 0; i < 10; ++i) DMAIN(so_, dst + i * 1024, vo[i % 5], src + (i / 5) * (64 * DM * 2));
        };
#pragma unroll 1
        for (int b = 0; b < WO_AH; ++b) ahead(b);
#pragma unroll 1
        for (int b = 0; b < 4; ++b) fill(b);
        const int q4 = r >> 2;
        lptr tb = lds + w * 40960 + (32 * (g >> 1) + 16 * (q4 >> 1) + 8 * ((r >> 1) & 1) + 4 * (g & 1) + (q4 & 1)) * 80 + (r & 1) * 8;
        SFOR(b, 8)
            VMW((b < 5 ? 30 : 70 - 10 * b) + (b + 1 + WO_AH < 8 ? 5 : 0) + (b + 2 + WO_AH < 8 ? 5 : 0) + (b + 3 + WO_AH < 8 ? 5 : 0));
            if constexpr (b + WO_AH == 7) PIN(junk);
#pragma unroll
            for (int t = 0; t < 5; ++t) {
                lptr q = tb + (b & 3) * 10240 + t * 16;
                wo[b][t] = tr_frag(q, q + 160, q + 64 * 80, q + 64 * 80 + 160);
                if (b * 5 + t < 32) asm volatile("" : "+a"(wo[b][t])); else asm volatile("" : "+v"(wo[b][t]));
            }
            CFENCE();
            if constexpr (b < 4) fill(b + 4);
        SEND
    }
    if (j == 0) LST(265, rt32());
    LST(1100 + j * 4 + w, rt32());
    const u32 idx = (r & 2) ? 0xEEEEu : 0x4444u;
    const u32 pol0 = pol_wait(rs, ep);
    BAR();
    const u32 l16 = (u32)lane * 16u;
    u32 oo[8];
#pragma unroll
    for (int i = 0; i < 8; ++i) oo[i] = uu(ring_map((u32)(rg * 32768 + (8 * w + ((i + rot) & 7)) * 1024)));
    const u32 hint = (pol0 ^ 16384u) + 4 * 65536u;
    u32 hvo[2];
#pragma unroll
    for (int l = 0; l < 2; ++l) hvo[l] = lane < 2 ? hint + (u32)(l ^ 1) * MB + (u32)lane * 4u : lane == 2 ? hint + (u32)l * MB + 128u : lane == 3 ? foff(F_ERR) : lane == 4 ? hint + (u32)l * MB + 256u :
                 (lane >= 32 && j == 0 && w == 3) ? hint + (u32)l * MB + (u32)lane * 128u : OOB;
    lptr xw = r < 8 ? lds + w * 640 + (2 * (g & 1) + (g >> 1)) * 32 + r * 4 : lds + 8192 + lane * 4;
    const int G = ((5 * cg) >> 1) + w, tt = 2 * G + ((lane >> 3) & 1) - 5 * cg;
    const bool pub = w < 3 && lane < 32 && tt >= 0 && tt < 5;
    lptr xr = lds + (pub ? tt * 128 + (2 * (lane >> 4) + ((lane >> 2) & 1)) * 32 + (lane & 3) * 8 : 0);
    const u32 mpo = pub ? (pol0 ^ 16384u) + ring_map((u32)(G * 256 + rg * 128 + lane * 4)) : OOB;
    constexpr u32 PER0 = CH_W0 + CH_MW + CH_H0 + CH_MH + 2 * CH_VIS, DW0 = CH_W0 + CH_VIS + CH_MW + CH_REC;
    const bool lead = j == 0 && w == 3;
    const u32 hso = (lead && lane == 0) ? hint : OOB;
    MK_SRD();
    u32 t = 0, tn = 0;
    int sn[2] = {0, 0};
    u32 hav = 8 * CH_H0, wav = 8 * CH_W0, mh = 8 * CH_MH, mw = 8 * CH_MW, ok[2] = {4u, 0u}, dw[2] = {DW0, CH_T0 + CH_REC - 8};
    u32 vo = l16 + pol0 + MB, tp = 0, fin, left, cnt = 0;
    unsigned long long clk = 0;
    {
        if (w == 0) LST(1310 + j, rt32());
        if (lead) {
#pragma unroll 1
            for (int n = 0; n < SPIN_CAP; ++n) {
                CFENCE();
                const u32 a = ld4(rs, foff(F_QKV + lane)), b = ld4(rs, lane < 32 ? foff(F_QKV + 64 + lane) : hvo[0]);
                if (all_(a != 0u && b != (lane < 32 ? 0u : PSENT))) break;
                __builtin_amdgcn_s_sleep(1);
            }
        }
        if (lead) {
            t = rt32() + CH_T0;
            st16(rs, hso, (v4u){t, PER0, t + DW0, t + DW0});
        } else {
#pragma unroll 1
            for (int n = 0; n < SPIN_CAP; ++n) {
                CFENCE();
                const u32 hv = ld4(rs, hvo[1]);
                t = rdl(hv, 0);
                if (rdl(hv, 1) < 400u) break;
                __builtin_amdgcn_s_sleep(1);
            }
        }
    }
    const u32 tm0 = t + DW0;
    tn = t; t -= CH_T0; clk = t - 100u;
    u32 zero = 0, hv = 0xFFFFu, pst = 1, pmp = OOB;
    const u32 psent = PSENT;
    lptr sw = lds + 2560 + w * 4;
    asm volatile("" : "+v"(zero), "+v"(sw));
    auto turn = [&](auto lc) __attribute__((always_inline)) {
        constexpr int L = decltype(lc)::value, M = L ^ 1;
        u32 done, rep, rd, lost, dh, dn, nt, za, pdur, mp; v4u hw;
        const int sc = sn[L];
        auto books = [&](auto ic) __attribute__((always_inline)) {
            constexpr int i = decltype(ic)::value;
            if constexpr (i == 0) { rep = max(rdl(hv, 2), rdl(hv, 4)); PINS(rep); }
            if constexpr (i == 1) { lost = (u32)(__builtin_amdgcn_ballot_w64(hv == (u32)sn[M]) >> 32); PINS(lost); }
            if constexpr (i == 2) { lost |= (rep >> 16) ^ (u32)sn[M]; PINS(lost); }
            if constexpr (i == 3) { lost |= ok[M] ^ 2u; rd = rep & 0xFFFFu; PINS(lost); PINS(rd); }
            if constexpr (i == 4) { lost = lost == 0u ? CH_UP * 8u : 0u; PINS(lost); }
            if constexpr (i == 5) { pdur = ((u32)clk - tp) * 8u; PINS(pdur); pdur = min(pdur, wav + CH_RISE); PINS(pdur); }
            if constexpr (i == 6) { pdur = (done & ok[M]) ? pdur : wav; PINS(pdur); }
            if constexpr (i == 7) { wav = max(pdur, wav - CH_FALL); PINS(wav); }
            if constexpr (i == 8) { nt = min(rd * 8u, hav + CH_RISE); PINS(nt); }
            if constexpr (i == 9) { nt = rd < 0x3FFFu ? nt : hav; PINS(nt); }
            if constexpr (i == 10) { hav = max(nt, hav - CH_FALL); PINS(hav); }
            if constexpr (i == 11) { mw = max(mw - 1u, CH_MW * 8u); PINS(mw); }
            if constexpr (i == 12) { mh = max(mh - 1u, CH_MH * 8u); PINS(mh); }
            if constexpr (i == 13) { rd = rd >> 14 == 1u ? lost : 0u; PINS(rd); }
            if constexpr (i == 14) { mw += rd; mh += lost - rd; PINS(mw); PINS(mh); }
            if constexpr (i == 15) { ok[M] = done ? 1u : (ok[M] & 1u) << 1 | ok[M] >> 2; PINS(ok[M]); }
            if constexpr (i == 16) { dh = (hav + mh) / 8u + (sn[1] < 3 ? CH_VIS + CH_PRE : CH_VIS); PINS(dh); }
            if constexpr (i == 17) { dn = (wav + mw) / 8u + (sn[1] < 3 ? CH_VIS + CH_PRW : CH_VIS); PINS(dn); }
            if constexpr (i == 18) { nt = dw[L] + dh + (u32)max((int)((u32)clk + CH_WX - t), 0); PINS(nt); }
            if constexpr (i == 19) { dh += dn; dw[L] = dn; sn[M] += (int)done; PINS(dh); PINS(sn[M]); }
            if constexpr (i == 20) { rd = tn - t + (dh >> 1); PINS(rd); }
            if constexpr (i == 21) { lost = (u32)(sc - L) < NSEG - 1 - L ? CH_RTY : 0u; PINS(lost); nt += ok[L] != 1u ? lost : 0u; PINS(nt); }
            if constexpr (i == 22) { nt = t + (u32)max((int)nt, (int)rd); PINS(nt); }
            if constexpr (i == 23) { hw.x = nt; hw.y = dh; PIN(hw.x); PIN(hw.y); }
            if constexpr (i == 24) { hw.z = nt + dn; hw.w = tm0; PIN(hw); }
            if constexpr (i == 25) st16(rs, hso + L * MB, hw);
        };
        {
#define WT(k) do {} while (0)
            v4u d[8];
            f32x4 acc[5] = {};
            done = uu(pst) ^ 1u; PINS(done);
            za = pmp | pst << 31;
            *(LAS u32*)(sw + L * 4096) = vo >> 31;
            mp = mpo + (L * MB + (u32)((sc + 1) & 3) * 65536u); pmp = mpo + (L * MB + (u32)(sc & 3) * 65536u);
            asm volatile("" : "+v"(acc[0]), "+v"(acc[1]), "+v"(acc[2]), "+v"(acc[3]), "+v"(acc[4]), "+v"(mp), "+v"(pmp), "+v"(za));
            {
                u32 tmp;
                asm volatile(WAIT_TXT("%1", "%2") "buffer_load_dwordx4 %0, %3, %4, %5 offen sc1\n\ts_waitcnt lgkmcnt(0)"
                             : "=&v"(d[0]), "=&s"(tmp) : "s"(uu(t - 4u)), "v"(vo), "s"(srd), "s"(oo[0]) : "vcc", "scc", "memory");
                PINS(clk);
                WT(0);
            }
            SFOR(i, 7)
                asm volatile("buffer_load_dwordx4 %0, %1, %2, %3 offen sc1" : "=v"(d[i + 1]) : "v"(vo), "s"(srd), "s"(oo[i + 1]) : "memory");
                SCHED();
                static_for<(25 * i) / 7, (25 * (i + 1)) / 7>(books);
                SCHED();
            SEND
            WT(1);
            float f0[5], f1[5]; unsigned long long bad;
#define WSW(o_) asm volatile("v_permlane32_swap_b32 %0, %1" : "+v"(f0[o_]), "+v"(f1[o_]))
#define WAD(o_) asm volatile("v_add_f32_dpp %0, %1, %0 row_ror:8 row_mask:0xf bank_mask:0xf" : "+v"(f0[o_]) : "v"(f1[o_]))
#define WFO(o_) do { VADD(f0[o_], acc[o_][0], acc[o_][2]); VADD(f1[o_], acc[o_][1], acc[o_][3]); } while (0)
#define WWR(o_) *(LAS float*)(xw + L * 4096 + (o_) * 128) = f0[o_]
            u32 th, per, so;
            auto next = [&](auto ic_) __attribute__((always_inline)) {
                constexpr int i = decltype(ic_)::value;
                if constexpr (i == 0) { so = (u32)(sn[M] & 3) * 0x42000u; PINS(so); }
                if constexpr (i == 1) { so = (so & 0xC4000u) ^ (pol0 + M * MB); PINS(so); }
                if constexpr (i == 2) { so = sn[M] + 1 < NSEG ? so : OOB; PINS(so); }
                if constexpr (i == 4) { vo = l16 + so; PIN(vo); }
                if constexpr (i == 5 && !L) { fin = (u32)(sn[0] + sn[1] + 2); PINS(fin); }
                if constexpr (i == 6 && L == 0) { fin = (fin | uu(left)) >> 7; PIN(fin); }
                if constexpr (i == 6 && L == 1) { cnt += 1u; PIN(cnt); left = cnt >> 9; PIN(left); }
                if constexpr (i == 7) { PIN(hv); th = rdl(hv, 0); PINS(th); }
                if constexpr (i == 8) { per = rdl(hv, 1); PINS(per); so = th - t; PINS(so); }
                if constexpr (i == 9) { per = (int)so < 20 ? per : 0u; PINS(per); }
                if constexpr (i == 10) { th += per; PINS(th); if constexpr (!L) { per = rdl(hv, 3); PINS(per); } }
                if constexpr (i == 11) { tp = t; t = lead ? tn : th; PINS(t); }
                if constexpr (i == 12) { if constexpr (!L) { fin |= per; PIN(fin); } tn = nt; }
            };
            auto slot = [&](auto ic_) __attribute__((always_inline)) {
                constexpr int i = decltype(ic_)::value;
                constexpr int NL = WO_NL, I0 = 40 - 5 * NL, pc = i < I0 ? i / 5 : 8 - NL + (i - I0) % NL, oc = i < I0 ? i % 5 : (i - I0) / NL;
                f32x4& ac = acc[oc]; v4u& dp = d[pc]; v8u& wp = wo[pc][oc];
                if constexpr (oc == 0) {
                    if constexpr (pc * 5 < 32) asm volatile("s_waitcnt vmcnt(%4)\n\tv_smfmac_f32_16x16x64_bf16 %0, %1, %2, %3" : "+v"(ac), "+v"(dp) : "a"(wp), "v"(idx), "i"(pc < 2 ? 7 - pc : 9 - pc));
                    else asm volatile("s_waitcnt vmcnt(%4)\n\tv_smfmac_f32_16x16x64_bf16 %0, %1, %2, %3" : "+v"(ac), "+v"(dp) : "v"(wp), "v"(idx), "i"(9 - pc));
                } else if constexpr (pc * 5 + oc < 32) SMF_A(ac, dp, wp); else SMF_V(ac, dp, wp);
                SCHED();
                if constexpr (i == 0) WT(2);
                if constexpr (i < 25 && oc == 0) LGK(15);
                if constexpr (i == 6) { u32& h_ = hv; (void)&srd; LD4A(h_, hvo[L], 0); }
                if constexpr (i == 8) books(IC<25>{});
                if constexpr (i >= 11 && i < 19 && i != 15) next(IC<i - 11 - (i > 15)>{});
                if constexpr (i == 20) WT(4);
                constexpr int e = i - (I0 + NL + 1), o = e < 0 ? 0 : e / 3;
                if constexpr (e >= 0 && e % 3 == 0) { CADD(f0[o], acc[o][0], acc[o][2]); if constexpr (o > 0) WAD(o - 1); }
                if constexpr (e >= 0 && e % 3 == 1) { if constexpr (o > 0) { WWR(o - 1); SCHED(); } CADD(f1[o], acc[o][1], acc[o][3]); }
                if constexpr (e >= 0 && e % 3 == 2) {
                    if constexpr (o == 0) asm volatile("v_cmp_u_f32 %0, %1, %2" : "=s"(bad) : "v"(f0[0]), "v"(f1[0]));
                    else LGK(15);
                    WSW(o);
                }
                SCHED();
            };
            WT(3);
            static_for<0, 40>(slot);
            WT(5);
            static_assert(WO_NL == 3, "tail");
            if (__builtin_expect(bad != 0ull, 0)) {
                bool miss = false;
#pragma unroll
                for (int i = 0; i < 8; ++i) miss |= d[i].x == PSENT;
                if (all_(!miss)) {
#pragma unroll
                    for (int i = 0; i < 8; ++i) miss |= d[i].y == PSENT || d[i].z == PSENT || d[i].w == PSENT;
                }
                if (miss) *(LAS u32*)(sw + L * 4096) = 1u;
            }
            asm volatile("v_permlane32_swap_b32 %0, %1\n\ts_nop 1\n\tv_add_f32_dpp %0, %1, %0 row_ror:8 row_mask:0xf bank_mask:0xf\n\tv_add_f32 %2, %4, %6\n\tv_add_f32 %3, %5, %7"
                         : "+v"(f0[3]), "+v"(f1[3]), "=&v"(f0[4]), "=&v"(f1[4]) : "v"(acc[4][0]), "v"(acc[4][1]), "v"(acc[4][2]), "v"(acc[4][3]));
            WWR(3); SCHED();
            asm volatile("s_nop 0\n\tv_permlane32_swap_b32 %0, %1\n\ts_nop 1\n\tv_add_f32_dpp %0, %1, %0 row_ror:8 row_mask:0xf bank_mask:0xf" : "+v"(f0[4]), "+v"(f1[4]));
            WWR(4);
            WT(6);
            f32x2 a, b, c, e; u32 mq; v4u sv;
            asm volatile("s_waitcnt lgkmcnt(0)\n\ts_barrier\n\tds_read_b128 %4, %6 offset:%7+2560\n\t"
                         "ds_read_b64 %0, %5 offset:%7\n\tds_read_b64 %1, %5 offset:%7+640\n\tds_read_b64 %2, %5 offset:%7+1280\n\tds_read_b64 %3, %5 offset:%7+1920"
                         : "=&v"(a), "=&v"(b), "=&v"(c), "=&v"(e), "=&v"(sv) : "v"(xr), "v"(zero), "i"(L * 4096) : "memory");
            asm volatile("s_waitcnt lgkmcnt(4)\n\tv_or_b32 %4, %6, %7\n\tv_or3_b32 %4, %4, %8, %9\n\tv_lshl_or_b32 %5, %4, 31, %10\n\t"
                         "s_waitcnt lgkmcnt(2)\n\tv_pk_add_f32 %0, %0, %1\n\ts_waitcnt lgkmcnt(0)\n\tv_pk_add_f32 %2, %2, %3\n\ts_nop 0\n\tv_pk_add_f32 %0, %0, %2"
                         : "+v"(a), "+v"(b), "+v"(c), "+v"(e), "=&v"(pst), "=&v"(mq) : "v"(sv.x), "v"(sv.y), "v"(sv.z), "v"(sv.w), "v"(mp) : "memory");
            st4s(rs, mq, 0, pkv(a));
            SCHED();
            asm volatile("s_memrealtime %0" : "=s"(clk));
            WT(7);
            st4s(rs, za, 0, psent);
            VMW(3); SCHED();
            static_for<7, 13>(next);
        }
    };
    do {
        turn(IC<1>{});
        turn(IC<0>{});
    } while (uu(fin) == 0u);
    if (sn[0] + 1 < NSEG || sn[1] + 1 < NSEG) st4s(rs, tid == 0 ? foff(F_ERR) : OOB, 0, 1u);
    if (w == 0) LST(1900 + j, rt32());
    LDUMP(w == 0, 224 + j);
}

__global__ void __launch_bounds__(256, 1) k_main(Params p) {
    lptr lds = (lptr)(u32)16 - 16;
    if (blockIdx.x == 0) LST(256, rt32());
    unsigned long long pc; int len;
    asm volatile("s_getpc_b64 %0\n.Lk_main_here:\n\ts_mov_b32 %1, .Lk_end_here-.Lk_main_here" : "=s"(pc), "=s"(len));
    const rsrc_t rs = mk_rsrc(p.ws, W_END);
    const v2u e0 = ld8s(rs, threadIdx.x == 0 ? foff(F_EPOCH) : OOB, 0);
    const v4u code = __builtin_amdgcn_raw_buffer_load_b128(mk_rsrc((const void*)(pc & ~15ull), (u32)max(len, 0)), (((blockIdx.x >> 3) & 31u) * 256u + threadIdx.x) * 16u, 0, 0);
    CFENCE();
    auto conv_id = [](int role) {
        const int x = role >> 5, i = role & 31, no = x < 5 ? 5 : 10, og = x < 5 ? x - 3 : 2 * (x - 4) + (i >= 5), r = (x - 3) * 5 + i - no, q = r / 3;
        return x < 3 || i >= no + 5 || r >= 24 ? NCV : i < no ? ((og & 1) ? (og >> 1) * 5 + i % 5 : NCV) : r % 3 == 0 ? 20 + (q & 1) * 20 + (q >> 1) : 16 + (r % 3) * 8 + q;
    };
    int role = (int)(((u32)__builtin_amdgcn_s_getreg(20 | (3 << 11)) & 7u) * 32u + (blockIdx.x >> 3));
    Early e;
    v4u rv;
#pragma unroll 1
    for (int n = 0;; ++n) {
        conv_early(p, rs, conv_id(role), e);
        if (n) break;
        CFENCE();
        if (threadIdx.x == 0) {
            GAS unsigned long long* slots = (GAS unsigned long long*)(p.ws + W_CTRL + F_SLOT * 128);
            const unsigned long long tag = (unsigned long long)e0.y << 32 | (e0.x + 1u);
            u32 mine = (u32)role;
#pragma unroll 1
            for (int k = 0; k < 256 && __hip_atomic_exchange(slots + mine * 16, tag, __ATOMIC_RELAXED, __HIP_MEMORY_SCOPE_AGENT) == tag; ++k) mine = (mine + 1u) & 255u;
            lds_w16(lds, (v4u){mine, e0.x, e0.y, 0u});
        }
        BAR();
        rv = lds_r16(lds);
        BAR();
        if (uni((int)rv.x) == role) break;
        role = uni((int)rv.x);
    }
    const int x = role >> 5, i = role & 31, w = uni(threadIdx.x >> 6), no = x < 5 ? 5 : 10, nr = x < 7 ? 5 : 4, j = conv_id(role);
    const v2u ep = {uu(rv.y), uu(rv.z)};
    asm volatile("" :: "v"(code));
    if (x == 1) { role_wo(p, lds, i, ep); __builtin_amdgcn_endpgm(); }
    if (x < 3) { role_head(p, lds, i >> 4, x * 8 + (i & 15), ep); __builtin_amdgcn_endpgm(); }
    if (i >= no + nr) { role_qkv(p, lds, (x < 5 ? (x - 3) * 22 : 44 + (x - 5) * 17) + i - no - nr, ep); __builtin_amdgcn_endpgm(); }
    if (j < NCV) conv_stage(p, rs, lds, j, ep, e);
    if (i >= no) {
        if (j >= 24 && j < NPRE) role_row(p, lds, (j - 24) * 4 + w, ep);
        __builtin_amdgcn_endpgm();
    }
    const int og = x < 5 ? x - 3 : 2 * (x - 4) + (i >= 5);
    role_out(p, lds, og & 1, og >> 1, i % 5, ep);
}
__global__ void k_end() { asm volatile(".Lk_end_here:"); }

extern "C" void kernel_launch(void* const* d_in, const int* in_sizes, int n_in, void* d_out, int out_size, void* d_ws, size_t ws_size,
                              hipStream_t stream) {
    Params p;
    p.x = (const u16*)d_in[0]; p.mem0 = (const u16*)d_in[1]; p.Wq = (const u16*)d_in[2]; p.Wk = (const u16*)d_in[3];
    p.Wv = (const u16*)d_in[4]; p.Wo = (const u16*)d_in[5];
    p.bk = (const u16*)d_in[8]; p.bv = (const u16*)d_in[9]; p.shift = (const u16*)d_in[10]; p.ssmk = (const u16*)d_in[11]; p.ssmD = (const u16*)d_in[12];
    p.out = (u16*)d_out;
    p.ws = (unsigned char*)(((size_t)d_ws + 2 * MB - 1) & ~(size_t)(2 * MB - 1));
    (void)hipFuncSetAttribute((const void*)k_main, hipFuncAttributeMaxDynamicSharedMemorySize, LDS_BYTES);
    hipLaunchKernelGGL(k_main, dim3(256), dim3(256), LDS_BYTES, stream, p);
}
```

```cpp
#include <hip/hip_runtime.h>
#include <hip/hip_bf16.h>

typedef unsigned short u16;
typedef unsigned int u32;
typedef u32 v2u __attribute__((ext_vector_type(2)));
typedef u32 v4u __attribute__((ext_vector_type(4)));
typedef u32 v8u __attribute__((ext_vector_type(8)));
typedef float f32x2 __attribute__((ext_vector_type(2)));
typedef float f32x4 __attribute__((ext_vector_type(4)));
typedef float f32x16 __attribute__((ext_vector_type(16)));
typedef __bf16 bf16x2 __attribute__((ext_vector_type(2)));
typedef __bf16 bf16x8 __attribute__((ext_vector_type(8)));
typedef short s16x4 __attribute__((ext_vector_type(4)));
#define LAS __attribute__((address_space(3)))
#define GAS __attribute__((address_space(1)))
typedef LAS unsigned char* lptr;
#define DEV __device__ __forceinline__

constexpr int SEQ = 2048, DM = 640, NH = 32, DH = 128, NSEG = 64, NQ = NH * DH;

constexpr u32 MB = 1u << 20;
constexpr u32 W_MEM0 = 2 * MB;
constexpr u32 W_CTRL = 4 * MB;
constexpr u32 CTRL_BYTES = 128 * 1024;
constexpr u32 ACT = SEQ * DM * 2;
constexpr u32 W_H1 = 5 * MB, W_Y = W_H1 + ACT, W_H2 = W_Y + ACT, W_X2 = W_H2 + ACT;
constexpr u32 W_KQ = 16 * MB;
constexpr u32 W_VO = W_KQ + 64 * MB;
constexpr u32 W_P = W_VO + 64 * MB;
constexpr u32 P_ITEM = 16 * 4 * DM * 4;
constexpr u32 W_KVT = W_P + 4 * NSEG * P_ITEM;
constexpr u32 W_SP = W_KVT + ACT;
constexpr u32 W_END = W_KVT + 3 * MB;
static_assert((size_t)W_END + 2 * MB <= 256ull * MB, "workspace");
constexpr u32 F_ERR = 0, F_QKV = 16, F_O = F_QKV + 192, F_P = F_O + 64, F_H2 = F_P + 64, F_CLR = F_H2 + 32,
              F_EPOCH = F_CLR + 32, F_SLOT = F_EPOCH + 1, F_PRE = F_SLOT + 256, F_N = F_PRE + 288;
constexpr int NCV = 44, NPRE = 40;
static_assert(F_N * 128 <= CTRL_BYTES, "ctrl");
DEV u32 foff(u32 idx) { return W_CTRL + idx * 128u; }

constexpr u32 OOB = 0x80000000u;
constexpr u32 PSENT = 0x7F817F81u;
constexpr int SPIN_CAP = 40000;
constexpr int LDS_BYTES = 160 * 1024;

#define LDUMP(cond, id) do {} while (0)
#define LMX(cond, k, s_, v) do {} while (0)
#define LS3(cond, slot, idx) do {} while (0)
#define LST(idx, v) do {} while (0)
#define LS2(cond, idx) do {} while (0)
struct Params {
    const u16 *x, *mem0, *Wq, *Wk, *Wv, *Wo, *bk, *bv, *shift, *ssmk, *ssmD;
    u16* out;
    unsigned char* ws;
};

#define INAUX 2
#define INNT "nt"
typedef __amdgpu_buffer_rsrc_t rsrc_t;
DEV rsrc_t mk_rsrc(const void* p, u32 bytes) { return __builtin_amdgcn_make_buffer_rsrc((void*)p, 0, bytes, 0x00020000); }
DEV v4u ld16s(rsrc_t r, u32 off, u32 so) { return __builtin_amdgcn_raw_buffer_load_b128(r, off, so, 16); }
DEV v2u ld8s(rsrc_t r, u32 off, u32 so) { return __builtin_amdgcn_raw_buffer_load_b64(r, off, so, 16); }
DEV v4u ldin16(rsrc_t r, u32 off, u32 so) { return __builtin_amdgcn_raw_buffer_load_b128(r, off, so, INAUX); }
DEV v2u ldin8(rsrc_t r, u32 off, u32 so) { return __builtin_amdgcn_raw_buffer_load_b64(r, off, so, INAUX); }
DEV u32 ld4(rsrc_t r, u32 off) { return __builtin_amdgcn_raw_buffer_load_b32(r, off, 0, 16); }
DEV void st16(rsrc_t r, u32 off, v4u v) { __builtin_amdgcn_raw_buffer_store_b128(v, r, off, 0, 16); }
DEV void st8s(rsrc_t r, u32 off, u32 so, v2u v) { __builtin_amdgcn_raw_buffer_store_b64(v, r, off, so, 16); }
DEV void st4s(rsrc_t r, u32 off, u32 so, u32 v) { __builtin_amdgcn_raw_buffer_store_b32(v, r, off, so, 16); }
#define DRAIN() asm volatile("s_waitcnt vmcnt(0)" ::: "memory")
#define VMW(n) asm volatile("s_waitcnt vmcnt(%0)" :: "i"(n) : "memory")
#define BAR() asm volatile("s_waitcnt lgkmcnt(0)\n\ts_barrier" ::: "memory")
#define SCHED() __builtin_amdgcn_sched_barrier(0)
#define CFENCE() asm volatile("" ::: "memory")
#define NOPS(n) asm volatile("s_nop %0" :: "i"(n))
#define PIN(x) asm volatile("" : "+v"(x))
#define PINS(x) asm volatile("" : "+s"(x))

template <int V> struct IC { static constexpr int value = V; };
template <int I, int N, class F> DEV void static_for(F&& f) { if constexpr (I < N) { f(IC<I>{}); static_for<I + 1, N>(f); } }
#define SFOR(var, n) static_for<0, n>([&](auto ic_) __attribute__((always_inline)) { constexpr int var = decltype(ic_)::value;
#define SEND });

DEV u32 pk(float a, float b) { bf16x2 v = {(__bf16)a, (__bf16)b}; return __builtin_bit_cast(u32, v); }
DEV float lo_f(u32 u) { return __builtin_bit_cast(float, u << 16); }
DEV float hi_f(u32 u) { return __builtin_bit_cast(float, u & 0xffff0000u); }
DEV f32x2 up2(u32 u) { return (f32x2){lo_f(u), hi_f(u)}; }
DEV u32 pk2(f32x2 v) { return pk(v.x, v.y); }
DEV float rbf(float a) { return (float)(__bf16)a; }
DEV v4u lds_r16(lptr p) { return *(LAS v4u*)p; }
DEV void lds_w16(lptr p, v4u v) { *(LAS v4u*)p = v; }
DEV v2u lds_tr(lptr p) { return __builtin_bit_cast(v2u, __builtin_amdgcn_ds_read_tr16_b64_v4i16((LAS s16x4*)p)); }
DEV v4u tr2(lptr p0, lptr p1) { const v2u a = lds_tr(p0), b = lds_tr(p1); return (v4u){a.x, a.y, b.x, b.y}; }
DEV v8u cat8(v4u a, v4u b) { return __builtin_shufflevector(a, b, 0, 1, 2, 3, 4, 5, 6, 7); }
DEV f32x4 mfma16(v4u a, v4u b, f32x4 c) { return __builtin_amdgcn_mfma_f32_16x16x32_bf16(__builtin_bit_cast(bf16x8, a), __builtin_bit_cast(bf16x8, b), c, 0, 0, 0); }
DEV f32x16 mfma32(v4u a, v4u b, f32x16 c) { return __builtin_amdgcn_mfma_f32_32x32x16_bf16(__builtin_bit_cast(bf16x8, a), __builtin_bit_cast(bf16x8, b), c, 0, 0, 0); }

DEV float f_(u32 u) { return __builtin_bit_cast(float, u); }
DEV u32 u_(float f) { return __builtin_bit_cast(u32, f); }
template <int CTRL> DEV float dpp(float v) { return f_((u32)__builtin_amdgcn_update_dpp(0, (int)u_(v), CTRL, 0xf, 0xf, false)); }
DEV float max16(float v) { auto r = __builtin_amdgcn_permlane16_swap(u_(v), u_(v), false, false); return fmaxf(f_(r[0]), f_(r[1])); }
DEV float max32(float v) { auto r = __builtin_amdgcn_permlane32_swap(u_(v), u_(v), false, false); return fmaxf(f_(r[0]), f_(r[1])); }
DEV float add16(float v) { auto r = __builtin_amdgcn_permlane16_swap(u_(v), u_(v), false, false); return f_(r[0]) + f_(r[1]); }
DEV float add32(float v) { auto r = __builtin_amdgcn_permlane32_swap(u_(v), u_(v), false, false); return f_(r[0]) + f_(r[1]); }
DEV float wave_sum(float v) {
    v += dpp<0xB1>(v);
    v += dpp<0x4E>(v);
    v += dpp<0x124>(v);
    v += dpp<0x128>(v);
    return add32(add16(v));
}
DEV bool all_(bool c) { return __builtin_amdgcn_ballot_w64(!c) == 0ull; }
DEV int uni(int v) { return __builtin_amdgcn_readfirstlane(v); }
DEV u32 uu(u32 v) { return (u32)__builtin_amdgcn_readfirstlane((int)v); }
DEV u32 rdl(u32 v, int l) { return (u32)__builtin_amdgcn_readlane((int)v, l); }
DEV u32 rt32() { return (u32)__builtin_amdgcn_s_memrealtime(); }

#define WSL 2
DEV void wait_flags(rsrc_t rs, u32 off, u32 want, bool& dead) {
    if (dead) return;
#pragma unroll 1
    for (int n = 0;; ++n) {
        CFENCE();
        const u32 v = ld4(rs, off);
        if (all_(off >= OOB || v >= want)) break;
        __builtin_amdgcn_s_sleep(WSL);
        if ((n & 63) == 63) { if (uni((int)ld4(rs, foff(F_ERR))) != 0) { dead = true; break; } }
        if (n > SPIN_CAP) { st4s(rs, foff(F_ERR), 0, 1u); dead = true; break; }
    }
    CFENCE();
}

DEV void pre_post(rsrc_t rs, int idx, u32 stage, v2u ep) { st8s(rs, threadIdx.x == 0 ? foff(F_PRE + idx) : OOB, 0, (v2u){ep.x + stage, ep.y}); }
DEV void pre_poll(rsrc_t rs, int first, int n, u32 stage, v2u ep) {
    const int lane = threadIdx.x & 63;
#pragma unroll 1
    for (int it = 0; it < SPIN_CAP; ++it) {
        CFENCE();
        const v2u a = ld8s(rs, foff(F_PRE + first + min(lane, n - 1)), 0), b = ld8s(rs, foff(F_PRE + first + min(lane + 64, n - 1)), 0);
        if (all_(a.y == ep.y && b.y == ep.y && max(a.x - ep.x - stage, b.x - ep.x - stage) < 8u - stage)) break;
        __builtin_amdgcn_s_sleep(2);
    }
    CFENCE();
}
DEV void pre_wait(rsrc_t rs, int first, int n, u32 stage, v2u ep) {
    if (uni(threadIdx.x >> 6) == 0) pre_poll(rs, first, n, stage, ep);
    BAR();
}
#define POLX 0u
DEV u32 pol_wait(rsrc_t rs, v2u ep) {
    const int lane = threadIdx.x & 63;
    v4u a;
#pragma unroll 1
    for (int it = 0; it < SPIN_CAP; ++it) {
        CFENCE();
        a = ld16s(rs, foff(F_PRE + (lane < NPRE || lane >= 48 ? lane : 0)), 0);
        const v2u b = ld8s(rs, foff(F_PRE + 256 + min(lane, 19)), 0);
        if (all_(a.y == ep.y && b.y == ep.y && max(a.x - ep.x - 1u, b.x - ep.x - 1u) < 7u)) break;
        __builtin_amdgcn_s_sleep(2);
    }
    CFENCE();
    return uu((wave_sum(lane >= 48 ? (float)(int)a.z : 0.f) >= 0.f ? 8192u : 0u) + (wave_sum(lane >= 48 ? (float)(int)a.w : 0.f) >= 0.f ? 16384u : 0u)) ^ POLX;
}
#define PROBE_C 7
#define PROBE_T 620
DEV v2u pol_probe(rsrc_t rs, int id) {
    const int lane = threadIdx.x & 63;
    const u32 off0 = (u32)id * 32768u, tend = rt32() + PROBE_T;
    st4s(rs, lane < 32 ? off0 + (u32)((lane >> 3) * 8192 + (lane & 7) * 256) : OOB, 0, PSENT); DRAIN();
    int d = 1 << 16, calm = 0;
#pragma unroll 1
    for (int q = 0; q < 32 && ((q & 3) || (calm < PROBE_C && (int)(rt32() - tend) < 0)); ++q) {
        const int kind = (q ^ (q >> 2) ^ id) & 3;
        const unsigned long long t0 = __builtin_amdgcn_s_memtime();
        const u32 v = ld4(rs, lane == 0 ? off0 + (u32)(kind * 8192 + (q >> 2) * 256) : OOB);
        asm volatile("s_waitcnt vmcnt(0)" :: "v"(v) : "memory");
        const int dt = (int)(__builtin_amdgcn_s_memtime() - t0);
        d = lane == kind ? min(d, dt) : d;
        calm += dt < 1100;
    }
    const int d0 = (int)rdl(d, 0), d1 = (int)rdl(d, 1), d2 = (int)rdl(d, 2), d3 = (int)rdl(d, 3), a = min(d0, d2) - min(d1, d3);
    const int b0 = min(max(d0 - d2, -80), 80), b1 = min(max(d1 - d3, -80), 80);
    const u32 xcc = (u32)__builtin_amdgcn_s_getreg(20 | (3 << 11));
    return (v2u){(u32)min(max((xcc & 4u) ? -a : a, -250), 250), (u32)((xcc & 1u) ? b0 + b1 : a > 0 ? b0 - b1 : b1 - b0)};
}
DEV f32x2 ln_stats(const f32x2 (&x)[5]) {
    const float c = f_(uu(u_(x[0].x)));
    f32x2 d[5];
#pragma unroll
    for (int i = 0; i < 5; ++i) d[i] = x[i] - c;
    const f32x2 s2 = (d[0] + d[1]) + (d[2] + d[3]) + d[4], q2 = (d[0] * d[0] + d[1] * d[1]) + (d[2] * d[2] + d[3] * d[3] + d[4] * d[4]);
    float a = s2.x + s2.y, b = q2.x + q2.y, a2, b2;
#define LN_DPP(m) "v_add_f32_dpp %0, %0, %0 " m " row_mask:0xf bank_mask:0xf\n\tv_add_f32_dpp %1, %1, %1 " m " row_mask:0xf bank_mask:0xf\n\ts_nop 0\n\t"
    asm("s_nop 1\n\t" LN_DPP("quad_perm:[1,0,3,2]") LN_DPP("quad_perm:[2,3,0,1]") LN_DPP("row_ror:4")
        "v_add_f32_dpp %0, %0, %0 row_ror:8 row_mask:0xf bank_mask:0xf\n\tv_add_f32_dpp %1, %1, %1 row_ror:8 row_mask:0xf bank_mask:0xf\n\ts_nop 1\n\t"
        "v_permlane16_swap_b32 %0, %1\n\tv_add_f32 %0, %0, %1\n\tv_mov_b32 %1, %0\n\ts_nop 1\n\tv_permlane32_swap_b32 %0, %1\n\tv_add_f32 %0, %0, %1\n\ts_nop 0\n\t"
        "v_readlane_b32 %2, %0, 0\n\tv_readlane_b32 %3, %0, 16\n\ts_nop 1"
        : "+v"(a), "+v"(b), "=s"(a2), "=s"(b2));
    const float m = a2 * (1.0f / DM), var = fmaxf(b2 * (1.0f / DM) - m * m, 0.f);
    const float sd = rbf(__builtin_amdgcn_sqrtf(rbf(rbf(var) + 1e-5f)));
    return (f32x2){rbf(c + m), __builtin_amdgcn_rcpf(sd)};
}
DEV u32 pkv(f32x2 v) { u32 d; asm("v_cvt_pk_bf16_f32 %0, %1, %2" : "=v"(d) : "v"(v.x), "v"(v.y)); return d; }
DEV void store_h(rsrc_t rs, u32 base, int row, const u32 (&hp)[5]) {
    const u32 lane = threadIdx.x & 63;
    const u32 sw = (u32)(row & 15) * 16u, o8 = (lane >> 5) * 8192u + (((lane & 31u) * 8u) ^ sw), hb = base + (u32)(row >> 5) * (32 * DM * 2) + (u32)(row & 31) * 256u;
    st8s(rs, o8, hb, (v2u){hp[0], hp[1]});
    st8s(rs, o8, hb + 16384, (v2u){hp[2], hp[3]});
    st4s(rs, (lane * 4) ^ sw, hb + 32768, hp[4]);
}

#define DMA16(ldsb, vo, so) asm volatile("s_mov_b32 m0, %0\n\ts_nop 0\n\tbuffer_load_dwordx4 %1, %2, %3 offen sc1 lds" :: "s"(ldsb), "v"(vo), "s"(srd), "s"(so) : "memory")
#define DMA16I(ldsb, lo, vo, so, go) do { u32 so_; asm volatile("s_add_u32 m0, %1, %5\n\ts_add_u32 %0, %4, %6\n\tbuffer_load_dwordx4 %2, %3, %0 offen sc1 lds" \
                         : "=&s"(so_) : "s"(ldsb), "v"(vo), "s"(srd), "s"(so), "i"(lo), "i"(go) : "memory", "scc"); } while (0)
#define DMA32(ldsb, vo, so) asm volatile("s_mov_b32 m0, %0\n\ts_nop 0\n\tbuffer_load_dwordx4 %1, %2, %3 offen sc1 lds\n\tbuffer_load_dwordx4 %1, %2, %3 offen offset:1024 sc1 lds" \
                         :: "s"(ldsb), "v"(vo), "s"(srd), "s"(so) : "memory")
#define DMAIN(sr, ldsb, vo, so) asm volatile("s_mov_b32 m0, %0\n\ts_nop 0\n\tbuffer_load_dwordx4 %1, %2, %3 offen lds" :: "s"(ldsb), "v"(vo), "s"(sr), "s"(so) : "memory")
DEV v4u mk_srd(const void* q, u32 bytes) { return (v4u){uu((u32)(size_t)q), uu((u32)((size_t)q >> 32) & 0xffffu), bytes, 0x00020000u}; }
#define DMANT(sr, ldsb, vo, so) asm volatile("s_mov_b32 m0, %0\n\ts_nop 0\n\tbuffer_load_dwordx4 %1, %2, %3 offen " INNT " lds" :: "s"(ldsb), "v"(vo), "s"(sr), "s"(so) : "memory")
#define AHEAD(sr, vo, so) asm volatile("buffer_load_dwordx4 %0, %1, %2, %3 offen" : "+v"(junk) : "v"(vo), "s"(sr), "s"(so) : "memory")
#define MK_SRD() const v4u srd = mk_srd(p.ws, W_END)

constexpr int CV_W = 40960;
template <bool B> struct CvL {
    static constexpr int NF = B ? 5 : 3, NK = B ? 4 : 2, KP = 544 + NK * 256, CH = NF * 1024 + 4 * KP;
};
constexpr int CV_Y = 4 * CvL<false>::CH, CV_YP = 272;
static_assert(CV_Y + 32 * CV_YP <= CV_W && 2 * CvL<true>::CH <= CV_Y, "conv lds");
DEV int cv_chan(int u, int i) { return 16 * (u % 40) + 4 * i + u / 40; }
template <bool B> DEV void cv_filt(const Params& p, u32 dst, int d, int i) {
    const u32 lane = threadIdx.x & 63;
    const v4u sf = mk_srd(p.ssmk + d * SEQ, SEQ * 2);
    if (i == 0) DMANT(sf, dst, lane < 4u ? OOB : lane * 16u - 64u, 0);
    else DMANT(sf, uu(dst + i * 1024), i == CvL<B>::NF - 1 && lane >= 4u ? OOB : lane * 16u, i * 1024 - 64);
}
DEV v4u cv_kvld(rsrc_t rs, int d, int i) { return ld16s(rs, (threadIdx.x & 63) * 16u, uu(W_KVT + (u32)d * (SEQ * 2) + i * 1024)); }
template <bool B> DEV void cv_kvput(lptr F, const v4u* k) {
    const int lane = threadIdx.x & 63;
#pragma unroll
    for (int i = 0; i < CvL<B>::NK; ++i) lds_w16(F + CvL<B>::NF * 1024 + (lane & 3) * CvL<B>::KP + 512 + i * 256 + (lane >> 2) * 16, k[i]);
}
template <bool B> DEV void cv_zero(lptr F) {
    const int lane = threadIdx.x & 63;
#pragma unroll
    for (int i = 0; i < 4; ++i) lds_w16(F + (lane >> 5) * CvL<B>::CH + CvL<B>::NF * 1024 + i * CvL<B>::KP + (lane & 31) * 16, (v4u){0u, 0u, 0u, 0u});
}
#define M32V(d, a, b) asm volatile("v_mfma_f32_32x32x16_bf16 %0, %1, %2, %0" : "+v"(d) : "v"(a), "v"(b))
template <bool B, class G> DEV void cv_run(const Params& p, rsrc_t rs, lptr F, int d, u32 D, f32x4 sp, float (&y)[16], G&& gap) {
    constexpr int KP = CvL<B>::KP, FO = B ? 2048 : 0;
    const int lane = threadIdx.x & 63, n = lane & 31, hh = lane >> 5, c = lane & 3;
    lptr fa = F + 64 + (32 * hh + 16 * (c & 1) + 8 * ((lane >> 4) & 1) + 4 * (c >> 1) - (lane & 12)) * 2;
    lptr kl = F + CvL<B>::NF * 1024 + (32 + n - hh) * 16;
    v4u dg;
#pragma unroll
    for (int q = 0; q < 4; ++q) { const int k = (lane & 35) ? -1 : (n & 4) + (n >> 3); dg[q] = k == 2 * q ? D : k == 2 * q + 1 ? D << 16 : 0u; }
    v4u l[4], h[4]; v2u fc[4], fh[4];
    f32x16 acc[4];
#pragma unroll
    for (int j = 0; j < 4; ++j) { l[j] = lds_r16(kl + j * KP); if constexpr (B) h[j] = lds_r16(kl + j * KP + 512); }
#pragma unroll
    for (int j = 0; j < 2; ++j) { fc[j] = lds_tr(fa + FO - 32 * j); if constexpr (B) fh[j] = lds_tr(fa - 32 * j); }
    asm volatile("s_nop 1" : "+v"(dg));
#pragma unroll
    for (int j = 0; j < 4; ++j) asm volatile("v_mfma_f32_32x32x16_bf16 %0, %1, %2, 0" : "=&v"(acc[j]) : "v"(dg), "v"(B ? h[j] : l[j]));
    constexpr int U = B ? 2 : 4;
    kl -= 32 * U; fa -= 32;
    auto body = [&](bool first) __attribute__((always_inline)) {
#pragma unroll
        for (int z = 0; z < U; ++z) {
            const int c = 2 * (z & 1);
            const v4u f = __builtin_shufflevector(fc[c], fc[c + 1], 0, 1, 2, 3), g = __builtin_shufflevector(fh[c], fh[c + 1], 0, 1, 2, 3);
#pragma unroll
            for (int j = 0; j < 4; ++j) {
                M32V(acc[j], f, l[j]); l[j] = lds_r16(kl + j * KP + 32 * (U - z - 1));
                if (j < 2) fc[(c ^ 2) + j] = lds_tr(fa + FO + (z + 1) * 128 + 32 - 32 * j);
                if (first && j > 0) gap(5 * z + j - 1);
                SCHED();
            }
            if constexpr (B) {
#pragma unroll
                for (int j = 0; j < 4; ++j) {
                    M32V(acc[j], g, h[j]); h[j] = lds_r16(kl + j * KP + 512 + 32 * (U - z - 1));
                    if (j < 2) fh[(c ^ 2) + j] = lds_tr(fa + (z + 1) * 128 + 32 - 32 * j);
                    if (first && j > 1) gap(5 * z + j + 1);
                    SCHED();
                }
            }
        }
        kl -= 32 * U; fa += 128 * U;
    };
    if constexpr (B) body(true);
#pragma unroll 1
    for (int m = B ? 2 * U : 0; m < 32; m += 2 * U) body(false);
    asm volatile("s_nop 11" : "+v"(acc[0]), "+v"(acc[1]), "+v"(acc[2]), "+v"(acc[3]));
    const float o[3] = {acc[1][15] + acc[2][14] + acc[3][13], acc[2][15] + acc[3][14], acc[3][15]};
    if constexpr (!B) st16(rs, lane == 63 ? W_SP + (u32)d * 16u : OOB, (v4u){u_(o[0]), u_(o[1]), u_(o[2]), 0u});
#pragma unroll
    for (int i = 0; i < 16; ++i) {
        float v = acc[0][i];
#pragma unroll
        for (int j = 1; j < 4; ++j) if (j <= i) v += acc[j][i - j];
        if (i < 3) { const float e = f_((u32)__builtin_amdgcn_ds_bpermute((hh ? lane - 32 : lane + 31) * 4, (int)u_(o[i]))); v += lane == 0 ? sp[i] : e; }
        y[i] = v;
    }
}
DEV void cv_put(lptr F, int pr, const float (&y0)[16], const float (&y1)[16]) {
    const int lane = threadIdx.x & 63;
#pragma unroll
    for (int i = 0; i < 16; ++i) *(LAS u32*)(F + CV_Y + (lane & 31) * CV_YP + (lane >> 5) * 128 + i * 8 + pr * 4) = pk(y0[i], y1[i]);
}
DEV void cv_out(rsrc_t rs, lptr F, int u, int blk0) {
    const int lane = threadIdx.x & 63;
    lptr q = F + CV_Y + ((lane >> 2) & 3) * CV_YP + ((lane >> 4) * 4 + (lane & 3)) * 8;
    const u32 yo = W_Y + (u32)(((lane >> 2) * 8 + blk0) * DM + 4 * u + (lane & 3)) * 16u;
#pragma unroll
    for (int i = 0; i < 8; ++i) { lptr a = q + (i >> 1) * (8 * CV_YP) + (i & 1) * 128; st16(rs, yo + (u32)((i & 1) * 128 + (i >> 1)) * (DM * 16), tr2(a, a + 4 * CV_YP)); }
}
struct KvIn { v2u k[19], v[16]; v4u s[2]; };
template <class F> DEV void kvt_load(const Params& p, int tile, KvIn& in, F&& mid) {
    const int lane = threadIdx.x & 63;
    const rsrc_t rk = mk_rsrc(p.bk, ACT), rv = mk_rsrc(p.bv, ACT), rh = mk_rsrc(p.shift, DM * 8);
    const int row = (tile / 10) * 64 + (lane >> 4) * 16, col = (tile % 10) * 64 + (lane & 15) * 4;
    const u32 vo = (u32)(row * DM + col) * 2u, vp = row > 0 ? vo - 3 * (DM * 2) : OOB;
#pragma unroll
    for (int i = 0; i < 2; ++i) in.s[i] = ldin16(rh, (u32)col * 8u + i * 16, 0);
#pragma unroll
    for (int i = 0; i < 19; ++i) { if (i == 7) mid(); in.k[i] = i < 3 ? ldin8(rk, vp, i * (DM * 2)) : ldin8(rk, vo, (i - 3) * (DM * 2)); if (i >= 3) in.v[i - 3] = ldin8(rv, vo, (i - 3) * (DM * 2)); }
}
DEV void kvt_make(rsrc_t rs, lptr xt, int tile, const KvIn& in) {
    const int lane = threadIdx.x & 63;
#pragma unroll
    for (int c = 0; c < 2; ++c) {
        const v4u sv = in.s[c];
        const f32x2 sh[4] = {{lo_f(sv.x), lo_f(sv.z)}, {hi_f(sv.x), hi_f(sv.z)}, {lo_f(sv.y), lo_f(sv.w)}, {hi_f(sv.y), hi_f(sv.w)}};
        f32x2 kf[19];
#pragma unroll
        for (int i = 0; i < 19; ++i) kf[i] = up2(in.k[i][c]);
        u32 kv[16];
#pragma unroll
        for (int i = 0; i < 16; ++i) kv[i] = pk2(((kf[i] * sh[3] + kf[i + 1] * sh[2]) + (kf[i + 2] * sh[1] + kf[i + 3] * sh[0])) * up2(in.v[i][c]));
#pragma unroll
        for (int q = 0; q < 8; ++q) {
            const u32 sel = (q & 1) ? 0x07060302u : 0x05040100u; const u32* z = kv + (q >> 1);
            *(LAS v2u*)(xt + ((2 * c + (q & 1)) * 16 + (lane & 15)) * 144 + (lane >> 5) * 64 + (q >> 1) * 16 + ((lane >> 4) & 1) * 8) =
                (v2u){__builtin_amdgcn_perm(z[4], z[0], sel), __builtin_amdgcn_perm(z[12], z[8], sel)};
        }
    }
    const u32 o = W_KVT + (u32)(((tile % 10) * 64 + 4 * (lane >> 3)) * SEQ + (tile / 10) * 64 + (lane & 7) * 8) * 2u;
#pragma unroll
    for (int i = 0; i < 8; ++i) st16(rs, o + (u32)(32 * (i & 1) + (i >> 1)) * (SEQ * 2), lds_r16(xt + (8 * i + (lane >> 3)) * 144 + (lane & 7) * 16));
}
struct RowX { v2u a, b; u32 c; };
DEV void ln_row(rsrc_t rs, int row, const RowX& r) {
    const f32x2 x[5] = {up2(r.a.x), up2(r.a.y), up2(r.b.x), up2(r.b.y), up2(r.c)};
    const f32x2 st = ln_stats(x);
    u32 hp[5];
#pragma unroll
    for (int q = 0; q < 5; ++q) hp[q] = pk2(up2(pkv(x[q] - st.x)) * st.y);
    store_h(rs, W_H1, row, hp);
}
DEV v4u mem0_load(const Params& p, int j) {
    const u32 n = (u32)(j * 256 + threadIdx.x);
    return ldin16(mk_rsrc(p.mem0, 8 * DM * 2), n < 640u ? ((((n >> 1) & 4) + (n & 3)) * DM + (n >> 4) * 16 + (n & 4) * 2) * 2u : OOB, 0);
}
DEV void pre_flags(rsrc_t rs, int j, v4u m) {
    const u32 n = (u32)(j * 256 + threadIdx.x);
#pragma unroll
    for (int i = 0; i < 4; ++i) m[i] = m[i] == PSENT ? 0x7FC17FC1u : m[i];
    st4s(rs, n - 36u * 256u < F_CLR ? foff(n - 36u * 256u) : OOB, 0, 0u);
    st16(rs, n < 640u ? W_MEM0 + (n >> 9) * 32768u + (n & 511u) * 16u : OOB, m);
}
constexpr int KT0 = NPRE * 4, LN0 = NPRE * 8;
struct Early { RowX r[2]; v4u m; };
DEV void conv_early(const Params& p, rsrc_t rs, int j, Early& e) {
    const int lane = threadIdx.x & 63, w = uni(threadIdx.x >> 6), u = j * 4 + w;
    if (j < NPRE) {
        const rsrc_t rx = mk_rsrc(p.x, ACT);
#pragma unroll
        for (int i = 0; i < 2; ++i) {
            const u32 so = uu((u + i * KT0) * (DM * 2));
            e.r[i].a = __builtin_amdgcn_raw_buffer_load_b64(rx, lane * 8u, so, 0); e.r[i].b = __builtin_amdgcn_raw_buffer_load_b64(rx, lane * 8u + 512u, so, 0);
            e.r[i].c = __builtin_amdgcn_raw_buffer_load_b32(rx, lane * 4u + 1024u, so, 0);
        }
        e.m = mem0_load(p, j);
    }
}
DEV void conv_stage(const Params& p, rsrc_t rs, lptr lds, int j, v2u ep, Early& e) {
    const int tid = threadIdx.x, lane = tid & 63, w = uni(tid >> 6), u = j * 4 + w;
    lptr F = lds + w * CV_W;
    const u32 fb = uu(w * CV_W);
    const rsrc_t rd = mk_rsrc(p.ssmD, DM * 2);
    float y0[16], y1[16];
    KvIn in;
    if (j < NPRE) {
        constexpr int CH = CvL<false>::CH;
        if (w == 1) LST(1500 + j, rt32());
        pre_flags(rs, j, e.m);
        ln_row(rs, u, e.r[0]); ln_row(rs, u + KT0, e.r[1]);
        if (w == 1) LST(1550 + j, rt32());
        CFENCE();
        kvt_load(p, u, in, [&] { VMW(13); BAR(); pre_post(rs, j, 1u, ep); if (w == 0) LST(1450 + j, rt32()); });
        if (j == 20) LST(260, rt32());
        const u32 D = __builtin_amdgcn_raw_buffer_load_b16(rd, (u32)cv_chan(u, lane & 3) * 2u, 0, 0);
#pragma unroll 1
        for (int i = 0; i < 4; ++i) { const int d = cv_chan(u, i); cv_filt<false>(p, fb + i * CH, d, 0); cv_filt<false>(p, fb + i * CH, d, 1); cv_filt<false>(p, fb + i * CH, d, 2); }
        kvt_make(rs, F + CV_Y, u, in);
        cv_zero<false>(F); cv_zero<false>(F + 2 * CH);
        DRAIN(); BAR();
        pre_post(rs, j, 2u, ep);
        if (j == 20) LST(262, rt32());
        pre_wait(rs, 0, NPRE, 2u, ep);
        if (j == 20) LST(281, rt32());
        {
            v4u k[8];
#pragma unroll
            for (int i = 0; i < 8; ++i) k[i] = cv_kvld(rs, cv_chan(u, i >> 1), i & 1);
#pragma unroll
            for (int i = 0; i < 4; ++i) cv_kvput<false>(F + i * CH, k + 2 * i);
        }
        if (j == 20) LST(286, rt32());
#pragma unroll 1
        for (int i = 0; i < 4; i += 2) {
            cv_run<false>(p, rs, F + i * CH, cv_chan(u, i), rdl(D, i), f32x4{}, y0, [](int) {});
            cv_run<false>(p, rs, F + (i + 1) * CH, cv_chan(u, i + 1), rdl(D, i + 1), f32x4{}, y1, [](int) {});
            cv_put(F, i >> 1, y0, y1);
        }
        if (j == 20) LST(287, rt32());
        cv_out(rs, F, u, 0);
        if (j == 20) LST(263, rt32());
    } else {
        const v2u vote = pol_probe(rs, u - NPRE * 4);
        st16(rs, lane == 0 ? foff(F_PRE + 48 + u - NPRE * 4) : OOB, (v4u){ep.x + 1u, ep.y, vote.x, vote.y});
#pragma unroll 1
        for (int t = u + 128; t < 320; t += 16) { kvt_load(p, t, in, [] {}); kvt_make(rs, F, t, in); }
    }
    DRAIN(); BAR();
    pre_post(rs, j, 4u, ep);
    const int b = j < 20 ? -1 : j < 24 ? u - 80 : j >= NPRE ? u - 144 : (w & 1) ? (u >> 1) - 16 : -1, u0 = b < 32 ? b : b + 96, ue = b < 32 ? 128 : 160;
    if (b >= 0) {
        constexpr int CH = CvL<true>::CH;
        cv_zero<true>(F);
        pre_poll(rs, 64, 32, 4u, ep); pre_poll(rs, 0, NCV, 4u, ep);
        v4u k[4];
        auto fetch = [&](u32 dst, int d, int i) { if (i < 5) cv_filt<true>(p, dst, d, i); else if (i < 9) k[i - 5] = cv_kvld(rs, d, i - 5); };
#pragma unroll
        for (int i = 0; i < 9; ++i) fetch(fb, cv_chan(u0, 0), i);
        const u32 ch = (u32)cv_chan(min(u0 + 32 * (lane >> 2), 159), lane & 3);
        u32 D = __builtin_amdgcn_raw_buffer_load_b16(rd, ch * 2u, 0, 0);
        v4u sp = ld16s(rs, W_SP + ch * 16u, 0);
        asm volatile("" : "+v"(D), "+v"(sp));
#pragma unroll 1
        for (int un = u0, n = 0; un < ue; un += 32) {
            if (un == 64) { LST(282, rt32()); LST(288, __builtin_amdgcn_s_memtime()); }
#pragma unroll 1
            for (int i = 0; i < 4; i += 2, n += 2) {
                const int d1 = (int)rdl(ch, n + 1), d2 = (int)rdl(ch, n + 2);
                cv_kvput<true>(F, k);
                cv_run<true>(p, rs, F, 0, rdl(D, n), (f32x4){f_(rdl(sp.x, n)), f_(rdl(sp.y, n)), f_(rdl(sp.z, n)), 0.f}, y0, [&](int q) { fetch(fb + CH, d1, q); });
                cv_kvput<true>(F + CH, k);
                cv_run<true>(p, rs, F + CH, 326, rdl(D, n + 1), (f32x4){f_(rdl(sp.x, n + 1)), f_(rdl(sp.y, n + 1)), f_(rdl(sp.z, n + 1)), 0.f}, y1, [&](int q) { fetch(fb, d2, q); });
                cv_put(F, i >> 1, y0, y1);
            }
            cv_out(rs, F, un, 4);
            if (un == 64) { LST(283, rt32()); LST(289, __builtin_amdgcn_s_memtime()); }
        }
        DRAIN();
        st8s(rs, lane == 0 ? foff(F_PRE + 192 + b) : OOB, 0, (v2u){ep.x + 4u, ep.y});
    }
    if (j == 20) LST(284, rt32());
    if (b == 62) LST(285, rt32());
}

#define M32A(first, d, a, b) do { if (first) asm volatile("v_mfma_f32_32x32x16_bf16 %0, %1, %2, 0" : "=&v"(d) : "a"(a), "v"(b)); \
                                  else asm volatile("v_mfma_f32_32x32x16_bf16 %0, %1, %2, %0" : "+v"(d) : "a"(a), "v"(b)); } while (0)
#define M32B(first, d, a, b) do { if (first) asm volatile("v_mfma_f32_32x32x16_bf16 %0, %1, %2, 0" : "=&v"(d) : "v"(a), "a"(b)); \
                                  else asm volatile("v_mfma_f32_32x32x16_bf16 %0, %1, %2, %0" : "+v"(d) : "v"(a), "a"(b)); } while (0)
#define VCVT(d, a, b) asm volatile("v_cvt_pk_bf16_f32 %0, %1, %2" : "=v"(d) : "v"(a), "v"(b))
#define VADD(d, a, b) asm volatile("v_add_f32 %0, %1, %2" : "=v"(d) : "v"(a), "v"(b))
#define LD4A(d, vo, so) asm volatile("buffer_load_dword %0, %1, %2, %3 offen sc1" : "=v"(d) : "v"(vo), "s"(srd), "s"(so) : "memory")
DEV u32 wst_src(int lane, int ld) { return (u32)((lane >> 4) * ld + 8 * ((lane & 15) ^ ((lane >> 4) << 2))) * 2u; }
DEV u32 wst_rd(int lane, int w) { const int q4 = (lane >> 2) & 3; return (u32)(q4 * 256 + ((w ^ q4) * 64) + ((lane >> 4) & 1) * 32 + (lane & 3) * 8); }

constexpr int QK_SEL = 81920;
#define QK_LEAD 12
#define QK_SMP 29
#define QK_YB 26
#define QK_W0 2
DEV void role_qkv(const Params& p, lptr lds, int c, v2u ep) {
    const int tid = threadIdx.x, lane = tid & 63, w = uni(tid >> 6), hh = lane >> 5, r32 = lane & 31;
    const int m = c >> 5, h = c & 31;
    const rsrc_t rs = mk_rsrc(p.ws, W_END);
    v4u wt[40];
    {
        const v4u sw = mk_srd(m == 0 ? p.Wq : m == 1 ? p.Wk : p.Wv, DM * NQ * 2), sx = mk_srd(p.x, ACT);
        const u32 vo = wst_src(lane, NQ) + (u32)h * (DH * 2), x8 = (u32)lane * 8u, x4 = (u32)lane * 4u;
        RowX xr[5];
        auto wfill = [&](int i0, int n) {
#pragma unroll 1
            for (int i = i0; i < i0 + n; ++i) DMAIN(sw, uu((4 * i + w) * 1024), vo, uu((16 * i + 4 * w) * (NQ * 2)));
        };
        wfill(0, QK_W0);
#pragma unroll
        for (int i = 0; i < 5; ++i)
            asm volatile("buffer_load_dwordx2 %0, %3, %5, %6 offen\n\tbuffer_load_dwordx2 %1, %3, %5, %6 offen offset:512\n\tbuffer_load_dword %2, %4, %5, %6 offen offset:1024"
                         : "=&v"(xr[i].a), "=&v"(xr[i].b), "=&v"(xr[i].c) : "v"(x8), "v"(x4), "s"(sx), "s"(uu((LN0 + 4 * c + w + 384 * i) * (DM * 2))) : "memory");
        wfill(QK_W0, 10 - QK_W0);
        if (c == 3) LST(275, rt32());
        VMW(10 - QK_W0);
        if (c == 3) LST(276, rt32());
#pragma unroll
        for (int i = 0; i < 5; ++i) {
            asm volatile("" : "+v"(xr[i].a), "+v"(xr[i].b), "+v"(xr[i].c));
            const int row = LN0 + 4 * c + w + 384 * i;
            if (i < 4 || row < SEQ) ln_row(rs, row, xr[i]);
            wfill(10 + 6 * i, 6);
        }
        if (c == 3) LST(264, rt32());
        lptr tb = lds + hh * 2048 + wst_rd(lane, w);
        SFOR(k, 40)
            if constexpr (k == 0) { VMW(15); BAR(); }
            if constexpr (k == 28) {
                if (w == 0) pre_poll(rs, 0, NPRE, 1u, ep);
                DRAIN(); BAR();
                pre_post(rs, 96 + c, 1u, ep);
            }
            lptr q = tb + (k >> 3) * 32768 + (k & 7) * 4096; wt[k] = tr2(q, q + 1024); asm volatile("" : "+a"(wt[k]));
        SEND
    }
    if (c == 3) LST(268, rt32());
    if (w == 0) LST(1350 + c, rt32());
    bool dead = false;
    int n0, n1;
    const u32 t16 = (u32)(w * 2048 + lane * 16), wb = uu(w * 2048);
    u32 ar[16];
#pragma unroll
    for (int j = 0; j < 16; ++j) ar[j] = (u32)(r32 * 256 + ((2 * (j & 7) + hh) ^ (r32 & 15)) * 16 + (j >> 3) * 49152);
    MK_SRD();
#define XRD(y, ks) lds_r16(lds + ar[((ks) & 7) + ((ks) >= 24 ? 8 : 0)] + ((ks) >> 3) % 3 * 16384 + (y) * 8192)
#define QM0(y, b) asm volatile("s_add_u32 m0, %0, %1" :: "s"(wb), "i"((b) * 16384 + (y) * 8192) : "scc", "memory")
#define QDMA(i, vo) asm volatile("buffer_load_dwordx4 %0, %1, %2 offen offset:%3 sc1 lds" :: "v"(vo), "s"(srd), "s"(((i) >> 1) * 8192), "i"(((i) & 1) * 1024) : "memory")
    const u32 h2flag = (lane < 16 && w == 0) ? foff(F_H2 + lane) : OOB;
    const u32 ol = (m == 2 ? (u32)(2 * w + (r32 >> 4)) * 1024u : (u32)((r32 >> 4) * 4 + w) * 1024u) + (u32)(hh * 16 + (r32 & 15)) * 16u
                 + (m == 2 ? W_VO : m == 1 ? W_KQ : W_KQ + 8192u) + (u32)h * 16384u;
    const u32 fo = tid < 2 ? foff(F_QKV + tid * 96 + c) : OOB;
    lptr selw = lds + QK_SEL + (w == 0 ? 0 : 64), selr = lds + QK_SEL;
    PIN(selw);
    static_assert(QK_LEAD > 8 && QK_LEAD % 2 == 0 && LN0 == 10 * 32, "X = 8 comes up, before any item of layer 1");
    static_assert(W_H2 - W_H1 == 2 * 64 * (32 * DM * 2), "h");
    auto run = [&](auto isv) __attribute__((always_inline)) {
        constexpr bool V = decltype(isv)::value;
        constexpr int YB = QK_YB;
        f32x16 ax, ay = {};
        v4u x[8], pp, pq;
        u32 xv = t16 + W_H1, ox = ol, oy = OOB, nn = 1, lt = 0, fcnt = 0, fa = OOB;
        asm volatile("" : "+v"(lt), "+v"(nn));
#pragma unroll
        for (int z = 0; z < 5; ++z) x[z] = XRD(1, 40 - YB + z);
#define QPK(r_, a1, a_, q_) do { if constexpr (V) VCVT(r_[q_], a1[4 * (a_) + 8 * ((q_) >> 1) + 2 * ((q_) & 1)], a1[4 * (a_) + 8 * ((q_) >> 1) + 2 * ((q_) & 1) + 1]); \
                                 else VCVT(r_[q_], a1[4 * (a_) + (q_)], a1[4 * (a_) + (q_) + 8]); } while (0)
#define QPK4(r_, a1, a_) do { QPK(r_, a1, a_, 0); QPK(r_, a1, a_, 1); QPK(r_, a1, a_, 2); QPK(r_, a1, a_, 3); } while (0)
        for (;;) {
            u32 ls = uu(lt), yv, fv, nx, need;
            if (ls != 0u && uu(nn) < 256u) pre_wait(rs, 96, 96, 1u, ep);
            else if (ls != 0u) {
                n1 = (int)(uu(nn) >> 8);
                CFENCE();
                const u32 hf = h2flag + (u32)(~n1 & 1) * 2048u;
                u32 f2 = ld4(rs, hf);
                asm volatile("s_waitcnt vmcnt(0)" : "+v"(f2) :: "memory");
                *(LAS u32*)(selw + 16) = (u32)__builtin_amdgcn_ballot_w64(f2 < (u32)n1) & 0xFFFFu;
                BAR();
                st4s(rs, fa, 0, fcnt);
                fa = OOB;
                if (n1 > NSEG) { LDUMP(w == 0, 64 + c); __builtin_amdgcn_endpgm(); }
                if (uu(*(LAS u32*)(selr + 16)) != 0u) {
                    if (w == 0) wait_flags(rs, hf, (u32)n1, dead);
                    BAR();
                }
            }
#define QT(k) do {} while (0)
            QT(0);
            SCHED();
            QM0(0, 0); NOPS(0); QDMA(0, xv); QDMA(1, xv); QM0(0, 1);
            SFOR(z, 80)
                constexpr int ks = (z + 40 - YB) % 40, zx = z - YB;
                constexpr auto upto = [](int q) { const int d = (q - YB + 49) % 40; return q + 6 + (d == 0 || d == 2 ? 1 : d == 1 ? 2 : 0); };
                if constexpr (zx >= 0 && zx < 40) { if constexpr (V) M32B(ks == 0, ax, x[z & 7], wt[ks]); else M32A(ks == 0, ax, wt[ks], x[z & 7]); }
                else { if constexpr (V) M32B(ks == 0, ay, x[z & 7], wt[ks]); else M32A(ks == 0, ay, wt[ks], x[z & 7]); }
                if constexpr (z == 78) lt = *(LAS u32*)selr;
#pragma unroll
                for (int q = upto(z - 1); q < upto(z); ++q) x[q & 7] = XRD(q % 80 < YB || q % 80 >= YB + 40, (q + 40 - YB) % 40);
                SCHED();
                (void)&srd; (void)&wb;
                if constexpr (z < 2) QDMA(z + 2, xv);
                if constexpr (z >= 2 && z < 14 && (z & 3) == 2) QM0(0, (z + 6) >> 2);
                if constexpr (z >= 3 && z < 15 && (z & 1)) QDMA((z + 5) >> 1, xv);
                if constexpr (z == 4) { n0 = uni((int)nn); n1 = n0 >> 8; n0 &= 255; PINS(n0); PINS(n1); }
                if constexpr (z == 8) { nx = n0 < NSEG ? W_H1 + (u32)n0 * (32 * DM * 2) : OOB; PINS(nx); }
                if constexpr (z == 12) { yv = t16 + nx; PIN(yv); }
                if constexpr (zx == -6) { QT(1); asm volatile("s_waitcnt vmcnt(6) lgkmcnt(0)\n\ts_barrier" ::: "memory"); QT(2); }
                if constexpr (zx == -5) st4s(rs, fa, 0, fcnt);
                if constexpr (zx == -4 || zx == 0 || zx == 4) QM0(1, (zx + 4) >> 2);
                if constexpr (zx >= -3 && zx < 8 && (zx & 1)) QDMA((zx + 3) >> 1, yv);
                if constexpr (zx == 2 || zx == 6) { QPK(pp, ay, 0, (zx - 2) >> 1); QPK(pp, ay, 0, zx >> 1); }
                if constexpr (zx == 8 || zx == 11) { QPK(pq, ay, 1, zx & 2); QPK(pq, ay, 1, (zx & 2) + 1); }
                if constexpr (zx == 9) st16(rs, oy, pp);
                if constexpr (zx == 10) { QT(3); asm volatile("s_waitcnt vmcnt(8)\n\ts_barrier" ::: "memory"); QT(4); }
                if constexpr (zx == 12) st16(rs, oy + 512, pq);
                if constexpr (zx == 13 || zx == 17) QM0(1, (zx - 1) >> 2);
                if constexpr (zx >= 14 && zx < 22 && (zx & 1) == 0) QDMA((zx - 2) >> 1, yv);
                if constexpr (zx == 21) { fcnt = lane == 0 ? (u32)n0 : (u32)n1; fa = fo; asm volatile("" : "+v"(fcnt), "+v"(fa)); }
                if constexpr (zx == 22) { oy = (ol + (u32)n0 * (NH * 16384u)) | (yv & OOB); PIN(oy); }
                if constexpr (zx == 23) { n0 = min(n0 + 1, NSEG); PINS(n0); nx = (u32)min(NSEG, n1 + QK_LEAD); PINS(nx); }
                if constexpr (zx == 24)
                    asm volatile("s_cmp_ge_u32 %1, %0\n\ts_cselect_b32 %0, %4, %1\n\ts_addc_u32 %2, %2, 0\n\ts_cmp_lt_u32 %0, 64\n\ts_cselect_b32 %3, 0, %2"
                                 : "+s"(nx), "+s"(n0), "+s"(n1), "=&s"(need) : "s"(n1 + 64) : "scc");
                if constexpr (zx == 25) { n0 += 1 - (int)min(nx >> 6, 1u); PINS(n0); }
                if constexpr (zx == 26) { nn = (u32)(n0 | n1 << 8); PIN(nn); }
                if constexpr (zx == 27) { ls = W_H1 + (nx + (nx & 64u)) * (32 * DM * 2); PINS(ls); }
                if constexpr (zx == 28) { xv = t16 + ls; PIN(xv); need = nx == 8u ? 255u : need; PINS(need); }
                if constexpr (zx == 34) { asm volatile("s_waitcnt vmcnt(%0) lgkmcnt(0)\n\ts_barrier" :: "i"(QK_SMP < 34 ? 7 : 6) : "memory"); QT(5); }
                if constexpr (zx == QK_SMP) { const u32 po = (nx & 1u) * 2048u; LD4A(fv, h2flag, po); }
                if constexpr (zx == 42) QPK4(pp, ax, 0);
                if constexpr (zx == 43) st16(rs, ox, pp);
                if constexpr (zx == 44) QPK4(pq, ax, 1);
                if constexpr (zx == 45) st16(rs, ox + 512, pq);
                if constexpr (zx == 47) { ox = ol + nx * (NH * 16384u); PIN(ox); }
                if constexpr (z == 74) { asm volatile("s_waitcnt vmcnt(2)" : "+v"(fv) :: "memory"); QT(6); ls = (u32)__builtin_amdgcn_ballot_w64(fv < need) & 0xFFFFu; PINS(ls); }
                if constexpr (z == 75) { lt = ls; PIN(lt); *(LAS u32*)selw = lt; }
                if constexpr (z == 77) asm volatile("s_waitcnt lgkmcnt(2)\n\ts_barrier" ::: "memory");
                SCHED();
            SEND
        }
    };
    if (m == 2) run(IC<1>{}); else run(IC<0>{});
}

#define OU_FLG 25
#define OU_SMP 17
DEV void role_out(const Params& p, lptr lds, int L, int kq, int ng, v2u ep) {
    const int tid = threadIdx.x, lane = tid & 63, w = uni(tid >> 6), hh = lane >> 5, r32 = lane & 31;
    const rsrc_t rs = mk_rsrc(p.ws, W_END);
    v4u wo[64];
    bool dead = false;
    {
        const v4u sw = mk_srd(p.Wo, DM * NQ * 2);
        const u32 vo = wst_src(lane, DM) + (u32)ng * 256u;
        auto fill = [&](int b) {
#pragma unroll 1
            for (int i = 0; i < 8; ++i) DMAIN(sw, uu((b % 5) * 32768 + (4 * i + w) * 1024), vo, uu((kq * 1024 + b * 128 + 16 * i + 4 * w) * (DM * 2)));
        };
#pragma unroll 1
        for (u32 o = (u32)((kq * 5 + ng) * 256 + tid) * 16u + (u32)L * (2 * MB); o < 2 * MB; o += 20 * 4096) st16(rs, o, (v4u){PSENT, PSENT, PSENT, PSENT});
        fill(0);
        VMW(8); BAR();
        if (L == 0) pre_post(rs, 256 + kq * 5 + ng, 1u, ep);
#pragma unroll 1
        for (int b = 1; b < 5; ++b) fill(b);
        lptr tb = lds + hh * 1024 + wst_rd(lane, w);
        SFOR(b, 8)
            VMW(b < 4 ? 32 : 56 - 8 * b); BAR();
            lptr q = tb + (b % 5) * 32768;
#pragma unroll
            for (int k = 0; k < 8; ++k) { lptr a = q + (k >> 1) * 8192 + (k & 1) * 2048; wo[8 * b + k] = tr2(a, a + 4096); if (b < 7) asm volatile("" : "+a"(wo[8 * b + k])); else PIN(wo[8 * b + k]); }
            if constexpr (b < 3) { BAR(); fill(b + 5); }
        SEND
    }
    pre_wait(rs, 0, NPRE, 1u, ep);
    if (kq == 0 && ng == 0) LST(272 + L, rt32());
    MK_SRD();
    const u32 ofl = lane < 8 ? foff(F_O + L * 32 + kq * 8 + lane) : OOB, l16 = (u32)lane * 16u;
    const u32 fo = (tid == 0) ? foff(F_P + L * 32 + kq * 5 + ng) : OOB;
#define OM0(dst, b) asm volatile("s_add_u32 m0, %0, %1" :: "s"(dst), "i"((b) * 8192) : "scc", "memory")
#define ODMA(i, vo) asm volatile("buffer_load_dwordx4 %0, %1, %2 offen offset:%3 sc1 lds" :: "v"(vo), "s"(srd), "s"(((i) >> 1) * 16384), "i"(((i) & 1) * 1024) : "memory")
    auto first = [&](u32 dst, u32 vo) {
#pragma unroll
        for (int b = 0; b < 5; ++b) { OM0(dst, b); NOPS(0); ODMA(2 * b, vo); ODMA(2 * b + 1, vo); }
    };
    u32 pa = W_P + (u32)(L * NSEG) * P_ITEM + (u32)(2 * hh * 4 + kq) * (DM * 4) + (u32)(ng * 128 + w * 32 + r32) * 4u, fv = 0;
    u32 ov = W_VO + 8192u + (u32)(L * NSEG * NH + kq * 8) * 16384u + (u32)w * 2048u + l16;
    bool early = false;
    u32 farv = 1, owed = OOB;
    lptr selw = lds + 131072 + (w == 0 ? 0 : 64), selr = lds + 131072;
    asm volatile("" : "+v"(farv), "+v"(selw), "+v"(selr));
#define OWAIT(strict, loose) asm volatile("s_cmp_eq_u32 %0, 0\n\ts_cbranch_scc1 1f\n\ts_waitcnt vmcnt(%1)\n1:\n\ts_waitcnt vmcnt(%2)\n\ts_barrier" :: "s"(far), "i"(strict), "i"(loose) : "scc", "memory")
    for (int s = 0; s < NSEG; ++s) {
        const u32 dst = uu((s & 1) * 65536 + w * 2048), ndst = dst ^ 65536u, need = s + 1 < NSEG ? (u32)s + 2u : 255u;
        lptr A = lds + (s & 1) * 65536 + l16;
        if (!early) {
            if (!all_(ofl == OOB || fv >= (u32)s + 1)) wait_flags(rs, ofl, (u32)s + 1, dead);
            LS3(L == 0 && w == 1, 1, kq * 5 + ng);
            first(dst, ov);
        }
#define OT(k) do {} while (0)
        u32 far = uu(farv), nv, sv = (u32)s;
        OM0(dst, 5);
        OWAIT(8, 17);
        OT(0);
        v4u o[64];
#pragma unroll
        for (int k = 0; k < 4; ++k) o[k] = lds_r16(A + k * 1024);
        f32x16 acc;
        SCHED();
        SFOR(k, 64)
            if constexpr (k < 56) M32B(k == 0, acc, o[k], wo[k]); else M32V(acc, o[k], wo[k]);
            if constexpr (k < 60) o[k + 4] = lds_r16(A + (k + 4) * 1024);
            SCHED();
            (void)&srd; (void)&dst; (void)&ndst;
            if constexpr (k < 12 && (k & 1) == 0) ODMA(10 + k / 2, ov);
            if constexpr (k == 3) OWAIT(6, 14);
            if constexpr (k == 3 || k == 7) OM0(dst, (k + 21) >> 2);
            if constexpr (k == 12) { ov += NH * 16384u; PIN(ov); }
            if constexpr (k == OU_SMP) LD4A(fv, ofl, 0);
            if constexpr (k == 19) OWAIT(7, 15);
            if constexpr (k == 33) { asm volatile("s_waitcnt vmcnt(1)" : "+v"(fv) :: "memory"); far = (u32)__builtin_amdgcn_ballot_w64(fv < need) & 255u; PINS(far); }
            if constexpr (k == 34) { farv = far; PIN(farv); *(LAS u32*)selw = farv; }
            if constexpr (k == 35) asm volatile("s_waitcnt lgkmcnt(1)\n\ts_barrier" ::: "memory");
            if constexpr (k == OU_FLG) asm volatile("s_waitcnt vmcnt(7)\n\ts_barrier" ::: "memory");
            if constexpr (k == OU_FLG + 1) st4s(rs, owed, 0, sv);
            if constexpr (k == 36) farv = *(LAS u32*)selr;
            if constexpr (k == 41) { nv = farv ? OOB : ov; PIN(nv); }
            if constexpr (k >= 42 && k < 62 && (k & 3) == 2) OM0(ndst, (k - 42) >> 2);
            if constexpr (k >= 43 && k < 63 && (k & 1)) ODMA((k - 43) >> 1, nv);
            if constexpr (k == 48) LD4A(fv, ofl, 0);
            if constexpr (k == 4) OT(1);
            if constexpr (k == 20) OT(2);
            if constexpr (k == 36) OT(3);
            if constexpr (k == 41) OT(4);
            SCHED();
        SEND
        OT(5);
        asm volatile("s_nop 11" : "+v"(acc), "+v"(pa));
#pragma unroll
        for (int i = 0; i < 8; ++i) st4s(rs, pa, (u32)((4 * (i >> 1) + (i & 1)) * (4 * DM * 4)), pk(acc[2 * i], acc[2 * i + 1]));
        asm volatile("s_waitcnt vmcnt(15)" : "+v"(fv) :: "memory");
        OT(6);
        LS3(L == 0 && w == 1, 2, kq * 5 + ng);
        far = uu(farv);
        early = far == 0u || all_(ofl == OOB || fv >= need);
        owed = fo;
        if (far != 0u) {
            if (early) { first(ndst, ov); VMW(10); }
            else { fv = ld4(rs, ofl); asm volatile("s_waitcnt vmcnt(0)" : "+v"(fv) :: "memory"); }
            BAR();
            st4s(rs, fo, 0, (u32)s + 1);
            owed = OOB;
        }
        pa += P_ITEM;
        LS2(L == 0 && w == 1, 160 + kq * 5 + ng);
    }
    LDUMP(L == 0 && w == 1, kq * 5 + ng);
}

DEV void ln_stats2(const f32x2 (&x)[10], f32x2& mean, f32x2& rstd) {
    const f32x2 c = {f_(uu(u_(x[0].x))), f_(uu(u_(x[0].y)))};
    f32x2 d[10];
#pragma unroll
    for (int i = 0; i < 10; ++i) d[i] = x[i] - c;
    f32x2 a = ((d[0] + d[1]) + (d[2] + d[3])) + ((d[4] + d[5]) + (d[6] + d[7])) + (d[8] + d[9]);
    f32x2 b = (d[0] * d[0] + d[2] * d[2] + d[4] * d[4] + d[6] * d[6] + d[8] * d[8]) + (d[1] * d[1] + d[3] * d[3] + d[5] * d[5] + d[7] * d[7] + d[9] * d[9]);
    float a0, a1, b0, b1;
#define LN_DP4(m) "v_add_f32_dpp %0, %0, %0 " m " row_mask:0xf bank_mask:0xf\n\tv_add_f32_dpp %1, %1, %1 " m " row_mask:0xf bank_mask:0xf\n\t" \
                  "v_add_f32_dpp %2, %2, %2 " m " row_mask:0xf bank_mask:0xf\n\tv_add_f32_dpp %3, %3, %3 " m " row_mask:0xf bank_mask:0xf\n\t"
    asm("s_nop 1\n\t" LN_DP4("quad_perm:[1,0,3,2]") LN_DP4("quad_perm:[2,3,0,1]") LN_DP4("row_ror:4") LN_DP4("row_ror:8")
        "v_permlane16_swap_b32 %0, %1\n\tv_add_f32 %0, %0, %1\n\tv_permlane16_swap_b32 %2, %3\n\tv_add_f32 %2, %2, %3\n\ts_nop 1\n\t"
        "v_permlane32_swap_b32 %0, %2\n\tv_add_f32 %0, %0, %2\n\ts_nop 0\n\t"
        "v_readlane_b32 %4, %0, 0\n\tv_readlane_b32 %5, %0, 16\n\tv_readlane_b32 %6, %0, 32\n\tv_readlane_b32 %7, %0, 48\n\ts_nop 1"
        : "+v"(a.x), "+v"(b.x), "+v"(a.y), "+v"(b.y), "=s"(a0), "=s"(b0), "=s"(a1), "=s"(b1));
    const f32x2 m = (f32x2){a0, a1} * (1.0f / DM), v = (f32x2){b0, b1} * (1.0f / DM) - m * m;
    const f32x2 q = up2(pkv(up2(pkv((f32x2){fmaxf(v.x, 0.f), fmaxf(v.y, 0.f)})) + 1e-5f));
    const f32x2 sd = up2(pk(__builtin_amdgcn_sqrtf(q.x), __builtin_amdgcn_sqrtf(q.y)));
    mean = up2(pkv(c + m)); rstd = (f32x2){__builtin_amdgcn_rcpf(sd.x), __builtin_amdgcn_rcpf(sd.y)};
}
DEV void role_row(const Params& p, lptr lds, int id, v2u ep) {
    const int lane = threadIdx.x & 63, w = uni(threadIdx.x >> 6);
    const int pr = id >> 2, L = id & 1, par = (id >> 1) & 1;
    const rsrc_t rs = mk_rsrc(p.ws, W_END), rx = mk_rsrc(p.x, ACT), ro_ = mk_rsrc(p.out, ACT);
    bool dead = false;
    const u32 fp = lane < 20 ? foff(F_P + L * 32 + lane) : OOB, l16 = (u32)lane * 16u, l8 = (u32)lane * 8u, l4 = (u32)lane * 4u, fb = uu(w * CV_W);
    lptr F = lds + w * CV_W;
    MK_SRD();
    for (int s = par; s < NSEG; s += 2) {
        const u32 ro = (u32)(s * 32 + 2 * pr) * (DM * 2);
        if ((s & 6) == 0) {
            if ((s & 30) == 0) pre_poll(rs, s < 32 ? 0 : 192, s < 32 ? NPRE : 64, 4u, ep);
            if (id == 0 && s == 0) LST(274, rt32());
            if (s == 48) st8s(rs, (id == 1 && lane == 0) ? foff(F_EPOCH) : OOB, 0, (v2u){ep.x + 8u, ep.y});
#pragma unroll 1
            for (int i = 0; i < 5; ++i) {
                const u32 d = fb + i * 2048, o = W_Y + (u32)(16 * pr + (s >> 3)) * 10240u + i * 2048;
                DMA32(d, l16, o); DMA32(d + 10240u, l16, o + 81920u);
            }
            DRAIN();
        }
        RowX xr[2];
        auto xload = [&](rsrc_t r, u32 o, auto aux) {
#pragma unroll
            for (int i = 0; i < 2; ++i) {
                xr[i].a = __builtin_amdgcn_raw_buffer_load_b64(r, l8, o + i * (DM * 2), aux.value); xr[i].b = __builtin_amdgcn_raw_buffer_load_b64(r, l8 + 512u, o + i * (DM * 2), aux.value);
                xr[i].c = __builtin_amdgcn_raw_buffer_load_b32(r, l4 + 1024u, o + i * (DM * 2), aux.value);
            }
        };
        if (L == 0) xload(rx, ro, IC<0>{});
        f32x2 y[10], x[10];
        {
            lptr ya = F + (s & 7) * 2 + l16, yc = F + (s & 7) * 2 + 2048 + (lane >> 1) * 16 + (lane & 1) * 5120;
#pragma unroll
            for (int i = 0; i < 10; ++i) {
                lptr q = i < 8 ? ya + (i & 3) * 2560 + (i >> 2) * 1024 : yc + (i & 1) * 2560;
                y[i] = (f32x2){lo_f(*(LAS u16*)q), lo_f(*(LAS u16*)(q + 10240))}; PIN(y[i]);
            }
        }
        wait_flags(rs, fp, (u32)s + 1, dead);
        LS3(L == 0, 3, ri);
        LMX(L == 0, 0, s, rt32());
        {
            const u32 o = uu(W_P + (u32)(L * NSEG + s) * P_ITEM + (u32)pr * (4 * DM * 4));
            if (L == 1) xload(rs, W_X2 + ro, IC<16>{});
            v4u a[4], b[4]; v2u c[4];
#pragma unroll
            for (int kq = 0; kq < 4; ++kq) { a[kq] = ld16s(rs, l16, o + kq * (DM * 4)); SCHED(); b[kq] = ld16s(rs, l16 + 1024u, o + kq * (DM * 4)); SCHED(); c[kq] = ld8s(rs, l8 + 2048u, o + kq * (DM * 4)); SCHED(); }
            const u32 u0[5] = {xr[0].a.x, xr[0].a.y, xr[0].b.x, xr[0].b.y, xr[0].c}, u1[5] = {xr[1].a.x, xr[1].a.y, xr[1].b.x, xr[1].b.y, xr[1].c};
#pragma unroll
            for (int i = 0; i < 5; ++i) { x[2 * i] = (f32x2){lo_f(u0[i]), lo_f(u1[i])}; x[2 * i + 1] = (f32x2){hi_f(u0[i]), hi_f(u1[i])}; }
#pragma unroll
            for (int kq = 0; kq < 4; ++kq)
#pragma unroll
                for (int i = 0; i < 10; ++i)
                    asm volatile("v_dot2_f32_bf16 %0, %2, %3, %0\n\tv_dot2_f32_bf16 %1, %2, %4, %1" : "+v"(x[i].x), "+v"(x[i].y)
                                 : "v"(i < 4 ? a[kq][i & 3] : i < 8 ? b[kq][i & 3] : c[kq][i & 1]), "s"(0x3F80u), "s"(0x3F800000u));
            asm volatile("s_nop 2" : "+v"(x[8]), "+v"(x[9]));
        }
        LS3(L == 0, 4, ri);
        f32x2 mean, rstd;
        ln_stats2(x, mean, rstd);
#pragma unroll
        for (int i = 0; i < 10; ++i) x[i] += (x[i] - mean) * (y[i] * rstd);
        u32 xp[2][5];
#pragma unroll
        for (int i = 0; i < 5; ++i) { xp[0][i] = pkv((f32x2){x[2 * i].x, x[2 * i + 1].x}); xp[1][i] = pkv((f32x2){x[2 * i].y, x[2 * i + 1].y}); }
        if (L == 0) {
#pragma unroll
            for (int i = 0; i < 5; ++i) { x[2 * i] = (f32x2){lo_f(xp[0][i]), lo_f(xp[1][i])}; x[2 * i + 1] = (f32x2){hi_f(xp[0][i]), hi_f(xp[1][i])}; }
            ln_stats2(x, mean, rstd);
#pragma unroll
            for (int i = 0; i < 10; ++i) x[i] = (x[i] - mean) * rstd;
#pragma unroll
            for (int r = 0; r < 2; ++r) {
                u32 hp[5];
#pragma unroll
                for (int i = 0; i < 5; ++i) hp[i] = pkv((f32x2){x[2 * i][r], x[2 * i + 1][r]});
                store_h(rs, W_H2, s * 32 + 2 * pr + r, hp);
            }
#pragma unroll
            for (int r = 0; r < 2; ++r) {
                st8s(rs, l8, W_X2 + ro + r * (DM * 2), (v2u){xp[r][0], xp[r][1]});
                st8s(rs, l8 + 512u, W_X2 + ro + r * (DM * 2), (v2u){xp[r][2], xp[r][3]});
                st4s(rs, l4 + 1024u, W_X2 + ro + r * (DM * 2), xp[r][4]);
            }
            LS3(L == 0, 5, ri);
            DRAIN();
            st4s(rs, lane == 0 ? foff(F_H2 + par * 16 + pr) : OOB, 0, (u32)s + 1);
        } else {
#pragma unroll
            for (int r = 0; r < 2; ++r) {
                st8s(ro_, l8, ro + r * (DM * 2), (v2u){xp[r][0], xp[r][1]});
                st8s(ro_, l8 + 512u, ro + r * (DM * 2), (v2u){xp[r][2], xp[r][3]});
                st4s(ro_, l4 + 1024u, ro + r * (DM * 2), xp[r][4]);
            }
        }
        if (pr == 0) LST(320 + L * 64 + s, rt32());
        LS2(L == 0, 224 + ri);
        LMX(L == 0, 1, s, rt32());
    }
    LDUMP(L == 0, 32 + ri);
}

constexpr float SCALE = 0.08838834764831845f, LOG2E = 1.4426950408889634f;
constexpr int C_K = 0, C_V = 8192, C_QX = 16384, C_MEM = 24576, C_Q = C_MEM + 10240, C_KM = C_Q + 2304, C_VM = C_KM + 2304, C_DUMP = C_VM + 2048,
              C_ZERO = C_DUMP + 1024, C_WL = C_ZERO + 128;
constexpr int NLF = 14;
constexpr int C_MSK = C_WL + 4 * NLF * 2048;
static_assert(C_WL % 16 == 0 && C_MSK + 4096 <= LDS_BYTES, "head lds");
#define CH_VIS 8
#define CH_H0 104
#define CH_W0 80
#define CH_MH 3
#define CH_MW 3
#define CH_REC 28
#define CH_RTY 54u
#define CH_RISE 20u
#define CH_FALL 3u
#define CH_PRE 7
#define CH_PRW 2
#define CH_T0 205
#define CH_UP 2
#define CH_WX 6
#define WAIT_TXT(tmp, t4) "s_memrealtime vcc\n\t.p2align 5\n1:\n\ts_waitcnt lgkmcnt(0)\n\ts_sub_u32 " tmp ", " t4 ", vcc_lo\n\ts_memrealtime vcc\n\ts_cmpk_lt_u32 " tmp ", 400\n\ts_cbranch_scc1 1b\n\t" \
                          "s_add_u32 " tmp ", " tmp ", 4\n\ts_cselect_b32 " tmp ", " tmp ", 0\n2:\n\ts_sub_u32 " tmp ", " tmp ", 1\n\ts_cbranch_scc0 2b\n\t"
DEV void wait_until(u32 t) { u32 tmp; asm volatile(WAIT_TXT("%0", "%1") "s_waitcnt lgkmcnt(0)" : "=&s"(tmp) : "s"(uu(t - 4u)) : "vcc", "scc", "memory"); }
#define SMF_A(acc, a, b) asm volatile("v_smfmac_f32_16x16x64_bf16 %0, %1, %2, %3" : "+v"(acc) : "v"(a), "a"(b), "v"(idx))
#define SMF_V(acc, a, b) asm volatile("v_smfmac_f32_16x16x64_bf16 %0, %1, %2, %3" : "+v"(acc) : "v"(a), "v"(b), "v"(idx))
#define MF0(d, a, b) asm volatile("v_mfma_f32_16x16x32_bf16 %0, %1, %2, 0" : "=&v"(d) : "v"(a), "v"(b))
#define MF1(d, a, b) asm volatile("v_mfma_f32_16x16x32_bf16 %0, %1, %2, %0" : "+v"(d) : "v"(a), "v"(b))
#define MF0H(d, a, b) asm volatile("v_mfma_f32_16x16x16_bf16 %0, %1, %2, 0" : "=&v"(d) : "v"(a), "v"(b))
#define MF1H(d, a, b) asm volatile("v_mfma_f32_16x16x16_bf16 %0, %1, %2, %0" : "+v"(d) : "v"(a), "v"(b))
DEV v8u tr_frag(lptr p0, lptr p1, lptr p2, lptr p3) { return cat8(tr2(p0, p1), tr2(p2, p3)); }
constexpr int qslot(int ks) { return (ks & 1) && ks < 8 ? ks >> 1 : -1; }
constexpr int kslot(int ks) { return ks < 4 ? 4 + ks : -1; }
constexpr int vslot(int ks) { return ks >= 4 ? 4 + ks : -1; }
DEV u32 ring_map(u32 off) { return ((off >> 13) << 15) + (off & 8191u); }
DEV u32 oslot(int s) { return (u32)(s & 3) << 18 | (u32)(s & 2) << 13; }
#define ZRD(x, i) x = __builtin_bit_cast(f32x4, lds_r16(ZP + 16 * (i)))
#define VMAX(d, a, b) asm volatile("v_max_f32 %0, %1, %2" : "=v"(d) : "v"(a), "v"(b))
#define VMAX3(d, a, b, c_) asm volatile("v_max3_f32 %0, %1, %2, %3" : "=v"(d) : "v"(a), "v"(b), "v"(c_))
#define VEXP(d, a) asm volatile("v_exp_f32 %0, %1" : "=v"(d) : "v"(a))
#define VMULC(d, a) asm volatile("v_mul_f32 %0, 0x3e0293ee, %1" : "=v"(d) : "v"(a))
#define CADD(d, a, b) do { d = (a) + (b); PIN(d); } while (0)
#define CMULC(d, a) do { d = (a) * CEXP; PIN(d); } while (0)
#define CCVT(d, a, b) do { d = pk(a, b); PIN(d); } while (0)
#define LGK(n) __builtin_amdgcn_s_waitcnt(0xC07F | (n) << 8)
#define SEL(d, a, b) asm volatile("v_cndmask_b32 %0, %1, %2, %3" : "=v"(d) : "v"(a), "v"(b), "s"(himask))
#define QPERM(d, a, q) asm volatile("v_mov_b32_dpp %0, %1 quad_perm:" q " row_mask:0xf bank_mask:0xf" : "=v"(d) : "v"(a))

#define AL_H 3
#define HD_SMP 70
DEV void role_head(const Params& p, lptr lds, int L, int h, v2u ep) {
    const int tid = threadIdx.x, lane = tid & 63, w = uni(tid >> 6), g = lane >> 4, r = lane & 15;
    const rsrc_t rs = mk_rsrc(p.ws, W_END);
    v8u wq0[10], wq1[10], wk0[10], wk1[10], wv0[10], wv1[10];
    lptr WL = lds + C_WL + w * (NLF * 2048) + lane * 16;
    {
        constexpr int GS = 1056, CS = 48 * GS;
        const v4u sq = mk_srd(p.Wq, DM * NQ * 2), sk = mk_srd(p.Wk, DM * NQ * 2), sv = mk_srd(p.Wv, DM * NQ * 2);
        const u32 vo = (u32)(g * NQ + h * DH + 8 * (r ^ ((g & 1) << 3))) * 2u;
        auto fill = [&](int ch) {
#pragma unroll 1
            for (int i = 0; i < 4; ++i) {
                const u32 dst = uu((ch % 3) * CS + (4 * i + w) * GS), so = uu((ch * 64 + 16 * i + 4 * w) * (NQ * 2));
                DMAIN(sq, dst, vo, so); DMAIN(sk, dst + 16 * GS, vo, so); DMAIN(sv, dst + 32 * GS, vo, so);
            }
        };
        fill(0); fill(1); fill(2);
        const int q4 = r >> 2;
        lptr tb = lds + (4 * (g >> 1) + 2 * (q4 >> 1) + (g & 1)) * GS + (q4 & 1) * 256 + ((w * 64 + (r & 3) * 8) ^ ((q4 & 1) << 7));
        SFOR(ch, 10)
            VMW(ch < 8 ? 24 : 108 - 12 * ch);
            BAR();
            auto frag = [&](int m, int t) { lptr b = tb + (ch % 3) * CS + m * 16 * GS + t * 32; return tr_frag(b, b + 512, b + 8 * GS, b + 8 * GS + 512); };
            wq0[ch] = frag(0, 0); wq1[ch] = frag(0, 1); wk0[ch] = frag(1, 0); wk1[ch] = frag(1, 1); wv0[ch] = frag(2, 0); wv1[ch] = frag(2, 1);
            asm volatile("" : "+a"(wq0[ch]), "+a"(wk0[ch]), "+v"(wv0[ch]), "+v"(wv1[ch]));
            if constexpr (qslot(ch) < 0) asm volatile("" : "+a"(wq1[ch])); else asm volatile("" : "+v"(wq1[ch]));
            if constexpr (kslot(ch) < 0) asm volatile("" : "+a"(wk1[ch])); else asm volatile("" : "+v"(wk1[ch]));
            BAR();
            if constexpr (ch < 7) fill(ch + 3);
        SEND
#pragma unroll
        for (int ch = 0; ch < 10; ++ch) {
            if (qslot(ch) >= 0) { lds_w16(WL + qslot(ch) * 2048, wq1[ch].lo); lds_w16(WL + qslot(ch) * 2048 + 1024, wq1[ch].hi); }
            if (kslot(ch) >= 0) { lds_w16(WL + kslot(ch) * 2048, wk1[ch].lo); lds_w16(WL + kslot(ch) * 2048 + 1024, wk1[ch].hi); }
            if (vslot(ch) >= 0) { lds_w16(WL + vslot(ch) * 2048, wv1[ch].lo); lds_w16(WL + vslot(ch) * 2048 + 1024, wv1[ch].hi); }
        }
        CFENCE();
    }
    const u32 idx = (r & 2) ? 0xEEEEu : 0x4444u;
    unsigned long long himask = 0xCCCCCCCCCCCCCCCCull;
    PINS(himask);
    bool dead = false;
    if (h == 3) LST(266 + L, rt32());
    if (w == 0) LST(1240 + L * 32 + h, rt32());
    if (L == 1) {
        KvIn in;
        kvt_load(p, KT0 + 4 * h + w, in, [] {}); kvt_make(rs, lds + w * 9216, KT0 + 4 * h + w, in);
        DRAIN(); BAR();
        pre_post(rs, 64 + h, 4u, ep);
    }
    const u32 pol0 = pol_wait(rs, ep);
    if (h == 3 && L == 0) LST(277, rt32());
    const u32 l16 = (u32)lane * 16u;
    const u32 mring = (u32)L * MB + (pol0 ^ 16384u), hint = mring + 4 * 65536u;
    const u32 fkh = lane < 3 ? foff(F_QKV + L * 96 + lane * 32 + h) : lane < 7 ? hint + (u32)(lane - 3) * 4u : OOB;
    u32 tm = 0;
    MK_SRD();
    u32 ksrc = uu(W_KQ + (u32)(L * (NSEG - 1) * NH + h) * 16384u + w * 2048), vsrc = ksrc + (W_VO - W_KQ);
    u32 dk = uu(C_K + w * 2048), dv = dk + (C_V - C_K), dq = dk + (C_QX - C_K), dm = uu(C_MEM + w * 2048), dm2 = uu(w < 2 ? C_MEM + 8192 + w * 1024 : 0);
    asm volatile("" : "+s"(dk), "+s"(dv), "+s"(dq), "+s"(dm), "+s"(dm2));
    auto keys_wait = [&](u32 need) {
#pragma unroll 1
        for (int n = 0; !dead; ++n) {
            const bool beat = need + 2 * L > 2u;
            if (beat) wait_until(tm);
            const u32 hv = ld4(rs, lane == 7 ? foff(F_ERR) : fkh);
            if (beat) { tm = rdl(hv, 5); wait_until(rdl(hv, 3) - 30u); }
            const bool any = need == (u32)L;
            const u32 f0 = ld4(rs, any ? foff(F_QKV + 96 + lane) : fkh), f1 = ld4(rs, any ? foff(F_QKV + 128 + lane) : fkh);
            if (all_(min(f0, f1) >= need || (!any && lane >= 3))) break;
            dead = n > SPIN_CAP || rdl(hv, 7) != 0u;
            st4s(rs, dead ? foff(F_ERR) : OOB, 0, 1u);
        }
        CFENCE();
        DMA32(dk, l16, ksrc); DMA32(dv, l16, vsrc); DMA32(dq, l16, ksrc ^ 8192u);
    };
    if (L == 0) { st4s(rs, tid == 0 ? hint + 4096u + (u32)h * 128u : OOB, 0, 0u); keys_wait(1u); }
    lptr qw = lds + C_Q + (2 * g) * 288 + (32 * w + 2 * r) * 2;
    lptr kw = qw + (C_KM - C_Q); PIN(kw);
    lptr qr = lds + C_Q + (2 * (r >> 2) + (r & 1)) * 288 + g * 16;
    lptr LB = lds + l16, VX = lds + C_V + w * 2048 + l16;
    lptr AB = lds + C_MEM + ((lane & 57) + (lane & 2) * 2 + ((lane & 4) >> 1)) * 16;
    PIN(VX); PIN(AB);
    if (tid >= 64 && tid < 70) { const u32 z = tid == 68 ? 0x3F803F80u : tid == 69 ? PSENT : 0u; lds_w16(lds + C_ZERO + (tid - 64) * 16, (v4u){z, z, z, z}); }
    {
        f32x4 mk;
#pragma unroll
        for (int i = 0; i < 4; ++i) mk[i] = 16 * (w & 1) + 4 * g + i <= 16 * (w >> 1) + r ? 0.f : -1000000000.0f / SCALE;
        *(LAS f32x4*)(lds + C_MSK + tid * 16) = mk;
    }
    u32 zaddr = 0; PIN(zaddr);
    lptr ZP = lds + zaddr + C_ZERO;
    const float CEXP = SCALE * LOG2E;
#define LF_RD(slot, hf) lf[slot][hf] = lds_r16(WL + (slot) * 2048 + (hf) * 1024)
    BAR();
    const u32 ofl = tid == 0 ? foff(F_O + L * 32 + h) : OOB;
    u32 oso = (((g | L) & 1) ? OOB : 0u) + (u32)L * MB + (pol0 ^ oslot(-L)) + ring_map((u32)((r >> 3) * 32768 + h * 1024 + w * 256 + (g >> 1) * 128 + (r & 7) * 16));
    u32 missed = 0;
    for (int s = -L; s < NSEG;) {
        u32 sv = (u32)max(s, 0), ms = s > 0 ? mring + (sv & 3u) * 65536u : W_MEM0, ms2 = ms + 32768u + (w & 1) * 1024;
        ms += w * 2048;
        asm volatile("" : "+s"(ms), "+s"(ms2)); PIN(sv);
        f32x4 aq0, aq1, ak0, ak1;
        v4u lf[NLF][2];
        ZRD(aq0, 0); ZRD(aq1, 1); ZRD(ak0, 2); ZRD(ak1, 3);
#pragma unroll
        for (int i = 0; i < 4; ++i) { LF_RD(i, 0); LF_RD(i, 1); }
        SCHED();
        v4u A[10];
#define HT(k) do {} while (0)
        v4u kx[8], qf[4], kmf[4], vx[2], ones, P;
        float fa, fb, fc, fd, inv, sx[4], t[4], tm0, tm1; u32 pa, pb, fl, e01, e23, pmm;
        f32x4 av0, av1, s0, s1, sm, oa, ob, rsum;
        v2u vm, pm;
#define LW(sl) cat8(lf[sl][0], lf[sl][1])
#define PQ(ks, tl) do { if constexpr (!(tl)) SMF_A(aq0, A[ks], wq0[ks]); else if constexpr (qslot(ks) < 0) SMF_A(aq1, A[ks], wq1[ks]); else { const v8u lw_ = LW(qslot(ks)); SMF_V(aq1, A[ks], lw_); } } while (0)
#define PK(ks, tl) do { if constexpr (!(tl)) SMF_A(ak0, A[ks], wk0[ks]); else if constexpr (kslot(ks) < 0) SMF_A(ak1, A[ks], wk1[ks]); else { const v8u lw_ = LW(kslot(ks)); SMF_V(ak1, A[ks], lw_); } } while (0)
#define PV(ks, tl) do { if constexpr (!(tl)) SMF_V(av0, A[ks], wv0[ks]); else if constexpr (vslot(ks) < 0) SMF_V(av1, A[ks], wv1[ks]); else { const v8u lw_ = LW(vslot(ks)); SMF_V(av1, A[ks], lw_); } } while (0)
#define KXR(i) kx[i] = lds_r16(LB + C_K + ((i) & 1) * 4096 + ((i) >> 1) * 1024)
#define XW(q_) do { *(LAS u32*)(q_) = pa; *(LAS u32*)((q_) + 288) = pb; } while (0)
        auto slot = [&](auto zc) __attribute__((always_inline)) {
            constexpr int z = decltype(zc)::value;
            if constexpr (z < 14) PQ(z >> 1, z & 1);
            else if constexpr (z < 17) PQ(z - 7, 0);
            else if constexpr (z < 20) PQ(z - 10, 1);
            else if constexpr (z < 34) PK((z - 20) >> 1, z & 1);
            else if constexpr (z < 37) PK(z - 27, 0);
            else if constexpr (z < 40) PK(z - 30, 1);
            else if constexpr (z < 42) { if constexpr (z == 40) MF0(s0, kx[0], qf[0]); else MF0(s1, kx[1], qf[0]); }
            else if constexpr (z < 48) { if constexpr (z & 1) MF1(s1, kx[z - 40], qf[(z - 40) >> 1]); else MF1(s0, kx[z - 40], qf[(z - 40) >> 1]); }
            else if constexpr (z < 54) PV(z - 48, 0);
            else if constexpr (z == 54) MF0(sm, kmf[0], qf[0]);
            else if constexpr (z < 58) MF1(sm, kmf[z - 54], qf[z - 54]);
            else if constexpr (z < 62) PV(z - 52, 0);
            else if constexpr (z < 64) PV(z - 62, 1);
            else if constexpr (z == 64) MF0(oa, vx[0], P);
            else if constexpr (z == 65) MF0(ob, vx[1], P);
            else if constexpr (z == 66) MF0(rsum, ones, P);
            else if constexpr (z < 75) PV(z < 70 ? z - 63 : z == 70 ? 2 : z == 71 ? 7 : z == 72 ? 3 : z - 65, 1);
            else { const v2u ones2 = ones.lo; MF1H(rsum, ones2, pm); }
            if constexpr (z < 6) A[4 + z] = lds_r16(AB + (4 + z) * 1024);
            if constexpr (z >= 6 && z < 10) LF_RD(4 + ((z - 6) >> 1), z & 1);
            if constexpr (z >= 14 && z < 18) LF_RD(6 + ((z - 14) >> 1), z & 1);
            if constexpr (z >= 22 && z < 28) KXR(z - 22);
            if constexpr (z == 28) XW(qw);
            if constexpr (z == 29 || z == 30) KXR(z - 23);
            if constexpr (z == 32 || z == 47) asm volatile("s_barrier" ::: "memory");
            if constexpr (z >= 33 && z < 37) qf[z - 33] = lds_r16(qr + (z - 33) * 64);
            if constexpr (z == 20) ZRD(av0, 0);
            if constexpr (z == 43) XW(kw);
            if constexpr (z == 44 || z == 45) vx[z - 44] = lds_r16(VX + (z - 44) * 1024);
            if constexpr (z >= 48 && z < 52) kmf[z - 48] = lds_r16(qr + (C_KM - C_Q) + (z - 48) * 64);
            if constexpr (z == 52) ZRD(av1, 1);
            if constexpr (z == 37) ones = lds_r16(ZP + 64);
            if constexpr (z >= 57 && z < 64) LF_RD(8 + ((z - 57) >> 1), (z - 57) & 1);
            if constexpr (z >= 65 && z < 70) LF_RD(8 + ((z - 58) >> 1), (z - 58) & 1);
            if constexpr (z == HD_SMP) LD4A(fl, fkh, 0);
            SCHED();
            if constexpr (z == 19) HT(2);
            if constexpr (z == 32) HT(3);
            if constexpr (z == 47) HT(4);
            if constexpr (z == 64) HT(5);
            if constexpr (z == 19) LGK(4);
            if constexpr (z == 21 || z == 41) LGK(1);
            if constexpr (z == 56 || z == 73) LGK(0);
            if constexpr (z == 31 || z == 46) LGK(2);
            if constexpr (z == 39) LGK(4);
            if constexpr (z >= 53 && z < 56) LGK(57 - z);
            if constexpr (z == 64) LGK(3);
            if constexpr (z == 68) LGK(5);
            if constexpr (z >= 70 && z < 73) LGK(74 - z);
            SCHED();
            if constexpr (z == 18) CADD(fa, aq0[0], aq0[2]);
            if constexpr (z == 19) CADD(fb, aq0[1], aq0[3]);
            if constexpr (z == 20) CMULC(fa, fa);
            if constexpr (z == 21) CADD(fc, aq1[0], aq1[2]);
            if constexpr (z == 22) CADD(fd, aq1[1], aq1[3]);
            if constexpr (z == 23) CMULC(fc, fc);
            if constexpr (z == 24) CMULC(fb, fb);
            if constexpr (z == 25) CMULC(fd, fd);
            if constexpr (z == 26) CCVT(pa, fa, fc);
            if constexpr (z == 27) CCVT(pb, fb, fd);
            if constexpr (z == 38) CADD(fa, ak0[0], ak0[2]);
            if constexpr (z == 39) CADD(fb, ak0[1], ak0[3]);
            if constexpr (z == 41) { CADD(fc, ak1[0], ak1[2]); CADD(fd, ak1[1], ak1[3]); }
            if constexpr (z == 42) { CCVT(pa, fa, fc); CCVT(pb, fb, fd); }
            if constexpr (z >= 49 && z < 53) SEL(sx[z - 49], s0[z - 49], s1[z - 49]);
            if constexpr (z >= 53 && z < 57) VEXP(t[z - 53], sx[z - 53]);
            if constexpr (z == 57) CCVT(e01, t[0], t[1]);
            if constexpr (z == 58) CCVT(e23, t[2], t[3]);
            if constexpr (z == 59) QPERM(P.x, e01, "[0,1,0,1]");
            if constexpr (z == 60) QPERM(P.z, e01, "[2,3,2,3]");
            if constexpr (z == 61) QPERM(P.y, e23, "[0,1,0,1]");
            if constexpr (z == 62) QPERM(P.w, e23, "[2,3,2,3]");
            if constexpr (z == 63) CADD(fa, av0[0], av0[2]);
            if constexpr (z == 64) { asm volatile("" :: "v"(sm)); VEXP(tm0, sm[0]); }
            if constexpr (z == 65) VEXP(tm1, sm[1]);
            if constexpr (z == 66) CADD(fb, av0[1], av0[3]);
            if constexpr (z == 67) CCVT(pmm, tm0, tm1);
            if constexpr (z >= 68 && z < 72) SEL(oa[z - 68], oa[z - 68], ob[z - 68]);
            if constexpr (z == 72) asm volatile("v_cndmask_b32 %0, %1, 0, %2" : "=v"(pm.x) : "v"(pmm), "s"(himask));
            if constexpr (z == 73) asm volatile("v_cndmask_b32 %0, 0, %1, %2" : "=v"(pm.y) : "v"(pmm), "s"(himask));
            if constexpr (z == 74) LGK(15);
            if constexpr (z == 75) CCVT(vm.x, fa, fb);
            SCHED();
        };
#define HT0 ""
#define HT0O
        {
            u32 tmp;
            asm volatile("s_mov_b32 m0, %2\n\t" WAIT_TXT("%0", "%1") HT0 "buffer_load_dwordx4 %4, %5, %6 offen sc1 lds\n\tbuffer_load_dwordx4 %4, %5, %6 offen offset:1024 sc1 lds\n\t"
                         "s_and_b32 m0, %3, %3\n\ts_cbranch_scc0 3f\n\tbuffer_load_dwordx4 %4, %5, %7 offen sc1 lds\n3:\n\t.p2align 6\n\t.rept %8\n\ts_nop 0\n\t.endr\n\ts_waitcnt vmcnt(0) lgkmcnt(0)\n\ts_barrier"
                         : "=&s"(tmp) : "s"(uu(tm - 4u)), "s"(dm), "s"(dm2), "v"(l16), "s"(srd), "s"(ms), "s"(ms2), "i"(AL_H) HT0O : "vcc", "scc", "memory");
        }
        HT(1);
#pragma unroll
        for (int ks = 0; ks < 4; ++ks) A[ks] = lds_r16(AB + ks * 1024);
        SCHED();
        st4s(rs, ofl, 0, sv);
        SCHED();
        static_for<0, 76>(slot);
        asm volatile("s_nop 2\n\tv_add_f32 %1, %3, %5\n\tv_add_f32 %2, %4, %6\n\tv_cvt_pk_bf16_f32 %0, %1, %2"
                     : "=v"(vm.y), "=&v"(fc), "=&v"(fd) : "v"(av1[0]), "v"(av1[1]), "v"(av1[2]), "v"(av1[3]));
        __builtin_amdgcn_s_waitcnt(0xCF7F);
        f32x2 i2; u32 oad; unsigned long long far;
        auto publish = [&] {
            v4u oc; f32x2 u0 = oa.lo, u1 = oa.hi;
            asm volatile("v_pk_mul_f32 %0, %0, %2\n\tv_pk_mul_f32 %1, %1, %2" : "+v"(u0), "+v"(u1) : "v"(i2));
            __builtin_amdgcn_s_waitcnt(0xCF7F);
            asm volatile("v_cvt_pk_bf16_f32 %0, %4, %5\n\tv_cvt_pk_bf16_f32 %2, %4, %5\n\tv_cvt_pk_bf16_f32 %1, %6, %7\n\tv_cvt_pk_bf16_f32 %3, %6, %7\n\t"
                         "v_permlane16_swap_b32 %0, %2\n\ts_nop 0\n\tv_permlane16_swap_b32 %1, %3"
                         : "=&v"(oc.x), "=&v"(oc.y), "=&v"(oc.z), "=&v"(oc.w) : "v"(u0.x), "v"(u0.y), "v"(u1.x), "v"(u1.y));
            st16(rs, oad, oc);
        };
        asm volatile("s_nop 0\n\tv_mfma_f32_16x16x16_bf16 %0, %5, %6, %0\n\tv_rcp_f32 %1, %7\n\tv_add_u32 %3, 0xde800000, %7\n\tv_cmp_lt_u32 %4, %9, %3\n\tv_mov_b32 %2, %1"
                     : "+v"(oa), "=&v"(i2.x), "=&v"(i2.y), "=&v"(oad), "=&s"(far) : "v"(vm), "v"(pm), "v"(rsum[0]), "v"(rsum), "s"(0x3C000000u));
        if constexpr (HD_SMP > 75) LD4A(fl, fkh, 0); else NOPS(0);
        asm volatile("v_cndmask_b32 %0, %1, -16, %2" : "=v"(oad) : "v"(oso), "s"(far));
        __builtin_amdgcn_s_waitcnt(0xCF7F);
        publish();
        SCHED();
        if (__builtin_expect(far != 0ull, 0)) {
            DRAIN(); SCHED();
            bool miss = false;
#pragma unroll 1
            for (int i = 0; i < 10; ++i) { const v4u a = lds_r16(LB + C_MEM + i * 1024); miss |= a.x == PSENT || a.y == PSENT || a.z == PSENT || a.w == PSENT; }
            if (missed < SPIN_CAP && !all_(!miss)) {
                *(LAS u32*)(lds + C_DUMP + (w == 0 ? 0 : 64)) = ld4(rs, foff(F_ERR));
                asm volatile("" : "+v"(fl) :: "memory");
                tm = rdl(fl, 5);
                BAR();
                missed = uu(*(LAS u32*)(lds + C_DUMP)) != 0u ? SPIN_CAP : missed + 1;
                st4s(rs, missed == SPIN_CAP ? foff(F_ERR) : OOB, 0, 1u);
                BAR();
                continue;
            }
            float mx = fmaxf(fmaxf(fmaxf(sx[0], sx[1]), fmaxf(sx[2], sx[3])), fmaxf(sm[0], sm[1]));
            mx = max32(max16(fmaxf(mx, dpp<0x4E>(mx))));
#pragma unroll
            for (int i = 0; i < 4; ++i) t[i] = __builtin_amdgcn_exp2f(sx[i] - mx);
            e01 = pk(t[0], t[1]); e23 = pk(t[2], t[3]);
            pmm = pk(__builtin_amdgcn_exp2f(sm[0] - mx), __builtin_amdgcn_exp2f(sm[1] - mx));
            asm volatile("v_cndmask_b32 %0, %2, 0, %3\n\tv_cndmask_b32 %1, 0, %2, %3" : "=&v"(pm.x), "=&v"(pm.y) : "v"(pmm), "s"(himask));
            asm volatile("s_nop 1" : "+v"(e01), "+v"(e23), "+v"(pm));
            QPERM(P.x, e01, "[0,1,0,1]"); QPERM(P.z, e01, "[2,3,2,3]"); QPERM(P.y, e23, "[0,1,0,1]"); QPERM(P.w, e23, "[2,3,2,3]");
            NOPS(1);
            MF0(rsum, ones, P); MF0(oa, vx[0], P); MF0(ob, vx[1], P);
            NOPS(7);
            { const v2u ones2 = ones.lo; MF1H(rsum, ones2, pm); }
#pragma unroll
            for (int i = 0; i < 4; ++i) SEL(oa[i], oa[i], ob[i]);
            NOPS(1);
            MF1H(oa, vm, pm);
            asm volatile("s_nop 7\n\tv_rcp_f32 %0, %3\n\tv_cndmask_b32 %2, -16, %5, %6\n\tv_mov_b32 %1, %0\n\ts_nop 0"
                         : "=&v"(i2.x), "=&v"(i2.y), "=&v"(oad) : "v"(rsum[0]), "v"(rsum), "v"(oso), "s"(far));
            publish();
        }
        SCHED();
        unsigned long long clk;
        asm volatile("s_memrealtime %0" : "=s"(clk) :: "memory");
        HT(6);
        v4u tq[4], tk[8], tv[2];
        f32x4 c0, c1, cm;
        u32 oo, oz, kvo; bool kvok;
        u32 ln = l16; PIN(ln);
        lptr QX = lds + C_QX + (w & 1) * 4096 + ln, VO = lds + C_V + (w ^ 1) * 2048 + ln, VMX = lds + C_VM + w * 512 + (ln >> 1);
#pragma unroll
        for (int i = 0; i < 2; ++i) { tq[i] = lds_r16(QX + i * 1024); kmf[i] = lds_r16(qr + (C_KM - C_Q) + i * 64); tk[2 * i] = lds_r16(LB + C_K + i * 1024); tk[2 * i + 1] = lds_r16(LB + C_K + 4096 + i * 1024); }
        { lptr MK = lds + C_MSK + (w & 1) * 2048 + ln; c0 = *(LAS f32x4*)MK; c1 = *(LAS f32x4*)(MK + 1024); }
        *(LAS v2u*)VMX = vm;
        SCHED();
        SFOR(i, 12)
            if constexpr (i == 0) MF0(cm, kmf[0], tq[0]);
            else if constexpr (i % 3 == 0) MF1(cm, kmf[i / 3], tq[i / 3]); else if constexpr (i % 3 == 1) MF1(c0, tk[2 * (i / 3)], tq[i / 3]); else MF1(c1, tk[2 * (i / 3) + 1], tq[i / 3]);
            if constexpr (i < 2) kmf[2 + i] = lds_r16(qr + (C_KM - C_Q) + (2 + i) * 64);
            if constexpr (i == 0) { tq[2] = lds_r16(QX + 2048); tk[4] = lds_r16(LB + C_K + 2048); }
            if constexpr (i == 1) { tk[5] = lds_r16(LB + C_K + 6144); tq[3] = lds_r16(QX + 3072); }
            if constexpr (i == 2) { tk[6] = lds_r16(LB + C_K + 3072); tk[7] = lds_r16(LB + C_K + 7168); }
            if constexpr (i == 3) { tv[0] = lds_r16(VO); tv[1] = lds_r16(VO + 1024); }
            if constexpr (i == 4) { oo = ln + (ln & 0x300u) + ((vsrc ^ 8192u) + (u32)((w & 1) * 256) + ((u32)s & OOB)); PIN(oo); }
            if constexpr (i == 5) { ksrc += NH * 16384; vsrc += NH * 16384; PINS(ksrc); PINS(vsrc); }
            if constexpr (i == 6) { oso ^= (s & 1) ? oslot(3) : oslot(1); PIN(oso); }
            if constexpr (i == 7) { oz = oso ^ oslot(2); PIN(oz); }
            if constexpr (i == 9) { asm volatile("s_waitcnt vmcnt(1)" : "+v"(fl) :: "memory"); kvok = ((u32)__builtin_amdgcn_ballot_w64(fl < (u32)s + 2) & 7u) == 0u; }
            if constexpr (i == 10) { kvo = l16 | (kvok ? 0u : OOB); PIN(kvo); }
            SCHED();
        SEND
        asm volatile("s_waitcnt lgkmcnt(0)\n\ts_barrier" : "+s"(clk) :: "memory");
        const v2u tvm = *(LAS v2u*)(VMX + (512 - (w & 1) * 1024));
        const v4u pv = lds_r16(ZP + 80);
        DMA32(dk, kvo, ksrc);
        SCHED(); asm volatile("" :: "v"(cm));
        const float u[8] = {c0[0], c0[1], c0[2], c0[3], c1[0], c1[1], c1[2], c1[3]};
        float m, m2;
        {
            asm volatile("v_max3_f32 %0, %2, %3, %10\n\tv_max3_f32 %1, %4, %5, %11\n\tv_max3_f32 %0, %0, %6, %7\n\tv_max3_f32 %1, %1, %8, %9\n\t"
                         "v_max_f32 %0, %0, %1\n\tv_mov_b32 %1, %0\n\ts_nop 1\n\tv_permlane16_swap_b32 %0, %1\n\tv_max_f32 %0, %0, %1\n\tv_mov_b32 %1, %0\n\ts_nop 1\n\t"
                         "v_permlane32_swap_b32 %0, %1\n\tv_max_f32 %0, %0, %1\n\tv_mul_f32 %0, 0xbe0293ee, %0"
                         : "=&v"(m), "=&v"(m2) : "v"(u[0]), "v"(u[1]), "v"(u[2]), "v"(u[3]), "v"(u[4]), "v"(u[5]), "v"(u[6]), "v"(u[7]), "v"(cm[0]), "v"(cm[1]));
        }
        DMA32(dv, kvo, vsrc);
        {
            v4u px; v2u p0, p1;
#define EX2(d, a, b) asm volatile("v_fmamk_f32 %0, %2, 0x3e0293ee, %4\n\tv_fmamk_f32 %1, %3, 0x3e0293ee, %4\n\tv_exp_f32 %0, %0\n\tv_exp_f32 %1, %1" : "=&v"(d.x), "=&v"(d.y) : "v"(a), "v"(b), "v"(m))
            f32x2 ex[5];
            EX2(ex[0], u[0], u[1]); EX2(ex[1], u[2], u[3]); EX2(ex[2], u[4], u[5]); EX2(ex[3], u[6], u[7]); EX2(ex[4], cm[0], cm[1]);
            asm volatile("v_cvt_pk_bf16_f32 %0, %8, %9\n\tv_cvt_pk_bf16_f32 %1, %10, %11\n\tv_cvt_pk_bf16_f32 %2, %12, %13\n\tv_cvt_pk_bf16_f32 %3, %14, %15\n\t"
                         "v_mov_b32 %5, 0\n\tv_mov_b32 %6, 0\n\tv_cvt_pk_bf16_f32 %4, %16, %17\n\tv_cvt_pk_bf16_f32 %7, %16, %17"
                         : "=&v"(px.x), "=&v"(px.y), "=&v"(px.z), "=&v"(px.w), "=&v"(p0.x), "=&v"(p0.y), "=&v"(p1.x), "=&v"(p1.y)
                         : "v"(ex[0].x), "v"(ex[0].y), "v"(ex[1].x), "v"(ex[1].y), "v"(ex[2].x), "v"(ex[2].y), "v"(ex[3].x), "v"(ex[3].y), "v"(ex[4].x), "v"(ex[4].y));
            f32x4 o[4], sum;
            const v2u ones2 = ones.lo;
            const u32 now = (u32)clk;
            SCHED();
            MF0(sum, ones, px); { const u32 q = ksrc ^ 8192u; DMA32(dq, kvo, q); } SCHED();
            MF0(o[0], vx[0], px);
            MF0(o[1], vx[1], px);
            MF0(o[2], tv[0], px);
            MF0(o[3], tv[1], px);
            MF1H(sum, ones2, p0);
            MF1H(o[0], vm, p0);
            MF1H(o[1], vm, p1); st16(rs, oz, pv); SCHED();
            MF1H(o[2], tvm, p0);
            MF1H(o[3], tvm, p1);
            asm volatile("s_nop 2\n\tv_rcp_f32 %0, %1\n\ts_nop 0" : "=v"(inv) : "v"(sum[0]), "v"(sum));
            asm volatile("" : "+v"(o[0]), "+v"(o[1]));
            st16(rs, oo, (v4u){pk2(o[0].lo * inv), pk2(o[0].hi * inv), pk2(o[1].lo * inv), pk2(o[1].hi * inv)});
            asm volatile("" : "+v"(o[2]), "+v"(o[3]));
            st16(rs, oo ^ 2048u, (v4u){pk2(o[2].lo * inv), pk2(o[2].hi * inv), pk2(o[3].lo * inv), pk2(o[3].hi * inv)});
            SCHED();
            LS2(L == 0 && w == 0, h);
            st4s(rs, ((h & 15) == 0 && tid == 192) ? hint + 128u + (u32)h * 8u : OOB, 0, (u32)s << 16 | min(missed, 1u) << 14 | min(now - tm, 0x3FFFu));
            tm = rdl(fl, 5);
            missed = missed == SPIN_CAP ? missed : 0u;
            if (__builtin_expect(s < 1 - L, 0)) {
                if (s < 0) oso -= (ln & 256u) ? 0u : OOB;
#pragma unroll 1
                for (int n = 0; n < (s < 0 ? 0 : 200); ++n) {
                    CFENCE();
                    tm = rdl(ld4(rs, fkh), 6);
                    if (tm != PSENT) break;
                }
            }
        }
        if (__builtin_expect(!kvok, 0) && s + 1 < NSEG) {
            st4s(rs, ln == 0 ? hint + 4096u + (u32)h * 128u : OOB, 0, (u32)s + 1u);
            keys_wait((u32)s + 2u);
        }
        ++s;
    }
    DRAIN(); BAR();
    st4s(rs, ofl, 0, NSEG);
    LDUMP(w == 0, 160 + L * 32 + h);
}

#define WO_NL 3
#define WO_ROT w
#define WO_AH 1
DEV void role_wo(const Params& p, lptr lds, int j, v2u ep) {
    const int tid = threadIdx.x, lane = tid & 63, w = uni(tid >> 6), g = lane >> 4, r = lane & 15;
    const int rg = j & 1, cg = j >> 1, rot = WO_ROT;
    const rsrc_t rs = mk_rsrc(p.ws, W_END);
    v8u wo[8][5];
    {
        const v4u so_ = mk_srd(p.Wo, DM * NQ * 2);
        u32 vo[5];
#pragma unroll
        for (int i = 0; i < 5; ++i) { const int n = 64 * i + lane; vo[i] = (u32)((n / 5) * DM + cg * 40 + (n % 5) * 8) * 2u; }
        v4u junk = {};
        const u32 av = (u32)(j * 5120 + lane * 16);
        auto ahead = [&](int b) {
#pragma unroll
            for (int i = 0; i < 5; ++i) AHEAD(so_, av, uu((8 * w + ((b + rot) & 7)) * (DH * DM * 2) + i * 1024));
        };
        auto fill = [&](int b) {
            if (b + WO_AH < 8) ahead(b + WO_AH);
            const u32 dst = uu(w * 40960 + (b & 3) * 10240), src = uu((8 * w + ((b + rot) & 7)) * (DH * DM * 2));
#pragma unroll
            for (int i = 0; i < 10; ++i) DMAIN(so_, dst + i * 1024, vo[i % 5], src + (i / 5) * (64 * DM * 2));
        };
#pragma unroll 1
        for (int b = 0; b < WO_AH; ++b) ahead(b);
#pragma unroll 1
        for (int b = 0; b < 4; ++b) fill(b);
        const int q4 = r >> 2;
        lptr tb = lds + w * 40960 + (32 * (g >> 1) + 16 * (q4 >> 1) + 8 * ((r >> 1) & 1) + 4 * (g & 1) + (q4 & 1)) * 80 + (r & 1) * 8;
        SFOR(b, 8)
            VMW((b < 5 ? 30 : 70 - 10 * b) + (b + 1 + WO_AH < 8 ? 5 : 0) + (b + 2 + WO_AH < 8 ? 5 : 0) + (b + 3 + WO_AH < 8 ? 5 : 0));
            if constexpr (b + WO_AH == 7) PIN(junk);
#pragma unroll
            for (int t = 0; t < 5; ++t) {
                lptr q = tb + (b & 3) * 10240 + t * 16;
                wo[b][t] = tr_frag(q, q + 160, q + 64 * 80, q + 64 * 80 + 160);
                if (b * 5 + t < 32) asm volatile("" : "+a"(wo[b][t])); else asm volatile("" : "+v"(wo[b][t]));
            }
            CFENCE();
            if constexpr (b < 4) fill(b + 4);
        SEND
    }
    if (j == 0) LST(265, rt32());
    LST(1100 + j * 4 + w, rt32());
    const u32 idx = (r & 2) ? 0xEEEEu : 0x4444u;
    const u32 pol0 = pol_wait(rs, ep);
    BAR();
    const u32 l16 = (u32)lane * 16u;
    u32 oo[8];
#pragma unroll
    for (int i = 0; i < 8; ++i) oo[i] = uu(ring_map((u32)(rg * 32768 + (8 * w + ((i + rot) & 7)) * 1024)));
    const u32 hint = (pol0 ^ 16384u) + 4 * 65536u;
    u32 hvo[2];
#pragma unroll
    for (int l = 0; l < 2; ++l) hvo[l] = lane < 2 ? hint + (u32)(l ^ 1) * MB + (u32)lane * 4u : lane == 2 ? hint + (u32)l * MB + 128u : lane == 3 ? foff(F_ERR) : lane == 4 ? hint + (u32)l * MB + 256u :
                 (lane >= 32 && j == 0 && w == 3) ? hint + (u32)l * MB + (u32)lane * 128u : OOB;
    lptr xw = r < 8 ? lds + w * 640 + (2 * (g & 1) + (g >> 1)) * 32 + r * 4 : lds + 8192 + lane * 4;
    const int G = ((5 * cg) >> 1) + w, tt = 2 * G + ((lane >> 4) & 1) - 5 * cg;
    const bool pub = w < 3 && lane < 32 && tt >= 0 && tt < 5;
    lptr xr = lds + (pub ? tt * 128 + (lane & 15) * 8 : 0);
    const u32 mpo = pub ? (pol0 ^ 16384u) + ring_map((u32)(G * 256 + rg * 128 + lane * 4)) : OOB;
    constexpr u32 PER0 = CH_W0 + CH_MW + CH_H0 + CH_MH + 2 * CH_VIS, DW0 = CH_W0 + CH_VIS + CH_MW + CH_REC;
    const bool lead = j == 0 && w == 3;
    const u32 hso = (lead && lane == 0) ? hint : OOB;
    MK_SRD();
    u32 t = 0, tn = 0;
    int sn[2] = {0, 0};
    u32 hav = 8 * CH_H0, wav = 8 * CH_W0, mh = 8 * CH_MH, mw = 8 * CH_MW, ok[2] = {4u, 0u}, dw[2] = {DW0, CH_T0 + CH_REC - 8};
    u32 vo = l16 + pol0 + MB, tp = 0, fin, left, cnt = 0;
    unsigned long long clk = 0;
    {
        if (w == 0) LST(1310 + j, rt32());
        if (lead) {
#pragma unroll 1
            for (int n = 0; n < SPIN_CAP; ++n) {
                CFENCE();
                const u32 a = ld4(rs, foff(F_QKV + lane)), b = ld4(rs, lane < 32 ? foff(F_QKV + 64 + lane) : hvo[0]);
                if (all_(a != 0u && b != (lane < 32 ? 0u : PSENT))) break;
                __builtin_amdgcn_s_sleep(1);
            }
        }
        if (lead) {
            t = rt32() + CH_T0;
            st16(rs, hso, (v4u){t, PER0, t + DW0, t + DW0});
        } else {
#pragma unroll 1
            for (int n = 0; n < SPIN_CAP; ++n) {
                CFENCE();
                const u32 hv = ld4(rs, hvo[1]);
                t = rdl(hv, 0);
                if (rdl(hv, 1) < 400u) break;
                __builtin_amdgcn_s_sleep(1);
            }
        }
    }
    const u32 tm0 = t + DW0;
    tn = t; t -= CH_T0; clk = t - 100u;
    u32 zero = 0, hv = 0xFFFFu, pst = 1, pmp = OOB;
    const u32 psent = PSENT;
    lptr sw = lds + 2560 + w * 4;
    asm volatile("" : "+v"(zero), "+v"(sw));
    auto turn = [&](auto lc) __attribute__((always_inline)) {
        constexpr int L = decltype(lc)::value, M = L ^ 1;
        u32 done, rep, rd, lost, dh, dn, nt, za, pdur, mp; v4u hw;
        const int sc = sn[L];
        auto books = [&](auto ic) __attribute__((always_inline)) {
            constexpr int i = decltype(ic)::value;
            if constexpr (i == 0) { rep = max(rdl(hv, 2), rdl(hv, 4)); PINS(rep); }
            if constexpr (i == 1) { lost = (u32)(__builtin_amdgcn_ballot_w64(hv == (u32)sn[M]) >> 32); PINS(lost); }
            if constexpr (i == 2) { lost |= (rep >> 16) ^ (u32)sn[M]; PINS(lost); }
            if constexpr (i == 3) { lost |= ok[M] ^ 2u; rd = rep & 0xFFFFu; PINS(lost); PINS(rd); }
            if constexpr (i == 4) { lost = lost == 0u ? CH_UP * 8u : 0u; PINS(lost); }
            if constexpr (i == 5) { pdur = ((u32)clk - tp) * 8u; PINS(pdur); pdur = min(pdur, wav + CH_RISE); PINS(pdur); }
            if constexpr (i == 6) { pdur = (done & ok[M]) ? pdur : wav; PINS(pdur); }
            if constexpr (i == 7) { wav = max(pdur, wav - CH_FALL); PINS(wav); }
            if constexpr (i == 8) { nt = min(rd * 8u, hav + CH_RISE); PINS(nt); }
            if constexpr (i == 9) { nt = rd < 0x3FFFu ? nt : hav; PINS(nt); }
            if constexpr (i == 10) { hav = max(nt, hav - CH_FALL); PINS(hav); }
            if constexpr (i == 11) { mw = max(mw - 1u, CH_MW * 8u); PINS(mw); }
            if constexpr (i == 12) { mh = max(mh - 1u, CH_MH * 8u); PINS(mh); }
            if constexpr (i == 13) { rd = rd >> 14 == 1u ? lost : 0u; PINS(rd); }
            if constexpr (i == 14) { mw += rd; mh += lost - rd; PINS(mw); PINS(mh); }
            if constexpr (i == 15) { ok[M] = done ? 1u : (ok[M] & 1u) << 1 | ok[M] >> 2; PINS(ok[M]); }
            if constexpr (i == 16) { dh = (hav + mh) / 8u + (sn[1] < 3 ? CH_VIS + CH_PRE : CH_VIS); PINS(dh); }
            if constexpr (i == 17) { dn = (wav + mw) / 8u + (sn[1] < 3 ? CH_VIS + CH_PRW : CH_VIS); PINS(dn); }
            if constexpr (i == 18) { nt = dw[L] + dh + (u32)max((int)((u32)clk + CH_WX - t), 0); PINS(nt); }
            if constexpr (i == 19) { dh += dn; dw[L] = dn; sn[M] += (int)done; PINS(dh); PINS(sn[M]); }
            if constexpr (i == 20) { rd = tn - t + (dh >> 1); PINS(rd); }
            if constexpr (i == 21) { lost = (u32)(sc - L) < NSEG - 1 - L ? CH_RTY : 0u; PINS(lost); nt += ok[L] != 1u ? lost : 0u; PINS(nt); }
            if constexpr (i == 22) { nt = t + (u32)max((int)nt, (int)rd); PINS(nt); }
            if constexpr (i == 23) { hw.x = nt; hw.y = dh; PIN(hw.x); PIN(hw.y); }
            if constexpr (i == 24) { hw.z = nt + dn; hw.w = tm0; PIN(hw); }
            if constexpr (i == 25) st16(rs, hso + L * MB, hw);
        };
        {
#define WT(k) do {} while (0)
            v4u d[8];
            f32x4 acc[5] = {};
            done = uu(pst) ^ 1u; PINS(done);
            za = pmp | pst << 31;
            *(LAS u32*)(sw + L * 4096) = vo >> 31;
            mp = mpo + (L * MB + (u32)((sc + 1) & 3) * 65536u); pmp = mpo + (L * MB + (u32)(sc & 3) * 65536u);
            asm volatile("" : "+v"(acc[0]), "+v"(acc[1]), "+v"(acc[2]), "+v"(acc[3]), "+v"(acc[4]), "+v"(mp), "+v"(pmp), "+v"(za));
            {
                u32 tmp;
                asm volatile(WAIT_TXT("%1", "%2") "buffer_load_dwordx4 %0, %3, %4, %5 offen sc1\n\ts_waitcnt lgkmcnt(0)"
                             : "=&v"(d[0]), "=&s"(tmp) : "s"(uu(t - 4u)), "v"(vo), "s"(srd), "s"(oo[0]) : "vcc", "scc", "memory");
                PINS(clk);
                WT(0);
            }
            SFOR(i, 7)
                asm volatile("buffer_load_dwordx4 %0, %1, %2, %3 offen sc1" : "=v"(d[i + 1]) : "v"(vo), "s"(srd), "s"(oo[i + 1]) : "memory");
                SCHED();
                static_for<(25 * i) / 7, (25 * (i + 1)) / 7>(books);
                SCHED();
            SEND
            WT(1);
            float f0[5], f1[5]; unsigned long long bad;
#define WSW(o_) asm volatile("v_permlane32_swap_b32 %0, %1" : "+v"(f0[o_]), "+v"(f1[o_]))
#define WAD(o_) asm volatile("v_add_f32_dpp %0, %1, %0 row_ror:8 row_mask:0xf bank_mask:0xf" : "+v"(f0[o_]) : "v"(f1[o_]))
#define WFO(o_) do { VADD(f0[o_], acc[o_][0], acc[o_][2]); VADD(f1[o_], acc[o_][1], acc[o_][3]); } while (0)
#define WWR(o_) *(LAS float*)(xw + L * 4096 + (o_) * 128) = f0[o_]
            u32 th, per, so;
            auto next = [&](auto ic_) __attribute__((always_inline)) {
                constexpr int i = decltype(ic_)::value;
                if constexpr (i == 0) { so = (u32)(sn[M] & 3) * 0x42000u; PINS(so); }
                if constexpr (i == 1) { so = (so & 0xC4000u) ^ (pol0 + M * MB); PINS(so); }
                if constexpr (i == 2) { so = sn[M] + 1 < NSEG ? so : OOB; PINS(so); }
                if constexpr (i == 4) { vo = l16 + so; PIN(vo); }
                if constexpr (i == 5 && !L) { fin = (u32)(sn[0] + sn[1] + 2); PINS(fin); }
                if constexpr (i == 6 && L == 0) { fin = (fin | uu(left)) >> 7; PIN(fin); }
                if constexpr (i == 6 && L == 1) { cnt += 1u; PIN(cnt); left = cnt >> 9; PIN(left); }
                if constexpr (i == 7) { PIN(hv); th = rdl(hv, 0); PINS(th); }
                if constexpr (i == 8) { per = rdl(hv, 1); PINS(per); so = th - t; PINS(so); }
                if constexpr (i == 9) { per = (int)so < 20 ? per : 0u; PINS(per); }
                if constexpr (i == 10) { th += per; PINS(th); if constexpr (!L) { per = rdl(hv, 3); PINS(per); } }
                if constexpr (i == 11) { tp = t; t = lead ? tn : th; PINS(t); }
                if constexpr (i == 12) { if constexpr (!L) { fin |= per; PIN(fin); } tn = nt; }
            };
            auto slot = [&](auto ic_) __attribute__((always_inline)) {
                constexpr int i = decltype(ic_)::value;
                constexpr int NL = WO_NL, I0 = 40 - 5 * NL, pc = i < I0 ? i / 5 : 8 - NL + (i - I0) % NL, oc = i < I0 ? i % 5 : (i - I0) / NL;
                f32x4& ac = acc[oc]; v4u& dp = d[pc]; v8u& wp = wo[pc][oc];
                if constexpr (oc == 0) {
                    if constexpr (pc * 5 < 32) asm volatile("s_waitcnt vmcnt(%4)\n\tv_smfmac_f32_16x16x64_bf16 %0, %1, %2, %3" : "+v"(ac), "+v"(dp) : "a"(wp), "v"(idx), "i"(pc < 2 ? 7 - pc : 9 - pc));
                    else asm volatile("s_waitcnt vmcnt(%4)\n\tv_smfmac_f32_16x16x64_bf16 %0, %1, %2, %3" : "+v"(ac), "+v"(dp) : "v"(wp), "v"(idx), "i"(9 - pc));
                } else if constexpr (pc * 5 + oc < 32) SMF_A(ac, dp, wp); else SMF_V(ac, dp, wp);
                SCHED();
                if constexpr (i == 0) WT(2);
                if constexpr (i < 25 && oc == 0) LGK(15);
                if constexpr (i == 6) { u32& h_ = hv; (void)&srd; LD4A(h_, hvo[L], 0); }
                if constexpr (i == 8) books(IC<25>{});
                if constexpr (i >= 11 && i < 19 && i != 15) next(IC<i - 11 - (i > 15)>{});
                if constexpr (i == 20) WT(4);
                constexpr int e = i - (I0 + NL + 1), o = e < 0 ? 0 : e / 3;
                if constexpr (e >= 0 && e % 3 == 0) { CADD(f0[o], acc[o][0], acc[o][2]); if constexpr (o > 0) WAD(o - 1); }
                if constexpr (e >= 0 && e % 3 == 1) { if constexpr (o > 0) { WWR(o - 1); SCHED(); } CADD(f1[o], acc[o][1], acc[o][3]); }
                if constexpr (e >= 0 && e % 3 == 2) {
                    if constexpr (o == 0) asm volatile("v_cmp_u_f32 %0, %1, %2" : "=s"(bad) : "v"(f0[0]), "v"(f1[0]));
                    else LGK(15);
                    WSW(o);
                }
                SCHED();
            };
            WT(3);
            static_for<0, 40>(slot);
            WT(5);
            static_assert(WO_NL == 3, "tail");
            if (__builtin_expect(bad != 0ull, 0)) {
                bool miss = false;
#pragma unroll
                for (int i = 0; i < 8; ++i) miss |= d[i].x == PSENT;
                if (all_(!miss)) {
#pragma unroll
                    for (int i = 0; i < 8; ++i) miss |= d[i].y == PSENT || d[i].z == PSENT || d[i].w == PSENT;
                }
                if (miss) *(LAS u32*)(sw + L * 4096) = 1u;
            }
            asm volatile("v_permlane32_swap_b32 %0, %1\n\ts_nop 1\n\tv_add_f32_dpp %0, %1, %0 row_ror:8 row_mask:0xf bank_mask:0xf\n\tv_add_f32 %2, %4, %6\n\tv_add_f32 %3, %5, %7"
                         : "+v"(f0[3]), "+v"(f1[3]), "=&v"(f0[4]), "=&v"(f1[4]) : "v"(acc[4][0]), "v"(acc[4][1]), "v"(acc[4][2]), "v"(acc[4][3]));
            WWR(3); SCHED();
            asm volatile("s_nop 0\n\tv_permlane32_swap_b32 %0, %1\n\ts_nop 1\n\tv_add_f32_dpp %0, %1, %0 row_ror:8 row_mask:0xf bank_mask:0xf" : "+v"(f0[4]), "+v"(f1[4]));
            WWR(4);
            WT(6);
            f32x2 a, b, c, e; u32 mq; v4u sv;
            asm volatile("s_waitcnt lgkmcnt(0)\n\ts_barrier\n\tds_read_b128 %4, %6 offset:%7+2560\n\t"
                         "ds_read_b64 %0, %5 offset:%7\n\tds_read_b64 %1, %5 offset:%7+640\n\tds_read_b64 %2, %5 offset:%7+1280\n\tds_read_b64 %3, %5 offset:%7+1920"
                         : "=&v"(a), "=&v"(b), "=&v"(c), "=&v"(e), "=&v"(sv) : "v"(xr), "v"(zero), "i"(L * 4096) : "memory");
            asm volatile("s_waitcnt lgkmcnt(4)\n\tv_or_b32 %4, %6, %7\n\tv_or3_b32 %4, %4, %8, %9\n\tv_lshl_or_b32 %5, %4, 31, %10\n\t"
                         "s_waitcnt lgkmcnt(2)\n\tv_pk_add_f32 %0, %0, %1\n\ts_waitcnt lgkmcnt(0)\n\tv_pk_add_f32 %2, %2, %3\n\ts_nop 0\n\tv_pk_add_f32 %0, %0, %2"
                         : "+v"(a), "+v"(b), "+v"(c), "+v"(e), "=&v"(pst), "=&v"(mq) : "v"(sv.x), "v"(sv.y), "v"(sv.z), "v"(sv.w), "v"(mp) : "memory");
            st4s(rs, mq, 0, pkv(a));
            SCHED();
            asm volatile("s_memrealtime %0" : "=s"(clk));
            WT(7);
            st4s(rs, za, 0, psent);
            VMW(3); SCHED();
            static_for<7, 13>(next);
        }
    };
    do {
        turn(IC<1>{});
        turn(IC<0>{});
    } while (uu(fin) == 0u);
    if (sn[0] + 1 < NSEG || sn[1] + 1 < NSEG) st4s(rs, tid == 0 ? foff(F_ERR) : OOB, 0, 1u);
    if (w == 0) LST(1900 + j, rt32());
    LDUMP(w == 0, 224 + j);
}

__global__ void __launch_bounds__(256, 1) k_main(Params p) {
    lptr lds = (lptr)(u32)16 - 16;
    if (blockIdx.x == 0) LST(256, rt32());
    unsigned long long pc; int len;
    asm volatile("s_getpc_b64 %0\n.Lk_main_here:\n\ts_mov_b32 %1, .Lk_end_here-.Lk_main_here" : "=s"(pc), "=s"(len));
    const rsrc_t rs = mk_rsrc(p.ws, W_END);
    const v2u e0 = ld8s(rs, threadIdx.x == 0 ? foff(F_EPOCH) : OOB, 0);
    const v4u code = __builtin_amdgcn_raw_buffer_load_b128(mk_rsrc((const void*)(pc & ~15ull), (u32)max(len, 0)), (((blockIdx.x >> 3) & 31u) * 256u + threadIdx.x) * 16u, 0, 0);
    CFENCE();
    auto conv_id = [](int role) {
        const int x = role >> 5, i = role & 31, no = x < 5 ? 5 : 10, og = x < 5 ? x - 3 : 2 * (x - 4) + (i >= 5), r = (x - 3) * 5 + i - no, q = r / 3;
        return x < 3 || i >= no + 5 || r >= 24 ? NCV : i < no ? ((og & 1) ? (og >> 1) * 5 + i % 5 : NCV) : r % 3 == 0 ? 20 + (q & 1) * 20 + (q >> 1) : 16 + (r % 3) * 8 + q;
    };
    int role = (int)(((u32)__builtin_amdgcn_s_getreg(20 | (3 << 11)) & 7u) * 32u + (blockIdx.x >> 3));
    Early e;
    v4u rv;
#pragma unroll 1
    for (int n = 0;; ++n) {
        conv_early(p, rs, conv_id(role), e);
        if (n) break;
        CFENCE();
        if (threadIdx.x == 0) {
            GAS unsigned long long* slots = (GAS unsigned long long*)(p.ws + W_CTRL + F_SLOT * 128);
            const unsigned long long tag = (unsigned long long)e0.y << 32 | (e0.x + 1u);
            u32 mine = (u32)role;
#pragma unroll 1
            for (int k = 0; k < 256 && __hip_atomic_exchange(slots + mine * 16, tag, __ATOMIC_RELAXED, __HIP_MEMORY_SCOPE_AGENT) == tag; ++k) mine = (mine + 1u) & 255u;
            lds_w16(lds, (v4u){mine, e0.x, e0.y, 0u});
        }
        BAR();
        rv = lds_r16(lds);
        BAR();
        if (uni((int)rv.x) == role) break;
        role = uni((int)rv.x);
    }
    const int x = role >> 5, i = role & 31, w = uni(threadIdx.x >> 6), no = x < 5 ? 5 : 10, nr = x < 7 ? 5 : 4, j = conv_id(role);
    const v2u ep = {uu(rv.y), uu(rv.z)};
    asm volatile("" :: "v"(code));
    if (x == 1) { role_wo(p, lds, i, ep); __builtin_amdgcn_endpgm(); }
    if (x < 3) { role_head(p, lds, i >> 4, x * 8 + (i & 15), ep); __builtin_amdgcn_endpgm(); }
    if (i >= no + nr) { role_qkv(p, lds, (x < 5 ? (x - 3) * 22 : 44 + (x - 5) * 17) + i - no - nr, ep); __builtin_amdgcn_endpgm(); }
    if (j < NCV) conv_stage(p, rs, lds, j, ep, e);
    if (i >= no) {
        if (j >= 24 && j < NPRE) role_row(p, lds, (j - 24) * 4 + w, ep);
        __builtin_amdgcn_endpgm();
    }
    const int og = x < 5 ? x - 3 : 2 * (x - 4) + (i >= 5);
    role_out(p, lds, og & 1, og >> 1, i % 5, ep);
}
__global__ void k_end() { asm volatile(".Lk_end_here:"); }

extern "C" void kernel_launch(void* const* d_in, const int* in_sizes, int n_in, void* d_out, int out_size, void* d_ws, size_t ws_size,
                              hipStream_t stream) {
    Params p;
    p.x = (const u16*)d_in[0]; p.mem0 = (const u16*)d_in[1]; p.Wq = (const u16*)d_in[2]; p.Wk = (const u16*)d_in[3];
    p.Wv = (const u16*)d_in[4]; p.Wo = (const u16*)d_in[5];
    p.bk = (const u16*)d_in[8]; p.bv = (const u16*)d_in[9]; p.shift = (const u16*)d_in[10]; p.ssmk = (const u16*)d_in[11]; p.ssmD = (const u16*)d_in[12];
    p.out = (u16*)d_out;
    p.ws = (unsigned char*)(((size_t)d_ws + 2 * MB - 1) & ~(size_t)(2 * MB - 1));
    (void)hipFuncSetAttribute((const void*)k_main, hipFuncAttributeMaxDynamicSharedMemorySize, LDS_BYTES);
    hipLaunchKernelGGL(k_main, dim3(256), dim3(256), LDS_BYTES, stream, p);
}
```

```cpp
#include <hip/hip_runtime.h>
#include <hip/hip_bf16.h>

typedef unsigned short u16;
typedef unsigned int u32;
typedef u32 v2u __attribute__((ext_vector_type(2)));
typedef u32 v4u __attribute__((ext_vector_type(4)));
typedef u32 v8u __attribute__((ext_vector_type(8)));
typedef float f32x2 __attribute__((ext_vector_type(2)));
typedef float f32x4 __attribute__((ext_vector_type(4)));
typedef float f32x16 __attribute__((ext_vector_type(16)));
typedef __bf16 bf16x2 __attribute__((ext_vector_type(2)));
typedef __bf16 bf16x8 __attribute__((ext_vector_type(8)));
typedef short s16x4 __attribute__((ext_vector_type(4)));
#define LAS __attribute__((address_space(3)))
#define GAS __attribute__((address_space(1)))
typedef LAS unsigned char* lptr;
#define DEV __device__ __forceinline__

constexpr int SEQ = 2048, DM = 640, NH = 32, DH = 128, NSEG = 64, NQ = NH * DH;

constexpr u32 MB = 1u << 20;
constexpr u32 W_MEM0 = 2 * MB;
constexpr u32 W_CTRL = 4 * MB;
constexpr u32 CTRL_BYTES = 128 * 1024;
constexpr u32 ACT = SEQ * DM * 2;
constexpr u32 W_H1 = 5 * MB, W_Y = W_H1 + ACT, W_H2 = W_Y + ACT, W_X2 = W_H2 + ACT;
constexpr u32 W_KQ = 16 * MB;
constexpr u32 W_VO = W_KQ + 64 * MB;
constexpr u32 W_P = W_VO + 64 * MB;
constexpr u32 P_ITEM = 16 * 4 * DM * 4;
constexpr u32 W_KVT = W_P + 4 * NSEG * P_ITEM;
constexpr u32 W_SP = W_KVT + ACT;
constexpr u32 W_END = W_KVT + 3 * MB;
static_assert((size_t)W_END + 2 * MB <= 256ull * MB, "workspace");
constexpr u32 F_ERR = 0, F_QKV = 16, F_O = F_QKV + 192, F_CLR = F_O + 160,
              F_EPOCH = F_CLR + 32, F_SLOT = F_EPOCH + 1, F_PRE = F_SLOT + 256, F_N = F_PRE + 288;
constexpr int NCV = 44, NPRE = 40;
static_assert(F_N * 128 <= CTRL_BYTES, "ctrl");
DEV u32 foff(u32 idx) { return W_CTRL + idx * 128u; }
DEV u32 fp_off(u32 pol, u32 idx) { return (pol ^ 8192u) + 40u * 32768u + idx * 128u; }
DEV u32 fh_off(u32 pol, u32 idx) { return (pol ^ 8192u) + 41u * 32768u + idx * 128u; }

constexpr u32 OOB = 0x80000000u;
constexpr u32 PSENT = 0x7F817F81u;
constexpr int SPIN_CAP = 40000;
constexpr int LDS_BYTES = 160 * 1024;

#define LDUMP(cond, id) do {} while (0)
#define LMX(cond, k, s_, v) do {} while (0)
#define LS3(cond, slot, idx) do {} while (0)
#define LST(idx, v) do {} while (0)
#define LS2(cond, idx) do {} while (0)
struct Params {
    const u16 *x, *mem0, *Wq, *Wk, *Wv, *Wo, *bk, *bv, *shift, *ssmk, *ssmD;
    u16* out;
    unsigned char* ws;
};

#define INAUX 2
#define INNT "nt"
typedef __amdgpu_buffer_rsrc_t rsrc_t;
DEV rsrc_t mk_rsrc(const void* p, u32 bytes) { return __builtin_amdgcn_make_buffer_rsrc((void*)p, 0, bytes, 0x00020000); }
DEV v4u ld16s(rsrc_t r, u32 off, u32 so) { return __builtin_amdgcn_raw_buffer_load_b128(r, off, so, 16); }
DEV v2u ld8s(rsrc_t r, u32 off, u32 so) { return __builtin_amdgcn_raw_buffer_load_b64(r, off, so, 16); }
DEV v4u ldin16(rsrc_t r, u32 off, u32 so) { return __builtin_amdgcn_raw_buffer_load_b128(r, off, so, INAUX); }
DEV v2u ldin8(rsrc_t r, u32 off, u32 so) { return __builtin_amdgcn_raw_buffer_load_b64(r, off, so, INAUX); }
DEV u32 ld4(rsrc_t r, u32 off) { return __builtin_amdgcn_raw_buffer_load_b32(r, off, 0, 16); }
DEV void st16(rsrc_t r, u32 off, v4u v) { __builtin_amdgcn_raw_buffer_store_b128(v, r, off, 0, 16); }
DEV void st8s(rsrc_t r, u32 off, u32 so, v2u v) { __builtin_amdgcn_raw_buffer_store_b64(v, r, off, so, 16); }
DEV void st4s(rsrc_t r, u32 off, u32 so, u32 v) { __builtin_amdgcn_raw_buffer_store_b32(v, r, off, so, 16); }
#define DRAIN() asm volatile("s_waitcnt vmcnt(0)" ::: "memory")
#define VMW(n) asm volatile("s_waitcnt vmcnt(%0)" :: "i"(n) : "memory")
#define BAR() asm volatile("s_waitcnt lgkmcnt(0)\n\ts_barrier" ::: "memory")
#define SCHED() __builtin_amdgcn_sched_barrier(0)
#define CFENCE() asm volatile("" ::: "memory")
#define NOPS(n) asm volatile("s_nop %0" :: "i"(n))
#define PIN(x) asm volatile("" : "+v"(x))
#define PINS(x) asm volatile("" : "+s"(x))

template <int V> struct IC { static constexpr int value = V; };
template <int I, int N, class F> DEV void static_for(F&& f) { if constexpr (I < N) { f(IC<I>{}); static_for<I + 1, N>(f); } }
#define SFOR(var, n) static_for<0, n>([&](auto ic_) __attribute__((always_inline)) { constexpr int var = decltype(ic_)::value;
#define SEND });

DEV u32 pk(float a, float b) { bf16x2 v = {(__bf16)a, (__bf16)b}; return __builtin_bit_cast(u32, v); }
DEV float lo_f(u32 u) { return __builtin_bit_cast(float, u << 16); }
DEV float hi_f(u32 u) { return __builtin_bit_cast(float, u & 0xffff0000u); }
DEV f32x2 up2(u32 u) { return (f32x2){lo_f(u), hi_f(u)}; }
DEV u32 pk2(f32x2 v) { return pk(v.x, v.y); }
DEV float rbf(float a) { return (float)(__bf16)a; }
DEV v4u lds_r16(lptr p) { return *(LAS v4u*)p; }
DEV void lds_w16(lptr p, v4u v) { *(LAS v4u*)p = v; }
DEV v2u lds_tr(lptr p) { return __builtin_bit_cast(v2u, __builtin_amdgcn_ds_read_tr16_b64_v4i16((LAS s16x4*)p)); }
DEV v4u tr2(lptr p0, lptr p1) { const v2u a = lds_tr(p0), b = lds_tr(p1); return (v4u){a.x, a.y, b.x, b.y}; }
DEV v8u cat8(v4u a, v4u b) { return __builtin_shufflevector(a, b, 0, 1, 2, 3, 4, 5, 6, 7); }
DEV f32x4 mfma16(v4u a, v4u b, f32x4 c) { return __builtin_amdgcn_mfma_f32_16x16x32_bf16(__builtin_bit_cast(bf16x8, a), __builtin_bit_cast(bf16x8, b), c, 0, 0, 0); }
DEV f32x16 mfma32(v4u a, v4u b, f32x16 c) { return __builtin_amdgcn_mfma_f32_32x32x16_bf16(__builtin_bit_cast(bf16x8, a), __builtin_bit_cast(bf16x8, b), c, 0, 0, 0); }

DEV float f_(u32 u) { return __builtin_bit_cast(float, u); }
DEV u32 u_(float f) { return __builtin_bit_cast(u32, f); }
template <int CTRL> DEV float dpp(float v) { return f_((u32)__builtin_amdgcn_update_dpp(0, (int)u_(v), CTRL, 0xf, 0xf, false)); }
DEV float max16(float v) { auto r = __builtin_amdgcn_permlane16_swap(u_(v), u_(v), false, false); return fmaxf(f_(r[0]), f_(r[1])); }
DEV float max32(float v) { auto r = __builtin_amdgcn_permlane32_swap(u_(v), u_(v), false, false); return fmaxf(f_(r[0]), f_(r[1])); }
DEV float add16(float v) { auto r = __builtin_amdgcn_permlane16_swap(u_(v), u_(v), false, false); return f_(r[0]) + f_(r[1]); }
DEV float add32(float v) { auto r = __builtin_amdgcn_permlane32_swap(u_(v), u_(v), false, false); return f_(r[0]) + f_(r[1]); }
DEV float wave_sum(float v) {
    v += dpp<0xB1>(v);
    v += dpp<0x4E>(v);
    v += dpp<0x124>(v);
    v += dpp<0x128>(v);
    return add32(add16(v));
}
DEV bool all_(bool c) { return __builtin_amdgcn_ballot_w64(!c) == 0ull; }
DEV int uni(int v) { return __builtin_amdgcn_readfirstlane(v); }
DEV u32 uu(u32 v) { return (u32)__builtin_amdgcn_readfirstlane((int)v); }
DEV u32 rdl(u32 v, int l) { return (u32)__builtin_amdgcn_readlane((int)v, l); }
DEV u32 rt32() { return (u32)__builtin_amdgcn_s_memrealtime(); }

#define WSL 2
DEV void wait_flags(rsrc_t rs, u32 off, u32 want, bool& dead) {
    if (dead) return;
#pragma unroll 1
    for (int n = 0;; ++n) {
        CFENCE();
        const u32 v = ld4(rs, off);
        if (all_(off >= OOB || v >= want)) break;
        __builtin_amdgcn_s_sleep(WSL);
        if ((n & 63) == 63) { if (uni((int)ld4(rs, foff(F_ERR))) != 0) { dead = true; break; } }
        if (n > SPIN_CAP) { st4s(rs, foff(F_ERR), 0, 1u); dead = true; break; }
    }
    CFENCE();
}

DEV void pre_post(rsrc_t rs, int idx, u32 stage, v2u ep) { st8s(rs, threadIdx.x == 0 ? foff(F_PRE + idx) : OOB, 0, (v2u){ep.x + stage, ep.y}); }
DEV void pre_poll(rsrc_t rs, int first, int n, u32 stage, v2u ep) {
    const int lane = threadIdx.x & 63;
#pragma unroll 1
    for (int it = 0; it < SPIN_CAP; ++it) {
        CFENCE();
        const v2u a = ld8s(rs, foff(F_PRE + first + min(lane, n - 1)), 0), b = ld8s(rs, foff(F_PRE + first + min(lane + 64, n - 1)), 0);
        if (all_(a.y == ep.y && b.y == ep.y && max(a.x - ep.x - stage, b.x - ep.x - stage) < 8u - stage)) break;
        __builtin_amdgcn_s_sleep(2);
    }
    CFENCE();
}
DEV void pre_wait(rsrc_t rs, int first, int n, u32 stage, v2u ep) {
    if (uni(threadIdx.x >> 6) == 0) pre_poll(rs, first, n, stage, ep);
    BAR();
}
#define POLX 0u
DEV u32 pol_wait(rsrc_t rs, v2u ep) {
    const int lane = threadIdx.x & 63;
    v4u a;
#pragma unroll 1
    for (int it = 0; it < SPIN_CAP; ++it) {
        CFENCE();
        a = ld16s(rs, foff(F_PRE + (lane < NPRE || lane >= 48 ? lane : 0)), 0);
        const v2u b = ld8s(rs, foff(F_PRE + 256 + min(lane, 19)), 0);
        if (all_(a.y == ep.y && b.y == ep.y && max(a.x - ep.x - 1u, b.x - ep.x - 1u) < 7u)) break;
        __builtin_amdgcn_s_sleep(2);
    }
    CFENCE();
    return uu((wave_sum(lane >= 48 ? (float)(int)a.z : 0.f) >= 0.f ? 8192u : 0u) + (wave_sum(lane >= 48 ? (float)(int)a.w : 0.f) >= 0.f ? 16384u : 0u)) ^ POLX;
}
#define PROBE_C 7
#define PROBE_T 620
DEV v2u pol_probe(rsrc_t rs, int id) {
    const int lane = threadIdx.x & 63;
    const u32 off0 = (u32)id * 32768u, tend = rt32() + PROBE_T;
    st4s(rs, lane < 32 ? off0 + (u32)((lane >> 3) * 8192 + (lane & 7) * 256) : OOB, 0, PSENT); DRAIN();
    int d = 1 << 16, calm = 0;
#pragma unroll 1
    for (int q = 0; q < 32 && ((q & 3) || (calm < PROBE_C && (int)(rt32() - tend) < 0)); ++q) {
        const int kind = (q ^ (q >> 2) ^ id) & 3;
        const unsigned long long t0 = __builtin_amdgcn_s_memtime();
        const u32 v = ld4(rs, lane == 0 ? off0 + (u32)(kind * 8192 + (q >> 2) * 256) : OOB);
        asm volatile("s_waitcnt vmcnt(0)" :: "v"(v) : "memory");
        const int dt = (int)(__builtin_amdgcn_s_memtime() - t0);
        d = lane == kind ? min(d, dt) : d;
        calm += dt < 1100;
    }
    const int d0 = (int)rdl(d, 0), d1 = (int)rdl(d, 1), d2 = (int)rdl(d, 2), d3 = (int)rdl(d, 3), a = min(d0, d2) - min(d1, d3);
    const int b0 = min(max(d0 - d2, -80), 80), b1 = min(max(d1 - d3, -80), 80);
    const u32 xcc = (u32)__builtin_amdgcn_s_getreg(20 | (3 << 11));
    return (v2u){(u32)min(max((xcc & 4u) ? -a : a, -250), 250), (u32)((xcc & 1u) ? b0 + b1 : a > 0 ? b0 - b1 : b1 - b0)};
}
DEV f32x2 ln_stats(const f32x2 (&x)[5]) {
    const float c = f_(uu(u_(x[0].x)));
    f32x2 d[5];
#pragma unroll
    for (int i = 0; i < 5; ++i) d[i] = x[i] - c;
    const f32x2 s2 = (d[0] + d[1]) + (d[2] + d[3]) + d[4], q2 = (d[0] * d[0] + d[1] * d[1]) + (d[2] * d[2] + d[3] * d[3] + d[4] * d[4]);
    float a = s2.x + s2.y, b = q2.x + q2.y, a2, b2;
#define LN_DPP(m) "v_add_f32_dpp %0, %0, %0 " m " row_mask:0xf bank_mask:0xf\n\tv_add_f32_dpp %1, %1, %1 " m " row_mask:0xf bank_mask:0xf\n\ts_nop 0\n\t"
    asm("s_nop 1\n\t" LN_DPP("quad_perm:[1,0,3,2]") LN_DPP("quad_perm:[2,3,0,1]") LN_DPP("row_ror:4")
        "v_add_f32_dpp %0, %0, %0 row_ror:8 row_mask:0xf bank_mask:0xf\n\tv_add_f32_dpp %1, %1, %1 row_ror:8 row_mask:0xf bank_mask:0xf\n\ts_nop 1\n\t"
        "v_permlane16_swap_b32 %0, %1\n\tv_add_f32 %0, %0, %1\n\tv_mov_b32 %1, %0\n\ts_nop 1\n\tv_permlane32_swap_b32 %0, %1\n\tv_add_f32 %0, %0, %1\n\ts_nop 0\n\t"
        "v_readlane_b32 %2, %0, 0\n\tv_readlane_b32 %3, %0, 16\n\ts_nop 1"
        : "+v"(a), "+v"(b), "=s"(a2), "=s"(b2));
    const float m = a2 * (1.0f / DM), var = fmaxf(b2 * (1.0f / DM) - m * m, 0.f);
    const float sd = rbf(__builtin_amdgcn_sqrtf(rbf(rbf(var) + 1e-5f)));
    return (f32x2){rbf(c + m), __builtin_amdgcn_rcpf(sd)};
}
DEV u32 pkv(f32x2 v) { u32 d; asm("v_cvt_pk_bf16_f32 %0, %1, %2" : "=v"(d) : "v"(v.x), "v"(v.y)); return d; }
DEV void store_h(rsrc_t rs, u32 base, int row, const u32 (&hp)[5]) {
    const u32 lane = threadIdx.x & 63;
    const u32 sw = (u32)(row & 15) * 16u, o8 = (lane >> 5) * 8192u + (((lane & 31u) * 8u) ^ sw), hb = base + (u32)(row >> 5) * (32 * DM * 2) + (u32)(row & 31) * 256u;
    st8s(rs, o8, hb, (v2u){hp[0], hp[1]});
    st8s(rs, o8, hb + 16384, (v2u){hp[2], hp[3]});
    st4s(rs, (lane * 4) ^ sw, hb + 32768, hp[4]);
}

#define DMA16(ldsb, vo, so) asm volatile("s_mov_b32 m0, %0\n\ts_nop 0\n\tbuffer_load_dwordx4 %1, %2, %3 offen sc1 lds" :: "s"(ldsb), "v"(vo), "s"(srd), "s"(so) : "memory")
#define DMA16I(ldsb, lo, vo, so, go) do { u32 so_; asm volatile("s_add_u32 m0, %1, %5\n\ts_add_u32 %0, %4, %6\n\tbuffer_load_dwordx4 %2, %3, %0 offen sc1 lds" \
                         : "=&s"(so_) : "s"(ldsb), "v"(vo), "s"(srd), "s"(so), "i"(lo), "i"(go) : "memory", "scc"); } while (0)
#define DMA32(ldsb, vo, so) asm volatile("s_mov_b32 m0, %0\n\ts_nop 0\n\tbuffer_load_dwordx4 %1, %2, %3 offen sc1 lds\n\tbuffer_load_dwordx4 %1, %2, %3 offen offset:1024 sc1 lds" \
                         :: "s"(ldsb), "v"(vo), "s"(srd), "s"(so) : "memory")
#define DMAIN(sr, ldsb, vo, so) asm volatile("s_mov_b32 m0, %0\n\ts_nop 0\n\tbuffer_load_dwordx4 %1, %2, %3 offen lds" :: "s"(ldsb), "v"(vo), "s"(sr), "s"(so) : "memory")
DEV v4u mk_srd(const void* q, u32 bytes) { return (v4u){uu((u32)(size_t)q), uu((u32)((size_t)q >> 32) & 0xffffu), bytes, 0x00020000u}; }
#define DMANT(sr, ldsb, vo, so) asm volatile("s_mov_b32 m0, %0\n\ts_nop 0\n\tbuffer_load_dwordx4 %1, %2, %3 offen " INNT " lds" :: "s"(ldsb), "v"(vo), "s"(sr), "s"(so) : "memory")
#define AHEAD(sr, vo, so) asm volatile("buffer_load_dwordx4 %0, %1, %2, %3 offen" : "+v"(junk) : "v"(vo), "s"(sr), "s"(so) : "memory")
#define MK_SRD() const v4u srd = mk_srd(p.ws, W_END)

constexpr int CV_W = 40960;
template <bool B> struct CvL {
    static constexpr int NF = B ? 5 : 3, NK = B ? 4 : 2, KP = 544 + NK * 256, CH = NF * 1024 + 4 * KP;
};
constexpr int CV_Y = 4 * CvL<false>::CH, CV_YP = 272;
static_assert(CV_Y + 32 * CV_YP <= CV_W && 2 * CvL<true>::CH <= CV_Y, "conv lds");
DEV int cv_chan(int u, int i) { return 16 * (u % 40) + 4 * i + u / 40; }
template <bool B> DEV void cv_filt(const Params& p, u32 dst, int d, int i) {
    const u32 lane = threadIdx.x & 63;
    const v4u sf = mk_srd(p.ssmk + d * SEQ, SEQ * 2);
    if (i == 0) DMANT(sf, dst, lane < 4u ? OOB : lane * 16u - 64u, 0);
    else DMANT(sf, uu(dst + i * 1024), i == CvL<B>::NF - 1 && lane >= 4u ? OOB : lane * 16u, i * 1024 - 64);
}
DEV v4u cv_kvld(rsrc_t rs, int d, int i) { return ld16s(rs, (threadIdx.x & 63) * 16u, uu(W_KVT + (u32)d * (SEQ * 2) + i * 1024)); }
template <bool B> DEV void cv_kvput(lptr F, const v4u* k) {
    const int lane = threadIdx.x & 63;
#pragma unroll
    for (int i = 0; i < CvL<B>::NK; ++i) lds_w16(F + CvL<B>::NF * 1024 + (lane & 3) * CvL<B>::KP + 512 + i * 256 + (lane >> 2) * 16, k[i]);
}
template <bool B> DEV void cv_zero(lptr F) {
    const int lane = threadIdx.x & 63;
#pragma unroll
    for (int i = 0; i < 4; ++i) lds_w16(F + (lane >> 5) * CvL<B>::CH + CvL<B>::NF * 1024 + i * CvL<B>::KP + (lane & 31) * 16, (v4u){0u, 0u, 0u, 0u});
}
#define M32V(d, a, b) asm volatile("v_mfma_f32_32x32x16_bf16 %0, %1, %2, %0" : "+v"(d) : "v"(a), "v"(b))
template <bool B, class G> DEV void cv_run(const Params& p, rsrc_t rs, lptr F, int d, u32 D, f32x4 sp, float (&y)[16], G&& gap) {
    constexpr int KP = CvL<B>::KP, FO = B ? 2048 : 0;
    const int lane = threadIdx.x & 63, n = lane & 31, hh = lane >> 5, c = lane & 3;
    lptr fa = F + 64 + (32 * hh + 16 * (c & 1) + 8 * ((lane >> 4) & 1) + 4 * (c >> 1) - (lane & 12)) * 2;
    lptr kl = F + CvL<B>::NF * 1024 + (32 + n - hh) * 16;
    v4u dg;
#pragma unroll
    for (int q = 0; q < 4; ++q) { const int k = (lane & 35) ? -1 : (n & 4) + (n >> 3); dg[q] = k == 2 * q ? D : k == 2 * q + 1 ? D << 16 : 0u; }
    v4u l[4], h[4]; v2u fc[4], fh[4];
    f32x16 acc[4];
#pragma unroll
    for (int j = 0; j < 4; ++j) { l[j] = lds_r16(kl + j * KP); if constexpr (B) h[j] = lds_r16(kl + j * KP + 512); }
#pragma unroll
    for (int j = 0; j < 2; ++j) { fc[j] = lds_tr(fa + FO - 32 * j); if constexpr (B) fh[j] = lds_tr(fa - 32 * j); }
    asm volatile("s_nop 1" : "+v"(dg));
#pragma unroll
    for (int j = 0; j < 4; ++j) asm volatile("v_mfma_f32_32x32x16_bf16 %0, %1, %2, 0" : "=&v"(acc[j]) : "v"(dg), "v"(B ? h[j] : l[j]));
    constexpr int U = B ? 2 : 4;
    kl -= 32 * U; fa -= 32;
    auto body = [&](bool first) __attribute__((always_inline)) {
#pragma unroll
        for (int z = 0; z < U; ++z) {
            const int c = 2 * (z & 1);
            const v4u f = __builtin_shufflevector(fc[c], fc[c + 1], 0, 1, 2, 3), g = __builtin_shufflevector(fh[c], fh[c + 1], 0, 1, 2, 3);
#pragma unroll
            for (int j = 0; j < 4; ++j) {
                M32V(acc[j], f, l[j]); l[j] = lds_r16(kl + j * KP + 32 * (U - z - 1));
                if (j < 2) fc[(c ^ 2) + j] = lds_tr(fa + FO + (z + 1) * 128 + 32 - 32 * j);
                if (first && j > 0) gap(5 * z + j - 1);
                SCHED();
            }
            if constexpr (B) {
#pragma unroll
                for (int j = 0; j < 4; ++j) {
                    M32V(acc[j], g, h[j]); h[j] = lds_r16(kl + j * KP + 512 + 32 * (U - z - 1));
                    if (j < 2) fh[(c ^ 2) + j] = lds_tr(fa + (z + 1) * 128 + 32 - 32 * j);
                    if (first && j > 1) gap(5 * z + j + 1);
                    SCHED();
                }
            }
        }
        kl -= 32 * U; fa += 128 * U;
    };
    if constexpr (B) body(true);
#pragma unroll 1
    for (int m = B ? 2 * U : 0; m < 32; m += 2 * U) body(false);
    asm volatile("s_nop 11" : "+v"(acc[0]), "+v"(acc[1]), "+v"(acc[2]), "+v"(acc[3]));
    const float o[3] = {acc[1][15] + acc[2][14] + acc[3][13], acc[2][15] + acc[3][14], acc[3][15]};
    if constexpr (!B) st16(rs, lane == 63 ? W_SP + (u32)d * 16u : OOB, (v4u){u_(o[0]), u_(o[1]), u_(o[2]), 0u});
#pragma unroll
    for (int i = 0; i < 16; ++i) {
        float v = acc[0][i];
#pragma unroll
        for (int j = 1; j < 4; ++j) if (j <= i) v += acc[j][i - j];
        if (i < 3) { const float e = f_((u32)__builtin_amdgcn_ds_bpermute((hh ? lane - 32 : lane + 31) * 4, (int)u_(o[i]))); v += lane == 0 ? sp[i] : e; }
        y[i] = v;
    }
}
DEV void cv_put(lptr F, int pr, const float (&y0)[16], const float (&y1)[16]) {
    const int lane = threadIdx.x & 63;
#pragma unroll
    for (int i = 0; i < 16; ++i) *(LAS u32*)(F + CV_Y + (lane & 31) * CV_YP + (lane >> 5) * 128 + i * 8 + pr * 4) = pk(y0[i], y1[i]);
}
DEV void cv_out(rsrc_t rs, lptr F, int u, int blk0) {
    const int lane = threadIdx.x & 63;
    lptr q = F + CV_Y + ((lane >> 2) & 3) * CV_YP + ((lane >> 4) * 4 + (lane & 3)) * 8;
    const u32 yo = W_Y + (u32)(((lane >> 2) * 8 + blk0) * DM + 4 * u + (lane & 3)) * 16u;
#pragma unroll
    for (int i = 0; i < 8; ++i) { lptr a = q + (i >> 1) * (8 * CV_YP) + (i & 1) * 128; st16(rs, yo + (u32)((i & 1) * 128 + (i >> 1)) * (DM * 16), tr2(a, a + 4 * CV_YP)); }
}
struct KvIn { v2u k[19], v[16]; v4u s[2]; };
template <class F> DEV void kvt_load(const Params& p, int tile, KvIn& in, F&& mid) {
    const int lane = threadIdx.x & 63;
    const rsrc_t rk = mk_rsrc(p.bk, ACT), rv = mk_rsrc(p.bv, ACT), rh = mk_rsrc(p.shift, DM * 8);
    const int row = (tile / 10) * 64 + (lane >> 4) * 16, col = (tile % 10) * 64 + (lane & 15) * 4;
    const u32 vo = (u32)(row * DM + col) * 2u, vp = row > 0 ? vo - 3 * (DM * 2) : OOB;
#pragma unroll
    for (int i = 0; i < 2; ++i) in.s[i] = ldin16(rh, (u32)col * 8u + i * 16, 0);
#pragma unroll
    for (int i = 0; i < 19; ++i) { if (i == 7) mid(); in.k[i] = i < 3 ? ldin8(rk, vp, i * (DM * 2)) : ldin8(rk, vo, (i - 3) * (DM * 2)); if (i >= 3) in.v[i - 3] = ldin8(rv, vo, (i - 3) * (DM * 2)); }
}
DEV void kvt_make(rsrc_t rs, lptr xt, int tile, const KvIn& in) {
    const int lane = threadIdx.x & 63;
#pragma unroll
    for (int c = 0; c < 2; ++c) {
        const v4u sv = in.s[c];
        const f32x2 sh[4] = {{lo_f(sv.x), lo_f(sv.z)}, {hi_f(sv.x), hi_f(sv.z)}, {lo_f(sv.y), lo_f(sv.w)}, {hi_f(sv.y), hi_f(sv.w)}};
        f32x2 kf[19];
#pragma unroll
        for (int i = 0; i < 19; ++i) kf[i] = up2(in.k[i][c]);
        u32 kv[16];
#pragma unroll
        for (int i = 0; i < 16; ++i) kv[i] = pk2(((kf[i] * sh[3] + kf[i + 1] * sh[2]) + (kf[i + 2] * sh[1] + kf[i + 3] * sh[0])) * up2(in.v[i][c]));
#pragma unroll
        for (int q = 0; q < 8; ++q) {
            const u32 sel = (q & 1) ? 0x07060302u : 0x05040100u; const u32* z = kv + (q >> 1);
            *(LAS v2u*)(xt + ((2 * c + (q & 1)) * 16 + (lane & 15)) * 144 + (lane >> 5) * 64 + (q >> 1) * 16 + ((lane >> 4) & 1) * 8) =
                (v2u){__builtin_amdgcn_perm(z[4], z[0], sel), __builtin_amdgcn_perm(z[12], z[8], sel)};
        }
    }
    const u32 o = W_KVT + (u32)(((tile % 10) * 64 + 4 * (lane >> 3)) * SEQ + (tile / 10) * 64 + (lane & 7) * 8) * 2u;
#pragma unroll
    for (int i = 0; i < 8; ++i) st16(rs, o + (u32)(32 * (i & 1) + (i >> 1)) * (SEQ * 2), lds_r16(xt + (8 * i + (lane >> 3)) * 144 + (lane & 7) * 16));
}
struct RowX { v2u a, b; u32 c; };
DEV void ln_row(rsrc_t rs, int row, const RowX& r) {
    const f32x2 x[5] = {up2(r.a.x), up2(r.a.y), up2(r.b.x), up2(r.b.y), up2(r.c)};
    const f32x2 st = ln_stats(x);
    u32 hp[5];
#pragma unroll
    for (int q = 0; q < 5; ++q) hp[q] = pk2(up2(pkv(x[q] - st.x)) * st.y);
    store_h(rs, W_H1, row, hp);
}
DEV v4u mem0_load(const Params& p, int j) {
    const u32 n = (u32)(j * 256 + threadIdx.x);
    return ldin16(mk_rsrc(p.mem0, 8 * DM * 2), n < 640u ? ((((n >> 1) & 4) + (n & 3)) * DM + (n >> 4) * 16 + (n & 4) * 2) * 2u : OOB, 0);
}
DEV void pre_flags(rsrc_t rs, int j, v4u m) {
    const u32 n = (u32)(j * 256 + threadIdx.x);
#pragma unroll
    for (int i = 0; i < 4; ++i) m[i] = m[i] == PSENT ? 0x7FC17FC1u : m[i];
    st4s(rs, n - 36u * 256u < F_CLR ? foff(n - 36u * 256u) : OOB, 0, 0u);
    st16(rs, n < 640u ? W_MEM0 + (n >> 9) * 32768u + (n & 511u) * 16u : OOB, m);
}
constexpr int KT0 = NPRE * 4, LN0 = NPRE * 8;
struct Early { RowX r[2]; v4u m; };
DEV void conv_early(const Params& p, rsrc_t rs, int j, Early& e) {
    const int lane = threadIdx.x & 63, w = uni(threadIdx.x >> 6), u = j * 4 + w;
    if (j < NPRE) {
        const rsrc_t rx = mk_rsrc(p.x, ACT);
#pragma unroll
        for (int i = 0; i < 2; ++i) {
            const u32 so = uu((u + i * KT0) * (DM * 2));
            e.r[i].a = __builtin_amdgcn_raw_buffer_load_b64(rx, lane * 8u, so, 0); e.r[i].b = __builtin_amdgcn_raw_buffer_load_b64(rx, lane * 8u + 512u, so, 0);
            e.r[i].c = __builtin_amdgcn_raw_buffer_load_b32(rx, lane * 4u + 1024u, so, 0);
        }
        e.m = mem0_load(p, j);
    }
}
DEV void conv_stage(const Params& p, rsrc_t rs, lptr lds, int j, v2u ep, Early& e) {
    const int tid = threadIdx.x, lane = tid & 63, w = uni(tid >> 6), u = j * 4 + w;
    lptr F = lds + w * CV_W;
    const u32 fb = uu(w * CV_W);
    const rsrc_t rd = mk_rsrc(p.ssmD, DM * 2);
    float y0[16], y1[16];
    KvIn in;
    if (j < NPRE) {
        constexpr int CH = CvL<false>::CH;
        if (w == 1) LST(1500 + j, rt32());
        pre_flags(rs, j, e.m);
        ln_row(rs, u, e.r[0]); ln_row(rs, u + KT0, e.r[1]);
        if (w == 1) LST(1550 + j, rt32());
        CFENCE();
        kvt_load(p, u, in, [&] { VMW(13); BAR(); pre_post(rs, j, 1u, ep); if (w == 0) LST(1450 + j, rt32()); });
        if (j == 20) LST(260, rt32());
        const u32 D = __builtin_amdgcn_raw_buffer_load_b16(rd, (u32)cv_chan(u, lane & 3) * 2u, 0, 0);
#pragma unroll 1
        for (int i = 0; i < 4; ++i) { const int d = cv_chan(u, i); cv_filt<false>(p, fb + i * CH, d, 0); cv_filt<false>(p, fb + i * CH, d, 1); cv_filt<false>(p, fb + i * CH, d, 2); }
        kvt_make(rs, F + CV_Y, u, in);
        cv_zero<false>(F); cv_zero<false>(F + 2 * CH);
        DRAIN(); BAR();
        pre_post(rs, j, 2u, ep);
        if (j == 20) LST(262, rt32());
        pre_wait(rs, 0, NPRE, 2u, ep);
        if (j == 20) LST(281, rt32());
        {
            v4u k[8];
#pragma unroll
            for (int i = 0; i < 8; ++i) k[i] = cv_kvld(rs, cv_chan(u, i >> 1), i & 1);
#pragma unroll
            for (int i = 0; i < 4; ++i) cv_kvput<false>(F + i * CH, k + 2 * i);
        }
        if (j == 20) LST(286, rt32());
#pragma unroll 1
        for (int i = 0; i < 4; i += 2) {
            cv_run<false>(p, rs, F + i * CH, cv_chan(u, i), rdl(D, i), f32x4{}, y0, [](int) {});
            cv_run<false>(p, rs, F + (i + 1) * CH, cv_chan(u, i + 1), rdl(D, i + 1), f32x4{}, y1, [](int) {});
            cv_put(F, i >> 1, y0, y1);
        }
        if (j == 20) LST(287, rt32());
        cv_out(rs, F, u, 0);
        if (j == 20) LST(263, rt32());
    } else {
        const v2u vote = pol_probe(rs, u - NPRE * 4);
        st16(rs, lane == 0 ? foff(F_PRE + 48 + u - NPRE * 4) : OOB, (v4u){ep.x + 1u, ep.y, vote.x, vote.y});
#pragma unroll 1
        for (int t = u + 128; t < 320; t += 16) { kvt_load(p, t, in, [] {}); kvt_make(rs, F, t, in); }
    }
    DRAIN(); BAR();
    pre_post(rs, j, 4u, ep);
    const int b = j < 20 ? -1 : j < 24 ? u - 80 : j >= NPRE ? u - 144 : (w & 1) ? (u >> 1) - 16 : -1, u0 = b < 32 ? b : b + 96, ue = b < 32 ? 128 : 160;
    if (b >= 0) {
        constexpr int CH = CvL<true>::CH;
        cv_zero<true>(F);
        pre_poll(rs, 64, 32, 4u, ep); pre_poll(rs, 0, NCV, 4u, ep);
        v4u k[4];
        auto fetch = [&](u32 dst, int d, int i) { if (i < 5) cv_filt<true>(p, dst, d, i); else if (i < 9) k[i - 5] = cv_kvld(rs, d, i - 5); };
#pragma unroll
        for (int i = 0; i < 9; ++i) fetch(fb, cv_chan(u0, 0), i);
        const u32 ch = (u32)cv_chan(min(u0 + 32 * (lane >> 2), 159), lane & 3);
        u32 D = __builtin_amdgcn_raw_buffer_load_b16(rd, ch * 2u, 0, 0);
        v4u sp = ld16s(rs, W_SP + ch * 16u, 0);
        asm volatile("" : "+v"(D), "+v"(sp));
#pragma unroll 1
        for (int un = u0, n = 0; un < ue; un += 32) {
            if (un == 64) { LST(282, rt32()); LST(288, __builtin_amdgcn_s_memtime()); }
#pragma unroll 1
            for (int i = 0; i < 4; i += 2, n += 2) {
                const int d1 = (int)rdl(ch, n + 1), d2 = (int)rdl(ch, n + 2);
                cv_kvput<true>(F, k);
                cv_run<true>(p, rs, F, 0, rdl(D, n), (f32x4){f_(rdl(sp.x, n)), f_(rdl(sp.y, n)), f_(rdl(sp.z, n)), 0.f}, y0, [&](int q) { fetch(fb + CH, d1, q); });
                cv_kvput<true>(F + CH, k);
                cv_run<true>(p, rs, F + CH, 326, rdl(D, n + 1), (f32x4){f_(rdl(sp.x, n + 1)), f_(rdl(sp.y, n + 1)), f_(rdl(sp.z, n + 1)), 0.f}, y1, [&](int q) { fetch(fb, d2, q); });
                cv_put(F, i >> 1, y0, y1);
            }
            cv_out(rs, F, un, 4);
            if (un == 64) { LST(283, rt32()); LST(289, __builtin_amdgcn_s_memtime()); }
        }
        DRAIN();
        st8s(rs, lane == 0 ? foff(F_PRE + 192 + b) : OOB, 0, (v2u){ep.x + 4u, ep.y});
    }
    if (j == 20) LST(284, rt32());
    if (b == 62) LST(285, rt32());
}

#define M32A(first, d, a, b) do { if (first) asm volatile("v_mfma_f32_32x32x16_bf16 %0, %1, %2, 0" : "=&v"(d) : "a"(a), "v"(b)); \
                                  else asm volatile("v_mfma_f32_32x32x16_bf16 %0, %1, %2, %0" : "+v"(d) : "a"(a), "v"(b)); } while (0)
#define M32B(first, d, a, b) do { if (first) asm volatile("v_mfma_f32_32x32x16_bf16 %0, %1, %2, 0" : "=&v"(d) : "v"(a), "a"(b)); \
                                  else asm volatile("v_mfma_f32_32x32x16_bf16 %0, %1, %2, %0" : "+v"(d) : "v"(a), "a"(b)); } while (0)
#define VCVT(d, a, b) asm volatile("v_cvt_pk_bf16_f32 %0, %1, %2" : "=v"(d) : "v"(a), "v"(b))
#define VADD(d, a, b) asm volatile("v_add_f32 %0, %1, %2" : "=v"(d) : "v"(a), "v"(b))
#define LD4A(d, vo, so) asm volatile("buffer_load_dword %0, %1, %2, %3 offen sc1" : "=v"(d) : "v"(vo), "s"(srd), "s"(so) : "memory")
DEV u32 wst_src(int lane, int ld) { return (u32)((lane >> 4) * ld + 8 * ((lane & 15) ^ ((lane >> 4) << 2))) * 2u; }
DEV u32 wst_rd(int lane, int w) { const int q4 = (lane >> 2) & 3; return (u32)(q4 * 256 + ((w ^ q4) * 64) + ((lane >> 4) & 1) * 32 + (lane & 3) * 8); }

constexpr int QK_SEL = 81920;
#define QK_LEAD 12
#define QK_SMP 29
#define QK_YB 26
#define QK_W0 2
DEV void role_qkv(const Params& p, lptr lds, int c, v2u ep) {
    const int tid = threadIdx.x, lane = tid & 63, w = uni(tid >> 6), hh = lane >> 5, r32 = lane & 31;
    const int m = c >> 5, h = c & 31;
    const rsrc_t rs = mk_rsrc(p.ws, W_END);
    v4u wt[40];
    {
        const v4u sw = mk_srd(m == 0 ? p.Wq : m == 1 ? p.Wk : p.Wv, DM * NQ * 2), sx = mk_srd(p.x, ACT);
        const u32 vo = wst_src(lane, NQ) + (u32)h * (DH * 2), x8 = (u32)lane * 8u, x4 = (u32)lane * 4u;
        RowX xr[5];
        auto wfill = [&](int i0, int n) {
#pragma unroll 1
            for (int i = i0; i < i0 + n; ++i) DMAIN(sw, uu((4 * i + w) * 1024), vo, uu((16 * i + 4 * w) * (NQ * 2)));
        };
        wfill(0, QK_W0);
#pragma unroll
        for (int i = 0; i < 5; ++i)
            asm volatile("buffer_load_dwordx2 %0, %3, %5, %6 offen\n\tbuffer_load_dwordx2 %1, %3, %5, %6 offen offset:512\n\tbuffer_load_dword %2, %4, %5, %6 offen offset:1024"
                         : "=&v"(xr[i].a), "=&v"(xr[i].b), "=&v"(xr[i].c) : "v"(x8), "v"(x4), "s"(sx), "s"(uu((LN0 + 4 * c + w + 384 * i) * (DM * 2))) : "memory");
        wfill(QK_W0, 10 - QK_W0);
        if (c == 3) LST(275, rt32());
        VMW(10 - QK_W0);
        if (c == 3) LST(276, rt32());
#pragma unroll
        for (int i = 0; i < 5; ++i) {
            asm volatile("" : "+v"(xr[i].a), "+v"(xr[i].b), "+v"(xr[i].c));
            const int row = LN0 + 4 * c + w + 384 * i;
            if (i < 4 || row < SEQ) ln_row(rs, row, xr[i]);
            wfill(10 + 6 * i, 6);
        }
        if (c == 3) LST(264, rt32());
        lptr tb = lds + hh * 2048 + wst_rd(lane, w);
        SFOR(k, 40)
            if constexpr (k == 0) { VMW(15); BAR(); }
            if constexpr (k == 28) {
                if (w == 0) pre_poll(rs, 0, NPRE, 1u, ep);
                DRAIN(); BAR();
                pre_post(rs, 96 + c, 1u, ep);
            }
            lptr q = tb + (k >> 3) * 32768 + (k & 7) * 4096; wt[k] = tr2(q, q + 1024); asm volatile("" : "+a"(wt[k]));
        SEND
    }
    if (c == 3) LST(268, rt32());
    if (w == 0) LST(1350 + c, rt32());
    bool dead = false;
    int n0, n1;
    const u32 t16 = (u32)(w * 2048 + lane * 16), wb = uu(w * 2048);
    u32 ar[16];
#pragma unroll
    for (int j = 0; j < 16; ++j) ar[j] = (u32)(r32 * 256 + ((2 * (j & 7) + hh) ^ (r32 & 15)) * 16 + (j >> 3) * 49152);
    MK_SRD();
#define XRD(y, ks) lds_r16(lds + ar[((ks) & 7) + ((ks) >= 24 ? 8 : 0)] + ((ks) >> 3) % 3 * 16384 + (y) * 8192)
#define QM0(y, b) asm volatile("s_add_u32 m0, %0, %1" :: "s"(wb), "i"((b) * 16384 + (y) * 8192) : "scc", "memory")
#define QDMA(i, vo) asm volatile("buffer_load_dwordx4 %0, %1, %2 offen offset:%3 sc1 lds" :: "v"(vo), "s"(srd), "s"(((i) >> 1) * 8192), "i"(((i) & 1) * 1024) : "memory")
    u32 h2flag = OOB;
    const u32 ol = (m == 2 ? (u32)(2 * w + (r32 >> 4)) * 1024u : (u32)((r32 >> 4) * 4 + w) * 1024u) + (u32)(hh * 16 + (r32 & 15)) * 16u
                 + (m == 2 ? W_VO : m == 1 ? W_KQ : W_KQ + 8192u) + (u32)h * 16384u;
    const u32 fo = tid < 2 ? foff(F_QKV + tid * 96 + c) : OOB;
    lptr selw = lds + QK_SEL + (w == 0 ? 0 : 64), selr = lds + QK_SEL;
    PIN(selw);
    static_assert(QK_LEAD > 8 && QK_LEAD % 2 == 0 && LN0 == 10 * 32, "X = 8 comes up, before any item of layer 1");
    static_assert(W_H2 - W_H1 == 2 * 64 * (32 * DM * 2), "h");
    auto run = [&](auto isv) __attribute__((always_inline)) {
        constexpr bool V = decltype(isv)::value;
        constexpr int YB = QK_YB;
        f32x16 ax, ay = {};
        v4u x[8], pp, pq;
        u32 xv = t16 + W_H1, ox = ol, oy = OOB, nn = 1, lt = 0, fcnt = 0, fa = OOB;
        asm volatile("" : "+v"(lt), "+v"(nn));
#pragma unroll
        for (int z = 0; z < 5; ++z) x[z] = XRD(1, 40 - YB + z);
#define QPK(r_, a1, a_, q_) do { if constexpr (V) VCVT(r_[q_], a1[4 * (a_) + 8 * ((q_) >> 1) + 2 * ((q_) & 1)], a1[4 * (a_) + 8 * ((q_) >> 1) + 2 * ((q_) & 1) + 1]); \
                                 else VCVT(r_[q_], a1[4 * (a_) + (q_)], a1[4 * (a_) + (q_) + 8]); } while (0)
#define QPK4(r_, a1, a_) do { QPK(r_, a1, a_, 0); QPK(r_, a1, a_, 1); QPK(r_, a1, a_, 2); QPK(r_, a1, a_, 3); } while (0)
        for (;;) {
            u32 ls = uu(lt), yv, fv, nx, need;
            if (ls != 0u && uu(nn) < 256u) {
                if (w == 0) { const u32 pol = pol_wait(rs, ep); h2flag = lane < 16 ? fh_off(pol, lane) : OOB; }
                pre_wait(rs, 96, 96, 1u, ep);
            }
            else if (ls != 0u) {
                n1 = (int)(uu(nn) >> 8);
                CFENCE();
                const u32 hf = h2flag + (u32)(~n1 & 1) * 2048u;
                u32 f2 = ld4(rs, hf);
                asm volatile("s_waitcnt vmcnt(0)" : "+v"(f2) :: "memory");
                *(LAS u32*)(selw + 16) = (u32)__builtin_amdgcn_ballot_w64(f2 < PSENT + (u32)n1) & 0xFFFFu;
                BAR();
                st4s(rs, fa, 0, fcnt);
                fa = OOB;
                if (n1 > NSEG) { LDUMP(w == 0, 64 + c); __builtin_amdgcn_endpgm(); }
                if (uu(*(LAS u32*)(selr + 16)) != 0u) {
                    if (w == 0) wait_flags(rs, hf, PSENT + (u32)n1, dead);
                    BAR();
                }
            }
#define QT(k) do {} while (0)
            QT(0);
            SCHED();
            QM0(0, 0); NOPS(0); QDMA(0, xv); QDMA(1, xv); QM0(0, 1);
            SFOR(z, 80)
                constexpr int ks = (z + 40 - YB) % 40, zx = z - YB;
                constexpr auto upto = [](int q) { const int d = (q - YB + 49) % 40; return q + 6 + (d == 0 || d == 2 ? 1 : d == 1 ? 2 : 0); };
                if constexpr (zx >= 0 && zx < 40) { if constexpr (V) M32B(ks == 0, ax, x[z & 7], wt[ks]); else M32A(ks == 0, ax, wt[ks], x[z & 7]); }
                else { if constexpr (V) M32B(ks == 0, ay, x[z & 7], wt[ks]); else M32A(ks == 0, ay, wt[ks], x[z & 7]); }
                if constexpr (z == 78) lt = *(LAS u32*)selr;
#pragma unroll
                for (int q = upto(z - 1); q < upto(z); ++q) x[q & 7] = XRD(q % 80 < YB || q % 80 >= YB + 40, (q + 40 - YB) % 40);
                SCHED();
                (void)&srd; (void)&wb;
                if constexpr (z < 2) QDMA(z + 2, xv);
                if constexpr (z >= 2 && z < 14 && (z & 3) == 2) QM0(0, (z + 6) >> 2);
                if constexpr (z >= 3 && z < 15 && (z & 1)) QDMA((z + 5) >> 1, xv);
                if constexpr (z == 4) { n0 = uni((int)nn); n1 = n0 >> 8; n0 &= 255; PINS(n0); PINS(n1); }
                if constexpr (z == 8) { nx = n0 < NSEG ? W_H1 + (u32)n0 * (32 * DM * 2) : OOB; PINS(nx); }
                if constexpr (z == 12) { yv = t16 + nx; PIN(yv); }
                if constexpr (zx == -6) { QT(1); asm volatile("s_waitcnt vmcnt(6) lgkmcnt(0)\n\ts_barrier" ::: "memory"); QT(2); }
                if constexpr (zx == -5) st4s(rs, fa, 0, fcnt);
                if constexpr (zx == -4 || zx == 0 || zx == 4) QM0(1, (zx + 4) >> 2);
                if constexpr (zx >= -3 && zx < 8 && (zx & 1)) QDMA((zx + 3) >> 1, yv);
                if constexpr (zx == 2 || zx == 6) { QPK(pp, ay, 0, (zx - 2) >> 1); QPK(pp, ay, 0, zx >> 1); }
                if constexpr (zx == 8 || zx == 11) { QPK(pq, ay, 1, zx & 2); QPK(pq, ay, 1, (zx & 2) + 1); }
                if constexpr (zx == 9) st16(rs, oy, pp);
                if constexpr (zx == 10) { QT(3); asm volatile("s_waitcnt vmcnt(8)\n\ts_barrier" ::: "memory"); QT(4); }
                if constexpr (zx == 12) st16(rs, oy + 512, pq);
                if constexpr (zx == 13 || zx == 17) QM0(1, (zx - 1) >> 2);
                if constexpr (zx >= 14 && zx < 22 && (zx & 1) == 0) QDMA((zx - 2) >> 1, yv);
                if constexpr (zx == 21) { fcnt = lane == 0 ? (u32)n0 : (u32)n1; fa = fo; asm volatile("" : "+v"(fcnt), "+v"(fa)); }
                if constexpr (zx == 22) { oy = (ol + (u32)n0 * (NH * 16384u)) | (yv & OOB); PIN(oy); }
                if constexpr (zx == 23) { n0 = min(n0 + 1, NSEG); PINS(n0); nx = (u32)min(NSEG, n1 + QK_LEAD); PINS(nx); }
                if constexpr (zx == 24)
                    asm volatile("s_cmp_ge_u32 %1, %0\n\ts_cselect_b32 %0, %4, %1\n\ts_addc_u32 %2, %2, 0\n\ts_cmp_lt_u32 %0, 64\n\ts_cselect_b32 %3, 0, %2"
                                 : "+s"(nx), "+s"(n0), "+s"(n1), "=&s"(need) : "s"(n1 + 64) : "scc");
                if constexpr (zx == 25) { n0 += 1 - (int)min(nx >> 6, 1u); PINS(n0); }
                if constexpr (zx == 26) { nn = (u32)(n0 | n1 << 8); PIN(nn); }
                if constexpr (zx == 27) { ls = W_H1 + (nx + (nx & 64u)) * (32 * DM * 2); PINS(ls); }
                if constexpr (zx == 28) { xv = t16 + ls; PIN(xv); need = nx == 8u ? 255u : need; PINS(need); }
                if constexpr (zx == 30) { need = need ? need + PSENT : 0u; PINS(need); }
                if constexpr (zx == 34) { asm volatile("s_waitcnt vmcnt(%0) lgkmcnt(0)\n\ts_barrier" :: "i"(QK_SMP < 34 ? 7 : 6) : "memory"); QT(5); }
                if constexpr (zx == QK_SMP) { const u32 po = (nx & 1u) * 2048u; LD4A(fv, h2flag, po); }
                if constexpr (zx == 42) QPK4(pp, ax, 0);
                if constexpr (zx == 43) st16(rs, ox, pp);
                if constexpr (zx == 44) QPK4(pq, ax, 1);
                if constexpr (zx == 45) st16(rs, ox + 512, pq);
                if constexpr (zx == 47) { ox = ol + nx * (NH * 16384u); PIN(ox); }
                if constexpr (z == 74) { asm volatile("s_waitcnt vmcnt(2)" : "+v"(fv) :: "memory"); QT(6); ls = (u32)__builtin_amdgcn_ballot_w64(fv < need) & 0xFFFFu; PINS(ls); }
                if constexpr (z == 75) { lt = ls; PIN(lt); *(LAS u32*)selw = lt; }
                if constexpr (z == 77) asm volatile("s_waitcnt lgkmcnt(2)\n\ts_barrier" ::: "memory");
                SCHED();
            SEND
        }
    };
    if (m == 2) run(IC<1>{}); else run(IC<0>{});
}

#define OU_FLG 25
#define OU_SMP 17
DEV void role_out(const Params& p, lptr lds, int L, int kq, int ng, v2u ep) {
    const int tid = threadIdx.x, lane = tid & 63, w = uni(tid >> 6), hh = lane >> 5, r32 = lane & 31;
    const rsrc_t rs = mk_rsrc(p.ws, W_END);
    v4u wo[64];
    bool dead = false;
    {
        const v4u sw = mk_srd(p.Wo, DM * NQ * 2);
        const u32 vo = wst_src(lane, DM) + (u32)ng * 256u;
        auto fill = [&](int b) {
#pragma unroll 1
            for (int i = 0; i < 8; ++i) DMAIN(sw, uu((b % 5) * 32768 + (4 * i + w) * 1024), vo, uu((kq * 1024 + b * 128 + 16 * i + 4 * w) * (DM * 2)));
        };
#pragma unroll 1
        for (u32 o = (u32)((kq * 5 + ng) * 256 + tid) * 16u + (u32)L * (2 * MB); o < 2 * MB; o += 20 * 4096) st16(rs, o, (v4u){PSENT, PSENT, PSENT, PSENT});
        fill(0);
        VMW(8); BAR();
        if (L == 0) pre_post(rs, 256 + kq * 5 + ng, 1u, ep);
#pragma unroll 1
        for (int b = 1; b < 5; ++b) fill(b);
        lptr tb = lds + hh * 1024 + wst_rd(lane, w);
        SFOR(b, 8)
            VMW(b < 4 ? 32 : 56 - 8 * b); BAR();
            lptr q = tb + (b % 5) * 32768;
#pragma unroll
            for (int k = 0; k < 8; ++k) { lptr a = q + (k >> 1) * 8192 + (k & 1) * 2048; wo[8 * b + k] = tr2(a, a + 4096); if (b < 7) asm volatile("" : "+a"(wo[8 * b + k])); else PIN(wo[8 * b + k]); }
            if constexpr (b < 3) { BAR(); fill(b + 5); }
        SEND
    }
    const u32 pol = pol_wait(rs, ep);
    BAR();
    if (kq == 0 && ng == 0) LST(272 + L, rt32());
    MK_SRD();
    const u32 ofl = lane < 8 ? foff(F_O + L * 32 + kq * 8 + lane) : OOB, l16 = (u32)lane * 16u;
    const u32 fo = (tid == 0) ? fp_off(pol, L * 32 + kq * 5 + ng) : OOB;
#define OM0(dst, b) asm volatile("s_add_u32 m0, %0, %1" :: "s"(dst), "i"((b) * 8192) : "scc", "memory")
#define ODMA(i, vo) asm volatile("buffer_load_dwordx4 %0, %1, %2 offen offset:%3 sc1 lds" :: "v"(vo), "s"(srd), "s"(((i) >> 1) * 16384), "i"(((i) & 1) * 1024) : "memory")
    auto first = [&](u32 dst, u32 vo) {
#pragma unroll
        for (int b = 0; b < 5; ++b) { OM0(dst, b); NOPS(0); ODMA(2 * b, vo); ODMA(2 * b + 1, vo); }
    };
    u32 pa = W_P + (u32)(L * NSEG) * P_ITEM + (u32)(2 * hh * 4 + kq) * (DM * 4) + (u32)(ng * 128 + w * 32 + r32) * 4u, fv = 0;
    u32 ov = W_VO + 8192u + (u32)(L * NSEG * NH + kq * 8) * 16384u + (u32)w * 2048u + l16;
    bool early = false;
    u32 farv = 1, owed = OOB;
    lptr selw = lds + 131072 + (w == 0 ? 0 : 64), selr = lds + 131072;
    asm volatile("" : "+v"(farv), "+v"(selw), "+v"(selr));
#define OWAIT(strict, loose) asm volatile("s_cmp_eq_u32 %0, 0\n\ts_cbranch_scc1 1f\n\ts_waitcnt vmcnt(%1)\n1:\n\ts_waitcnt vmcnt(%2)\n\ts_barrier" :: "s"(far), "i"(strict), "i"(loose) : "scc", "memory")
    for (int s = 0; s < NSEG; ++s) {
        const u32 dst = uu((s & 1) * 65536 + w * 2048), ndst = dst ^ 65536u, need = s + 1 < NSEG ? (u32)s + 2u : 255u;
        lptr A = lds + (s & 1) * 65536 + l16;
        if (!early) {
            if (!all_(ofl == OOB || fv >= (u32)s + 1)) wait_flags(rs, ofl, (u32)s + 1, dead);
            LS3(L == 0 && w == 1, 1, kq * 5 + ng);
            first(dst, ov);
        }
#define OT(k) do {} while (0)
        u32 far = uu(farv), nv, sv = PSENT + (u32)s;
        OM0(dst, 5);
        OWAIT(8, 17);
        OT(0);
        v4u o[64];
#pragma unroll
        for (int k = 0; k < 4; ++k) o[k] = lds_r16(A + k * 1024);
        f32x16 acc;
        SCHED();
        SFOR(k, 64)
            if constexpr (k < 56) M32B(k == 0, acc, o[k], wo[k]); else M32V(acc, o[k], wo[k]);
            if constexpr (k < 60) o[k + 4] = lds_r16(A + (k + 4) * 1024);
            SCHED();
            (void)&srd; (void)&dst; (void)&ndst;
            if constexpr (k < 12 && (k & 1) == 0) ODMA(10 + k / 2, ov);
            if constexpr (k == 3) OWAIT(6, 14);
            if constexpr (k == 3 || k == 7) OM0(dst, (k + 21) >> 2);
            if constexpr (k == 12) { ov += NH * 16384u; PIN(ov); }
            if constexpr (k == OU_SMP) LD4A(fv, ofl, 0);
            if constexpr (k == 19) OWAIT(7, 15);
            if constexpr (k == 33) { asm volatile("s_waitcnt vmcnt(1)" : "+v"(fv) :: "memory"); far = (u32)__builtin_amdgcn_ballot_w64(fv < need) & 255u; PINS(far); }
            if constexpr (k == 34) { farv = far; PIN(farv); *(LAS u32*)selw = farv; }
            if constexpr (k == 35) asm volatile("s_waitcnt lgkmcnt(1)\n\ts_barrier" ::: "memory");
            if constexpr (k == OU_FLG) asm volatile("s_waitcnt vmcnt(7)\n\ts_barrier" ::: "memory");
            if constexpr (k == OU_FLG + 1) st4s(rs, owed, 0, sv);
            if constexpr (k == 36) farv = *(LAS u32*)selr;
            if constexpr (k == 41) { nv = farv ? OOB : ov; PIN(nv); }
            if constexpr (k >= 42 && k < 62 && (k & 3) == 2) OM0(ndst, (k - 42) >> 2);
            if constexpr (k >= 43 && k < 63 && (k & 1)) ODMA((k - 43) >> 1, nv);
            if constexpr (k == 48) LD4A(fv, ofl, 0);
            if constexpr (k == 4) OT(1);
            if constexpr (k == 20) OT(2);
            if constexpr (k == 36) OT(3);
            if constexpr (k == 41) OT(4);
            SCHED();
        SEND
        OT(5);
        asm volatile("s_nop 11" : "+v"(acc), "+v"(pa));
#pragma unroll
        for (int i = 0; i < 8; ++i) st4s(rs, pa, (u32)((4 * (i >> 1) + (i & 1)) * (4 * DM * 4)), pk(acc[2 * i], acc[2 * i + 1]));
        asm volatile("s_waitcnt vmcnt(15)" : "+v"(fv) :: "memory");
        OT(6);
        LS3(L == 0 && w == 1, 2, kq * 5 + ng);
        far = uu(farv);
        early = far == 0u || all_(ofl == OOB || fv >= need);
        owed = fo;
        if (far != 0u) {
            if (early) { first(ndst, ov); VMW(10); }
            else { fv = ld4(rs, ofl); asm volatile("s_waitcnt vmcnt(0)" : "+v"(fv) :: "memory"); }
            BAR();
            st4s(rs, fo, 0, sv + 1u);
            owed = OOB;
        }
        pa += P_ITEM;
        LS2(L == 0 && w == 1, 160 + kq * 5 + ng);
    }
    LDUMP(L == 0 && w == 1, kq * 5 + ng);
}

DEV void ln_stats2(const f32x2 (&x)[10], f32x2& mean, f32x2& rstd) {
    const f32x2 c = {f_(uu(u_(x[0].x))), f_(uu(u_(x[0].y)))};
    f32x2 d[10];
#pragma unroll
    for (int i = 0; i < 10; ++i) d[i] = x[i] - c;
    f32x2 a = ((d[0] + d[1]) + (d[2] + d[3])) + ((d[4] + d[5]) + (d[6] + d[7])) + (d[8] + d[9]);
    f32x2 b = (d[0] * d[0] + d[2] * d[2] + d[4] * d[4] + d[6] * d[6] + d[8] * d[8]) + (d[1] * d[1] + d[3] * d[3] + d[5] * d[5] + d[7] * d[7] + d[9] * d[9]);
    float a0, a1, b0, b1;
#define LN_DP4(m) "v_add_f32_dpp %0, %0, %0 " m " row_mask:0xf bank_mask:0xf\n\tv_add_f32_dpp %1, %1, %1 " m " row_mask:0xf bank_mask:0xf\n\t" \
                  "v_add_f32_dpp %2, %2, %2 " m " row_mask:0xf bank_mask:0xf\n\tv_add_f32_dpp %3, %3, %3 " m " row_mask:0xf bank_mask:0xf\n\t"
    asm("s_nop 1\n\t" LN_DP4("quad_perm:[1,0,3,2]") LN_DP4("quad_perm:[2,3,0,1]") LN_DP4("row_ror:4") LN_DP4("row_ror:8")
        "v_permlane16_swap_b32 %0, %1\n\tv_add_f32 %0, %0, %1\n\tv_permlane16_swap_b32 %2, %3\n\tv_add_f32 %2, %2, %3\n\ts_nop 1\n\t"
        "v_permlane32_swap_b32 %0, %2\n\tv_add_f32 %0, %0, %2\n\ts_nop 0\n\t"
        "v_readlane_b32 %4, %0, 0\n\tv_readlane_b32 %5, %0, 16\n\tv_readlane_b32 %6, %0, 32\n\tv_readlane_b32 %7, %0, 48\n\ts_nop 1"
        : "+v"(a.x), "+v"(b.x), "+v"(a.y), "+v"(b.y), "=s"(a0), "=s"(b0), "=s"(a1), "=s"(b1));
    const f32x2 m = (f32x2){a0, a1} * (1.0f / DM), v = (f32x2){b0, b1} * (1.0f / DM) - m * m;
    const f32x2 q = up2(pkv(up2(pkv((f32x2){fmaxf(v.x, 0.f), fmaxf(v.y, 0.f)})) + 1e-5f));
    const f32x2 sd = up2(pk(__builtin_amdgcn_sqrtf(q.x), __builtin_amdgcn_sqrtf(q.y)));
    mean = up2(pkv(c + m)); rstd = (f32x2){__builtin_amdgcn_rcpf(sd.x), __builtin_amdgcn_rcpf(sd.y)};
}
DEV void role_row(const Params& p, lptr lds, int id, v2u ep) {
    const int lane = threadIdx.x & 63, w = uni(threadIdx.x >> 6);
    const int pr = id >> 2, L = id & 1, par = (id >> 1) & 1;
    const rsrc_t rs = mk_rsrc(p.ws, W_END), rx = mk_rsrc(p.x, ACT), ro_ = mk_rsrc(p.out, ACT);
    bool dead = false;
    u32 fp = OOB, fh = OOB;
    const u32 l16 = (u32)lane * 16u, l8 = (u32)lane * 8u, l4 = (u32)lane * 4u, fb = uu(w * CV_W);
    lptr F = lds + w * CV_W;
    MK_SRD();
    for (int s = par; s < NSEG; s += 2) {
        const u32 ro = (u32)(s * 32 + 2 * pr) * (DM * 2);
        if ((s & 6) == 0) {
            if ((s & 30) == 0) pre_poll(rs, s < 32 ? 0 : 192, s < 32 ? NPRE : 64, 4u, ep);
            if (id == 0 && s == 0) LST(274, rt32());
            if (s == 48) st8s(rs, (id == 1 && lane == 0) ? foff(F_EPOCH) : OOB, 0, (v2u){ep.x + 8u, ep.y});
#pragma unroll 1
            for (int i = 0; i < 5; ++i) {
                const u32 d = fb + i * 2048, o = W_Y + (u32)(16 * pr + (s >> 3)) * 10240u + i * 2048;
                DMA32(d, l16, o); DMA32(d + 10240u, l16, o + 81920u);
            }
            if (s < 2) { const u32 pol = pol_wait(rs, ep); fp = lane < 20 ? fp_off(pol, L * 32 + lane) : OOB; fh = lane == 0 ? fh_off(pol, par * 16 + pr) : OOB; }
            DRAIN();
        }
        RowX xr[2];
        auto xload = [&](rsrc_t r, u32 o, auto aux) {
#pragma unroll
            for (int i = 0; i < 2; ++i) {
                xr[i].a = __builtin_amdgcn_raw_buffer_load_b64(r, l8, o + i * (DM * 2), aux.value); xr[i].b = __builtin_amdgcn_raw_buffer_load_b64(r, l8 + 512u, o + i * (DM * 2), aux.value);
                xr[i].c = __builtin_amdgcn_raw_buffer_load_b32(r, l4 + 1024u, o + i * (DM * 2), aux.value);
            }
        };
        if (L == 0) xload(rx, ro, IC<0>{});
        f32x2 y[10], x[10];
        {
            lptr ya = F + (s & 7) * 2 + l16, yc = F + (s & 7) * 2 + 2048 + (lane >> 1) * 16 + (lane & 1) * 5120;
#pragma unroll
            for (int i = 0; i < 10; ++i) {
                lptr q = i < 8 ? ya + (i & 3) * 2560 + (i >> 2) * 1024 : yc + (i & 1) * 2560;
                y[i] = (f32x2){lo_f(*(LAS u16*)q), lo_f(*(LAS u16*)(q + 10240))}; PIN(y[i]);
            }
        }
        wait_flags(rs, fp, PSENT + (u32)s + 1u, dead);
        LS3(L == 0, 3, ri);
        LMX(L == 0, 0, s, rt32());
        {
            const u32 o = uu(W_P + (u32)(L * NSEG + s) * P_ITEM + (u32)pr * (4 * DM * 4));
            if (L == 1) xload(rs, W_X2 + ro, IC<16>{});
            v4u a[4], b[4]; v2u c[4];
#pragma unroll
            for (int kq = 0; kq < 4; ++kq) { a[kq] = ld16s(rs, l16, o + kq * (DM * 4)); SCHED(); b[kq] = ld16s(rs, l16 + 1024u, o + kq * (DM * 4)); SCHED(); c[kq] = ld8s(rs, l8 + 2048u, o + kq * (DM * 4)); SCHED(); }
            const u32 u0[5] = {xr[0].a.x, xr[0].a.y, xr[0].b.x, xr[0].b.y, xr[0].c}, u1[5] = {xr[1].a.x, xr[1].a.y, xr[1].b.x, xr[1].b.y, xr[1].c};
#pragma unroll
            for (int i = 0; i < 5; ++i) { x[2 * i] = (f32x2){lo_f(u0[i]), lo_f(u1[i])}; x[2 * i + 1] = (f32x2){hi_f(u0[i]), hi_f(u1[i])}; }
#pragma unroll
            for (int kq = 0; kq < 4; ++kq)
#pragma unroll
                for (int i = 0; i < 10; ++i)
                    asm volatile("v_dot2_f32_bf16 %0, %2, %3, %0\n\tv_dot2_f32_bf16 %1, %2, %4, %1" : "+v"(x[i].x), "+v"(x[i].y)
                                 : "v"(i < 4 ? a[kq][i & 3] : i < 8 ? b[kq][i & 3] : c[kq][i & 1]), "s"(0x3F80u), "s"(0x3F800000u));
            asm volatile("s_nop 2" : "+v"(x[8]), "+v"(x[9]));
        }
        LS3(L == 0, 4, ri);
        f32x2 mean, rstd;
        ln_stats2(x, mean, rstd);
#pragma unroll
        for (int i = 0; i < 10; ++i) x[i] += (x[i] - mean) * (y[i] * rstd);
        u32 xp[2][5];
#pragma unroll
        for (int i = 0; i < 5; ++i) { xp[0][i] = pkv((f32x2){x[2 * i].x, x[2 * i + 1].x}); xp[1][i] = pkv((f32x2){x[2 * i].y, x[2 * i + 1].y}); }
        if (L == 0) {
#pragma unroll
            for (int i = 0; i < 5; ++i) { x[2 * i] = (f32x2){lo_f(xp[0][i]), lo_f(xp[1][i])}; x[2 * i + 1] = (f32x2){hi_f(xp[0][i]), hi_f(xp[1][i])}; }
            ln_stats2(x, mean, rstd);
#pragma unroll
            for (int i = 0; i < 10; ++i) x[i] = (x[i] - mean) * rstd;
#pragma unroll
            for (int r = 0; r < 2; ++r) {
                u32 hp[5];
#pragma unroll
                for (int i = 0; i < 5; ++i) hp[i] = pkv((f32x2){x[2 * i][r], x[2 * i + 1][r]});
                store_h(rs, W_H2, s * 32 + 2 * pr + r, hp);
            }
#pragma unroll
            for (int r = 0; r < 2; ++r) {
                st8s(rs, l8, W_X2 + ro + r * (DM * 2), (v2u){xp[r][0], xp[r][1]});
                st8s(rs, l8 + 512u, W_X2 + ro + r * (DM * 2), (v2u){xp[r][2], xp[r][3]});
                st4s(rs, l4 + 1024u, W_X2 + ro + r * (DM * 2), xp[r][4]);
            }
            LS3(L == 0, 5, ri);
            DRAIN();
            st4s(rs, fh, 0, PSENT + (u32)s + 1u);
        } else {
#pragma unroll
            for (int r = 0; r < 2; ++r) {
                st8s(ro_, l8, ro + r * (DM * 2), (v2u){xp[r][0], xp[r][1]});
                st8s(ro_, l8 + 512u, ro + r * (DM * 2), (v2u){xp[r][2], xp[r][3]});
                st4s(ro_, l4 + 1024u, ro + r * (DM * 2), xp[r][4]);
            }
        }
        if (pr == 0) LST(320 + L * 64 + s, rt32());
        LS2(L == 0, 224 + ri);
        LMX(L == 0, 1, s, rt32());
    }
    LDUMP(L == 0, 32 + ri);
}

constexpr float SCALE = 0.08838834764831845f, LOG2E = 1.4426950408889634f;
constexpr int C_K = 0, C_V = 8192, C_QX = 16384, C_MEM = 24576, C_Q = C_MEM + 10240, C_KM = C_Q + 2304, C_VM = C_KM + 2304, C_DUMP = C_VM + 2048,
              C_ZERO = C_DUMP + 1024, C_WL = C_ZERO + 128;
constexpr int NLF = 14;
constexpr int C_MSK = C_WL + 4 * NLF * 2048;
static_assert(C_WL % 16 == 0 && C_MSK + 4096 <= LDS_BYTES, "head lds");
#define CH_VIS 8
#define CH_H0 104
#define CH_W0 80
#define CH_MH 3
#define CH_MW 3
#define CH_REC 28
#define CH_RTY 54u
#define CH_RISE 20u
#define CH_FALL 3u
#define CH_PRE 7
#define CH_PRW 2
#define CH_T0 205
#define CH_UP 2
#define CH_WX 6
#define WAIT_TXT(tmp, t4) "s_memrealtime vcc\n\t.p2align 5\n1:\n\ts_waitcnt lgkmcnt(0)\n\ts_sub_u32 " tmp ", " t4 ", vcc_lo\n\ts_memrealtime vcc\n\ts_cmpk_lt_u32 " tmp ", 400\n\ts_cbranch_scc1 1b\n\t" \
                          "s_add_u32 " tmp ", " tmp ", 4\n\ts_cselect_b32 " tmp ", " tmp ", 0\n2:\n\ts_sub_u32 " tmp ", " tmp ", 1\n\ts_cbranch_scc0 2b\n\t"
DEV void wait_until(u32 t) { u32 tmp; asm volatile(WAIT_TXT("%0", "%1") "s_waitcnt lgkmcnt(0)" : "=&s"(tmp) : "s"(uu(t - 4u)) : "vcc", "scc", "memory"); }
#define SMF_A(acc, a, b) asm volatile("v_smfmac_f32_16x16x64_bf16 %0, %1, %2, %3" : "+v"(acc) : "v"(a), "a"(b), "v"(idx))
#define SMF_V(acc, a, b) asm volatile("v_smfmac_f32_16x16x64_bf16 %0, %1, %2, %3" : "+v"(acc) : "v"(a), "v"(b), "v"(idx))
#define MF0(d, a, b) asm volatile("v_mfma_f32_16x16x32_bf16 %0, %1, %2, 0" : "=&v"(d) : "v"(a), "v"(b))
#define MF1(d, a, b) asm volatile("v_mfma_f32_16x16x32_bf16 %0, %1, %2, %0" : "+v"(d) : "v"(a), "v"(b))
#define MF0H(d, a, b) asm volatile("v_mfma_f32_16x16x16_bf16 %0, %1, %2, 0" : "=&v"(d) : "v"(a), "v"(b))
#define MF1H(d, a, b) asm volatile("v_mfma_f32_16x16x16_bf16 %0, %1, %2, %0" : "+v"(d) : "v"(a), "v"(b))
DEV v8u tr_frag(lptr p0, lptr p1, lptr p2, lptr p3) { return cat8(tr2(p0, p1), tr2(p2, p3)); }
constexpr int qslot(int ks) { return (ks & 1) && ks < 8 ? ks >> 1 : -1; }
constexpr int kslot(int ks) { return ks < 4 ? 4 + ks : -1; }
constexpr int vslot(int ks) { return ks >= 4 ? 4 + ks : -1; }
DEV u32 ring_map(u32 off) { return ((off >> 13) << 15) + (off & 8191u); }
DEV u32 oslot(int s) { return (u32)(s & 3) << 18 | (u32)(s & 2) << 13; }
#define ZRD(x, i) x = __builtin_bit_cast(f32x4, lds_r16(ZP + 16 * (i)))
#define VMAX(d, a, b) asm volatile("v_max_f32 %0, %1, %2" : "=v"(d) : "v"(a), "v"(b))
#define VMAX3(d, a, b, c_) asm volatile("v_max3_f32 %0, %1, %2, %3" : "=v"(d) : "v"(a), "v"(b), "v"(c_))
#define VEXP(d, a) asm volatile("v_exp_f32 %0, %1" : "=v"(d) : "v"(a))
#define VMULC(d, a) asm volatile("v_mul_f32 %0, 0x3e0293ee, %1" : "=v"(d) : "v"(a))
#define CADD(d, a, b) do { d = (a) + (b); PIN(d); } while (0)
#define CMULC(d, a) do { d = (a) * CEXP; PIN(d); } while (0)
#define CCVT(d, a, b) do { d = pk(a, b); PIN(d); } while (0)
#define LGK(n) __builtin_amdgcn_s_waitcnt(0xC07F | (n) << 8)
#define SEL(d, a, b) asm volatile("v_cndmask_b32 %0, %1, %2, %3" : "=v"(d) : "v"(a), "v"(b), "s"(himask))
#define QPERM(d, a, q) asm volatile("v_mov_b32_dpp %0, %1 quad_perm:" q " row_mask:0xf bank_mask:0xf" : "=v"(d) : "v"(a))

#define HD_SMP 70
DEV void role_head(const Params& p, lptr lds, int L, int h, v2u ep) {
    const int tid = threadIdx.x, lane = tid & 63, w = uni(tid >> 6), g = lane >> 4, r = lane & 15;
    const rsrc_t rs = mk_rsrc(p.ws, W_END);
    v8u wq0[10], wq1[10], wk0[10], wk1[10], wv0[10], wv1[10];
    lptr WL = lds + C_WL + w * (NLF * 2048) + lane * 16;
    {
        constexpr int GS = 1056, CS = 48 * GS;
        const v4u sq = mk_srd(p.Wq, DM * NQ * 2), sk = mk_srd(p.Wk, DM * NQ * 2), sv = mk_srd(p.Wv, DM * NQ * 2);
        const u32 vo = (u32)(g * NQ + h * DH + 8 * (r ^ ((g & 1) << 3))) * 2u;
        auto fill = [&](int ch) {
#pragma unroll 1
            for (int i = 0; i < 4; ++i) {
                const u32 dst = uu((ch % 3) * CS + (4 * i + w) * GS), so = uu((ch * 64 + 16 * i + 4 * w) * (NQ * 2));
                DMAIN(sq, dst, vo, so); DMAIN(sk, dst + 16 * GS, vo, so); DMAIN(sv, dst + 32 * GS, vo, so);
            }
        };
        fill(0); fill(1); fill(2);
        const int q4 = r >> 2;
        lptr tb = lds + (4 * (g >> 1) + 2 * (q4 >> 1) + (g & 1)) * GS + (q4 & 1) * 256 + ((w * 64 + (r & 3) * 8) ^ ((q4 & 1) << 7));
        SFOR(ch, 10)
            VMW(ch < 8 ? 24 : 108 - 12 * ch);
            BAR();
            auto frag = [&](int m, int t) { lptr b = tb + (ch % 3) * CS + m * 16 * GS + t * 32; return tr_frag(b, b + 512, b + 8 * GS, b + 8 * GS + 512); };
            wq0[ch] = frag(0, 0); wq1[ch] = frag(0, 1); wk0[ch] = frag(1, 0); wk1[ch] = frag(1, 1); wv0[ch] = frag(2, 0); wv1[ch] = frag(2, 1);
            asm volatile("" : "+a"(wq0[ch]), "+a"(wk0[ch]), "+v"(wv0[ch]), "+v"(wv1[ch]));
            if constexpr (qslot(ch) < 0) asm volatile("" : "+a"(wq1[ch])); else asm volatile("" : "+v"(wq1[ch]));
            if constexpr (kslot(ch) < 0) asm volatile("" : "+a"(wk1[ch])); else asm volatile("" : "+v"(wk1[ch]));
            BAR();
            if constexpr (ch < 7) fill(ch + 3);
        SEND
#pragma unroll
        for (int ch = 0; ch < 10; ++ch) {
            if (qslot(ch) >= 0) { lds_w16(WL + qslot(ch) * 2048, wq1[ch].lo); lds_w16(WL + qslot(ch) * 2048 + 1024, wq1[ch].hi); }
            if (kslot(ch) >= 0) { lds_w16(WL + kslot(ch) * 2048, wk1[ch].lo); lds_w16(WL + kslot(ch) * 2048 + 1024, wk1[ch].hi); }
            if (vslot(ch) >= 0) { lds_w16(WL + vslot(ch) * 2048, wv1[ch].lo); lds_w16(WL + vslot(ch) * 2048 + 1024, wv1[ch].hi); }
        }
        CFENCE();
    }
    const u32 idx = (r & 2) ? 0xEEEEu : 0x4444u;
    unsigned long long himask = 0xCCCCCCCCCCCCCCCCull;
    PINS(himask);
    bool dead = false;
    if (h == 3) LST(266 + L, rt32());
    if (w == 0) LST(1240 + L * 32 + h, rt32());
    if (L == 1) {
        KvIn in;
        kvt_load(p, KT0 + 4 * h + w, in, [] {}); kvt_make(rs, lds + w * 9216, KT0 + 4 * h + w, in);
        DRAIN(); BAR();
        pre_post(rs, 64 + h, 4u, ep);
    }
    const u32 pol0 = pol_wait(rs, ep);
    if (h == 3 && L == 0) LST(277, rt32());
    const u32 l16 = (u32)lane * 16u;
    const u32 mring = (u32)L * MB + (pol0 ^ 16384u), hint = mring + 4 * 65536u;
    const u32 fkh = lane < 3 ? foff(F_QKV + L * 96 + lane * 32 + h) : lane < 7 ? hint + (u32)(lane - 3) * 4u : OOB;
    u32 tm = 0;
    MK_SRD();
    u32 ksrc = uu(W_KQ + (u32)(L * (NSEG - 1) * NH + h) * 16384u + w * 2048), vsrc = ksrc + (W_VO - W_KQ);
    u32 dk = uu(C_K + w * 2048), dv = dk + (C_V - C_K), dq = dk + (C_QX - C_K), dm = uu(C_MEM + w * 2048), dm2 = uu(w < 2 ? C_MEM + 8192 + w * 1024 : 0);
    asm volatile("" : "+s"(dk), "+s"(dv), "+s"(dq), "+s"(dm), "+s"(dm2));
    auto keys_wait = [&](u32 need) {
#pragma unroll 1
        for (int n = 0; !dead; ++n) {
            const bool beat = need + 2 * L > 2u;
            if (beat) wait_until(tm);
            const u32 hv = ld4(rs, lane == 7 ? foff(F_ERR) : fkh);
            if (beat) { tm = rdl(hv, 5); wait_until(rdl(hv, 3) - 30u); }
            const bool any = need == (u32)L;
            const u32 f0 = ld4(rs, any ? foff(F_QKV + 96 + lane) : fkh), f1 = ld4(rs, any ? foff(F_QKV + 128 + lane) : fkh);
            if (all_(min(f0, f1) >= need || (!any && lane >= 3))) break;
            dead = n > SPIN_CAP || rdl(hv, 7) != 0u;
            st4s(rs, dead ? foff(F_ERR) : OOB, 0, 1u);
        }
        CFENCE();
        DMA32(dk, l16, ksrc); DMA32(dv, l16, vsrc); DMA32(dq, l16, ksrc ^ 8192u);
    };
    if (L == 0) { st4s(rs, tid == 0 ? hint + 4096u + (u32)h * 128u : OOB, 0, 0u); keys_wait(1u); }
    lptr qw = lds + C_Q + (2 * g) * 288 + (32 * w + 2 * r) * 2;
    lptr kw = qw + (C_KM - C_Q); PIN(kw);
    lptr qr = lds + C_Q + (2 * (r >> 2) + (r & 1)) * 288 + g * 16;
    lptr LB = lds + l16, VX = lds + C_V + w * 2048 + l16;
    lptr AB = lds + C_MEM + ((lane & 57) + (lane & 2) * 2 + ((lane & 4) >> 1)) * 16;
    PIN(VX); PIN(AB);
    if (tid >= 64 && tid < 70) { const u32 z = tid == 68 ? 0x3F803F80u : tid == 69 ? PSENT : 0u; lds_w16(lds + C_ZERO + (tid - 64) * 16, (v4u){z, z, z, z}); }
    {
        f32x4 mk;
#pragma unroll
        for (int i = 0; i < 4; ++i) mk[i] = 16 * (w & 1) + 4 * g + i <= 16 * (w >> 1) + r ? 0.f : -1000000000.0f / SCALE;
        *(LAS f32x4*)(lds + C_MSK + tid * 16) = mk;
    }
    u32 zaddr = 0; PIN(zaddr);
    lptr ZP = lds + zaddr + C_ZERO;
    const float CEXP = SCALE * LOG2E;
#define LF_RD(slot, hf) lf[slot][hf] = lds_r16(WL + (slot) * 2048 + (hf) * 1024)
    BAR();
    const u32 ofl = tid == 0 ? foff(F_O + L * 32 + h) : OOB;
    u32 oso = (((g | L) & 1) ? OOB : 0u) + (u32)L * MB + (pol0 ^ oslot(-L)) + ring_map((u32)((r >> 3) * 32768 + h * 1024 + w * 256 + (g >> 1) * 128 + (r & 7) * 16));
    u32 missed = 0;
    for (int s = -L; s < NSEG;) {
        u32 sv = (u32)max(s, 0), ms = s > 0 ? mring + (sv & 3u) * 65536u : W_MEM0, ms2 = ms + 32768u + (w & 1) * 1024;
        ms += w * 2048;
        asm volatile("" : "+s"(ms), "+s"(ms2)); PIN(sv);
        f32x4 aq0, aq1, ak0, ak1;
        v4u lf[NLF][2];
        ZRD(aq0, 0); ZRD(aq1, 1); ZRD(ak0, 2); ZRD(ak1, 3);
#pragma unroll
        for (int i = 0; i < 4; ++i) { LF_RD(i, 0); LF_RD(i, 1); }
        SCHED();
        v4u A[10];
#define HT(k) do {} while (0)
        v4u kx[8], qf[4], kmf[4], vx[2], ones, P;
        float fa, fb, fc, fd, inv, sx[4], t[4], tm0, tm1; u32 pa, pb, fl, e01, e23, pmm;
        f32x4 av0, av1, s0, s1, sm, oa, ob, rsum;
        v2u vm, pm;
#define LW(sl) cat8(lf[sl][0], lf[sl][1])
#define PQ(ks, tl) do { if constexpr (!(tl)) SMF_A(aq0, A[ks], wq0[ks]); else if constexpr (qslot(ks) < 0) SMF_A(aq1, A[ks], wq1[ks]); else { const v8u lw_ = LW(qslot(ks)); SMF_V(aq1, A[ks], lw_); } } while (0)
#define PK(ks, tl) do { if constexpr (!(tl)) SMF_A(ak0, A[ks], wk0[ks]); else if constexpr (kslot(ks) < 0) SMF_A(ak1, A[ks], wk1[ks]); else { const v8u lw_ = LW(kslot(ks)); SMF_V(ak1, A[ks], lw_); } } while (0)
#define PV(ks, tl) do { if constexpr (!(tl)) SMF_V(av0, A[ks], wv0[ks]); else if constexpr (vslot(ks) < 0) SMF_V(av1, A[ks], wv1[ks]); else { const v8u lw_ = LW(vslot(ks)); SMF_V(av1, A[ks], lw_); } } while (0)
#define KXR(i) kx[i] = lds_r16(LB + C_K + ((i) & 1) * 4096 + ((i) >> 1) * 1024)
#define XW(q_) do { *(LAS u32*)(q_) = pa; *(LAS u32*)((q_) + 288) = pb; } while (0)
        auto slot = [&](auto zc) __attribute__((always_inline)) {
            constexpr int z = decltype(zc)::value;
            if constexpr (z < 14) PQ(z >> 1, z & 1);
            else if constexpr (z < 17) PQ(z - 7, 0);
            else if constexpr (z < 20) PQ(z - 10, 1);
            else if constexpr (z < 34) PK((z - 20) >> 1, z & 1);
            else if constexpr (z < 37) PK(z - 27, 0);
            else if constexpr (z < 40) PK(z - 30, 1);
            else if constexpr (z < 42) { if constexpr (z == 40) MF0(s0, kx[0], qf[0]); else MF0(s1, kx[1], qf[0]); }
            else if constexpr (z < 48) { if constexpr (z & 1) MF1(s1, kx[z - 40], qf[(z - 40) >> 1]); else MF1(s0, kx[z - 40], qf[(z - 40) >> 1]); }
            else if constexpr (z < 54) PV(z - 48, 0);
            else if constexpr (z == 54) MF0(sm, kmf[0], qf[0]);
            else if constexpr (z < 58) MF1(sm, kmf[z - 54], qf[z - 54]);
            else if constexpr (z < 62) PV(z - 52, 0);
            else if constexpr (z < 64) PV(z - 62, 1);
            else if constexpr (z == 64) MF0(oa, vx[0], P);
            else if constexpr (z == 65) MF0(ob, vx[1], P);
            else if constexpr (z == 66) MF0(rsum, ones, P);
            else if constexpr (z < 75) PV(z < 70 ? z - 63 : z == 70 ? 2 : z == 71 ? 7 : z == 72 ? 3 : z - 65, 1);
            else { const v2u ones2 = ones.lo; MF1H(rsum, ones2, pm); }
            if constexpr (z < 6) A[4 + z] = lds_r16(AB + (4 + z) * 1024);
            if constexpr (z >= 6 && z < 10) LF_RD(4 + ((z - 6) >> 1), z & 1);
            if constexpr (z >= 14 && z < 18) LF_RD(6 + ((z - 14) >> 1), z & 1);
            if constexpr (z >= 22 && z < 28) KXR(z - 22);
            if constexpr (z == 28) XW(qw);
            if constexpr (z == 29 || z == 30) KXR(z - 23);
            if constexpr (z == 32 || z == 47) asm volatile("s_barrier" ::: "memory");
            if constexpr (z >= 33 && z < 37) qf[z - 33] = lds_r16(qr + (z - 33) * 64);
            if constexpr (z == 20) ZRD(av0, 0);
            if constexpr (z == 43) XW(kw);
            if constexpr (z == 44 || z == 45) vx[z - 44] = lds_r16(VX + (z - 44) * 1024);
            if constexpr (z >= 48 && z < 52) kmf[z - 48] = lds_r16(qr + (C_KM - C_Q) + (z - 48) * 64);
            if constexpr (z == 52) ZRD(av1, 1);
            if constexpr (z == 37) ones = lds_r16(ZP + 64);
            if constexpr (z >= 57 && z < 64) LF_RD(8 + ((z - 57) >> 1), (z - 57) & 1);
            if constexpr (z >= 65 && z < 70) LF_RD(8 + ((z - 58) >> 1), (z - 58) & 1);
            if constexpr (z == HD_SMP) LD4A(fl, fkh, 0);
            SCHED();
            if constexpr (z == 19) HT(2);
            if constexpr (z == 32) HT(3);
            if constexpr (z == 47) HT(4);
            if constexpr (z == 64) HT(5);
            if constexpr (z == 19) LGK(4);
            if constexpr (z == 21 || z == 41) LGK(1);
            if constexpr (z == 56 || z == 73) LGK(0);
            if constexpr (z == 31 || z == 46) LGK(2);
            if constexpr (z == 39) LGK(4);
            if constexpr (z >= 53 && z < 56) LGK(57 - z);
            if constexpr (z == 64) LGK(3);
            if constexpr (z == 68) LGK(5);
            if constexpr (z >= 70 && z < 73) LGK(74 - z);
            SCHED();
            if constexpr (z == 18) CADD(fa, aq0[0], aq0[2]);
            if constexpr (z == 19) CADD(fb, aq0[1], aq0[3]);
            if constexpr (z == 20) CMULC(fa, fa);
            if constexpr (z == 21) CADD(fc, aq1[0], aq1[2]);
            if constexpr (z == 22) CADD(fd, aq1[1], aq1[3]);
            if constexpr (z == 23) CMULC(fc, fc);
            if constexpr (z == 24) CMULC(fb, fb);
            if constexpr (z == 25) CMULC(fd, fd);
            if constexpr (z == 26) CCVT(pa, fa, fc);
            if constexpr (z == 27) CCVT(pb, fb, fd);
            if constexpr (z == 38) CADD(fa, ak0[0], ak0[2]);
            if constexpr (z == 39) CADD(fb, ak0[1], ak0[3]);
            if constexpr (z == 41) { CADD(fc, ak1[0], ak1[2]); CADD(fd, ak1[1], ak1[3]); }
            if constexpr (z == 42) { CCVT(pa, fa, fc); CCVT(pb, fb, fd); }
            if constexpr (z >= 49 && z < 53) SEL(sx[z - 49], s0[z - 49], s1[z - 49]);
            if constexpr (z >= 53 && z < 57) VEXP(t[z - 53], sx[z - 53]);
            if constexpr (z == 57) CCVT(e01, t[0], t[1]);
            if constexpr (z == 58) CCVT(e23, t[2], t[3]);
            if constexpr (z == 59) QPERM(P.x, e01, "[0,1,0,1]");
            if constexpr (z == 60) QPERM(P.z, e01, "[2,3,2,3]");
            if constexpr (z == 61) QPERM(P.y, e23, "[0,1,0,1]");
            if constexpr (z == 62) QPERM(P.w, e23, "[2,3,2,3]");
            if constexpr (z == 63) CADD(fa, av0[0], av0[2]);
            if constexpr (z == 64) { asm volatile("" :: "v"(sm)); VEXP(tm0, sm[0]); }
            if constexpr (z == 65) VEXP(tm1, sm[1]);
            if constexpr (z == 66) CADD(fb, av0[1], av0[3]);
            if constexpr (z == 67) CCVT(pmm, tm0, tm1);
            if constexpr (z >= 68 && z < 72) SEL(oa[z - 68], oa[z - 68], ob[z - 68]);
            if constexpr (z == 72) asm volatile("v_cndmask_b32 %0, %1, 0, %2" : "=v"(pm.x) : "v"(pmm), "s"(himask));
            if constexpr (z == 73) asm volatile("v_cndmask_b32 %0, 0, %1, %2" : "=v"(pm.y) : "v"(pmm), "s"(himask));
            if constexpr (z == 74) LGK(15);
            if constexpr (z == 75) CCVT(vm.x, fa, fb);
            SCHED();
        };
#define HT0 ""
#define HT0O
        {
            u32 tmp;
            asm volatile("s_mov_b32 m0, %2\n\t" WAIT_TXT("%0", "%1") HT0 "buffer_load_dwordx4 %4, %5, %6 offen sc1 lds\n\tbuffer_load_dwordx4 %4, %5, %6 offen offset:1024 sc1 lds\n\t"
                         "s_and_b32 m0, %3, %3\n\ts_cbranch_scc1 3f\n\ts_waitcnt vmcnt(2)\n\ts_branch 4f\n3:\n\tbuffer_load_dwordx4 %4, %5, %7 offen sc1 lds\n4:\n\ts_waitcnt vmcnt(3) lgkmcnt(0)\n\ts_barrier"
                         : "=&s"(tmp) : "s"(uu(tm - 4u)), "s"(dm), "s"(dm2), "v"(l16), "s"(srd), "s"(ms), "s"(ms2), "i"(0) HT0O : "vcc", "scc", "memory");
        }
        st4s(rs, ofl, 0, sv);
        asm volatile("s_waitcnt vmcnt(1)\n\ts_barrier" ::: "memory");
        HT(1);
#pragma unroll
        for (int ks = 0; ks < 4; ++ks) A[ks] = lds_r16(AB + ks * 1024);
        SCHED();
        static_for<0, 76>(slot);
        asm volatile("s_nop 2\n\tv_add_f32 %1, %3, %5\n\tv_add_f32 %2, %4, %6\n\tv_cvt_pk_bf16_f32 %0, %1, %2"
                     : "=v"(vm.y), "=&v"(fc), "=&v"(fd) : "v"(av1[0]), "v"(av1[1]), "v"(av1[2]), "v"(av1[3]));
        __builtin_amdgcn_s_waitcnt(0xCF7F);
        f32x2 i2; u32 oad; unsigned long long far;
        auto publish = [&] {
            v4u oc; f32x2 u0 = oa.lo, u1 = oa.hi;
            asm volatile("v_pk_mul_f32 %0, %0, %2\n\tv_pk_mul_f32 %1, %1, %2" : "+v"(u0), "+v"(u1) : "v"(i2));
            __builtin_amdgcn_s_waitcnt(0xCF7F);
            asm volatile("v_cvt_pk_bf16_f32 %0, %4, %5\n\tv_cvt_pk_bf16_f32 %2, %4, %5\n\tv_cvt_pk_bf16_f32 %1, %6, %7\n\tv_cvt_pk_bf16_f32 %3, %6, %7\n\t"
                         "v_permlane16_swap_b32 %0, %2\n\ts_nop 0\n\tv_permlane16_swap_b32 %1, %3"
                         : "=&v"(oc.x), "=&v"(oc.y), "=&v"(oc.z), "=&v"(oc.w) : "v"(u0.x), "v"(u0.y), "v"(u1.x), "v"(u1.y));
            st16(rs, oad, oc);
        };
        asm volatile("s_nop 0\n\tv_mfma_f32_16x16x16_bf16 %0, %5, %6, %0\n\tv_rcp_f32 %1, %7\n\tv_add_u32 %3, 0xde800000, %7\n\tv_cmp_lt_u32 %4, %9, %3\n\tv_mov_b32 %2, %1"
                     : "+v"(oa), "=&v"(i2.x), "=&v"(i2.y), "=&v"(oad), "=&s"(far) : "v"(vm), "v"(pm), "v"(rsum[0]), "v"(rsum), "s"(0x3C000000u));
        if constexpr (HD_SMP > 75) LD4A(fl, fkh, 0); else NOPS(0);
        asm volatile("v_cndmask_b32 %0, %1, -16, %2" : "=v"(oad) : "v"(oso), "s"(far));
        __builtin_amdgcn_s_waitcnt(0xCF7F);
        publish();
        SCHED();
        if (__builtin_expect(far != 0ull, 0)) {
            DRAIN(); SCHED();
            bool miss = false;
#pragma unroll 1
            for (int i = 0; i < 10; ++i) { const v4u a = lds_r16(LB + C_MEM + i * 1024); miss |= a.x == PSENT || a.y == PSENT || a.z == PSENT || a.w == PSENT; }
            if (missed < SPIN_CAP && !all_(!miss)) {
                *(LAS u32*)(lds + C_DUMP + (w == 0 ? 0 : 64)) = ld4(rs, foff(F_ERR));
                asm volatile("" : "+v"(fl) :: "memory");
                tm = rdl(fl, 5);
                BAR();
                missed = uu(*(LAS u32*)(lds + C_DUMP)) != 0u ? SPIN_CAP : missed + 1;
                st4s(rs, missed == SPIN_CAP ? foff(F_ERR) : OOB, 0, 1u);
                BAR();
                continue;
            }
            float mx = fmaxf(fmaxf(fmaxf(sx[0], sx[1]), fmaxf(sx[2], sx[3])), fmaxf(sm[0], sm[1]));
            mx = max32(max16(fmaxf(mx, dpp<0x4E>(mx))));
#pragma unroll
            for (int i = 0; i < 4; ++i) t[i] = __builtin_amdgcn_exp2f(sx[i] - mx);
            e01 = pk(t[0], t[1]); e23 = pk(t[2], t[3]);
            pmm = pk(__builtin_amdgcn_exp2f(sm[0] - mx), __builtin_amdgcn_exp2f(sm[1] - mx));
            asm volatile("v_cndmask_b32 %0, %2, 0, %3\n\tv_cndmask_b32 %1, 0, %2, %3" : "=&v"(pm.x), "=&v"(pm.y) : "v"(pmm), "s"(himask));
            asm volatile("s_nop 1" : "+v"(e01), "+v"(e23), "+v"(pm));
            QPERM(P.x, e01, "[0,1,0,1]"); QPERM(P.z, e01, "[2,3,2,3]"); QPERM(P.y, e23, "[0,1,0,1]"); QPERM(P.w, e23, "[2,3,2,3]");
            NOPS(1);
            MF0(rsum, ones, P); MF0(oa, vx[0], P); MF0(ob, vx[1], P);
            NOPS(7);
            { const v2u ones2 = ones.lo; MF1H(rsum, ones2, pm); }
#pragma unroll
            for (int i = 0; i < 4; ++i) SEL(oa[i], oa[i], ob[i]);
            NOPS(1);
            MF1H(oa, vm, pm);
            asm volatile("s_nop 7\n\tv_rcp_f32 %0, %3\n\tv_cndmask_b32 %2, -16, %5, %6\n\tv_mov_b32 %1, %0\n\ts_nop 0"
                         : "=&v"(i2.x), "=&v"(i2.y), "=&v"(oad) : "v"(rsum[0]), "v"(rsum), "v"(oso), "s"(far));
            publish();
        }
        SCHED();
        unsigned long long clk;
        asm volatile("s_memrealtime %0" : "=s"(clk) :: "memory");
        HT(6);
        v4u tq[4], tk[8], tv[2];
        f32x4 c0, c1, cm;
        u32 oo, oz, kvo; bool kvok;
        u32 ln = l16; PIN(ln);
        lptr QX = lds + C_QX + (w & 1) * 4096 + ln, VO = lds + C_V + (w ^ 1) * 2048 + ln, VMX = lds + C_VM + w * 512 + (ln >> 1);
#pragma unroll
        for (int i = 0; i < 2; ++i) { tq[i] = lds_r16(QX + i * 1024); kmf[i] = lds_r16(qr + (C_KM - C_Q) + i * 64); tk[2 * i] = lds_r16(LB + C_K + i * 1024); tk[2 * i + 1] = lds_r16(LB + C_K + 4096 + i * 1024); }
        { lptr MK = lds + C_MSK + (w & 1) * 2048 + ln; c0 = *(LAS f32x4*)MK; c1 = *(LAS f32x4*)(MK + 1024); }
        *(LAS v2u*)VMX = vm;
        SCHED();
        SFOR(i, 12)
            if constexpr (i == 0) MF0(cm, kmf[0], tq[0]);
            else if constexpr (i % 3 == 0) MF1(cm, kmf[i / 3], tq[i / 3]); else if constexpr (i % 3 == 1) MF1(c0, tk[2 * (i / 3)], tq[i / 3]); else MF1(c1, tk[2 * (i / 3) + 1], tq[i / 3]);
            if constexpr (i < 2) kmf[2 + i] = lds_r16(qr + (C_KM - C_Q) + (2 + i) * 64);
            if constexpr (i == 0) { tq[2] = lds_r16(QX + 2048); tk[4] = lds_r16(LB + C_K + 2048); }
            if constexpr (i == 1) { tk[5] = lds_r16(LB + C_K + 6144); tq[3] = lds_r16(QX + 3072); }
            if constexpr (i == 2) { tk[6] = lds_r16(LB + C_K + 3072); tk[7] = lds_r16(LB + C_K + 7168); }
            if constexpr (i == 3) { tv[0] = lds_r16(VO); tv[1] = lds_r16(VO + 1024); }
            if constexpr (i == 4) { oo = ln + (ln & 0x300u) + ((vsrc ^ 8192u) + (u32)((w & 1) * 256) + ((u32)s & OOB)); PIN(oo); }
            if constexpr (i == 5) { ksrc += NH * 16384; vsrc += NH * 16384; PINS(ksrc); PINS(vsrc); }
            if constexpr (i == 6) { oso ^= (s & 1) ? oslot(3) : oslot(1); PIN(oso); }
            if constexpr (i == 7) { oz = oso ^ oslot(2); PIN(oz); }
            if constexpr (i == 9) { asm volatile("s_waitcnt vmcnt(1)" : "+v"(fl) :: "memory"); kvok = ((u32)__builtin_amdgcn_ballot_w64(fl < (u32)s + 2) & 7u) == 0u; }
            if constexpr (i == 10) { kvo = l16 | (kvok ? 0u : OOB); PIN(kvo); }
            SCHED();
        SEND
        asm volatile("s_waitcnt lgkmcnt(0)\n\ts_barrier" : "+s"(clk) :: "memory");
        const v2u tvm = *(LAS v2u*)(VMX + (512 - (w & 1) * 1024));
        const v4u pv = lds_r16(ZP + 80);
        DMA32(dk, kvo, ksrc);
        SCHED(); asm volatile("" :: "v"(cm));
        const float u[8] = {c0[0], c0[1], c0[2], c0[3], c1[0], c1[1], c1[2], c1[3]};
        float m, m2;
        {
            asm volatile("v_max3_f32 %0, %2, %3, %10\n\tv_max3_f32 %1, %4, %5, %11\n\tv_max3_f32 %0, %0, %6, %7\n\tv_max3_f32 %1, %1, %8, %9\n\t"
                         "v_max_f32 %0, %0, %1\n\tv_mov_b32 %1, %0\n\ts_nop 1\n\tv_permlane16_swap_b32 %0, %1\n\tv_max_f32 %0, %0, %1\n\tv_mov_b32 %1, %0\n\ts_nop 1\n\t"
                         "v_permlane32_swap_b32 %0, %1\n\tv_max_f32 %0, %0, %1\n\tv_mul_f32 %0, 0xbe0293ee, %0"
                         : "=&v"(m), "=&v"(m2) : "v"(u[0]), "v"(u[1]), "v"(u[2]), "v"(u[3]), "v"(u[4]), "v"(u[5]), "v"(u[6]), "v"(u[7]), "v"(cm[0]), "v"(cm[1]));
        }
        DMA32(dv, kvo, vsrc);
        {
            v4u px; v2u p0, p1;
#define EX2(d, a, b) asm volatile("v_fmamk_f32 %0, %2, 0x3e0293ee, %4\n\tv_fmamk_f32 %1, %3, 0x3e0293ee, %4\n\tv_exp_f32 %0, %0\n\tv_exp_f32 %1, %1" : "=&v"(d.x), "=&v"(d.y) : "v"(a), "v"(b), "v"(m))
            f32x2 ex[5];
            EX2(ex[0], u[0], u[1]); EX2(ex[1], u[2], u[3]); EX2(ex[2], u[4], u[5]); EX2(ex[3], u[6], u[7]); EX2(ex[4], cm[0], cm[1]);
            asm volatile("v_cvt_pk_bf16_f32 %0, %8, %9\n\tv_cvt_pk_bf16_f32 %1, %10, %11\n\tv_cvt_pk_bf16_f32 %2, %12, %13\n\tv_cvt_pk_bf16_f32 %3, %14, %15\n\t"
                         "v_mov_b32 %5, 0\n\tv_mov_b32 %6, 0\n\tv_cvt_pk_bf16_f32 %4, %16, %17\n\tv_cvt_pk_bf16_f32 %7, %16, %17"
                         : "=&v"(px.x), "=&v"(px.y), "=&v"(px.z), "=&v"(px.w), "=&v"(p0.x), "=&v"(p0.y), "=&v"(p1.x), "=&v"(p1.y)
                         : "v"(ex[0].x), "v"(ex[0].y), "v"(ex[1].x), "v"(ex[1].y), "v"(ex[2].x), "v"(ex[2].y), "v"(ex[3].x), "v"(ex[3].y), "v"(ex[4].x), "v"(ex[4].y));
            f32x4 o[4], sum;
            const v2u ones2 = ones.lo;
            const u32 now = (u32)clk;
            SCHED();
            MF0(sum, ones, px); { const u32 q = ksrc ^ 8192u; DMA32(dq, kvo, q); } SCHED();
            MF0(o[0], vx[0], px);
            MF0(o[1], vx[1], px);
            MF0(o[2], tv[0], px);
            MF0(o[3], tv[1], px);
            MF1H(sum, ones2, p0);
            MF1H(o[0], vm, p0);
            MF1H(o[1], vm, p1); st16(rs, oz, pv); SCHED();
            MF1H(o[2], tvm, p0);
            MF1H(o[3], tvm, p1);
            asm volatile("s_nop 2\n\tv_rcp_f32 %0, %1\n\ts_nop 0" : "=v"(inv) : "v"(sum[0]), "v"(sum));
            asm volatile("" : "+v"(o[0]), "+v"(o[1]));
            st16(rs, oo, (v4u){pk2(o[0].lo * inv), pk2(o[0].hi * inv), pk2(o[1].lo * inv), pk2(o[1].hi * inv)});
            asm volatile("" : "+v"(o[2]), "+v"(o[3]));
            st16(rs, oo ^ 2048u, (v4u){pk2(o[2].lo * inv), pk2(o[2].hi * inv), pk2(o[3].lo * inv), pk2(o[3].hi * inv)});
            SCHED();
            LS2(L == 0 && w == 0, h);
            st4s(rs, ((h & 15) == 0 && tid == 192) ? hint + 128u + (u32)h * 8u : OOB, 0, (u32)s << 16 | min(missed, 1u) << 14 | min(now - tm, 0x3FFFu));
            tm = rdl(fl, 5);
            missed = missed == SPIN_CAP ? missed : 0u;
            if (__builtin_expect(s < 1 - L, 0)) {
                if (s < 0) oso -= (ln & 256u) ? 0u : OOB;
#pragma unroll 1
                for (int n = 0; n < (s < 0 ? 0 : 200); ++n) {
                    CFENCE();
                    tm = rdl(ld4(rs, fkh), 6);
                    if (tm != PSENT) break;
                }
            }
        }
        if (__builtin_expect(!kvok, 0) && s + 1 < NSEG) {
            st4s(rs, ln == 0 ? hint + 4096u + (u32)h * 128u : OOB, 0, (u32)s + 1u);
            keys_wait((u32)s + 2u);
        }
        ++s;
    }
    DRAIN(); BAR();
    st4s(rs, ofl, 0, NSEG);
    LDUMP(w == 0, 160 + L * 32 + h);
}

#define WO_NL 3
#define WO_ROT w
#define WO_AH 1
DEV void role_wo(const Params& p, lptr lds, int j, v2u ep) {
    const int tid = threadIdx.x, lane = tid & 63, w = uni(tid >> 6), g = lane >> 4, r = lane & 15;
    const int rg = j & 1, cg = j >> 1, rot = WO_ROT;
    const rsrc_t rs = mk_rsrc(p.ws, W_END);
    v8u wo[8][5];
    {
        const v4u so_ = mk_srd(p.Wo, DM * NQ * 2);
        u32 vo[5];
#pragma unroll
        for (int i = 0; i < 5; ++i) { const int n = 64 * i + lane; vo[i] = (u32)((n / 5) * DM + cg * 40 + (n % 5) * 8) * 2u; }
        v4u junk = {};
        const u32 av = (u32)(j * 5120 + lane * 16);
        auto ahead = [&](int b) {
#pragma unroll
            for (int i = 0; i < 5; ++i) AHEAD(so_, av, uu((8 * w + ((b + rot) & 7)) * (DH * DM * 2) + i * 1024));
        };
        auto fill = [&](int b) {
            if (b + WO_AH < 8) ahead(b + WO_AH);
            const u32 dst = uu(w * 40960 + (b & 3) * 10240), src = uu((8 * w + ((b + rot) & 7)) * (DH * DM * 2));
#pragma unroll
            for (int i = 0; i < 10; ++i) DMAIN(so_, dst + i * 1024, vo[i % 5], src + (i / 5) * (64 * DM * 2));
        };
#pragma unroll 1
        for (int b = 0; b < WO_AH; ++b) ahead(b);
#pragma unroll 1
        for (int b = 0; b < 4; ++b) fill(b);
        const int q4 = r >> 2;
        lptr tb = lds + w * 40960 + (32 * (g >> 1) + 16 * (q4 >> 1) + 8 * ((r >> 1) & 1) + 4 * (g & 1) + (q4 & 1)) * 80 + (r & 1) * 8;
        SFOR(b, 8)
            VMW((b < 5 ? 30 : 70 - 10 * b) + (b + 1 + WO_AH < 8 ? 5 : 0) + (b + 2 + WO_AH < 8 ? 5 : 0) + (b + 3 + WO_AH < 8 ? 5 : 0));
            if constexpr (b + WO_AH == 7) PIN(junk);
#pragma unroll
            for (int t = 0; t < 5; ++t) {
                lptr q = tb + (b & 3) * 10240 + t * 16;
                wo[b][t] = tr_frag(q, q + 160, q + 64 * 80, q + 64 * 80 + 160);
                if (b * 5 + t < 32) asm volatile("" : "+a"(wo[b][t])); else asm volatile("" : "+v"(wo[b][t]));
            }
            CFENCE();
            if constexpr (b < 4) fill(b + 4);
        SEND
    }
    if (j == 0) LST(265, rt32());
    LST(1100 + j * 4 + w, rt32());
    const u32 idx = (r & 2) ? 0xEEEEu : 0x4444u;
    const u32 pol0 = pol_wait(rs, ep);
    BAR();
    const u32 l16 = (u32)lane * 16u;
    u32 oo[8];
#pragma unroll
    for (int i = 0; i < 8; ++i) oo[i] = uu(ring_map((u32)(rg * 32768 + (8 * w + ((i + rot) & 7)) * 1024)));
    const u32 hint = (pol0 ^ 16384u) + 4 * 65536u;
    u32 hvo[2];
#pragma unroll
    for (int l = 0; l < 2; ++l) hvo[l] = lane < 2 ? hint + (u32)(l ^ 1) * MB + (u32)lane * 4u : lane == 2 ? hint + (u32)l * MB + 128u : lane == 3 ? foff(F_ERR) : lane == 4 ? hint + (u32)l * MB + 256u :
                 (lane >= 32 && j == 0 && w == 3) ? hint + (u32)l * MB + (u32)lane * 128u : OOB;
    lptr xw = r < 8 ? lds + w * 640 + (2 * (g & 1) + (g >> 1)) * 32 + r * 4 : lds + 8192 + lane * 4;
    const int G = ((5 * cg) >> 1) + w, tt = 2 * G + ((lane >> 4) & 1) - 5 * cg;
    const bool pub = w < 3 && lane < 32 && tt >= 0 && tt < 5;
    lptr xr = lds + (pub ? tt * 128 + (lane & 15) * 8 : 0);
    const u32 mpo = pub ? (pol0 ^ 16384u) + ring_map((u32)(G * 256 + rg * 128 + lane * 4)) : OOB;
    constexpr u32 PER0 = CH_W0 + CH_MW + CH_H0 + CH_MH + 2 * CH_VIS, DW0 = CH_W0 + CH_VIS + CH_MW + CH_REC;
    const bool lead = j == 0 && w == 3;
    const u32 hso = (lead && lane == 0) ? hint : OOB;
    MK_SRD();
    u32 t = 0, tn = 0;
    int sn[2] = {0, 0};
    u32 hav = 8 * CH_H0, wav = 8 * CH_W0, mh = 8 * CH_MH, mw = 8 * CH_MW, ok[2] = {4u, 0u}, dw[2] = {DW0, CH_T0 + CH_REC - 8};
    u32 vo = l16 + pol0 + MB, tp = 0, fin, left, cnt = 0;
    unsigned long long clk = 0;
    {
        if (w == 0) LST(1310 + j, rt32());
        if (lead) {
#pragma unroll 1
            for (int n = 0; n < SPIN_CAP; ++n) {
                CFENCE();
                const u32 a = ld4(rs, foff(F_QKV + lane)), b = ld4(rs, lane < 32 ? foff(F_QKV + 64 + lane) : hvo[0]);
                if (all_(a != 0u && b != (lane < 32 ? 0u : PSENT))) break;
                __builtin_amdgcn_s_sleep(1);
            }
        }
        if (lead) {
            t = rt32() + CH_T0;
            st16(rs, hso, (v4u){t, PER0, t + DW0, t + DW0});
        } else {
#pragma unroll 1
            for (int n = 0; n < SPIN_CAP; ++n) {
                CFENCE();
                const u32 hv = ld4(rs, hvo[1]);
                t = rdl(hv, 0);
                if (rdl(hv, 1) < 400u) break;
                __builtin_amdgcn_s_sleep(1);
            }
        }
    }
    const u32 tm0 = t + DW0;
    tn = t; t -= CH_T0; clk = t - 100u;
    u32 zero = 0, hv = 0xFFFFu, pst = 1, pmp = OOB;
    const u32 psent = PSENT;
    lptr sw = lds + 2560 + w * 4;
    asm volatile("" : "+v"(zero), "+v"(sw));
    auto turn = [&](auto lc) __attribute__((always_inline)) {
        constexpr int L = decltype(lc)::value, M = L ^ 1;
        u32 done, rep, rd, lost, dh, dn, nt, za, pdur, mp; v4u hw;
        const int sc = sn[L];
        auto books = [&](auto ic) __attribute__((always_inline)) {
            constexpr int i = decltype(ic)::value;
            if constexpr (i == 0) { rep = max(rdl(hv, 2), rdl(hv, 4)); PINS(rep); }
            if constexpr (i == 1) { lost = (u32)(__builtin_amdgcn_ballot_w64(hv == (u32)sn[M]) >> 32); PINS(lost); }
            if constexpr (i == 2) { lost |= (rep >> 16) ^ (u32)sn[M]; PINS(lost); }
            if constexpr (i == 3) { lost |= ok[M] ^ 2u; rd = rep & 0xFFFFu; PINS(lost); PINS(rd); }
            if constexpr (i == 4) { lost = lost == 0u ? CH_UP * 8u : 0u; PINS(lost); }
            if constexpr (i == 5) { pdur = ((u32)clk - tp) * 8u; PINS(pdur); pdur = min(pdur, wav + CH_RISE); PINS(pdur); }
            if constexpr (i == 6) { pdur = (done & ok[M]) ? pdur : wav; PINS(pdur); }
            if constexpr (i == 7) { wav = max(pdur, wav - CH_FALL); PINS(wav); }
            if constexpr (i == 8) { nt = min(rd * 8u, hav + CH_RISE); PINS(nt); }
            if constexpr (i == 9) { nt = rd < 0x3FFFu ? nt : hav; PINS(nt); }
            if constexpr (i == 10) { hav = max(nt, hav - CH_FALL); PINS(hav); }
            if constexpr (i == 11) { mw = max(mw - 1u, CH_MW * 8u); PINS(mw); }
            if constexpr (i == 12) { mh = max(mh - 1u, CH_MH * 8u); PINS(mh); }
            if constexpr (i == 13) { rd = rd >> 14 == 1u ? lost : 0u; PINS(rd); }
            if constexpr (i == 14) { mw += rd; mh += lost - rd; PINS(mw); PINS(mh); }
            if constexpr (i == 15) { ok[M] = done ? 1u : (ok[M] & 1u) << 1 | ok[M] >> 2; PINS(ok[M]); }
            if constexpr (i == 16) { dh = (hav + mh) / 8u + (sn[1] < 3 ? CH_VIS + CH_PRE : CH_VIS); PINS(dh); }
            if constexpr (i == 17) { dn = (wav + mw) / 8u + (sn[1] < 3 ? CH_VIS + CH_PRW : CH_VIS); PINS(dn); }
            if constexpr (i == 18) { nt = dw[L] + dh + (u32)max((int)((u32)clk + CH_WX - t), 0); PINS(nt); }
            if constexpr (i == 19) { dh += dn; dw[L] = dn; sn[M] += (int)done; PINS(dh); PINS(sn[M]); }
            if constexpr (i == 20) { rd = tn - t + (dh >> 1); PINS(rd); }
            if constexpr (i == 21) { lost = (u32)(sc - L) < NSEG - 1 - L ? CH_RTY : 0u; PINS(lost); nt += ok[L] != 1u ? lost : 0u; PINS(nt); }
            if constexpr (i == 22) { nt = t + (u32)max((int)nt, (int)rd); PINS(nt); }
            if constexpr (i == 23) { hw.x = nt; hw.y = dh; PIN(hw.x); PIN(hw.y); }
            if constexpr (i == 24) { hw.z = nt + dn; hw.w = tm0; PIN(hw); }
            if constexpr (i == 25) st16(rs, hso + L * MB, hw);
        };
        {
#define WT(k) do {} while (0)
            v4u d[8];
            f32x4 acc[5] = {};
            done = uu(pst) ^ 1u; PINS(done);
            za = pmp | pst << 31;
            *(LAS u32*)(sw + L * 4096) = vo >> 31;
            mp = mpo + (L * MB + (u32)((sc + 1) & 3) * 65536u); pmp = mpo + (L * MB + (u32)(sc & 3) * 65536u);
            asm volatile("" : "+v"(acc[0]), "+v"(acc[1]), "+v"(acc[2]), "+v"(acc[3]), "+v"(acc[4]), "+v"(mp), "+v"(pmp), "+v"(za));
            {
                u32 tmp;
                asm volatile(WAIT_TXT("%1", "%2") "buffer_load_dwordx4 %0, %3, %4, %5 offen sc1\n\ts_waitcnt lgkmcnt(0)"
                             : "=&v"(d[0]), "=&s"(tmp) : "s"(uu(t - 4u)), "v"(vo), "s"(srd), "s"(oo[0]) : "vcc", "scc", "memory");
                PINS(clk);
                WT(0);
            }
            SFOR(i, 7)
                asm volatile("buffer_load_dwordx4 %0, %1, %2, %3 offen sc1" : "=v"(d[i + 1]) : "v"(vo), "s"(srd), "s"(oo[i + 1]) : "memory");
                SCHED();
                static_for<(25 * i) / 7, (25 * (i + 1)) / 7>(books);
                SCHED();
            SEND
            WT(1);
            float f0[5], f1[5]; unsigned long long bad;
#define WSW(o_) asm volatile("v_permlane32_swap_b32 %0, %1" : "+v"(f0[o_]), "+v"(f1[o_]))
#define WAD(o_) asm volatile("v_add_f32_dpp %0, %1, %0 row_ror:8 row_mask:0xf bank_mask:0xf" : "+v"(f0[o_]) : "v"(f1[o_]))
#define WFO(o_) do { VADD(f0[o_], acc[o_][0], acc[o_][2]); VADD(f1[o_], acc[o_][1], acc[o_][3]); } while (0)
#define WWR(o_) *(LAS float*)(xw + L * 4096 + (o_) * 128) = f0[o_]
            u32 th, per, so;
            auto next = [&](auto ic_) __attribute__((always_inline)) {
                constexpr int i = decltype(ic_)::value;
                if constexpr (i == 0) { so = (u32)(sn[M] & 3) * 0x42000u; PINS(so); }
                if constexpr (i == 1) { so = (so & 0xC4000u) ^ (pol0 + M * MB); PINS(so); }
                if constexpr (i == 2) { so = sn[M] + 1 < NSEG ? so : OOB; PINS(so); }
                if constexpr (i == 4) { vo = l16 + so; PIN(vo); }
                if constexpr (i == 5 && !L) { fin = (u32)(sn[0] + sn[1] + 2); PINS(fin); }
                if constexpr (i == 6 && L == 0) { fin = (fin | uu(left)) >> 7; PIN(fin); }
                if constexpr (i == 6 && L == 1) { cnt += 1u; PIN(cnt); left = cnt >> 9; PIN(left); }
                if constexpr (i == 7) { PIN(hv); th = rdl(hv, 0); PINS(th); }
                if constexpr (i == 8) { per = rdl(hv, 1); PINS(per); so = th - t; PINS(so); }
                if constexpr (i == 9) { per = (int)so < 20 ? per : 0u; PINS(per); }
                if constexpr (i == 10) { th += per; PINS(th); if constexpr (!L) { per = rdl(hv, 3); PINS(per); } }
                if constexpr (i == 11) { tp = t; t = lead ? tn : th; PINS(t); }
                if constexpr (i == 12) { if constexpr (!L) { fin |= per; PIN(fin); } tn = nt; }
            };
            auto slot = [&](auto ic_) __attribute__((always_inline)) {
                constexpr int i = decltype(ic_)::value;
                constexpr int NL = WO_NL, I0 = 40 - 5 * NL, pc = i < I0 ? i / 5 : 8 - NL + (i - I0) % NL, oc = i < I0 ? i % 5 : (i - I0) / NL;
                f32x4& ac = acc[oc]; v4u& dp = d[pc]; v8u& wp = wo[pc][oc];
                if constexpr (oc == 0) {
                    if constexpr (pc * 5 < 32) asm volatile("s_waitcnt vmcnt(%4)\n\tv_smfmac_f32_16x16x64_bf16 %0, %1, %2, %3" : "+v"(ac), "+v"(dp) : "a"(wp), "v"(idx), "i"(pc < 2 ? 7 - pc : 9 - pc));
                    else asm volatile("s_waitcnt vmcnt(%4)\n\tv_smfmac_f32_16x16x64_bf16 %0, %1, %2, %3" : "+v"(ac), "+v"(dp) : "v"(wp), "v"(idx), "i"(9 - pc));
                } else if constexpr (pc * 5 + oc < 32) SMF_A(ac, dp, wp); else SMF_V(ac, dp, wp);
                SCHED();
                if constexpr (i == 0) WT(2);
                if constexpr (i < 25 && oc == 0) LGK(15);
                if constexpr (i == 6) { u32& h_ = hv; (void)&srd; LD4A(h_, hvo[L], 0); }
                if constexpr (i == 8) books(IC<25>{});
                if constexpr (i >= 11 && i < 19 && i != 15) next(IC<i - 11 - (i > 15)>{});
                if constexpr (i == 20) WT(4);
                constexpr int e = i - (I0 + NL + 1), o = e < 0 ? 0 : e / 3;
                if constexpr (e >= 0 && e % 3 == 0) { CADD(f0[o], acc[o][0], acc[o][2]); if constexpr (o > 0) WAD(o - 1); }
                if constexpr (e >= 0 && e % 3 == 1) { if constexpr (o > 0) { WWR(o - 1); SCHED(); } CADD(f1[o], acc[o][1], acc[o][3]); }
                if constexpr (e >= 0 && e % 3 == 2) {
                    if constexpr (o == 0) asm volatile("v_cmp_u_f32 %0, %1, %2" : "=s"(bad) : "v"(f0[0]), "v"(f1[0]));
                    else LGK(15);
                    WSW(o);
                }
                SCHED();
            };
            WT(3);
            static_for<0, 40>(slot);
            WT(5);
            static_assert(WO_NL == 3, "tail");
            if (__builtin_expect(bad != 0ull, 0)) {
                bool miss = false;
#pragma unroll
                for (int i = 0; i < 8; ++i) miss |= d[i].x == PSENT;
                if (all_(!miss)) {
#pragma unroll
                    for (int i = 0; i < 8; ++i) miss |= d[i].y == PSENT || d[i].z == PSENT || d[i].w == PSENT;
                }
                if (miss) *(LAS u32*)(sw + L * 4096) = 1u;
            }
            asm volatile("v_permlane32_swap_b32 %0, %1\n\ts_nop 1\n\tv_add_f32_dpp %0, %1, %0 row_ror:8 row_mask:0xf bank_mask:0xf\n\tv_add_f32 %2, %4, %6\n\tv_add_f32 %3, %5, %7"
                         : "+v"(f0[3]), "+v"(f1[3]), "=&v"(f0[4]), "=&v"(f1[4]) : "v"(acc[4][0]), "v"(acc[4][1]), "v"(acc[4][2]), "v"(acc[4][3]));
            WWR(3); SCHED();
            asm volatile("s_nop 0\n\tv_permlane32_swap_b32 %0, %1\n\ts_nop 1\n\tv_add_f32_dpp %0, %1, %0 row_ror:8 row_mask:0xf bank_mask:0xf" : "+v"(f0[4]), "+v"(f1[4]));
            WWR(4);
            WT(6);
            f32x2 a, b, c, e; u32 mq; v4u sv;
            asm volatile("s_waitcnt lgkmcnt(0)\n\ts_barrier\n\tds_read_b128 %4, %6 offset:%7+2560\n\t"
                         "ds_read_b64 %0, %5 offset:%7\n\tds_read_b64 %1, %5 offset:%7+640\n\tds_read_b64 %2, %5 offset:%7+1280\n\tds_read_b64 %3, %5 offset:%7+1920"
                         : "=&v"(a), "=&v"(b), "=&v"(c), "=&v"(e), "=&v"(sv) : "v"(xr), "v"(zero), "i"(L * 4096) : "memory");
            asm volatile("s_waitcnt lgkmcnt(4)\n\tv_or_b32 %4, %6, %7\n\tv_or3_b32 %4, %4, %8, %9\n\tv_lshl_or_b32 %5, %4, 31, %10\n\t"
                         "s_waitcnt lgkmcnt(2)\n\tv_pk_add_f32 %0, %0, %1\n\ts_waitcnt lgkmcnt(0)\n\tv_pk_add_f32 %2, %2, %3\n\ts_nop 0\n\tv_pk_add_f32 %0, %0, %2"
                         : "+v"(a), "+v"(b), "+v"(c), "+v"(e), "=&v"(pst), "=&v"(mq) : "v"(sv.x), "v"(sv.y), "v"(sv.z), "v"(sv.w), "v"(mp) : "memory");
            st4s(rs, mq, 0, pkv(a));
            SCHED();
            asm volatile("s_memrealtime %0" : "=s"(clk));
            WT(7);
            st4s(rs, za, 0, psent);
            VMW(3); SCHED();
            static_for<7, 13>(next);
        }
    };
    do {
        turn(IC<1>{});
        turn(IC<0>{});
    } while (uu(fin) == 0u);
    if (sn[0] + 1 < NSEG || sn[1] + 1 < NSEG) st4s(rs, tid == 0 ? foff(F_ERR) : OOB, 0, 1u);
    if (w == 0) LST(1900 + j, rt32());
    LDUMP(w == 0, 224 + j);
}

__global__ void __launch_bounds__(256, 1) k_main(Params p) {
    lptr lds = (lptr)(u32)16 - 16;
    if (blockIdx.x == 0) LST(256, rt32());
    unsigned long long pc; int len;
    asm volatile("s_getpc_b64 %0\n.Lk_main_here:\n\ts_mov_b32 %1, .Lk_end_here-.Lk_main_here" : "=s"(pc), "=s"(len));
    const rsrc_t rs = mk_rsrc(p.ws, W_END);
    const v2u e0 = ld8s(rs, threadIdx.x == 0 ? foff(F_EPOCH) : OOB, 0);
    const v4u code = __builtin_amdgcn_raw_buffer_load_b128(mk_rsrc((const void*)(pc & ~15ull), (u32)max(len, 0)), (((blockIdx.x >> 3) & 31u) * 256u + threadIdx.x) * 16u, 0, 0);
    CFENCE();
    auto conv_id = [](int role) {
        const int x = role >> 5, i = role & 31, no = x < 5 ? 5 : 10, og = x < 5 ? x - 3 : 2 * (x - 4) + (i >= 5), r = (x - 3) * 5 + i - no, q = r / 3;
        return x < 3 || i >= no + 5 || r >= 24 ? NCV : i < no ? ((og & 1) ? (og >> 1) * 5 + i % 5 : NCV) : r % 3 == 0 ? 20 + (q & 1) * 20 + (q >> 1) : 16 + (r % 3) * 8 + q;
    };
    int role = (int)(((u32)__builtin_amdgcn_s_getreg(20 | (3 << 11)) & 7u) * 32u + (blockIdx.x >> 3));
    Early e;
    v4u rv;
#pragma unroll 1
    for (int n = 0;; ++n) {
        conv_early(p, rs, conv_id(role), e);
        if (n) break;
        CFENCE();
        if (threadIdx.x == 0) {
            GAS unsigned long long* slots = (GAS unsigned long long*)(p.ws + W_CTRL + F_SLOT * 128);
            const unsigned long long tag = (unsigned long long)e0.y << 32 | (e0.x + 1u);
            u32 mine = (u32)role;
#pragma unroll 1
            for (int k = 0; k < 256 && __hip_atomic_exchange(slots + mine * 16, tag, __ATOMIC_RELAXED, __HIP_MEMORY_SCOPE_AGENT) == tag; ++k) mine = (mine + 1u) & 255u;
            lds_w16(lds, (v4u){mine, e0.x, e0.y, 0u});
        }
        BAR();
        rv = lds_r16(lds);
        BAR();
        if (uni((int)rv.x) == role) break;
        role = uni((int)rv.x);
    }
    const int x = role >> 5, i = role & 31, w = uni(threadIdx.x >> 6), no = x < 5 ? 5 : 10, nr = x < 7 ? 5 : 4, j = conv_id(role);
    const v2u ep = {uu(rv.y), uu(rv.z)};
    asm volatile("" :: "v"(code));
    if (x == 1) { role_wo(p, lds, i, ep); __builtin_amdgcn_endpgm(); }
    if (x < 3) { role_head(p, lds, i >> 4, x * 8 + (i & 15), ep); __builtin_amdgcn_endpgm(); }
    if (i >= no + nr) { role_qkv(p, lds, (x < 5 ? (x - 3) * 22 : 44 + (x - 5) * 17) + i - no - nr, ep); __builtin_amdgcn_endpgm(); }
    if (j < NCV) conv_stage(p, rs, lds, j, ep, e);
    if (i >= no) {
        if (j >= 24 && j < NPRE) role_row(p, lds, (j - 24) * 4 + w, ep);
        __builtin_amdgcn_endpgm();
    }
    const int og = x < 5 ? x - 3 : 2 * (x - 4) + (i >= 5);
    role_out(p, lds, og & 1, og >> 1, i % 5, ep);
}
__global__ void k_end() { asm volatile(".Lk_end_here:"); }

extern "C" void kernel_launch(void* const* d_in, const int* in_sizes, int n_in, void* d_out, int out_size, void* d_ws, size_t ws_size,
                              hipStream_t stream) {
    Params p;
    p.x = (const u16*)d_in[0]; p.mem0 = (const u16*)d_in[1]; p.Wq = (const u16*)d_in[2]; p.Wk = (const u16*)d_in[3];
    p.Wv = (const u16*)d_in[4]; p.Wo = (const u16*)d_in[5];
    p.bk = (const u16*)d_in[8]; p.bv = (const u16*)d_in[9]; p.shift = (const u16*)d_in[10]; p.ssmk = (const u16*)d_in[11]; p.ssmD = (const u16*)d_in[12];
    p.out = (u16*)d_out;
    p.ws = (unsigned char*)(((size_t)d_ws + 2 * MB - 1) & ~(size_t)(2 * MB - 1));
    (void)hipFuncSetAttribute((const void*)k_main, hipFuncAttributeMaxDynamicSharedMemorySize, LDS_BYTES);
    hipLaunchKernelGGL(k_main, dim3(256), dim3(256), LDS_BYTES, stream, p);
}
```
